```python
import torch

def _softmax(x, axis=-1):
    return torch.softmax(x, dim=axis)
def _gelu(x):
    return torch.nn.functional.gelu(x, approximate="tanh")
def _silu(x):
    return torch.nn.functional.silu(x)
def _sigmoid(x):
    return torch.sigmoid(x)
def _layernorm(x, axis=-1, eps=1e-5):
    m = x.mean(dim=axis, keepdim=True)
    v = x.var(dim=axis, keepdim=True, unbiased=False)
    return (x - m) / torch.sqrt(v + eps)
def _scatter_add(tgt, idx, src, axis=0):
    return tgt.clone().index_add_(axis, idx, src)
def _segment_sum(src, seg, n):
    out = torch.zeros((n,) + src.shape[1:], dtype=src.dtype)
    return out.index_add_(0, seg, src)
def _pad(x, pw):
    flat = tuple(v for pair in reversed(pw) for v in pair)
    return torch.nn.functional.pad(x, flat)

kernel_name = 'standalone__deep_cross_pnn__recsys'

def setup_inputs():

    _w = (lambda a: (lambda t: t.to(torch.bfloat16) if t.is_floating_point() else t)(torch.as_tensor(a)))
    B, F, D, V = 16384, 100, 128, min(1000, 100000)
    H = 4 * D
    return {
        "idx": _w((lambda lo, hi, *s: torch.randint(lo, hi, s))(0, V, B, F).to(torch.int64)),
        "table": _w((lambda *s: torch.randn(*s, dtype=torch.bfloat16) * 1.6)(V, D) * (D ** -0.5)),
        "w_p": _w((lambda *s: torch.randn(*s, dtype=torch.bfloat16) * 0.05)(F * F) * ((F * F) ** -0.5)),
        "w1": _w((lambda *s: torch.randn(*s, dtype=torch.bfloat16) * 1.6)(F * D, H) * ((F * D) ** -0.5)),
        "b1": _w((lambda *s: torch.randn(*s, dtype=torch.bfloat16) * 12.8)(H) * 0.01),
        "w2": _w((lambda *s: torch.randn(*s, dtype=torch.bfloat16) * 0.05)(H) * (H ** -0.5)),
        "b2": _w((lambda *s: torch.randn(*s, dtype=torch.bfloat16) * 1.6)(1) * 0.01),
    }

def reference(idx, table, w_p, w1, b1, w2, b2):
    emb = table[idx]
    B = emb.shape[0]; F = emb.shape[1]; D = emb.shape[2]
    ip = torch.einsum('bid,bjd->bij', emb, emb)
    ip = ip.reshape(B, F * F)
    logit_p = ip @ w_p
    z = emb.reshape(B, F * D)
    h = _gelu(z @ w1 + b1)
    logit_d = h @ w2 + b2
    return logit_p + logit_d
```

```cpp
#include <hip/hip_runtime.h>
#include <hip/hip_bf16.h>
#include <stdint.h>

typedef __attribute__((ext_vector_type(2))) unsigned u32x2;

constexpr int BATCH = 16384, NF = 100, DIM = 128, VOC = 1000, HID = 512;
constexpr size_t NWIN = 13;
constexpr size_t WS_IDX = 0;
constexpr size_t IDX_STREAM = 212992;
constexpr size_t WS_W1F = 3407872;
constexpr size_t W1F_SLAB = 102400;
constexpr size_t WS_TF = 16515072;
constexpr size_t WS_WT = 16777216;
constexpr size_t WS_PART = 17825792;
constexpr size_t WS_P0 = 26214400;
constexpr size_t WS_XCH = 30408704;
constexpr size_t XCH_PITCH = 69632;
constexpr size_t WS_CNT = 48234496;
constexpr size_t WS_CH = 48242688;
constexpr size_t WS_RP = 48243712;
constexpr size_t WS_TS = 48244736;
constexpr size_t WS_RDY = 48245760;
constexpr size_t WS_DBG = 48278528;
constexpr size_t WS_END = 48671744;

__device__ __forceinline__ __amdgpu_buffer_rsrc_t mk_srd(const void* p, unsigned bytes) {
  return __builtin_amdgcn_make_buffer_rsrc((void*)p, (short)0, (int)bytes, 0x00020000);
}

__global__ __launch_bounds__(512, 2) void dcp_all(const int* __restrict__ idx, const unsigned short* __restrict__ table,
                                                  const unsigned short* __restrict__ w_p, const unsigned short* __restrict__ w1,
                                                  const unsigned short* __restrict__ b1, const unsigned short* __restrict__ w2,
                                                  const unsigned short* __restrict__ b2, char* __restrict__ ws,
                                                  __hip_bfloat16* __restrict__ out) {
  __shared__ __attribute__((aligned(1024))) char smem[163840];
  const int w = __builtin_amdgcn_readfirstlane(threadIdx.x >> 6) + (int)(uintptr_t)smem;
  const int bid = blockIdx.x;
  asm volatile(
    "s_getpc_b64 s[36:37]\n\t"
    ".Ldcp_pc:\n\t"
    "s_bfe_u32 s38, %0, 0x40003\n\t"
    "s_lshl_b32 s38, s38, 13\n\t"
    "v_and_b32 v122, 63, %1\n\t"
    "v_lshlrev_b32 v122, 7, v122\n\t"
    "v_add_u32 v122, s38, v122\n\t"
    "v_min_u32 v122, .Ldcp_end-.Ldcp_pc-4, v122\n\t"
    "s_cmp_lg_u32 %2, 3\n\t"
    "s_cbranch_scc1 L_nopf_%=\n\t"
    "global_load_dword v123, v122, s[36:37]\n\t"
    "L_nopf_%=:\n\t"
    :: "s"(bid), "v"(threadIdx.x), "s"(w) : "memory", "scc", "s36", "s37", "s38", "s74", "s75", "v122", "v123");
  const int xcd = bid & 7, loc = bid >> 3;
  const int cg = xcd * 16 + (loc >> 1);
  const __amdgpu_buffer_rsrc_t s_idx = mk_srd(idx + (size_t)bid * 64 * NF, (unsigned)((BATCH - bid * 64) * NF * 4));
  const __amdgpu_buffer_rsrc_t s_tab = mk_srd(table, VOC * DIM * 2);
  const __amdgpu_buffer_rsrc_t s_ws = mk_srd(ws, (unsigned)WS_END);
  const unsigned short* pb1 = b1 + 4 * cg;
  const unsigned short* pw2 = w2 + 4 * cg;
  const unsigned long long pb2 = (unsigned long long)b2 & ~3ull;
  const unsigned b2s = ((unsigned)(unsigned long long)b2 & 2u) * 8u;
  const unsigned long long p_wp = (unsigned long long)w_p & 0xFFFFFFFFFFFFull, p_w1 = (unsigned long long)w1 & 0xFFFFFFFFFFFFull;
  const unsigned long long p_out = (unsigned long long)out & 0xFFFFFFFFFFFFull;
  asm volatile(
    "v_mbcnt_lo_u32_b32 v147, -1, 0\n\t"
    "v_mbcnt_hi_u32_b32 v147, -1, v147\n\t"
    "s_mov_b64 s[92:93], %1\n\t"
    "s_mov_b32 s94, 20000\n\t"
    "s_mov_b32 s95, 0x20000\n\t"
    "s_lshr_b32 s46, %14, 3\n\t"
    "s_lshl_b32 s51, s46, 2\n\t"
    "v_lshlrev_b32 v122, 4, v147\n\t"
    "s_cmp_ge_u32 %16, 2\n\t"
    "s_cbranch_scc1 L_aimg_%=\n\t"
    "s_lshl_b32 s44, %16, 10\n\t"
    "s_add_u32 m0, s44, 140288\n\t"
    "s_nop 0\n\t"
    "buffer_load_dwordx4 v122, s[92:95], s44 offen lds\n\t"
    "s_add_u32 s44, s44, 0x800\n\t"
    "s_add_u32 m0, s44, 140288\n\t"
    "s_nop 0\n\t"
    "buffer_load_dwordx4 v122, s[92:95], s44 offen lds\n\t"
    "s_add_u32 s44, s44, 0x800\n\t"
    "s_add_u32 m0, s44, 140288\n\t"
    "s_nop 0\n\t"
    "buffer_load_dwordx4 v122, s[92:95], s44 offen lds\n\t"
    "s_add_u32 s44, s44, 0x800\n\t"
    "s_add_u32 m0, s44, 140288\n\t"
    "s_nop 0\n\t"
    "buffer_load_dwordx4 v122, s[92:95], s44 offen lds\n\t"
    "s_add_u32 s44, s44, 0x800\n\t"
    "s_add_u32 m0, s44, 140288\n\t"
    "s_nop 0\n\t"
    "buffer_load_dwordx4 v122, s[92:95], s44 offen lds\n\t"
    "s_add_u32 s44, s44, 0x800\n\t"
    "s_add_u32 m0, s44, 140288\n\t"
    "s_nop 0\n\t"
    "buffer_load_dwordx4 v122, s[92:95], s44 offen lds\n\t"
    "s_add_u32 s44, s44, 0x800\n\t"
    "s_add_u32 m0, s44, 140288\n\t"
    "s_nop 0\n\t"
    "buffer_load_dwordx4 v122, s[92:95], s44 offen lds\n\t"
    "s_add_u32 s44, s44, 0x800\n\t"
    "s_add_u32 m0, s44, 140288\n\t"
    "s_nop 0\n\t"
    "buffer_load_dwordx4 v122, s[92:95], s44 offen lds\n\t"
    "s_add_u32 s44, s44, 0x800\n\t"
    "s_add_u32 m0, s44, 140288\n\t"
    "s_nop 0\n\t"
    "buffer_load_dwordx4 v122, s[92:95], s44 offen lds\n\t"
    "s_add_u32 s44, s44, 0x800\n\t"
    "s_add_u32 m0, s44, 140288\n\t"
    "s_nop 0\n\t"
    "buffer_load_dwordx4 v122, s[92:95], s44 offen lds\n\t"
    "s_add_u32 s44, s44, 0x800\n\t"
    "s_mov_b32 s98, 13107200\n\t"
    "s_mov_b32 s99, 0x20000\n\t"
    "s_cmp_eq_u32 %16, 0\n\t"
    "s_cbranch_scc1 L_aside_%=\n\t"
    "s_branch L_ahelp_%=\n\t"
    "L_aimg_%=:\n\t"
    "s_cmp_lg_u32 %16, 2\n\t"
    "s_cbranch_scc1 L_anoinit_%=\n\t"
    "v_mov_b32 v0, 0\n\t"
    "v_mov_b32 v1, 0\n\t"
    "v_mov_b32 v2, 0\n\t"
    "v_mov_b32 v3, 0\n\t"
    "v_lshlrev_b32 v8, 4, v147\n\t"
    "v_add_u32 v8, 130048, v8\n\t"
    "v_mov_b32 v9, 1024\n\t"
    "ds_write_b128 v9, v[0:3]\n\t"
    "ds_write_b128 v8, v[0:3] offset:0\n\t"
    "ds_write_b128 v8, v[0:3] offset:1024\n\t"
    "ds_write_b128 v8, v[0:3] offset:2048\n\t"
    "ds_write_b32 v9, v0 offset:2084\n\t"
    "ds_write_b32 v9, v0 offset:2088\n\t"
    "ds_write_b32 v9, v0 offset:1044\n\t"
    "v_mov_b32 v4, 3\n\t"
    "ds_write_b32 v9, v4 offset:1040\n\t"
    "v_mov_b32 v5, 16\n\t"
    "ds_write_b32 v9, v5 offset:2080\n\t"
    "v_mov_b32 v6, 33\n\t"
    "ds_write_b32 v9, v6 offset:3120\n\t"
    "v_mov_b32 v7, 52\n\t"
    "ds_write_b32 v9, v7 offset:4160\n\t"
    "L_anoinit_%=:\n\t"
    "s_and_b32 s46, %16, 3\n\t"
    "s_mul_i32 s49, s46, 1040\n\t"
    "s_add_u32 s49, s49, 2064\n\t"
    "s_lshl_b32 s46, s46, 3\n\t"
    "s_lshr_b32 s47, 1076763402, s46\n\t"
    "s_and_b32 s47, s47, 0xff\n\t"
    "s_lshl_b32 s48, s47, 16\n\t"
    "s_lshr_b32 s47, 773523968, s46\n\t"
    "s_and_b32 s47, s47, 0xff\n\t"
    "s_add_u32 s48, s48, s47\n\t"
    "s_lshr_b32 s47, %16, 2\n\t"
    "s_add_u32 s47, s47, 0x200\n\t"
    "s_cmp_eq_u32 s46, 0\n\t"
    "s_cselect_b32 s47, 0x100, s47\n\t"
    "s_add_u32 s48, s48, s47\n\t"
    "v_lshrrev_b32 v126, 3, v147\n\t"
    "v_and_b32 v154, 7, v147\n\t"
    "v_lshlrev_b32 v126, 8, v126\n\t"
    "v_lshl_add_u32 v126, v154, 4, v126\n\t"
    "v_lshlrev_b32 v117, 20, v147\n\t"
    "v_add_u32 v117, 1024, v117\n\t"
    "v_mov_b32 v118, 1\n\t"
    "s_and_b32 s46, %16, 6\n\t"
    "s_cmp_eq_u32 s46, 4\n\t"
    "s_cbranch_scc1 L_adma_%=\n\t"
    "s_lshr_b32 s61, %16, 2\n\t"
    "s_lshl_b32 s61, s61, 1\n\t"
    "s_and_b32 s46, %16, 1\n\t"
    "s_add_u32 s61, s61, s46\n\t"
    "s_add_u32 s46, s61, 84\n\t"
    "s_add_u32 s69, s51, s46\n\t"
    "s_sub_u32 s44, s69, 125\n\t"
    "s_cmp_ge_u32 s69, 125\n\t"
    "s_cselect_b32 s69, s44, s69\n\t"
    "s_mul_i32 m0, s69, 1040\n\t"
    "s_lshl_b32 s44, s69, 11\n\t"
    "buffer_load_dwordx4 v126, %15, s44 offen lds\n\t"
    "s_add_u32 s69, s69, 4\n\t"
    "s_sub_u32 s44, s69, 125\n\t"
    "s_cmp_ge_u32 s69, 125\n\t"
    "s_cselect_b32 s69, s44, s69\n\t"
    "s_mul_i32 m0, s69, 1040\n\t"
    "s_lshl_b32 s44, s69, 11\n\t"
    "buffer_load_dwordx4 v126, %15, s44 offen lds\n\t"
    "s_add_u32 s69, s69, 4\n\t"
    "s_sub_u32 s44, s69, 125\n\t"
    "s_cmp_ge_u32 s69, 125\n\t"
    "s_cselect_b32 s69, s44, s69\n\t"
    "s_waitcnt lgkmcnt(0)\n\t"
    "s_barrier\n\t"
    "s_load_dwordx2 s[72:73], %6, 0\n\t"
    "s_load_dwordx2 s[88:89], %7, 0\n\t"
    "s_load_dword s91, %8, 0\n\t"
    "s_mul_i32 m0, s69, 1040\n\t"
    "s_lshl_b32 s44, s69, 11\n\t"
    "buffer_load_dwordx4 v126, %15, s44 offen lds\n\t"
    "s_add_u32 s69, s69, 4\n\t"
    "s_sub_u32 s44, s69, 125\n\t"
    "s_cmp_ge_u32 s69, 125\n\t"
    "s_cselect_b32 s69, s44, s69\n\t"
    "s_mul_i32 m0, s69, 1040\n\t"
    "s_lshl_b32 s44, s69, 11\n\t"
    "buffer_load_dwordx4 v126, %15, s44 offen lds\n\t"
    "s_add_u32 s69, s69, 4\n\t"
    "s_sub_u32 s44, s69, 125\n\t"
    "s_cmp_ge_u32 s69, 125\n\t"
    "s_cselect_b32 s69, s44, s69\n\t"
    "s_mul_i32 m0, s69, 1040\n\t"
    "s_lshl_b32 s44, s69, 11\n\t"
    "buffer_load_dwordx4 v126, %15, s44 offen lds\n\t"
    "s_add_u32 s69, s69, 4\n\t"
    "s_sub_u32 s44, s69, 125\n\t"
    "s_cmp_ge_u32 s69, 125\n\t"
    "s_cselect_b32 s69, s44, s69\n\t"
    "s_mul_i32 m0, s69, 1040\n\t"
    "s_lshl_b32 s44, s69, 11\n\t"
    "buffer_load_dwordx4 v126, %15, s44 offen lds\n\t"
    "s_add_u32 s69, s69, 4\n\t"
    "s_sub_u32 s44, s69, 125\n\t"
    "s_cmp_ge_u32 s69, 125\n\t"
    "s_cselect_b32 s69, s44, s69\n\t"
    "v_bfe_u32 v154, v147, 2, 2\n\t"
    "v_and_b32 v155, 3, v147\n\t"
    "v_mul_u32_u24 v154, 25, v154\n\t"
    "v_mad_u32_u24 v154, v155, 6, v154\n\t"
    "v_lshlrev_b32 v153, 2, v154\n\t"
    "s_and_b32 s53, s48, 0xff\n\t"
    "s_bfe_u32 s55, s48, 0x80008\n\t"
    "s_add_u32 s54, s53, s55\n\t"
    "s_add_u32 s55, s54, s55\n\t"
    "s_lshr_b32 s64, s48, 16\n\t"
    "v_cmp_eq_u32 s[62:63], 0, v155\n\t"
    "v_and_b32 v151, 48, v147\n\t"
    "v_or_b32 v155, 0x100000, v151\n\t"
    "v_cndmask_b32 v152, v155, v151, s[62:63]\n\t"
    "v_lshrrev_b32 v94, 4, v147\n\t"
    "v_mul_u32_u24 v92, 0x1388, v94\n\t"
    "v_lshl_add_u32 v92, v154, 1, v92\n\t"
    "v_add_u32 v92, 140288, v92\n\t"
    "v_mul_u32_u24 v93, 200, v154\n\t"
    "v_mad_u32_u24 v93, v94, 50, v93\n\t"
    "v_add_u32 v93, 140288, v93\n\t"
    "v_add_u32 v95, 140288, v122\n\t"
    "v_mov_b32 v0, 0\n\t"
    "v_mov_b32 v28, 0\n\t"
    "v_mov_b32 v1, 0\n\t"
    "v_mov_b32 v29, 0\n\t"
    "v_mov_b32 v2, 0\n\t"
    "v_mov_b32 v30, 0\n\t"
    "v_mov_b32 v3, 0\n\t"
    "v_mov_b32 v31, 0\n\t"
    "v_mov_b32 v4, 0\n\t"
    "v_mov_b32 v32, 0\n\t"
    "v_mov_b32 v5, 0\n\t"
    "v_mov_b32 v33, 0\n\t"
    "v_mov_b32 v6, 0\n\t"
    "v_mov_b32 v34, 0\n\t"
    "v_mov_b32 v7, 0\n\t"
    "v_mov_b32 v35, 0\n\t"
    "v_mov_b32 v8, 0\n\t"
    "v_mov_b32 v36, 0\n\t"
    "v_mov_b32 v9, 0\n\t"
    "v_mov_b32 v37, 0\n\t"
    "v_mov_b32 v10, 0\n\t"
    "v_mov_b32 v38, 0\n\t"
    "v_mov_b32 v11, 0\n\t"
    "v_mov_b32 v39, 0\n\t"
    "v_mov_b32 v12, 0\n\t"
    "v_mov_b32 v40, 0\n\t"
    "v_mov_b32 v13, 0\n\t"
    "v_mov_b32 v41, 0\n\t"
    "v_mov_b32 v14, 0\n\t"
    "v_mov_b32 v42, 0\n\t"
    "v_mov_b32 v15, 0\n\t"
    "v_mov_b32 v43, 0\n\t"
    "v_mov_b32 v16, 0\n\t"
    "v_mov_b32 v44, 0\n\t"
    "v_mov_b32 v17, 0\n\t"
    "v_mov_b32 v45, 0\n\t"
    "v_mov_b32 v18, 0\n\t"
    "v_mov_b32 v46, 0\n\t"
    "v_mov_b32 v19, 0\n\t"
    "v_mov_b32 v47, 0\n\t"
    "v_mov_b32 v20, 0\n\t"
    "v_mov_b32 v48, 0\n\t"
    "v_mov_b32 v21, 0\n\t"
    "v_mov_b32 v49, 0\n\t"
    "v_mov_b32 v22, 0\n\t"
    "v_mov_b32 v50, 0\n\t"
    "v_mov_b32 v23, 0\n\t"
    "v_mov_b32 v51, 0\n\t"
    "s_cmp_eq_u32 s61, 1\n\t"
    "s_cbranch_scc1 L_afold1_%=\n\t"
    "s_cmp_eq_u32 s61, 2\n\t"
    "s_cbranch_scc1 L_afold2_%=\n\t"
    "s_cmp_eq_u32 s61, 3\n\t"
    "s_cbranch_scc1 L_afold3_%=\n\t"
    "L_afold0_%=:\n\t"
    "L_asp0_%=:\n\t"
    "ds_read_b32 v119, v117 offset:1044\n\t"
    "s_waitcnt lgkmcnt(0)\n\t"
    "v_readfirstlane_b32 s46, v119\n\t"
    "s_nop 0\n\t"
    "s_cmp_ge_u32 s46, 2\n\t"
    "s_cbranch_scc1 L_ago0_%=\n\t"
    "s_sleep 1\n\t"
    "s_branch L_asp0_%=\n\t"
    "L_ago0_%=:\n\t"
    "ds_read_u16_d16_hi v0, v92 offset:204\n\t"
    "ds_read_u16_d16_hi v28, v93 offset:402\n\t"
    "ds_read_u16_d16_hi v1, v92 offset:1404\n\t"
    "ds_read_u16_d16_hi v29, v93 offset:414\n\t"
    "ds_read_u16_d16_hi v2, v92 offset:2604\n\t"
    "ds_read_u16_d16_hi v30, v93 offset:426\n\t"
    "ds_read_u16_d16_hi v3, v92 offset:3804\n\t"
    "ds_read_u16_d16_hi v31, v93 offset:438\n\t"
    "ds_read_u16_d16_hi v4, v92 offset:408\n\t"
    "ds_read_u16_d16_hi v32, v93 offset:804\n\t"
    "ds_read_u16_d16_hi v5, v92 offset:1608\n\t"
    "ds_read_u16_d16_hi v33, v93 offset:816\n\t"
    "ds_read_u16_d16_hi v6, v92 offset:2808\n\t"
    "ds_read_u16_d16_hi v34, v93 offset:828\n\t"
    "ds_read_u16_d16_hi v7, v92 offset:4008\n\t"
    "ds_read_u16_d16_hi v35, v93 offset:840\n\t"
    "ds_read_u16_d16_hi v8, v92 offset:412\n\t"
    "ds_read_u16_d16_hi v36, v93 offset:1204\n\t"
    "ds_read_u16_d16_hi v9, v92 offset:1612\n\t"
    "ds_read_u16_d16_hi v37, v93 offset:1216\n\t"
    "ds_read_u16_d16_hi v10, v92 offset:2812\n\t"
    "ds_read_u16_d16_hi v38, v93 offset:1228\n\t"
    "ds_read_u16_d16_hi v11, v92 offset:4012\n\t"
    "ds_read_u16_d16_hi v39, v93 offset:1240\n\t"
    "ds_read_u16_d16_hi v12, v92 offset:810\n\t"
    "ds_read_u16_d16_hi v40, v93 offset:1008\n\t"
    "ds_read_u16_d16_hi v13, v92 offset:2010\n\t"
    "ds_read_u16_d16_hi v41, v93 offset:1020\n\t"
    "ds_read_u16_d16_hi v14, v92 offset:3210\n\t"
    "ds_read_u16_d16_hi v42, v93 offset:1032\n\t"
    "ds_read_u16_d16_hi v15, v92 offset:4410\n\t"
    "ds_read_u16_d16_hi v43, v93 offset:1044\n\t"
    "ds_read_u16_d16_hi v16, v92 offset:202\n\t"
    "ds_read_u16_d16_hi v17, v92 offset:1402\n\t"
    "ds_read_u16_d16_hi v18, v92 offset:2602\n\t"
    "ds_read_u16_d16_hi v19, v92 offset:3802\n\t"
    "ds_read_u16_d16_hi v20, v92 offset:808\n\t"
    "ds_read_u16_d16_hi v21, v92 offset:2008\n\t"
    "ds_read_u16_d16_hi v22, v92 offset:3208\n\t"
    "ds_read_u16_d16_hi v23, v92 offset:4408\n\t"
    "ds_add_u32 v117, v118 offset:0\n\t"
    "s_waitcnt lgkmcnt(15)\n\t"
    "v_add_f32 v0, v0, v28\n\t"
    "v_add_f32 v1, v1, v29\n\t"
    "v_add_f32 v2, v2, v30\n\t"
    "v_add_f32 v3, v3, v31\n\t"
    "v_add_f32 v4, v4, v32\n\t"
    "v_add_f32 v5, v5, v33\n\t"
    "v_add_f32 v6, v6, v34\n\t"
    "v_add_f32 v7, v7, v35\n\t"
    "v_add_f32 v8, v8, v36\n\t"
    "v_add_f32 v9, v9, v37\n\t"
    "v_add_f32 v10, v10, v38\n\t"
    "v_add_f32 v11, v11, v39\n\t"
    "s_waitcnt lgkmcnt(9)\n\t"
    "v_add_f32 v12, v12, v40\n\t"
    "v_add_f32 v13, v13, v41\n\t"
    "v_add_f32 v14, v14, v42\n\t"
    "v_add_f32 v15, v15, v43\n\t"
    "s_waitcnt lgkmcnt(5)\n\t"
    "s_waitcnt lgkmcnt(1)\n\t"
    "L_asp1_%=:\n\t"
    "ds_read_b32 v119, v117 offset:0\n\t"
    "s_waitcnt lgkmcnt(0)\n\t"
    "v_readfirstlane_b32 s46, v119\n\t"
    "s_nop 0\n\t"
    "s_cmp_ge_u32 s46, 4\n\t"
    "s_cbranch_scc1 L_ago1_%=\n\t"
    "s_sleep 1\n\t"
    "s_branch L_asp1_%=\n\t"
    "L_ago1_%=:\n\t"
    "ds_write_b128 v95, v[0:3] offset:1024\n\t"
    "ds_write_b128 v95, v[4:7] offset:7168\n\t"
    "ds_write_b128 v95, v[8:11] offset:11264\n\t"
    "ds_write_b128 v95, v[12:15] offset:16384\n\t"
    "ds_write_b128 v95, v[16:19] offset:0\n\t"
    "ds_write_b128 v95, v[20:23] offset:15360\n\t"
    "ds_add_u32 v117, v118 offset:4\n\t"
    "s_branch L_ab0_%=\n\t"
    "L_afold1_%=:\n\t"
    "L_asp2_%=:\n\t"
    "ds_read_b32 v119, v117 offset:1044\n\t"
    "s_waitcnt lgkmcnt(0)\n\t"
    "v_readfirstlane_b32 s46, v119\n\t"
    "s_nop 0\n\t"
    "s_cmp_ge_u32 s46, 2\n\t"
    "s_cbranch_scc1 L_ago2_%=\n\t"
    "s_sleep 1\n\t"
    "s_branch L_asp2_%=\n\t"
    "L_ago2_%=:\n\t"
    "ds_read_u16_d16_hi v0, v92 offset:206\n\t"
    "ds_read_u16_d16_hi v28, v93 offset:602\n\t"
    "ds_read_u16_d16_hi v1, v92 offset:1406\n\t"
    "ds_read_u16_d16_hi v29, v93 offset:614\n\t"
    "ds_read_u16_d16_hi v2, v92 offset:2606\n\t"
    "ds_read_u16_d16_hi v30, v93 offset:626\n\t"
    "ds_read_u16_d16_hi v3, v92 offset:3806\n\t"
    "ds_read_u16_d16_hi v31, v93 offset:638\n\t"
    "ds_read_u16_d16_hi v4, v92 offset:210\n\t"
    "ds_read_u16_d16_hi v32, v93 offset:1002\n\t"
    "ds_read_u16_d16_hi v5, v92 offset:1410\n\t"
    "ds_read_u16_d16_hi v33, v93 offset:1014\n\t"
    "ds_read_u16_d16_hi v6, v92 offset:2610\n\t"
    "ds_read_u16_d16_hi v34, v93 offset:1026\n\t"
    "ds_read_u16_d16_hi v7, v92 offset:3810\n\t"
    "ds_read_u16_d16_hi v35, v93 offset:1038\n\t"
    "ds_read_u16_d16_hi v8, v92 offset:608\n\t"
    "ds_read_u16_d16_hi v36, v93 offset:806\n\t"
    "ds_read_u16_d16_hi v9, v92 offset:1808\n\t"
    "ds_read_u16_d16_hi v37, v93 offset:818\n\t"
    "ds_read_u16_d16_hi v10, v92 offset:3008\n\t"
    "ds_read_u16_d16_hi v38, v93 offset:830\n\t"
    "ds_read_u16_d16_hi v11, v92 offset:4208\n\t"
    "ds_read_u16_d16_hi v39, v93 offset:842\n\t"
    "ds_read_u16_d16_hi v12, v92 offset:812\n\t"
    "ds_read_u16_d16_hi v40, v93 offset:1208\n\t"
    "ds_read_u16_d16_hi v13, v92 offset:2012\n\t"
    "ds_read_u16_d16_hi v41, v93 offset:1220\n\t"
    "ds_read_u16_d16_hi v14, v92 offset:3212\n\t"
    "ds_read_u16_d16_hi v42, v93 offset:1232\n\t"
    "ds_read_u16_d16_hi v15, v92 offset:4412\n\t"
    "ds_read_u16_d16_hi v43, v93 offset:1244\n\t"
    "ds_read_u16_d16_hi v16, v92 offset:404\n\t"
    "ds_read_u16_d16_hi v17, v92 offset:1604\n\t"
    "ds_read_u16_d16_hi v18, v92 offset:2804\n\t"
    "ds_read_u16_d16_hi v19, v92 offset:4004\n\t"
    "ds_read_u16_d16_hi v20, v92 offset:1010\n\t"
    "ds_read_u16_d16_hi v21, v92 offset:2210\n\t"
    "ds_read_u16_d16_hi v22, v92 offset:3410\n\t"
    "ds_read_u16_d16_hi v23, v92 offset:4610\n\t"
    "ds_add_u32 v117, v118 offset:0\n\t"
    "s_waitcnt lgkmcnt(15)\n\t"
    "v_add_f32 v0, v0, v28\n\t"
    "v_add_f32 v1, v1, v29\n\t"
    "v_add_f32 v2, v2, v30\n\t"
    "v_add_f32 v3, v3, v31\n\t"
    "v_add_f32 v4, v4, v32\n\t"
    "v_add_f32 v5, v5, v33\n\t"
    "v_add_f32 v6, v6, v34\n\t"
    "v_add_f32 v7, v7, v35\n\t"
    "v_add_f32 v8, v8, v36\n\t"
    "v_add_f32 v9, v9, v37\n\t"
    "v_add_f32 v10, v10, v38\n\t"
    "v_add_f32 v11, v11, v39\n\t"
    "s_waitcnt lgkmcnt(9)\n\t"
    "v_add_f32 v12, v12, v40\n\t"
    "v_add_f32 v13, v13, v41\n\t"
    "v_add_f32 v14, v14, v42\n\t"
    "v_add_f32 v15, v15, v43\n\t"
    "s_waitcnt lgkmcnt(5)\n\t"
    "s_waitcnt lgkmcnt(1)\n\t"
    "L_asp3_%=:\n\t"
    "ds_read_b32 v119, v117 offset:0\n\t"
    "s_waitcnt lgkmcnt(0)\n\t"
    "v_readfirstlane_b32 s46, v119\n\t"
    "s_nop 0\n\t"
    "s_cmp_ge_u32 s46, 4\n\t"
    "s_cbranch_scc1 L_ago3_%=\n\t"
    "s_sleep 1\n\t"
    "s_branch L_asp3_%=\n\t"
    "L_ago3_%=:\n\t"
    "ds_write_b128 v95, v[0:3] offset:3072\n\t"
    "ds_write_b128 v95, v[4:7] offset:8192\n\t"
    "ds_write_b128 v95, v[8:11] offset:12288\n\t"
    "ds_write_b128 v95, v[12:15] offset:17408\n\t"
    "ds_write_b128 v95, v[16:19] offset:2048\n\t"
    "ds_write_b128 v95, v[20:23] offset:18432\n\t"
    "ds_add_u32 v117, v118 offset:4\n\t"
    "s_branch L_ab0_%=\n\t"
    "L_afold2_%=:\n\t"
    "L_asp4_%=:\n\t"
    "ds_read_b32 v119, v117 offset:1044\n\t"
    "s_waitcnt lgkmcnt(0)\n\t"
    "v_readfirstlane_b32 s46, v119\n\t"
    "s_nop 0\n\t"
    "s_cmp_ge_u32 s46, 2\n\t"
    "s_cbranch_scc1 L_ago4_%=\n\t"
    "s_sleep 1\n\t"
    "s_branch L_asp4_%=\n\t"
    "L_ago4_%=:\n\t"
    "ds_read_u16_d16_hi v0, v92 offset:406\n\t"
    "ds_read_u16_d16_hi v28, v93 offset:604\n\t"
    "ds_read_u16_d16_hi v1, v92 offset:1606\n\t"
    "ds_read_u16_d16_hi v29, v93 offset:616\n\t"
    "ds_read_u16_d16_hi v2, v92 offset:2806\n\t"
    "ds_read_u16_d16_hi v30, v93 offset:628\n\t"
    "ds_read_u16_d16_hi v3, v92 offset:4006\n\t"
    "ds_read_u16_d16_hi v31, v93 offset:640\n\t"
    "ds_read_u16_d16_hi v4, v92 offset:212\n\t"
    "ds_read_u16_d16_hi v32, v93 offset:1202\n\t"
    "ds_read_u16_d16_hi v5, v92 offset:1412\n\t"
    "ds_read_u16_d16_hi v33, v93 offset:1214\n\t"
    "ds_read_u16_d16_hi v6, v92 offset:2612\n\t"
    "ds_read_u16_d16_hi v34, v93 offset:1226\n\t"
    "ds_read_u16_d16_hi v7, v92 offset:3812\n\t"
    "ds_read_u16_d16_hi v35, v93 offset:1238\n\t"
    "ds_read_u16_d16_hi v8, v92 offset:610\n\t"
    "ds_read_u16_d16_hi v36, v93 offset:1006\n\t"
    "ds_read_u16_d16_hi v9, v92 offset:1810\n\t"
    "ds_read_u16_d16_hi v37, v93 offset:1018\n\t"
    "ds_read_u16_d16_hi v10, v92 offset:3010\n\t"
    "ds_read_u16_d16_hi v38, v93 offset:1030\n\t"
    "ds_read_u16_d16_hi v11, v92 offset:4210\n\t"
    "ds_read_u16_d16_hi v39, v93 offset:1042\n\t"
    "ds_read_u16_d16_hi v12, v92 offset:1012\n\t"
    "ds_read_u16_d16_hi v40, v93 offset:1210\n\t"
    "ds_read_u16_d16_hi v13, v92 offset:2212\n\t"
    "ds_read_u16_d16_hi v41, v93 offset:1222\n\t"
    "ds_read_u16_d16_hi v14, v92 offset:3412\n\t"
    "ds_read_u16_d16_hi v42, v93 offset:1234\n\t"
    "ds_read_u16_d16_hi v15, v92 offset:4612\n\t"
    "ds_read_u16_d16_hi v43, v93 offset:1246\n\t"
    "ds_read_u16_d16_hi v16, v92 offset:606\n\t"
    "ds_read_u16_d16_hi v17, v92 offset:1806\n\t"
    "ds_read_u16_d16_hi v18, v92 offset:3006\n\t"
    "ds_read_u16_d16_hi v19, v92 offset:4206\n\t"
    "ds_read_u16_d16_hi v20, v92 offset:1212\n\t"
    "ds_read_u16_d16_hi v21, v92 offset:2412\n\t"
    "ds_read_u16_d16_hi v22, v92 offset:3612\n\t"
    "ds_read_u16_d16_hi v23, v92 offset:4812\n\t"
    "ds_add_u32 v117, v118 offset:0\n\t"
    "s_waitcnt lgkmcnt(15)\n\t"
    "v_add_f32 v0, v0, v28\n\t"
    "v_add_f32 v1, v1, v29\n\t"
    "v_add_f32 v2, v2, v30\n\t"
    "v_add_f32 v3, v3, v31\n\t"
    "v_add_f32 v4, v4, v32\n\t"
    "v_add_f32 v5, v5, v33\n\t"
    "v_add_f32 v6, v6, v34\n\t"
    "v_add_f32 v7, v7, v35\n\t"
    "v_add_f32 v8, v8, v36\n\t"
    "v_add_f32 v9, v9, v37\n\t"
    "v_add_f32 v10, v10, v38\n\t"
    "v_add_f32 v11, v11, v39\n\t"
    "s_waitcnt lgkmcnt(9)\n\t"
    "v_add_f32 v12, v12, v40\n\t"
    "v_add_f32 v13, v13, v41\n\t"
    "v_add_f32 v14, v14, v42\n\t"
    "v_add_f32 v15, v15, v43\n\t"
    "s_waitcnt lgkmcnt(5)\n\t"
    "s_waitcnt lgkmcnt(1)\n\t"
    "L_asp5_%=:\n\t"
    "ds_read_b32 v119, v117 offset:0\n\t"
    "s_waitcnt lgkmcnt(0)\n\t"
    "v_readfirstlane_b32 s46, v119\n\t"
    "s_nop 0\n\t"
    "s_cmp_ge_u32 s46, 4\n\t"
    "s_cbranch_scc1 L_ago5_%=\n\t"
    "s_sleep 1\n\t"
    "s_branch L_asp5_%=\n\t"
    "L_ago5_%=:\n\t"
    "ds_write_b128 v95, v[0:3] offset:4096\n\t"
    "ds_write_b128 v95, v[4:7] offset:9216\n\t"
    "ds_write_b128 v95, v[8:11] offset:13312\n\t"
    "ds_write_b128 v95, v[12:15] offset:19456\n\t"
    "ds_write_b128 v95, v[16:19] offset:5120\n\t"
    "ds_write_b128 v95, v[20:23] offset:20480\n\t"
    "ds_add_u32 v117, v118 offset:4\n\t"
    "s_branch L_ab0_%=\n\t"
    "L_afold3_%=:\n\t"
    "L_asp6_%=:\n\t"
    "ds_read_b32 v119, v117 offset:1044\n\t"
    "s_waitcnt lgkmcnt(0)\n\t"
    "v_readfirstlane_b32 s46, v119\n\t"
    "s_nop 0\n\t"
    "s_cmp_ge_u32 s46, 2\n\t"
    "s_cbranch_scc1 L_ago6_%=\n\t"
    "s_sleep 1\n\t"
    "s_branch L_asp6_%=\n\t"
    "L_ago6_%=:\n\t"
    "ds_read_u16_d16_hi v0, v92 offset:208\n\t"
    "ds_read_u16_d16_hi v28, v93 offset:802\n\t"
    "ds_read_u16_d16_hi v1, v92 offset:1408\n\t"
    "ds_read_u16_d16_hi v29, v93 offset:814\n\t"
    "ds_read_u16_d16_hi v2, v92 offset:2608\n\t"
    "ds_read_u16_d16_hi v30, v93 offset:826\n\t"
    "ds_read_u16_d16_hi v3, v92 offset:3808\n\t"
    "ds_read_u16_d16_hi v31, v93 offset:838\n\t"
    "ds_read_u16_d16_hi v4, v92 offset:410\n\t"
    "ds_read_u16_d16_hi v32, v93 offset:1004\n\t"
    "ds_read_u16_d16_hi v5, v92 offset:1610\n\t"
    "ds_read_u16_d16_hi v33, v93 offset:1016\n\t"
    "ds_read_u16_d16_hi v6, v92 offset:2810\n\t"
    "ds_read_u16_d16_hi v34, v93 offset:1028\n\t"
    "ds_read_u16_d16_hi v7, v92 offset:4010\n\t"
    "ds_read_u16_d16_hi v35, v93 offset:1040\n\t"
    "ds_read_u16_d16_hi v8, v92 offset:612\n\t"
    "ds_read_u16_d16_hi v36, v93 offset:1206\n\t"
    "ds_read_u16_d16_hi v9, v92 offset:1812\n\t"
    "ds_read_u16_d16_hi v37, v93 offset:1218\n\t"
    "ds_read_u16_d16_hi v10, v92 offset:3012\n\t"
    "ds_read_u16_d16_hi v38, v93 offset:1230\n\t"
    "ds_read_u16_d16_hi v11, v92 offset:4212\n\t"
    "ds_read_u16_d16_hi v39, v93 offset:1242\n\t"
    "ds_read_u16_d16_hi v12, v92 offset:0\n\t"
    "ds_read_u16_d16_hi v13, v92 offset:2\n\t"
    "ds_read_u16_d16_hi v41, v93 offset:200\n\t"
    "ds_read_u16_d16_hi v14, v92 offset:4\n\t"
    "ds_read_u16_d16_hi v42, v93 offset:400\n\t"
    "ds_read_u16_d16_hi v15, v92 offset:6\n\t"
    "ds_read_u16_d16_hi v43, v93 offset:600\n\t"
    "ds_read_u16_d16_hi v16, v92 offset:8\n\t"
    "ds_read_u16_d16_hi v44, v93 offset:800\n\t"
    "ds_read_u16_d16_hi v17, v92 offset:10\n\t"
    "ds_read_u16_d16_hi v45, v93 offset:1000\n\t"
    "ds_read_u16_d16_hi v18, v92 offset:12\n\t"
    "ds_read_u16_d16_hi v46, v93 offset:1200\n\t"
    "ds_add_u32 v117, v118 offset:0\n\t"
    "s_waitcnt lgkmcnt(15)\n\t"
    "v_add_f32 v0, v0, v28\n\t"
    "v_add_f32 v1, v1, v29\n\t"
    "v_add_f32 v2, v2, v30\n\t"
    "v_add_f32 v3, v3, v31\n\t"
    "v_add_f32 v4, v4, v32\n\t"
    "v_add_f32 v5, v5, v33\n\t"
    "v_add_f32 v6, v6, v34\n\t"
    "v_add_f32 v7, v7, v35\n\t"
    "s_waitcnt lgkmcnt(14)\n\t"
    "v_add_f32 v8, v8, v36\n\t"
    "v_add_f32 v9, v9, v37\n\t"
    "v_add_f32 v10, v10, v38\n\t"
    "v_add_f32 v11, v11, v39\n\t"
    "s_waitcnt lgkmcnt(7)\n\t"
    "v_cndmask_b32 v12, 0, v12, s[62:63]\n\t"
    "v_add_f32 v13, v13, v41\n\t"
    "v_add_f32 v14, v14, v42\n\t"
    "v_add_f32 v15, v15, v43\n\t"
    "s_waitcnt lgkmcnt(1)\n\t"
    "v_add_f32 v16, v16, v44\n\t"
    "v_add_f32 v17, v17, v45\n\t"
    "v_add_f32 v18, v18, v46\n\t"
    "L_asp7_%=:\n\t"
    "ds_read_b32 v119, v117 offset:0\n\t"
    "s_waitcnt lgkmcnt(0)\n\t"
    "v_readfirstlane_b32 s46, v119\n\t"
    "s_nop 0\n\t"
    "s_cmp_ge_u32 s46, 4\n\t"
    "s_cbranch_scc1 L_ago7_%=\n\t"
    "s_sleep 1\n\t"
    "s_branch L_asp7_%=\n\t"
    "L_ago7_%=:\n\t"
    "ds_write_b128 v95, v[0:3] offset:6144\n\t"
    "ds_write_b128 v95, v[4:7] offset:10240\n\t"
    "ds_write_b128 v95, v[8:11] offset:14336\n\t"
    "ds_write_b128 v95, v[12:15] offset:21504\n\t"
    "ds_write_b128 v95, v[16:19] offset:22528\n\t"
    "ds_add_u32 v117, v118 offset:4\n\t"
    "L_ab0_%=:\n\t"
    "s_mul_i32 m0, s69, 1040\n\t"
    "s_lshl_b32 s44, s69, 11\n\t"
    "buffer_load_dwordx4 v126, %15, s44 offen lds\n\t"
    "s_add_u32 s69, s69, 4\n\t"
    "s_sub_u32 s44, s69, 125\n\t"
    "s_cmp_ge_u32 s69, 125\n\t"
    "s_cselect_b32 s69, s44, s69\n\t"
    "s_mul_i32 m0, s69, 1040\n\t"
    "s_lshl_b32 s44, s69, 11\n\t"
    "buffer_load_dwordx4 v126, %15, s44 offen lds\n\t"
    "s_add_u32 s69, s69, 4\n\t"
    "s_sub_u32 s44, s69, 125\n\t"
    "s_cmp_ge_u32 s69, 125\n\t"
    "s_cselect_b32 s69, s44, s69\n\t"
    "s_mul_i32 m0, s69, 1040\n\t"
    "s_lshl_b32 s44, s69, 11\n\t"
    "buffer_load_dwordx4 v126, %15, s44 offen lds\n\t"
    "s_add_u32 s69, s69, 4\n\t"
    "s_sub_u32 s44, s69, 125\n\t"
    "s_cmp_ge_u32 s69, 125\n\t"
    "s_cselect_b32 s69, s44, s69\n\t"
    "s_mul_i32 m0, s69, 1040\n\t"
    "s_lshl_b32 s44, s69, 11\n\t"
    "buffer_load_dwordx4 v126, %15, s44 offen lds\n\t"
    "s_add_u32 s69, s69, 4\n\t"
    "s_sub_u32 s44, s69, 125\n\t"
    "s_cmp_ge_u32 s69, 125\n\t"
    "s_cselect_b32 s69, s44, s69\n\t"
    "s_mul_i32 m0, s69, 1040\n\t"
    "s_lshl_b32 s44, s69, 11\n\t"
    "buffer_load_dwordx4 v126, %15, s44 offen lds\n\t"
    "s_add_u32 s69, s69, 4\n\t"
    "s_sub_u32 s44, s69, 125\n\t"
    "s_cmp_ge_u32 s69, 125\n\t"
    "s_cselect_b32 s69, s44, s69\n\t"
    "s_branch L_ab_%=\n\t"
    "L_adma_%=:\n\t"
    "s_sub_u32 s46, %16, 4\n\t"
    "s_add_u32 s69, s51, s46\n\t"
    "s_sub_u32 s44, s69, 125\n\t"
    "s_cmp_ge_u32 s69, 125\n\t"
    "s_cselect_b32 s69, s44, s69\n\t"
    "s_mul_i32 m0, s69, 1040\n\t"
    "s_lshl_b32 s44, s69, 11\n\t"
    "buffer_load_dwordx4 v126, %15, s44 offen lds\n\t"
    "s_add_u32 s69, s69, 2\n\t"
    "s_sub_u32 s44, s69, 125\n\t"
    "s_cmp_ge_u32 s69, 125\n\t"
    "s_cselect_b32 s69, s44, s69\n\t"
    "s_mul_i32 m0, s69, 1040\n\t"
    "s_lshl_b32 s44, s69, 11\n\t"
    "buffer_load_dwordx4 v126, %15, s44 offen lds\n\t"
    "s_add_u32 s69, s69, 2\n\t"
    "s_sub_u32 s44, s69, 125\n\t"
    "s_cmp_ge_u32 s69, 125\n\t"
    "s_cselect_b32 s69, s44, s69\n\t"
    "s_barrier\n\t"
    "s_load_dwordx2 s[72:73], %6, 0\n\t"
    "s_load_dwordx2 s[88:89], %7, 0\n\t"
    "s_load_dword s91, %8, 0\n\t"
    "s_mul_i32 m0, s69, 1040\n\t"
    "s_lshl_b32 s44, s69, 11\n\t"
    "buffer_load_dwordx4 v126, %15, s44 offen lds\n\t"
    "s_add_u32 s69, s69, 2\n\t"
    "s_sub_u32 s44, s69, 125\n\t"
    "s_cmp_ge_u32 s69, 125\n\t"
    "s_cselect_b32 s69, s44, s69\n\t"
    "s_mul_i32 m0, s69, 1040\n\t"
    "s_lshl_b32 s44, s69, 11\n\t"
    "buffer_load_dwordx4 v126, %15, s44 offen lds\n\t"
    "s_add_u32 s69, s69, 2\n\t"
    "s_sub_u32 s44, s69, 125\n\t"
    "s_cmp_ge_u32 s69, 125\n\t"
    "s_cselect_b32 s69, s44, s69\n\t"
    "s_mul_i32 m0, s69, 1040\n\t"
    "s_lshl_b32 s44, s69, 11\n\t"
    "buffer_load_dwordx4 v126, %15, s44 offen lds\n\t"
    "s_add_u32 s69, s69, 2\n\t"
    "s_sub_u32 s44, s69, 125\n\t"
    "s_cmp_ge_u32 s69, 125\n\t"
    "s_cselect_b32 s69, s44, s69\n\t"
    "s_mul_i32 m0, s69, 1040\n\t"
    "s_lshl_b32 s44, s69, 11\n\t"
    "buffer_load_dwordx4 v126, %15, s44 offen lds\n\t"
    "s_add_u32 s69, s69, 2\n\t"
    "s_sub_u32 s44, s69, 125\n\t"
    "s_cmp_ge_u32 s69, 125\n\t"
    "s_cselect_b32 s69, s44, s69\n\t"
    "s_mul_i32 m0, s69, 1040\n\t"
    "s_lshl_b32 s44, s69, 11\n\t"
    "buffer_load_dwordx4 v126, %15, s44 offen lds\n\t"
    "s_add_u32 s69, s69, 2\n\t"
    "s_sub_u32 s44, s69, 125\n\t"
    "s_cmp_ge_u32 s69, 125\n\t"
    "s_cselect_b32 s69, s44, s69\n\t"
    "s_mul_i32 m0, s69, 1040\n\t"
    "s_lshl_b32 s44, s69, 11\n\t"
    "buffer_load_dwordx4 v126, %15, s44 offen lds\n\t"
    "s_add_u32 s69, s69, 2\n\t"
    "s_sub_u32 s44, s69, 125\n\t"
    "s_cmp_ge_u32 s69, 125\n\t"
    "s_cselect_b32 s69, s44, s69\n\t"
    "s_mul_i32 m0, s69, 1040\n\t"
    "s_lshl_b32 s44, s69, 11\n\t"
    "buffer_load_dwordx4 v126, %15, s44 offen lds\n\t"
    "s_add_u32 s69, s69, 2\n\t"
    "s_sub_u32 s44, s69, 125\n\t"
    "s_cmp_ge_u32 s69, 125\n\t"
    "s_cselect_b32 s69, s44, s69\n\t"
    "s_mul_i32 m0, s69, 1040\n\t"
    "s_lshl_b32 s44, s69, 11\n\t"
    "buffer_load_dwordx4 v126, %15, s44 offen lds\n\t"
    "s_add_u32 s69, s69, 2\n\t"
    "s_sub_u32 s44, s69, 125\n\t"
    "s_cmp_ge_u32 s69, 125\n\t"
    "s_cselect_b32 s69, s44, s69\n\t"
    "s_mul_i32 m0, s69, 1040\n\t"
    "s_lshl_b32 s44, s69, 11\n\t"
    "buffer_load_dwordx4 v126, %15, s44 offen lds\n\t"
    "s_add_u32 s69, s69, 2\n\t"
    "s_sub_u32 s44, s69, 125\n\t"
    "s_cmp_ge_u32 s69, 125\n\t"
    "s_cselect_b32 s69, s44, s69\n\t"
    "s_mul_i32 m0, s69, 1040\n\t"
    "s_lshl_b32 s44, s69, 11\n\t"
    "buffer_load_dwordx4 v126, %15, s44 offen lds\n\t"
    "s_add_u32 s69, s69, 2\n\t"
    "s_sub_u32 s44, s69, 125\n\t"
    "s_cmp_ge_u32 s69, 125\n\t"
    "s_cselect_b32 s69, s44, s69\n\t"
    "v_bfe_u32 v154, v147, 2, 2\n\t"
    "v_and_b32 v155, 3, v147\n\t"
    "v_mul_u32_u24 v154, 25, v154\n\t"
    "v_mad_u32_u24 v154, v155, 6, v154\n\t"
    "v_lshlrev_b32 v153, 2, v154\n\t"
    "s_and_b32 s53, s48, 0xff\n\t"
    "s_bfe_u32 s55, s48, 0x80008\n\t"
    "s_add_u32 s54, s53, s55\n\t"
    "s_add_u32 s55, s54, s55\n\t"
    "s_lshr_b32 s64, s48, 16\n\t"
    "v_cmp_eq_u32 s[62:63], 0, v155\n\t"
    "v_and_b32 v151, 48, v147\n\t"
    "v_or_b32 v155, 0x100000, v151\n\t"
    "v_cndmask_b32 v152, v155, v151, s[62:63]\n\t"
    "s_mul_i32 m0, s69, 1040\n\t"
    "s_lshl_b32 s44, s69, 11\n\t"
    "buffer_load_dwordx4 v126, %15, s44 offen lds\n\t"
    "s_add_u32 s69, s69, 2\n\t"
    "s_sub_u32 s44, s69, 125\n\t"
    "s_cmp_ge_u32 s69, 125\n\t"
    "s_cselect_b32 s69, s44, s69\n\t"
    "s_mul_i32 m0, s69, 1040\n\t"
    "s_lshl_b32 s44, s69, 11\n\t"
    "buffer_load_dwordx4 v126, %15, s44 offen lds\n\t"
    "s_add_u32 s69, s69, 2\n\t"
    "s_sub_u32 s44, s69, 125\n\t"
    "s_cmp_ge_u32 s69, 125\n\t"
    "s_cselect_b32 s69, s44, s69\n\t"
    "s_mul_i32 m0, s69, 1040\n\t"
    "s_lshl_b32 s44, s69, 11\n\t"
    "buffer_load_dwordx4 v126, %15, s44 offen lds\n\t"
    "s_add_u32 s69, s69, 2\n\t"
    "s_sub_u32 s44, s69, 125\n\t"
    "s_cmp_ge_u32 s69, 125\n\t"
    "s_cselect_b32 s69, s44, s69\n\t"
    "s_mul_i32 m0, s69, 1040\n\t"
    "s_lshl_b32 s44, s69, 11\n\t"
    "buffer_load_dwordx4 v126, %15, s44 offen lds\n\t"
    "s_add_u32 s69, s69, 2\n\t"
    "s_sub_u32 s44, s69, 125\n\t"
    "s_cmp_ge_u32 s69, 125\n\t"
    "s_cselect_b32 s69, s44, s69\n\t"
    "s_mul_i32 m0, s69, 1040\n\t"
    "s_lshl_b32 s44, s69, 11\n\t"
    "buffer_load_dwordx4 v126, %15, s44 offen lds\n\t"
    "s_add_u32 s69, s69, 2\n\t"
    "s_sub_u32 s44, s69, 125\n\t"
    "s_cmp_ge_u32 s69, 125\n\t"
    "s_cselect_b32 s69, s44, s69\n\t"
    "s_mul_i32 m0, s69, 1040\n\t"
    "s_lshl_b32 s44, s69, 11\n\t"
    "buffer_load_dwordx4 v126, %15, s44 offen lds\n\t"
    "s_add_u32 s69, s69, 2\n\t"
    "s_sub_u32 s44, s69, 125\n\t"
    "s_cmp_ge_u32 s69, 125\n\t"
    "s_cselect_b32 s69, s44, s69\n\t"
    "s_mul_i32 m0, s69, 1040\n\t"
    "s_lshl_b32 s44, s69, 11\n\t"
    "buffer_load_dwordx4 v126, %15, s44 offen lds\n\t"
    "s_add_u32 s69, s69, 2\n\t"
    "s_sub_u32 s44, s69, 125\n\t"
    "s_cmp_ge_u32 s69, 125\n\t"
    "s_cselect_b32 s69, s44, s69\n\t"
    "s_mul_i32 m0, s69, 1040\n\t"
    "s_lshl_b32 s44, s69, 11\n\t"
    "buffer_load_dwordx4 v126, %15, s44 offen lds\n\t"
    "s_add_u32 s69, s69, 2\n\t"
    "s_sub_u32 s44, s69, 125\n\t"
    "s_cmp_ge_u32 s69, 125\n\t"
    "s_cselect_b32 s69, s44, s69\n\t"
    "s_mul_i32 m0, s69, 1040\n\t"
    "s_lshl_b32 s44, s69, 11\n\t"
    "buffer_load_dwordx4 v126, %15, s44 offen lds\n\t"
    "s_add_u32 s69, s69, 2\n\t"
    "s_sub_u32 s44, s69, 125\n\t"
    "s_cmp_ge_u32 s69, 125\n\t"
    "s_cselect_b32 s69, s44, s69\n\t"
    "s_mul_i32 m0, s69, 1040\n\t"
    "s_lshl_b32 s44, s69, 11\n\t"
    "buffer_load_dwordx4 v126, %15, s44 offen lds\n\t"
    "s_add_u32 s69, s69, 2\n\t"
    "s_sub_u32 s44, s69, 125\n\t"
    "s_cmp_ge_u32 s69, 125\n\t"
    "s_cselect_b32 s69, s44, s69\n\t"
    "s_mul_i32 m0, s69, 1040\n\t"
    "s_lshl_b32 s44, s69, 11\n\t"
    "buffer_load_dwordx4 v126, %15, s44 offen lds\n\t"
    "s_add_u32 s69, s69, 2\n\t"
    "s_sub_u32 s44, s69, 125\n\t"
    "s_cmp_ge_u32 s69, 125\n\t"
    "s_cselect_b32 s69, s44, s69\n\t"
    "s_mul_i32 m0, s69, 1040\n\t"
    "s_lshl_b32 s44, s69, 11\n\t"
    "buffer_load_dwordx4 v126, %15, s44 offen lds\n\t"
    "s_add_u32 s69, s69, 2\n\t"
    "s_sub_u32 s44, s69, 125\n\t"
    "s_cmp_ge_u32 s69, 125\n\t"
    "s_cselect_b32 s69, s44, s69\n\t"
    "s_mul_i32 m0, s69, 1040\n\t"
    "s_lshl_b32 s44, s69, 11\n\t"
    "buffer_load_dwordx4 v126, %15, s44 offen lds\n\t"
    "s_add_u32 s69, s69, 2\n\t"
    "s_sub_u32 s44, s69, 125\n\t"
    "s_cmp_ge_u32 s69, 125\n\t"
    "s_cselect_b32 s69, s44, s69\n\t"
    "s_mul_i32 m0, s69, 1040\n\t"
    "s_lshl_b32 s44, s69, 11\n\t"
    "buffer_load_dwordx4 v126, %15, s44 offen lds\n\t"
    "s_add_u32 s69, s69, 2\n\t"
    "s_sub_u32 s44, s69, 125\n\t"
    "s_cmp_ge_u32 s69, 125\n\t"
    "s_cselect_b32 s69, s44, s69\n\t"
    "s_mul_i32 m0, s69, 1040\n\t"
    "s_lshl_b32 s44, s69, 11\n\t"
    "buffer_load_dwordx4 v126, %15, s44 offen lds\n\t"
    "s_add_u32 s69, s69, 2\n\t"
    "s_sub_u32 s44, s69, 125\n\t"
    "s_cmp_ge_u32 s69, 125\n\t"
    "s_cselect_b32 s69, s44, s69\n\t"
    "s_mul_i32 m0, s69, 1040\n\t"
    "s_lshl_b32 s44, s69, 11\n\t"
    "buffer_load_dwordx4 v126, %15, s44 offen lds\n\t"
    "s_add_u32 s69, s69, 2\n\t"
    "s_sub_u32 s44, s69, 125\n\t"
    "s_cmp_ge_u32 s69, 125\n\t"
    "s_cselect_b32 s69, s44, s69\n\t"
    "s_mul_i32 m0, s69, 1040\n\t"
    "s_lshl_b32 s44, s69, 11\n\t"
    "buffer_load_dwordx4 v126, %15, s44 offen lds\n\t"
    "s_add_u32 s69, s69, 2\n\t"
    "s_sub_u32 s44, s69, 125\n\t"
    "s_cmp_ge_u32 s69, 125\n\t"
    "s_cselect_b32 s69, s44, s69\n\t"
    "s_mul_i32 m0, s69, 1040\n\t"
    "s_lshl_b32 s44, s69, 11\n\t"
    "buffer_load_dwordx4 v126, %15, s44 offen lds\n\t"
    "s_add_u32 s69, s69, 2\n\t"
    "s_sub_u32 s44, s69, 125\n\t"
    "s_cmp_ge_u32 s69, 125\n\t"
    "s_cselect_b32 s69, s44, s69\n\t"
    "s_mul_i32 m0, s69, 1040\n\t"
    "s_lshl_b32 s44, s69, 11\n\t"
    "buffer_load_dwordx4 v126, %15, s44 offen lds\n\t"
    "s_add_u32 s69, s69, 2\n\t"
    "s_sub_u32 s44, s69, 125\n\t"
    "s_cmp_ge_u32 s69, 125\n\t"
    "s_cselect_b32 s69, s44, s69\n\t"
    "s_mul_i32 m0, s69, 1040\n\t"
    "s_lshl_b32 s44, s69, 11\n\t"
    "buffer_load_dwordx4 v126, %15, s44 offen lds\n\t"
    "s_add_u32 s69, s69, 2\n\t"
    "s_sub_u32 s44, s69, 125\n\t"
    "s_cmp_ge_u32 s69, 125\n\t"
    "s_cselect_b32 s69, s44, s69\n\t"
    "s_mul_i32 m0, s69, 1040\n\t"
    "s_lshl_b32 s44, s69, 11\n\t"
    "buffer_load_dwordx4 v126, %15, s44 offen lds\n\t"
    "s_add_u32 s69, s69, 2\n\t"
    "s_sub_u32 s44, s69, 125\n\t"
    "s_cmp_ge_u32 s69, 125\n\t"
    "s_cselect_b32 s69, s44, s69\n\t"
    "s_mul_i32 m0, s69, 1040\n\t"
    "s_lshl_b32 s44, s69, 11\n\t"
    "buffer_load_dwordx4 v126, %15, s44 offen lds\n\t"
    "s_add_u32 s69, s69, 2\n\t"
    "s_sub_u32 s44, s69, 125\n\t"
    "s_cmp_ge_u32 s69, 125\n\t"
    "s_cselect_b32 s69, s44, s69\n\t"
    "s_mul_i32 m0, s69, 1040\n\t"
    "s_lshl_b32 s44, s69, 11\n\t"
    "buffer_load_dwordx4 v126, %15, s44 offen lds\n\t"
    "s_add_u32 s69, s69, 2\n\t"
    "s_sub_u32 s44, s69, 125\n\t"
    "s_cmp_ge_u32 s69, 125\n\t"
    "s_cselect_b32 s69, s44, s69\n\t"
    "s_mul_i32 m0, s69, 1040\n\t"
    "s_lshl_b32 s44, s69, 11\n\t"
    "buffer_load_dwordx4 v126, %15, s44 offen lds\n\t"
    "s_add_u32 s69, s69, 2\n\t"
    "s_sub_u32 s44, s69, 125\n\t"
    "s_cmp_ge_u32 s69, 125\n\t"
    "s_cselect_b32 s69, s44, s69\n\t"
    "s_mul_i32 m0, s69, 1040\n\t"
    "s_lshl_b32 s44, s69, 11\n\t"
    "buffer_load_dwordx4 v126, %15, s44 offen lds\n\t"
    "s_add_u32 s69, s69, 2\n\t"
    "s_sub_u32 s44, s69, 125\n\t"
    "s_cmp_ge_u32 s69, 125\n\t"
    "s_cselect_b32 s69, s44, s69\n\t"
    "s_mul_i32 m0, s69, 1040\n\t"
    "s_lshl_b32 s44, s69, 11\n\t"
    "buffer_load_dwordx4 v126, %15, s44 offen lds\n\t"
    "s_add_u32 s69, s69, 2\n\t"
    "s_sub_u32 s44, s69, 125\n\t"
    "s_cmp_ge_u32 s69, 125\n\t"
    "s_cselect_b32 s69, s44, s69\n\t"
    "s_mul_i32 m0, s69, 1040\n\t"
    "s_lshl_b32 s44, s69, 11\n\t"
    "buffer_load_dwordx4 v126, %15, s44 offen lds\n\t"
    "s_add_u32 s69, s69, 2\n\t"
    "s_sub_u32 s44, s69, 125\n\t"
    "s_cmp_ge_u32 s69, 125\n\t"
    "s_cselect_b32 s69, s44, s69\n\t"
    "s_mul_i32 m0, s69, 1040\n\t"
    "s_lshl_b32 s44, s69, 11\n\t"
    "buffer_load_dwordx4 v126, %15, s44 offen lds\n\t"
    "s_add_u32 s69, s69, 2\n\t"
    "s_sub_u32 s44, s69, 125\n\t"
    "s_cmp_ge_u32 s69, 125\n\t"
    "s_cselect_b32 s69, s44, s69\n\t"
    "s_mul_i32 m0, s69, 1040\n\t"
    "s_lshl_b32 s44, s69, 11\n\t"
    "buffer_load_dwordx4 v126, %15, s44 offen lds\n\t"
    "s_add_u32 s69, s69, 2\n\t"
    "s_sub_u32 s44, s69, 125\n\t"
    "s_cmp_ge_u32 s69, 125\n\t"
    "s_cselect_b32 s69, s44, s69\n\t"
    "s_mul_i32 m0, s69, 1040\n\t"
    "s_lshl_b32 s44, s69, 11\n\t"
    "buffer_load_dwordx4 v126, %15, s44 offen lds\n\t"
    "s_add_u32 s69, s69, 2\n\t"
    "s_sub_u32 s44, s69, 125\n\t"
    "s_cmp_ge_u32 s69, 125\n\t"
    "s_cselect_b32 s69, s44, s69\n\t"
    "L_ab_%=:\n\t"
    "s_mul_i32 s66, s53, 400\n\t"
    "buffer_load_dwordx3 v[128:130], v153, %0, s66 offen\n\t"
    "buffer_load_dwordx4 v[132:135], v153, %0, s66 offen offset:12\n\t"
    "s_mul_i32 s66, s54, 400\n\t"
    "buffer_load_dwordx3 v[136:138], v153, %0, s66 offen\n\t"
    "buffer_load_dwordx4 v[140:143], v153, %0, s66 offen offset:12\n\t"
    "v_add_u32 v95, 140288, v122\n\t"
    "v_mbcnt_lo_u32_b32 v100, -1, 0\n\t"
    "v_mbcnt_hi_u32_b32 v100, -1, v100\n\t"
    "s_lshl_b32 s61, %16, 15\n\t"
    "s_add_u32 s61, s61, 16515072\n\t"
    "v_lshlrev_b32 v127, 4, v100\n\t"
    "s_lshr_b32 s37, %14, 4\n\t"
    "s_and_b32 s39, %14, 7\n\t"
    "v_and_b32 v101, 15, v100\n\t"
    "s_lshl_b32 s39, s39, 4\n\t"
    "s_add_u32 s39, s39, s37\n\t"
    "s_mul_i32 s65, %16, 125\n\t"
    "v_add_u32 v155, s65, v101\n\t"
    "s_mul_i32 s63, s39, 102400\n\t"
    "s_add_u32 s63, s63, 3407872\n\t"
    "v_lshrrev_b32 v102, 2, v101\n\t"
    "s_and_b32 s37, s37, 7\n\t"
    "s_lshl_b32 s37, s37, 11\n\t"
    "v_lshrrev_b32 v103, 3, v100\n\t"
    "s_lshl_b32 s36, s39, 16\n\t"
    "s_bfe_u32 s65, %14, 0x10003\n\t"
    "v_and_b32 v103, 2, v103\n\t"
    "s_lshl_b32 s38, s65, 15\n\t"
    "s_add_u32 s36, s36, s38\n\t"
    "v_and_b32 v104, 1, v100\n\t"
    "s_lshl_b32 s38, %16, 12\n\t"
    "s_add_u32 s36, s36, s38\n\t"
    "v_lshlrev_b32 v104, 4, v104\n\t"
    "s_add_u32 s36, s36, 17825792\n\t"
    "v_mov_b32 v101, 0x3f80\n\t"
    "v_lshlrev_b32 v104, v104, v101\n\t"
    "v_cmp_eq_u32 vcc, v102, v103\n\t"
    "s_waitcnt lgkmcnt(0)\n\t"
    "s_lshr_b32 s39, s91, %9\n\t"
    "s_lshl_b32 s39, s39, 16\n\t"
    "v_cndmask_b32 v120, 0, v104, vcc\n\t"
    "v_cndmask_b32 v121, 0, v104, vcc\n\t"
    "v_add_u32 v103, 1, v103\n\t"
    "v_cmp_eq_u32 vcc, v102, v103\n\t"
    "s_lshl_b32 s40, s88, 16\n\t"
    "s_and_b32 s41, s88, 0xffff0000\n\t"
    "v_cndmask_b32 v122, 0, v104, vcc\n\t"
    "v_cndmask_b32 v123, 0, v104, vcc\n\t"
    "v_and_b32 v101, 2, v100\n\t"
    "v_mov_b32 v102, 0x44444444\n\t"
    "v_mov_b32 v103, 0xeeeeeeee\n\t"
    "v_cmp_eq_u32 vcc, 0, v101\n\t"
    "s_lshl_b32 s42, s89, 16\n\t"
    "s_and_b32 s43, s89, 0xffff0000\n\t"
    "v_cndmask_b32 v124, v103, v102, vcc\n\t"
    "v_lshrrev_b32 v101, 4, v100\n\t"
    "v_mul_u32_u24 v101, 8000, v101\n\t"
    "v_lshl_add_u32 v125, v155, 3, v101\n\t"
    "v_add_u32 v125, 64000, v125\n\t"
    "s_lshl_b32 s70, s72, 16\n\t"
    "s_and_b32 s71, s72, 0xffff0000\n\t"
    "s_lshl_b32 s72, s73, 16\n\t"
    "s_and_b32 s73, s73, 0xffff0000\n\t"
    "s_mov_b32 s67, 0xffff0000\n\t"
    "s_and_b32 s65, %14, 8\n\t"
    "s_add_u32 s38, s65, %16\n\t"
    "s_mul_i32 s38, s38, 212992\n\t"
    "s_add_u32 s38, s38, 196608\n\t"
    "s_add_u32 s84, s37, s38\n\t"
    "s_add_u32 s37, s37, 0x800\n\t"
    "s_and_b32 s37, s37, 0x3800\n\t"
    "s_add_u32 s85, s37, s38\n\t"
    "s_add_u32 s37, s37, 0x800\n\t"
    "s_and_b32 s37, s37, 0x3800\n\t"
    "s_add_u32 s86, s37, s38\n\t"
    "s_add_u32 s37, s37, 0x800\n\t"
    "s_and_b32 s37, s37, 0x3800\n\t"
    "s_add_u32 s87, s37, s38\n\t"
    "s_add_u32 s37, s37, 0x800\n\t"
    "s_and_b32 s37, s37, 0x3800\n\t"
    "s_add_u32 s88, s37, s38\n\t"
    "s_add_u32 s37, s37, 0x800\n\t"
    "s_and_b32 s37, s37, 0x3800\n\t"
    "s_add_u32 s89, s37, s38\n\t"
    "s_add_u32 s37, s37, 0x800\n\t"
    "s_and_b32 s37, s37, 0x3800\n\t"
    "s_add_u32 s90, s37, s38\n\t"
    "s_add_u32 s37, s37, 0x800\n\t"
    "s_and_b32 s37, s37, 0x3800\n\t"
    "s_add_u32 s91, s37, s38\n\t"
    "s_mul_i32 s94, s65, 425984\n\t"
    "s_sub_u32 s94, s94, 1703936\n\t"
    "s_sub_u32 s95, 0x8000, s94\n\t"
    "s_lshl_b32 s97, s65, 11\n\t"
    "s_sub_u32 s97, s97, 229376\n\t"
    "s_mov_b32 s92, 64000\n\t"
    "s_mov_b32 s93, -64000\n\t"
    "s_mov_b32 s96, 0x2000\n\t"
    "s_mov_b32 s62, 2\n\t"
    "v_rcp_f32 v101, s40\n\t"
    "v_rcp_f32 v102, s41\n\t"
    "v_rcp_f32 v103, s42\n\t"
    "v_rcp_f32 v104, s43\n\t"
    "v_mov_b32 v100, 0x7f7fffff\n\t"
    "v_med3_f32 v101, v101, -v100, v100\n\t"
    "v_med3_f32 v102, v102, -v100, v100\n\t"
    "v_med3_f32 v103, v103, -v100, v100\n\t"
    "v_med3_f32 v104, v104, -v100, v100\n\t"
    "v_readfirstlane_b32 s40, v101\n\t"
    "v_readfirstlane_b32 s41, v102\n\t"
    "v_readfirstlane_b32 s42, v103\n\t"
    "v_readfirstlane_b32 s43, v104\n\t"
    "L_asp8_%=:\n\t"
    "ds_read_b32 v119, v117 offset:4\n\t"
    "s_waitcnt lgkmcnt(0)\n\t"
    "v_readfirstlane_b32 s46, v119\n\t"
    "s_nop 0\n\t"
    "s_cmp_ge_u32 s46, 4\n\t"
    "s_cbranch_scc1 L_ago8_%=\n\t"
    "s_sleep 1\n\t"
    "s_branch L_asp8_%=\n\t"
    "L_ago8_%=:\n\t"
    "ds_read_b128 v[0:3], v95 offset:0\n\t"
    "ds_read_b128 v[4:7], v95 offset:1024\n\t"
    "ds_read_b128 v[8:11], v95 offset:2048\n\t"
    "ds_read_b128 v[12:15], v95 offset:3072\n\t"
    "ds_read_b128 v[16:19], v95 offset:4096\n\t"
    "ds_read_b128 v[20:23], v95 offset:5120\n\t"
    "ds_read_b128 v[24:27], v95 offset:6144\n\t"
    "ds_read_b128 v[28:31], v95 offset:7168\n\t"
    "ds_read_b128 v[32:35], v95 offset:8192\n\t"
    "ds_read_b128 v[36:39], v95 offset:9216\n\t"
    "ds_read_b128 v[40:43], v95 offset:10240\n\t"
    "ds_read_b128 v[44:47], v95 offset:11264\n\t"
    "ds_read_b128 v[48:51], v95 offset:12288\n\t"
    "ds_read_b128 v[52:55], v95 offset:13312\n\t"
    "ds_read_b128 v[56:59], v95 offset:14336\n\t"
    "ds_read_b128 v[60:63], v95 offset:15360\n\t"
    "ds_read_b128 v[64:67], v95 offset:16384\n\t"
    "ds_read_b128 v[68:71], v95 offset:17408\n\t"
    "ds_read_b128 v[72:75], v95 offset:18432\n\t"
    "ds_read_b128 v[76:79], v95 offset:19456\n\t"
    "ds_read_b128 v[80:83], v95 offset:20480\n\t"
    "ds_read_b128 v[84:87], v95 offset:21504\n\t"
    "ds_read_b128 v[88:91], v95 offset:22528\n\t"
    "s_waitcnt vmcnt(4)\n\t"
    "ds_add_u32 v117, v118 offset:8\n\t"
    "s_waitcnt vmcnt(0)\n\t"
    "s_branch L_awd_%=\n\t"
    "s_waitcnt vmcnt(4)\n\t"
    "ds_add_u32 v117, v118 offset:8\n\t"
    "s_branch L_await_%=\n\t"
    "L_aside_%=:\n\t"
    "s_setprio 3\n\t"
    "v_cmp_gt_u32 vcc, 48, v147\n\t"
    "v_mov_b32 v133, 0x40000000\n\t"
    "v_lshrrev_b32 v134, 3, v147\n\t"
    "v_bfe_u32 v135, v147, 2, 1\n\t"
    "v_and_b32 v130, 3, v147\n\t"
    "v_lshl_add_u32 v132, v135, 5, v130\n\t"
    "v_mul_u32_u24 v64, 400, v132\n\t"
    "v_lshl_add_u32 v64, v134, 5, v64\n\t"
    "s_movk_i32 s50, 0\n\t"
    "buffer_load_dwordx4 v[0:3], v64, %0, s50 offen offset:16\n\t"
    "buffer_load_dwordx4 v[4:7], v64, %0, s50 offen offset:32\n\t"
    "v_lshlrev_b32 v66, 14, v134\n\t"
    "v_lshl_add_u32 v66, v132, 4, v66\n\t"
    "v_lshlrev_b32 v68, 8, v134\n\t"
    "v_lshl_add_u32 v68, v135, 2, v68\n\t"
    "v_add_u32 v68, 130048, v68\n\t"
    "v_cndmask_b32 v68, v133, v68, vcc\n\t"
    "v_cndmask_b32 v66, v133, v66, vcc\n\t"
    "v_lshrrev_b32 v134, 2, v147\n\t"
    "v_bfe_u32 v135, v147, 1, 1\n\t"
    "v_and_b32 v130, 1, v147\n\t"
    "v_lshl_add_u32 v132, v135, 5, v130\n\t"
    "v_mul_u32_u24 v65, 400, v132\n\t"
    "v_lshl_add_u32 v65, v134, 5, v65\n\t"
    "v_lshlrev_b32 v67, 14, v134\n\t"
    "v_lshl_add_u32 v67, v132, 4, v67\n\t"
    "v_lshlrev_b32 v69, 8, v134\n\t"
    "v_lshl_add_u32 v69, v135, 2, v69\n\t"
    "v_add_u32 v69, 130048, v69\n\t"
    "v_cndmask_b32 v69, v133, v69, vcc\n\t"
    "v_cndmask_b32 v67, v133, v67, vcc\n\t"
    "s_barrier\n\t"
    "s_load_dwordx2 s[72:73], %6, 0\n\t"
    "s_load_dwordx2 s[88:89], %7, 0\n\t"
    "s_load_dword s91, %8, 0\n\t"
    "v_lshlrev_b32 v128, 4, v147\n\t"
    "v_add_u32 v128, 133120, v128\n\t"
    "v_and_b32 v130, 1, v147\n\t"
    "v_cndmask_b32 v128, v133, v128, vcc\n\t"
    "v_mul_u32_u24 v127, 7, v130\n\t"
    "v_cmp_eq_u32 vcc, 1, v130\n\t"
    "v_mov_b32 v126, 0x07060100\n\t"
    "v_mov_b32 v133, 0x05040100\n\t"
    "v_mov_b32 v136, 1\n\t"
    "v_cndmask_b32 v126, v126, v133, vcc\n\t"
    "s_mov_b32 s36, 0\n\t"
    "s_mov_b32 s37, 8000\n\t"
    "s_mov_b32 s38, 16000\n\t"
    "s_mov_b32 s39, 24000\n\t"
    "s_mov_b32 s40, 32000\n\t"
    "s_mov_b32 s41, 40000\n\t"
    "s_mov_b32 s42, 48000\n\t"
    "s_mov_b32 s43, 56000\n\t"
    "s_movk_i32 s44, 0xf8\n\t"
    "s_mov_b32 s45, 0x01010000\n\t"
    "s_mov_b32 s46, 0x020c000c\n\t"
    "s_lshr_b32 s49, %14, 4\n\t"
    "s_mul_i32 s49, s49, 212992\n\t"
    "s_and_b32 s50, %14, 15\n\t"
    "s_lshl_b32 s50, s50, 10\n\t"
    "s_add_u32 s49, s49, s50\n\t"
    "v_lshrrev_b32 v134, 4, v147\n\t"
    "v_bfe_u32 v135, v147, 2, 2\n\t"
    "v_and_b32 v130, 3, v147\n\t"
    "v_lshlrev_b32 v137, 17, v134\n\t"
    "v_lshl_add_u32 v137, v135, 10, v137\n\t"
    "v_lshl_add_u32 v137, v130, 4, v137\n\t"
    "v_add_u32 v138, 0x1000, v137\n\t"
    "v_mul_u32_u24 v141, 0x210, v134\n\t"
    "v_lshl_add_u32 v141, v135, 6, v141\n\t"
    "v_lshl_add_u32 v141, v130, 4, v141\n\t"
    "v_add_u32 v141, 134656, v141\n\t"
    "v_mul_u32_u24 v142, 0x210, v130\n\t"
    "v_lshl_add_u32 v142, v135, 6, v142\n\t"
    "v_lshl_add_u32 v142, v134, 3, v142\n\t"
    "v_add_u32 v142, 134656, v142\n\t"
    "s_mov_b32 s50, 102400\n\t"
    "v_mul_u32_u24 v139, s50, v134\n\t"
    "v_and_b32 v132, 15, v147\n\t"
    "v_lshl_add_u32 v139, v132, 4, v139\n\t"
    "v_add_u32 v140, 409600, v139\n\t"
    "s_lshr_b32 s50, %14, 4\n\t"
    "s_lshl_b32 s47, s50, 13\n\t"
    "s_lshl_b32 s48, s50, 8\n\t"
    "s_and_b32 s50, %14, 7\n\t"
    "s_lshl_b32 s51, s50, 7\n\t"
    "s_add_u32 s47, s47, s51\n\t"
    "s_mul_i32 s51, s50, 1638400\n\t"
    "s_add_u32 s48, s48, s51\n\t"
    "s_bfe_u32 s50, %14, 0x10003\n\t"
    "s_lshl_b32 s51, s50, 19\n\t"
    "s_add_u32 s47, s47, s51\n\t"
    "s_lshl_b32 s51, s50, 12\n\t"
    "s_add_u32 s48, s48, s51\n\t"
    "s_add_u32 s48, s48, 3407872\n\t"
    "s_waitcnt vmcnt(2)\n\t"
    "v_lshlrev_b32 v134, 20, v147\n\t"
    "v_add_u32 v134, 2068, v134\n\t"
    "ds_add_u32 v134, v136\n\t"
    "v_mov_b32 v110, 65664\n\t"
    "v_mov_b32 v111, 131136\n\t"
    "v_mov_b32 v112, 262176\n\t"
    "v_mov_b32 v113, 524304\n\t"
    "v_mov_b32 v114, 1048584\n\t"
    "v_mov_b32 v115, 2097156\n\t"
    "v_mov_b32 v116, 4194306\n\t"
    "v_mov_b32 v117, 8388609\n\t"
    "s_waitcnt vmcnt(0)\n\t"
    "v_lshl_add_u32 v76, v0, 3, s36\n\t"
    "v_lshl_add_u32 v77, v1, 3, s37\n\t"
    "v_lshl_add_u32 v78, v2, 3, s38\n\t"
    "v_lshl_add_u32 v79, v3, 3, s39\n\t"
    "v_lshl_add_u32 v80, v4, 3, s40\n\t"
    "v_lshl_add_u32 v81, v5, 3, s41\n\t"
    "v_lshl_add_u32 v82, v6, 3, s42\n\t"
    "v_lshl_add_u32 v83, v7, 3, s43\n\t"
    "v_and_or_b32 v92, v76, s44, v68\n\t"
    "ds_or_rtn_b32 v118, v92, v110\n\t"
    "v_and_or_b32 v93, v77, s44, v68\n\t"
    "ds_or_rtn_b32 v119, v93, v111\n\t"
    "v_and_or_b32 v94, v78, s44, v68\n\t"
    "ds_or_rtn_b32 v120, v94, v112\n\t"
    "v_and_or_b32 v95, v79, s44, v68\n\t"
    "ds_or_rtn_b32 v121, v95, v113\n\t"
    "v_and_or_b32 v96, v80, s44, v68\n\t"
    "ds_or_rtn_b32 v122, v96, v114\n\t"
    "v_and_or_b32 v97, v81, s44, v68\n\t"
    "ds_or_rtn_b32 v123, v97, v115\n\t"
    "v_and_or_b32 v98, v82, s44, v68\n\t"
    "ds_or_rtn_b32 v124, v98, v116\n\t"
    "v_and_or_b32 v99, v83, s44, v68\n\t"
    "ds_or_rtn_b32 v125, v99, v117\n\t"
    "v_lshl_or_b32 v164, v77, 16, v76\n\t"
    "v_lshl_or_b32 v165, v79, 16, v78\n\t"
    "v_lshl_or_b32 v166, v81, 16, v80\n\t"
    "v_lshl_or_b32 v167, v83, 16, v82\n\t"
    "s_movk_i32 s50, 0\n\t"
    "buffer_load_dwordx4 v[8:11], v64, %0, s50 offen offset:1616\n\t"
    "buffer_load_dwordx4 v[12:15], v64, %0, s50 offen offset:1632\n\t"
    "s_movk_i32 s50, 3200\n\t"
    "buffer_load_dwordx4 v[16:19], v64, %0, s50 offen offset:16\n\t"
    "buffer_load_dwordx4 v[20:23], v64, %0, s50 offen offset:32\n\t"
    "s_waitcnt vmcnt(2)\n\t"
    "v_lshl_add_u32 v84, v8, 3, s36\n\t"
    "v_lshl_add_u32 v85, v9, 3, s37\n\t"
    "v_lshl_add_u32 v86, v10, 3, s38\n\t"
    "v_lshl_add_u32 v87, v11, 3, s39\n\t"
    "v_lshl_add_u32 v88, v12, 3, s40\n\t"
    "v_lshl_add_u32 v89, v13, 3, s41\n\t"
    "v_lshl_add_u32 v90, v14, 3, s42\n\t"
    "v_lshl_add_u32 v91, v15, 3, s43\n\t"
    "s_waitcnt lgkmcnt(4)\n\t"
    "v_and_b32 v118, v118, v110\n\t"
    "v_perm_b32 v118, v118, v118, s46\n\t"
    "ds_or_b32 v92, v118\n\t"
    "v_and_b32 v119, v119, v111\n\t"
    "v_perm_b32 v119, v119, v119, s46\n\t"
    "ds_or_b32 v93, v119\n\t"
    "v_and_b32 v120, v120, v112\n\t"
    "v_perm_b32 v120, v120, v120, s46\n\t"
    "ds_or_b32 v94, v120\n\t"
    "v_and_b32 v121, v121, v113\n\t"
    "v_perm_b32 v121, v121, v121, s46\n\t"
    "ds_or_b32 v95, v121\n\t"
    "s_waitcnt lgkmcnt(4)\n\t"
    "v_and_b32 v122, v122, v114\n\t"
    "v_perm_b32 v122, v122, v122, s46\n\t"
    "ds_or_b32 v96, v122\n\t"
    "v_and_b32 v123, v123, v115\n\t"
    "v_perm_b32 v123, v123, v123, s46\n\t"
    "ds_or_b32 v97, v123\n\t"
    "v_and_b32 v124, v124, v116\n\t"
    "v_perm_b32 v124, v124, v124, s46\n\t"
    "ds_or_b32 v98, v124\n\t"
    "v_and_b32 v125, v125, v117\n\t"
    "v_perm_b32 v125, v125, v125, s46\n\t"
    "ds_or_b32 v99, v125\n\t"
    "buffer_store_dwordx4 v[164:167], v66, %12, s49 offen offset:0 sc1\n\t"
    "v_and_or_b32 v100, v84, s44, v68\n\t"
    "ds_read_b32 v100, v100\n\t"
    "v_and_or_b32 v101, v85, s44, v68\n\t"
    "ds_read_b32 v101, v101\n\t"
    "v_and_or_b32 v102, v86, s44, v68\n\t"
    "ds_read_b32 v102, v102\n\t"
    "v_and_or_b32 v103, v87, s44, v68\n\t"
    "ds_read_b32 v103, v103\n\t"
    "v_and_or_b32 v104, v88, s44, v68\n\t"
    "ds_read_b32 v104, v104\n\t"
    "v_and_or_b32 v105, v89, s44, v68\n\t"
    "ds_read_b32 v105, v105\n\t"
    "v_and_or_b32 v106, v90, s44, v68\n\t"
    "ds_read_b32 v106, v106\n\t"
    "v_and_or_b32 v107, v91, s44, v68\n\t"
    "ds_read_b32 v107, v107\n\t"
    "s_waitcnt vmcnt(1)\n\t"
    "v_lshl_add_u32 v76, v16, 3, s36\n\t"
    "v_lshl_add_u32 v77, v17, 3, s37\n\t"
    "v_lshl_add_u32 v78, v18, 3, s38\n\t"
    "v_lshl_add_u32 v79, v19, 3, s39\n\t"
    "v_lshl_add_u32 v80, v20, 3, s40\n\t"
    "v_lshl_add_u32 v81, v21, 3, s41\n\t"
    "v_lshl_add_u32 v82, v22, 3, s42\n\t"
    "v_lshl_add_u32 v83, v23, 3, s43\n\t"
    "s_movk_i32 s50, 3200\n\t"
    "buffer_load_dwordx4 v[24:27], v64, %0, s50 offen offset:1616\n\t"
    "buffer_load_dwordx4 v[28:31], v64, %0, s50 offen offset:1632\n\t"
    "s_movk_i32 s50, 6400\n\t"
    "buffer_load_dwordx4 v[32:35], v64, %0, s50 offen offset:16\n\t"
    "buffer_load_dwordx4 v[36:39], v64, %0, s50 offen offset:32\n\t"
    "v_mov_b32 v129, 0xffff0000\n\t"
    "s_waitcnt lgkmcnt(4)\n\t"
    "v_perm_b32 v100, v100, v84, v126\n\t"
    "v_perm_b32 v101, v101, v85, v126\n\t"
    "v_perm_b32 v102, v102, v86, v126\n\t"
    "v_perm_b32 v103, v103, v87, v126\n\t"
    "s_waitcnt lgkmcnt(0)\n\t"
    "v_perm_b32 v104, v104, v88, v126\n\t"
    "v_perm_b32 v105, v105, v89, v126\n\t"
    "v_perm_b32 v106, v106, v90, v126\n\t"
    "v_perm_b32 v107, v107, v91, v126\n\t"
    "v_max_u32 v108, v100, v101\n\t"
    "v_min_u32 v101, v100, v101\n\t"
    "v_max_u32 v100, v102, v103\n\t"
    "v_min_u32 v103, v102, v103\n\t"
    "v_max_u32 v102, v104, v105\n\t"
    "v_min_u32 v105, v104, v105\n\t"
    "v_max_u32 v104, v106, v107\n\t"
    "v_min_u32 v107, v106, v107\n\t"
    "v_max_u32 v106, v108, v100\n\t"
    "v_min_u32 v100, v108, v100\n\t"
    "v_max_u32 v108, v101, v103\n\t"
    "v_min_u32 v103, v101, v103\n\t"
    "v_max_u32 v101, v102, v104\n\t"
    "v_min_u32 v104, v102, v104\n\t"
    "v_max_u32 v102, v105, v107\n\t"
    "v_min_u32 v107, v105, v107\n\t"
    "v_bfi_b32 v130, v106, 0, v129\n\t"
    "v_cmp_eq_u32 vcc, 0, v130\n\t"
    "v_and_or_b32 v92, v106, s44, v68\n\t"
    "s_nop 0\n\t"
    "v_cndmask_b32 v130, v130, v129, vcc\n\t"
    "v_ffbl_b32 v131, v130\n\t"
    "v_and_b32 v134, 7, v131\n\t"
    "v_lshlrev_b32 v134, v134, s45\n\t"
    "v_xor_b32 v129, v129, v134\n\t"
    "v_xor_b32 v132, v131, v127\n\t"
    "v_bfi_b32 v130, v101, 0, v129\n\t"
    "v_cmp_eq_u32 vcc, 0, v130\n\t"
    "v_and_or_b32 v93, v101, s44, v68\n\t"
    "v_xor_b32 v133, 23, v132\n\t"
    "v_lshlrev_b32 v110, v132, v136\n\t"
    "v_and_b32 v132, 7, v132\n\t"
    "v_lshl_or_b32 v110, v136, v133, v110\n\t"
    "v_lshl_add_u32 v132, v132, 1, v128\n\t"
    "ds_or_rtn_b32 v118, v92, v110\n\t"
    "ds_write_b16 v132, v106\n\t"
    "v_cndmask_b32 v130, v130, v129, vcc\n\t"
    "v_ffbl_b32 v131, v130\n\t"
    "v_and_b32 v134, 7, v131\n\t"
    "v_lshlrev_b32 v134, v134, s45\n\t"
    "v_xor_b32 v129, v129, v134\n\t"
    "v_xor_b32 v132, v131, v127\n\t"
    "v_bfi_b32 v130, v108, 0, v129\n\t"
    "v_cmp_eq_u32 vcc, 0, v130\n\t"
    "v_and_or_b32 v94, v108, s44, v68\n\t"
    "v_xor_b32 v133, 23, v132\n\t"
    "v_lshlrev_b32 v111, v132, v136\n\t"
    "v_and_b32 v132, 7, v132\n\t"
    "v_lshl_or_b32 v111, v136, v133, v111\n\t"
    "v_lshl_add_u32 v132, v132, 1, v128\n\t"
    "ds_or_rtn_b32 v119, v93, v111\n\t"
    "ds_write_b16 v132, v101\n\t"
    "v_cndmask_b32 v130, v130, v129, vcc\n\t"
    "v_ffbl_b32 v131, v130\n\t"
    "v_and_b32 v134, 7, v131\n\t"
    "v_lshlrev_b32 v134, v134, s45\n\t"
    "v_xor_b32 v129, v129, v134\n\t"
    "v_xor_b32 v132, v131, v127\n\t"
    "v_bfi_b32 v130, v100, 0, v129\n\t"
    "v_cmp_eq_u32 vcc, 0, v130\n\t"
    "v_and_or_b32 v95, v100, s44, v68\n\t"
    "v_xor_b32 v133, 23, v132\n\t"
    "v_lshlrev_b32 v112, v132, v136\n\t"
    "v_and_b32 v132, 7, v132\n\t"
    "v_lshl_or_b32 v112, v136, v133, v112\n\t"
    "v_lshl_add_u32 v132, v132, 1, v128\n\t"
    "ds_or_rtn_b32 v120, v94, v112\n\t"
    "ds_write_b16 v132, v108\n\t"
    "v_cndmask_b32 v130, v130, v129, vcc\n\t"
    "v_ffbl_b32 v131, v130\n\t"
    "v_and_b32 v134, 7, v131\n\t"
    "v_lshlrev_b32 v134, v134, s45\n\t"
    "v_xor_b32 v129, v129, v134\n\t"
    "v_xor_b32 v132, v131, v127\n\t"
    "v_bfi_b32 v130, v102, 0, v129\n\t"
    "v_cmp_eq_u32 vcc, 0, v130\n\t"
    "v_and_or_b32 v96, v102, s44, v68\n\t"
    "v_xor_b32 v133, 23, v132\n\t"
    "v_lshlrev_b32 v113, v132, v136\n\t"
    "v_and_b32 v132, 7, v132\n\t"
    "v_lshl_or_b32 v113, v136, v133, v113\n\t"
    "v_lshl_add_u32 v132, v132, 1, v128\n\t"
    "ds_or_rtn_b32 v121, v95, v113\n\t"
    "ds_write_b16 v132, v100\n\t"
    "v_cndmask_b32 v130, v130, v129, vcc\n\t"
    "v_ffbl_b32 v131, v130\n\t"
    "v_and_b32 v134, 7, v131\n\t"
    "v_lshlrev_b32 v134, v134, s45\n\t"
    "v_xor_b32 v129, v129, v134\n\t"
    "v_xor_b32 v132, v131, v127\n\t"
    "s_waitcnt lgkmcnt(7)\n\t"
    "v_and_b32 v118, v118, v110\n\t"
    "v_perm_b32 v118, v118, v118, s46\n\t"
    "ds_or_b32 v92, v118\n\t"
    "v_bfi_b32 v130, v104, 0, v129\n\t"
    "v_cmp_eq_u32 vcc, 0, v130\n\t"
    "v_and_or_b32 v97, v104, s44, v68\n\t"
    "v_xor_b32 v133, 23, v132\n\t"
    "v_lshlrev_b32 v114, v132, v136\n\t"
    "v_and_b32 v132, 7, v132\n\t"
    "v_lshl_or_b32 v114, v136, v133, v114\n\t"
    "v_lshl_add_u32 v132, v132, 1, v128\n\t"
    "ds_or_rtn_b32 v122, v96, v114\n\t"
    "ds_write_b16 v132, v102\n\t"
    "v_cndmask_b32 v130, v130, v129, vcc\n\t"
    "v_ffbl_b32 v131, v130\n\t"
    "v_and_b32 v134, 7, v131\n\t"
    "v_lshlrev_b32 v134, v134, s45\n\t"
    "v_xor_b32 v129, v129, v134\n\t"
    "v_xor_b32 v132, v131, v127\n\t"
    "s_waitcnt lgkmcnt(8)\n\t"
    "v_and_b32 v119, v119, v111\n\t"
    "v_perm_b32 v119, v119, v119, s46\n\t"
    "ds_or_b32 v93, v119\n\t"
    "v_bfi_b32 v130, v103, 0, v129\n\t"
    "v_cmp_eq_u32 vcc, 0, v130\n\t"
    "v_and_or_b32 v98, v103, s44, v68\n\t"
    "v_xor_b32 v133, 23, v132\n\t"
    "v_lshlrev_b32 v115, v132, v136\n\t"
    "v_and_b32 v132, 7, v132\n\t"
    "v_lshl_or_b32 v115, v136, v133, v115\n\t"
    "v_lshl_add_u32 v132, v132, 1, v128\n\t"
    "ds_or_rtn_b32 v123, v97, v115\n\t"
    "ds_write_b16 v132, v104\n\t"
    "v_cndmask_b32 v130, v130, v129, vcc\n\t"
    "v_ffbl_b32 v131, v130\n\t"
    "v_and_b32 v134, 7, v131\n\t"
    "v_lshlrev_b32 v134, v134, s45\n\t"
    "v_xor_b32 v129, v129, v134\n\t"
    "v_xor_b32 v132, v131, v127\n\t"
    "s_waitcnt lgkmcnt(9)\n\t"
    "v_and_b32 v120, v120, v112\n\t"
    "v_perm_b32 v120, v120, v120, s46\n\t"
    "ds_or_b32 v94, v120\n\t"
    "v_bfi_b32 v130, v107, 0, v129\n\t"
    "v_cmp_eq_u32 vcc, 0, v130\n\t"
    "v_and_or_b32 v99, v107, s44, v68\n\t"
    "v_xor_b32 v133, 23, v132\n\t"
    "v_lshlrev_b32 v116, v132, v136\n\t"
    "v_and_b32 v132, 7, v132\n\t"
    "v_lshl_or_b32 v116, v136, v133, v116\n\t"
    "v_lshl_add_u32 v132, v132, 1, v128\n\t"
    "ds_or_rtn_b32 v124, v98, v116\n\t"
    "ds_write_b16 v132, v103\n\t"
    "v_cndmask_b32 v130, v130, v129, vcc\n\t"
    "v_ffbl_b32 v131, v130\n\t"
    "v_xor_b32 v132, v131, v127\n\t"
    "s_waitcnt lgkmcnt(10)\n\t"
    "v_and_b32 v121, v121, v113\n\t"
    "v_perm_b32 v121, v121, v121, s46\n\t"
    "ds_or_b32 v95, v121\n\t"
    "v_xor_b32 v133, 23, v132\n\t"
    "v_lshlrev_b32 v117, v132, v136\n\t"
    "v_and_b32 v132, 7, v132\n\t"
    "v_lshl_or_b32 v117, v136, v133, v117\n\t"
    "v_lshl_add_u32 v132, v132, 1, v128\n\t"
    "ds_or_rtn_b32 v125, v99, v117\n\t"
    "ds_write_b16 v132, v107\n\t"
    "s_waitcnt lgkmcnt(10)\n\t"
    "v_and_b32 v122, v122, v114\n\t"
    "v_perm_b32 v122, v122, v122, s46\n\t"
    "ds_or_b32 v96, v122\n\t"
    "ds_read_b128 v[72:75], v128\n\t"
    "v_and_or_b32 v100, v76, s44, v68\n\t"
    "ds_read_b32 v100, v100\n\t"
    "v_and_or_b32 v101, v77, s44, v68\n\t"
    "ds_read_b32 v101, v101\n\t"
    "v_and_or_b32 v102, v78, s44, v68\n\t"
    "ds_read_b32 v102, v102\n\t"
    "v_and_or_b32 v103, v79, s44, v68\n\t"
    "ds_read_b32 v103, v103\n\t"
    "v_and_or_b32 v104, v80, s44, v68\n\t"
    "ds_read_b32 v104, v104\n\t"
    "v_and_or_b32 v105, v81, s44, v68\n\t"
    "ds_read_b32 v105, v105\n\t"
    "v_and_or_b32 v106, v82, s44, v68\n\t"
    "ds_read_b32 v106, v106\n\t"
    "v_and_or_b32 v107, v83, s44, v68\n\t"
    "ds_read_b32 v107, v107\n\t"
    "s_waitcnt vmcnt(2)\n\t"
    "v_lshl_add_u32 v84, v24, 3, s36\n\t"
    "v_lshl_add_u32 v85, v25, 3, s37\n\t"
    "v_lshl_add_u32 v86, v26, 3, s38\n\t"
    "v_lshl_add_u32 v87, v27, 3, s39\n\t"
    "v_lshl_add_u32 v88, v28, 3, s40\n\t"
    "v_lshl_add_u32 v89, v29, 3, s41\n\t"
    "v_lshl_add_u32 v90, v30, 3, s42\n\t"
    "v_lshl_add_u32 v91, v31, 3, s43\n\t"
    "s_movk_i32 s50, 6400\n\t"
    "buffer_load_dwordx4 v[40:43], v65, %0, s50 offen offset:1616\n\t"
    "buffer_load_dwordx4 v[44:47], v65, %0, s50 offen offset:1632\n\t"
    "s_movk_i32 s50, 6400\n\t"
    "buffer_load_dwordx4 v[48:51], v65, %0, s50 offen offset:2416\n\t"
    "buffer_load_dwordx4 v[52:55], v65, %0, s50 offen offset:2432\n\t"
    "s_movk_i32 s50, 9600\n\t"
    "buffer_load_dwordx4 v[56:59], v65, %0, s50 offen offset:16\n\t"
    "buffer_load_dwordx4 v[60:63], v65, %0, s50 offen offset:32\n\t"
    "s_movk_i32 s50, 9600\n\t"
    "buffer_load_dwordx4 v[0:3], v65, %0, s50 offen offset:816\n\t"
    "buffer_load_dwordx4 v[4:7], v65, %0, s50 offen offset:832\n\t"
    "s_movk_i32 s50, 9600\n\t"
    "buffer_load_dwordx4 v[8:11], v65, %0, s50 offen offset:1616\n\t"
    "buffer_load_dwordx4 v[12:15], v65, %0, s50 offen offset:1632\n\t"
    "s_waitcnt lgkmcnt(11)\n\t"
    "v_and_b32 v123, v123, v115\n\t"
    "v_perm_b32 v123, v123, v123, s46\n\t"
    "ds_or_b32 v97, v123\n\t"
    "v_and_b32 v124, v124, v116\n\t"
    "v_perm_b32 v124, v124, v124, s46\n\t"
    "ds_or_b32 v98, v124\n\t"
    "v_and_b32 v125, v125, v117\n\t"
    "v_perm_b32 v125, v125, v125, s46\n\t"
    "ds_or_b32 v99, v125\n\t"
    "s_waitcnt lgkmcnt(11)\n\t"
    "buffer_store_dwordx4 v[72:75], v66, %12, s49 offen offset:64 sc1\n\t"
    "v_mov_b32 v129, 0xffff0000\n\t"
    "s_waitcnt lgkmcnt(7)\n\t"
    "v_perm_b32 v100, v100, v76, v126\n\t"
    "v_perm_b32 v101, v101, v77, v126\n\t"
    "v_perm_b32 v102, v102, v78, v126\n\t"
    "v_perm_b32 v103, v103, v79, v126\n\t"
    "s_waitcnt lgkmcnt(3)\n\t"
    "v_perm_b32 v104, v104, v80, v126\n\t"
    "v_perm_b32 v105, v105, v81, v126\n\t"
    "v_perm_b32 v106, v106, v82, v126\n\t"
    "v_perm_b32 v107, v107, v83, v126\n\t"
    "v_max_u32 v108, v100, v101\n\t"
    "v_min_u32 v101, v100, v101\n\t"
    "v_max_u32 v100, v102, v103\n\t"
    "v_min_u32 v103, v102, v103\n\t"
    "v_max_u32 v102, v104, v105\n\t"
    "v_min_u32 v105, v104, v105\n\t"
    "v_max_u32 v104, v106, v107\n\t"
    "v_min_u32 v107, v106, v107\n\t"
    "v_max_u32 v106, v108, v100\n\t"
    "v_min_u32 v100, v108, v100\n\t"
    "v_max_u32 v108, v101, v103\n\t"
    "v_min_u32 v103, v101, v103\n\t"
    "v_max_u32 v101, v102, v104\n\t"
    "v_min_u32 v104, v102, v104\n\t"
    "v_max_u32 v102, v105, v107\n\t"
    "v_min_u32 v107, v105, v107\n\t"
    "v_bfi_b32 v130, v106, 0, v129\n\t"
    "v_cmp_eq_u32 vcc, 0, v130\n\t"
    "v_and_or_b32 v92, v106, s44, v68\n\t"
    "s_nop 0\n\t"
    "v_cndmask_b32 v130, v130, v129, vcc\n\t"
    "v_ffbl_b32 v131, v130\n\t"
    "v_and_b32 v134, 7, v131\n\t"
    "v_lshlrev_b32 v134, v134, s45\n\t"
    "v_xor_b32 v129, v129, v134\n\t"
    "v_xor_b32 v132, v131, v127\n\t"
    "v_bfi_b32 v130, v101, 0, v129\n\t"
    "v_cmp_eq_u32 vcc, 0, v130\n\t"
    "v_and_or_b32 v93, v101, s44, v68\n\t"
    "v_xor_b32 v133, 23, v132\n\t"
    "v_lshlrev_b32 v110, v132, v136\n\t"
    "v_and_b32 v132, 7, v132\n\t"
    "v_lshl_or_b32 v110, v136, v133, v110\n\t"
    "v_lshl_add_u32 v132, v132, 1, v128\n\t"
    "ds_or_rtn_b32 v118, v92, v110\n\t"
    "ds_write_b16 v132, v106\n\t"
    "v_cndmask_b32 v130, v130, v129, vcc\n\t"
    "v_ffbl_b32 v131, v130\n\t"
    "v_and_b32 v134, 7, v131\n\t"
    "v_lshlrev_b32 v134, v134, s45\n\t"
    "v_xor_b32 v129, v129, v134\n\t"
    "v_xor_b32 v132, v131, v127\n\t"
    "v_bfi_b32 v130, v108, 0, v129\n\t"
    "v_cmp_eq_u32 vcc, 0, v130\n\t"
    "v_and_or_b32 v94, v108, s44, v68\n\t"
    "v_xor_b32 v133, 23, v132\n\t"
    "v_lshlrev_b32 v111, v132, v136\n\t"
    "v_and_b32 v132, 7, v132\n\t"
    "v_lshl_or_b32 v111, v136, v133, v111\n\t"
    "v_lshl_add_u32 v132, v132, 1, v128\n\t"
    "ds_or_rtn_b32 v119, v93, v111\n\t"
    "ds_write_b16 v132, v101\n\t"
    "v_cndmask_b32 v130, v130, v129, vcc\n\t"
    "v_ffbl_b32 v131, v130\n\t"
    "v_and_b32 v134, 7, v131\n\t"
    "v_lshlrev_b32 v134, v134, s45\n\t"
    "v_xor_b32 v129, v129, v134\n\t"
    "v_xor_b32 v132, v131, v127\n\t"
    "v_bfi_b32 v130, v100, 0, v129\n\t"
    "v_cmp_eq_u32 vcc, 0, v130\n\t"
    "v_and_or_b32 v95, v100, s44, v68\n\t"
    "v_xor_b32 v133, 23, v132\n\t"
    "v_lshlrev_b32 v112, v132, v136\n\t"
    "v_and_b32 v132, 7, v132\n\t"
    "v_lshl_or_b32 v112, v136, v133, v112\n\t"
    "v_lshl_add_u32 v132, v132, 1, v128\n\t"
    "ds_or_rtn_b32 v120, v94, v112\n\t"
    "ds_write_b16 v132, v108\n\t"
    "v_cndmask_b32 v130, v130, v129, vcc\n\t"
    "v_ffbl_b32 v131, v130\n\t"
    "v_and_b32 v134, 7, v131\n\t"
    "v_lshlrev_b32 v134, v134, s45\n\t"
    "v_xor_b32 v129, v129, v134\n\t"
    "v_xor_b32 v132, v131, v127\n\t"
    "v_bfi_b32 v130, v102, 0, v129\n\t"
    "v_cmp_eq_u32 vcc, 0, v130\n\t"
    "v_and_or_b32 v96, v102, s44, v68\n\t"
    "v_xor_b32 v133, 23, v132\n\t"
    "v_lshlrev_b32 v113, v132, v136\n\t"
    "v_and_b32 v132, 7, v132\n\t"
    "v_lshl_or_b32 v113, v136, v133, v113\n\t"
    "v_lshl_add_u32 v132, v132, 1, v128\n\t"
    "ds_or_rtn_b32 v121, v95, v113\n\t"
    "ds_write_b16 v132, v100\n\t"
    "v_cndmask_b32 v130, v130, v129, vcc\n\t"
    "v_ffbl_b32 v131, v130\n\t"
    "v_and_b32 v134, 7, v131\n\t"
    "v_lshlrev_b32 v134, v134, s45\n\t"
    "v_xor_b32 v129, v129, v134\n\t"
    "v_xor_b32 v132, v131, v127\n\t"
    "s_waitcnt lgkmcnt(7)\n\t"
    "v_and_b32 v118, v118, v110\n\t"
    "v_perm_b32 v118, v118, v118, s46\n\t"
    "ds_or_b32 v92, v118\n\t"
    "v_bfi_b32 v130, v104, 0, v129\n\t"
    "v_cmp_eq_u32 vcc, 0, v130\n\t"
    "v_and_or_b32 v97, v104, s44, v68\n\t"
    "v_xor_b32 v133, 23, v132\n\t"
    "v_lshlrev_b32 v114, v132, v136\n\t"
    "v_and_b32 v132, 7, v132\n\t"
    "v_lshl_or_b32 v114, v136, v133, v114\n\t"
    "v_lshl_add_u32 v132, v132, 1, v128\n\t"
    "ds_or_rtn_b32 v122, v96, v114\n\t"
    "ds_write_b16 v132, v102\n\t"
    "v_cndmask_b32 v130, v130, v129, vcc\n\t"
    "v_ffbl_b32 v131, v130\n\t"
    "v_and_b32 v134, 7, v131\n\t"
    "v_lshlrev_b32 v134, v134, s45\n\t"
    "v_xor_b32 v129, v129, v134\n\t"
    "v_xor_b32 v132, v131, v127\n\t"
    "s_waitcnt lgkmcnt(8)\n\t"
    "v_and_b32 v119, v119, v111\n\t"
    "v_perm_b32 v119, v119, v119, s46\n\t"
    "ds_or_b32 v93, v119\n\t"
    "v_bfi_b32 v130, v103, 0, v129\n\t"
    "v_cmp_eq_u32 vcc, 0, v130\n\t"
    "v_and_or_b32 v98, v103, s44, v68\n\t"
    "v_xor_b32 v133, 23, v132\n\t"
    "v_lshlrev_b32 v115, v132, v136\n\t"
    "v_and_b32 v132, 7, v132\n\t"
    "v_lshl_or_b32 v115, v136, v133, v115\n\t"
    "v_lshl_add_u32 v132, v132, 1, v128\n\t"
    "ds_or_rtn_b32 v123, v97, v115\n\t"
    "ds_write_b16 v132, v104\n\t"
    "v_cndmask_b32 v130, v130, v129, vcc\n\t"
    "v_ffbl_b32 v131, v130\n\t"
    "v_and_b32 v134, 7, v131\n\t"
    "v_lshlrev_b32 v134, v134, s45\n\t"
    "v_xor_b32 v129, v129, v134\n\t"
    "v_xor_b32 v132, v131, v127\n\t"
    "s_waitcnt lgkmcnt(9)\n\t"
    "v_and_b32 v120, v120, v112\n\t"
    "v_perm_b32 v120, v120, v120, s46\n\t"
    "ds_or_b32 v94, v120\n\t"
    "v_bfi_b32 v130, v107, 0, v129\n\t"
    "v_cmp_eq_u32 vcc, 0, v130\n\t"
    "v_and_or_b32 v99, v107, s44, v68\n\t"
    "v_xor_b32 v133, 23, v132\n\t"
    "v_lshlrev_b32 v116, v132, v136\n\t"
    "v_and_b32 v132, 7, v132\n\t"
    "v_lshl_or_b32 v116, v136, v133, v116\n\t"
    "v_lshl_add_u32 v132, v132, 1, v128\n\t"
    "ds_or_rtn_b32 v124, v98, v116\n\t"
    "ds_write_b16 v132, v103\n\t"
    "v_cndmask_b32 v130, v130, v129, vcc\n\t"
    "v_ffbl_b32 v131, v130\n\t"
    "v_xor_b32 v132, v131, v127\n\t"
    "s_waitcnt lgkmcnt(10)\n\t"
    "v_and_b32 v121, v121, v113\n\t"
    "v_perm_b32 v121, v121, v121, s46\n\t"
    "ds_or_b32 v95, v121\n\t"
    "v_xor_b32 v133, 23, v132\n\t"
    "v_lshlrev_b32 v117, v132, v136\n\t"
    "v_and_b32 v132, 7, v132\n\t"
    "v_lshl_or_b32 v117, v136, v133, v117\n\t"
    "v_lshl_add_u32 v132, v132, 1, v128\n\t"
    "ds_or_rtn_b32 v125, v99, v117\n\t"
    "ds_write_b16 v132, v107\n\t"
    "s_waitcnt lgkmcnt(10)\n\t"
    "v_and_b32 v122, v122, v114\n\t"
    "v_perm_b32 v122, v122, v122, s46\n\t"
    "ds_or_b32 v96, v122\n\t"
    "ds_read_b128 v[72:75], v128\n\t"
    "v_and_or_b32 v100, v84, s44, v68\n\t"
    "ds_read_b32 v100, v100\n\t"
    "v_and_or_b32 v101, v85, s44, v68\n\t"
    "ds_read_b32 v101, v101\n\t"
    "v_and_or_b32 v102, v86, s44, v68\n\t"
    "ds_read_b32 v102, v102\n\t"
    "v_and_or_b32 v103, v87, s44, v68\n\t"
    "ds_read_b32 v103, v103\n\t"
    "v_and_or_b32 v104, v88, s44, v68\n\t"
    "ds_read_b32 v104, v104\n\t"
    "v_and_or_b32 v105, v89, s44, v68\n\t"
    "ds_read_b32 v105, v105\n\t"
    "v_and_or_b32 v106, v90, s44, v68\n\t"
    "ds_read_b32 v106, v106\n\t"
    "v_and_or_b32 v107, v91, s44, v68\n\t"
    "ds_read_b32 v107, v107\n\t"
    "s_waitcnt vmcnt(11)\n\t"
    "v_lshl_add_u32 v76, v32, 3, s36\n\t"
    "v_lshl_add_u32 v77, v33, 3, s37\n\t"
    "v_lshl_add_u32 v78, v34, 3, s38\n\t"
    "v_lshl_add_u32 v79, v35, 3, s39\n\t"
    "v_lshl_add_u32 v80, v36, 3, s40\n\t"
    "v_lshl_add_u32 v81, v37, 3, s41\n\t"
    "v_lshl_add_u32 v82, v38, 3, s42\n\t"
    "v_lshl_add_u32 v83, v39, 3, s43\n\t"
    "s_movk_i32 s50, 9600\n\t"
    "buffer_load_dwordx4 v[16:19], v65, %0, s50 offen offset:2416\n\t"
    "buffer_load_dwordx4 v[20:23], v65, %0, s50 offen offset:2432\n\t"
    "v_mov_b32 v134, 1032\n\t"
    "L_asvsp8_%=:\n\t"
    "ds_read_b32 v130, v134\n\t"
    "s_waitcnt lgkmcnt(0)\n\t"
    "v_readfirstlane_b32 s50, v130\n\t"
    "s_nop 0\n\t"
    "s_cmp_lt_u32 s50, 6\n\t"
    "s_cbranch_scc1 L_asvsp8_%=\n\t"
    "s_mov_b64 s[96:97], %2\n\t"
    "s_and_b32 s97, s97, 0xffff\n\t"
    "buffer_load_dwordx4 v[156:159], v137, s[96:99], s47 offen offset:0\n\t"
    "buffer_load_dwordx4 v[160:163], v138, s[96:99], s47 offen offset:0\n\t"
    "buffer_load_dwordx4 v[164:167], v137, s[96:99], s47 offen offset:64\n\t"
    "buffer_load_dwordx4 v[168:171], v138, s[96:99], s47 offen offset:64\n\t"
    "s_add_u32 s47, s47, 0x100000\n\t"
    "buffer_load_dwordx4 v[172:175], v137, s[96:99], s47 offen offset:0\n\t"
    "buffer_load_dwordx4 v[176:179], v138, s[96:99], s47 offen offset:0\n\t"
    "buffer_load_dwordx4 v[180:183], v137, s[96:99], s47 offen offset:64\n\t"
    "buffer_load_dwordx4 v[184:187], v138, s[96:99], s47 offen offset:64\n\t"
    "s_add_u32 s47, s47, 0x100000\n\t"
    "buffer_load_dwordx4 v[188:191], v137, s[96:99], s47 offen offset:0\n\t"
    "buffer_load_dwordx4 v[192:195], v138, s[96:99], s47 offen offset:0\n\t"
    "buffer_load_dwordx4 v[196:199], v137, s[96:99], s47 offen offset:64\n\t"
    "buffer_load_dwordx4 v[200:203], v138, s[96:99], s47 offen offset:64\n\t"
    "s_add_u32 s47, s47, 0x100000\n\t"
    "v_and_b32 v123, v123, v115\n\t"
    "v_perm_b32 v123, v123, v123, s46\n\t"
    "ds_or_b32 v97, v123\n\t"
    "v_and_b32 v124, v124, v116\n\t"
    "v_perm_b32 v124, v124, v124, s46\n\t"
    "ds_or_b32 v98, v124\n\t"
    "v_and_b32 v125, v125, v117\n\t"
    "v_perm_b32 v125, v125, v125, s46\n\t"
    "ds_or_b32 v99, v125\n\t"
    "buffer_store_dwordx4 v[72:75], v66, %12, s49 offen offset:128 sc1\n\t"
    "v_mov_b32 v129, 0xffff0000\n\t"
    "v_perm_b32 v100, v100, v84, v126\n\t"
    "v_perm_b32 v101, v101, v85, v126\n\t"
    "v_perm_b32 v102, v102, v86, v126\n\t"
    "v_perm_b32 v103, v103, v87, v126\n\t"
    "v_perm_b32 v104, v104, v88, v126\n\t"
    "v_perm_b32 v105, v105, v89, v126\n\t"
    "v_perm_b32 v106, v106, v90, v126\n\t"
    "v_perm_b32 v107, v107, v91, v126\n\t"
    "v_max_u32 v108, v100, v101\n\t"
    "v_min_u32 v101, v100, v101\n\t"
    "v_max_u32 v100, v102, v103\n\t"
    "v_min_u32 v103, v102, v103\n\t"
    "v_max_u32 v102, v104, v105\n\t"
    "v_min_u32 v105, v104, v105\n\t"
    "v_max_u32 v104, v106, v107\n\t"
    "v_min_u32 v107, v106, v107\n\t"
    "v_max_u32 v106, v108, v100\n\t"
    "v_min_u32 v100, v108, v100\n\t"
    "v_max_u32 v108, v101, v103\n\t"
    "v_min_u32 v103, v101, v103\n\t"
    "v_max_u32 v101, v102, v104\n\t"
    "v_min_u32 v104, v102, v104\n\t"
    "v_max_u32 v102, v105, v107\n\t"
    "v_min_u32 v107, v105, v107\n\t"
    "v_bfi_b32 v130, v106, 0, v129\n\t"
    "v_cmp_eq_u32 vcc, 0, v130\n\t"
    "v_and_or_b32 v92, v106, s44, v68\n\t"
    "s_nop 0\n\t"
    "v_cndmask_b32 v130, v130, v129, vcc\n\t"
    "v_ffbl_b32 v131, v130\n\t"
    "v_and_b32 v134, 7, v131\n\t"
    "v_lshlrev_b32 v134, v134, s45\n\t"
    "v_xor_b32 v129, v129, v134\n\t"
    "v_xor_b32 v132, v131, v127\n\t"
    "v_bfi_b32 v130, v101, 0, v129\n\t"
    "v_cmp_eq_u32 vcc, 0, v130\n\t"
    "v_and_or_b32 v93, v101, s44, v68\n\t"
    "v_xor_b32 v133, 23, v132\n\t"
    "v_lshlrev_b32 v110, v132, v136\n\t"
    "v_and_b32 v132, 7, v132\n\t"
    "v_lshl_or_b32 v110, v136, v133, v110\n\t"
    "v_lshl_add_u32 v132, v132, 1, v128\n\t"
    "ds_or_rtn_b32 v118, v92, v110\n\t"
    "ds_write_b16 v132, v106\n\t"
    "v_cndmask_b32 v130, v130, v129, vcc\n\t"
    "v_ffbl_b32 v131, v130\n\t"
    "v_and_b32 v134, 7, v131\n\t"
    "v_lshlrev_b32 v134, v134, s45\n\t"
    "v_xor_b32 v129, v129, v134\n\t"
    "v_xor_b32 v132, v131, v127\n\t"
    "v_bfi_b32 v130, v108, 0, v129\n\t"
    "v_cmp_eq_u32 vcc, 0, v130\n\t"
    "v_and_or_b32 v94, v108, s44, v68\n\t"
    "v_xor_b32 v133, 23, v132\n\t"
    "v_lshlrev_b32 v111, v132, v136\n\t"
    "v_and_b32 v132, 7, v132\n\t"
    "v_lshl_or_b32 v111, v136, v133, v111\n\t"
    "v_lshl_add_u32 v132, v132, 1, v128\n\t"
    "ds_or_rtn_b32 v119, v93, v111\n\t"
    "ds_write_b16 v132, v101\n\t"
    "v_cndmask_b32 v130, v130, v129, vcc\n\t"
    "v_ffbl_b32 v131, v130\n\t"
    "v_and_b32 v134, 7, v131\n\t"
    "v_lshlrev_b32 v134, v134, s45\n\t"
    "v_xor_b32 v129, v129, v134\n\t"
    "v_xor_b32 v132, v131, v127\n\t"
    "v_bfi_b32 v130, v100, 0, v129\n\t"
    "v_cmp_eq_u32 vcc, 0, v130\n\t"
    "v_and_or_b32 v95, v100, s44, v68\n\t"
    "v_xor_b32 v133, 23, v132\n\t"
    "v_lshlrev_b32 v112, v132, v136\n\t"
    "v_and_b32 v132, 7, v132\n\t"
    "v_lshl_or_b32 v112, v136, v133, v112\n\t"
    "v_lshl_add_u32 v132, v132, 1, v128\n\t"
    "ds_or_rtn_b32 v120, v94, v112\n\t"
    "ds_write_b16 v132, v108\n\t"
    "v_cndmask_b32 v130, v130, v129, vcc\n\t"
    "v_ffbl_b32 v131, v130\n\t"
    "v_and_b32 v134, 7, v131\n\t"
    "v_lshlrev_b32 v134, v134, s45\n\t"
    "v_xor_b32 v129, v129, v134\n\t"
    "v_xor_b32 v132, v131, v127\n\t"
    "v_bfi_b32 v130, v102, 0, v129\n\t"
    "v_cmp_eq_u32 vcc, 0, v130\n\t"
    "v_and_or_b32 v96, v102, s44, v68\n\t"
    "v_xor_b32 v133, 23, v132\n\t"
    "v_lshlrev_b32 v113, v132, v136\n\t"
    "v_and_b32 v132, 7, v132\n\t"
    "v_lshl_or_b32 v113, v136, v133, v113\n\t"
    "v_lshl_add_u32 v132, v132, 1, v128\n\t"
    "ds_or_rtn_b32 v121, v95, v113\n\t"
    "ds_write_b16 v132, v100\n\t"
    "v_cndmask_b32 v130, v130, v129, vcc\n\t"
    "v_ffbl_b32 v131, v130\n\t"
    "v_and_b32 v134, 7, v131\n\t"
    "v_lshlrev_b32 v134, v134, s45\n\t"
    "v_xor_b32 v129, v129, v134\n\t"
    "v_xor_b32 v132, v131, v127\n\t"
    "s_waitcnt lgkmcnt(7)\n\t"
    "v_and_b32 v118, v118, v110\n\t"
    "v_perm_b32 v118, v118, v118, s46\n\t"
    "ds_or_b32 v92, v118\n\t"
    "v_bfi_b32 v130, v104, 0, v129\n\t"
    "v_cmp_eq_u32 vcc, 0, v130\n\t"
    "v_and_or_b32 v97, v104, s44, v68\n\t"
    "v_xor_b32 v133, 23, v132\n\t"
    "v_lshlrev_b32 v114, v132, v136\n\t"
    "v_and_b32 v132, 7, v132\n\t"
    "v_lshl_or_b32 v114, v136, v133, v114\n\t"
    "v_lshl_add_u32 v132, v132, 1, v128\n\t"
    "ds_or_rtn_b32 v122, v96, v114\n\t"
    "ds_write_b16 v132, v102\n\t"
    "v_cndmask_b32 v130, v130, v129, vcc\n\t"
    "v_ffbl_b32 v131, v130\n\t"
    "v_and_b32 v134, 7, v131\n\t"
    "v_lshlrev_b32 v134, v134, s45\n\t"
    "v_xor_b32 v129, v129, v134\n\t"
    "v_xor_b32 v132, v131, v127\n\t"
    "s_waitcnt lgkmcnt(8)\n\t"
    "v_and_b32 v119, v119, v111\n\t"
    "v_perm_b32 v119, v119, v119, s46\n\t"
    "ds_or_b32 v93, v119\n\t"
    "v_bfi_b32 v130, v103, 0, v129\n\t"
    "v_cmp_eq_u32 vcc, 0, v130\n\t"
    "v_and_or_b32 v98, v103, s44, v68\n\t"
    "v_xor_b32 v133, 23, v132\n\t"
    "v_lshlrev_b32 v115, v132, v136\n\t"
    "v_and_b32 v132, 7, v132\n\t"
    "v_lshl_or_b32 v115, v136, v133, v115\n\t"
    "v_lshl_add_u32 v132, v132, 1, v128\n\t"
    "ds_or_rtn_b32 v123, v97, v115\n\t"
    "ds_write_b16 v132, v104\n\t"
    "v_cndmask_b32 v130, v130, v129, vcc\n\t"
    "v_ffbl_b32 v131, v130\n\t"
    "v_and_b32 v134, 7, v131\n\t"
    "v_lshlrev_b32 v134, v134, s45\n\t"
    "v_xor_b32 v129, v129, v134\n\t"
    "v_xor_b32 v132, v131, v127\n\t"
    "s_waitcnt lgkmcnt(9)\n\t"
    "v_and_b32 v120, v120, v112\n\t"
    "v_perm_b32 v120, v120, v120, s46\n\t"
    "ds_or_b32 v94, v120\n\t"
    "v_bfi_b32 v130, v107, 0, v129\n\t"
    "v_cmp_eq_u32 vcc, 0, v130\n\t"
    "v_and_or_b32 v99, v107, s44, v68\n\t"
    "v_xor_b32 v133, 23, v132\n\t"
    "v_lshlrev_b32 v116, v132, v136\n\t"
    "v_and_b32 v132, 7, v132\n\t"
    "v_lshl_or_b32 v116, v136, v133, v116\n\t"
    "v_lshl_add_u32 v132, v132, 1, v128\n\t"
    "ds_or_rtn_b32 v124, v98, v116\n\t"
    "ds_write_b16 v132, v103\n\t"
    "v_cndmask_b32 v130, v130, v129, vcc\n\t"
    "v_ffbl_b32 v131, v130\n\t"
    "v_xor_b32 v132, v131, v127\n\t"
    "s_waitcnt lgkmcnt(10)\n\t"
    "v_and_b32 v121, v121, v113\n\t"
    "v_perm_b32 v121, v121, v121, s46\n\t"
    "ds_or_b32 v95, v121\n\t"
    "v_xor_b32 v133, 23, v132\n\t"
    "v_lshlrev_b32 v117, v132, v136\n\t"
    "v_and_b32 v132, 7, v132\n\t"
    "v_lshl_or_b32 v117, v136, v133, v117\n\t"
    "v_lshl_add_u32 v132, v132, 1, v128\n\t"
    "ds_or_rtn_b32 v125, v99, v117\n\t"
    "ds_write_b16 v132, v107\n\t"
    "s_waitcnt lgkmcnt(10)\n\t"
    "v_and_b32 v122, v122, v114\n\t"
    "v_perm_b32 v122, v122, v122, s46\n\t"
    "ds_or_b32 v96, v122\n\t"
    "ds_read_b128 v[72:75], v128\n\t"
    "v_and_or_b32 v100, v76, s44, v68\n\t"
    "ds_read_b32 v100, v100\n\t"
    "v_and_or_b32 v101, v77, s44, v68\n\t"
    "ds_read_b32 v101, v101\n\t"
    "v_and_or_b32 v102, v78, s44, v68\n\t"
    "ds_read_b32 v102, v102\n\t"
    "v_and_or_b32 v103, v79, s44, v68\n\t"
    "ds_read_b32 v103, v103\n\t"
    "v_and_or_b32 v104, v80, s44, v68\n\t"
    "ds_read_b32 v104, v104\n\t"
    "v_and_or_b32 v105, v81, s44, v68\n\t"
    "ds_read_b32 v105, v105\n\t"
    "v_and_or_b32 v106, v82, s44, v68\n\t"
    "ds_read_b32 v106, v106\n\t"
    "v_and_or_b32 v107, v83, s44, v68\n\t"
    "ds_read_b32 v107, v107\n\t"
    "s_waitcnt vmcnt(24)\n\t"
    "v_lshl_add_u32 v84, v40, 3, s36\n\t"
    "v_lshl_add_u32 v85, v41, 3, s37\n\t"
    "v_lshl_add_u32 v86, v42, 3, s38\n\t"
    "v_lshl_add_u32 v87, v43, 3, s39\n\t"
    "v_lshl_add_u32 v88, v44, 3, s40\n\t"
    "v_lshl_add_u32 v89, v45, 3, s41\n\t"
    "v_lshl_add_u32 v90, v46, 3, s42\n\t"
    "v_lshl_add_u32 v91, v47, 3, s43\n\t"
    "buffer_load_dwordx4 v[204:207], v137, s[96:99], s47 offen offset:0\n\t"
    "buffer_load_dwordx4 v[208:211], v138, s[96:99], s47 offen offset:0\n\t"
    "buffer_load_dwordx4 v[212:215], v137, s[96:99], s47 offen offset:64\n\t"
    "buffer_load_dwordx4 v[216:219], v138, s[96:99], s47 offen offset:64\n\t"
    "s_add_u32 s47, s47, 0x100000\n\t"
    "buffer_load_dwordx4 v[220:223], v137, s[96:99], s47 offen offset:0\n\t"
    "buffer_load_dwordx4 v[224:227], v138, s[96:99], s47 offen offset:0\n\t"
    "buffer_load_dwordx4 v[228:231], v137, s[96:99], s47 offen offset:64\n\t"
    "buffer_load_dwordx4 v[232:235], v138, s[96:99], s47 offen offset:64\n\t"
    "s_add_u32 s47, s47, 0x100000\n\t"
    "buffer_load_dwordx4 v[236:239], v137, s[96:99], s47 offen offset:0\n\t"
    "buffer_load_dwordx4 v[240:243], v138, s[96:99], s47 offen offset:0\n\t"
    "buffer_load_dwordx4 v[244:247], v137, s[96:99], s47 offen offset:64\n\t"
    "buffer_load_dwordx4 v[248:251], v138, s[96:99], s47 offen offset:64\n\t"
    "s_add_u32 s47, s47, 0x100000\n\t"
    "s_waitcnt lgkmcnt(11)\n\t"
    "v_and_b32 v123, v123, v115\n\t"
    "v_perm_b32 v123, v123, v123, s46\n\t"
    "ds_or_b32 v97, v123\n\t"
    "v_and_b32 v124, v124, v116\n\t"
    "v_perm_b32 v124, v124, v124, s46\n\t"
    "ds_or_b32 v98, v124\n\t"
    "v_and_b32 v125, v125, v117\n\t"
    "v_perm_b32 v125, v125, v125, s46\n\t"
    "ds_or_b32 v99, v125\n\t"
    "s_waitcnt lgkmcnt(11)\n\t"
    "buffer_store_dwordx4 v[72:75], v66, %12, s49 offen offset:192 sc1\n\t"
    "v_mov_b32 v129, 0xffff0000\n\t"
    "s_waitcnt lgkmcnt(7)\n\t"
    "v_perm_b32 v100, v100, v76, v126\n\t"
    "v_perm_b32 v101, v101, v77, v126\n\t"
    "v_perm_b32 v102, v102, v78, v126\n\t"
    "v_perm_b32 v103, v103, v79, v126\n\t"
    "s_waitcnt lgkmcnt(3)\n\t"
    "v_perm_b32 v104, v104, v80, v126\n\t"
    "v_perm_b32 v105, v105, v81, v126\n\t"
    "v_perm_b32 v106, v106, v82, v126\n\t"
    "v_perm_b32 v107, v107, v83, v126\n\t"
    "v_max_u32 v108, v100, v101\n\t"
    "v_min_u32 v101, v100, v101\n\t"
    "v_max_u32 v100, v102, v103\n\t"
    "v_min_u32 v103, v102, v103\n\t"
    "v_max_u32 v102, v104, v105\n\t"
    "v_min_u32 v105, v104, v105\n\t"
    "v_max_u32 v104, v106, v107\n\t"
    "v_min_u32 v107, v106, v107\n\t"
    "v_max_u32 v106, v108, v100\n\t"
    "v_min_u32 v100, v108, v100\n\t"
    "v_max_u32 v108, v101, v103\n\t"
    "v_min_u32 v103, v101, v103\n\t"
    "v_max_u32 v101, v102, v104\n\t"
    "v_min_u32 v104, v102, v104\n\t"
    "v_max_u32 v102, v105, v107\n\t"
    "v_min_u32 v107, v105, v107\n\t"
    "v_bfi_b32 v130, v106, 0, v129\n\t"
    "v_cmp_eq_u32 vcc, 0, v130\n\t"
    "v_and_or_b32 v92, v106, s44, v68\n\t"
    "s_nop 0\n\t"
    "v_cndmask_b32 v130, v130, v129, vcc\n\t"
    "v_ffbl_b32 v131, v130\n\t"
    "v_and_b32 v134, 7, v131\n\t"
    "v_lshlrev_b32 v134, v134, s45\n\t"
    "v_xor_b32 v129, v129, v134\n\t"
    "v_xor_b32 v132, v131, v127\n\t"
    "v_bfi_b32 v130, v101, 0, v129\n\t"
    "v_cmp_eq_u32 vcc, 0, v130\n\t"
    "v_and_or_b32 v93, v101, s44, v68\n\t"
    "v_xor_b32 v133, 23, v132\n\t"
    "v_lshlrev_b32 v110, v132, v136\n\t"
    "v_and_b32 v132, 7, v132\n\t"
    "v_lshl_or_b32 v110, v136, v133, v110\n\t"
    "v_lshl_add_u32 v132, v132, 1, v128\n\t"
    "ds_or_rtn_b32 v118, v92, v110\n\t"
    "ds_write_b16 v132, v106\n\t"
    "v_cndmask_b32 v130, v130, v129, vcc\n\t"
    "v_ffbl_b32 v131, v130\n\t"
    "v_and_b32 v134, 7, v131\n\t"
    "v_lshlrev_b32 v134, v134, s45\n\t"
    "v_xor_b32 v129, v129, v134\n\t"
    "v_xor_b32 v132, v131, v127\n\t"
    "v_bfi_b32 v130, v108, 0, v129\n\t"
    "v_cmp_eq_u32 vcc, 0, v130\n\t"
    "v_and_or_b32 v94, v108, s44, v68\n\t"
    "v_xor_b32 v133, 23, v132\n\t"
    "v_lshlrev_b32 v111, v132, v136\n\t"
    "v_and_b32 v132, 7, v132\n\t"
    "v_lshl_or_b32 v111, v136, v133, v111\n\t"
    "v_lshl_add_u32 v132, v132, 1, v128\n\t"
    "ds_or_rtn_b32 v119, v93, v111\n\t"
    "ds_write_b16 v132, v101\n\t"
    "v_cndmask_b32 v130, v130, v129, vcc\n\t"
    "v_ffbl_b32 v131, v130\n\t"
    "v_and_b32 v134, 7, v131\n\t"
    "v_lshlrev_b32 v134, v134, s45\n\t"
    "v_xor_b32 v129, v129, v134\n\t"
    "v_xor_b32 v132, v131, v127\n\t"
    "v_bfi_b32 v130, v100, 0, v129\n\t"
    "v_cmp_eq_u32 vcc, 0, v130\n\t"
    "v_and_or_b32 v95, v100, s44, v68\n\t"
    "v_xor_b32 v133, 23, v132\n\t"
    "v_lshlrev_b32 v112, v132, v136\n\t"
    "v_and_b32 v132, 7, v132\n\t"
    "v_lshl_or_b32 v112, v136, v133, v112\n\t"
    "v_lshl_add_u32 v132, v132, 1, v128\n\t"
    "ds_or_rtn_b32 v120, v94, v112\n\t"
    "ds_write_b16 v132, v108\n\t"
    "v_cndmask_b32 v130, v130, v129, vcc\n\t"
    "v_ffbl_b32 v131, v130\n\t"
    "v_and_b32 v134, 7, v131\n\t"
    "v_lshlrev_b32 v134, v134, s45\n\t"
    "v_xor_b32 v129, v129, v134\n\t"
    "v_xor_b32 v132, v131, v127\n\t"
    "v_bfi_b32 v130, v102, 0, v129\n\t"
    "v_cmp_eq_u32 vcc, 0, v130\n\t"
    "v_and_or_b32 v96, v102, s44, v68\n\t"
    "v_xor_b32 v133, 23, v132\n\t"
    "v_lshlrev_b32 v113, v132, v136\n\t"
    "v_and_b32 v132, 7, v132\n\t"
    "v_lshl_or_b32 v113, v136, v133, v113\n\t"
    "v_lshl_add_u32 v132, v132, 1, v128\n\t"
    "ds_or_rtn_b32 v121, v95, v113\n\t"
    "ds_write_b16 v132, v100\n\t"
    "v_cndmask_b32 v130, v130, v129, vcc\n\t"
    "v_ffbl_b32 v131, v130\n\t"
    "v_and_b32 v134, 7, v131\n\t"
    "v_lshlrev_b32 v134, v134, s45\n\t"
    "v_xor_b32 v129, v129, v134\n\t"
    "v_xor_b32 v132, v131, v127\n\t"
    "s_waitcnt lgkmcnt(7)\n\t"
    "v_and_b32 v118, v118, v110\n\t"
    "v_perm_b32 v118, v118, v118, s46\n\t"
    "ds_or_b32 v92, v118\n\t"
    "v_bfi_b32 v130, v104, 0, v129\n\t"
    "v_cmp_eq_u32 vcc, 0, v130\n\t"
    "v_and_or_b32 v97, v104, s44, v68\n\t"
    "v_xor_b32 v133, 23, v132\n\t"
    "v_lshlrev_b32 v114, v132, v136\n\t"
    "v_and_b32 v132, 7, v132\n\t"
    "v_lshl_or_b32 v114, v136, v133, v114\n\t"
    "v_lshl_add_u32 v132, v132, 1, v128\n\t"
    "ds_or_rtn_b32 v122, v96, v114\n\t"
    "ds_write_b16 v132, v102\n\t"
    "v_cndmask_b32 v130, v130, v129, vcc\n\t"
    "v_ffbl_b32 v131, v130\n\t"
    "v_and_b32 v134, 7, v131\n\t"
    "v_lshlrev_b32 v134, v134, s45\n\t"
    "v_xor_b32 v129, v129, v134\n\t"
    "v_xor_b32 v132, v131, v127\n\t"
    "s_waitcnt lgkmcnt(8)\n\t"
    "v_and_b32 v119, v119, v111\n\t"
    "v_perm_b32 v119, v119, v119, s46\n\t"
    "ds_or_b32 v93, v119\n\t"
    "v_bfi_b32 v130, v103, 0, v129\n\t"
    "v_cmp_eq_u32 vcc, 0, v130\n\t"
    "v_and_or_b32 v98, v103, s44, v68\n\t"
    "v_xor_b32 v133, 23, v132\n\t"
    "v_lshlrev_b32 v115, v132, v136\n\t"
    "v_and_b32 v132, 7, v132\n\t"
    "v_lshl_or_b32 v115, v136, v133, v115\n\t"
    "v_lshl_add_u32 v132, v132, 1, v128\n\t"
    "ds_or_rtn_b32 v123, v97, v115\n\t"
    "ds_write_b16 v132, v104\n\t"
    "v_cndmask_b32 v130, v130, v129, vcc\n\t"
    "v_ffbl_b32 v131, v130\n\t"
    "v_and_b32 v134, 7, v131\n\t"
    "v_lshlrev_b32 v134, v134, s45\n\t"
    "v_xor_b32 v129, v129, v134\n\t"
    "v_xor_b32 v132, v131, v127\n\t"
    "s_waitcnt lgkmcnt(9)\n\t"
    "v_and_b32 v120, v120, v112\n\t"
    "v_perm_b32 v120, v120, v120, s46\n\t"
    "ds_or_b32 v94, v120\n\t"
    "v_bfi_b32 v130, v107, 0, v129\n\t"
    "v_cmp_eq_u32 vcc, 0, v130\n\t"
    "v_and_or_b32 v99, v107, s44, v68\n\t"
    "v_xor_b32 v133, 23, v132\n\t"
    "v_lshlrev_b32 v116, v132, v136\n\t"
    "v_and_b32 v132, 7, v132\n\t"
    "v_lshl_or_b32 v116, v136, v133, v116\n\t"
    "v_lshl_add_u32 v132, v132, 1, v128\n\t"
    "ds_or_rtn_b32 v124, v98, v116\n\t"
    "ds_write_b16 v132, v103\n\t"
    "v_cndmask_b32 v130, v130, v129, vcc\n\t"
    "v_ffbl_b32 v131, v130\n\t"
    "v_xor_b32 v132, v131, v127\n\t"
    "s_waitcnt lgkmcnt(10)\n\t"
    "v_and_b32 v121, v121, v113\n\t"
    "v_perm_b32 v121, v121, v121, s46\n\t"
    "ds_or_b32 v95, v121\n\t"
    "v_xor_b32 v133, 23, v132\n\t"
    "v_lshlrev_b32 v117, v132, v136\n\t"
    "v_and_b32 v132, 7, v132\n\t"
    "v_lshl_or_b32 v117, v136, v133, v117\n\t"
    "v_lshl_add_u32 v132, v132, 1, v128\n\t"
    "ds_or_rtn_b32 v125, v99, v117\n\t"
    "ds_write_b16 v132, v107\n\t"
    "s_waitcnt lgkmcnt(10)\n\t"
    "v_and_b32 v122, v122, v114\n\t"
    "v_perm_b32 v122, v122, v122, s46\n\t"
    "ds_or_b32 v96, v122\n\t"
    "ds_read_b128 v[72:75], v128\n\t"
    "v_mov_b32 v134, 1036\n\t"
    "L_asvsp12_%=:\n\t"
    "ds_read_b32 v130, v134\n\t"
    "s_waitcnt lgkmcnt(0)\n\t"
    "v_readfirstlane_b32 s50, v130\n\t"
    "s_nop 0\n\t"
    "s_cmp_lt_u32 s50, 1\n\t"
    "s_cbranch_scc1 L_asvsp12_%=\n\t"
    "v_and_or_b32 v100, v84, s44, v69\n\t"
    "ds_read_b32 v100, v100\n\t"
    "v_and_or_b32 v101, v85, s44, v69\n\t"
    "ds_read_b32 v101, v101\n\t"
    "v_and_or_b32 v102, v86, s44, v69\n\t"
    "ds_read_b32 v102, v102\n\t"
    "v_and_or_b32 v103, v87, s44, v69\n\t"
    "ds_read_b32 v103, v103\n\t"
    "v_and_or_b32 v104, v88, s44, v69\n\t"
    "ds_read_b32 v104, v104\n\t"
    "v_and_or_b32 v105, v89, s44, v69\n\t"
    "ds_read_b32 v105, v105\n\t"
    "v_and_or_b32 v106, v90, s44, v69\n\t"
    "ds_read_b32 v106, v106\n\t"
    "v_and_or_b32 v107, v91, s44, v69\n\t"
    "ds_read_b32 v107, v107\n\t"
    "s_waitcnt vmcnt(35)\n\t"
    "v_lshl_add_u32 v76, v48, 3, s36\n\t"
    "v_lshl_add_u32 v77, v49, 3, s37\n\t"
    "v_lshl_add_u32 v78, v50, 3, s38\n\t"
    "v_lshl_add_u32 v79, v51, 3, s39\n\t"
    "v_lshl_add_u32 v80, v52, 3, s40\n\t"
    "v_lshl_add_u32 v81, v53, 3, s41\n\t"
    "v_lshl_add_u32 v82, v54, 3, s42\n\t"
    "v_lshl_add_u32 v83, v55, 3, s43\n\t"
    "s_lshl_b32 s52, %14, 2\n\t"
    "s_add_u32 s52, s52, 48242688\n\t"
    "v_mov_b32 v70, 2072\n\t"
    "buffer_load_dword v71, off, %12, s52 offset:1024 sc1\n\t"
    "ds_read_b32 v70, v70\n\t"
    "buffer_load_dwordx4 v[24:27], v137, s[96:99], s47 offen offset:0\n\t"
    "buffer_load_dwordx4 v[28:31], v138, s[96:99], s47 offen offset:0\n\t"
    "buffer_load_dwordx4 v[32:35], v137, s[96:99], s47 offen offset:64\n\t"
    "buffer_load_dwordx4 v[36:39], v138, s[96:99], s47 offen offset:64\n\t"
    "s_add_u32 s47, s47, 0x100000\n\t"
    "buffer_load_dwordx4 v[40:43], v137, s[96:99], s47 offen offset:0\n\t"
    "buffer_load_dwordx4 v[44:47], v138, s[96:99], s47 offen offset:0\n\t"
    "buffer_load_dwordx4 v[48:51], v137, s[96:99], s47 offen offset:64\n\t"
    "buffer_load_dwordx4 v[52:55], v138, s[96:99], s47 offen offset:64\n\t"
    "s_add_u32 s47, s47, 0x100000\n\t"
    "v_and_b32 v123, v123, v115\n\t"
    "v_perm_b32 v123, v123, v123, s46\n\t"
    "ds_or_b32 v97, v123\n\t"
    "v_and_b32 v124, v124, v116\n\t"
    "v_perm_b32 v124, v124, v124, s46\n\t"
    "ds_or_b32 v98, v124\n\t"
    "v_and_b32 v125, v125, v117\n\t"
    "v_perm_b32 v125, v125, v125, s46\n\t"
    "ds_or_b32 v99, v125\n\t"
    "buffer_store_dwordx4 v[72:75], v66, %12, s49 offen offset:256 sc1\n\t"
    "v_mov_b32 v129, 0xffff0000\n\t"
    "s_waitcnt lgkmcnt(8)\n\t"
    "v_perm_b32 v100, v100, v84, v126\n\t"
    "v_perm_b32 v101, v101, v85, v126\n\t"
    "v_perm_b32 v102, v102, v86, v126\n\t"
    "v_perm_b32 v103, v103, v87, v126\n\t"
    "s_waitcnt lgkmcnt(4)\n\t"
    "v_perm_b32 v104, v104, v88, v126\n\t"
    "v_perm_b32 v105, v105, v89, v126\n\t"
    "v_perm_b32 v106, v106, v90, v126\n\t"
    "v_perm_b32 v107, v107, v91, v126\n\t"
    "s_waitcnt vmcnt(34)\n\t"
    "ds_write_b128 v141, v[156:159] offset:0\n\t"
    "ds_write_b128 v141, v[160:163] offset:256\n\t"
    "ds_read_b64_tr_b16 v[156:157], v142 offset:0\n\t"
    "ds_read_b64_tr_b16 v[158:159], v142 offset:256\n\t"
    "ds_read_b64_tr_b16 v[160:161], v142 offset:32\n\t"
    "ds_read_b64_tr_b16 v[162:163], v142 offset:288\n\t"
    "s_waitcnt vmcnt(32)\n\t"
    "ds_write_b128 v141, v[164:167] offset:2112\n\t"
    "ds_write_b128 v141, v[168:171] offset:2368\n\t"
    "ds_read_b64_tr_b16 v[164:165], v142 offset:2112\n\t"
    "ds_read_b64_tr_b16 v[166:167], v142 offset:2368\n\t"
    "ds_read_b64_tr_b16 v[168:169], v142 offset:2144\n\t"
    "ds_read_b64_tr_b16 v[170:171], v142 offset:2400\n\t"
    "v_max_u32 v108, v100, v101\n\t"
    "v_min_u32 v101, v100, v101\n\t"
    "v_max_u32 v100, v102, v103\n\t"
    "v_min_u32 v103, v102, v103\n\t"
    "v_max_u32 v102, v104, v105\n\t"
    "v_min_u32 v105, v104, v105\n\t"
    "v_max_u32 v104, v106, v107\n\t"
    "v_min_u32 v107, v106, v107\n\t"
    "v_max_u32 v106, v108, v100\n\t"
    "v_min_u32 v100, v108, v100\n\t"
    "v_max_u32 v108, v101, v103\n\t"
    "v_min_u32 v103, v101, v103\n\t"
    "v_max_u32 v101, v102, v104\n\t"
    "v_min_u32 v104, v102, v104\n\t"
    "v_max_u32 v102, v105, v107\n\t"
    "v_min_u32 v107, v105, v107\n\t"
    "v_bfi_b32 v130, v106, 0, v129\n\t"
    "v_cmp_eq_u32 vcc, 0, v130\n\t"
    "v_and_or_b32 v92, v106, s44, v69\n\t"
    "s_nop 0\n\t"
    "v_cndmask_b32 v130, v130, v129, vcc\n\t"
    "v_ffbl_b32 v131, v130\n\t"
    "v_and_b32 v134, 7, v131\n\t"
    "v_lshlrev_b32 v134, v134, s45\n\t"
    "v_xor_b32 v129, v129, v134\n\t"
    "v_xor_b32 v132, v131, v127\n\t"
    "v_bfi_b32 v130, v101, 0, v129\n\t"
    "v_cmp_eq_u32 vcc, 0, v130\n\t"
    "v_and_or_b32 v93, v101, s44, v69\n\t"
    "v_xor_b32 v133, 23, v132\n\t"
    "v_lshlrev_b32 v110, v132, v136\n\t"
    "v_and_b32 v132, 7, v132\n\t"
    "v_lshl_or_b32 v110, v136, v133, v110\n\t"
    "v_lshl_add_u32 v132, v132, 1, v128\n\t"
    "ds_or_rtn_b32 v118, v92, v110\n\t"
    "ds_write_b16 v132, v106\n\t"
    "v_cndmask_b32 v130, v130, v129, vcc\n\t"
    "v_ffbl_b32 v131, v130\n\t"
    "v_and_b32 v134, 7, v131\n\t"
    "v_lshlrev_b32 v134, v134, s45\n\t"
    "v_xor_b32 v129, v129, v134\n\t"
    "v_xor_b32 v132, v131, v127\n\t"
    "v_bfi_b32 v130, v108, 0, v129\n\t"
    "v_cmp_eq_u32 vcc, 0, v130\n\t"
    "v_and_or_b32 v94, v108, s44, v69\n\t"
    "v_xor_b32 v133, 23, v132\n\t"
    "v_lshlrev_b32 v111, v132, v136\n\t"
    "v_and_b32 v132, 7, v132\n\t"
    "v_lshl_or_b32 v111, v136, v133, v111\n\t"
    "v_lshl_add_u32 v132, v132, 1, v128\n\t"
    "ds_or_rtn_b32 v119, v93, v111\n\t"
    "ds_write_b16 v132, v101\n\t"
    "v_cndmask_b32 v130, v130, v129, vcc\n\t"
    "v_ffbl_b32 v131, v130\n\t"
    "v_and_b32 v134, 7, v131\n\t"
    "v_lshlrev_b32 v134, v134, s45\n\t"
    "v_xor_b32 v129, v129, v134\n\t"
    "v_xor_b32 v132, v131, v127\n\t"
    "s_waitcnt lgkmcnt(10)\n\t"
    "buffer_store_dwordx4 v[156:159], v139, %12, s48 offen sc1\n\t"
    "buffer_store_dwordx4 v[160:163], v140, %12, s48 offen sc1\n\t"
    "s_add_u32 s48, s48, 819200\n\t"
    "s_waitcnt lgkmcnt(4)\n\t"
    "buffer_store_dwordx4 v[164:167], v139, %12, s48 offen sc1\n\t"
    "buffer_store_dwordx4 v[168:171], v140, %12, s48 offen sc1\n\t"
    "s_add_u32 s48, s48, -811008\n\t"
    "s_waitcnt vmcnt(34)\n\t"
    "ds_write_b128 v141, v[172:175] offset:0\n\t"
    "ds_write_b128 v141, v[176:179] offset:256\n\t"
    "ds_read_b64_tr_b16 v[172:173], v142 offset:0\n\t"
    "ds_read_b64_tr_b16 v[174:175], v142 offset:256\n\t"
    "ds_read_b64_tr_b16 v[176:177], v142 offset:32\n\t"
    "ds_read_b64_tr_b16 v[178:179], v142 offset:288\n\t"
    "s_waitcnt vmcnt(32)\n\t"
    "ds_write_b128 v141, v[180:183] offset:2112\n\t"
    "ds_write_b128 v141, v[184:187] offset:2368\n\t"
    "ds_read_b64_tr_b16 v[180:181], v142 offset:2112\n\t"
    "ds_read_b64_tr_b16 v[182:183], v142 offset:2368\n\t"
    "ds_read_b64_tr_b16 v[184:185], v142 offset:2144\n\t"
    "ds_read_b64_tr_b16 v[186:187], v142 offset:2400\n\t"
    "v_bfi_b32 v130, v100, 0, v129\n\t"
    "v_cmp_eq_u32 vcc, 0, v130\n\t"
    "v_and_or_b32 v95, v100, s44, v69\n\t"
    "v_xor_b32 v133, 23, v132\n\t"
    "v_lshlrev_b32 v112, v132, v136\n\t"
    "v_and_b32 v132, 7, v132\n\t"
    "v_lshl_or_b32 v112, v136, v133, v112\n\t"
    "v_lshl_add_u32 v132, v132, 1, v128\n\t"
    "ds_or_rtn_b32 v120, v94, v112\n\t"
    "ds_write_b16 v132, v108\n\t"
    "v_cndmask_b32 v130, v130, v129, vcc\n\t"
    "v_ffbl_b32 v131, v130\n\t"
    "v_and_b32 v134, 7, v131\n\t"
    "v_lshlrev_b32 v134, v134, s45\n\t"
    "v_xor_b32 v129, v129, v134\n\t"
    "v_xor_b32 v132, v131, v127\n\t"
    "v_bfi_b32 v130, v102, 0, v129\n\t"
    "v_cmp_eq_u32 vcc, 0, v130\n\t"
    "v_and_or_b32 v96, v102, s44, v69\n\t"
    "v_xor_b32 v133, 23, v132\n\t"
    "v_lshlrev_b32 v113, v132, v136\n\t"
    "v_and_b32 v132, 7, v132\n\t"
    "v_lshl_or_b32 v113, v136, v133, v113\n\t"
    "v_lshl_add_u32 v132, v132, 1, v128\n\t"
    "ds_or_rtn_b32 v121, v95, v113\n\t"
    "ds_write_b16 v132, v100\n\t"
    "v_cndmask_b32 v130, v130, v129, vcc\n\t"
    "v_ffbl_b32 v131, v130\n\t"
    "v_and_b32 v134, 7, v131\n\t"
    "v_lshlrev_b32 v134, v134, s45\n\t"
    "v_xor_b32 v129, v129, v134\n\t"
    "v_xor_b32 v132, v131, v127\n\t"
    "s_waitcnt lgkmcnt(15)\n\t"
    "v_and_b32 v118, v118, v110\n\t"
    "v_perm_b32 v118, v118, v118, s46\n\t"
    "ds_or_b32 v92, v118\n\t"
    "v_bfi_b32 v130, v104, 0, v129\n\t"
    "v_cmp_eq_u32 vcc, 0, v130\n\t"
    "v_and_or_b32 v97, v104, s44, v69\n\t"
    "v_xor_b32 v133, 23, v132\n\t"
    "v_lshlrev_b32 v114, v132, v136\n\t"
    "v_and_b32 v132, 7, v132\n\t"
    "v_lshl_or_b32 v114, v136, v133, v114\n\t"
    "v_lshl_add_u32 v132, v132, 1, v128\n\t"
    "ds_or_rtn_b32 v122, v96, v114\n\t"
    "ds_write_b16 v132, v102\n\t"
    "v_cndmask_b32 v130, v130, v129, vcc\n\t"
    "v_ffbl_b32 v131, v130\n\t"
    "v_and_b32 v134, 7, v131\n\t"
    "v_lshlrev_b32 v134, v134, s45\n\t"
    "v_xor_b32 v129, v129, v134\n\t"
    "v_xor_b32 v132, v131, v127\n\t"
    "v_and_b32 v119, v119, v111\n\t"
    "v_perm_b32 v119, v119, v119, s46\n\t"
    "ds_or_b32 v93, v119\n\t"
    "s_waitcnt lgkmcnt(14)\n\t"
    "buffer_store_dwordx4 v[172:175], v139, %12, s48 offen sc1\n\t"
    "buffer_store_dwordx4 v[176:179], v140, %12, s48 offen sc1\n\t"
    "s_add_u32 s48, s48, 819200\n\t"
    "s_waitcnt lgkmcnt(8)\n\t"
    "buffer_store_dwordx4 v[180:183], v139, %12, s48 offen sc1\n\t"
    "buffer_store_dwordx4 v[184:187], v140, %12, s48 offen sc1\n\t"
    "s_add_u32 s48, s48, -811008\n\t"
    "s_waitcnt vmcnt(34)\n\t"
    "ds_write_b128 v141, v[188:191] offset:0\n\t"
    "ds_write_b128 v141, v[192:195] offset:256\n\t"
    "ds_read_b64_tr_b16 v[188:189], v142 offset:0\n\t"
    "ds_read_b64_tr_b16 v[190:191], v142 offset:256\n\t"
    "ds_read_b64_tr_b16 v[192:193], v142 offset:32\n\t"
    "ds_read_b64_tr_b16 v[194:195], v142 offset:288\n\t"
    "s_waitcnt vmcnt(32)\n\t"
    "ds_write_b128 v141, v[196:199] offset:2112\n\t"
    "ds_write_b128 v141, v[200:203] offset:2368\n\t"
    "ds_read_b64_tr_b16 v[196:197], v142 offset:2112\n\t"
    "ds_read_b64_tr_b16 v[198:199], v142 offset:2368\n\t"
    "ds_read_b64_tr_b16 v[200:201], v142 offset:2144\n\t"
    "ds_read_b64_tr_b16 v[202:203], v142 offset:2400\n\t"
    "v_bfi_b32 v130, v103, 0, v129\n\t"
    "v_cmp_eq_u32 vcc, 0, v130\n\t"
    "v_and_or_b32 v98, v103, s44, v69\n\t"
    "v_xor_b32 v133, 23, v132\n\t"
    "v_lshlrev_b32 v115, v132, v136\n\t"
    "v_and_b32 v132, 7, v132\n\t"
    "v_lshl_or_b32 v115, v136, v133, v115\n\t"
    "v_lshl_add_u32 v132, v132, 1, v128\n\t"
    "ds_or_rtn_b32 v123, v97, v115\n\t"
    "ds_write_b16 v132, v104\n\t"
    "v_cndmask_b32 v130, v130, v129, vcc\n\t"
    "v_ffbl_b32 v131, v130\n\t"
    "v_and_b32 v134, 7, v131\n\t"
    "v_lshlrev_b32 v134, v134, s45\n\t"
    "v_xor_b32 v129, v129, v134\n\t"
    "v_xor_b32 v132, v131, v127\n\t"
    "s_waitcnt lgkmcnt(15)\n\t"
    "v_and_b32 v120, v120, v112\n\t"
    "v_perm_b32 v120, v120, v120, s46\n\t"
    "ds_or_b32 v94, v120\n\t"
    "v_bfi_b32 v130, v107, 0, v129\n\t"
    "v_cmp_eq_u32 vcc, 0, v130\n\t"
    "v_and_or_b32 v99, v107, s44, v69\n\t"
    "v_xor_b32 v133, 23, v132\n\t"
    "v_lshlrev_b32 v116, v132, v136\n\t"
    "v_and_b32 v132, 7, v132\n\t"
    "v_lshl_or_b32 v116, v136, v133, v116\n\t"
    "v_lshl_add_u32 v132, v132, 1, v128\n\t"
    "ds_or_rtn_b32 v124, v98, v116\n\t"
    "ds_write_b16 v132, v103\n\t"
    "v_cndmask_b32 v130, v130, v129, vcc\n\t"
    "v_ffbl_b32 v131, v130\n\t"
    "v_xor_b32 v132, v131, v127\n\t"
    "v_and_b32 v121, v121, v113\n\t"
    "v_perm_b32 v121, v121, v121, s46\n\t"
    "ds_or_b32 v95, v121\n\t"
    "v_xor_b32 v133, 23, v132\n\t"
    "v_lshlrev_b32 v117, v132, v136\n\t"
    "v_and_b32 v132, 7, v132\n\t"
    "v_lshl_or_b32 v117, v136, v133, v117\n\t"
    "v_lshl_add_u32 v132, v132, 1, v128\n\t"
    "ds_or_rtn_b32 v125, v99, v117\n\t"
    "ds_write_b16 v132, v107\n\t"
    "v_and_b32 v122, v122, v114\n\t"
    "v_perm_b32 v122, v122, v122, s46\n\t"
    "ds_or_b32 v96, v122\n\t"
    "ds_read_b128 v[72:75], v128\n\t"
    "s_waitcnt lgkmcnt(15)\n\t"
    "buffer_store_dwordx4 v[188:191], v139, %12, s48 offen sc1\n\t"
    "buffer_store_dwordx4 v[192:195], v140, %12, s48 offen sc1\n\t"
    "s_add_u32 s48, s48, 819200\n\t"
    "s_waitcnt lgkmcnt(10)\n\t"
    "buffer_store_dwordx4 v[196:199], v139, %12, s48 offen sc1\n\t"
    "buffer_store_dwordx4 v[200:203], v140, %12, s48 offen sc1\n\t"
    "s_add_u32 s48, s48, -811008\n\t"
    "v_and_or_b32 v100, v76, s44, v69\n\t"
    "ds_read_b32 v100, v100\n\t"
    "v_and_or_b32 v101, v77, s44, v69\n\t"
    "ds_read_b32 v101, v101\n\t"
    "v_and_or_b32 v102, v78, s44, v69\n\t"
    "ds_read_b32 v102, v102\n\t"
    "v_and_or_b32 v103, v79, s44, v69\n\t"
    "ds_read_b32 v103, v103\n\t"
    "v_and_or_b32 v104, v80, s44, v69\n\t"
    "ds_read_b32 v104, v104\n\t"
    "v_and_or_b32 v105, v81, s44, v69\n\t"
    "ds_read_b32 v105, v105\n\t"
    "v_and_or_b32 v106, v82, s44, v69\n\t"
    "ds_read_b32 v106, v106\n\t"
    "v_and_or_b32 v107, v83, s44, v69\n\t"
    "ds_read_b32 v107, v107\n\t"
    "v_lshl_add_u32 v84, v56, 3, s36\n\t"
    "v_lshl_add_u32 v85, v57, 3, s37\n\t"
    "v_lshl_add_u32 v86, v58, 3, s38\n\t"
    "v_lshl_add_u32 v87, v59, 3, s39\n\t"
    "v_lshl_add_u32 v88, v60, 3, s40\n\t"
    "v_lshl_add_u32 v89, v61, 3, s41\n\t"
    "v_lshl_add_u32 v90, v62, 3, s42\n\t"
    "v_lshl_add_u32 v91, v63, 3, s43\n\t"
    "v_mov_b32 v134, 1036\n\t"
    "L_asvsp12b_%=:\n\t"
    "ds_read_b32 v130, v134\n\t"
    "s_waitcnt lgkmcnt(0)\n\t"
    "v_readfirstlane_b32 s50, v130\n\t"
    "s_nop 0\n\t"
    "s_cmp_lt_u32 s50, 3\n\t"
    "s_cbranch_scc1 L_asvsp12b_%=\n\t"
    "s_and_b32 s50, %14, 7\n\t"
    "s_lshr_b32 s51, %14, 5\n\t"
    "s_add_u32 s50, s50, 8\n\t"
    "s_lshl_b32 s50, s50, 7\n\t"
    "s_lshl_b32 s51, s51, 7\n\t"
    "v_mov_b32 v132, s51\n\t"
    "v_writelane_b32 v132, s50, 0\n\t"
    "s_waitcnt vmcnt(0)\n\t"
    "v_cmp_ne_u32 vcc, v71, v70\n\t"
    "s_cbranch_vccz L_ahsok_%=\n\t"
    "s_mov_b32 s51, 1048576\n\t"
    "L_ahs_%=:\n\t"
    "buffer_load_dword v71, off, %12, s52 offset:1024 sc1\n\t"
    "s_sub_u32 s51, s51, 1\n\t"
    "s_waitcnt vmcnt(0)\n\t"
    "s_cmp_eq_u32 s51, 0\n\t"
    "s_cbranch_scc1 L_ahsok_%=\n\t"
    "v_cmp_ne_u32 vcc, v71, v70\n\t"
    "s_cbranch_vccnz L_ahs_%=\n\t"
    "L_ahsok_%=:\n\t"
    "s_mov_b32 s50, 48235520\n\t"
    "s_mov_b64 exec, 3\n\t"
    "buffer_atomic_add v136, v132, %12, s50 offen sc1\n\t"
    "s_mov_b64 exec, -1\n\t"
    "buffer_load_dwordx4 v[156:159], v137, s[96:99], s47 offen offset:0\n\t"
    "buffer_load_dwordx4 v[160:163], v138, s[96:99], s47 offen offset:0\n\t"
    "buffer_load_dwordx4 v[164:167], v137, s[96:99], s47 offen offset:64\n\t"
    "buffer_load_dwordx4 v[168:171], v138, s[96:99], s47 offen offset:64\n\t"
    "s_add_u32 s47, s47, 0x100000\n\t"
    "buffer_load_dwordx4 v[172:175], v137, s[96:99], s47 offen offset:0\n\t"
    "buffer_load_dwordx4 v[176:179], v138, s[96:99], s47 offen offset:0\n\t"
    "buffer_load_dwordx4 v[180:183], v137, s[96:99], s47 offen offset:64\n\t"
    "buffer_load_dwordx4 v[184:187], v138, s[96:99], s47 offen offset:64\n\t"
    "s_add_u32 s47, s47, 0x100000\n\t"
    "buffer_load_dwordx4 v[188:191], v137, s[96:99], s47 offen offset:0\n\t"
    "buffer_load_dwordx4 v[192:195], v138, s[96:99], s47 offen offset:0\n\t"
    "buffer_load_dwordx4 v[196:199], v137, s[96:99], s47 offen offset:64\n\t"
    "buffer_load_dwordx4 v[200:203], v138, s[96:99], s47 offen offset:64\n\t"
    "s_add_u32 s47, s47, 0x100000\n\t"
    "v_and_b32 v123, v123, v115\n\t"
    "v_perm_b32 v123, v123, v123, s46\n\t"
    "ds_or_b32 v97, v123\n\t"
    "v_and_b32 v124, v124, v116\n\t"
    "v_perm_b32 v124, v124, v124, s46\n\t"
    "ds_or_b32 v98, v124\n\t"
    "v_and_b32 v125, v125, v117\n\t"
    "v_perm_b32 v125, v125, v125, s46\n\t"
    "ds_or_b32 v99, v125\n\t"
    "buffer_store_dwordx4 v[72:75], v67, %12, s49 offen offset:320 sc1\n\t"
    "v_mov_b32 v129, 0xffff0000\n\t"
    "v_perm_b32 v100, v100, v76, v126\n\t"
    "v_perm_b32 v101, v101, v77, v126\n\t"
    "v_perm_b32 v102, v102, v78, v126\n\t"
    "v_perm_b32 v103, v103, v79, v126\n\t"
    "v_perm_b32 v104, v104, v80, v126\n\t"
    "v_perm_b32 v105, v105, v81, v126\n\t"
    "v_perm_b32 v106, v106, v82, v126\n\t"
    "v_perm_b32 v107, v107, v83, v126\n\t"
    "ds_write_b128 v141, v[204:207] offset:0\n\t"
    "ds_write_b128 v141, v[208:211] offset:256\n\t"
    "ds_read_b64_tr_b16 v[204:205], v142 offset:0\n\t"
    "ds_read_b64_tr_b16 v[206:207], v142 offset:256\n\t"
    "ds_read_b64_tr_b16 v[208:209], v142 offset:32\n\t"
    "ds_read_b64_tr_b16 v[210:211], v142 offset:288\n\t"
    "ds_write_b128 v141, v[212:215] offset:2112\n\t"
    "ds_write_b128 v141, v[216:219] offset:2368\n\t"
    "ds_read_b64_tr_b16 v[212:213], v142 offset:2112\n\t"
    "ds_read_b64_tr_b16 v[214:215], v142 offset:2368\n\t"
    "ds_read_b64_tr_b16 v[216:217], v142 offset:2144\n\t"
    "ds_read_b64_tr_b16 v[218:219], v142 offset:2400\n\t"
    "v_max_u32 v108, v100, v101\n\t"
    "v_min_u32 v101, v100, v101\n\t"
    "v_max_u32 v100, v102, v103\n\t"
    "v_min_u32 v103, v102, v103\n\t"
    "v_max_u32 v102, v104, v105\n\t"
    "v_min_u32 v105, v104, v105\n\t"
    "v_max_u32 v104, v106, v107\n\t"
    "v_min_u32 v107, v106, v107\n\t"
    "v_max_u32 v106, v108, v100\n\t"
    "v_min_u32 v100, v108, v100\n\t"
    "v_max_u32 v108, v101, v103\n\t"
    "v_min_u32 v103, v101, v103\n\t"
    "v_max_u32 v101, v102, v104\n\t"
    "v_min_u32 v104, v102, v104\n\t"
    "v_max_u32 v102, v105, v107\n\t"
    "v_min_u32 v107, v105, v107\n\t"
    "v_bfi_b32 v130, v106, 0, v129\n\t"
    "v_cmp_eq_u32 vcc, 0, v130\n\t"
    "v_and_or_b32 v92, v106, s44, v69\n\t"
    "s_nop 0\n\t"
    "v_cndmask_b32 v130, v130, v129, vcc\n\t"
    "v_ffbl_b32 v131, v130\n\t"
    "v_and_b32 v134, 7, v131\n\t"
    "v_lshlrev_b32 v134, v134, s45\n\t"
    "v_xor_b32 v129, v129, v134\n\t"
    "v_xor_b32 v132, v131, v127\n\t"
    "v_bfi_b32 v130, v101, 0, v129\n\t"
    "v_cmp_eq_u32 vcc, 0, v130\n\t"
    "v_and_or_b32 v93, v101, s44, v69\n\t"
    "v_xor_b32 v133, 23, v132\n\t"
    "v_lshlrev_b32 v110, v132, v136\n\t"
    "v_and_b32 v132, 7, v132\n\t"
    "v_lshl_or_b32 v110, v136, v133, v110\n\t"
    "v_lshl_add_u32 v132, v132, 1, v128\n\t"
    "ds_or_rtn_b32 v118, v92, v110\n\t"
    "ds_write_b16 v132, v106\n\t"
    "v_cndmask_b32 v130, v130, v129, vcc\n\t"
    "v_ffbl_b32 v131, v130\n\t"
    "v_and_b32 v134, 7, v131\n\t"
    "v_lshlrev_b32 v134, v134, s45\n\t"
    "v_xor_b32 v129, v129, v134\n\t"
    "v_xor_b32 v132, v131, v127\n\t"
    "v_bfi_b32 v130, v108, 0, v129\n\t"
    "v_cmp_eq_u32 vcc, 0, v130\n\t"
    "v_and_or_b32 v94, v108, s44, v69\n\t"
    "v_xor_b32 v133, 23, v132\n\t"
    "v_lshlrev_b32 v111, v132, v136\n\t"
    "v_and_b32 v132, 7, v132\n\t"
    "v_lshl_or_b32 v111, v136, v133, v111\n\t"
    "v_lshl_add_u32 v132, v132, 1, v128\n\t"
    "ds_or_rtn_b32 v119, v93, v111\n\t"
    "ds_write_b16 v132, v101\n\t"
    "v_cndmask_b32 v130, v130, v129, vcc\n\t"
    "v_ffbl_b32 v131, v130\n\t"
    "v_and_b32 v134, 7, v131\n\t"
    "v_lshlrev_b32 v134, v134, s45\n\t"
    "v_xor_b32 v129, v129, v134\n\t"
    "v_xor_b32 v132, v131, v127\n\t"
    "s_waitcnt lgkmcnt(10)\n\t"
    "buffer_store_dwordx4 v[204:207], v139, %12, s48 offen sc1\n\t"
    "buffer_store_dwordx4 v[208:211], v140, %12, s48 offen sc1\n\t"
    "s_add_u32 s48, s48, 819200\n\t"
    "s_waitcnt lgkmcnt(4)\n\t"
    "buffer_store_dwordx4 v[212:215], v139, %12, s48 offen sc1\n\t"
    "buffer_store_dwordx4 v[216:219], v140, %12, s48 offen sc1\n\t"
    "s_add_u32 s48, s48, -811008\n\t"
    "ds_write_b128 v141, v[220:223] offset:0\n\t"
    "ds_write_b128 v141, v[224:227] offset:256\n\t"
    "ds_read_b64_tr_b16 v[220:221], v142 offset:0\n\t"
    "ds_read_b64_tr_b16 v[222:223], v142 offset:256\n\t"
    "ds_read_b64_tr_b16 v[224:225], v142 offset:32\n\t"
    "ds_read_b64_tr_b16 v[226:227], v142 offset:288\n\t"
    "ds_write_b128 v141, v[228:231] offset:2112\n\t"
    "ds_write_b128 v141, v[232:235] offset:2368\n\t"
    "ds_read_b64_tr_b16 v[228:229], v142 offset:2112\n\t"
    "ds_read_b64_tr_b16 v[230:231], v142 offset:2368\n\t"
    "ds_read_b64_tr_b16 v[232:233], v142 offset:2144\n\t"
    "ds_read_b64_tr_b16 v[234:235], v142 offset:2400\n\t"
    "v_bfi_b32 v130, v100, 0, v129\n\t"
    "v_cmp_eq_u32 vcc, 0, v130\n\t"
    "v_and_or_b32 v95, v100, s44, v69\n\t"
    "v_xor_b32 v133, 23, v132\n\t"
    "v_lshlrev_b32 v112, v132, v136\n\t"
    "v_and_b32 v132, 7, v132\n\t"
    "v_lshl_or_b32 v112, v136, v133, v112\n\t"
    "v_lshl_add_u32 v132, v132, 1, v128\n\t"
    "ds_or_rtn_b32 v120, v94, v112\n\t"
    "ds_write_b16 v132, v108\n\t"
    "v_cndmask_b32 v130, v130, v129, vcc\n\t"
    "v_ffbl_b32 v131, v130\n\t"
    "v_and_b32 v134, 7, v131\n\t"
    "v_lshlrev_b32 v134, v134, s45\n\t"
    "v_xor_b32 v129, v129, v134\n\t"
    "v_xor_b32 v132, v131, v127\n\t"
    "v_bfi_b32 v130, v102, 0, v129\n\t"
    "v_cmp_eq_u32 vcc, 0, v130\n\t"
    "v_and_or_b32 v96, v102, s44, v69\n\t"
    "v_xor_b32 v133, 23, v132\n\t"
    "v_lshlrev_b32 v113, v132, v136\n\t"
    "v_and_b32 v132, 7, v132\n\t"
    "v_lshl_or_b32 v113, v136, v133, v113\n\t"
    "v_lshl_add_u32 v132, v132, 1, v128\n\t"
    "ds_or_rtn_b32 v121, v95, v113\n\t"
    "ds_write_b16 v132, v100\n\t"
    "v_cndmask_b32 v130, v130, v129, vcc\n\t"
    "v_ffbl_b32 v131, v130\n\t"
    "v_and_b32 v134, 7, v131\n\t"
    "v_lshlrev_b32 v134, v134, s45\n\t"
    "v_xor_b32 v129, v129, v134\n\t"
    "v_xor_b32 v132, v131, v127\n\t"
    "s_waitcnt lgkmcnt(15)\n\t"
    "v_and_b32 v118, v118, v110\n\t"
    "v_perm_b32 v118, v118, v118, s46\n\t"
    "ds_or_b32 v92, v118\n\t"
    "v_bfi_b32 v130, v104, 0, v129\n\t"
    "v_cmp_eq_u32 vcc, 0, v130\n\t"
    "v_and_or_b32 v97, v104, s44, v69\n\t"
    "v_xor_b32 v133, 23, v132\n\t"
    "v_lshlrev_b32 v114, v132, v136\n\t"
    "v_and_b32 v132, 7, v132\n\t"
    "v_lshl_or_b32 v114, v136, v133, v114\n\t"
    "v_lshl_add_u32 v132, v132, 1, v128\n\t"
    "ds_or_rtn_b32 v122, v96, v114\n\t"
    "ds_write_b16 v132, v102\n\t"
    "v_cndmask_b32 v130, v130, v129, vcc\n\t"
    "v_ffbl_b32 v131, v130\n\t"
    "v_and_b32 v134, 7, v131\n\t"
    "v_lshlrev_b32 v134, v134, s45\n\t"
    "v_xor_b32 v129, v129, v134\n\t"
    "v_xor_b32 v132, v131, v127\n\t"
    "v_and_b32 v119, v119, v111\n\t"
    "v_perm_b32 v119, v119, v119, s46\n\t"
    "ds_or_b32 v93, v119\n\t"
    "s_waitcnt lgkmcnt(14)\n\t"
    "buffer_store_dwordx4 v[220:223], v139, %12, s48 offen sc1\n\t"
    "buffer_store_dwordx4 v[224:227], v140, %12, s48 offen sc1\n\t"
    "s_add_u32 s48, s48, 819200\n\t"
    "s_waitcnt lgkmcnt(8)\n\t"
    "buffer_store_dwordx4 v[228:231], v139, %12, s48 offen sc1\n\t"
    "buffer_store_dwordx4 v[232:235], v140, %12, s48 offen sc1\n\t"
    "s_add_u32 s48, s48, -811008\n\t"
    "ds_write_b128 v141, v[236:239] offset:0\n\t"
    "ds_write_b128 v141, v[240:243] offset:256\n\t"
    "ds_read_b64_tr_b16 v[236:237], v142 offset:0\n\t"
    "ds_read_b64_tr_b16 v[238:239], v142 offset:256\n\t"
    "ds_read_b64_tr_b16 v[240:241], v142 offset:32\n\t"
    "ds_read_b64_tr_b16 v[242:243], v142 offset:288\n\t"
    "ds_write_b128 v141, v[244:247] offset:2112\n\t"
    "ds_write_b128 v141, v[248:251] offset:2368\n\t"
    "ds_read_b64_tr_b16 v[244:245], v142 offset:2112\n\t"
    "ds_read_b64_tr_b16 v[246:247], v142 offset:2368\n\t"
    "ds_read_b64_tr_b16 v[248:249], v142 offset:2144\n\t"
    "ds_read_b64_tr_b16 v[250:251], v142 offset:2400\n\t"
    "v_bfi_b32 v130, v103, 0, v129\n\t"
    "v_cmp_eq_u32 vcc, 0, v130\n\t"
    "v_and_or_b32 v98, v103, s44, v69\n\t"
    "v_xor_b32 v133, 23, v132\n\t"
    "v_lshlrev_b32 v115, v132, v136\n\t"
    "v_and_b32 v132, 7, v132\n\t"
    "v_lshl_or_b32 v115, v136, v133, v115\n\t"
    "v_lshl_add_u32 v132, v132, 1, v128\n\t"
    "ds_or_rtn_b32 v123, v97, v115\n\t"
    "ds_write_b16 v132, v104\n\t"
    "v_cndmask_b32 v130, v130, v129, vcc\n\t"
    "v_ffbl_b32 v131, v130\n\t"
    "v_and_b32 v134, 7, v131\n\t"
    "v_lshlrev_b32 v134, v134, s45\n\t"
    "v_xor_b32 v129, v129, v134\n\t"
    "v_xor_b32 v132, v131, v127\n\t"
    "s_waitcnt lgkmcnt(15)\n\t"
    "v_and_b32 v120, v120, v112\n\t"
    "v_perm_b32 v120, v120, v120, s46\n\t"
    "ds_or_b32 v94, v120\n\t"
    "v_bfi_b32 v130, v107, 0, v129\n\t"
    "v_cmp_eq_u32 vcc, 0, v130\n\t"
    "v_and_or_b32 v99, v107, s44, v69\n\t"
    "v_xor_b32 v133, 23, v132\n\t"
    "v_lshlrev_b32 v116, v132, v136\n\t"
    "v_and_b32 v132, 7, v132\n\t"
    "v_lshl_or_b32 v116, v136, v133, v116\n\t"
    "v_lshl_add_u32 v132, v132, 1, v128\n\t"
    "ds_or_rtn_b32 v124, v98, v116\n\t"
    "ds_write_b16 v132, v103\n\t"
    "v_cndmask_b32 v130, v130, v129, vcc\n\t"
    "v_ffbl_b32 v131, v130\n\t"
    "v_xor_b32 v132, v131, v127\n\t"
    "v_and_b32 v121, v121, v113\n\t"
    "v_perm_b32 v121, v121, v121, s46\n\t"
    "ds_or_b32 v95, v121\n\t"
    "v_xor_b32 v133, 23, v132\n\t"
    "v_lshlrev_b32 v117, v132, v136\n\t"
    "v_and_b32 v132, 7, v132\n\t"
    "v_lshl_or_b32 v117, v136, v133, v117\n\t"
    "v_lshl_add_u32 v132, v132, 1, v128\n\t"
    "ds_or_rtn_b32 v125, v99, v117\n\t"
    "ds_write_b16 v132, v107\n\t"
    "v_and_b32 v122, v122, v114\n\t"
    "v_perm_b32 v122, v122, v122, s46\n\t"
    "ds_or_b32 v96, v122\n\t"
    "ds_read_b128 v[72:75], v128\n\t"
    "v_and_or_b32 v100, v84, s44, v69\n\t"
    "ds_read_b32 v100, v100\n\t"
    "v_and_or_b32 v101, v85, s44, v69\n\t"
    "ds_read_b32 v101, v101\n\t"
    "v_and_or_b32 v102, v86, s44, v69\n\t"
    "ds_read_b32 v102, v102\n\t"
    "v_and_or_b32 v103, v87, s44, v69\n\t"
    "ds_read_b32 v103, v103\n\t"
    "v_and_or_b32 v104, v88, s44, v69\n\t"
    "ds_read_b32 v104, v104\n\t"
    "v_and_or_b32 v105, v89, s44, v69\n\t"
    "ds_read_b32 v105, v105\n\t"
    "v_and_or_b32 v106, v90, s44, v69\n\t"
    "ds_read_b32 v106, v106\n\t"
    "v_and_or_b32 v107, v91, s44, v69\n\t"
    "ds_read_b32 v107, v107\n\t"
    "v_lshl_add_u32 v76, v0, 3, s36\n\t"
    "v_lshl_add_u32 v77, v1, 3, s37\n\t"
    "v_lshl_add_u32 v78, v2, 3, s38\n\t"
    "v_lshl_add_u32 v79, v3, 3, s39\n\t"
    "v_lshl_add_u32 v80, v4, 3, s40\n\t"
    "v_lshl_add_u32 v81, v5, 3, s41\n\t"
    "v_lshl_add_u32 v82, v6, 3, s42\n\t"
    "v_lshl_add_u32 v83, v7, 3, s43\n\t"
    "s_waitcnt lgkmcnt(15)\n\t"
    "buffer_store_dwordx4 v[236:239], v139, %12, s48 offen sc1\n\t"
    "buffer_store_dwordx4 v[240:243], v140, %12, s48 offen sc1\n\t"
    "s_add_u32 s48, s48, 819200\n\t"
    "buffer_store_dwordx4 v[244:247], v139, %12, s48 offen sc1\n\t"
    "buffer_store_dwordx4 v[248:251], v140, %12, s48 offen sc1\n\t"
    "s_add_u32 s48, s48, -811008\n\t"
    "buffer_load_dwordx4 v[56:59], v137, s[96:99], s47 offen offset:0\n\t"
    "buffer_load_dwordx4 v[60:63], v138, s[96:99], s47 offen offset:0\n\t"
    "buffer_load_dwordx4 v[204:207], v137, s[96:99], s47 offen offset:64\n\t"
    "buffer_load_dwordx4 v[208:211], v138, s[96:99], s47 offen offset:64\n\t"
    "s_add_u32 s47, s47, 0x100000\n\t"
    "buffer_load_dwordx4 v[212:215], v137, s[96:99], s47 offen offset:0\n\t"
    "buffer_load_dwordx4 v[216:219], v138, s[96:99], s47 offen offset:0\n\t"
    "buffer_load_dwordx4 v[220:223], v137, s[96:99], s47 offen offset:64\n\t"
    "buffer_load_dwordx4 v[224:227], v138, s[96:99], s47 offen offset:64\n\t"
    "s_add_u32 s47, s47, 0x100000\n\t"
    "s_waitcnt lgkmcnt(11)\n\t"
    "v_and_b32 v123, v123, v115\n\t"
    "v_perm_b32 v123, v123, v123, s46\n\t"
    "ds_or_b32 v97, v123\n\t"
    "v_and_b32 v124, v124, v116\n\t"
    "v_perm_b32 v124, v124, v124, s46\n\t"
    "ds_or_b32 v98, v124\n\t"
    "v_and_b32 v125, v125, v117\n\t"
    "v_perm_b32 v125, v125, v125, s46\n\t"
    "ds_or_b32 v99, v125\n\t"
    "s_waitcnt lgkmcnt(11)\n\t"
    "buffer_store_dwordx4 v[72:75], v67, %12, s49 offen offset:352 sc1\n\t"
    "v_mov_b32 v129, 0xffff0000\n\t"
    "s_waitcnt lgkmcnt(7)\n\t"
    "v_perm_b32 v100, v100, v84, v126\n\t"
    "v_perm_b32 v101, v101, v85, v126\n\t"
    "v_perm_b32 v102, v102, v86, v126\n\t"
    "v_perm_b32 v103, v103, v87, v126\n\t"
    "s_waitcnt lgkmcnt(3)\n\t"
    "v_perm_b32 v104, v104, v88, v126\n\t"
    "v_perm_b32 v105, v105, v89, v126\n\t"
    "v_perm_b32 v106, v106, v90, v126\n\t"
    "v_perm_b32 v107, v107, v91, v126\n\t"
    "ds_write_b128 v141, v[24:27] offset:0\n\t"
    "ds_write_b128 v141, v[28:31] offset:256\n\t"
    "ds_read_b64_tr_b16 v[24:25], v142 offset:0\n\t"
    "ds_read_b64_tr_b16 v[26:27], v142 offset:256\n\t"
    "ds_read_b64_tr_b16 v[28:29], v142 offset:32\n\t"
    "ds_read_b64_tr_b16 v[30:31], v142 offset:288\n\t"
    "ds_write_b128 v141, v[32:35] offset:2112\n\t"
    "ds_write_b128 v141, v[36:39] offset:2368\n\t"
    "ds_read_b64_tr_b16 v[32:33], v142 offset:2112\n\t"
    "ds_read_b64_tr_b16 v[34:35], v142 offset:2368\n\t"
    "ds_read_b64_tr_b16 v[36:37], v142 offset:2144\n\t"
    "ds_read_b64_tr_b16 v[38:39], v142 offset:2400\n\t"
    "v_max_u32 v108, v100, v101\n\t"
    "v_min_u32 v101, v100, v101\n\t"
    "v_max_u32 v100, v102, v103\n\t"
    "v_min_u32 v103, v102, v103\n\t"
    "v_max_u32 v102, v104, v105\n\t"
    "v_min_u32 v105, v104, v105\n\t"
    "v_max_u32 v104, v106, v107\n\t"
    "v_min_u32 v107, v106, v107\n\t"
    "v_max_u32 v106, v108, v100\n\t"
    "v_min_u32 v100, v108, v100\n\t"
    "v_max_u32 v108, v101, v103\n\t"
    "v_min_u32 v103, v101, v103\n\t"
    "v_max_u32 v101, v102, v104\n\t"
    "v_min_u32 v104, v102, v104\n\t"
    "v_max_u32 v102, v105, v107\n\t"
    "v_min_u32 v107, v105, v107\n\t"
    "v_bfi_b32 v130, v106, 0, v129\n\t"
    "v_cmp_eq_u32 vcc, 0, v130\n\t"
    "v_and_or_b32 v92, v106, s44, v69\n\t"
    "s_nop 0\n\t"
    "v_cndmask_b32 v130, v130, v129, vcc\n\t"
    "v_ffbl_b32 v131, v130\n\t"
    "v_and_b32 v134, 7, v131\n\t"
    "v_lshlrev_b32 v134, v134, s45\n\t"
    "v_xor_b32 v129, v129, v134\n\t"
    "v_xor_b32 v132, v131, v127\n\t"
    "v_bfi_b32 v130, v101, 0, v129\n\t"
    "v_cmp_eq_u32 vcc, 0, v130\n\t"
    "v_and_or_b32 v93, v101, s44, v69\n\t"
    "v_xor_b32 v133, 23, v132\n\t"
    "v_lshlrev_b32 v110, v132, v136\n\t"
    "v_and_b32 v132, 7, v132\n\t"
    "v_lshl_or_b32 v110, v136, v133, v110\n\t"
    "v_lshl_add_u32 v132, v132, 1, v128\n\t"
    "ds_or_b32 v92, v110\n\t"
    "ds_write_b16 v132, v106\n\t"
    "v_cndmask_b32 v130, v130, v129, vcc\n\t"
    "v_ffbl_b32 v131, v130\n\t"
    "v_and_b32 v134, 7, v131\n\t"
    "v_lshlrev_b32 v134, v134, s45\n\t"
    "v_xor_b32 v129, v129, v134\n\t"
    "v_xor_b32 v132, v131, v127\n\t"
    "v_bfi_b32 v130, v108, 0, v129\n\t"
    "v_cmp_eq_u32 vcc, 0, v130\n\t"
    "v_and_or_b32 v94, v108, s44, v69\n\t"
    "v_xor_b32 v133, 23, v132\n\t"
    "v_lshlrev_b32 v111, v132, v136\n\t"
    "v_and_b32 v132, 7, v132\n\t"
    "v_lshl_or_b32 v111, v136, v133, v111\n\t"
    "v_lshl_add_u32 v132, v132, 1, v128\n\t"
    "ds_or_b32 v93, v111\n\t"
    "ds_write_b16 v132, v101\n\t"
    "v_cndmask_b32 v130, v130, v129, vcc\n\t"
    "v_ffbl_b32 v131, v130\n\t"
    "v_and_b32 v134, 7, v131\n\t"
    "v_lshlrev_b32 v134, v134, s45\n\t"
    "v_xor_b32 v129, v129, v134\n\t"
    "v_xor_b32 v132, v131, v127\n\t"
    "s_waitcnt lgkmcnt(10)\n\t"
    "buffer_store_dwordx4 v[24:27], v139, %12, s48 offen sc1\n\t"
    "buffer_store_dwordx4 v[28:31], v140, %12, s48 offen sc1\n\t"
    "s_add_u32 s48, s48, 819200\n\t"
    "s_waitcnt lgkmcnt(4)\n\t"
    "buffer_store_dwordx4 v[32:35], v139, %12, s48 offen sc1\n\t"
    "buffer_store_dwordx4 v[36:39], v140, %12, s48 offen sc1\n\t"
    "s_add_u32 s48, s48, -811008\n\t"
    "ds_write_b128 v141, v[40:43] offset:0\n\t"
    "ds_write_b128 v141, v[44:47] offset:256\n\t"
    "ds_read_b64_tr_b16 v[40:41], v142 offset:0\n\t"
    "ds_read_b64_tr_b16 v[42:43], v142 offset:256\n\t"
    "ds_read_b64_tr_b16 v[44:45], v142 offset:32\n\t"
    "ds_read_b64_tr_b16 v[46:47], v142 offset:288\n\t"
    "ds_write_b128 v141, v[48:51] offset:2112\n\t"
    "ds_write_b128 v141, v[52:55] offset:2368\n\t"
    "ds_read_b64_tr_b16 v[48:49], v142 offset:2112\n\t"
    "ds_read_b64_tr_b16 v[50:51], v142 offset:2368\n\t"
    "ds_read_b64_tr_b16 v[52:53], v142 offset:2144\n\t"
    "ds_read_b64_tr_b16 v[54:55], v142 offset:2400\n\t"
    "v_bfi_b32 v130, v100, 0, v129\n\t"
    "v_cmp_eq_u32 vcc, 0, v130\n\t"
    "v_and_or_b32 v95, v100, s44, v69\n\t"
    "v_xor_b32 v133, 23, v132\n\t"
    "v_lshlrev_b32 v112, v132, v136\n\t"
    "v_and_b32 v132, 7, v132\n\t"
    "v_lshl_or_b32 v112, v136, v133, v112\n\t"
    "v_lshl_add_u32 v132, v132, 1, v128\n\t"
    "ds_or_b32 v94, v112\n\t"
    "ds_write_b16 v132, v108\n\t"
    "v_cndmask_b32 v130, v130, v129, vcc\n\t"
    "v_ffbl_b32 v131, v130\n\t"
    "v_and_b32 v134, 7, v131\n\t"
    "v_lshlrev_b32 v134, v134, s45\n\t"
    "v_xor_b32 v129, v129, v134\n\t"
    "v_xor_b32 v132, v131, v127\n\t"
    "v_bfi_b32 v130, v102, 0, v129\n\t"
    "v_cmp_eq_u32 vcc, 0, v130\n\t"
    "v_and_or_b32 v96, v102, s44, v69\n\t"
    "v_xor_b32 v133, 23, v132\n\t"
    "v_lshlrev_b32 v113, v132, v136\n\t"
    "v_and_b32 v132, 7, v132\n\t"
    "v_lshl_or_b32 v113, v136, v133, v113\n\t"
    "v_lshl_add_u32 v132, v132, 1, v128\n\t"
    "ds_or_b32 v95, v113\n\t"
    "ds_write_b16 v132, v100\n\t"
    "v_cndmask_b32 v130, v130, v129, vcc\n\t"
    "v_ffbl_b32 v131, v130\n\t"
    "v_and_b32 v134, 7, v131\n\t"
    "v_lshlrev_b32 v134, v134, s45\n\t"
    "v_xor_b32 v129, v129, v134\n\t"
    "v_xor_b32 v132, v131, v127\n\t"
    "v_mbcnt_lo_u32_b32 v70, -1, 0\n\t"
    "v_mbcnt_hi_u32_b32 v70, -1, v70\n\t"
    "v_add_u32 v70, 8, v70\n\t"
    "s_and_b32 s50, %14, 7\n\t"
    "s_add_u32 s50, s50, 16\n\t"
    "v_writelane_b32 v70, s50, 8\n\t"
    "v_mov_b32 v148, 32\n\t"
    "v_lshlrev_b32 v70, 7, v70\n\t"
    "s_mov_b32 s52, 48234496\n\t"
    "s_mov_b64 exec, 0x1ff\n\t"
    "buffer_load_dword v71, v70, %12, s52 offen sc1\n\t"
    "s_mov_b64 exec, -1\n\t"
    "v_bfi_b32 v130, v104, 0, v129\n\t"
    "v_cmp_eq_u32 vcc, 0, v130\n\t"
    "v_and_or_b32 v97, v104, s44, v69\n\t"
    "v_xor_b32 v133, 23, v132\n\t"
    "v_lshlrev_b32 v114, v132, v136\n\t"
    "v_and_b32 v132, 7, v132\n\t"
    "v_lshl_or_b32 v114, v136, v133, v114\n\t"
    "v_lshl_add_u32 v132, v132, 1, v128\n\t"
    "ds_or_b32 v96, v114\n\t"
    "ds_write_b16 v132, v102\n\t"
    "v_cndmask_b32 v130, v130, v129, vcc\n\t"
    "v_ffbl_b32 v131, v130\n\t"
    "v_and_b32 v134, 7, v131\n\t"
    "v_lshlrev_b32 v134, v134, s45\n\t"
    "v_xor_b32 v129, v129, v134\n\t"
    "v_xor_b32 v132, v131, v127\n\t"
    "s_waitcnt lgkmcnt(12)\n\t"
    "buffer_store_dwordx4 v[40:43], v139, %12, s48 offen sc1\n\t"
    "buffer_store_dwordx4 v[44:47], v140, %12, s48 offen sc1\n\t"
    "s_add_u32 s48, s48, 819200\n\t"
    "s_waitcnt lgkmcnt(6)\n\t"
    "buffer_store_dwordx4 v[48:51], v139, %12, s48 offen sc1\n\t"
    "buffer_store_dwordx4 v[52:55], v140, %12, s48 offen sc1\n\t"
    "s_add_u32 s48, s48, -811008\n\t"
    "v_bfi_b32 v130, v103, 0, v129\n\t"
    "v_cmp_eq_u32 vcc, 0, v130\n\t"
    "v_and_or_b32 v98, v103, s44, v69\n\t"
    "v_xor_b32 v133, 23, v132\n\t"
    "v_lshlrev_b32 v115, v132, v136\n\t"
    "v_and_b32 v132, 7, v132\n\t"
    "v_lshl_or_b32 v115, v136, v133, v115\n\t"
    "v_lshl_add_u32 v132, v132, 1, v128\n\t"
    "ds_or_b32 v97, v115\n\t"
    "ds_write_b16 v132, v104\n\t"
    "v_cndmask_b32 v130, v130, v129, vcc\n\t"
    "v_ffbl_b32 v131, v130\n\t"
    "v_and_b32 v134, 7, v131\n\t"
    "v_lshlrev_b32 v134, v134, s45\n\t"
    "v_xor_b32 v129, v129, v134\n\t"
    "v_xor_b32 v132, v131, v127\n\t"
    "v_bfi_b32 v130, v107, 0, v129\n\t"
    "v_cmp_eq_u32 vcc, 0, v130\n\t"
    "v_and_or_b32 v99, v107, s44, v69\n\t"
    "v_xor_b32 v133, 23, v132\n\t"
    "v_lshlrev_b32 v116, v132, v136\n\t"
    "v_and_b32 v132, 7, v132\n\t"
    "v_lshl_or_b32 v116, v136, v133, v116\n\t"
    "v_lshl_add_u32 v132, v132, 1, v128\n\t"
    "ds_or_b32 v98, v116\n\t"
    "ds_write_b16 v132, v103\n\t"
    "v_cndmask_b32 v130, v130, v129, vcc\n\t"
    "v_ffbl_b32 v131, v130\n\t"
    "v_xor_b32 v132, v131, v127\n\t"
    "v_xor_b32 v133, 23, v132\n\t"
    "v_lshlrev_b32 v117, v132, v136\n\t"
    "v_and_b32 v132, 7, v132\n\t"
    "v_lshl_or_b32 v117, v136, v133, v117\n\t"
    "v_lshl_add_u32 v132, v132, 1, v128\n\t"
    "ds_or_b32 v99, v117\n\t"
    "ds_write_b16 v132, v107\n\t"
    "ds_read_b128 v[72:75], v128\n\t"
    "v_and_or_b32 v100, v76, s44, v69\n\t"
    "ds_read_b32 v100, v100\n\t"
    "v_and_or_b32 v101, v77, s44, v69\n\t"
    "ds_read_b32 v101, v101\n\t"
    "v_and_or_b32 v102, v78, s44, v69\n\t"
    "ds_read_b32 v102, v102\n\t"
    "v_and_or_b32 v103, v79, s44, v69\n\t"
    "ds_read_b32 v103, v103\n\t"
    "v_and_or_b32 v104, v80, s44, v69\n\t"
    "ds_read_b32 v104, v104\n\t"
    "v_and_or_b32 v105, v81, s44, v69\n\t"
    "ds_read_b32 v105, v105\n\t"
    "v_and_or_b32 v106, v82, s44, v69\n\t"
    "ds_read_b32 v106, v106\n\t"
    "v_and_or_b32 v107, v83, s44, v69\n\t"
    "ds_read_b32 v107, v107\n\t"
    "v_lshl_add_u32 v84, v8, 3, s36\n\t"
    "v_lshl_add_u32 v85, v9, 3, s37\n\t"
    "v_lshl_add_u32 v86, v10, 3, s38\n\t"
    "v_lshl_add_u32 v87, v11, 3, s39\n\t"
    "v_lshl_add_u32 v88, v12, 3, s40\n\t"
    "v_lshl_add_u32 v89, v13, 3, s41\n\t"
    "v_lshl_add_u32 v90, v14, 3, s42\n\t"
    "v_lshl_add_u32 v91, v15, 3, s43\n\t"
    "s_waitcnt vmcnt(4)\n\t"
    "s_mov_b32 s51, 1048576\n\t"
    "s_mov_b64 exec, 0x1ff\n\t"
    "L_af1_%=:\n\t"
    "v_cmp_gt_u32 vcc, v148, v71\n\t"
    "s_cbranch_vccz L_af1ok_%=\n\t"
    "s_sleep 8\n\t"
    "buffer_load_dword v71, v70, %12, s52 offen sc1\n\t"
    "s_sub_u32 s51, s51, 1\n\t"
    "s_waitcnt vmcnt(0)\n\t"
    "s_cmp_lg_u32 s51, 0\n\t"
    "s_cbranch_scc1 L_af1_%=\n\t"
    "L_af1ok_%=:\n\t"
    "s_mov_b64 exec, -1\n\t"
    "v_mov_b32 v134, 3112\n\t"
    "ds_write_b32 v134, v136\n\t"
    "v_mbcnt_lo_u32_b32 v70, -1, 0\n\t"
    "v_mbcnt_hi_u32_b32 v70, -1, v70\n\t"
    "s_and_b32 s50, %14, 7\n\t"
    "s_lshl_b32 s50, s50, 4\n\t"
    "s_lshr_b32 s51, %14, 4\n\t"
    "s_add_u32 s50, s50, s51\n\t"
    "s_mul_i32 s50, s50, 102400\n\t"
    "s_add_u32 s50, s50, 3407872\n\t"
    "v_lshlrev_b32 v148, 7, v70\n\t"
    "buffer_load_dword v71, v148, %12, s50 offen sc1\n\t"
    "s_and_b32 s51, %14, 8\n\t"
    "s_lshl_b32 s51, s51, 10\n\t"
    "s_add_u32 s50, s50, s51\n\t"
    "s_add_u32 s50, s50, 0x1000\n\t"
    "v_lshrrev_b32 v149, 4, v70\n\t"
    "v_lshl_add_u32 v149, v149, 11, v148\n\t"
    "buffer_load_dword v71, v148, %12, s50 offen sc1\n\t"
    "s_bfe_u32 s50, %14, 0x40003\n\t"
    "s_lshl_b32 s50, s50, 14\n\t"
    "s_add_u32 s50, s50, 16517120\n\t"
    "v_lshrrev_b32 v150, 3, v70\n\t"
    "v_lshl_add_u32 v150, v150, 10, v148\n\t"
    "buffer_load_dword v71, v149, %12, s50 offen sc1\n\t"
    "s_and_b32 s50, %14, 8\n\t"
    "s_bfe_u32 s51, %14, 0x30004\n\t"
    "s_add_u32 s50, s50, s51\n\t"
    "s_mul_i32 s50, s50, 212992\n\t"
    "s_add_u32 s50, s50, 196608\n\t"
    "buffer_load_dword v71, v150, %12, s50 offen sc1\n\t"
    "s_waitcnt lgkmcnt(9)\n\t"
    "buffer_store_dwordx4 v[72:75], v67, %12, s49 offen offset:384 sc1\n\t"
    "v_mov_b32 v129, 0xffff0000\n\t"
    "s_waitcnt lgkmcnt(5)\n\t"
    "v_perm_b32 v100, v100, v76, v126\n\t"
    "v_perm_b32 v101, v101, v77, v126\n\t"
    "v_perm_b32 v102, v102, v78, v126\n\t"
    "v_perm_b32 v103, v103, v79, v126\n\t"
    "s_waitcnt lgkmcnt(1)\n\t"
    "v_perm_b32 v104, v104, v80, v126\n\t"
    "v_perm_b32 v105, v105, v81, v126\n\t"
    "v_perm_b32 v106, v106, v82, v126\n\t"
    "v_perm_b32 v107, v107, v83, v126\n\t"
    "ds_write_b128 v141, v[156:159] offset:0\n\t"
    "ds_write_b128 v141, v[160:163] offset:256\n\t"
    "ds_read_b64_tr_b16 v[156:157], v142 offset:0\n\t"
    "ds_read_b64_tr_b16 v[158:159], v142 offset:256\n\t"
    "ds_read_b64_tr_b16 v[160:161], v142 offset:32\n\t"
    "ds_read_b64_tr_b16 v[162:163], v142 offset:288\n\t"
    "ds_write_b128 v141, v[164:167] offset:2112\n\t"
    "ds_write_b128 v141, v[168:171] offset:2368\n\t"
    "ds_read_b64_tr_b16 v[164:165], v142 offset:2112\n\t"
    "ds_read_b64_tr_b16 v[166:167], v142 offset:2368\n\t"
    "ds_read_b64_tr_b16 v[168:169], v142 offset:2144\n\t"
    "ds_read_b64_tr_b16 v[170:171], v142 offset:2400\n\t"
    "v_max_u32 v108, v100, v101\n\t"
    "v_min_u32 v101, v100, v101\n\t"
    "v_max_u32 v100, v102, v103\n\t"
    "v_min_u32 v103, v102, v103\n\t"
    "v_max_u32 v102, v104, v105\n\t"
    "v_min_u32 v105, v104, v105\n\t"
    "v_max_u32 v104, v106, v107\n\t"
    "v_min_u32 v107, v106, v107\n\t"
    "v_max_u32 v106, v108, v100\n\t"
    "v_min_u32 v100, v108, v100\n\t"
    "v_max_u32 v108, v101, v103\n\t"
    "v_min_u32 v103, v101, v103\n\t"
    "v_max_u32 v101, v102, v104\n\t"
    "v_min_u32 v104, v102, v104\n\t"
    "v_max_u32 v102, v105, v107\n\t"
    "v_min_u32 v107, v105, v107\n\t"
    "v_bfi_b32 v130, v106, 0, v129\n\t"
    "v_cmp_eq_u32 vcc, 0, v130\n\t"
    "v_and_or_b32 v92, v106, s44, v69\n\t"
    "s_nop 0\n\t"
    "v_cndmask_b32 v130, v130, v129, vcc\n\t"
    "v_ffbl_b32 v131, v130\n\t"
    "v_and_b32 v134, 7, v131\n\t"
    "v_lshlrev_b32 v134, v134, s45\n\t"
    "v_xor_b32 v129, v129, v134\n\t"
    "v_xor_b32 v132, v131, v127\n\t"
    "v_bfi_b32 v130, v101, 0, v129\n\t"
    "v_cmp_eq_u32 vcc, 0, v130\n\t"
    "v_and_or_b32 v93, v101, s44, v69\n\t"
    "v_xor_b32 v133, 23, v132\n\t"
    "v_lshlrev_b32 v110, v132, v136\n\t"
    "v_and_b32 v132, 7, v132\n\t"
    "v_lshl_or_b32 v110, v136, v133, v110\n\t"
    "v_lshl_add_u32 v132, v132, 1, v128\n\t"
    "ds_or_b32 v92, v110\n\t"
    "ds_write_b16 v132, v106\n\t"
    "v_cndmask_b32 v130, v130, v129, vcc\n\t"
    "v_ffbl_b32 v131, v130\n\t"
    "v_and_b32 v134, 7, v131\n\t"
    "v_lshlrev_b32 v134, v134, s45\n\t"
    "v_xor_b32 v129, v129, v134\n\t"
    "v_xor_b32 v132, v131, v127\n\t"
    "v_bfi_b32 v130, v108, 0, v129\n\t"
    "v_cmp_eq_u32 vcc, 0, v130\n\t"
    "v_and_or_b32 v94, v108, s44, v69\n\t"
    "v_xor_b32 v133, 23, v132\n\t"
    "v_lshlrev_b32 v111, v132, v136\n\t"
    "v_and_b32 v132, 7, v132\n\t"
    "v_lshl_or_b32 v111, v136, v133, v111\n\t"
    "v_lshl_add_u32 v132, v132, 1, v128\n\t"
    "ds_or_b32 v93, v111\n\t"
    "ds_write_b16 v132, v101\n\t"
    "v_cndmask_b32 v130, v130, v129, vcc\n\t"
    "v_ffbl_b32 v131, v130\n\t"
    "v_and_b32 v134, 7, v131\n\t"
    "v_lshlrev_b32 v134, v134, s45\n\t"
    "v_xor_b32 v129, v129, v134\n\t"
    "v_xor_b32 v132, v131, v127\n\t"
    "s_waitcnt lgkmcnt(10)\n\t"
    "buffer_store_dwordx4 v[156:159], v139, %12, s48 offen sc1\n\t"
    "buffer_store_dwordx4 v[160:163], v140, %12, s48 offen sc1\n\t"
    "s_add_u32 s48, s48, 819200\n\t"
    "s_waitcnt lgkmcnt(4)\n\t"
    "buffer_store_dwordx4 v[164:167], v139, %12, s48 offen sc1\n\t"
    "buffer_store_dwordx4 v[168:171], v140, %12, s48 offen sc1\n\t"
    "s_add_u32 s48, s48, -811008\n\t"
    "ds_write_b128 v141, v[172:175] offset:0\n\t"
    "ds_write_b128 v141, v[176:179] offset:256\n\t"
    "ds_read_b64_tr_b16 v[172:173], v142 offset:0\n\t"
    "ds_read_b64_tr_b16 v[174:175], v142 offset:256\n\t"
    "ds_read_b64_tr_b16 v[176:177], v142 offset:32\n\t"
    "ds_read_b64_tr_b16 v[178:179], v142 offset:288\n\t"
    "ds_write_b128 v141, v[180:183] offset:2112\n\t"
    "ds_write_b128 v141, v[184:187] offset:2368\n\t"
    "ds_read_b64_tr_b16 v[180:181], v142 offset:2112\n\t"
    "ds_read_b64_tr_b16 v[182:183], v142 offset:2368\n\t"
    "ds_read_b64_tr_b16 v[184:185], v142 offset:2144\n\t"
    "ds_read_b64_tr_b16 v[186:187], v142 offset:2400\n\t"
    "v_bfi_b32 v130, v100, 0, v129\n\t"
    "v_cmp_eq_u32 vcc, 0, v130\n\t"
    "v_and_or_b32 v95, v100, s44, v69\n\t"
    "v_xor_b32 v133, 23, v132\n\t"
    "v_lshlrev_b32 v112, v132, v136\n\t"
    "v_and_b32 v132, 7, v132\n\t"
    "v_lshl_or_b32 v112, v136, v133, v112\n\t"
    "v_lshl_add_u32 v132, v132, 1, v128\n\t"
    "ds_or_b32 v94, v112\n\t"
    "ds_write_b16 v132, v108\n\t"
    "v_cndmask_b32 v130, v130, v129, vcc\n\t"
    "v_ffbl_b32 v131, v130\n\t"
    "v_and_b32 v134, 7, v131\n\t"
    "v_lshlrev_b32 v134, v134, s45\n\t"
    "v_xor_b32 v129, v129, v134\n\t"
    "v_xor_b32 v132, v131, v127\n\t"
    "v_bfi_b32 v130, v102, 0, v129\n\t"
    "v_cmp_eq_u32 vcc, 0, v130\n\t"
    "v_and_or_b32 v96, v102, s44, v69\n\t"
    "v_xor_b32 v133, 23, v132\n\t"
    "v_lshlrev_b32 v113, v132, v136\n\t"
    "v_and_b32 v132, 7, v132\n\t"
    "v_lshl_or_b32 v113, v136, v133, v113\n\t"
    "v_lshl_add_u32 v132, v132, 1, v128\n\t"
    "ds_or_b32 v95, v113\n\t"
    "ds_write_b16 v132, v100\n\t"
    "v_cndmask_b32 v130, v130, v129, vcc\n\t"
    "v_ffbl_b32 v131, v130\n\t"
    "v_and_b32 v134, 7, v131\n\t"
    "v_lshlrev_b32 v134, v134, s45\n\t"
    "v_xor_b32 v129, v129, v134\n\t"
    "v_xor_b32 v132, v131, v127\n\t"
    "v_bfi_b32 v130, v104, 0, v129\n\t"
    "v_cmp_eq_u32 vcc, 0, v130\n\t"
    "v_and_or_b32 v97, v104, s44, v69\n\t"
    "v_xor_b32 v133, 23, v132\n\t"
    "v_lshlrev_b32 v114, v132, v136\n\t"
    "v_and_b32 v132, 7, v132\n\t"
    "v_lshl_or_b32 v114, v136, v133, v114\n\t"
    "v_lshl_add_u32 v132, v132, 1, v128\n\t"
    "ds_or_b32 v96, v114\n\t"
    "ds_write_b16 v132, v102\n\t"
    "v_cndmask_b32 v130, v130, v129, vcc\n\t"
    "v_ffbl_b32 v131, v130\n\t"
    "v_and_b32 v134, 7, v131\n\t"
    "v_lshlrev_b32 v134, v134, s45\n\t"
    "v_xor_b32 v129, v129, v134\n\t"
    "v_xor_b32 v132, v131, v127\n\t"
    "s_waitcnt lgkmcnt(12)\n\t"
    "buffer_store_dwordx4 v[172:175], v139, %12, s48 offen sc1\n\t"
    "buffer_store_dwordx4 v[176:179], v140, %12, s48 offen sc1\n\t"
    "s_add_u32 s48, s48, 819200\n\t"
    "s_waitcnt lgkmcnt(6)\n\t"
    "buffer_store_dwordx4 v[180:183], v139, %12, s48 offen sc1\n\t"
    "buffer_store_dwordx4 v[184:187], v140, %12, s48 offen sc1\n\t"
    "s_add_u32 s48, s48, -811008\n\t"
    "ds_write_b128 v141, v[188:191] offset:0\n\t"
    "ds_write_b128 v141, v[192:195] offset:256\n\t"
    "ds_read_b64_tr_b16 v[188:189], v142 offset:0\n\t"
    "ds_read_b64_tr_b16 v[190:191], v142 offset:256\n\t"
    "ds_read_b64_tr_b16 v[192:193], v142 offset:32\n\t"
    "ds_read_b64_tr_b16 v[194:195], v142 offset:288\n\t"
    "ds_write_b128 v141, v[196:199] offset:2112\n\t"
    "ds_write_b128 v141, v[200:203] offset:2368\n\t"
    "ds_read_b64_tr_b16 v[196:197], v142 offset:2112\n\t"
    "ds_read_b64_tr_b16 v[198:199], v142 offset:2368\n\t"
    "ds_read_b64_tr_b16 v[200:201], v142 offset:2144\n\t"
    "ds_read_b64_tr_b16 v[202:203], v142 offset:2400\n\t"
    "v_bfi_b32 v130, v103, 0, v129\n\t"
    "v_cmp_eq_u32 vcc, 0, v130\n\t"
    "v_and_or_b32 v98, v103, s44, v69\n\t"
    "v_xor_b32 v133, 23, v132\n\t"
    "v_lshlrev_b32 v115, v132, v136\n\t"
    "v_and_b32 v132, 7, v132\n\t"
    "v_lshl_or_b32 v115, v136, v133, v115\n\t"
    "v_lshl_add_u32 v132, v132, 1, v128\n\t"
    "ds_or_b32 v97, v115\n\t"
    "ds_write_b16 v132, v104\n\t"
    "v_cndmask_b32 v130, v130, v129, vcc\n\t"
    "v_ffbl_b32 v131, v130\n\t"
    "v_and_b32 v134, 7, v131\n\t"
    "v_lshlrev_b32 v134, v134, s45\n\t"
    "v_xor_b32 v129, v129, v134\n\t"
    "v_xor_b32 v132, v131, v127\n\t"
    "v_bfi_b32 v130, v107, 0, v129\n\t"
    "v_cmp_eq_u32 vcc, 0, v130\n\t"
    "v_and_or_b32 v99, v107, s44, v69\n\t"
    "v_xor_b32 v133, 23, v132\n\t"
    "v_lshlrev_b32 v116, v132, v136\n\t"
    "v_and_b32 v132, 7, v132\n\t"
    "v_lshl_or_b32 v116, v136, v133, v116\n\t"
    "v_lshl_add_u32 v132, v132, 1, v128\n\t"
    "ds_or_b32 v98, v116\n\t"
    "ds_write_b16 v132, v103\n\t"
    "v_cndmask_b32 v130, v130, v129, vcc\n\t"
    "v_ffbl_b32 v131, v130\n\t"
    "v_xor_b32 v132, v131, v127\n\t"
    "v_xor_b32 v133, 23, v132\n\t"
    "v_lshlrev_b32 v117, v132, v136\n\t"
    "v_and_b32 v132, 7, v132\n\t"
    "v_lshl_or_b32 v117, v136, v133, v117\n\t"
    "v_lshl_add_u32 v132, v132, 1, v128\n\t"
    "ds_or_b32 v99, v117\n\t"
    "ds_write_b16 v132, v107\n\t"
    "ds_read_b128 v[72:75], v128\n\t"
    "v_and_or_b32 v100, v84, s44, v69\n\t"
    "ds_read_b32 v100, v100\n\t"
    "v_and_or_b32 v101, v85, s44, v69\n\t"
    "ds_read_b32 v101, v101\n\t"
    "v_and_or_b32 v102, v86, s44, v69\n\t"
    "ds_read_b32 v102, v102\n\t"
    "v_and_or_b32 v103, v87, s44, v69\n\t"
    "ds_read_b32 v103, v103\n\t"
    "v_and_or_b32 v104, v88, s44, v69\n\t"
    "ds_read_b32 v104, v104\n\t"
    "v_and_or_b32 v105, v89, s44, v69\n\t"
    "ds_read_b32 v105, v105\n\t"
    "v_and_or_b32 v106, v90, s44, v69\n\t"
    "ds_read_b32 v106, v106\n\t"
    "v_and_or_b32 v107, v91, s44, v69\n\t"
    "ds_read_b32 v107, v107\n\t"
    "v_lshl_add_u32 v76, v16, 3, s36\n\t"
    "v_lshl_add_u32 v77, v17, 3, s37\n\t"
    "v_lshl_add_u32 v78, v18, 3, s38\n\t"
    "v_lshl_add_u32 v79, v19, 3, s39\n\t"
    "v_lshl_add_u32 v80, v20, 3, s40\n\t"
    "v_lshl_add_u32 v81, v21, 3, s41\n\t"
    "v_lshl_add_u32 v82, v22, 3, s42\n\t"
    "v_lshl_add_u32 v83, v23, 3, s43\n\t"
    "s_waitcnt lgkmcnt(15)\n\t"
    "buffer_store_dwordx4 v[188:191], v139, %12, s48 offen sc1\n\t"
    "buffer_store_dwordx4 v[192:195], v140, %12, s48 offen sc1\n\t"
    "s_add_u32 s48, s48, 819200\n\t"
    "buffer_store_dwordx4 v[196:199], v139, %12, s48 offen sc1\n\t"
    "buffer_store_dwordx4 v[200:203], v140, %12, s48 offen sc1\n\t"
    "s_add_u32 s48, s48, -811008\n\t"
    "s_waitcnt lgkmcnt(8)\n\t"
    "buffer_store_dwordx4 v[72:75], v67, %12, s49 offen offset:416 sc1\n\t"
    "v_mov_b32 v129, 0xffff0000\n\t"
    "s_waitcnt lgkmcnt(4)\n\t"
    "v_perm_b32 v100, v100, v84, v126\n\t"
    "v_perm_b32 v101, v101, v85, v126\n\t"
    "v_perm_b32 v102, v102, v86, v126\n\t"
    "v_perm_b32 v103, v103, v87, v126\n\t"
    "s_waitcnt lgkmcnt(0)\n\t"
    "v_perm_b32 v104, v104, v88, v126\n\t"
    "v_perm_b32 v105, v105, v89, v126\n\t"
    "v_perm_b32 v106, v106, v90, v126\n\t"
    "v_perm_b32 v107, v107, v91, v126\n\t"
    "ds_write_b128 v141, v[56:59] offset:0\n\t"
    "ds_write_b128 v141, v[60:63] offset:256\n\t"
    "ds_read_b64_tr_b16 v[56:57], v142 offset:0\n\t"
    "ds_read_b64_tr_b16 v[58:59], v142 offset:256\n\t"
    "ds_read_b64_tr_b16 v[60:61], v142 offset:32\n\t"
    "ds_read_b64_tr_b16 v[62:63], v142 offset:288\n\t"
    "ds_write_b128 v141, v[204:207] offset:2112\n\t"
    "ds_write_b128 v141, v[208:211] offset:2368\n\t"
    "ds_read_b64_tr_b16 v[204:205], v142 offset:2112\n\t"
    "ds_read_b64_tr_b16 v[206:207], v142 offset:2368\n\t"
    "ds_read_b64_tr_b16 v[208:209], v142 offset:2144\n\t"
    "ds_read_b64_tr_b16 v[210:211], v142 offset:2400\n\t"
    "v_max_u32 v108, v100, v101\n\t"
    "v_min_u32 v101, v100, v101\n\t"
    "v_max_u32 v100, v102, v103\n\t"
    "v_min_u32 v103, v102, v103\n\t"
    "v_max_u32 v102, v104, v105\n\t"
    "v_min_u32 v105, v104, v105\n\t"
    "v_max_u32 v104, v106, v107\n\t"
    "v_min_u32 v107, v106, v107\n\t"
    "v_max_u32 v106, v108, v100\n\t"
    "v_min_u32 v100, v108, v100\n\t"
    "v_max_u32 v108, v101, v103\n\t"
    "v_min_u32 v103, v101, v103\n\t"
    "v_max_u32 v101, v102, v104\n\t"
    "v_min_u32 v104, v102, v104\n\t"
    "v_max_u32 v102, v105, v107\n\t"
    "v_min_u32 v107, v105, v107\n\t"
    "v_bfi_b32 v130, v106, 0, v129\n\t"
    "v_cmp_eq_u32 vcc, 0, v130\n\t"
    "v_and_or_b32 v92, v106, s44, v69\n\t"
    "s_nop 0\n\t"
    "v_cndmask_b32 v130, v130, v129, vcc\n\t"
    "v_ffbl_b32 v131, v130\n\t"
    "v_and_b32 v134, 7, v131\n\t"
    "v_lshlrev_b32 v134, v134, s45\n\t"
    "v_xor_b32 v129, v129, v134\n\t"
    "v_xor_b32 v132, v131, v127\n\t"
    "v_bfi_b32 v130, v101, 0, v129\n\t"
    "v_cmp_eq_u32 vcc, 0, v130\n\t"
    "v_and_or_b32 v93, v101, s44, v69\n\t"
    "v_xor_b32 v133, 23, v132\n\t"
    "v_lshlrev_b32 v110, v132, v136\n\t"
    "v_and_b32 v132, 7, v132\n\t"
    "v_lshl_or_b32 v110, v136, v133, v110\n\t"
    "v_lshl_add_u32 v132, v132, 1, v128\n\t"
    "ds_or_b32 v92, v110\n\t"
    "ds_write_b16 v132, v106\n\t"
    "v_cndmask_b32 v130, v130, v129, vcc\n\t"
    "v_ffbl_b32 v131, v130\n\t"
    "v_and_b32 v134, 7, v131\n\t"
    "v_lshlrev_b32 v134, v134, s45\n\t"
    "v_xor_b32 v129, v129, v134\n\t"
    "v_xor_b32 v132, v131, v127\n\t"
    "v_bfi_b32 v130, v108, 0, v129\n\t"
    "v_cmp_eq_u32 vcc, 0, v130\n\t"
    "v_and_or_b32 v94, v108, s44, v69\n\t"
    "v_xor_b32 v133, 23, v132\n\t"
    "v_lshlrev_b32 v111, v132, v136\n\t"
    "v_and_b32 v132, 7, v132\n\t"
    "v_lshl_or_b32 v111, v136, v133, v111\n\t"
    "v_lshl_add_u32 v132, v132, 1, v128\n\t"
    "ds_or_b32 v93, v111\n\t"
    "ds_write_b16 v132, v101\n\t"
    "v_cndmask_b32 v130, v130, v129, vcc\n\t"
    "v_ffbl_b32 v131, v130\n\t"
    "v_and_b32 v134, 7, v131\n\t"
    "v_lshlrev_b32 v134, v134, s45\n\t"
    "v_xor_b32 v129, v129, v134\n\t"
    "v_xor_b32 v132, v131, v127\n\t"
    "s_waitcnt lgkmcnt(10)\n\t"
    "buffer_store_dwordx4 v[56:59], v139, %12, s48 offen sc1\n\t"
    "buffer_store_dwordx4 v[60:63], v140, %12, s48 offen sc1\n\t"
    "s_add_u32 s48, s48, 819200\n\t"
    "s_waitcnt lgkmcnt(4)\n\t"
    "buffer_store_dwordx4 v[204:207], v139, %12, s48 offen sc1\n\t"
    "buffer_store_dwordx4 v[208:211], v140, %12, s48 offen sc1\n\t"
    "s_add_u32 s48, s48, -811008\n\t"
    "ds_write_b128 v141, v[212:215] offset:0\n\t"
    "ds_write_b128 v141, v[216:219] offset:256\n\t"
    "ds_read_b64_tr_b16 v[212:213], v142 offset:0\n\t"
    "ds_read_b64_tr_b16 v[214:215], v142 offset:256\n\t"
    "ds_read_b64_tr_b16 v[216:217], v142 offset:32\n\t"
    "ds_read_b64_tr_b16 v[218:219], v142 offset:288\n\t"
    "ds_write_b128 v141, v[220:223] offset:2112\n\t"
    "ds_write_b128 v141, v[224:227] offset:2368\n\t"
    "ds_read_b64_tr_b16 v[220:221], v142 offset:2112\n\t"
    "ds_read_b64_tr_b16 v[222:223], v142 offset:2368\n\t"
    "ds_read_b64_tr_b16 v[224:225], v142 offset:2144\n\t"
    "ds_read_b64_tr_b16 v[226:227], v142 offset:2400\n\t"
    "v_bfi_b32 v130, v100, 0, v129\n\t"
    "v_cmp_eq_u32 vcc, 0, v130\n\t"
    "v_and_or_b32 v95, v100, s44, v69\n\t"
    "v_xor_b32 v133, 23, v132\n\t"
    "v_lshlrev_b32 v112, v132, v136\n\t"
    "v_and_b32 v132, 7, v132\n\t"
    "v_lshl_or_b32 v112, v136, v133, v112\n\t"
    "v_lshl_add_u32 v132, v132, 1, v128\n\t"
    "ds_or_b32 v94, v112\n\t"
    "ds_write_b16 v132, v108\n\t"
    "v_cndmask_b32 v130, v130, v129, vcc\n\t"
    "v_ffbl_b32 v131, v130\n\t"
    "v_and_b32 v134, 7, v131\n\t"
    "v_lshlrev_b32 v134, v134, s45\n\t"
    "v_xor_b32 v129, v129, v134\n\t"
    "v_xor_b32 v132, v131, v127\n\t"
    "v_bfi_b32 v130, v102, 0, v129\n\t"
    "v_cmp_eq_u32 vcc, 0, v130\n\t"
    "v_and_or_b32 v96, v102, s44, v69\n\t"
    "v_xor_b32 v133, 23, v132\n\t"
    "v_lshlrev_b32 v113, v132, v136\n\t"
    "v_and_b32 v132, 7, v132\n\t"
    "v_lshl_or_b32 v113, v136, v133, v113\n\t"
    "v_lshl_add_u32 v132, v132, 1, v128\n\t"
    "ds_or_b32 v95, v113\n\t"
    "ds_write_b16 v132, v100\n\t"
    "v_cndmask_b32 v130, v130, v129, vcc\n\t"
    "v_ffbl_b32 v131, v130\n\t"
    "v_and_b32 v134, 7, v131\n\t"
    "v_lshlrev_b32 v134, v134, s45\n\t"
    "v_xor_b32 v129, v129, v134\n\t"
    "v_xor_b32 v132, v131, v127\n\t"
    "v_bfi_b32 v130, v104, 0, v129\n\t"
    "v_cmp_eq_u32 vcc, 0, v130\n\t"
    "v_and_or_b32 v97, v104, s44, v69\n\t"
    "v_xor_b32 v133, 23, v132\n\t"
    "v_lshlrev_b32 v114, v132, v136\n\t"
    "v_and_b32 v132, 7, v132\n\t"
    "v_lshl_or_b32 v114, v136, v133, v114\n\t"
    "v_lshl_add_u32 v132, v132, 1, v128\n\t"
    "ds_or_b32 v96, v114\n\t"
    "ds_write_b16 v132, v102\n\t"
    "v_cndmask_b32 v130, v130, v129, vcc\n\t"
    "v_ffbl_b32 v131, v130\n\t"
    "v_and_b32 v134, 7, v131\n\t"
    "v_lshlrev_b32 v134, v134, s45\n\t"
    "v_xor_b32 v129, v129, v134\n\t"
    "v_xor_b32 v132, v131, v127\n\t"
    "s_waitcnt lgkmcnt(12)\n\t"
    "s_bitcmp1_b32 %14, 3\n\t"
    "s_cselect_b32 s48, 0x40000000, s48\n\t"
    "buffer_store_dwordx4 v[212:215], v139, %12, s48 offen sc1\n\t"
    "buffer_store_dwordx4 v[216:219], v140, %12, s48 offen sc1\n\t"
    "s_add_u32 s48, s48, 819200\n\t"
    "s_waitcnt lgkmcnt(6)\n\t"
    "buffer_store_dwordx4 v[220:223], v139, %12, s48 offen sc1\n\t"
    "buffer_store_dwordx4 v[224:227], v140, %12, s48 offen sc1\n\t"
    "s_add_u32 s48, s48, -811008\n\t"
    "v_bfi_b32 v130, v103, 0, v129\n\t"
    "v_cmp_eq_u32 vcc, 0, v130\n\t"
    "v_and_or_b32 v98, v103, s44, v69\n\t"
    "v_xor_b32 v133, 23, v132\n\t"
    "v_lshlrev_b32 v115, v132, v136\n\t"
    "v_and_b32 v132, 7, v132\n\t"
    "v_lshl_or_b32 v115, v136, v133, v115\n\t"
    "v_lshl_add_u32 v132, v132, 1, v128\n\t"
    "ds_or_b32 v97, v115\n\t"
    "ds_write_b16 v132, v104\n\t"
    "v_cndmask_b32 v130, v130, v129, vcc\n\t"
    "v_ffbl_b32 v131, v130\n\t"
    "v_and_b32 v134, 7, v131\n\t"
    "v_lshlrev_b32 v134, v134, s45\n\t"
    "v_xor_b32 v129, v129, v134\n\t"
    "v_xor_b32 v132, v131, v127\n\t"
    "v_bfi_b32 v130, v107, 0, v129\n\t"
    "v_cmp_eq_u32 vcc, 0, v130\n\t"
    "v_and_or_b32 v99, v107, s44, v69\n\t"
    "v_xor_b32 v133, 23, v132\n\t"
    "v_lshlrev_b32 v116, v132, v136\n\t"
    "v_and_b32 v132, 7, v132\n\t"
    "v_lshl_or_b32 v116, v136, v133, v116\n\t"
    "v_lshl_add_u32 v132, v132, 1, v128\n\t"
    "ds_or_b32 v98, v116\n\t"
    "ds_write_b16 v132, v103\n\t"
    "v_cndmask_b32 v130, v130, v129, vcc\n\t"
    "v_ffbl_b32 v131, v130\n\t"
    "v_xor_b32 v132, v131, v127\n\t"
    "v_xor_b32 v133, 23, v132\n\t"
    "v_lshlrev_b32 v117, v132, v136\n\t"
    "v_and_b32 v132, 7, v132\n\t"
    "v_lshl_or_b32 v117, v136, v133, v117\n\t"
    "v_lshl_add_u32 v132, v132, 1, v128\n\t"
    "ds_or_b32 v99, v117\n\t"
    "ds_write_b16 v132, v107\n\t"
    "ds_read_b128 v[72:75], v128\n\t"
    "v_and_or_b32 v100, v76, s44, v69\n\t"
    "ds_read_b32 v100, v100\n\t"
    "v_and_or_b32 v101, v77, s44, v69\n\t"
    "ds_read_b32 v101, v101\n\t"
    "v_and_or_b32 v102, v78, s44, v69\n\t"
    "ds_read_b32 v102, v102\n\t"
    "v_and_or_b32 v103, v79, s44, v69\n\t"
    "ds_read_b32 v103, v103\n\t"
    "v_and_or_b32 v104, v80, s44, v69\n\t"
    "ds_read_b32 v104, v104\n\t"
    "v_and_or_b32 v105, v81, s44, v69\n\t"
    "ds_read_b32 v105, v105\n\t"
    "v_and_or_b32 v106, v82, s44, v69\n\t"
    "ds_read_b32 v106, v106\n\t"
    "v_and_or_b32 v107, v83, s44, v69\n\t"
    "ds_read_b32 v107, v107\n\t"
    "s_waitcnt lgkmcnt(8)\n\t"
    "buffer_store_dwordx4 v[72:75], v67, %12, s49 offen offset:448 sc1\n\t"
    "v_mov_b32 v129, 0xffff0000\n\t"
    "s_waitcnt lgkmcnt(4)\n\t"
    "v_perm_b32 v100, v100, v76, v126\n\t"
    "v_perm_b32 v101, v101, v77, v126\n\t"
    "v_perm_b32 v102, v102, v78, v126\n\t"
    "v_perm_b32 v103, v103, v79, v126\n\t"
    "s_waitcnt lgkmcnt(0)\n\t"
    "v_perm_b32 v104, v104, v80, v126\n\t"
    "v_perm_b32 v105, v105, v81, v126\n\t"
    "v_perm_b32 v106, v106, v82, v126\n\t"
    "v_perm_b32 v107, v107, v83, v126\n\t"
    "v_max_u32 v108, v100, v101\n\t"
    "v_min_u32 v101, v100, v101\n\t"
    "v_max_u32 v100, v102, v103\n\t"
    "v_min_u32 v103, v102, v103\n\t"
    "v_max_u32 v102, v104, v105\n\t"
    "v_min_u32 v105, v104, v105\n\t"
    "v_max_u32 v104, v106, v107\n\t"
    "v_min_u32 v107, v106, v107\n\t"
    "v_max_u32 v106, v108, v100\n\t"
    "v_min_u32 v100, v108, v100\n\t"
    "v_max_u32 v108, v101, v103\n\t"
    "v_min_u32 v103, v101, v103\n\t"
    "v_max_u32 v101, v102, v104\n\t"
    "v_min_u32 v104, v102, v104\n\t"
    "v_max_u32 v102, v105, v107\n\t"
    "v_min_u32 v107, v105, v107\n\t"
    "v_bfi_b32 v130, v106, 0, v129\n\t"
    "v_cmp_eq_u32 vcc, 0, v130\n\t"
    "v_and_or_b32 v92, v106, s44, v69\n\t"
    "s_nop 0\n\t"
    "v_cndmask_b32 v130, v130, v129, vcc\n\t"
    "v_ffbl_b32 v131, v130\n\t"
    "v_and_b32 v134, 7, v131\n\t"
    "v_lshlrev_b32 v134, v134, s45\n\t"
    "v_xor_b32 v129, v129, v134\n\t"
    "v_xor_b32 v132, v131, v127\n\t"
    "v_bfi_b32 v130, v101, 0, v129\n\t"
    "v_cmp_eq_u32 vcc, 0, v130\n\t"
    "v_and_b32 v132, 7, v132\n\t"
    "v_lshl_add_u32 v132, v132, 1, v128\n\t"
    "ds_write_b16 v132, v106\n\t"
    "v_cndmask_b32 v130, v130, v129, vcc\n\t"
    "v_ffbl_b32 v131, v130\n\t"
    "v_and_b32 v134, 7, v131\n\t"
    "v_lshlrev_b32 v134, v134, s45\n\t"
    "v_xor_b32 v129, v129, v134\n\t"
    "v_xor_b32 v132, v131, v127\n\t"
    "v_bfi_b32 v130, v108, 0, v129\n\t"
    "v_cmp_eq_u32 vcc, 0, v130\n\t"
    "v_and_b32 v132, 7, v132\n\t"
    "v_lshl_add_u32 v132, v132, 1, v128\n\t"
    "ds_write_b16 v132, v101\n\t"
    "v_cndmask_b32 v130, v130, v129, vcc\n\t"
    "v_ffbl_b32 v131, v130\n\t"
    "v_and_b32 v134, 7, v131\n\t"
    "v_lshlrev_b32 v134, v134, s45\n\t"
    "v_xor_b32 v129, v129, v134\n\t"
    "v_xor_b32 v132, v131, v127\n\t"
    "v_bfi_b32 v130, v100, 0, v129\n\t"
    "v_cmp_eq_u32 vcc, 0, v130\n\t"
    "v_and_b32 v132, 7, v132\n\t"
    "v_lshl_add_u32 v132, v132, 1, v128\n\t"
    "ds_write_b16 v132, v108\n\t"
    "v_cndmask_b32 v130, v130, v129, vcc\n\t"
    "v_ffbl_b32 v131, v130\n\t"
    "v_and_b32 v134, 7, v131\n\t"
    "v_lshlrev_b32 v134, v134, s45\n\t"
    "v_xor_b32 v129, v129, v134\n\t"
    "v_xor_b32 v132, v131, v127\n\t"
    "v_bfi_b32 v130, v102, 0, v129\n\t"
    "v_cmp_eq_u32 vcc, 0, v130\n\t"
    "v_and_b32 v132, 7, v132\n\t"
    "v_lshl_add_u32 v132, v132, 1, v128\n\t"
    "ds_write_b16 v132, v100\n\t"
    "v_cndmask_b32 v130, v130, v129, vcc\n\t"
    "v_ffbl_b32 v131, v130\n\t"
    "v_and_b32 v134, 7, v131\n\t"
    "v_lshlrev_b32 v134, v134, s45\n\t"
    "v_xor_b32 v129, v129, v134\n\t"
    "v_xor_b32 v132, v131, v127\n\t"
    "v_bfi_b32 v130, v104, 0, v129\n\t"
    "v_cmp_eq_u32 vcc, 0, v130\n\t"
    "v_and_b32 v132, 7, v132\n\t"
    "v_lshl_add_u32 v132, v132, 1, v128\n\t"
    "ds_write_b16 v132, v102\n\t"
    "v_cndmask_b32 v130, v130, v129, vcc\n\t"
    "v_ffbl_b32 v131, v130\n\t"
    "v_and_b32 v134, 7, v131\n\t"
    "v_lshlrev_b32 v134, v134, s45\n\t"
    "v_xor_b32 v129, v129, v134\n\t"
    "v_xor_b32 v132, v131, v127\n\t"
    "v_bfi_b32 v130, v103, 0, v129\n\t"
    "v_cmp_eq_u32 vcc, 0, v130\n\t"
    "v_and_b32 v132, 7, v132\n\t"
    "v_lshl_add_u32 v132, v132, 1, v128\n\t"
    "ds_write_b16 v132, v104\n\t"
    "v_cndmask_b32 v130, v130, v129, vcc\n\t"
    "v_ffbl_b32 v131, v130\n\t"
    "v_and_b32 v134, 7, v131\n\t"
    "v_lshlrev_b32 v134, v134, s45\n\t"
    "v_xor_b32 v129, v129, v134\n\t"
    "v_xor_b32 v132, v131, v127\n\t"
    "v_bfi_b32 v130, v107, 0, v129\n\t"
    "v_cmp_eq_u32 vcc, 0, v130\n\t"
    "v_and_b32 v132, 7, v132\n\t"
    "v_lshl_add_u32 v132, v132, 1, v128\n\t"
    "ds_write_b16 v132, v103\n\t"
    "v_cndmask_b32 v130, v130, v129, vcc\n\t"
    "v_ffbl_b32 v131, v130\n\t"
    "v_xor_b32 v132, v131, v127\n\t"
    "v_and_b32 v132, 7, v132\n\t"
    "v_lshl_add_u32 v132, v132, 1, v128\n\t"
    "ds_write_b16 v132, v107\n\t"
    "ds_read_b128 v[72:75], v128\n\t"
    "s_waitcnt lgkmcnt(0)\n\t"
    "buffer_store_dwordx4 v[72:75], v67, %12, s49 offen offset:480 sc1\n\t"
    "s_setprio 0\n\t"
    "v_mov_b32 v134, 1036\n\t"
    "L_asvsp12d_%=:\n\t"
    "ds_read_b32 v130, v134\n\t"
    "s_waitcnt lgkmcnt(0)\n\t"
    "v_readfirstlane_b32 s50, v130\n\t"
    "s_nop 0\n\t"
    "s_cmp_lt_u32 s50, 4\n\t"
    "s_cbranch_scc1 L_asvsp12d_%=\n\t"
    "v_mbcnt_lo_u32_b32 v100, -1, 0\n\t"
    "v_mbcnt_hi_u32_b32 v100, -1, v100\n\t"
    "s_lshl_b32 s61, %16, 15\n\t"
    "s_add_u32 s61, s61, 16515072\n\t"
    "v_lshlrev_b32 v127, 4, v100\n\t"
    "s_lshr_b32 s37, %14, 4\n\t"
    "s_and_b32 s39, %14, 7\n\t"
    "v_and_b32 v101, 15, v100\n\t"
    "s_lshl_b32 s39, s39, 4\n\t"
    "s_add_u32 s39, s39, s37\n\t"
    "s_mul_i32 s65, %16, 125\n\t"
    "v_add_u32 v155, s65, v101\n\t"
    "s_mul_i32 s63, s39, 102400\n\t"
    "s_add_u32 s63, s63, 3407872\n\t"
    "v_lshrrev_b32 v102, 2, v101\n\t"
    "s_and_b32 s37, s37, 7\n\t"
    "s_lshl_b32 s37, s37, 11\n\t"
    "v_lshrrev_b32 v103, 3, v100\n\t"
    "s_lshl_b32 s36, s39, 16\n\t"
    "s_bfe_u32 s65, %14, 0x10003\n\t"
    "v_and_b32 v103, 2, v103\n\t"
    "s_lshl_b32 s38, s65, 15\n\t"
    "s_add_u32 s36, s36, s38\n\t"
    "v_and_b32 v104, 1, v100\n\t"
    "s_lshl_b32 s38, %16, 12\n\t"
    "s_add_u32 s36, s36, s38\n\t"
    "v_lshlrev_b32 v104, 4, v104\n\t"
    "s_add_u32 s36, s36, 17825792\n\t"
    "v_mov_b32 v101, 0x3f80\n\t"
    "v_lshlrev_b32 v104, v104, v101\n\t"
    "v_cmp_eq_u32 vcc, v102, v103\n\t"
    "s_waitcnt lgkmcnt(0)\n\t"
    "s_lshr_b32 s39, s91, %9\n\t"
    "s_lshl_b32 s39, s39, 16\n\t"
    "v_cndmask_b32 v120, 0, v104, vcc\n\t"
    "v_cndmask_b32 v121, 0, v104, vcc\n\t"
    "v_add_u32 v103, 1, v103\n\t"
    "v_cmp_eq_u32 vcc, v102, v103\n\t"
    "s_lshl_b32 s40, s88, 16\n\t"
    "s_and_b32 s41, s88, 0xffff0000\n\t"
    "v_cndmask_b32 v122, 0, v104, vcc\n\t"
    "v_cndmask_b32 v123, 0, v104, vcc\n\t"
    "v_and_b32 v101, 2, v100\n\t"
    "v_mov_b32 v102, 0x44444444\n\t"
    "v_mov_b32 v103, 0xeeeeeeee\n\t"
    "v_cmp_eq_u32 vcc, 0, v101\n\t"
    "s_lshl_b32 s42, s89, 16\n\t"
    "s_and_b32 s43, s89, 0xffff0000\n\t"
    "v_cndmask_b32 v124, v103, v102, vcc\n\t"
    "v_lshrrev_b32 v101, 4, v100\n\t"
    "v_mul_u32_u24 v101, 8000, v101\n\t"
    "v_lshl_add_u32 v125, v155, 3, v101\n\t"
    "v_add_u32 v125, 64000, v125\n\t"
    "s_lshl_b32 s70, s72, 16\n\t"
    "s_and_b32 s71, s72, 0xffff0000\n\t"
    "s_lshl_b32 s72, s73, 16\n\t"
    "s_and_b32 s73, s73, 0xffff0000\n\t"
    "s_mov_b32 s67, 0xffff0000\n\t"
    "s_and_b32 s65, %14, 8\n\t"
    "s_add_u32 s38, s65, %16\n\t"
    "s_mul_i32 s38, s38, 212992\n\t"
    "s_add_u32 s38, s38, 196608\n\t"
    "s_add_u32 s84, s37, s38\n\t"
    "s_add_u32 s37, s37, 0x800\n\t"
    "s_and_b32 s37, s37, 0x3800\n\t"
    "s_add_u32 s85, s37, s38\n\t"
    "s_add_u32 s37, s37, 0x800\n\t"
    "s_and_b32 s37, s37, 0x3800\n\t"
    "s_add_u32 s86, s37, s38\n\t"
    "s_add_u32 s37, s37, 0x800\n\t"
    "s_and_b32 s37, s37, 0x3800\n\t"
    "s_add_u32 s87, s37, s38\n\t"
    "s_add_u32 s37, s37, 0x800\n\t"
    "s_and_b32 s37, s37, 0x3800\n\t"
    "s_add_u32 s88, s37, s38\n\t"
    "s_add_u32 s37, s37, 0x800\n\t"
    "s_and_b32 s37, s37, 0x3800\n\t"
    "s_add_u32 s89, s37, s38\n\t"
    "s_add_u32 s37, s37, 0x800\n\t"
    "s_and_b32 s37, s37, 0x3800\n\t"
    "s_add_u32 s90, s37, s38\n\t"
    "s_add_u32 s37, s37, 0x800\n\t"
    "s_and_b32 s37, s37, 0x3800\n\t"
    "s_add_u32 s91, s37, s38\n\t"
    "s_mul_i32 s94, s65, 425984\n\t"
    "s_sub_u32 s94, s94, 1703936\n\t"
    "s_sub_u32 s95, 0x8000, s94\n\t"
    "s_lshl_b32 s97, s65, 11\n\t"
    "s_sub_u32 s97, s97, 229376\n\t"
    "s_mov_b32 s92, 64000\n\t"
    "s_mov_b32 s93, -64000\n\t"
    "s_mov_b32 s96, 0x2000\n\t"
    "s_mov_b32 s62, 2\n\t"
    "v_rcp_f32 v101, s40\n\t"
    "v_rcp_f32 v102, s41\n\t"
    "v_rcp_f32 v103, s42\n\t"
    "v_rcp_f32 v104, s43\n\t"
    "v_mov_b32 v100, 0x7f7fffff\n\t"
    "v_med3_f32 v101, v101, -v100, v100\n\t"
    "v_med3_f32 v102, v102, -v100, v100\n\t"
    "v_med3_f32 v103, v103, -v100, v100\n\t"
    "v_med3_f32 v104, v104, -v100, v100\n\t"
    "v_readfirstlane_b32 s40, v101\n\t"
    "v_readfirstlane_b32 s41, v102\n\t"
    "v_readfirstlane_b32 s42, v103\n\t"
    "v_readfirstlane_b32 s43, v104\n\t"
    "v_lshrrev_b32 v105, 4, v127\n\t"
    "v_and_b32 v105, 48, v105\n\t"
    "s_movk_i32 s65, 130\n\t"
    "v_mad_u32_u24 v104, v155, s65, v105\n\t"
    "v_add_u32 v105, 14170, v104\n\t"
    "v_and_b32 v104, -16, v104\n\t"
    "v_and_b32 v105, -16, v105\n\t"
    "ds_read_b128 v[164:167], v104 offset:0\n\t"
    "ds_read_b128 v[168:171], v104 offset:64\n\t"
    "ds_read_b128 v[172:175], v104 offset:2080\n\t"
    "ds_read_b128 v[176:179], v104 offset:2144\n\t"
    "ds_read_b128 v[180:183], v104 offset:4160\n\t"
    "ds_read_b128 v[184:187], v104 offset:4224\n\t"
    "ds_read_b128 v[188:191], v104 offset:6240\n\t"
    "ds_read_b128 v[192:195], v104 offset:6304\n\t"
    "ds_read_b128 v[196:199], v104 offset:8320\n\t"
    "ds_read_b128 v[200:203], v104 offset:8384\n\t"
    "ds_read_b128 v[204:207], v104 offset:10400\n\t"
    "ds_read_b128 v[208:211], v104 offset:10464\n\t"
    "s_cmp_lt_u32 s62, 2\n\t"
    "s_cbranch_scc1 L_mel0_%=\n\t"
    "buffer_load_dwordx4 v[220:223], v127, %12, s61 offen offset:2048 sc1\n\t"
    "buffer_load_dwordx4 v[224:227], v127, %12, s61 offen offset:3072 sc1\n\t"
    "s_add_u32 s61, s61, 0x1000\n\t"
    "buffer_load_dwordx4 v[228:231], v127, %12, s61 offen offset:2048 sc1\n\t"
    "buffer_load_dwordx4 v[232:235], v127, %12, s61 offen offset:3072 sc1\n\t"
    "s_add_u32 s61, s61, 0x1000\n\t"
    "buffer_load_dwordx4 v[236:239], v127, %12, s61 offen offset:2048 sc1\n\t"
    "buffer_load_dwordx4 v[240:243], v127, %12, s61 offen offset:3072 sc1\n\t"
    "s_add_u32 s61, s61, 0x1000\n\t"
    "buffer_load_dwordx4 v[244:247], v127, %12, s61 offen offset:2048 sc1\n\t"
    "buffer_load_dwordx4 v[248:251], v127, %12, s61 offen offset:3072 sc1\n\t"
    "s_add_u32 s61, s61, 0x1000\n\t"
    "buffer_load_dwordx4 v[252:255], v127, %12, s61 offen offset:2048 sc1\n\t"
    "s_mov_b32 s62, 1\n\t"
    "L_mel0_%=:\n\t"
    "buffer_load_dwordx4 v[0:3], v127, %12, s63 offen offset:0 sc1\n\t"
    "buffer_load_dwordx4 v[4:7], v127, %12, s63 offen offset:1024 sc1\n\t"
    "buffer_load_dwordx4 v[8:11], v127, %12, s63 offen offset:2048 sc1\n\t"
    "buffer_load_dwordx4 v[12:15], v127, %12, s63 offen offset:3072 sc1\n\t"
    "s_add_u32 s63, s63, 0x1000\n\t"
    "s_mov_b32 s62, 0\n\t"
    "s_and_b32 s65, %14, 8\n\t"
    "s_lshl_b32 s65, s65, 10\n\t"
    "s_add_u32 s65, s65, s63\n\t"
    "s_mov_b32 m0, 136192\n\t"
    "s_add_u32 s65, s65, 0\n\t"
    "buffer_load_dwordx4 v127, %12, s65 offen offset:0 sc1 lds\n\t"
    "buffer_load_dwordx4 v127, %12, s65 offen offset:1024 sc1 lds\n\t"
    "buffer_load_dwordx4 v127, %12, s65 offen offset:2048 sc1 lds\n\t"
    "buffer_load_dwordx4 v127, %12, s65 offen offset:3072 sc1 lds\n\t"
    "s_mov_b32 m0, 140288\n\t"
    "s_add_u32 s65, s65, 4096\n\t"
    "buffer_load_dwordx4 v127, %12, s65 offen offset:0 sc1 lds\n\t"
    "buffer_load_dwordx4 v127, %12, s65 offen offset:1024 sc1 lds\n\t"
    "buffer_load_dwordx4 v127, %12, s65 offen offset:2048 sc1 lds\n\t"
    "buffer_load_dwordx4 v127, %12, s65 offen offset:3072 sc1 lds\n\t"
    "s_waitcnt vmcnt(21)\n\t"
    "s_and_b32 s50, %14, 7\n\t"
    "s_lshr_b32 s51, %14, 4\n\t"
    "s_add_u32 s51, s51, 8\n\t"
    "s_lshl_b32 s50, s50, 7\n\t"
    "s_lshl_b32 s51, s51, 7\n\t"
    "v_mov_b32 v132, s51\n\t"
    "v_writelane_b32 v132, s50, 0\n\t"
    "s_mov_b32 s50, 48237568\n\t"
    "s_mov_b64 exec, 3\n\t"
    "buffer_atomic_add v136, v132, %12, s50 offen sc1\n\t"
    "s_mov_b64 exec, -1\n\t"
    "s_mov_b32 s69, 0x40000000\n\t"
    "s_branch L_aend2_%=\n\t"
    "L_ahelp_%=:\n\t"
    "s_setprio 3\n\t"
    "v_cmp_gt_u32 vcc, 48, v147\n\t"
    "v_mov_b32 v133, 0x40000000\n\t"
    "v_lshrrev_b32 v134, 3, v147\n\t"
    "v_add_u32 v134, 6, v134\n\t"
    "v_bfe_u32 v135, v147, 2, 1\n\t"
    "v_and_b32 v130, 3, v147\n\t"
    "v_lshl_add_u32 v132, v135, 5, v130\n\t"
    "v_mul_u32_u24 v64, 400, v132\n\t"
    "v_lshl_add_u32 v64, v134, 5, v64\n\t"
    "s_movk_i32 s50, 0\n\t"
    "buffer_load_dwordx4 v[0:3], v64, %0, s50 offen offset:16\n\t"
    "buffer_load_dwordx4 v[4:7], v64, %0, s50 offen offset:32\n\t"
    "v_lshlrev_b32 v66, 14, v134\n\t"
    "v_lshl_add_u32 v66, v132, 4, v66\n\t"
    "v_lshlrev_b32 v68, 8, v134\n\t"
    "v_lshl_add_u32 v68, v135, 2, v68\n\t"
    "v_add_u32 v68, 130048, v68\n\t"
    "v_cndmask_b32 v68, v133, v68, vcc\n\t"
    "v_cndmask_b32 v66, v133, v66, vcc\n\t"
    "s_barrier\n\t"
    "s_load_dwordx2 s[72:73], %6, 0\n\t"
    "s_load_dwordx2 s[88:89], %7, 0\n\t"
    "s_load_dword s91, %8, 0\n\t"
    "v_lshlrev_b32 v128, 4, v147\n\t"
    "v_add_u32 v128, 133888, v128\n\t"
    "v_and_b32 v130, 1, v147\n\t"
    "v_cndmask_b32 v128, v133, v128, vcc\n\t"
    "v_mul_u32_u24 v127, 7, v130\n\t"
    "v_cmp_eq_u32 vcc, 1, v130\n\t"
    "v_mov_b32 v126, 0x07060100\n\t"
    "v_mov_b32 v133, 0x05040100\n\t"
    "v_mov_b32 v136, 1\n\t"
    "v_cndmask_b32 v126, v126, v133, vcc\n\t"
    "s_mov_b32 s36, 0\n\t"
    "s_mov_b32 s37, 8000\n\t"
    "s_mov_b32 s38, 16000\n\t"
    "s_mov_b32 s39, 24000\n\t"
    "s_mov_b32 s40, 32000\n\t"
    "s_mov_b32 s41, 40000\n\t"
    "s_mov_b32 s42, 48000\n\t"
    "s_mov_b32 s43, 56000\n\t"
    "s_movk_i32 s44, 0xf8\n\t"
    "s_mov_b32 s45, 0x01010000\n\t"
    "s_mov_b32 s46, 0x020c000c\n\t"
    "s_lshr_b32 s49, %14, 4\n\t"
    "s_mul_i32 s49, s49, 212992\n\t"
    "s_and_b32 s50, %14, 15\n\t"
    "s_lshl_b32 s50, s50, 10\n\t"
    "s_add_u32 s49, s49, s50\n\t"
    "s_lshl_b32 s52, %14, 2\n\t"
    "s_add_u32 s52, s52, 48242688\n\t"
    "buffer_load_dword v70, off, %12, s52 offset:1024 sc1\n\t"
    "s_cmp_eq_u32 %14, 0\n\t"
    "s_cselect_b32 s47, 48242688, 0x40000000\n\t"
    "s_cselect_b32 s48, 48234496, 0x40000000\n\t"
    "v_lshlrev_b32 v152, 4, v147\n\t"
    "v_lshlrev_b32 v132, 7, v147\n\t"
    "v_mov_b32 v71, 0\n\t"
    "buffer_store_dword v71, v132, %12, s48 offen sc1\n\t"
    "v_mov_b32 v148, 0\n\t"
    "v_mov_b32 v149, 0\n\t"
    "v_mov_b32 v150, 0\n\t"
    "v_mov_b32 v151, 0\n\t"
    "buffer_store_dwordx4 v[148:151], v152, %12, s47 offen offset:2048 sc1\n\t"
    "s_add_u32 s50, s48, 11264\n\t"
    "buffer_store_dword v71, v132, %12, s50 offen sc1\n\t"
    "s_add_u32 s50, s50, 0x2000\n\t"
    "buffer_store_dword v71, v132, %12, s50 offen sc1\n\t"
    "s_add_u32 s50, s50, 0x2000\n\t"
    "buffer_store_dword v71, v132, %12, s50 offen sc1\n\t"
    "s_add_u32 s50, s50, 0x2000\n\t"
    "buffer_store_dword v71, v132, %12, s50 offen sc1\n\t"
    "s_add_u32 s50, s50, 0x2000\n\t"
    "s_waitcnt vmcnt(9)\n\t"
    "v_lshlrev_b32 v134, 20, v147\n\t"
    "v_add_u32 v134, 2068, v134\n\t"
    "ds_add_u32 v134, v136\n\t"
    "v_mov_b32 v110, 65664\n\t"
    "v_mov_b32 v111, 131136\n\t"
    "v_mov_b32 v112, 262176\n\t"
    "v_mov_b32 v113, 524304\n\t"
    "v_mov_b32 v114, 1048584\n\t"
    "v_mov_b32 v115, 2097156\n\t"
    "v_mov_b32 v116, 4194306\n\t"
    "v_mov_b32 v117, 8388609\n\t"
    "s_waitcnt vmcnt(7)\n\t"
    "v_lshl_add_u32 v76, v0, 3, s36\n\t"
    "v_lshl_add_u32 v77, v1, 3, s37\n\t"
    "v_lshl_add_u32 v78, v2, 3, s38\n\t"
    "v_lshl_add_u32 v79, v3, 3, s39\n\t"
    "v_lshl_add_u32 v80, v4, 3, s40\n\t"
    "v_lshl_add_u32 v81, v5, 3, s41\n\t"
    "v_lshl_add_u32 v82, v6, 3, s42\n\t"
    "v_lshl_add_u32 v83, v7, 3, s43\n\t"
    "v_and_or_b32 v92, v76, s44, v68\n\t"
    "ds_or_rtn_b32 v118, v92, v110\n\t"
    "v_and_or_b32 v93, v77, s44, v68\n\t"
    "ds_or_rtn_b32 v119, v93, v111\n\t"
    "v_and_or_b32 v94, v78, s44, v68\n\t"
    "ds_or_rtn_b32 v120, v94, v112\n\t"
    "v_and_or_b32 v95, v79, s44, v68\n\t"
    "ds_or_rtn_b32 v121, v95, v113\n\t"
    "v_and_or_b32 v96, v80, s44, v68\n\t"
    "ds_or_rtn_b32 v122, v96, v114\n\t"
    "v_and_or_b32 v97, v81, s44, v68\n\t"
    "ds_or_rtn_b32 v123, v97, v115\n\t"
    "v_and_or_b32 v98, v82, s44, v68\n\t"
    "ds_or_rtn_b32 v124, v98, v116\n\t"
    "v_and_or_b32 v99, v83, s44, v68\n\t"
    "ds_or_rtn_b32 v125, v99, v117\n\t"
    "v_lshl_or_b32 v164, v77, 16, v76\n\t"
    "v_lshl_or_b32 v165, v79, 16, v78\n\t"
    "v_lshl_or_b32 v166, v81, 16, v80\n\t"
    "v_lshl_or_b32 v167, v83, 16, v82\n\t"
    "s_movk_i32 s50, 0\n\t"
    "buffer_load_dwordx4 v[8:11], v64, %0, s50 offen offset:1616\n\t"
    "buffer_load_dwordx4 v[12:15], v64, %0, s50 offen offset:1632\n\t"
    "s_movk_i32 s50, 3200\n\t"
    "buffer_load_dwordx4 v[16:19], v64, %0, s50 offen offset:16\n\t"
    "buffer_load_dwordx4 v[20:23], v64, %0, s50 offen offset:32\n\t"
    "s_waitcnt vmcnt(10)\n\t"
    "v_add_u32 v70, 1, v70\n\t"
    "buffer_store_dword v70, off, %12, s52 sc1\n\t"
    "s_waitcnt vmcnt(3)\n\t"
    "v_lshl_add_u32 v84, v8, 3, s36\n\t"
    "v_lshl_add_u32 v85, v9, 3, s37\n\t"
    "v_lshl_add_u32 v86, v10, 3, s38\n\t"
    "v_lshl_add_u32 v87, v11, 3, s39\n\t"
    "v_lshl_add_u32 v88, v12, 3, s40\n\t"
    "v_lshl_add_u32 v89, v13, 3, s41\n\t"
    "v_lshl_add_u32 v90, v14, 3, s42\n\t"
    "v_lshl_add_u32 v91, v15, 3, s43\n\t"
    "s_waitcnt lgkmcnt(4)\n\t"
    "v_and_b32 v118, v118, v110\n\t"
    "v_perm_b32 v118, v118, v118, s46\n\t"
    "ds_or_b32 v92, v118\n\t"
    "v_and_b32 v119, v119, v111\n\t"
    "v_perm_b32 v119, v119, v119, s46\n\t"
    "ds_or_b32 v93, v119\n\t"
    "v_and_b32 v120, v120, v112\n\t"
    "v_perm_b32 v120, v120, v120, s46\n\t"
    "ds_or_b32 v94, v120\n\t"
    "v_and_b32 v121, v121, v113\n\t"
    "v_perm_b32 v121, v121, v121, s46\n\t"
    "ds_or_b32 v95, v121\n\t"
    "s_waitcnt lgkmcnt(4)\n\t"
    "v_and_b32 v122, v122, v114\n\t"
    "v_perm_b32 v122, v122, v122, s46\n\t"
    "ds_or_b32 v96, v122\n\t"
    "v_and_b32 v123, v123, v115\n\t"
    "v_perm_b32 v123, v123, v123, s46\n\t"
    "ds_or_b32 v97, v123\n\t"
    "v_and_b32 v124, v124, v116\n\t"
    "v_perm_b32 v124, v124, v124, s46\n\t"
    "ds_or_b32 v98, v124\n\t"
    "v_and_b32 v125, v125, v117\n\t"
    "v_perm_b32 v125, v125, v125, s46\n\t"
    "ds_or_b32 v99, v125\n\t"
    "buffer_store_dwordx4 v[164:167], v66, %12, s49 offen offset:0 sc1\n\t"
    "v_and_or_b32 v100, v84, s44, v68\n\t"
    "ds_read_b32 v100, v100\n\t"
    "v_and_or_b32 v101, v85, s44, v68\n\t"
    "ds_read_b32 v101, v101\n\t"
    "v_and_or_b32 v102, v86, s44, v68\n\t"
    "ds_read_b32 v102, v102\n\t"
    "v_and_or_b32 v103, v87, s44, v68\n\t"
    "ds_read_b32 v103, v103\n\t"
    "v_and_or_b32 v104, v88, s44, v68\n\t"
    "ds_read_b32 v104, v104\n\t"
    "v_and_or_b32 v105, v89, s44, v68\n\t"
    "ds_read_b32 v105, v105\n\t"
    "v_and_or_b32 v106, v90, s44, v68\n\t"
    "ds_read_b32 v106, v106\n\t"
    "v_and_or_b32 v107, v91, s44, v68\n\t"
    "ds_read_b32 v107, v107\n\t"
    "s_waitcnt vmcnt(2)\n\t"
    "v_lshl_add_u32 v76, v16, 3, s36\n\t"
    "v_lshl_add_u32 v77, v17, 3, s37\n\t"
    "v_lshl_add_u32 v78, v18, 3, s38\n\t"
    "v_lshl_add_u32 v79, v19, 3, s39\n\t"
    "v_lshl_add_u32 v80, v20, 3, s40\n\t"
    "v_lshl_add_u32 v81, v21, 3, s41\n\t"
    "v_lshl_add_u32 v82, v22, 3, s42\n\t"
    "v_lshl_add_u32 v83, v23, 3, s43\n\t"
    "s_movk_i32 s50, 3200\n\t"
    "buffer_load_dwordx4 v[24:27], v64, %0, s50 offen offset:1616\n\t"
    "buffer_load_dwordx4 v[28:31], v64, %0, s50 offen offset:1632\n\t"
    "s_movk_i32 s50, 6400\n\t"
    "buffer_load_dwordx4 v[32:35], v64, %0, s50 offen offset:16\n\t"
    "buffer_load_dwordx4 v[36:39], v64, %0, s50 offen offset:32\n\t"
    "s_cmp_lg_u32 %14, 0\n\t"
    "s_cbranch_scc1 L_ahe189_%=\n\t"
    "buffer_load_dwordx4 v[148:151], v152, %12, s47 offen sc1\n\t"
    "L_ahe189_%=:\n\t"
    "v_mov_b32 v129, 0xffff0000\n\t"
    "s_waitcnt lgkmcnt(4)\n\t"
    "v_perm_b32 v100, v100, v84, v126\n\t"
    "v_perm_b32 v101, v101, v85, v126\n\t"
    "v_perm_b32 v102, v102, v86, v126\n\t"
    "v_perm_b32 v103, v103, v87, v126\n\t"
    "s_waitcnt lgkmcnt(0)\n\t"
    "v_perm_b32 v104, v104, v88, v126\n\t"
    "v_perm_b32 v105, v105, v89, v126\n\t"
    "v_perm_b32 v106, v106, v90, v126\n\t"
    "v_perm_b32 v107, v107, v91, v126\n\t"
    "v_max_u32 v108, v100, v101\n\t"
    "v_min_u32 v101, v100, v101\n\t"
    "v_max_u32 v100, v102, v103\n\t"
    "v_min_u32 v103, v102, v103\n\t"
    "v_max_u32 v102, v104, v105\n\t"
    "v_min_u32 v105, v104, v105\n\t"
    "v_max_u32 v104, v106, v107\n\t"
    "v_min_u32 v107, v106, v107\n\t"
    "v_max_u32 v106, v108, v100\n\t"
    "v_min_u32 v100, v108, v100\n\t"
    "v_max_u32 v108, v101, v103\n\t"
    "v_min_u32 v103, v101, v103\n\t"
    "v_max_u32 v101, v102, v104\n\t"
    "v_min_u32 v104, v102, v104\n\t"
    "v_max_u32 v102, v105, v107\n\t"
    "v_min_u32 v107, v105, v107\n\t"
    "v_bfi_b32 v130, v106, 0, v129\n\t"
    "v_cmp_eq_u32 vcc, 0, v130\n\t"
    "v_and_or_b32 v92, v106, s44, v68\n\t"
    "s_nop 0\n\t"
    "v_cndmask_b32 v130, v130, v129, vcc\n\t"
    "v_ffbl_b32 v131, v130\n\t"
    "v_and_b32 v134, 7, v131\n\t"
    "v_lshlrev_b32 v134, v134, s45\n\t"
    "v_xor_b32 v129, v129, v134\n\t"
    "v_xor_b32 v132, v131, v127\n\t"
    "v_bfi_b32 v130, v101, 0, v129\n\t"
    "v_cmp_eq_u32 vcc, 0, v130\n\t"
    "v_and_or_b32 v93, v101, s44, v68\n\t"
    "v_xor_b32 v133, 23, v132\n\t"
    "v_lshlrev_b32 v110, v132, v136\n\t"
    "v_and_b32 v132, 7, v132\n\t"
    "v_lshl_or_b32 v110, v136, v133, v110\n\t"
    "v_lshl_add_u32 v132, v132, 1, v128\n\t"
    "ds_or_rtn_b32 v118, v92, v110\n\t"
    "ds_write_b16 v132, v106\n\t"
    "v_cndmask_b32 v130, v130, v129, vcc\n\t"
    "v_ffbl_b32 v131, v130\n\t"
    "v_and_b32 v134, 7, v131\n\t"
    "v_lshlrev_b32 v134, v134, s45\n\t"
    "v_xor_b32 v129, v129, v134\n\t"
    "v_xor_b32 v132, v131, v127\n\t"
    "v_bfi_b32 v130, v108, 0, v129\n\t"
    "v_cmp_eq_u32 vcc, 0, v130\n\t"
    "v_and_or_b32 v94, v108, s44, v68\n\t"
    "v_xor_b32 v133, 23, v132\n\t"
    "v_lshlrev_b32 v111, v132, v136\n\t"
    "v_and_b32 v132, 7, v132\n\t"
    "v_lshl_or_b32 v111, v136, v133, v111\n\t"
    "v_lshl_add_u32 v132, v132, 1, v128\n\t"
    "ds_or_rtn_b32 v119, v93, v111\n\t"
    "ds_write_b16 v132, v101\n\t"
    "v_cndmask_b32 v130, v130, v129, vcc\n\t"
    "v_ffbl_b32 v131, v130\n\t"
    "v_and_b32 v134, 7, v131\n\t"
    "v_lshlrev_b32 v134, v134, s45\n\t"
    "v_xor_b32 v129, v129, v134\n\t"
    "v_xor_b32 v132, v131, v127\n\t"
    "v_bfi_b32 v130, v100, 0, v129\n\t"
    "v_cmp_eq_u32 vcc, 0, v130\n\t"
    "v_and_or_b32 v95, v100, s44, v68\n\t"
    "v_xor_b32 v133, 23, v132\n\t"
    "v_lshlrev_b32 v112, v132, v136\n\t"
    "v_and_b32 v132, 7, v132\n\t"
    "v_lshl_or_b32 v112, v136, v133, v112\n\t"
    "v_lshl_add_u32 v132, v132, 1, v128\n\t"
    "ds_or_rtn_b32 v120, v94, v112\n\t"
    "ds_write_b16 v132, v108\n\t"
    "v_cndmask_b32 v130, v130, v129, vcc\n\t"
    "v_ffbl_b32 v131, v130\n\t"
    "v_and_b32 v134, 7, v131\n\t"
    "v_lshlrev_b32 v134, v134, s45\n\t"
    "v_xor_b32 v129, v129, v134\n\t"
    "v_xor_b32 v132, v131, v127\n\t"
    "v_bfi_b32 v130, v102, 0, v129\n\t"
    "v_cmp_eq_u32 vcc, 0, v130\n\t"
    "v_and_or_b32 v96, v102, s44, v68\n\t"
    "v_xor_b32 v133, 23, v132\n\t"
    "v_lshlrev_b32 v113, v132, v136\n\t"
    "v_and_b32 v132, 7, v132\n\t"
    "v_lshl_or_b32 v113, v136, v133, v113\n\t"
    "v_lshl_add_u32 v132, v132, 1, v128\n\t"
    "ds_or_rtn_b32 v121, v95, v113\n\t"
    "ds_write_b16 v132, v100\n\t"
    "v_cndmask_b32 v130, v130, v129, vcc\n\t"
    "v_ffbl_b32 v131, v130\n\t"
    "v_and_b32 v134, 7, v131\n\t"
    "v_lshlrev_b32 v134, v134, s45\n\t"
    "v_xor_b32 v129, v129, v134\n\t"
    "v_xor_b32 v132, v131, v127\n\t"
    "s_waitcnt lgkmcnt(7)\n\t"
    "v_and_b32 v118, v118, v110\n\t"
    "v_perm_b32 v118, v118, v118, s46\n\t"
    "ds_or_b32 v92, v118\n\t"
    "v_bfi_b32 v130, v104, 0, v129\n\t"
    "v_cmp_eq_u32 vcc, 0, v130\n\t"
    "v_and_or_b32 v97, v104, s44, v68\n\t"
    "v_xor_b32 v133, 23, v132\n\t"
    "v_lshlrev_b32 v114, v132, v136\n\t"
    "v_and_b32 v132, 7, v132\n\t"
    "v_lshl_or_b32 v114, v136, v133, v114\n\t"
    "v_lshl_add_u32 v132, v132, 1, v128\n\t"
    "ds_or_rtn_b32 v122, v96, v114\n\t"
    "ds_write_b16 v132, v102\n\t"
    "v_cndmask_b32 v130, v130, v129, vcc\n\t"
    "v_ffbl_b32 v131, v130\n\t"
    "v_and_b32 v134, 7, v131\n\t"
    "v_lshlrev_b32 v134, v134, s45\n\t"
    "v_xor_b32 v129, v129, v134\n\t"
    "v_xor_b32 v132, v131, v127\n\t"
    "s_waitcnt lgkmcnt(8)\n\t"
    "v_and_b32 v119, v119, v111\n\t"
    "v_perm_b32 v119, v119, v119, s46\n\t"
    "ds_or_b32 v93, v119\n\t"
    "v_bfi_b32 v130, v103, 0, v129\n\t"
    "v_cmp_eq_u32 vcc, 0, v130\n\t"
    "v_and_or_b32 v98, v103, s44, v68\n\t"
    "v_xor_b32 v133, 23, v132\n\t"
    "v_lshlrev_b32 v115, v132, v136\n\t"
    "v_and_b32 v132, 7, v132\n\t"
    "v_lshl_or_b32 v115, v136, v133, v115\n\t"
    "v_lshl_add_u32 v132, v132, 1, v128\n\t"
    "ds_or_rtn_b32 v123, v97, v115\n\t"
    "ds_write_b16 v132, v104\n\t"
    "v_cndmask_b32 v130, v130, v129, vcc\n\t"
    "v_ffbl_b32 v131, v130\n\t"
    "v_and_b32 v134, 7, v131\n\t"
    "v_lshlrev_b32 v134, v134, s45\n\t"
    "v_xor_b32 v129, v129, v134\n\t"
    "v_xor_b32 v132, v131, v127\n\t"
    "s_waitcnt lgkmcnt(9)\n\t"
    "v_and_b32 v120, v120, v112\n\t"
    "v_perm_b32 v120, v120, v120, s46\n\t"
    "ds_or_b32 v94, v120\n\t"
    "v_bfi_b32 v130, v107, 0, v129\n\t"
    "v_cmp_eq_u32 vcc, 0, v130\n\t"
    "v_and_or_b32 v99, v107, s44, v68\n\t"
    "v_xor_b32 v133, 23, v132\n\t"
    "v_lshlrev_b32 v116, v132, v136\n\t"
    "v_and_b32 v132, 7, v132\n\t"
    "v_lshl_or_b32 v116, v136, v133, v116\n\t"
    "v_lshl_add_u32 v132, v132, 1, v128\n\t"
    "ds_or_rtn_b32 v124, v98, v116\n\t"
    "ds_write_b16 v132, v103\n\t"
    "v_cndmask_b32 v130, v130, v129, vcc\n\t"
    "v_ffbl_b32 v131, v130\n\t"
    "v_xor_b32 v132, v131, v127\n\t"
    "s_waitcnt lgkmcnt(10)\n\t"
    "v_and_b32 v121, v121, v113\n\t"
    "v_perm_b32 v121, v121, v121, s46\n\t"
    "ds_or_b32 v95, v121\n\t"
    "v_xor_b32 v133, 23, v132\n\t"
    "v_lshlrev_b32 v117, v132, v136\n\t"
    "v_and_b32 v132, 7, v132\n\t"
    "v_lshl_or_b32 v117, v136, v133, v117\n\t"
    "v_lshl_add_u32 v132, v132, 1, v128\n\t"
    "ds_or_rtn_b32 v125, v99, v117\n\t"
    "ds_write_b16 v132, v107\n\t"
    "s_waitcnt lgkmcnt(10)\n\t"
    "v_and_b32 v122, v122, v114\n\t"
    "v_perm_b32 v122, v122, v122, s46\n\t"
    "ds_or_b32 v96, v122\n\t"
    "ds_read_b128 v[72:75], v128\n\t"
    "v_and_or_b32 v100, v76, s44, v68\n\t"
    "ds_read_b32 v100, v100\n\t"
    "v_and_or_b32 v101, v77, s44, v68\n\t"
    "ds_read_b32 v101, v101\n\t"
    "v_and_or_b32 v102, v78, s44, v68\n\t"
    "ds_read_b32 v102, v102\n\t"
    "v_and_or_b32 v103, v79, s44, v68\n\t"
    "ds_read_b32 v103, v103\n\t"
    "v_and_or_b32 v104, v80, s44, v68\n\t"
    "ds_read_b32 v104, v104\n\t"
    "v_and_or_b32 v105, v81, s44, v68\n\t"
    "ds_read_b32 v105, v105\n\t"
    "v_and_or_b32 v106, v82, s44, v68\n\t"
    "ds_read_b32 v106, v106\n\t"
    "v_and_or_b32 v107, v83, s44, v68\n\t"
    "ds_read_b32 v107, v107\n\t"
    "s_cmp_lg_u32 %14, 0\n\t"
    "s_cbranch_scc1 L_ahe383_%=\n\t"
    "s_waitcnt vmcnt(0)\n\t"
    "buffer_store_dwordx4 v[148:151], v152, %12, s47 offen offset:1024 sc1\n\t"
    "L_ahe383_%=:\n\t"
    "s_waitcnt vmcnt(2)\n\t"
    "v_lshl_add_u32 v84, v24, 3, s36\n\t"
    "v_lshl_add_u32 v85, v25, 3, s37\n\t"
    "v_lshl_add_u32 v86, v26, 3, s38\n\t"
    "v_lshl_add_u32 v87, v27, 3, s39\n\t"
    "v_lshl_add_u32 v88, v28, 3, s40\n\t"
    "v_lshl_add_u32 v89, v29, 3, s41\n\t"
    "v_lshl_add_u32 v90, v30, 3, s42\n\t"
    "v_lshl_add_u32 v91, v31, 3, s43\n\t"
    "v_mul_u32_u24 v58, 400, v147\n\t"
    "buffer_load_dwordx4 v[48:51], v58, %0, 0 offen\n\t"
    "s_lshr_b32 s50, %14, 5\n\t"
    "s_mul_i32 s50, s50, 125\n\t"
    "s_bfe_u32 s51, %14, 0x30002\n\t"
    "s_lshl_b32 s51, s51, 4\n\t"
    "s_min_u32 s51, s51, 109\n\t"
    "s_add_u32 s50, s50, s51\n\t"
    "s_lshl_b32 s50, s50, 8\n\t"
    "s_and_b32 s51, %14, 3\n\t"
    "s_lshl_b32 s51, s51, 6\n\t"
    "s_add_u32 s50, s50, s51\n\t"
    "s_bitcmp1_b32 %14, 1\n\t"
    "s_cselect_b32 s50, s50, 0x40000000\n\t"
    "v_and_b32 v59, 48, v147\n\t"
    "v_and_b32 v60, 15, v147\n\t"
    "v_lshl_add_u32 v59, v60, 8, v59\n\t"
    "buffer_load_dwordx4 v[52:55], v59, %15, s50 offen\n\t"
    "s_cmp_lg_u32 %14, 0\n\t"
    "s_cbranch_scc1 L_ahe415_%=\n\t"
    "buffer_load_dwordx4 v[148:151], v152, %12, s47 offen sc1\n\t"
    "L_ahe415_%=:\n\t"
    "s_waitcnt lgkmcnt(11)\n\t"
    "v_and_b32 v123, v123, v115\n\t"
    "v_perm_b32 v123, v123, v123, s46\n\t"
    "ds_or_b32 v97, v123\n\t"
    "v_and_b32 v124, v124, v116\n\t"
    "v_perm_b32 v124, v124, v124, s46\n\t"
    "ds_or_b32 v98, v124\n\t"
    "v_and_b32 v125, v125, v117\n\t"
    "v_perm_b32 v125, v125, v125, s46\n\t"
    "ds_or_b32 v99, v125\n\t"
    "s_waitcnt lgkmcnt(11)\n\t"
    "buffer_store_dwordx4 v[72:75], v66, %12, s49 offen offset:64 sc1\n\t"
    "v_mov_b32 v129, 0xffff0000\n\t"
    "s_waitcnt lgkmcnt(7)\n\t"
    "v_perm_b32 v100, v100, v76, v126\n\t"
    "v_perm_b32 v101, v101, v77, v126\n\t"
    "v_perm_b32 v102, v102, v78, v126\n\t"
    "v_perm_b32 v103, v103, v79, v126\n\t"
    "s_waitcnt lgkmcnt(3)\n\t"
    "v_perm_b32 v104, v104, v80, v126\n\t"
    "v_perm_b32 v105, v105, v81, v126\n\t"
    "v_perm_b32 v106, v106, v82, v126\n\t"
    "v_perm_b32 v107, v107, v83, v126\n\t"
    "v_max_u32 v108, v100, v101\n\t"
    "v_min_u32 v101, v100, v101\n\t"
    "v_max_u32 v100, v102, v103\n\t"
    "v_min_u32 v103, v102, v103\n\t"
    "v_max_u32 v102, v104, v105\n\t"
    "v_min_u32 v105, v104, v105\n\t"
    "v_max_u32 v104, v106, v107\n\t"
    "v_min_u32 v107, v106, v107\n\t"
    "v_max_u32 v106, v108, v100\n\t"
    "v_min_u32 v100, v108, v100\n\t"
    "v_max_u32 v108, v101, v103\n\t"
    "v_min_u32 v103, v101, v103\n\t"
    "v_max_u32 v101, v102, v104\n\t"
    "v_min_u32 v104, v102, v104\n\t"
    "v_max_u32 v102, v105, v107\n\t"
    "v_min_u32 v107, v105, v107\n\t"
    "v_bfi_b32 v130, v106, 0, v129\n\t"
    "v_cmp_eq_u32 vcc, 0, v130\n\t"
    "v_and_or_b32 v92, v106, s44, v68\n\t"
    "s_nop 0\n\t"
    "v_cndmask_b32 v130, v130, v129, vcc\n\t"
    "v_ffbl_b32 v131, v130\n\t"
    "v_and_b32 v134, 7, v131\n\t"
    "v_lshlrev_b32 v134, v134, s45\n\t"
    "v_xor_b32 v129, v129, v134\n\t"
    "v_xor_b32 v132, v131, v127\n\t"
    "v_bfi_b32 v130, v101, 0, v129\n\t"
    "v_cmp_eq_u32 vcc, 0, v130\n\t"
    "v_and_or_b32 v93, v101, s44, v68\n\t"
    "v_xor_b32 v133, 23, v132\n\t"
    "v_lshlrev_b32 v110, v132, v136\n\t"
    "v_and_b32 v132, 7, v132\n\t"
    "v_lshl_or_b32 v110, v136, v133, v110\n\t"
    "v_lshl_add_u32 v132, v132, 1, v128\n\t"
    "ds_or_rtn_b32 v118, v92, v110\n\t"
    "ds_write_b16 v132, v106\n\t"
    "v_cndmask_b32 v130, v130, v129, vcc\n\t"
    "v_ffbl_b32 v131, v130\n\t"
    "v_and_b32 v134, 7, v131\n\t"
    "v_lshlrev_b32 v134, v134, s45\n\t"
    "v_xor_b32 v129, v129, v134\n\t"
    "v_xor_b32 v132, v131, v127\n\t"
    "v_bfi_b32 v130, v108, 0, v129\n\t"
    "v_cmp_eq_u32 vcc, 0, v130\n\t"
    "v_and_or_b32 v94, v108, s44, v68\n\t"
    "v_xor_b32 v133, 23, v132\n\t"
    "v_lshlrev_b32 v111, v132, v136\n\t"
    "v_and_b32 v132, 7, v132\n\t"
    "v_lshl_or_b32 v111, v136, v133, v111\n\t"
    "v_lshl_add_u32 v132, v132, 1, v128\n\t"
    "ds_or_rtn_b32 v119, v93, v111\n\t"
    "ds_write_b16 v132, v101\n\t"
    "v_cndmask_b32 v130, v130, v129, vcc\n\t"
    "v_ffbl_b32 v131, v130\n\t"
    "v_and_b32 v134, 7, v131\n\t"
    "v_lshlrev_b32 v134, v134, s45\n\t"
    "v_xor_b32 v129, v129, v134\n\t"
    "v_xor_b32 v132, v131, v127\n\t"
    "v_bfi_b32 v130, v100, 0, v129\n\t"
    "v_cmp_eq_u32 vcc, 0, v130\n\t"
    "v_and_or_b32 v95, v100, s44, v68\n\t"
    "v_xor_b32 v133, 23, v132\n\t"
    "v_lshlrev_b32 v112, v132, v136\n\t"
    "v_and_b32 v132, 7, v132\n\t"
    "v_lshl_or_b32 v112, v136, v133, v112\n\t"
    "v_lshl_add_u32 v132, v132, 1, v128\n\t"
    "ds_or_rtn_b32 v120, v94, v112\n\t"
    "ds_write_b16 v132, v108\n\t"
    "v_cndmask_b32 v130, v130, v129, vcc\n\t"
    "v_ffbl_b32 v131, v130\n\t"
    "v_and_b32 v134, 7, v131\n\t"
    "v_lshlrev_b32 v134, v134, s45\n\t"
    "v_xor_b32 v129, v129, v134\n\t"
    "v_xor_b32 v132, v131, v127\n\t"
    "v_bfi_b32 v130, v102, 0, v129\n\t"
    "v_cmp_eq_u32 vcc, 0, v130\n\t"
    "v_and_or_b32 v96, v102, s44, v68\n\t"
    "v_xor_b32 v133, 23, v132\n\t"
    "v_lshlrev_b32 v113, v132, v136\n\t"
    "v_and_b32 v132, 7, v132\n\t"
    "v_lshl_or_b32 v113, v136, v133, v113\n\t"
    "v_lshl_add_u32 v132, v132, 1, v128\n\t"
    "ds_or_rtn_b32 v121, v95, v113\n\t"
    "ds_write_b16 v132, v100\n\t"
    "v_cndmask_b32 v130, v130, v129, vcc\n\t"
    "v_ffbl_b32 v131, v130\n\t"
    "v_and_b32 v134, 7, v131\n\t"
    "v_lshlrev_b32 v134, v134, s45\n\t"
    "v_xor_b32 v129, v129, v134\n\t"
    "v_xor_b32 v132, v131, v127\n\t"
    "s_waitcnt lgkmcnt(7)\n\t"
    "v_and_b32 v118, v118, v110\n\t"
    "v_perm_b32 v118, v118, v118, s46\n\t"
    "ds_or_b32 v92, v118\n\t"
    "v_bfi_b32 v130, v104, 0, v129\n\t"
    "v_cmp_eq_u32 vcc, 0, v130\n\t"
    "v_and_or_b32 v97, v104, s44, v68\n\t"
    "v_xor_b32 v133, 23, v132\n\t"
    "v_lshlrev_b32 v114, v132, v136\n\t"
    "v_and_b32 v132, 7, v132\n\t"
    "v_lshl_or_b32 v114, v136, v133, v114\n\t"
    "v_lshl_add_u32 v132, v132, 1, v128\n\t"
    "ds_or_rtn_b32 v122, v96, v114\n\t"
    "ds_write_b16 v132, v102\n\t"
    "v_cndmask_b32 v130, v130, v129, vcc\n\t"
    "v_ffbl_b32 v131, v130\n\t"
    "v_and_b32 v134, 7, v131\n\t"
    "v_lshlrev_b32 v134, v134, s45\n\t"
    "v_xor_b32 v129, v129, v134\n\t"
    "v_xor_b32 v132, v131, v127\n\t"
    "s_waitcnt lgkmcnt(8)\n\t"
    "v_and_b32 v119, v119, v111\n\t"
    "v_perm_b32 v119, v119, v119, s46\n\t"
    "ds_or_b32 v93, v119\n\t"
    "v_bfi_b32 v130, v103, 0, v129\n\t"
    "v_cmp_eq_u32 vcc, 0, v130\n\t"
    "v_and_or_b32 v98, v103, s44, v68\n\t"
    "v_xor_b32 v133, 23, v132\n\t"
    "v_lshlrev_b32 v115, v132, v136\n\t"
    "v_and_b32 v132, 7, v132\n\t"
    "v_lshl_or_b32 v115, v136, v133, v115\n\t"
    "v_lshl_add_u32 v132, v132, 1, v128\n\t"
    "ds_or_rtn_b32 v123, v97, v115\n\t"
    "ds_write_b16 v132, v104\n\t"
    "v_cndmask_b32 v130, v130, v129, vcc\n\t"
    "v_ffbl_b32 v131, v130\n\t"
    "v_and_b32 v134, 7, v131\n\t"
    "v_lshlrev_b32 v134, v134, s45\n\t"
    "v_xor_b32 v129, v129, v134\n\t"
    "v_xor_b32 v132, v131, v127\n\t"
    "s_waitcnt lgkmcnt(9)\n\t"
    "v_and_b32 v120, v120, v112\n\t"
    "v_perm_b32 v120, v120, v120, s46\n\t"
    "ds_or_b32 v94, v120\n\t"
    "v_bfi_b32 v130, v107, 0, v129\n\t"
    "v_cmp_eq_u32 vcc, 0, v130\n\t"
    "v_and_or_b32 v99, v107, s44, v68\n\t"
    "v_xor_b32 v133, 23, v132\n\t"
    "v_lshlrev_b32 v116, v132, v136\n\t"
    "v_and_b32 v132, 7, v132\n\t"
    "v_lshl_or_b32 v116, v136, v133, v116\n\t"
    "v_lshl_add_u32 v132, v132, 1, v128\n\t"
    "ds_or_rtn_b32 v124, v98, v116\n\t"
    "ds_write_b16 v132, v103\n\t"
    "v_cndmask_b32 v130, v130, v129, vcc\n\t"
    "v_ffbl_b32 v131, v130\n\t"
    "v_xor_b32 v132, v131, v127\n\t"
    "s_waitcnt lgkmcnt(10)\n\t"
    "v_and_b32 v121, v121, v113\n\t"
    "v_perm_b32 v121, v121, v121, s46\n\t"
    "ds_or_b32 v95, v121\n\t"
    "v_xor_b32 v133, 23, v132\n\t"
    "v_lshlrev_b32 v117, v132, v136\n\t"
    "v_and_b32 v132, 7, v132\n\t"
    "v_lshl_or_b32 v117, v136, v133, v117\n\t"
    "v_lshl_add_u32 v132, v132, 1, v128\n\t"
    "ds_or_rtn_b32 v125, v99, v117\n\t"
    "ds_write_b16 v132, v107\n\t"
    "s_waitcnt lgkmcnt(10)\n\t"
    "v_and_b32 v122, v122, v114\n\t"
    "v_perm_b32 v122, v122, v122, s46\n\t"
    "ds_or_b32 v96, v122\n\t"
    "ds_read_b128 v[72:75], v128\n\t"
    "v_and_or_b32 v100, v84, s44, v68\n\t"
    "ds_read_b32 v100, v100\n\t"
    "v_and_or_b32 v101, v85, s44, v68\n\t"
    "ds_read_b32 v101, v101\n\t"
    "v_and_or_b32 v102, v86, s44, v68\n\t"
    "ds_read_b32 v102, v102\n\t"
    "v_and_or_b32 v103, v87, s44, v68\n\t"
    "ds_read_b32 v103, v103\n\t"
    "v_and_or_b32 v104, v88, s44, v68\n\t"
    "ds_read_b32 v104, v104\n\t"
    "v_and_or_b32 v105, v89, s44, v68\n\t"
    "ds_read_b32 v105, v105\n\t"
    "v_and_or_b32 v106, v90, s44, v68\n\t"
    "ds_read_b32 v106, v106\n\t"
    "v_and_or_b32 v107, v91, s44, v68\n\t"
    "ds_read_b32 v107, v107\n\t"
    "s_cmp_lg_u32 %14, 0\n\t"
    "s_cbranch_scc1 L_ahe621_%=\n\t"
    "s_waitcnt vmcnt(0)\n\t"
    "buffer_store_dwordx4 v[148:151], v152, %12, s47 offen offset:1024 sc1\n\t"
    "L_ahe621_%=:\n\t"
    "s_waitcnt vmcnt(3)\n\t"
    "v_lshl_add_u32 v76, v32, 3, s36\n\t"
    "v_lshl_add_u32 v77, v33, 3, s37\n\t"
    "v_lshl_add_u32 v78, v34, 3, s38\n\t"
    "v_lshl_add_u32 v79, v35, 3, s39\n\t"
    "v_lshl_add_u32 v80, v36, 3, s40\n\t"
    "v_lshl_add_u32 v81, v37, 3, s41\n\t"
    "v_lshl_add_u32 v82, v38, 3, s42\n\t"
    "v_lshl_add_u32 v83, v39, 3, s43\n\t"
    "s_waitcnt vmcnt(1)\n\t"
    "v_lshl_add_u32 v48, v48, 3, s36\n\t"
    "v_lshl_add_u32 v49, v49, 3, s37\n\t"
    "v_lshl_add_u32 v50, v50, 3, s38\n\t"
    "v_lshl_add_u32 v51, v51, 3, s39\n\t"
    "v_lshl_or_b32 v56, v49, 16, v48\n\t"
    "v_lshl_or_b32 v57, v51, 16, v50\n\t"
    "s_and_b32 s51, %14, 1\n\t"
    "s_mul_i32 s51, s51, 1016\n\t"
    "s_sub_u32 s50, s49, s51\n\t"
    "s_add_u32 s50, s50, 196608\n\t"
    "buffer_store_dwordx2 v[56:57], v152, %12, s50 offen sc1\n\t"
    "s_lshl_b32 s50, %14, 10\n\t"
    "s_add_u32 s50, s50, 16515072\n\t"
    "s_bitcmp1_b32 %14, 1\n\t"
    "s_cselect_b32 s50, s50, 0x40000000\n\t"
    "buffer_store_dwordx4 v[52:55], v152, %12, s50 offen sc1\n\t"
    "s_mul_i32 s50, %14, 69632\n\t"
    "s_add_u32 s50, s50, 30474240\n\t"
    "v_mov_b32 v60, 0\n\t"
    "v_lshlrev_b32 v61, 3, v152\n\t"
    "s_mov_b64 exec, 0xffffffff\n\t"
    "buffer_store_dword v60, v61, %12, s50 offen sc1\n\t"
    "s_mov_b64 exec, -1\n\t"
    "s_cmp_lg_u32 %14, 0\n\t"
    "s_cbranch_scc1 L_ahe659_%=\n\t"
    "buffer_load_dwordx4 v[148:151], v152, %12, s47 offen sc1\n\t"
    "L_ahe659_%=:\n\t"
    "s_waitcnt lgkmcnt(11)\n\t"
    "v_and_b32 v123, v123, v115\n\t"
    "v_perm_b32 v123, v123, v123, s46\n\t"
    "ds_or_b32 v97, v123\n\t"
    "v_and_b32 v124, v124, v116\n\t"
    "v_perm_b32 v124, v124, v124, s46\n\t"
    "ds_or_b32 v98, v124\n\t"
    "v_and_b32 v125, v125, v117\n\t"
    "v_perm_b32 v125, v125, v125, s46\n\t"
    "ds_or_b32 v99, v125\n\t"
    "s_waitcnt lgkmcnt(11)\n\t"
    "buffer_store_dwordx4 v[72:75], v66, %12, s49 offen offset:128 sc1\n\t"
    "v_mov_b32 v129, 0xffff0000\n\t"
    "s_waitcnt lgkmcnt(7)\n\t"
    "v_perm_b32 v100, v100, v84, v126\n\t"
    "v_perm_b32 v101, v101, v85, v126\n\t"
    "v_perm_b32 v102, v102, v86, v126\n\t"
    "v_perm_b32 v103, v103, v87, v126\n\t"
    "s_waitcnt lgkmcnt(3)\n\t"
    "v_perm_b32 v104, v104, v88, v126\n\t"
    "v_perm_b32 v105, v105, v89, v126\n\t"
    "v_perm_b32 v106, v106, v90, v126\n\t"
    "v_perm_b32 v107, v107, v91, v126\n\t"
    "v_max_u32 v108, v100, v101\n\t"
    "v_min_u32 v101, v100, v101\n\t"
    "v_max_u32 v100, v102, v103\n\t"
    "v_min_u32 v103, v102, v103\n\t"
    "v_max_u32 v102, v104, v105\n\t"
    "v_min_u32 v105, v104, v105\n\t"
    "v_max_u32 v104, v106, v107\n\t"
    "v_min_u32 v107, v106, v107\n\t"
    "v_max_u32 v106, v108, v100\n\t"
    "v_min_u32 v100, v108, v100\n\t"
    "v_max_u32 v108, v101, v103\n\t"
    "v_min_u32 v103, v101, v103\n\t"
    "v_max_u32 v101, v102, v104\n\t"
    "v_min_u32 v104, v102, v104\n\t"
    "v_max_u32 v102, v105, v107\n\t"
    "v_min_u32 v107, v105, v107\n\t"
    "v_bfi_b32 v130, v106, 0, v129\n\t"
    "v_cmp_eq_u32 vcc, 0, v130\n\t"
    "v_and_or_b32 v92, v106, s44, v68\n\t"
    "s_nop 0\n\t"
    "v_cndmask_b32 v130, v130, v129, vcc\n\t"
    "v_ffbl_b32 v131, v130\n\t"
    "v_and_b32 v134, 7, v131\n\t"
    "v_lshlrev_b32 v134, v134, s45\n\t"
    "v_xor_b32 v129, v129, v134\n\t"
    "v_xor_b32 v132, v131, v127\n\t"
    "v_bfi_b32 v130, v101, 0, v129\n\t"
    "v_cmp_eq_u32 vcc, 0, v130\n\t"
    "v_and_or_b32 v93, v101, s44, v68\n\t"
    "v_xor_b32 v133, 23, v132\n\t"
    "v_lshlrev_b32 v110, v132, v136\n\t"
    "v_and_b32 v132, 7, v132\n\t"
    "v_lshl_or_b32 v110, v136, v133, v110\n\t"
    "v_lshl_add_u32 v132, v132, 1, v128\n\t"
    "ds_or_rtn_b32 v118, v92, v110\n\t"
    "ds_write_b16 v132, v106\n\t"
    "v_cndmask_b32 v130, v130, v129, vcc\n\t"
    "v_ffbl_b32 v131, v130\n\t"
    "v_and_b32 v134, 7, v131\n\t"
    "v_lshlrev_b32 v134, v134, s45\n\t"
    "v_xor_b32 v129, v129, v134\n\t"
    "v_xor_b32 v132, v131, v127\n\t"
    "v_bfi_b32 v130, v108, 0, v129\n\t"
    "v_cmp_eq_u32 vcc, 0, v130\n\t"
    "v_and_or_b32 v94, v108, s44, v68\n\t"
    "v_xor_b32 v133, 23, v132\n\t"
    "v_lshlrev_b32 v111, v132, v136\n\t"
    "v_and_b32 v132, 7, v132\n\t"
    "v_lshl_or_b32 v111, v136, v133, v111\n\t"
    "v_lshl_add_u32 v132, v132, 1, v128\n\t"
    "ds_or_rtn_b32 v119, v93, v111\n\t"
    "ds_write_b16 v132, v101\n\t"
    "v_cndmask_b32 v130, v130, v129, vcc\n\t"
    "v_ffbl_b32 v131, v130\n\t"
    "v_and_b32 v134, 7, v131\n\t"
    "v_lshlrev_b32 v134, v134, s45\n\t"
    "v_xor_b32 v129, v129, v134\n\t"
    "v_xor_b32 v132, v131, v127\n\t"
    "v_bfi_b32 v130, v100, 0, v129\n\t"
    "v_cmp_eq_u32 vcc, 0, v130\n\t"
    "v_and_or_b32 v95, v100, s44, v68\n\t"
    "v_xor_b32 v133, 23, v132\n\t"
    "v_lshlrev_b32 v112, v132, v136\n\t"
    "v_and_b32 v132, 7, v132\n\t"
    "v_lshl_or_b32 v112, v136, v133, v112\n\t"
    "v_lshl_add_u32 v132, v132, 1, v128\n\t"
    "ds_or_rtn_b32 v120, v94, v112\n\t"
    "ds_write_b16 v132, v108\n\t"
    "v_cndmask_b32 v130, v130, v129, vcc\n\t"
    "v_ffbl_b32 v131, v130\n\t"
    "v_and_b32 v134, 7, v131\n\t"
    "v_lshlrev_b32 v134, v134, s45\n\t"
    "v_xor_b32 v129, v129, v134\n\t"
    "v_xor_b32 v132, v131, v127\n\t"
    "v_bfi_b32 v130, v102, 0, v129\n\t"
    "v_cmp_eq_u32 vcc, 0, v130\n\t"
    "v_and_or_b32 v96, v102, s44, v68\n\t"
    "v_xor_b32 v133, 23, v132\n\t"
    "v_lshlrev_b32 v113, v132, v136\n\t"
    "v_and_b32 v132, 7, v132\n\t"
    "v_lshl_or_b32 v113, v136, v133, v113\n\t"
    "v_lshl_add_u32 v132, v132, 1, v128\n\t"
    "ds_or_rtn_b32 v121, v95, v113\n\t"
    "ds_write_b16 v132, v100\n\t"
    "v_cndmask_b32 v130, v130, v129, vcc\n\t"
    "v_ffbl_b32 v131, v130\n\t"
    "v_and_b32 v134, 7, v131\n\t"
    "v_lshlrev_b32 v134, v134, s45\n\t"
    "v_xor_b32 v129, v129, v134\n\t"
    "v_xor_b32 v132, v131, v127\n\t"
    "s_waitcnt lgkmcnt(7)\n\t"
    "v_and_b32 v118, v118, v110\n\t"
    "v_perm_b32 v118, v118, v118, s46\n\t"
    "ds_or_b32 v92, v118\n\t"
    "v_bfi_b32 v130, v104, 0, v129\n\t"
    "v_cmp_eq_u32 vcc, 0, v130\n\t"
    "v_and_or_b32 v97, v104, s44, v68\n\t"
    "v_xor_b32 v133, 23, v132\n\t"
    "v_lshlrev_b32 v114, v132, v136\n\t"
    "v_and_b32 v132, 7, v132\n\t"
    "v_lshl_or_b32 v114, v136, v133, v114\n\t"
    "v_lshl_add_u32 v132, v132, 1, v128\n\t"
    "ds_or_rtn_b32 v122, v96, v114\n\t"
    "ds_write_b16 v132, v102\n\t"
    "v_cndmask_b32 v130, v130, v129, vcc\n\t"
    "v_ffbl_b32 v131, v130\n\t"
    "v_and_b32 v134, 7, v131\n\t"
    "v_lshlrev_b32 v134, v134, s45\n\t"
    "v_xor_b32 v129, v129, v134\n\t"
    "v_xor_b32 v132, v131, v127\n\t"
    "s_waitcnt lgkmcnt(8)\n\t"
    "v_and_b32 v119, v119, v111\n\t"
    "v_perm_b32 v119, v119, v119, s46\n\t"
    "ds_or_b32 v93, v119\n\t"
    "v_bfi_b32 v130, v103, 0, v129\n\t"
    "v_cmp_eq_u32 vcc, 0, v130\n\t"
    "v_and_or_b32 v98, v103, s44, v68\n\t"
    "v_xor_b32 v133, 23, v132\n\t"
    "v_lshlrev_b32 v115, v132, v136\n\t"
    "v_and_b32 v132, 7, v132\n\t"
    "v_lshl_or_b32 v115, v136, v133, v115\n\t"
    "v_lshl_add_u32 v132, v132, 1, v128\n\t"
    "ds_or_rtn_b32 v123, v97, v115\n\t"
    "ds_write_b16 v132, v104\n\t"
    "v_cndmask_b32 v130, v130, v129, vcc\n\t"
    "v_ffbl_b32 v131, v130\n\t"
    "v_and_b32 v134, 7, v131\n\t"
    "v_lshlrev_b32 v134, v134, s45\n\t"
    "v_xor_b32 v129, v129, v134\n\t"
    "v_xor_b32 v132, v131, v127\n\t"
    "s_waitcnt lgkmcnt(9)\n\t"
    "v_and_b32 v120, v120, v112\n\t"
    "v_perm_b32 v120, v120, v120, s46\n\t"
    "ds_or_b32 v94, v120\n\t"
    "v_bfi_b32 v130, v107, 0, v129\n\t"
    "v_cmp_eq_u32 vcc, 0, v130\n\t"
    "v_and_or_b32 v99, v107, s44, v68\n\t"
    "v_xor_b32 v133, 23, v132\n\t"
    "v_lshlrev_b32 v116, v132, v136\n\t"
    "v_and_b32 v132, 7, v132\n\t"
    "v_lshl_or_b32 v116, v136, v133, v116\n\t"
    "v_lshl_add_u32 v132, v132, 1, v128\n\t"
    "ds_or_rtn_b32 v124, v98, v116\n\t"
    "ds_write_b16 v132, v103\n\t"
    "v_cndmask_b32 v130, v130, v129, vcc\n\t"
    "v_ffbl_b32 v131, v130\n\t"
    "v_xor_b32 v132, v131, v127\n\t"
    "s_waitcnt lgkmcnt(10)\n\t"
    "v_and_b32 v121, v121, v113\n\t"
    "v_perm_b32 v121, v121, v121, s46\n\t"
    "ds_or_b32 v95, v121\n\t"
    "v_xor_b32 v133, 23, v132\n\t"
    "v_lshlrev_b32 v117, v132, v136\n\t"
    "v_and_b32 v132, 7, v132\n\t"
    "v_lshl_or_b32 v117, v136, v133, v117\n\t"
    "v_lshl_add_u32 v132, v132, 1, v128\n\t"
    "ds_or_rtn_b32 v125, v99, v117\n\t"
    "ds_write_b16 v132, v107\n\t"
    "s_waitcnt lgkmcnt(10)\n\t"
    "v_and_b32 v122, v122, v114\n\t"
    "v_perm_b32 v122, v122, v122, s46\n\t"
    "ds_or_b32 v96, v122\n\t"
    "ds_read_b128 v[72:75], v128\n\t"
    "v_and_or_b32 v100, v76, s44, v68\n\t"
    "ds_read_b32 v100, v100\n\t"
    "v_and_or_b32 v101, v77, s44, v68\n\t"
    "ds_read_b32 v101, v101\n\t"
    "v_and_or_b32 v102, v78, s44, v68\n\t"
    "ds_read_b32 v102, v102\n\t"
    "v_and_or_b32 v103, v79, s44, v68\n\t"
    "ds_read_b32 v103, v103\n\t"
    "v_and_or_b32 v104, v80, s44, v68\n\t"
    "ds_read_b32 v104, v104\n\t"
    "v_and_or_b32 v105, v81, s44, v68\n\t"
    "ds_read_b32 v105, v105\n\t"
    "v_and_or_b32 v106, v82, s44, v68\n\t"
    "ds_read_b32 v106, v106\n\t"
    "v_and_or_b32 v107, v83, s44, v68\n\t"
    "ds_read_b32 v107, v107\n\t"
    "s_cmp_lg_u32 %14, 0\n\t"
    "s_cbranch_scc1 L_ahe865_%=\n\t"
    "s_waitcnt vmcnt(0)\n\t"
    "buffer_store_dwordx4 v[148:151], v152, %12, s47 offen offset:1024 sc1\n\t"
    "L_ahe865_%=:\n\t"
    "s_cmp_lg_u32 %14, 0\n\t"
    "s_cbranch_scc1 L_ahe870_%=\n\t"
    "buffer_load_dwordx4 v[148:151], v152, %12, s47 offen sc1\n\t"
    "L_ahe870_%=:\n\t"
    "s_waitcnt lgkmcnt(11)\n\t"
    "v_and_b32 v123, v123, v115\n\t"
    "v_perm_b32 v123, v123, v123, s46\n\t"
    "ds_or_b32 v97, v123\n\t"
    "v_and_b32 v124, v124, v116\n\t"
    "v_perm_b32 v124, v124, v124, s46\n\t"
    "ds_or_b32 v98, v124\n\t"
    "v_and_b32 v125, v125, v117\n\t"
    "v_perm_b32 v125, v125, v125, s46\n\t"
    "ds_or_b32 v99, v125\n\t"
    "s_waitcnt lgkmcnt(11)\n\t"
    "buffer_store_dwordx4 v[72:75], v66, %12, s49 offen offset:192 sc1\n\t"
    "v_mov_b32 v129, 0xffff0000\n\t"
    "s_waitcnt lgkmcnt(7)\n\t"
    "v_perm_b32 v100, v100, v76, v126\n\t"
    "v_perm_b32 v101, v101, v77, v126\n\t"
    "v_perm_b32 v102, v102, v78, v126\n\t"
    "v_perm_b32 v103, v103, v79, v126\n\t"
    "s_waitcnt lgkmcnt(3)\n\t"
    "v_perm_b32 v104, v104, v80, v126\n\t"
    "v_perm_b32 v105, v105, v81, v126\n\t"
    "v_perm_b32 v106, v106, v82, v126\n\t"
    "v_perm_b32 v107, v107, v83, v126\n\t"
    "v_max_u32 v108, v100, v101\n\t"
    "v_min_u32 v101, v100, v101\n\t"
    "v_max_u32 v100, v102, v103\n\t"
    "v_min_u32 v103, v102, v103\n\t"
    "v_max_u32 v102, v104, v105\n\t"
    "v_min_u32 v105, v104, v105\n\t"
    "v_max_u32 v104, v106, v107\n\t"
    "v_min_u32 v107, v106, v107\n\t"
    "v_max_u32 v106, v108, v100\n\t"
    "v_min_u32 v100, v108, v100\n\t"
    "v_max_u32 v108, v101, v103\n\t"
    "v_min_u32 v103, v101, v103\n\t"
    "v_max_u32 v101, v102, v104\n\t"
    "v_min_u32 v104, v102, v104\n\t"
    "v_max_u32 v102, v105, v107\n\t"
    "v_min_u32 v107, v105, v107\n\t"
    "v_bfi_b32 v130, v106, 0, v129\n\t"
    "v_cmp_eq_u32 vcc, 0, v130\n\t"
    "v_and_or_b32 v92, v106, s44, v68\n\t"
    "s_nop 0\n\t"
    "v_cndmask_b32 v130, v130, v129, vcc\n\t"
    "v_ffbl_b32 v131, v130\n\t"
    "v_and_b32 v134, 7, v131\n\t"
    "v_lshlrev_b32 v134, v134, s45\n\t"
    "v_xor_b32 v129, v129, v134\n\t"
    "v_xor_b32 v132, v131, v127\n\t"
    "v_bfi_b32 v130, v101, 0, v129\n\t"
    "v_cmp_eq_u32 vcc, 0, v130\n\t"
    "v_and_or_b32 v93, v101, s44, v68\n\t"
    "v_xor_b32 v133, 23, v132\n\t"
    "v_lshlrev_b32 v110, v132, v136\n\t"
    "v_and_b32 v132, 7, v132\n\t"
    "v_lshl_or_b32 v110, v136, v133, v110\n\t"
    "v_lshl_add_u32 v132, v132, 1, v128\n\t"
    "ds_or_rtn_b32 v118, v92, v110\n\t"
    "ds_write_b16 v132, v106\n\t"
    "v_cndmask_b32 v130, v130, v129, vcc\n\t"
    "v_ffbl_b32 v131, v130\n\t"
    "v_and_b32 v134, 7, v131\n\t"
    "v_lshlrev_b32 v134, v134, s45\n\t"
    "v_xor_b32 v129, v129, v134\n\t"
    "v_xor_b32 v132, v131, v127\n\t"
    "v_bfi_b32 v130, v108, 0, v129\n\t"
    "v_cmp_eq_u32 vcc, 0, v130\n\t"
    "v_and_or_b32 v94, v108, s44, v68\n\t"
    "v_xor_b32 v133, 23, v132\n\t"
    "v_lshlrev_b32 v111, v132, v136\n\t"
    "v_and_b32 v132, 7, v132\n\t"
    "v_lshl_or_b32 v111, v136, v133, v111\n\t"
    "v_lshl_add_u32 v132, v132, 1, v128\n\t"
    "ds_or_rtn_b32 v119, v93, v111\n\t"
    "ds_write_b16 v132, v101\n\t"
    "v_cndmask_b32 v130, v130, v129, vcc\n\t"
    "v_ffbl_b32 v131, v130\n\t"
    "v_and_b32 v134, 7, v131\n\t"
    "v_lshlrev_b32 v134, v134, s45\n\t"
    "v_xor_b32 v129, v129, v134\n\t"
    "v_xor_b32 v132, v131, v127\n\t"
    "v_bfi_b32 v130, v100, 0, v129\n\t"
    "v_cmp_eq_u32 vcc, 0, v130\n\t"
    "v_and_or_b32 v95, v100, s44, v68\n\t"
    "v_xor_b32 v133, 23, v132\n\t"
    "v_lshlrev_b32 v112, v132, v136\n\t"
    "v_and_b32 v132, 7, v132\n\t"
    "v_lshl_or_b32 v112, v136, v133, v112\n\t"
    "v_lshl_add_u32 v132, v132, 1, v128\n\t"
    "ds_or_rtn_b32 v120, v94, v112\n\t"
    "ds_write_b16 v132, v108\n\t"
    "v_cndmask_b32 v130, v130, v129, vcc\n\t"
    "v_ffbl_b32 v131, v130\n\t"
    "v_and_b32 v134, 7, v131\n\t"
    "v_lshlrev_b32 v134, v134, s45\n\t"
    "v_xor_b32 v129, v129, v134\n\t"
    "v_xor_b32 v132, v131, v127\n\t"
    "v_bfi_b32 v130, v102, 0, v129\n\t"
    "v_cmp_eq_u32 vcc, 0, v130\n\t"
    "v_and_or_b32 v96, v102, s44, v68\n\t"
    "v_xor_b32 v133, 23, v132\n\t"
    "v_lshlrev_b32 v113, v132, v136\n\t"
    "v_and_b32 v132, 7, v132\n\t"
    "v_lshl_or_b32 v113, v136, v133, v113\n\t"
    "v_lshl_add_u32 v132, v132, 1, v128\n\t"
    "ds_or_rtn_b32 v121, v95, v113\n\t"
    "ds_write_b16 v132, v100\n\t"
    "v_cndmask_b32 v130, v130, v129, vcc\n\t"
    "v_ffbl_b32 v131, v130\n\t"
    "v_and_b32 v134, 7, v131\n\t"
    "v_lshlrev_b32 v134, v134, s45\n\t"
    "v_xor_b32 v129, v129, v134\n\t"
    "v_xor_b32 v132, v131, v127\n\t"
    "s_waitcnt lgkmcnt(7)\n\t"
    "v_and_b32 v118, v118, v110\n\t"
    "v_perm_b32 v118, v118, v118, s46\n\t"
    "ds_or_b32 v92, v118\n\t"
    "v_bfi_b32 v130, v104, 0, v129\n\t"
    "v_cmp_eq_u32 vcc, 0, v130\n\t"
    "v_and_or_b32 v97, v104, s44, v68\n\t"
    "v_xor_b32 v133, 23, v132\n\t"
    "v_lshlrev_b32 v114, v132, v136\n\t"
    "v_and_b32 v132, 7, v132\n\t"
    "v_lshl_or_b32 v114, v136, v133, v114\n\t"
    "v_lshl_add_u32 v132, v132, 1, v128\n\t"
    "ds_or_rtn_b32 v122, v96, v114\n\t"
    "ds_write_b16 v132, v102\n\t"
    "v_cndmask_b32 v130, v130, v129, vcc\n\t"
    "v_ffbl_b32 v131, v130\n\t"
    "v_and_b32 v134, 7, v131\n\t"
    "v_lshlrev_b32 v134, v134, s45\n\t"
    "v_xor_b32 v129, v129, v134\n\t"
    "v_xor_b32 v132, v131, v127\n\t"
    "s_waitcnt lgkmcnt(8)\n\t"
    "v_and_b32 v119, v119, v111\n\t"
    "v_perm_b32 v119, v119, v119, s46\n\t"
    "ds_or_b32 v93, v119\n\t"
    "v_bfi_b32 v130, v103, 0, v129\n\t"
    "v_cmp_eq_u32 vcc, 0, v130\n\t"
    "v_and_or_b32 v98, v103, s44, v68\n\t"
    "v_xor_b32 v133, 23, v132\n\t"
    "v_lshlrev_b32 v115, v132, v136\n\t"
    "v_and_b32 v132, 7, v132\n\t"
    "v_lshl_or_b32 v115, v136, v133, v115\n\t"
    "v_lshl_add_u32 v132, v132, 1, v128\n\t"
    "ds_or_rtn_b32 v123, v97, v115\n\t"
    "ds_write_b16 v132, v104\n\t"
    "v_cndmask_b32 v130, v130, v129, vcc\n\t"
    "v_ffbl_b32 v131, v130\n\t"
    "v_and_b32 v134, 7, v131\n\t"
    "v_lshlrev_b32 v134, v134, s45\n\t"
    "v_xor_b32 v129, v129, v134\n\t"
    "v_xor_b32 v132, v131, v127\n\t"
    "s_waitcnt lgkmcnt(9)\n\t"
    "v_and_b32 v120, v120, v112\n\t"
    "v_perm_b32 v120, v120, v120, s46\n\t"
    "ds_or_b32 v94, v120\n\t"
    "v_bfi_b32 v130, v107, 0, v129\n\t"
    "v_cmp_eq_u32 vcc, 0, v130\n\t"
    "v_and_or_b32 v99, v107, s44, v68\n\t"
    "v_xor_b32 v133, 23, v132\n\t"
    "v_lshlrev_b32 v116, v132, v136\n\t"
    "v_and_b32 v132, 7, v132\n\t"
    "v_lshl_or_b32 v116, v136, v133, v116\n\t"
    "v_lshl_add_u32 v132, v132, 1, v128\n\t"
    "ds_or_rtn_b32 v124, v98, v116\n\t"
    "ds_write_b16 v132, v103\n\t"
    "v_cndmask_b32 v130, v130, v129, vcc\n\t"
    "v_ffbl_b32 v131, v130\n\t"
    "v_xor_b32 v132, v131, v127\n\t"
    "s_waitcnt lgkmcnt(10)\n\t"
    "v_and_b32 v121, v121, v113\n\t"
    "v_perm_b32 v121, v121, v121, s46\n\t"
    "ds_or_b32 v95, v121\n\t"
    "v_xor_b32 v133, 23, v132\n\t"
    "v_lshlrev_b32 v117, v132, v136\n\t"
    "v_and_b32 v132, 7, v132\n\t"
    "v_lshl_or_b32 v117, v136, v133, v117\n\t"
    "v_lshl_add_u32 v132, v132, 1, v128\n\t"
    "ds_or_rtn_b32 v125, v99, v117\n\t"
    "ds_write_b16 v132, v107\n\t"
    "s_waitcnt lgkmcnt(10)\n\t"
    "v_and_b32 v122, v122, v114\n\t"
    "v_perm_b32 v122, v122, v122, s46\n\t"
    "ds_or_b32 v96, v122\n\t"
    "ds_read_b128 v[72:75], v128\n\t"
    "s_waitcnt lgkmcnt(3)\n\t"
    "v_and_b32 v123, v123, v115\n\t"
    "v_perm_b32 v123, v123, v123, s46\n\t"
    "ds_or_b32 v97, v123\n\t"
    "v_and_b32 v124, v124, v116\n\t"
    "v_perm_b32 v124, v124, v124, s46\n\t"
    "ds_or_b32 v98, v124\n\t"
    "v_and_b32 v125, v125, v117\n\t"
    "v_perm_b32 v125, v125, v125, s46\n\t"
    "ds_or_b32 v99, v125\n\t"
    "s_waitcnt lgkmcnt(3)\n\t"
    "buffer_store_dwordx4 v[72:75], v66, %12, s49 offen offset:256 sc1\n\t"
    "v_mov_b32 v134, 1036\n\t"
    "ds_write_b32 v134, v70 offset:1036\n\t"
    "ds_write_b32 v134, v136\n\t"
    "s_cmp_lg_u32 %14, 0\n\t"
    "s_cbranch_scc1 L_ahsend_%=\n\t"
    "v_mov_b32 v132, 3\n\t"
    "s_waitcnt vmcnt(0)\n\t"
    "ds_write_b32 v134, v132\n\t"
    "buffer_store_dwordx4 v[148:151], v152, %12, s47 offen offset:1024 sc1\n\t"
    "s_setprio 0\n\t"
    "v_and_b32 v132, 0x70, v152\n\t"
    "v_lshlrev_b32 v132, 3, v132\n\t"
    "s_mov_b32 s51, 1048576\n\t"
    "L_ahs2_%=:\n\t"
    "s_sleep 8\n\t"
    "buffer_load_dword v71, v132, %12, s48 offen offset:1024 sc1\n\t"
    "buffer_load_dwordx4 v[148:151], v152, %12, s47 offen sc1\n\t"
    "s_sub_u32 s51, s51, 1\n\t"
    "s_cmp_eq_u32 s51, 0\n\t"
    "s_cbranch_scc1 L_ahsend_%=\n\t"
    "s_waitcnt vmcnt(0)\n\t"
    "buffer_store_dwordx4 v[148:151], v152, %12, s47 offen offset:1024 sc1\n\t"
    "v_cmp_gt_u32 vcc, 32, v71\n\t"
    "s_cbranch_vccnz L_ahs2_%=\n\t"
    "L_ahsend_%=:\n\t"
    "s_setprio 0\n\t"
    "s_and_b32 s46, %16, 3\n\t"
    "s_mul_i32 s49, s46, 1040\n\t"
    "s_add_u32 s49, s49, 2064\n\t"
    "s_lshl_b32 s46, s46, 3\n\t"
    "s_lshr_b32 s47, 1076763402, s46\n\t"
    "s_and_b32 s47, s47, 0xff\n\t"
    "s_lshl_b32 s48, s47, 16\n\t"
    "s_lshr_b32 s47, 773523968, s46\n\t"
    "s_and_b32 s47, s47, 0xff\n\t"
    "s_add_u32 s48, s48, s47\n\t"
    "s_lshr_b32 s47, %16, 2\n\t"
    "s_add_u32 s47, s47, 0x200\n\t"
    "s_cmp_eq_u32 s46, 0\n\t"
    "s_cselect_b32 s47, 0x100, s47\n\t"
    "s_add_u32 s48, s48, s47\n\t"
    "v_bfe_u32 v154, v147, 2, 2\n\t"
    "v_and_b32 v155, 3, v147\n\t"
    "v_mul_u32_u24 v154, 25, v154\n\t"
    "v_mad_u32_u24 v154, v155, 6, v154\n\t"
    "v_lshlrev_b32 v153, 2, v154\n\t"
    "s_and_b32 s53, s48, 0xff\n\t"
    "s_bfe_u32 s55, s48, 0x80008\n\t"
    "s_add_u32 s54, s53, s55\n\t"
    "s_add_u32 s55, s54, s55\n\t"
    "s_lshr_b32 s64, s48, 16\n\t"
    "s_mul_i32 s66, s53, 400\n\t"
    "buffer_load_dwordx3 v[128:130], v153, %0, s66 offen\n\t"
    "buffer_load_dwordx4 v[132:135], v153, %0, s66 offen offset:12\n\t"
    "s_mul_i32 s66, s54, 400\n\t"
    "buffer_load_dwordx3 v[136:138], v153, %0, s66 offen\n\t"
    "buffer_load_dwordx4 v[140:143], v153, %0, s66 offen offset:12\n\t"
    "v_cmp_eq_u32 s[62:63], 0, v155\n\t"
    "v_and_b32 v151, 48, v147\n\t"
    "v_or_b32 v155, 0x100000, v151\n\t"
    "v_cndmask_b32 v152, v155, v151, s[62:63]\n\t"
    "v_lshlrev_b32 v117, 20, v147\n\t"
    "v_add_u32 v117, 1024, v117\n\t"
    "v_mov_b32 v118, 1\n\t"
    "v_lshlrev_b32 v95, 4, v147\n\t"
    "v_add_u32 v95, 140288, v95\n\t"
    "v_mbcnt_lo_u32_b32 v100, -1, 0\n\t"
    "v_mbcnt_hi_u32_b32 v100, -1, v100\n\t"
    "s_lshl_b32 s61, %16, 15\n\t"
    "s_add_u32 s61, s61, 16515072\n\t"
    "v_lshlrev_b32 v127, 4, v100\n\t"
    "s_lshr_b32 s37, %14, 4\n\t"
    "s_and_b32 s39, %14, 7\n\t"
    "v_and_b32 v101, 15, v100\n\t"
    "s_lshl_b32 s39, s39, 4\n\t"
    "s_add_u32 s39, s39, s37\n\t"
    "s_mul_i32 s65, %16, 125\n\t"
    "v_add_u32 v155, s65, v101\n\t"
    "s_mul_i32 s63, s39, 102400\n\t"
    "s_add_u32 s63, s63, 3407872\n\t"
    "v_lshrrev_b32 v102, 2, v101\n\t"
    "s_and_b32 s37, s37, 7\n\t"
    "s_lshl_b32 s37, s37, 11\n\t"
    "v_lshrrev_b32 v103, 3, v100\n\t"
    "s_lshl_b32 s36, s39, 16\n\t"
    "s_bfe_u32 s65, %14, 0x10003\n\t"
    "v_and_b32 v103, 2, v103\n\t"
    "s_lshl_b32 s38, s65, 15\n\t"
    "s_add_u32 s36, s36, s38\n\t"
    "v_and_b32 v104, 1, v100\n\t"
    "s_lshl_b32 s38, %16, 12\n\t"
    "s_add_u32 s36, s36, s38\n\t"
    "v_lshlrev_b32 v104, 4, v104\n\t"
    "s_add_u32 s36, s36, 17825792\n\t"
    "v_mov_b32 v101, 0x3f80\n\t"
    "v_lshlrev_b32 v104, v104, v101\n\t"
    "v_cmp_eq_u32 vcc, v102, v103\n\t"
    "s_waitcnt lgkmcnt(0)\n\t"
    "s_lshr_b32 s39, s91, %9\n\t"
    "s_lshl_b32 s39, s39, 16\n\t"
    "v_cndmask_b32 v120, 0, v104, vcc\n\t"
    "v_cndmask_b32 v121, 0, v104, vcc\n\t"
    "v_add_u32 v103, 1, v103\n\t"
    "v_cmp_eq_u32 vcc, v102, v103\n\t"
    "s_lshl_b32 s40, s88, 16\n\t"
    "s_and_b32 s41, s88, 0xffff0000\n\t"
    "v_cndmask_b32 v122, 0, v104, vcc\n\t"
    "v_cndmask_b32 v123, 0, v104, vcc\n\t"
    "v_and_b32 v101, 2, v100\n\t"
    "v_mov_b32 v102, 0x44444444\n\t"
    "v_mov_b32 v103, 0xeeeeeeee\n\t"
    "v_cmp_eq_u32 vcc, 0, v101\n\t"
    "s_lshl_b32 s42, s89, 16\n\t"
    "s_and_b32 s43, s89, 0xffff0000\n\t"
    "v_cndmask_b32 v124, v103, v102, vcc\n\t"
    "v_lshrrev_b32 v101, 4, v100\n\t"
    "v_mul_u32_u24 v101, 8000, v101\n\t"
    "v_lshl_add_u32 v125, v155, 3, v101\n\t"
    "v_add_u32 v125, 64000, v125\n\t"
    "s_lshl_b32 s70, s72, 16\n\t"
    "s_and_b32 s71, s72, 0xffff0000\n\t"
    "s_lshl_b32 s72, s73, 16\n\t"
    "s_and_b32 s73, s73, 0xffff0000\n\t"
    "s_mov_b32 s67, 0xffff0000\n\t"
    "s_and_b32 s65, %14, 8\n\t"
    "s_add_u32 s38, s65, %16\n\t"
    "s_mul_i32 s38, s38, 212992\n\t"
    "s_add_u32 s38, s38, 196608\n\t"
    "s_add_u32 s84, s37, s38\n\t"
    "s_add_u32 s37, s37, 0x800\n\t"
    "s_and_b32 s37, s37, 0x3800\n\t"
    "s_add_u32 s85, s37, s38\n\t"
    "s_add_u32 s37, s37, 0x800\n\t"
    "s_and_b32 s37, s37, 0x3800\n\t"
    "s_add_u32 s86, s37, s38\n\t"
    "s_add_u32 s37, s37, 0x800\n\t"
    "s_and_b32 s37, s37, 0x3800\n\t"
    "s_add_u32 s87, s37, s38\n\t"
    "s_add_u32 s37, s37, 0x800\n\t"
    "s_and_b32 s37, s37, 0x3800\n\t"
    "s_add_u32 s88, s37, s38\n\t"
    "s_add_u32 s37, s37, 0x800\n\t"
    "s_and_b32 s37, s37, 0x3800\n\t"
    "s_add_u32 s89, s37, s38\n\t"
    "s_add_u32 s37, s37, 0x800\n\t"
    "s_and_b32 s37, s37, 0x3800\n\t"
    "s_add_u32 s90, s37, s38\n\t"
    "s_add_u32 s37, s37, 0x800\n\t"
    "s_and_b32 s37, s37, 0x3800\n\t"
    "s_add_u32 s91, s37, s38\n\t"
    "s_mul_i32 s94, s65, 425984\n\t"
    "s_sub_u32 s94, s94, 1703936\n\t"
    "s_sub_u32 s95, 0x8000, s94\n\t"
    "s_lshl_b32 s97, s65, 11\n\t"
    "s_sub_u32 s97, s97, 229376\n\t"
    "s_mov_b32 s92, 64000\n\t"
    "s_mov_b32 s93, -64000\n\t"
    "s_mov_b32 s96, 0x2000\n\t"
    "s_mov_b32 s62, 2\n\t"
    "v_rcp_f32 v101, s40\n\t"
    "v_rcp_f32 v102, s41\n\t"
    "v_rcp_f32 v103, s42\n\t"
    "v_rcp_f32 v104, s43\n\t"
    "v_mov_b32 v100, 0x7f7fffff\n\t"
    "v_med3_f32 v101, v101, -v100, v100\n\t"
    "v_med3_f32 v102, v102, -v100, v100\n\t"
    "v_med3_f32 v103, v103, -v100, v100\n\t"
    "v_med3_f32 v104, v104, -v100, v100\n\t"
    "v_readfirstlane_b32 s40, v101\n\t"
    "v_readfirstlane_b32 s41, v102\n\t"
    "v_readfirstlane_b32 s42, v103\n\t"
    "v_readfirstlane_b32 s43, v104\n\t"
    "L_await_%=:\n\t"
    "L_asp9_%=:\n\t"
    "ds_read_b32 v118, v117 offset:8\n\t"
    "ds_read_b32 v119, v117 offset:4\n\t"
    "s_waitcnt lgkmcnt(0)\n\t"
    "v_readfirstlane_b32 s46, v119\n\t"
    "s_nop 0\n\t"
    "s_cmp_ge_u32 s46, 4\n\t"
    "s_cbranch_scc1 L_ago9_%=\n\t"
    "s_sleep 1\n\t"
    "s_branch L_asp9_%=\n\t"
    "L_ago9_%=:\n\t"
    "v_readfirstlane_b32 s47, v118\n\t"
    "ds_read_b128 v[0:3], v95 offset:0\n\t"
    "ds_read_b128 v[4:7], v95 offset:1024\n\t"
    "ds_read_b128 v[8:11], v95 offset:2048\n\t"
    "ds_read_b128 v[12:15], v95 offset:3072\n\t"
    "ds_read_b128 v[16:19], v95 offset:4096\n\t"
    "ds_read_b128 v[20:23], v95 offset:5120\n\t"
    "ds_read_b128 v[24:27], v95 offset:6144\n\t"
    "ds_read_b128 v[28:31], v95 offset:7168\n\t"
    "ds_read_b128 v[32:35], v95 offset:8192\n\t"
    "ds_read_b128 v[36:39], v95 offset:9216\n\t"
    "ds_read_b128 v[40:43], v95 offset:10240\n\t"
    "ds_read_b128 v[44:47], v95 offset:11264\n\t"
    "ds_read_b128 v[48:51], v95 offset:12288\n\t"
    "ds_read_b128 v[52:55], v95 offset:13312\n\t"
    "ds_read_b128 v[56:59], v95 offset:14336\n\t"
    "ds_read_b128 v[60:63], v95 offset:15360\n\t"
    "ds_read_b128 v[64:67], v95 offset:16384\n\t"
    "ds_read_b128 v[68:71], v95 offset:17408\n\t"
    "ds_read_b128 v[72:75], v95 offset:18432\n\t"
    "ds_read_b128 v[76:79], v95 offset:19456\n\t"
    "ds_read_b128 v[80:83], v95 offset:20480\n\t"
    "ds_read_b128 v[84:87], v95 offset:21504\n\t"
    "ds_read_b128 v[88:91], v95 offset:22528\n\t"
    "s_waitcnt vmcnt(0)\n\t"
    "s_cmp_lg_u32 %16, 1\n\t"
    "s_cbranch_scc1 L_anh3_%=\n\t"
    "v_mov_b32 v119, 4\n\t"
    "ds_write_b32 v117, v119 offset:12\n\t"
    "s_lshl_b32 s65, %14, 12\n\t"
    "s_add_u32 s65, s65, 16777216\n\t"
    "s_waitcnt lgkmcnt(15)\n\t"
    "buffer_store_dwordx4 v[0:3], v127, %12, s65 offen offset:0 sc1\n\t"
    "buffer_store_dwordx4 v[4:7], v127, %12, s65 offen offset:1024 sc1\n\t"
    "buffer_store_dwordx4 v[8:11], v127, %12, s65 offen offset:2048 sc1\n\t"
    "buffer_store_dwordx4 v[12:15], v127, %12, s65 offen offset:3072 sc1\n\t"
    "L_anh3_%=:\n\t"
    "s_cmp_ge_u32 s47, 6\n\t"
    "s_cbranch_scc1 L_adok_%=\n\t"
    "L_awd_%=:\n\t"
    "L_asp10_%=:\n\t"
    "ds_read_b32 v119, v117 offset:8\n\t"
    "s_waitcnt lgkmcnt(0)\n\t"
    "v_readfirstlane_b32 s46, v119\n\t"
    "s_nop 0\n\t"
    "s_cmp_ge_u32 s46, 6\n\t"
    "s_cbranch_scc1 L_ago10_%=\n\t"
    "s_sleep 1\n\t"
    "s_branch L_asp10_%=\n\t"
    "L_ago10_%=:\n\t"
    "L_adok_%=:\n\t"
    "L_amain_%=:\n\t"
    "s_mov_b32 s68, 130\n\t"
    "v_mad_u32_u24 v128, v128, s68, v152\n\t"
    "v_and_b32 v128, -16, v128\n\t"
    "ds_read_b128 v[156:159], v128 offset:0\n\t"
    "ds_read_b128 v[160:163], v128 offset:64\n\t"
    "v_mad_u32_u24 v129, v129, s68, v151\n\t"
    "v_and_b32 v129, -16, v129\n\t"
    "ds_read_b128 v[164:167], v129 offset:0\n\t"
    "ds_read_b128 v[168:171], v129 offset:64\n\t"
    "v_mad_u32_u24 v130, v130, s68, v151\n\t"
    "v_and_b32 v130, -16, v130\n\t"
    "ds_read_b128 v[172:175], v130 offset:0\n\t"
    "ds_read_b128 v[176:179], v130 offset:64\n\t"
    "s_mul_i32 s66, s55, 400\n\t"
    "buffer_load_dwordx3 v[128:130], v153, %0, s66 offen\n\t"
    "v_mad_u32_u24 v132, v132, s68, v151\n\t"
    "v_and_b32 v132, -16, v132\n\t"
    "ds_read_b128 v[180:183], v132 offset:0\n\t"
    "ds_read_b128 v[184:187], v132 offset:64\n\t"
    "v_mad_u32_u24 v133, v133, s68, v151\n\t"
    "v_and_b32 v133, -16, v133\n\t"
    "ds_read_b128 v[188:191], v133 offset:0\n\t"
    "ds_read_b128 v[192:195], v133 offset:64\n\t"
    "v_mad_u32_u24 v134, v134, s68, v151\n\t"
    "v_and_b32 v134, -16, v134\n\t"
    "ds_read_b128 v[196:199], v134 offset:0\n\t"
    "ds_read_b128 v[200:203], v134 offset:64\n\t"
    "v_mad_u32_u24 v135, v135, s68, v151\n\t"
    "v_and_b32 v135, -16, v135\n\t"
    "ds_read_b128 v[204:207], v135 offset:0\n\t"
    "ds_read_b128 v[208:211], v135 offset:64\n\t"
    "v_mov_b32 v150, 1\n\t"
    "v_lshlrev_b32 v149, 20, v147\n\t"
    "v_add_u32 v149, s49, v149\n\t"
    "v_lshlrev_b32 v146, 2, v147\n\t"
    "s_mov_b32 s52, s53\n\t"
    "s_lshl_b32 s50, %14, 14\n\t"
    "s_add_u32 s50, s50, 26214400\n\t"
    "L_apnn_%=:\n\t"
    "s_mul_i32 s69, s52, 256\n\t"
    "s_add_u32 s69, s69, s50\n\t"
    "s_waitcnt lgkmcnt(8)\n\t"
    "v_mfma_f32_16x16x32_bf16 v[92:95], v[156:159], v[156:159], 0\n\t"
    "v_fmac_f32 v145, v108, v72\n\t"
    "v_fmac_f32 v144, v109, v73\n\t"
    "ds_add_rtn_u32 v148, v149, v150\n\t"
    "v_mfma_f32_16x16x32_bf16 v[92:95], v[160:163], v[160:163], v[92:95]\n\t"
    "v_fmac_f32 v145, v110, v74\n\t"
    "v_fmac_f32 v144, v111, v75\n\t"
    "buffer_load_dwordx4 v[132:135], v153, %0, s66 offen offset:12\n\t"
    "v_mfma_f32_16x16x32_bf16 v[96:99], v[164:167], v[164:167], 0\n\t"
    "v_fmac_f32 v145, v112, v76\n\t"
    "v_fmac_f32 v144, v113, v77\n\t"
    "v_mfma_f32_16x16x32_bf16 v[96:99], v[168:171], v[168:171], v[96:99]\n\t"
    "v_fmac_f32 v145, v114, v78\n\t"
    "v_fmac_f32 v144, v115, v79\n\t"
    "v_mfma_f32_16x16x32_bf16 v[100:103], v[164:167], v[172:175], 0\n\t"
    "v_fmac_f32 v145, v116, v80\n\t"
    "v_fmac_f32 v144, v117, v81\n\t"
    "v_mfma_f32_16x16x32_bf16 v[100:103], v[168:171], v[176:179], v[100:103]\n\t"
    "v_fmac_f32 v145, v118, v82\n\t"
    "v_fmac_f32 v144, v119, v83\n\t"
    "s_waitcnt vmcnt(4)\n\t"
    "s_cmp_ge_u32 s55, s64\n\t"
    "s_cbranch_scc1 L_ahk00_%=\n\t"
    "L_ahb00_%=:\n\t"
    "v_mfma_f32_16x16x32_bf16 v[104:107], v[172:175], v[172:175], 0\n\t"
    "v_mad_u32_u24 v136, v136, s68, v152\n\t"
    "v_and_b32 v136, -16, v136\n\t"
    "v_mfma_f32_16x16x32_bf16 v[104:107], v[176:179], v[176:179], v[104:107]\n\t"
    "v_add_f32 v154, v144, v145\n\t"
    "buffer_store_dword v154, v146, %12, s69 offen sc1\n\t"
    "v_mul_f32 v144, v92, v84\n\t"
    "v_mfma_f32_16x16x32_bf16 v[108:111], v[156:159], v[164:167], 0\n\t"
    "v_mul_f32 v145, v96, v0\n\t"
    "v_fmac_f32 v144, v97, v1\n\t"
    "v_mfma_f32_16x16x32_bf16 v[108:111], v[160:163], v[168:171], v[108:111]\n\t"
    "v_fmac_f32 v145, v98, v2\n\t"
    "v_fmac_f32 v144, v99, v3\n\t"
    "s_waitcnt lgkmcnt(7)\n\t"
    "v_mfma_f32_16x16x32_bf16 v[112:115], v[164:167], v[180:183], 0\n\t"
    "v_fmac_f32 v145, v100, v4\n\t"
    "v_fmac_f32 v144, v101, v5\n\t"
    "v_mfma_f32_16x16x32_bf16 v[112:115], v[168:171], v[184:187], v[112:115]\n\t"
    "v_fmac_f32 v145, v102, v6\n\t"
    "v_fmac_f32 v144, v103, v7\n\t"
    "v_mfma_f32_16x16x32_bf16 v[116:119], v[172:175], v[180:183], 0\n\t"
    "v_fmac_f32 v145, v104, v8\n\t"
    "v_fmac_f32 v144, v105, v9\n\t"
    "v_mfma_f32_16x16x32_bf16 v[116:119], v[176:179], v[184:187], v[116:119]\n\t"
    "v_fmac_f32 v145, v106, v10\n\t"
    "v_fmac_f32 v144, v107, v11\n\t"
    "ds_read_b128 v[212:215], v136 offset:0\n\t"
    "ds_read_b128 v[216:219], v136 offset:64\n\t"
    "v_mfma_f32_16x16x32_bf16 v[92:95], v[180:183], v[180:183], 0\n\t"
    "v_mad_u32_u24 v137, v137, s68, v151\n\t"
    "v_and_b32 v137, -16, v137\n\t"
    "v_mfma_f32_16x16x32_bf16 v[92:95], v[184:187], v[184:187], v[92:95]\n\t"
    "v_fmac_f32 v145, v108, v85\n\t"
    "v_mad_u32_u24 v138, v138, s68, v151\n\t"
    "v_mfma_f32_16x16x32_bf16 v[96:99], v[156:159], v[172:175], 0\n\t"
    "v_fmac_f32 v144, v112, v12\n\t"
    "v_fmac_f32 v145, v113, v13\n\t"
    "v_mfma_f32_16x16x32_bf16 v[96:99], v[160:163], v[176:179], v[96:99]\n\t"
    "v_fmac_f32 v144, v114, v14\n\t"
    "v_fmac_f32 v145, v115, v15\n\t"
    "s_waitcnt lgkmcnt(7)\n\t"
    "v_mfma_f32_16x16x32_bf16 v[100:103], v[164:167], v[188:191], 0\n\t"
    "v_and_b32 v138, -16, v138\n\t"
    "v_fmac_f32 v144, v116, v16\n\t"
    "v_mfma_f32_16x16x32_bf16 v[100:103], v[168:171], v[192:195], v[100:103]\n\t"
    "v_fmac_f32 v145, v117, v17\n\t"
    "v_fmac_f32 v144, v118, v18\n\t"
    "v_mfma_f32_16x16x32_bf16 v[104:107], v[172:175], v[188:191], 0\n\t"
    "v_fmac_f32 v145, v119, v19\n\t"
    "v_fmac_f32 v144, v92, v20\n\t"
    "v_mfma_f32_16x16x32_bf16 v[104:107], v[176:179], v[192:195], v[104:107]\n\t"
    "v_fmac_f32 v145, v93, v21\n\t"
    "v_fmac_f32 v144, v94, v22\n\t"
    "s_waitcnt lgkmcnt(5)\n\t"
    "v_mfma_f32_16x16x32_bf16 v[108:111], v[164:167], v[196:199], 0\n\t"
    "v_fmac_f32 v145, v95, v23\n\t"
    "v_fmac_f32 v144, v96, v86\n\t"
    "v_mfma_f32_16x16x32_bf16 v[108:111], v[168:171], v[200:203], v[108:111]\n\t"
    "v_mfma_f32_16x16x32_bf16 v[112:115], v[156:159], v[180:183], 0\n\t"
    "v_fmac_f32 v145, v100, v24\n\t"
    "v_fmac_f32 v144, v101, v25\n\t"
    "v_mfma_f32_16x16x32_bf16 v[112:115], v[160:163], v[184:187], v[112:115]\n\t"
    "v_fmac_f32 v145, v102, v26\n\t"
    "v_fmac_f32 v144, v103, v27\n\t"
    "s_waitcnt lgkmcnt(2)\n\t"
    "v_mfma_f32_16x16x32_bf16 v[116:119], v[164:167], v[204:207], 0\n\t"
    "v_readfirstlane_b32 s60, v148\n\t"
    "v_fmac_f32 v145, v104, v28\n\t"
    "v_mfma_f32_16x16x32_bf16 v[116:119], v[168:171], v[208:211], v[116:119]\n\t"
    "v_fmac_f32 v144, v105, v29\n\t"
    "v_fmac_f32 v145, v106, v30\n\t"
    "ds_read_b128 v[164:167], v137 offset:0\n\t"
    "ds_read_b128 v[168:171], v137 offset:64\n\t"
    "v_mfma_f32_16x16x32_bf16 v[92:95], v[172:175], v[196:199], 0\n\t"
    "v_fmac_f32 v144, v107, v31\n\t"
    "v_fmac_f32 v145, v108, v32\n\t"
    "v_mfma_f32_16x16x32_bf16 v[92:95], v[176:179], v[200:203], v[92:95]\n\t"
    "v_fmac_f32 v144, v109, v33\n\t"
    "v_fmac_f32 v145, v110, v34\n\t"
    "s_waitcnt vmcnt(4)\n\t"
    "s_cmp_ge_u32 s55, s64\n\t"
    "s_cbranch_scc1 L_ahk10_%=\n\t"
    "L_ahb10_%=:\n\t"
    "v_mfma_f32_16x16x32_bf16 v[96:99], v[172:175], v[204:207], 0\n\t"
    "v_mad_u32_u24 v140, v140, s68, v151\n\t"
    "v_and_b32 v140, -16, v140\n\t"
    "v_mfma_f32_16x16x32_bf16 v[96:99], v[176:179], v[208:211], v[96:99]\n\t"
    "v_fmac_f32 v144, v111, v35\n\t"
    "v_fmac_f32 v145, v112, v87\n\t"
    "ds_read_b128 v[172:175], v138 offset:0\n\t"
    "ds_read_b128 v[176:179], v138 offset:64\n\t"
    "s_mul_i32 s66, s60, 400\n\t"
    "v_mfma_f32_16x16x32_bf16 v[100:103], v[156:159], v[188:191], 0\n\t"
    "v_fmac_f32 v144, v116, v36\n\t"
    "v_fmac_f32 v145, v117, v37\n\t"
    "v_mfma_f32_16x16x32_bf16 v[100:103], v[160:163], v[192:195], v[100:103]\n\t"
    "v_fmac_f32 v144, v118, v38\n\t"
    "v_fmac_f32 v145, v119, v39\n\t"
    "buffer_load_dwordx3 v[136:138], v153, %0, s66 offen\n\t"
    "v_mfma_f32_16x16x32_bf16 v[104:107], v[180:183], v[188:191], 0\n\t"
    "v_mad_u32_u24 v141, v141, s68, v151\n\t"
    "v_and_b32 v141, -16, v141\n\t"
    "v_mfma_f32_16x16x32_bf16 v[104:107], v[184:187], v[192:195], v[104:107]\n\t"
    "v_fmac_f32 v144, v92, v40\n\t"
    "v_fmac_f32 v145, v93, v41\n\t"
    "v_mfma_f32_16x16x32_bf16 v[108:111], v[180:183], v[196:199], 0\n\t"
    "v_fmac_f32 v144, v94, v42\n\t"
    "v_fmac_f32 v145, v95, v43\n\t"
    "v_mfma_f32_16x16x32_bf16 v[108:111], v[184:187], v[200:203], v[108:111]\n\t"
    "v_fmac_f32 v144, v96, v44\n\t"
    "v_fmac_f32 v145, v97, v45\n\t"
    "v_mfma_f32_16x16x32_bf16 v[112:115], v[180:183], v[204:207], 0\n\t"
    "v_fmac_f32 v144, v98, v46\n\t"
    "v_fmac_f32 v145, v99, v47\n\t"
    "v_mfma_f32_16x16x32_bf16 v[112:115], v[184:187], v[208:211], v[112:115]\n\t"
    "v_fmac_f32 v144, v100, v88\n\t"
    "v_mad_u32_u24 v142, v142, s68, v151\n\t"
    "ds_read_b128 v[180:183], v140 offset:0\n\t"
    "ds_read_b128 v[184:187], v140 offset:64\n\t"
    "v_mfma_f32_16x16x32_bf16 v[116:119], v[156:159], v[196:199], 0\n\t"
    "v_and_b32 v142, -16, v142\n\t"
    "v_fmac_f32 v145, v104, v48\n\t"
    "v_mfma_f32_16x16x32_bf16 v[116:119], v[160:163], v[200:203], v[116:119]\n\t"
    "v_fmac_f32 v144, v105, v49\n\t"
    "v_fmac_f32 v145, v106, v50\n\t"
    "v_mfma_f32_16x16x32_bf16 v[92:95], v[188:191], v[188:191], 0\n\t"
    "v_fmac_f32 v144, v107, v51\n\t"
    "v_mad_u32_u24 v143, v143, s68, v151\n\t"
    "v_mfma_f32_16x16x32_bf16 v[92:95], v[192:195], v[192:195], v[92:95]\n\t"
    "v_and_b32 v143, -16, v143\n\t"
    "v_fmac_f32 v145, v108, v52\n\t"
    "v_mfma_f32_16x16x32_bf16 v[96:99], v[188:191], v[196:199], 0\n\t"
    "v_fmac_f32 v144, v109, v53\n\t"
    "v_fmac_f32 v145, v110, v54\n\t"
    "v_mfma_f32_16x16x32_bf16 v[96:99], v[192:195], v[200:203], v[96:99]\n\t"
    "v_fmac_f32 v144, v111, v55\n\t"
    "v_fmac_f32 v145, v112, v56\n\t"
    "v_mfma_f32_16x16x32_bf16 v[100:103], v[188:191], v[204:207], 0\n\t"
    "v_fmac_f32 v144, v113, v57\n\t"
    "v_fmac_f32 v145, v114, v58\n\t"
    "v_mfma_f32_16x16x32_bf16 v[100:103], v[192:195], v[208:211], v[100:103]\n\t"
    "v_fmac_f32 v144, v115, v59\n\t"
    "v_fmac_f32 v145, v116, v89\n\t"
    "ds_read_b128 v[188:191], v141 offset:0\n\t"
    "ds_read_b128 v[192:195], v141 offset:64\n\t"
    "v_mfma_f32_16x16x32_bf16 v[104:107], v[156:159], v[204:207], 0\n\t"
    "v_fmac_f32 v144, v92, v60\n\t"
    "v_fmac_f32 v145, v93, v61\n\t"
    "v_mfma_f32_16x16x32_bf16 v[104:107], v[160:163], v[208:211], v[104:107]\n\t"
    "v_fmac_f32 v144, v94, v62\n\t"
    "v_fmac_f32 v145, v95, v63\n\t"
    "v_mfma_f32_16x16x32_bf16 v[108:111], v[196:199], v[196:199], 0\n\t"
    "v_fmac_f32 v144, v96, v64\n\t"
    "v_fmac_f32 v145, v97, v65\n\t"
    "v_mfma_f32_16x16x32_bf16 v[108:111], v[200:203], v[200:203], v[108:111]\n\t"
    "v_fmac_f32 v144, v98, v66\n\t"
    "v_fmac_f32 v145, v99, v67\n\t"
    "v_mfma_f32_16x16x32_bf16 v[112:115], v[196:199], v[204:207], 0\n\t"
    "v_fmac_f32 v144, v100, v68\n\t"
    "v_fmac_f32 v145, v101, v69\n\t"
    "v_mfma_f32_16x16x32_bf16 v[112:115], v[200:203], v[208:211], v[112:115]\n\t"
    "v_fmac_f32 v144, v102, v70\n\t"
    "v_fmac_f32 v145, v103, v71\n\t"
    "ds_read_b128 v[196:199], v142 offset:0\n\t"
    "ds_read_b128 v[200:203], v142 offset:64\n\t"
    "v_mfma_f32_16x16x32_bf16 v[116:119], v[204:207], v[204:207], 0\n\t"
    "v_fmac_f32 v144, v104, v90\n\t"
    "v_mfma_f32_16x16x32_bf16 v[116:119], v[208:211], v[208:211], v[116:119]\n\t"
    "ds_read_b128 v[204:207], v143 offset:0\n\t"
    "ds_read_b128 v[208:211], v143 offset:64\n\t"
    "s_mov_b32 s52, s53\n\t"
    "s_mov_b32 s53, s54\n\t"
    "s_mov_b32 s54, s55\n\t"
    "s_mov_b32 s55, s60\n\t"
    "s_cmp_lt_u32 s53, s64\n\t"
    "s_cbranch_scc0 L_adone_%=\n\t"
    "s_mul_i32 s69, s52, 256\n\t"
    "s_add_u32 s69, s69, s50\n\t"
    "s_waitcnt lgkmcnt(8)\n\t"
    "v_mfma_f32_16x16x32_bf16 v[92:95], v[212:215], v[212:215], 0\n\t"
    "v_fmac_f32 v145, v108, v72\n\t"
    "v_fmac_f32 v144, v109, v73\n\t"
    "ds_add_rtn_u32 v148, v149, v150\n\t"
    "v_mfma_f32_16x16x32_bf16 v[92:95], v[216:219], v[216:219], v[92:95]\n\t"
    "v_fmac_f32 v145, v110, v74\n\t"
    "v_fmac_f32 v144, v111, v75\n\t"
    "buffer_load_dwordx4 v[140:143], v153, %0, s66 offen offset:12\n\t"
    "v_mfma_f32_16x16x32_bf16 v[96:99], v[164:167], v[164:167], 0\n\t"
    "v_fmac_f32 v145, v112, v76\n\t"
    "v_fmac_f32 v144, v113, v77\n\t"
    "v_mfma_f32_16x16x32_bf16 v[96:99], v[168:171], v[168:171], v[96:99]\n\t"
    "v_fmac_f32 v145, v114, v78\n\t"
    "v_fmac_f32 v144, v115, v79\n\t"
    "v_mfma_f32_16x16x32_bf16 v[100:103], v[164:167], v[172:175], 0\n\t"
    "v_fmac_f32 v145, v116, v80\n\t"
    "v_fmac_f32 v144, v117, v81\n\t"
    "v_mfma_f32_16x16x32_bf16 v[100:103], v[168:171], v[176:179], v[100:103]\n\t"
    "v_fmac_f32 v145, v118, v82\n\t"
    "v_fmac_f32 v144, v119, v83\n\t"
    "s_waitcnt vmcnt(4)\n\t"
    "s_cmp_ge_u32 s55, s64\n\t"
    "s_cbranch_scc1 L_ahk01_%=\n\t"
    "L_ahb01_%=:\n\t"
    "v_mfma_f32_16x16x32_bf16 v[104:107], v[172:175], v[172:175], 0\n\t"
    "v_mad_u32_u24 v128, v128, s68, v152\n\t"
    "v_and_b32 v128, -16, v128\n\t"
    "v_mfma_f32_16x16x32_bf16 v[104:107], v[176:179], v[176:179], v[104:107]\n\t"
    "v_add_f32 v154, v144, v145\n\t"
    "buffer_store_dword v154, v146, %12, s69 offen sc1\n\t"
    "v_mul_f32 v144, v92, v84\n\t"
    "v_mfma_f32_16x16x32_bf16 v[108:111], v[212:215], v[164:167], 0\n\t"
    "v_mul_f32 v145, v96, v0\n\t"
    "v_fmac_f32 v144, v97, v1\n\t"
    "v_mfma_f32_16x16x32_bf16 v[108:111], v[216:219], v[168:171], v[108:111]\n\t"
    "v_fmac_f32 v145, v98, v2\n\t"
    "v_fmac_f32 v144, v99, v3\n\t"
    "s_waitcnt lgkmcnt(7)\n\t"
    "v_mfma_f32_16x16x32_bf16 v[112:115], v[164:167], v[180:183], 0\n\t"
    "v_fmac_f32 v145, v100, v4\n\t"
    "v_fmac_f32 v144, v101, v5\n\t"
    "v_mfma_f32_16x16x32_bf16 v[112:115], v[168:171], v[184:187], v[112:115]\n\t"
    "v_fmac_f32 v145, v102, v6\n\t"
    "v_fmac_f32 v144, v103, v7\n\t"
    "v_mfma_f32_16x16x32_bf16 v[116:119], v[172:175], v[180:183], 0\n\t"
    "v_fmac_f32 v145, v104, v8\n\t"
    "v_fmac_f32 v144, v105, v9\n\t"
    "v_mfma_f32_16x16x32_bf16 v[116:119], v[176:179], v[184:187], v[116:119]\n\t"
    "v_fmac_f32 v145, v106, v10\n\t"
    "v_fmac_f32 v144, v107, v11\n\t"
    "ds_read_b128 v[156:159], v128 offset:0\n\t"
    "ds_read_b128 v[160:163], v128 offset:64\n\t"
    "v_mfma_f32_16x16x32_bf16 v[92:95], v[180:183], v[180:183], 0\n\t"
    "v_mad_u32_u24 v129, v129, s68, v151\n\t"
    "v_and_b32 v129, -16, v129\n\t"
    "v_mfma_f32_16x16x32_bf16 v[92:95], v[184:187], v[184:187], v[92:95]\n\t"
    "v_fmac_f32 v145, v108, v85\n\t"
    "v_mad_u32_u24 v130, v130, s68, v151\n\t"
    "v_mfma_f32_16x16x32_bf16 v[96:99], v[212:215], v[172:175], 0\n\t"
    "v_fmac_f32 v144, v112, v12\n\t"
    "v_fmac_f32 v145, v113, v13\n\t"
    "v_mfma_f32_16x16x32_bf16 v[96:99], v[216:219], v[176:179], v[96:99]\n\t"
    "v_fmac_f32 v144, v114, v14\n\t"
    "v_fmac_f32 v145, v115, v15\n\t"
    "s_waitcnt lgkmcnt(7)\n\t"
    "v_mfma_f32_16x16x32_bf16 v[100:103], v[164:167], v[188:191], 0\n\t"
    "v_and_b32 v130, -16, v130\n\t"
    "v_fmac_f32 v144, v116, v16\n\t"
    "v_mfma_f32_16x16x32_bf16 v[100:103], v[168:171], v[192:195], v[100:103]\n\t"
    "v_fmac_f32 v145, v117, v17\n\t"
    "v_fmac_f32 v144, v118, v18\n\t"
    "v_mfma_f32_16x16x32_bf16 v[104:107], v[172:175], v[188:191], 0\n\t"
    "v_fmac_f32 v145, v119, v19\n\t"
    "v_fmac_f32 v144, v92, v20\n\t"
    "v_mfma_f32_16x16x32_bf16 v[104:107], v[176:179], v[192:195], v[104:107]\n\t"
    "v_fmac_f32 v145, v93, v21\n\t"
    "v_fmac_f32 v144, v94, v22\n\t"
    "s_waitcnt lgkmcnt(5)\n\t"
    "v_mfma_f32_16x16x32_bf16 v[108:111], v[164:167], v[196:199], 0\n\t"
    "v_fmac_f32 v145, v95, v23\n\t"
    "v_fmac_f32 v144, v96, v86\n\t"
    "v_mfma_f32_16x16x32_bf16 v[108:111], v[168:171], v[200:203], v[108:111]\n\t"
    "v_mfma_f32_16x16x32_bf16 v[112:115], v[212:215], v[180:183], 0\n\t"
    "v_fmac_f32 v145, v100, v24\n\t"
    "v_fmac_f32 v144, v101, v25\n\t"
    "v_mfma_f32_16x16x32_bf16 v[112:115], v[216:219], v[184:187], v[112:115]\n\t"
    "v_fmac_f32 v145, v102, v26\n\t"
    "v_fmac_f32 v144, v103, v27\n\t"
    "s_waitcnt lgkmcnt(2)\n\t"
    "v_mfma_f32_16x16x32_bf16 v[116:119], v[164:167], v[204:207], 0\n\t"
    "v_readfirstlane_b32 s60, v148\n\t"
    "v_fmac_f32 v145, v104, v28\n\t"
    "v_mfma_f32_16x16x32_bf16 v[116:119], v[168:171], v[208:211], v[116:119]\n\t"
    "v_fmac_f32 v144, v105, v29\n\t"
    "v_fmac_f32 v145, v106, v30\n\t"
    "ds_read_b128 v[164:167], v129 offset:0\n\t"
    "ds_read_b128 v[168:171], v129 offset:64\n\t"
    "v_mfma_f32_16x16x32_bf16 v[92:95], v[172:175], v[196:199], 0\n\t"
    "v_fmac_f32 v144, v107, v31\n\t"
    "v_fmac_f32 v145, v108, v32\n\t"
    "v_mfma_f32_16x16x32_bf16 v[92:95], v[176:179], v[200:203], v[92:95]\n\t"
    "v_fmac_f32 v144, v109, v33\n\t"
    "v_fmac_f32 v145, v110, v34\n\t"
    "s_waitcnt vmcnt(4)\n\t"
    "s_cmp_ge_u32 s55, s64\n\t"
    "s_cbranch_scc1 L_ahk11_%=\n\t"
    "L_ahb11_%=:\n\t"
    "v_mfma_f32_16x16x32_bf16 v[96:99], v[172:175], v[204:207], 0\n\t"
    "v_mad_u32_u24 v132, v132, s68, v151\n\t"
    "v_and_b32 v132, -16, v132\n\t"
    "v_mfma_f32_16x16x32_bf16 v[96:99], v[176:179], v[208:211], v[96:99]\n\t"
    "v_fmac_f32 v144, v111, v35\n\t"
    "v_fmac_f32 v145, v112, v87\n\t"
    "ds_read_b128 v[172:175], v130 offset:0\n\t"
    "ds_read_b128 v[176:179], v130 offset:64\n\t"
    "s_mul_i32 s66, s60, 400\n\t"
    "v_mfma_f32_16x16x32_bf16 v[100:103], v[212:215], v[188:191], 0\n\t"
    "v_fmac_f32 v144, v116, v36\n\t"
    "v_fmac_f32 v145, v117, v37\n\t"
    "v_mfma_f32_16x16x32_bf16 v[100:103], v[216:219], v[192:195], v[100:103]\n\t"
    "v_fmac_f32 v144, v118, v38\n\t"
    "v_fmac_f32 v145, v119, v39\n\t"
    "buffer_load_dwordx3 v[128:130], v153, %0, s66 offen\n\t"
    "v_mfma_f32_16x16x32_bf16 v[104:107], v[180:183], v[188:191], 0\n\t"
    "v_mad_u32_u24 v133, v133, s68, v151\n\t"
    "v_and_b32 v133, -16, v133\n\t"
    "v_mfma_f32_16x16x32_bf16 v[104:107], v[184:187], v[192:195], v[104:107]\n\t"
    "v_fmac_f32 v144, v92, v40\n\t"
    "v_fmac_f32 v145, v93, v41\n\t"
    "v_mfma_f32_16x16x32_bf16 v[108:111], v[180:183], v[196:199], 0\n\t"
    "v_fmac_f32 v144, v94, v42\n\t"
    "v_fmac_f32 v145, v95, v43\n\t"
    "v_mfma_f32_16x16x32_bf16 v[108:111], v[184:187], v[200:203], v[108:111]\n\t"
    "v_fmac_f32 v144, v96, v44\n\t"
    "v_fmac_f32 v145, v97, v45\n\t"
    "v_mfma_f32_16x16x32_bf16 v[112:115], v[180:183], v[204:207], 0\n\t"
    "v_fmac_f32 v144, v98, v46\n\t"
    "v_fmac_f32 v145, v99, v47\n\t"
    "v_mfma_f32_16x16x32_bf16 v[112:115], v[184:187], v[208:211], v[112:115]\n\t"
    "v_fmac_f32 v144, v100, v88\n\t"
    "v_mad_u32_u24 v134, v134, s68, v151\n\t"
    "ds_read_b128 v[180:183], v132 offset:0\n\t"
    "ds_read_b128 v[184:187], v132 offset:64\n\t"
    "v_mfma_f32_16x16x32_bf16 v[116:119], v[212:215], v[196:199], 0\n\t"
    "v_and_b32 v134, -16, v134\n\t"
    "v_fmac_f32 v145, v104, v48\n\t"
    "v_mfma_f32_16x16x32_bf16 v[116:119], v[216:219], v[200:203], v[116:119]\n\t"
    "v_fmac_f32 v144, v105, v49\n\t"
    "v_fmac_f32 v145, v106, v50\n\t"
    "v_mfma_f32_16x16x32_bf16 v[92:95], v[188:191], v[188:191], 0\n\t"
    "v_fmac_f32 v144, v107, v51\n\t"
    "v_mad_u32_u24 v135, v135, s68, v151\n\t"
    "v_mfma_f32_16x16x32_bf16 v[92:95], v[192:195], v[192:195], v[92:95]\n\t"
    "v_and_b32 v135, -16, v135\n\t"
    "v_fmac_f32 v145, v108, v52\n\t"
    "v_mfma_f32_16x16x32_bf16 v[96:99], v[188:191], v[196:199], 0\n\t"
    "v_fmac_f32 v144, v109, v53\n\t"
    "v_fmac_f32 v145, v110, v54\n\t"
    "v_mfma_f32_16x16x32_bf16 v[96:99], v[192:195], v[200:203], v[96:99]\n\t"
    "v_fmac_f32 v144, v111, v55\n\t"
    "v_fmac_f32 v145, v112, v56\n\t"
    "v_mfma_f32_16x16x32_bf16 v[100:103], v[188:191], v[204:207], 0\n\t"
    "v_fmac_f32 v144, v113, v57\n\t"
    "v_fmac_f32 v145, v114, v58\n\t"
    "v_mfma_f32_16x16x32_bf16 v[100:103], v[192:195], v[208:211], v[100:103]\n\t"
    "v_fmac_f32 v144, v115, v59\n\t"
    "v_fmac_f32 v145, v116, v89\n\t"
    "ds_read_b128 v[188:191], v133 offset:0\n\t"
    "ds_read_b128 v[192:195], v133 offset:64\n\t"
    "v_mfma_f32_16x16x32_bf16 v[104:107], v[212:215], v[204:207], 0\n\t"
    "v_fmac_f32 v144, v92, v60\n\t"
    "v_fmac_f32 v145, v93, v61\n\t"
    "v_mfma_f32_16x16x32_bf16 v[104:107], v[216:219], v[208:211], v[104:107]\n\t"
    "v_fmac_f32 v144, v94, v62\n\t"
    "v_fmac_f32 v145, v95, v63\n\t"
    "v_mfma_f32_16x16x32_bf16 v[108:111], v[196:199], v[196:199], 0\n\t"
    "v_fmac_f32 v144, v96, v64\n\t"
    "v_fmac_f32 v145, v97, v65\n\t"
    "v_mfma_f32_16x16x32_bf16 v[108:111], v[200:203], v[200:203], v[108:111]\n\t"
    "v_fmac_f32 v144, v98, v66\n\t"
    "v_fmac_f32 v145, v99, v67\n\t"
    "v_mfma_f32_16x16x32_bf16 v[112:115], v[196:199], v[204:207], 0\n\t"
    "v_fmac_f32 v144, v100, v68\n\t"
    "v_fmac_f32 v145, v101, v69\n\t"
    "v_mfma_f32_16x16x32_bf16 v[112:115], v[200:203], v[208:211], v[112:115]\n\t"
    "v_fmac_f32 v144, v102, v70\n\t"
    "v_fmac_f32 v145, v103, v71\n\t"
    "ds_read_b128 v[196:199], v134 offset:0\n\t"
    "ds_read_b128 v[200:203], v134 offset:64\n\t"
    "v_mfma_f32_16x16x32_bf16 v[116:119], v[204:207], v[204:207], 0\n\t"
    "v_fmac_f32 v144, v104, v90\n\t"
    "v_mfma_f32_16x16x32_bf16 v[116:119], v[208:211], v[208:211], v[116:119]\n\t"
    "ds_read_b128 v[204:207], v135 offset:0\n\t"
    "ds_read_b128 v[208:211], v135 offset:64\n\t"
    "s_mov_b32 s52, s53\n\t"
    "s_mov_b32 s53, s54\n\t"
    "s_mov_b32 s54, s55\n\t"
    "s_mov_b32 s55, s60\n\t"
    "s_cmp_lt_u32 s53, s64\n\t"
    "s_cbranch_scc1 L_apnn_%=\n\t"
    "L_adone_%=:\n\t"
    "s_mul_i32 s69, s52, 256\n\t"
    "s_add_u32 s69, s69, s50\n\t"
    "v_fmac_f32 v145, v108, v72\n\t"
    "v_fmac_f32 v144, v109, v73\n\t"
    "v_fmac_f32 v145, v110, v74\n\t"
    "v_fmac_f32 v144, v111, v75\n\t"
    "v_fmac_f32 v145, v112, v76\n\t"
    "v_fmac_f32 v144, v113, v77\n\t"
    "v_fmac_f32 v145, v114, v78\n\t"
    "v_fmac_f32 v144, v115, v79\n\t"
    "v_fmac_f32 v145, v116, v80\n\t"
    "v_fmac_f32 v144, v117, v81\n\t"
    "v_fmac_f32 v145, v118, v82\n\t"
    "v_fmac_f32 v144, v119, v83\n\t"
    "v_add_f32 v40, v144, v145\n\t"
    "L_aend_%=:\n\t"
    "s_branch L_aend2_%=\n\t"
    "L_ahk00_%=:\n\t"
    "v_mov_b32 v147, 3112\n\t"
    "s_cmp_lt_u32 s54, s64\n\t"
    "ds_read_b32 v147, v147\n\t"
    "s_cbranch_scc1 L_ahb00_%=\n\t"
    "s_waitcnt vmcnt(3)\n\t"
    "v_add_u32 v137, 0, v155\n\t"
    "v_add_u32 v138, 16, v155\n\t"
    "v_add_u32 v140, 32, v155\n\t"
    "v_add_u32 v141, 48, v155\n\t"
    "v_add_u32 v142, 64, v155\n\t"
    "v_add_u32 v143, 80, v155\n\t"
    "s_branch L_ahb00_%=\n\t"
    "L_ahk10_%=:\n\t"
    "s_waitcnt lgkmcnt(4)\n\t"
    "v_readfirstlane_b32 s65, v147\n\t"
    "s_nop 0\n\t"
    "s_cmp_eq_u32 s65, 0\n\t"
    "s_cbranch_scc1 L_ahb10_%=\n\t"
    "s_cmp_lt_u32 s62, 2\n\t"
    "s_cbranch_scc1 L_mel1_%=\n\t"
    "buffer_load_dwordx4 v[220:223], v127, %12, s61 offen offset:2048 sc1\n\t"
    "buffer_load_dwordx4 v[224:227], v127, %12, s61 offen offset:3072 sc1\n\t"
    "s_add_u32 s61, s61, 0x1000\n\t"
    "buffer_load_dwordx4 v[228:231], v127, %12, s61 offen offset:2048 sc1\n\t"
    "buffer_load_dwordx4 v[232:235], v127, %12, s61 offen offset:3072 sc1\n\t"
    "s_add_u32 s61, s61, 0x1000\n\t"
    "buffer_load_dwordx4 v[236:239], v127, %12, s61 offen offset:2048 sc1\n\t"
    "buffer_load_dwordx4 v[240:243], v127, %12, s61 offen offset:3072 sc1\n\t"
    "s_add_u32 s61, s61, 0x1000\n\t"
    "buffer_load_dwordx4 v[244:247], v127, %12, s61 offen offset:2048 sc1\n\t"
    "buffer_load_dwordx4 v[248:251], v127, %12, s61 offen offset:3072 sc1\n\t"
    "s_add_u32 s61, s61, 0x1000\n\t"
    "buffer_load_dwordx4 v[252:255], v127, %12, s61 offen offset:2048 sc1\n\t"
    "s_mov_b32 s62, 1\n\t"
    "L_mel1_%=:\n\t"
    "s_cmp_lt_u32 s54, s64\n\t"
    "s_cbranch_scc1 L_ahb10_%=\n\t"
    "buffer_load_dwordx4 v[0:3], v127, %12, s63 offen offset:0 sc1\n\t"
    "buffer_load_dwordx4 v[4:7], v127, %12, s63 offen offset:1024 sc1\n\t"
    "buffer_load_dwordx4 v[8:11], v127, %12, s63 offen offset:2048 sc1\n\t"
    "buffer_load_dwordx4 v[12:15], v127, %12, s63 offen offset:3072 sc1\n\t"
    "s_add_u32 s63, s63, 0x1000\n\t"
    "s_mov_b32 s62, 0\n\t"
    "s_branch L_ahb10_%=\n\t"
    "L_ahk01_%=:\n\t"
    "v_mov_b32 v147, 3112\n\t"
    "s_cmp_lt_u32 s54, s64\n\t"
    "ds_read_b32 v147, v147\n\t"
    "s_cbranch_scc1 L_ahb01_%=\n\t"
    "s_waitcnt vmcnt(3)\n\t"
    "v_add_u32 v129, 0, v155\n\t"
    "v_add_u32 v130, 16, v155\n\t"
    "v_add_u32 v132, 32, v155\n\t"
    "v_add_u32 v133, 48, v155\n\t"
    "v_add_u32 v134, 64, v155\n\t"
    "v_add_u32 v135, 80, v155\n\t"
    "s_branch L_ahb01_%=\n\t"
    "L_ahk11_%=:\n\t"
    "s_waitcnt lgkmcnt(4)\n\t"
    "v_readfirstlane_b32 s65, v147\n\t"
    "s_nop 0\n\t"
    "s_cmp_eq_u32 s65, 0\n\t"
    "s_cbranch_scc1 L_ahb11_%=\n\t"
    "s_cmp_lt_u32 s62, 2\n\t"
    "s_cbranch_scc1 L_mel3_%=\n\t"
    "buffer_load_dwordx4 v[220:223], v127, %12, s61 offen offset:2048 sc1\n\t"
    "buffer_load_dwordx4 v[224:227], v127, %12, s61 offen offset:3072 sc1\n\t"
    "s_add_u32 s61, s61, 0x1000\n\t"
    "buffer_load_dwordx4 v[228:231], v127, %12, s61 offen offset:2048 sc1\n\t"
    "buffer_load_dwordx4 v[232:235], v127, %12, s61 offen offset:3072 sc1\n\t"
    "s_add_u32 s61, s61, 0x1000\n\t"
    "buffer_load_dwordx4 v[236:239], v127, %12, s61 offen offset:2048 sc1\n\t"
    "buffer_load_dwordx4 v[240:243], v127, %12, s61 offen offset:3072 sc1\n\t"
    "s_add_u32 s61, s61, 0x1000\n\t"
    "buffer_load_dwordx4 v[244:247], v127, %12, s61 offen offset:2048 sc1\n\t"
    "buffer_load_dwordx4 v[248:251], v127, %12, s61 offen offset:3072 sc1\n\t"
    "s_add_u32 s61, s61, 0x1000\n\t"
    "buffer_load_dwordx4 v[252:255], v127, %12, s61 offen offset:2048 sc1\n\t"
    "s_mov_b32 s62, 1\n\t"
    "L_mel3_%=:\n\t"
    "s_cmp_lt_u32 s54, s64\n\t"
    "s_cbranch_scc1 L_ahb11_%=\n\t"
    "buffer_load_dwordx4 v[0:3], v127, %12, s63 offen offset:0 sc1\n\t"
    "buffer_load_dwordx4 v[4:7], v127, %12, s63 offen offset:1024 sc1\n\t"
    "buffer_load_dwordx4 v[8:11], v127, %12, s63 offen offset:2048 sc1\n\t"
    "buffer_load_dwordx4 v[12:15], v127, %12, s63 offen offset:3072 sc1\n\t"
    "s_add_u32 s63, s63, 0x1000\n\t"
    "s_mov_b32 s62, 0\n\t"
    "s_branch L_ahb11_%=\n\t"
    "L_aend2_%=:\n\t"
    "s_setprio 0\n\t"
    "v_lshlrev_b32 v113, 16, v127\n\t"
    "v_add_u32 v113, 3108, v113\n\t"
    "s_cmp_eq_u32 s62, 0\n\t"
    "s_cbranch_scc1 L_mpf_%=\n\t"
    "L_ml0_%=:\n\t"
    "ds_read_b32 v114, v113 offset:4\n\t"
    "s_waitcnt lgkmcnt(0)\n\t"
    "v_readfirstlane_b32 s65, v114\n\t"
    "s_nop 0\n\t"
    "s_cmp_lg_u32 s65, 0\n\t"
    "s_cbranch_scc1 L_mm0_%=\n\t"
    "s_sleep 2\n\t"
    "s_branch L_ml0_%=\n\t"
    "L_mm0_%=:\n\t"
    "s_cmp_lt_u32 s62, 2\n\t"
    "s_cbranch_scc1 L_mel5_%=\n\t"
    "buffer_load_dwordx4 v[220:223], v127, %12, s61 offen offset:2048 sc1\n\t"
    "buffer_load_dwordx4 v[224:227], v127, %12, s61 offen offset:3072 sc1\n\t"
    "s_add_u32 s61, s61, 0x1000\n\t"
    "buffer_load_dwordx4 v[228:231], v127, %12, s61 offen offset:2048 sc1\n\t"
    "buffer_load_dwordx4 v[232:235], v127, %12, s61 offen offset:3072 sc1\n\t"
    "s_add_u32 s61, s61, 0x1000\n\t"
    "buffer_load_dwordx4 v[236:239], v127, %12, s61 offen offset:2048 sc1\n\t"
    "buffer_load_dwordx4 v[240:243], v127, %12, s61 offen offset:3072 sc1\n\t"
    "s_add_u32 s61, s61, 0x1000\n\t"
    "buffer_load_dwordx4 v[244:247], v127, %12, s61 offen offset:2048 sc1\n\t"
    "buffer_load_dwordx4 v[248:251], v127, %12, s61 offen offset:3072 sc1\n\t"
    "s_add_u32 s61, s61, 0x1000\n\t"
    "buffer_load_dwordx4 v[252:255], v127, %12, s61 offen offset:2048 sc1\n\t"
    "s_mov_b32 s62, 1\n\t"
    "L_mel5_%=:\n\t"
    "buffer_load_dwordx4 v[0:3], v127, %12, s63 offen offset:0 sc1\n\t"
    "buffer_load_dwordx4 v[4:7], v127, %12, s63 offen offset:1024 sc1\n\t"
    "buffer_load_dwordx4 v[8:11], v127, %12, s63 offen offset:2048 sc1\n\t"
    "buffer_load_dwordx4 v[12:15], v127, %12, s63 offen offset:3072 sc1\n\t"
    "s_add_u32 s63, s63, 0x1000\n\t"
    "s_mov_b32 s62, 0\n\t"
    "L_mpf_%=:\n\t"
    "v_lshrrev_b32 v105, 4, v127\n\t"
    "v_and_b32 v105, 48, v105\n\t"
    "s_movk_i32 s65, 130\n\t"
    "v_mad_u32_u24 v104, v155, s65, v105\n\t"
    "v_add_u32 v105, 14170, v104\n\t"
    "v_and_b32 v104, -16, v104\n\t"
    "v_and_b32 v105, -16, v105\n\t"
    "buffer_load_dwordx4 v[128:131], v127, %12, s61 offen offset:3072 sc1\n\t"
    "s_add_u32 s61, s61, 0x1000\n\t"
    "buffer_load_dwordx4 v[132:135], v127, %12, s61 offen offset:2048 sc1\n\t"
    "ds_read_b128 v[156:159], v104 offset:12480\n\t"
    "ds_read_b128 v[160:163], v104 offset:12544\n\t"
    "buffer_load_dwordx4 v[136:139], v127, %12, s61 offen offset:3072 sc1\n\t"
    "s_add_u32 s61, s61, 0x1000\n\t"
    "buffer_load_dwordx4 v[140:143], v127, %12, s61 offen offset:2048 sc1\n\t"
    "ds_read_b128 v[212:215], v105 offset:0\n\t"
    "ds_read_b128 v[216:219], v105 offset:64\n\t"
    "buffer_load_dwordx4 v[144:147], v127, %12, s61 offen offset:3072 sc1\n\t"
    "s_add_u32 s61, s61, 0x1000\n\t"
    "buffer_load_dwordx4 v[148:151], v127, %12, s61 offen offset:2048 sc1\n\t"
    "buffer_load_dwordx4 v[152:155], v127, %12, s61 offen offset:3072 sc1\n\t"
    "s_add_u32 s61, s61, 0x1000\n\t"
    "v_mov_b32 v114, 1\n\t"
    "ds_add_u32 v113, v114\n\t"
    "s_waitcnt lgkmcnt(5)\n\t"
    "s_waitcnt vmcnt(7)\n\t"
    "v_mfma_f32_16x16x32_bf16 v[16:19], v[0:3], v[164:167], 0\n\t"
    "v_mfma_f32_16x16x32_bf16 v[16:19], v[4:7], v[168:171], v[16:19]\n\t"
    "v_mfma_f32_16x16x32_bf16 v[16:19], v[8:11], v[220:223], v[16:19]\n\t"
    "v_mfma_f32_16x16x32_bf16 v[16:19], v[12:15], v[224:227], v[16:19]\n\t"
    "v_mfma_f32_16x16x32_bf16 v[20:23], v[0:3], v[172:175], 0\n\t"
    "v_mfma_f32_16x16x32_bf16 v[20:23], v[4:7], v[176:179], v[20:23]\n\t"
    "v_mfma_f32_16x16x32_bf16 v[20:23], v[8:11], v[228:231], v[20:23]\n\t"
    "v_mfma_f32_16x16x32_bf16 v[20:23], v[12:15], v[232:235], v[20:23]\n\t"
    "v_mfma_f32_16x16x32_bf16 v[80:83], v[0:3], v[180:183], 0\n\t"
    "v_mfma_f32_16x16x32_bf16 v[80:83], v[4:7], v[184:187], v[80:83]\n\t"
    "v_mfma_f32_16x16x32_bf16 v[80:83], v[8:11], v[236:239], v[80:83]\n\t"
    "v_mfma_f32_16x16x32_bf16 v[80:83], v[12:15], v[240:243], v[80:83]\n\t"
    "v_cvt_pk_bf16_f32 v24, v16, v17\n\t"
    "v_cvt_pk_bf16_f32 v25, v18, v19\n\t"
    "v_mfma_f32_16x16x32_bf16 v[16:19], v[0:3], v[188:191], 0\n\t"
    "v_mfma_f32_16x16x32_bf16 v[16:19], v[4:7], v[192:195], v[16:19]\n\t"
    "v_mfma_f32_16x16x32_bf16 v[16:19], v[8:11], v[244:247], v[16:19]\n\t"
    "v_mfma_f32_16x16x32_bf16 v[16:19], v[12:15], v[248:251], v[16:19]\n\t"
    "v_cvt_pk_bf16_f32 v26, v20, v21\n\t"
    "v_cvt_pk_bf16_f32 v27, v22, v23\n\t"
    "s_waitcnt vmcnt(6)\n\t"
    "v_mfma_f32_16x16x32_bf16 v[20:23], v[0:3], v[196:199], 0\n\t"
    "v_mfma_f32_16x16x32_bf16 v[20:23], v[4:7], v[200:203], v[20:23]\n\t"
    "v_mfma_f32_16x16x32_bf16 v[20:23], v[8:11], v[252:255], v[20:23]\n\t"
    "v_mfma_f32_16x16x32_bf16 v[20:23], v[12:15], v[128:131], v[20:23]\n\t"
    "v_cvt_pk_bf16_f32 v28, v80, v81\n\t"
    "v_cvt_pk_bf16_f32 v29, v82, v83\n\t"
    "s_waitcnt lgkmcnt(0)\n\t"
    "s_waitcnt vmcnt(4)\n\t"
    "v_mfma_f32_16x16x32_bf16 v[80:83], v[0:3], v[204:207], 0\n\t"
    "v_mfma_f32_16x16x32_bf16 v[80:83], v[4:7], v[208:211], v[80:83]\n\t"
    "v_mfma_f32_16x16x32_bf16 v[80:83], v[8:11], v[132:135], v[80:83]\n\t"
    "v_mfma_f32_16x16x32_bf16 v[80:83], v[12:15], v[136:139], v[80:83]\n\t"
    "v_cvt_pk_bf16_f32 v30, v16, v17\n\t"
    "v_cvt_pk_bf16_f32 v31, v18, v19\n\t"
    "s_waitcnt vmcnt(2)\n\t"
    "v_mfma_f32_16x16x32_bf16 v[16:19], v[0:3], v[156:159], 0\n\t"
    "buffer_load_dwordx4 v[100:103], v127, %12, s84 offen offset:0 sc1\n\t"
    "s_add_u32 s84, s84, s97\n\t"
    "v_mfma_f32_16x16x32_bf16 v[16:19], v[4:7], v[160:163], v[16:19]\n\t"
    "buffer_load_dwordx4 v[104:107], v127, %12, s85 offen offset:0 sc1\n\t"
    "s_add_u32 s85, s85, s97\n\t"
    "v_mfma_f32_16x16x32_bf16 v[16:19], v[8:11], v[140:143], v[16:19]\n\t"
    "v_mfma_f32_16x16x32_bf16 v[16:19], v[12:15], v[144:147], v[16:19]\n\t"
    "v_cvt_pk_bf16_f32 v32, v20, v21\n\t"
    "v_cvt_pk_bf16_f32 v33, v22, v23\n\t"
    "s_waitcnt vmcnt(2)\n\t"
    "v_mfma_f32_16x16x32_bf16 v[20:23], v[0:3], v[212:215], 0\n\t"
    "buffer_load_dwordx4 v[108:111], v127, %12, s86 offen offset:0 sc1\n\t"
    "s_add_u32 s86, s86, s97\n\t"
    "v_mfma_f32_16x16x32_bf16 v[20:23], v[4:7], v[216:219], v[20:23]\n\t"
    "buffer_load_dwordx4 v[112:115], v127, %12, s87 offen offset:0 sc1\n\t"
    "s_add_u32 s87, s87, s97\n\t"
    "v_mfma_f32_16x16x32_bf16 v[20:23], v[8:11], v[148:151], v[20:23]\n\t"
    "v_mfma_f32_16x16x32_bf16 v[20:23], v[12:15], v[152:155], v[20:23]\n\t"
    "v_cvt_pk_bf16_f32 v34, v80, v81\n\t"
    "v_cvt_pk_bf16_f32 v35, v82, v83\n\t"
    "v_cvt_pk_bf16_f32 v36, v16, v17\n\t"
    "v_cvt_pk_bf16_f32 v37, v18, v19\n\t"
    "s_nop 3\n\t"
    "v_cvt_pk_bf16_f32 v38, v20, v21\n\t"
    "v_cvt_pk_bf16_f32 v39, v22, v23\n\t"
    "v_lshrrev_b32 v41, 2, v127\n\t"
    "s_and_b32 s65, %14, 8\n\t"
    "s_lshl_b32 s65, s65, 10\n\t"
    "s_add_u32 s63, s63, s65\n\t"
    "buffer_store_dword v40, v41, %12, s69 offen sc1\n\t"
    "v_mov_b32 v82, 3108\n\t"
    "s_cmp_eq_u32 %16, 0\n\t"
    "s_cselect_b32 s47, 0x1ff, 0\n\t"
    "L_my_%=:\n\t"
    "ds_read_b32 v81, v82\n\t"
    "s_waitcnt lgkmcnt(0)\n\t"
    "v_cmp_gt_u32 vcc, 8, v81\n\t"
    "s_cbranch_vccz L_mz_%=\n\t"
    "s_sleep 2\n\t"
    "s_branch L_my_%=\n\t"
    "L_mz_%=:\n\t"
    "ds_write_b64 v125, v[24:25] offset:0\n\t"
    "v_mov_b64 v[0:1], 0\n\t"
    "ds_write_b64 v125, v[26:27] offset:128\n\t"
    "v_mov_b64 v[2:3], 0\n\t"
    "ds_write_b64 v125, v[28:29] offset:256\n\t"
    "v_mov_b64 v[4:5], 0\n\t"
    "ds_write_b64 v125, v[30:31] offset:384\n\t"
    "v_mov_b64 v[6:7], 0\n\t"
    "ds_write_b64 v125, v[32:33] offset:512\n\t"
    "ds_write_b64 v125, v[34:35] offset:640\n\t"
    "ds_write_b64 v125, v[36:37] offset:768\n\t"
    "ds_write_b64 v125, v[38:39] offset:872\n\t"
    "v_add_u32 v125, s93, v125\n\t"
    "s_sub_u32 s93, 0, s93\n\t"
    "s_waitcnt lgkmcnt(0)\n\t"
    "s_barrier\n\t"
    "s_memrealtime s[44:45]\n\t"
    "s_waitcnt lgkmcnt(0)\n\t"
    "s_waitcnt vmcnt(4)\n\t"
    "v_add_u32_sdwa v126, s92, v100 dst_sel:DWORD dst_unused:UNUSED_PAD src0_sel:DWORD src1_sel:WORD_1\n\t"
    "v_add_u32_sdwa v100, s92, v100 dst_sel:DWORD dst_unused:UNUSED_PAD src0_sel:DWORD src1_sel:WORD_0\n\t"
    "ds_read_b64 v[84:85], v100 offset:0\n\t"
    "ds_read_b64 v[86:87], v126 offset:0\n\t"
    "v_add_u32_sdwa v90, s92, v101 dst_sel:DWORD dst_unused:UNUSED_PAD src0_sel:DWORD src1_sel:WORD_1\n\t"
    "v_add_u32_sdwa v101, s92, v101 dst_sel:DWORD dst_unused:UNUSED_PAD src0_sel:DWORD src1_sel:WORD_0\n\t"
    "ds_read_b64 v[88:89], v101 offset:0\n\t"
    "ds_read_b64 v[90:91], v90 offset:0\n\t"
    "v_add_u32 v63, s96, v127\n\t"
    "s_add_u32 s96, s96, 0x1000\n\t"
    "s_and_b32 s96, s96, 0x3000\n\t"
    "v_add_u32 v63, 128000, v63\n\t"
    "ds_read_b128 v[48:51], v63 offset:0\n\t"
    "ds_read_b128 v[52:55], v63 offset:1024\n\t"
    "ds_read_b128 v[56:59], v63 offset:2048\n\t"
    "ds_read_b128 v[60:63], v63 offset:3072\n\t"
    "v_add_u32_sdwa v126, s92, v102 dst_sel:DWORD dst_unused:UNUSED_PAD src0_sel:DWORD src1_sel:WORD_1\n\t"
    "v_add_u32_sdwa v102, s92, v102 dst_sel:DWORD dst_unused:UNUSED_PAD src0_sel:DWORD src1_sel:WORD_0\n\t"
    "ds_read_b64 v[92:93], v102 offset:0\n\t"
    "ds_read_b64 v[94:95], v126 offset:0\n\t"
    "v_add_u32_sdwa v98, s92, v103 dst_sel:DWORD dst_unused:UNUSED_PAD src0_sel:DWORD src1_sel:WORD_1\n\t"
    "v_add_u32_sdwa v103, s92, v103 dst_sel:DWORD dst_unused:UNUSED_PAD src0_sel:DWORD src1_sel:WORD_0\n\t"
    "ds_read_b64 v[96:97], v103 offset:0\n\t"
    "ds_read_b64 v[98:99], v98 offset:0\n\t"
    "buffer_load_dwordx4 v[100:103], v127, %12, s88 offen offset:0 sc1\n\t"
    "s_add_u32 s88, s88, s97\n\t"
    "s_waitcnt vmcnt(4)\n\t"
    "v_add_u32_sdwa v126, s92, v104 dst_sel:DWORD dst_unused:UNUSED_PAD src0_sel:DWORD src1_sel:WORD_1\n\t"
    "v_add_u32_sdwa v104, s92, v104 dst_sel:DWORD dst_unused:UNUSED_PAD src0_sel:DWORD src1_sel:WORD_0\n\t"
    "v_mov_b64 v[8:9], 0\n\t"
    "v_mov_b64 v[10:11], 0\n\t"
    "v_mov_b64 v[12:13], 0\n\t"
    "v_mov_b64 v[14:15], 0\n\t"
    "v_mov_b64 v[16:17], 0\n\t"
    "v_mov_b64 v[18:19], 0\n\t"
    "s_waitcnt lgkmcnt(8)\n\t"
    "v_smfmac_f32_16x16x64_bf16 v[0:3], v[120:123], v[84:91], v124\n\t"
    "ds_read_b64 v[84:85], v104 offset:0\n\t"
    "s_waitcnt lgkmcnt(5)\n\t"
    "v_mfma_f32_16x16x32_bf16 v[116:119], v[48:51], v[164:167], 0\n\t"
    "ds_read_b64 v[86:87], v126 offset:0\n\t"
    "v_mfma_f32_16x16x32_bf16 v[116:119], v[52:55], v[168:171], v[116:119]\n\t"
    "v_add_u32_sdwa v90, s92, v105 dst_sel:DWORD dst_unused:UNUSED_PAD src0_sel:DWORD src1_sel:WORD_1\n\t"
    "v_add_u32_sdwa v105, s92, v105 dst_sel:DWORD dst_unused:UNUSED_PAD src0_sel:DWORD src1_sel:WORD_0\n\t"
    "ds_read_b64 v[88:89], v105 offset:0\n\t"
    "v_mfma_f32_16x16x32_bf16 v[116:119], v[56:59], v[220:223], v[116:119]\n\t"
    "ds_read_b64 v[90:91], v90 offset:0\n\t"
    "v_mfma_f32_16x16x32_bf16 v[116:119], v[60:63], v[224:227], v[116:119]\n\t"
    "v_add_u32_sdwa v126, s92, v106 dst_sel:DWORD dst_unused:UNUSED_PAD src0_sel:DWORD src1_sel:WORD_1\n\t"
    "v_add_u32_sdwa v106, s92, v106 dst_sel:DWORD dst_unused:UNUSED_PAD src0_sel:DWORD src1_sel:WORD_0\n\t"
    "v_mfma_f32_16x16x32_bf16 v[80:83], v[48:51], v[172:175], 0\n\t"
    "v_mov_b64 v[20:21], 0\n\t"
    "v_mov_b64 v[22:23], 0\n\t"
    "v_mov_b64 v[24:25], 0\n\t"
    "v_mov_b64 v[26:27], 0\n\t"
    "v_mov_b64 v[28:29], 0\n\t"
    "v_mov_b64 v[30:31], 0\n\t"
    "s_waitcnt vmcnt(3) lgkmcnt(4)\n\t"
    "v_smfmac_f32_16x16x64_bf16 v[4:7], v[120:123], v[92:99], v124\n\t"
    "ds_read_b64 v[92:93], v106 offset:0\n\t"
    "v_mfma_f32_16x16x32_bf16 v[80:83], v[52:55], v[176:179], v[80:83]\n\t"
    "ds_read_b64 v[94:95], v126 offset:0\n\t"
    "v_mfma_f32_16x16x32_bf16 v[80:83], v[56:59], v[228:231], v[80:83]\n\t"
    "v_add_u32_sdwa v98, s92, v107 dst_sel:DWORD dst_unused:UNUSED_PAD src0_sel:DWORD src1_sel:WORD_1\n\t"
    "v_add_u32_sdwa v107, s92, v107 dst_sel:DWORD dst_unused:UNUSED_PAD src0_sel:DWORD src1_sel:WORD_0\n\t"
    "ds_read_b64 v[96:97], v107 offset:0\n\t"
    "v_mfma_f32_16x16x32_bf16 v[80:83], v[60:63], v[232:235], v[80:83]\n\t"
    "ds_read_b64 v[98:99], v98 offset:0\n\t"
    "v_cvt_pk_bf16_f32 v116, v116, v117\n\t"
    "v_cvt_pk_bf16_f32 v117, v118, v119\n\t"
    "ds_write_b64 v125, v[116:117] offset:0\n\t"
    "v_mfma_f32_16x16x32_bf16 v[116:119], v[48:51], v[180:183], 0\n\t"
    "buffer_load_dwordx4 v[104:107], v127, %12, s89 offen offset:0 sc1\n\t"
    "s_add_u32 s89, s89, s97\n\t"
    "v_add_u32_sdwa v126, s92, v108 dst_sel:DWORD dst_unused:UNUSED_PAD src0_sel:DWORD src1_sel:WORD_1\n\t"
    "v_add_u32_sdwa v108, s92, v108 dst_sel:DWORD dst_unused:UNUSED_PAD src0_sel:DWORD src1_sel:WORD_0\n\t"
    "v_mfma_f32_16x16x32_bf16 v[116:119], v[52:55], v[184:187], v[116:119]\n\t"
    "v_mov_b64 v[32:33], 0\n\t"
    "v_mov_b64 v[34:35], 0\n\t"
    "v_mov_b64 v[36:37], 0\n\t"
    "v_mov_b64 v[38:39], 0\n\t"
    "v_mov_b64 v[40:41], 0\n\t"
    "v_mov_b64 v[42:43], 0\n\t"
    "v_mov_b64 v[44:45], 0\n\t"
    "v_mov_b64 v[46:47], 0\n\t"
    "s_waitcnt lgkmcnt(5)\n\t"
    "v_smfmac_f32_16x16x64_bf16 v[8:11], v[120:123], v[84:91], v124\n\t"
    "ds_read_b64 v[84:85], v108 offset:0\n\t"
    "v_mfma_f32_16x16x32_bf16 v[116:119], v[56:59], v[236:239], v[116:119]\n\t"
    "ds_read_b64 v[86:87], v126 offset:0\n\t"
    "v_mfma_f32_16x16x32_bf16 v[116:119], v[60:63], v[240:243], v[116:119]\n\t"
    "v_add_u32_sdwa v90, s92, v109 dst_sel:DWORD dst_unused:UNUSED_PAD src0_sel:DWORD src1_sel:WORD_1\n\t"
    "v_add_u32_sdwa v109, s92, v109 dst_sel:DWORD dst_unused:UNUSED_PAD src0_sel:DWORD src1_sel:WORD_0\n\t"
    "ds_read_b64 v[88:89], v109 offset:0\n\t"
    "v_cvt_pk_bf16_f32 v80, v80, v81\n\t"
    "v_cvt_pk_bf16_f32 v81, v82, v83\n\t"
    "ds_write_b64 v125, v[80:81] offset:128\n\t"
    "v_mfma_f32_16x16x32_bf16 v[80:83], v[48:51], v[188:191], 0\n\t"
    "ds_read_b64 v[90:91], v90 offset:0\n\t"
    "v_mfma_f32_16x16x32_bf16 v[80:83], v[52:55], v[192:195], v[80:83]\n\t"
    "v_add_u32_sdwa v126, s92, v110 dst_sel:DWORD dst_unused:UNUSED_PAD src0_sel:DWORD src1_sel:WORD_1\n\t"
    "v_add_u32_sdwa v110, s92, v110 dst_sel:DWORD dst_unused:UNUSED_PAD src0_sel:DWORD src1_sel:WORD_0\n\t"
    "v_mfma_f32_16x16x32_bf16 v[80:83], v[56:59], v[244:247], v[80:83]\n\t"
    "s_waitcnt vmcnt(3) lgkmcnt(6)\n\t"
    "v_smfmac_f32_16x16x64_bf16 v[12:15], v[120:123], v[92:99], v124\n\t"
    "ds_read_b64 v[92:93], v110 offset:0\n\t"
    "v_mfma_f32_16x16x32_bf16 v[80:83], v[60:63], v[248:251], v[80:83]\n\t"
    "ds_read_b64 v[94:95], v126 offset:0\n\t"
    "v_cvt_pk_bf16_f32 v116, v116, v117\n\t"
    "v_cvt_pk_bf16_f32 v117, v118, v119\n\t"
    "ds_write_b64 v125, v[116:117] offset:256\n\t"
    "v_mfma_f32_16x16x32_bf16 v[116:119], v[48:51], v[196:199], 0\n\t"
    "v_add_u32_sdwa v98, s92, v111 dst_sel:DWORD dst_unused:UNUSED_PAD src0_sel:DWORD src1_sel:WORD_1\n\t"
    "v_add_u32_sdwa v111, s92, v111 dst_sel:DWORD dst_unused:UNUSED_PAD src0_sel:DWORD src1_sel:WORD_0\n\t"
    "ds_read_b64 v[96:97], v111 offset:0\n\t"
    "v_mfma_f32_16x16x32_bf16 v[116:119], v[52:55], v[200:203], v[116:119]\n\t"
    "ds_read_b64 v[98:99], v98 offset:0\n\t"
    "v_mfma_f32_16x16x32_bf16 v[116:119], v[56:59], v[252:255], v[116:119]\n\t"
    "buffer_load_dwordx4 v[108:111], v127, %12, s90 offen offset:0 sc1\n\t"
    "s_add_u32 s90, s90, s97\n\t"
    "v_add_u32_sdwa v126, s92, v112 dst_sel:DWORD dst_unused:UNUSED_PAD src0_sel:DWORD src1_sel:WORD_1\n\t"
    "v_add_u32_sdwa v112, s92, v112 dst_sel:DWORD dst_unused:UNUSED_PAD src0_sel:DWORD src1_sel:WORD_0\n\t"
    "v_mfma_f32_16x16x32_bf16 v[116:119], v[60:63], v[128:131], v[116:119]\n\t"
    "v_cvt_pk_bf16_f32 v0, v0, v1\n\t"
    "v_cvt_pk_bf16_f32 v1, v2, v3\n\t"
    "v_cvt_pk_bf16_f32 v80, v80, v81\n\t"
    "v_cvt_pk_bf16_f32 v81, v82, v83\n\t"
    "ds_write_b64 v125, v[80:81] offset:384\n\t"
    "v_add_u32 v79, s96, v127\n\t"
    "s_add_u32 s96, s96, 0x1000\n\t"
    "s_and_b32 s96, s96, 0x3000\n\t"
    "v_add_u32 v79, 128000, v79\n\t"
    "ds_read_b128 v[64:67], v79 offset:0\n\t"
    "ds_read_b128 v[68:71], v79 offset:1024\n\t"
    "ds_read_b128 v[72:75], v79 offset:2048\n\t"
    "ds_read_b128 v[76:79], v79 offset:3072\n\t"
    "s_waitcnt lgkmcnt(10)\n\t"
    "v_smfmac_f32_16x16x64_bf16 v[16:19], v[120:123], v[84:91], v124\n\t"
    "ds_read_b64 v[84:85], v112 offset:0\n\t"
    "v_mfma_f32_16x16x32_bf16 v[80:83], v[48:51], v[204:207], 0\n\t"
    "ds_read_b64 v[86:87], v126 offset:0\n\t"
    "v_mfma_f32_16x16x32_bf16 v[80:83], v[52:55], v[208:211], v[80:83]\n\t"
    "v_add_u32_sdwa v90, s92, v113 dst_sel:DWORD dst_unused:UNUSED_PAD src0_sel:DWORD src1_sel:WORD_1\n\t"
    "v_add_u32_sdwa v113, s92, v113 dst_sel:DWORD dst_unused:UNUSED_PAD src0_sel:DWORD src1_sel:WORD_0\n\t"
    "ds_read_b64 v[88:89], v113 offset:0\n\t"
    "v_mfma_f32_16x16x32_bf16 v[80:83], v[56:59], v[132:135], v[80:83]\n\t"
    "ds_read_b64 v[90:91], v90 offset:0\n\t"
    "v_mfma_f32_16x16x32_bf16 v[80:83], v[60:63], v[136:139], v[80:83]\n\t"
    "v_add_u32_sdwa v126, s92, v114 dst_sel:DWORD dst_unused:UNUSED_PAD src0_sel:DWORD src1_sel:WORD_1\n\t"
    "v_add_u32_sdwa v114, s92, v114 dst_sel:DWORD dst_unused:UNUSED_PAD src0_sel:DWORD src1_sel:WORD_0\n\t"
    "v_cvt_pk_bf16_f32 v116, v116, v117\n\t"
    "v_cvt_pk_bf16_f32 v117, v118, v119\n\t"
    "ds_write_b64 v125, v[116:117] offset:512\n\t"
    "v_mfma_f32_16x16x32_bf16 v[116:119], v[48:51], v[156:159], 0\n\t"
    "v_cvt_pk_bf16_f32 v2, v4, v5\n\t"
    "v_cvt_pk_bf16_f32 v3, v6, v7\n\t"
    "v_mov_b64 v[4:5], 0\n\t"
    "v_mov_b64 v[6:7], 0\n\t"
    "s_waitcnt vmcnt(2) lgkmcnt(10)\n\t"
    "v_smfmac_f32_16x16x64_bf16 v[20:23], v[120:123], v[92:99], v124\n\t"
    "ds_read_b64 v[92:93], v114 offset:0\n\t"
    "v_mfma_f32_16x16x32_bf16 v[116:119], v[52:55], v[160:163], v[116:119]\n\t"
    "ds_read_b64 v[94:95], v126 offset:0\n\t"
    "v_mfma_f32_16x16x32_bf16 v[116:119], v[56:59], v[140:143], v[116:119]\n\t"
    "v_add_u32_sdwa v98, s92, v115 dst_sel:DWORD dst_unused:UNUSED_PAD src0_sel:DWORD src1_sel:WORD_1\n\t"
    "v_add_u32_sdwa v115, s92, v115 dst_sel:DWORD dst_unused:UNUSED_PAD src0_sel:DWORD src1_sel:WORD_0\n\t"
    "ds_read_b64 v[96:97], v115 offset:0\n\t"
    "v_mfma_f32_16x16x32_bf16 v[116:119], v[60:63], v[144:147], v[116:119]\n\t"
    "ds_read_b64 v[98:99], v98 offset:0\n\t"
    "v_cvt_pk_bf16_f32 v80, v80, v81\n\t"
    "v_cvt_pk_bf16_f32 v81, v82, v83\n\t"
    "ds_write_b64 v125, v[80:81] offset:640\n\t"
    "v_mfma_f32_16x16x32_bf16 v[80:83], v[48:51], v[212:215], 0\n\t"
    "buffer_load_dwordx4 v[112:115], v127, %12, s91 offen offset:0 sc1\n\t"
    "s_add_u32 s91, s91, s97\n\t"
    "v_add_u32_sdwa v126, s92, v100 dst_sel:DWORD dst_unused:UNUSED_PAD src0_sel:DWORD src1_sel:WORD_1\n\t"
    "v_add_u32_sdwa v100, s92, v100 dst_sel:DWORD dst_unused:UNUSED_PAD src0_sel:DWORD src1_sel:WORD_0\n\t"
    "v_mfma_f32_16x16x32_bf16 v[80:83], v[52:55], v[216:219], v[80:83]\n\t"
    "v_cvt_pk_bf16_f32 v8, v8, v9\n\t"
    "v_cvt_pk_bf16_f32 v9, v10, v11\n\t"
    "s_waitcnt lgkmcnt(6)\n\t"
    "v_smfmac_f32_16x16x64_bf16 v[24:27], v[120:123], v[84:91], v124\n\t"
    "ds_read_b64 v[84:85], v100 offset:0\n\t"
    "v_mfma_f32_16x16x32_bf16 v[80:83], v[56:59], v[148:151], v[80:83]\n\t"
    "ds_read_b64 v[86:87], v126 offset:0\n\t"
    "v_mfma_f32_16x16x32_bf16 v[80:83], v[60:63], v[152:155], v[80:83]\n\t"
    "v_add_u32_sdwa v90, s92, v101 dst_sel:DWORD dst_unused:UNUSED_PAD src0_sel:DWORD src1_sel:WORD_1\n\t"
    "v_add_u32_sdwa v101, s92, v101 dst_sel:DWORD dst_unused:UNUSED_PAD src0_sel:DWORD src1_sel:WORD_0\n\t"
    "ds_read_b64 v[88:89], v101 offset:0\n\t"
    "v_cvt_pk_bf16_f32 v116, v116, v117\n\t"
    "v_cvt_pk_bf16_f32 v117, v118, v119\n\t"
    "ds_write_b64 v125, v[116:117] offset:768\n\t"
    "v_mfma_f32_16x16x32_bf16 v[116:119], v[64:67], v[164:167], 0\n\t"
    "ds_read_b64 v[90:91], v90 offset:0\n\t"
    "v_mfma_f32_16x16x32_bf16 v[116:119], v[68:71], v[168:171], v[116:119]\n\t"
    "v_add_u32_sdwa v126, s92, v102 dst_sel:DWORD dst_unused:UNUSED_PAD src0_sel:DWORD src1_sel:WORD_1\n\t"
    "v_add_u32_sdwa v102, s92, v102 dst_sel:DWORD dst_unused:UNUSED_PAD src0_sel:DWORD src1_sel:WORD_0\n\t"
    "v_mfma_f32_16x16x32_bf16 v[116:119], v[72:75], v[220:223], v[116:119]\n\t"
    "v_cvt_pk_bf16_f32 v10, v12, v13\n\t"
    "v_cvt_pk_bf16_f32 v11, v14, v15\n\t"
    "v_mov_b64 v[12:13], 0\n\t"
    "v_mov_b64 v[14:15], 0\n\t"
    "s_cmp_lg_u32 s47, 0\n\t"
    "s_cbranch_scc1 L_mp2_%=\n\t"
    "L_mp2b_%=:\n\t"
    "s_waitcnt vmcnt(2) lgkmcnt(6)\n\t"
    "v_smfmac_f32_16x16x64_bf16 v[28:31], v[120:123], v[92:99], v124\n\t"
    "ds_read_b64 v[92:93], v102 offset:0\n\t"
    "v_mfma_f32_16x16x32_bf16 v[116:119], v[76:79], v[224:227], v[116:119]\n\t"
    "ds_read_b64 v[94:95], v126 offset:0\n\t"
    "v_cvt_pk_bf16_f32 v80, v80, v81\n\t"
    "v_cvt_pk_bf16_f32 v81, v82, v83\n\t"
    "ds_write_b64 v125, v[80:81] offset:872\n\t"
    "v_mfma_f32_16x16x32_bf16 v[80:83], v[64:67], v[172:175], 0\n\t"
    "v_add_u32_sdwa v98, s92, v103 dst_sel:DWORD dst_unused:UNUSED_PAD src0_sel:DWORD src1_sel:WORD_1\n\t"
    "v_add_u32_sdwa v103, s92, v103 dst_sel:DWORD dst_unused:UNUSED_PAD src0_sel:DWORD src1_sel:WORD_0\n\t"
    "ds_read_b64 v[96:97], v103 offset:0\n\t"
    "v_mfma_f32_16x16x32_bf16 v[80:83], v[68:71], v[176:179], v[80:83]\n\t"
    "ds_read_b64 v[98:99], v98 offset:0\n\t"
    "v_mfma_f32_16x16x32_bf16 v[80:83], v[72:75], v[228:231], v[80:83]\n\t"
    "v_add_u32_sdwa v126, s92, v104 dst_sel:DWORD dst_unused:UNUSED_PAD src0_sel:DWORD src1_sel:WORD_1\n\t"
    "v_add_u32_sdwa v104, s92, v104 dst_sel:DWORD dst_unused:UNUSED_PAD src0_sel:DWORD src1_sel:WORD_0\n\t"
    "v_mfma_f32_16x16x32_bf16 v[80:83], v[76:79], v[232:235], v[80:83]\n\t"
    "v_cvt_pk_bf16_f32 v16, v16, v17\n\t"
    "v_cvt_pk_bf16_f32 v17, v18, v19\n\t"
    "v_cvt_pk_bf16_f32 v116, v116, v117\n\t"
    "v_cvt_pk_bf16_f32 v117, v118, v119\n\t"
    "ds_write_b64 v125, v[116:117] offset:32000\n\t"
    "s_waitcnt lgkmcnt(6)\n\t"
    "v_smfmac_f32_16x16x64_bf16 v[32:35], v[120:123], v[84:91], v124\n\t"
    "ds_read_b64 v[84:85], v104 offset:0\n\t"
    "v_mfma_f32_16x16x32_bf16 v[116:119], v[64:67], v[180:183], 0\n\t"
    "ds_read_b64 v[86:87], v126 offset:0\n\t"
    "v_mfma_f32_16x16x32_bf16 v[116:119], v[68:71], v[184:187], v[116:119]\n\t"
    "v_add_u32_sdwa v90, s92, v105 dst_sel:DWORD dst_unused:UNUSED_PAD src0_sel:DWORD src1_sel:WORD_1\n\t"
    "v_add_u32_sdwa v105, s92, v105 dst_sel:DWORD dst_unused:UNUSED_PAD src0_sel:DWORD src1_sel:WORD_0\n\t"
    "ds_read_b64 v[88:89], v105 offset:0\n\t"
    "v_mfma_f32_16x16x32_bf16 v[116:119], v[72:75], v[236:239], v[116:119]\n\t"
    "ds_read_b64 v[90:91], v90 offset:0\n\t"
    "v_mfma_f32_16x16x32_bf16 v[116:119], v[76:79], v[240:243], v[116:119]\n\t"
    "v_add_u32_sdwa v126, s92, v106 dst_sel:DWORD dst_unused:UNUSED_PAD src0_sel:DWORD src1_sel:WORD_1\n\t"
    "v_add_u32_sdwa v106, s92, v106 dst_sel:DWORD dst_unused:UNUSED_PAD src0_sel:DWORD src1_sel:WORD_0\n\t"
    "v_cvt_pk_bf16_f32 v80, v80, v81\n\t"
    "v_cvt_pk_bf16_f32 v81, v82, v83\n\t"
    "ds_write_b64 v125, v[80:81] offset:32128\n\t"
    "v_mfma_f32_16x16x32_bf16 v[80:83], v[64:67], v[188:191], 0\n\t"
    "v_cvt_pk_bf16_f32 v18, v20, v21\n\t"
    "v_cvt_pk_bf16_f32 v19, v22, v23\n\t"
    "v_mov_b64 v[20:21], 0\n\t"
    "v_mov_b64 v[22:23], 0\n\t"
    "s_waitcnt vmcnt(0)\n\t"
    "s_cmp_lg_u32 s47, 0\n\t"
    "s_cbranch_scc1 L_mp3_%=\n\t"
    "L_mp3b_%=:\n\t"
    "s_barrier\n\t"
    "s_add_u32 s84, s84, s95\n\t"
    "buffer_load_dwordx4 v[100:103], v127, %12, s84 offen offset:0 sc1\n\t"
    "s_lshl_b32 s50, %16, 10\n\t"
    "s_add_u32 s63, s63, s50\n\t"
    "s_add_u32 s50, s50, 0x2000\n\t"
    "s_xor_b32 s50, s50, 0x2000\n\t"
    "s_add_u32 m0, s50, 128000\n\t"
    "s_add_u32 s63, s63, 0x4000\n\t"
    "buffer_load_dwordx4 v127, %12, s63 offen sc1 lds\n\t"
    "s_cmp_lt_u32 %16, 4\n\t"
    "s_cbranch_scc1 L_sp1_%=\n\t"
    "s_setprio 1\n\t"
    "L_sp1_%=:\n\t"
    "s_waitcnt lgkmcnt(6)\n\t"
    "v_smfmac_f32_16x16x64_bf16 v[36:39], v[120:123], v[92:99], v124\n\t"
    "ds_read_b64 v[92:93], v106 offset:0\n\t"
    "v_mfma_f32_16x16x32_bf16 v[80:83], v[68:71], v[192:195], v[80:83]\n\t"
    "ds_read_b64 v[94:95], v126 offset:0\n\t"
    "v_mfma_f32_16x16x32_bf16 v[80:83], v[72:75], v[244:247], v[80:83]\n\t"
    "v_add_u32_sdwa v98, s92, v107 dst_sel:DWORD dst_unused:UNUSED_PAD src0_sel:DWORD src1_sel:WORD_1\n\t"
    "v_add_u32_sdwa v107, s92, v107 dst_sel:DWORD dst_unused:UNUSED_PAD src0_sel:DWORD src1_sel:WORD_0\n\t"
    "ds_read_b64 v[96:97], v107 offset:0\n\t"
    "v_mfma_f32_16x16x32_bf16 v[80:83], v[76:79], v[248:251], v[80:83]\n\t"
    "ds_read_b64 v[98:99], v98 offset:0\n\t"
    "v_cvt_pk_bf16_f32 v116, v116, v117\n\t"
    "v_cvt_pk_bf16_f32 v117, v118, v119\n\t"
    "ds_write_b64 v125, v[116:117] offset:32256\n\t"
    "v_mfma_f32_16x16x32_bf16 v[116:119], v[64:67], v[196:199], 0\n\t"
    "buffer_load_dwordx4 v[104:107], v127, %12, s84 offen offset:1024 sc1\n\t"
    "v_add_u32_sdwa v126, s92, v108 dst_sel:DWORD dst_unused:UNUSED_PAD src0_sel:DWORD src1_sel:WORD_1\n\t"
    "v_add_u32_sdwa v108, s92, v108 dst_sel:DWORD dst_unused:UNUSED_PAD src0_sel:DWORD src1_sel:WORD_0\n\t"
    "v_mfma_f32_16x16x32_bf16 v[116:119], v[68:71], v[200:203], v[116:119]\n\t"
    "v_cvt_pk_bf16_f32 v24, v24, v25\n\t"
    "v_cvt_pk_bf16_f32 v25, v26, v27\n\t"
    "v_mov_b64 v[48:49], 0\n\t"
    "v_mov_b64 v[50:51], 0\n\t"
    "v_mov_b64 v[52:53], 0\n\t"
    "v_mov_b64 v[54:55], 0\n\t"
    "s_waitcnt lgkmcnt(6)\n\t"
    "v_smfmac_f32_16x16x64_bf16 v[40:43], v[120:123], v[84:91], v124\n\t"
    "ds_read_b64 v[84:85], v108 offset:0\n\t"
    "v_mfma_f32_16x16x32_bf16 v[116:119], v[72:75], v[252:255], v[116:119]\n\t"
    "ds_read_b64 v[86:87], v126 offset:0\n\t"
    "v_mfma_f32_16x16x32_bf16 v[116:119], v[76:79], v[128:131], v[116:119]\n\t"
    "v_add_u32_sdwa v90, s92, v109 dst_sel:DWORD dst_unused:UNUSED_PAD src0_sel:DWORD src1_sel:WORD_1\n\t"
    "v_add_u32_sdwa v109, s92, v109 dst_sel:DWORD dst_unused:UNUSED_PAD src0_sel:DWORD src1_sel:WORD_0\n\t"
    "ds_read_b64 v[88:89], v109 offset:0\n\t"
    "v_cvt_pk_bf16_f32 v80, v80, v81\n\t"
    "v_cvt_pk_bf16_f32 v81, v82, v83\n\t"
    "ds_write_b64 v125, v[80:81] offset:32384\n\t"
    "v_mfma_f32_16x16x32_bf16 v[80:83], v[64:67], v[204:207], 0\n\t"
    "ds_read_b64 v[90:91], v90 offset:0\n\t"
    "v_mfma_f32_16x16x32_bf16 v[80:83], v[68:71], v[208:211], v[80:83]\n\t"
    "v_add_u32_sdwa v126, s92, v110 dst_sel:DWORD dst_unused:UNUSED_PAD src0_sel:DWORD src1_sel:WORD_1\n\t"
    "v_add_u32_sdwa v110, s92, v110 dst_sel:DWORD dst_unused:UNUSED_PAD src0_sel:DWORD src1_sel:WORD_0\n\t"
    "v_mfma_f32_16x16x32_bf16 v[80:83], v[72:75], v[132:135], v[80:83]\n\t"
    "v_cvt_pk_bf16_f32 v26, v28, v29\n\t"
    "v_cvt_pk_bf16_f32 v27, v30, v31\n\t"
    "v_mov_b64 v[28:29], 0\n\t"
    "v_mov_b64 v[30:31], 0\n\t"
    "s_waitcnt lgkmcnt(6)\n\t"
    "v_smfmac_f32_16x16x64_bf16 v[44:47], v[120:123], v[92:99], v124\n\t"
    "ds_read_b64 v[92:93], v110 offset:0\n\t"
    "v_mfma_f32_16x16x32_bf16 v[80:83], v[76:79], v[136:139], v[80:83]\n\t"
    "ds_read_b64 v[94:95], v126 offset:0\n\t"
    "v_cvt_pk_bf16_f32 v116, v116, v117\n\t"
    "v_cvt_pk_bf16_f32 v117, v118, v119\n\t"
    "ds_write_b64 v125, v[116:117] offset:32512\n\t"
    "v_mfma_f32_16x16x32_bf16 v[116:119], v[64:67], v[156:159], 0\n\t"
    "v_add_u32_sdwa v98, s92, v111 dst_sel:DWORD dst_unused:UNUSED_PAD src0_sel:DWORD src1_sel:WORD_1\n\t"
    "v_add_u32_sdwa v111, s92, v111 dst_sel:DWORD dst_unused:UNUSED_PAD src0_sel:DWORD src1_sel:WORD_0\n\t"
    "ds_read_b64 v[96:97], v111 offset:0\n\t"
    "v_mfma_f32_16x16x32_bf16 v[116:119], v[68:71], v[160:163], v[116:119]\n\t"
    "ds_read_b64 v[98:99], v98 offset:0\n\t"
    "v_mfma_f32_16x16x32_bf16 v[116:119], v[72:75], v[140:143], v[116:119]\n\t"
    "s_add_u32 s85, s85, s95\n\t"
    "buffer_load_dwordx4 v[108:111], v127, %12, s85 offen offset:0 sc1\n\t"
    "v_add_u32_sdwa v126, s92, v112 dst_sel:DWORD dst_unused:UNUSED_PAD src0_sel:DWORD src1_sel:WORD_1\n\t"
    "v_add_u32_sdwa v112, s92, v112 dst_sel:DWORD dst_unused:UNUSED_PAD src0_sel:DWORD src1_sel:WORD_0\n\t"
    "v_mfma_f32_16x16x32_bf16 v[116:119], v[76:79], v[144:147], v[116:119]\n\t"
    "v_cvt_pk_bf16_f32 v32, v32, v33\n\t"
    "v_cvt_pk_bf16_f32 v33, v34, v35\n\t"
    "v_cvt_pk_bf16_f32 v80, v80, v81\n\t"
    "v_cvt_pk_bf16_f32 v81, v82, v83\n\t"
    "ds_write_b64 v125, v[80:81] offset:32640\n\t"
    "v_mov_b64 v[56:57], 0\n\t"
    "v_mov_b64 v[58:59], 0\n\t"
    "v_mov_b64 v[60:61], 0\n\t"
    "v_mov_b64 v[62:63], 0\n\t"
    "s_waitcnt lgkmcnt(6)\n\t"
    "v_smfmac_f32_16x16x64_bf16 v[48:51], v[120:123], v[84:91], v124\n\t"
    "ds_read_b64 v[84:85], v112 offset:0\n\t"
    "v_mfma_f32_16x16x32_bf16 v[80:83], v[64:67], v[212:215], 0\n\t"
    "ds_read_b64 v[86:87], v126 offset:0\n\t"
    "v_mfma_f32_16x16x32_bf16 v[80:83], v[68:71], v[216:219], v[80:83]\n\t"
    "v_add_u32_sdwa v90, s92, v113 dst_sel:DWORD dst_unused:UNUSED_PAD src0_sel:DWORD src1_sel:WORD_1\n\t"
    "v_add_u32_sdwa v113, s92, v113 dst_sel:DWORD dst_unused:UNUSED_PAD src0_sel:DWORD src1_sel:WORD_0\n\t"
    "ds_read_b64 v[88:89], v113 offset:0\n\t"
    "v_mfma_f32_16x16x32_bf16 v[80:83], v[72:75], v[148:151], v[80:83]\n\t"
    "ds_read_b64 v[90:91], v90 offset:0\n\t"
    "v_mfma_f32_16x16x32_bf16 v[80:83], v[76:79], v[152:155], v[80:83]\n\t"
    "v_add_u32_sdwa v126, s92, v114 dst_sel:DWORD dst_unused:UNUSED_PAD src0_sel:DWORD src1_sel:WORD_1\n\t"
    "v_add_u32_sdwa v114, s92, v114 dst_sel:DWORD dst_unused:UNUSED_PAD src0_sel:DWORD src1_sel:WORD_0\n\t"
    "v_cvt_pk_bf16_f32 v116, v116, v117\n\t"
    "v_cvt_pk_bf16_f32 v117, v118, v119\n\t"
    "ds_write_b64 v125, v[116:117] offset:32768\n\t"
    "s_nop 2\n\t"
    "v_cvt_pk_bf16_f32 v80, v80, v81\n\t"
    "v_cvt_pk_bf16_f32 v81, v82, v83\n\t"
    "ds_write_b64 v125, v[80:81] offset:32872\n\t"
    "v_cvt_pk_bf16_f32 v34, v36, v37\n\t"
    "v_cvt_pk_bf16_f32 v35, v38, v39\n\t"
    "v_mov_b64 v[36:37], 0\n\t"
    "v_mov_b64 v[38:39], 0\n\t"
    "v_cvt_pk_bf16_f32 v40, v40, v41\n\t"
    "v_cvt_pk_bf16_f32 v41, v42, v43\n\t"
    "v_cvt_pk_bf16_f32 v42, v44, v45\n\t"
    "v_cvt_pk_bf16_f32 v43, v46, v47\n\t"
    "v_mov_b64 v[44:45], 0\n\t"
    "v_mov_b64 v[46:47], 0\n\t"
    "s_waitcnt vmcnt(3) lgkmcnt(7)\n\t"
    "v_smfmac_f32_16x16x64_bf16 v[52:55], v[120:123], v[92:99], v124\n\t"
    "ds_read_b64 v[92:93], v114 offset:0\n\t"
    "ds_read_b64 v[94:95], v126 offset:0\n\t"
    "v_add_u32_sdwa v98, s92, v115 dst_sel:DWORD dst_unused:UNUSED_PAD src0_sel:DWORD src1_sel:WORD_1\n\t"
    "v_add_u32_sdwa v115, s92, v115 dst_sel:DWORD dst_unused:UNUSED_PAD src0_sel:DWORD src1_sel:WORD_0\n\t"
    "ds_read_b64 v[96:97], v115 offset:0\n\t"
    "ds_read_b64 v[98:99], v98 offset:0\n\t"
    "buffer_load_dwordx4 v[112:115], v127, %12, s85 offen offset:1024 sc1\n\t"
    "s_sub_u32 s92, 64000, s92\n\t"
    "v_add_u32_sdwa v126, s92, v100 dst_sel:DWORD dst_unused:UNUSED_PAD src0_sel:DWORD src1_sel:WORD_1\n\t"
    "v_add_u32_sdwa v100, s92, v100 dst_sel:DWORD dst_unused:UNUSED_PAD src0_sel:DWORD src1_sel:WORD_0\n\t"
    "v_cvt_pk_bf16_f32 v48, v48, v49\n\t"
    "v_cvt_pk_bf16_f32 v49, v50, v51\n\t"
    "s_waitcnt lgkmcnt(4)\n\t"
    "s_waitcnt lgkmcnt(0)\n\t"
    "s_waitcnt vmcnt(3)\n\t"
    "s_barrier\n\t"
    "v_add_u32 v125, s93, v125\n\t"
    "s_sub_u32 s93, 0, s93\n\t"
    "v_smfmac_f32_16x16x64_bf16 v[56:59], v[120:123], v[84:91], v124\n\t"
    "ds_read_b64 v[84:85], v100 offset:0\n\t"
    "ds_read_b64 v[86:87], v126 offset:0\n\t"
    "v_add_u32_sdwa v90, s92, v101 dst_sel:DWORD dst_unused:UNUSED_PAD src0_sel:DWORD src1_sel:WORD_1\n\t"
    "v_add_u32_sdwa v101, s92, v101 dst_sel:DWORD dst_unused:UNUSED_PAD src0_sel:DWORD src1_sel:WORD_0\n\t"
    "ds_read_b64 v[88:89], v101 offset:0\n\t"
    "ds_read_b64 v[90:91], v90 offset:0\n\t"
    "v_add_u32_sdwa v126, s92, v102 dst_sel:DWORD dst_unused:UNUSED_PAD src0_sel:DWORD src1_sel:WORD_1\n\t"
    "v_add_u32_sdwa v102, s92, v102 dst_sel:DWORD dst_unused:UNUSED_PAD src0_sel:DWORD src1_sel:WORD_0\n\t"
    "v_cvt_pk_bf16_f32 v50, v52, v53\n\t"
    "v_cvt_pk_bf16_f32 v51, v54, v55\n\t"
    "v_mov_b64 v[52:53], 0\n\t"
    "v_mov_b64 v[54:55], 0\n\t"
    "s_waitcnt vmcnt(2)\n\t"
    "v_smfmac_f32_16x16x64_bf16 v[60:63], v[120:123], v[92:99], v124\n\t"
    "ds_read_b64 v[92:93], v102 offset:0\n\t"
    "ds_read_b64 v[94:95], v126 offset:0\n\t"
    "v_add_u32_sdwa v98, s92, v103 dst_sel:DWORD dst_unused:UNUSED_PAD src0_sel:DWORD src1_sel:WORD_1\n\t"
    "v_add_u32_sdwa v103, s92, v103 dst_sel:DWORD dst_unused:UNUSED_PAD src0_sel:DWORD src1_sel:WORD_0\n\t"
    "ds_read_b64 v[96:97], v103 offset:0\n\t"
    "ds_read_b64 v[98:99], v98 offset:0\n\t"
    "s_add_u32 s86, s86, s95\n\t"
    "buffer_load_dwordx4 v[100:103], v127, %12, s86 offen offset:0 sc1\n\t"
    "v_add_u32_sdwa v126, s92, v104 dst_sel:DWORD dst_unused:UNUSED_PAD src0_sel:DWORD src1_sel:WORD_1\n\t"
    "v_add_u32_sdwa v104, s92, v104 dst_sel:DWORD dst_unused:UNUSED_PAD src0_sel:DWORD src1_sel:WORD_0\n\t"
    "v_cvt_pk_bf16_f32 v56, v56, v57\n\t"
    "v_cvt_pk_bf16_f32 v57, v58, v59\n\t"
    "v_add_u32 v79, s96, v127\n\t"
    "s_add_u32 s96, s96, 0x1000\n\t"
    "s_and_b32 s96, s96, 0x3000\n\t"
    "v_add_u32 v79, 128000, v79\n\t"
    "ds_read_b128 v[64:67], v79 offset:0\n\t"
    "ds_read_b128 v[68:71], v79 offset:1024\n\t"
    "ds_read_b128 v[72:75], v79 offset:2048\n\t"
    "ds_read_b128 v[76:79], v79 offset:3072\n\t"
    "v_mov_b64 v[116:117], 0\n\t"
    "v_mov_b64 v[118:119], 0\n\t"
    "v_cvt_pk_bf16_f32 v58, v60, v61\n\t"
    "v_cvt_pk_bf16_f32 v59, v62, v63\n\t"
    "v_mov_b64 v[60:61], 0\n\t"
    "v_mov_b64 v[62:63], 0\n\t"
    "s_mov_b32 s66, 4\n\t"
    "s_branch L_loop_%=\n\t"
    ".p2align 9\n\t"
    "s_nop 0\n\t"
    "s_nop 0\n\t"
    "s_nop 0\n\t"
    "s_nop 0\n\t"
    "s_nop 0\n\t"
    "s_nop 0\n\t"
    "s_nop 0\n\t"
    "s_nop 0\n\t"
    "s_nop 0\n\t"
    "s_nop 0\n\t"
    "s_nop 0\n\t"
    "s_nop 0\n\t"
    "s_nop 0\n\t"
    "s_nop 0\n\t"
    "s_nop 0\n\t"
    "s_nop 0\n\t"
    "s_nop 0\n\t"
    "s_nop 0\n\t"
    "s_nop 0\n\t"
    "s_nop 0\n\t"
    "s_nop 0\n\t"
    "s_nop 0\n\t"
    "s_nop 0\n\t"
    "s_nop 0\n\t"
    "s_nop 0\n\t"
    "s_nop 0\n\t"
    "s_nop 0\n\t"
    "s_nop 0\n\t"
    "s_nop 0\n\t"
    "s_nop 0\n\t"
    "s_nop 0\n\t"
    "s_nop 0\n\t"
    "s_nop 0\n\t"
    "L_loop_%=:\n\t"
    "s_waitcnt lgkmcnt(8)\n\t"
    "v_smfmac_f32_16x16x64_bf16 v[116:119], v[120:123], v[84:91], v124\n\t"
    "ds_read_b64 v[84:85], v104 offset:0\n\t"
    "ds_read_b64 v[86:87], v126 offset:0\n\t"
    "v_add_u32_sdwa v90, s92, v105 dst_sel:DWORD dst_unused:UNUSED_PAD src0_sel:DWORD src1_sel:WORD_1\n\t"
    "v_add_u32_sdwa v105, s92, v105 dst_sel:DWORD dst_unused:UNUSED_PAD src0_sel:DWORD src1_sel:WORD_0\n\t"
    "ds_read_b64 v[88:89], v105 offset:0\n\t"
    "ds_read_b64 v[90:91], v90 offset:0\n\t"
    "v_add_u32_sdwa v126, s92, v106 dst_sel:DWORD dst_unused:UNUSED_PAD src0_sel:DWORD src1_sel:WORD_1\n\t"
    "v_add_u32_sdwa v106, s92, v106 dst_sel:DWORD dst_unused:UNUSED_PAD src0_sel:DWORD src1_sel:WORD_0\n\t"
    "s_waitcnt vmcnt(2) lgkmcnt(8)\n\t"
    "v_smfmac_f32_16x16x64_bf16 v[116:119], v[120:123], v[92:99], v124\n\t"
    "ds_read_b64 v[92:93], v106 offset:0\n\t"
    "ds_read_b64 v[94:95], v126 offset:0\n\t"
    "v_add_u32_sdwa v98, s92, v107 dst_sel:DWORD dst_unused:UNUSED_PAD src0_sel:DWORD src1_sel:WORD_1\n\t"
    "v_add_u32_sdwa v107, s92, v107 dst_sel:DWORD dst_unused:UNUSED_PAD src0_sel:DWORD src1_sel:WORD_0\n\t"
    "ds_read_b64 v[96:97], v107 offset:0\n\t"
    "ds_read_b64 v[98:99], v98 offset:0\n\t"
    "buffer_load_dwordx4 v[104:107], v127, %12, s86 offen offset:1024 sc1\n\t"
    "v_add_u32_sdwa v126, s92, v108 dst_sel:DWORD dst_unused:UNUSED_PAD src0_sel:DWORD src1_sel:WORD_1\n\t"
    "v_cvt_pk_bf16_f32 v4, v116, v117\n\t"
    "v_cvt_pk_bf16_f32 v5, v118, v119\n\t"
    "v_and_b32 v119, s67, v7\n\t"
    "v_lshlrev_b32 v118, 16, v7\n\t"
    "v_and_b32 v117, s67, v6\n\t"
    "v_lshlrev_b32 v116, 16, v6\n\t"
    "v_add_u32_sdwa v108, s92, v108 dst_sel:DWORD dst_unused:UNUSED_PAD src0_sel:DWORD src1_sel:WORD_0\n\t"
    "s_waitcnt lgkmcnt(4)\n\t"
    "v_smfmac_f32_16x16x64_bf16 v[116:119], v[120:123], v[84:91], v124\n\t"
    "ds_read_b64 v[84:85], v108 offset:0\n\t"
    "v_mfma_f32_16x16x32_bf16 v[80:83], v[64:67], v[164:167], 0\n\t"
    "ds_read_b64 v[86:87], v126 offset:0\n\t"
    "v_mfma_f32_16x16x32_bf16 v[80:83], v[68:71], v[168:171], v[80:83]\n\t"
    "v_add_u32_sdwa v90, s92, v109 dst_sel:DWORD dst_unused:UNUSED_PAD src0_sel:DWORD src1_sel:WORD_1\n\t"
    "v_add_u32_sdwa v109, s92, v109 dst_sel:DWORD dst_unused:UNUSED_PAD src0_sel:DWORD src1_sel:WORD_0\n\t"
    "ds_read_b64 v[88:89], v109 offset:0\n\t"
    "v_mfma_f32_16x16x32_bf16 v[80:83], v[72:75], v[220:223], v[80:83]\n\t"
    "ds_read_b64 v[90:91], v90 offset:0\n\t"
    "v_mfma_f32_16x16x32_bf16 v[80:83], v[76:79], v[224:227], v[80:83]\n\t"
    "v_add_u32_sdwa v126, s92, v110 dst_sel:DWORD dst_unused:UNUSED_PAD src0_sel:DWORD src1_sel:WORD_1\n\t"
    "v_add_u32_sdwa v110, s92, v110 dst_sel:DWORD dst_unused:UNUSED_PAD src0_sel:DWORD src1_sel:WORD_0\n\t"
    "s_waitcnt vmcnt(2) lgkmcnt(4)\n\t"
    "v_smfmac_f32_16x16x64_bf16 v[116:119], v[120:123], v[92:99], v124\n\t"
    "ds_read_b64 v[92:93], v110 offset:0\n\t"
    "ds_read_b64 v[94:95], v126 offset:0\n\t"
    "v_add_u32_sdwa v98, s92, v111 dst_sel:DWORD dst_unused:UNUSED_PAD src0_sel:DWORD src1_sel:WORD_1\n\t"
    "v_add_u32_sdwa v111, s92, v111 dst_sel:DWORD dst_unused:UNUSED_PAD src0_sel:DWORD src1_sel:WORD_0\n\t"
    "ds_read_b64 v[96:97], v111 offset:0\n\t"
    "ds_read_b64 v[98:99], v98 offset:0\n\t"
    "s_add_u32 s87, s87, s95\n\t"
    "buffer_load_dwordx4 v[108:111], v127, %12, s87 offen offset:0 sc1\n\t"
    "v_add_u32_sdwa v126, s92, v112 dst_sel:DWORD dst_unused:UNUSED_PAD src0_sel:DWORD src1_sel:WORD_1\n\t"
    "v_cvt_pk_bf16_f32 v6, v116, v117\n\t"
    "v_cvt_pk_bf16_f32 v7, v118, v119\n\t"
    "v_and_b32 v119, s67, v13\n\t"
    "v_lshlrev_b32 v118, 16, v13\n\t"
    "v_and_b32 v117, s67, v12\n\t"
    "v_lshlrev_b32 v116, 16, v12\n\t"
    "v_add_u32_sdwa v112, s92, v112 dst_sel:DWORD dst_unused:UNUSED_PAD src0_sel:DWORD src1_sel:WORD_0\n\t"
    "s_waitcnt lgkmcnt(4)\n\t"
    "v_smfmac_f32_16x16x64_bf16 v[116:119], v[120:123], v[84:91], v124\n\t"
    "ds_read_b64 v[84:85], v112 offset:0\n\t"
    "ds_read_b64 v[86:87], v126 offset:0\n\t"
    "v_cvt_pk_bf16_f32 v80, v80, v81\n\t"
    "v_cvt_pk_bf16_f32 v81, v82, v83\n\t"
    "ds_write_b64 v125, v[80:81] offset:0\n\t"
    "v_mfma_f32_16x16x32_bf16 v[80:83], v[64:67], v[172:175], 0\n\t"
    "v_add_u32_sdwa v90, s92, v113 dst_sel:DWORD dst_unused:UNUSED_PAD src0_sel:DWORD src1_sel:WORD_1\n\t"
    "v_add_u32_sdwa v113, s92, v113 dst_sel:DWORD dst_unused:UNUSED_PAD src0_sel:DWORD src1_sel:WORD_0\n\t"
    "ds_read_b64 v[88:89], v113 offset:0\n\t"
    "v_mfma_f32_16x16x32_bf16 v[80:83], v[68:71], v[176:179], v[80:83]\n\t"
    "ds_read_b64 v[90:91], v90 offset:0\n\t"
    "v_mfma_f32_16x16x32_bf16 v[80:83], v[72:75], v[228:231], v[80:83]\n\t"
    "v_mfma_f32_16x16x32_bf16 v[80:83], v[76:79], v[232:235], v[80:83]\n\t"
    "v_add_u32_sdwa v126, s92, v114 dst_sel:DWORD dst_unused:UNUSED_PAD src0_sel:DWORD src1_sel:WORD_1\n\t"
    "v_add_u32_sdwa v114, s92, v114 dst_sel:DWORD dst_unused:UNUSED_PAD src0_sel:DWORD src1_sel:WORD_0\n\t"
    "s_waitcnt vmcnt(2) lgkmcnt(5)\n\t"
    "v_smfmac_f32_16x16x64_bf16 v[116:119], v[120:123], v[92:99], v124\n\t"
    "ds_read_b64 v[92:93], v114 offset:0\n\t"
    "ds_read_b64 v[94:95], v126 offset:0\n\t"
    "v_add_u32_sdwa v98, s92, v115 dst_sel:DWORD dst_unused:UNUSED_PAD src0_sel:DWORD src1_sel:WORD_1\n\t"
    "v_add_u32_sdwa v115, s92, v115 dst_sel:DWORD dst_unused:UNUSED_PAD src0_sel:DWORD src1_sel:WORD_0\n\t"
    "ds_read_b64 v[96:97], v115 offset:0\n\t"
    "ds_read_b64 v[98:99], v98 offset:0\n\t"
    "buffer_load_dwordx4 v[112:115], v127, %12, s87 offen offset:1024 sc1\n\t"
    "v_add_u32_sdwa v126, s92, v100 dst_sel:DWORD dst_unused:UNUSED_PAD src0_sel:DWORD src1_sel:WORD_1\n\t"
    "v_cvt_pk_bf16_f32 v12, v116, v117\n\t"
    "v_cvt_pk_bf16_f32 v13, v118, v119\n\t"
    "v_and_b32 v119, s67, v15\n\t"
    "v_lshlrev_b32 v118, 16, v15\n\t"
    "v_and_b32 v117, s67, v14\n\t"
    "v_lshlrev_b32 v116, 16, v14\n\t"
    "v_add_u32_sdwa v100, s92, v100 dst_sel:DWORD dst_unused:UNUSED_PAD src0_sel:DWORD src1_sel:WORD_0\n\t"
    "s_waitcnt lgkmcnt(4)\n\t"
    "v_smfmac_f32_16x16x64_bf16 v[116:119], v[120:123], v[84:91], v124\n\t"
    "ds_read_b64 v[84:85], v100 offset:0\n\t"
    "ds_read_b64 v[86:87], v126 offset:0\n\t"
    "v_cvt_pk_bf16_f32 v80, v80, v81\n\t"
    "v_cvt_pk_bf16_f32 v81, v82, v83\n\t"
    "ds_write_b64 v125, v[80:81] offset:128\n\t"
    "v_mfma_f32_16x16x32_bf16 v[80:83], v[64:67], v[180:183], 0\n\t"
    "v_add_u32_sdwa v90, s92, v101 dst_sel:DWORD dst_unused:UNUSED_PAD src0_sel:DWORD src1_sel:WORD_1\n\t"
    "v_add_u32_sdwa v101, s92, v101 dst_sel:DWORD dst_unused:UNUSED_PAD src0_sel:DWORD src1_sel:WORD_0\n\t"
    "ds_read_b64 v[88:89], v101 offset:0\n\t"
    "v_mfma_f32_16x16x32_bf16 v[80:83], v[68:71], v[184:187], v[80:83]\n\t"
    "ds_read_b64 v[90:91], v90 offset:0\n\t"
    "v_mfma_f32_16x16x32_bf16 v[80:83], v[72:75], v[236:239], v[80:83]\n\t"
    "v_mfma_f32_16x16x32_bf16 v[80:83], v[76:79], v[240:243], v[80:83]\n\t"
    "v_add_u32_sdwa v126, s92, v102 dst_sel:DWORD dst_unused:UNUSED_PAD src0_sel:DWORD src1_sel:WORD_1\n\t"
    "v_add_u32_sdwa v102, s92, v102 dst_sel:DWORD dst_unused:UNUSED_PAD src0_sel:DWORD src1_sel:WORD_0\n\t"
    "s_waitcnt vmcnt(2) lgkmcnt(5)\n\t"
    "v_smfmac_f32_16x16x64_bf16 v[116:119], v[120:123], v[92:99], v124\n\t"
    "ds_read_b64 v[92:93], v102 offset:0\n\t"
    "ds_read_b64 v[94:95], v126 offset:0\n\t"
    "v_add_u32_sdwa v98, s92, v103 dst_sel:DWORD dst_unused:UNUSED_PAD src0_sel:DWORD src1_sel:WORD_1\n\t"
    "v_add_u32_sdwa v103, s92, v103 dst_sel:DWORD dst_unused:UNUSED_PAD src0_sel:DWORD src1_sel:WORD_0\n\t"
    "ds_read_b64 v[96:97], v103 offset:0\n\t"
    "ds_read_b64 v[98:99], v98 offset:0\n\t"
    "s_add_u32 s88, s88, s95\n\t"
    "buffer_load_dwordx4 v[100:103], v127, %12, s88 offen offset:0 sc1\n\t"
    "v_add_u32_sdwa v126, s92, v104 dst_sel:DWORD dst_unused:UNUSED_PAD src0_sel:DWORD src1_sel:WORD_1\n\t"
    "v_cvt_pk_bf16_f32 v14, v116, v117\n\t"
    "v_cvt_pk_bf16_f32 v15, v118, v119\n\t"
    "v_and_b32 v119, s67, v21\n\t"
    "v_lshlrev_b32 v118, 16, v21\n\t"
    "v_and_b32 v117, s67, v20\n\t"
    "v_lshlrev_b32 v116, 16, v20\n\t"
    "v_add_u32_sdwa v104, s92, v104 dst_sel:DWORD dst_unused:UNUSED_PAD src0_sel:DWORD src1_sel:WORD_0\n\t"
    "s_waitcnt lgkmcnt(4)\n\t"
    "v_smfmac_f32_16x16x64_bf16 v[116:119], v[120:123], v[84:91], v124\n\t"
    "ds_read_b64 v[84:85], v104 offset:0\n\t"
    "ds_read_b64 v[86:87], v126 offset:0\n\t"
    "v_cvt_pk_bf16_f32 v80, v80, v81\n\t"
    "v_cvt_pk_bf16_f32 v81, v82, v83\n\t"
    "ds_write_b64 v125, v[80:81] offset:256\n\t"
    "v_add_u32_sdwa v90, s92, v105 dst_sel:DWORD dst_unused:UNUSED_PAD src0_sel:DWORD src1_sel:WORD_1\n\t"
    "v_add_u32_sdwa v105, s92, v105 dst_sel:DWORD dst_unused:UNUSED_PAD src0_sel:DWORD src1_sel:WORD_0\n\t"
    "ds_read_b64 v[88:89], v105 offset:0\n\t"
    "ds_read_b64 v[90:91], v90 offset:0\n\t"
    "v_add_u32_sdwa v126, s92, v106 dst_sel:DWORD dst_unused:UNUSED_PAD src0_sel:DWORD src1_sel:WORD_1\n\t"
    "v_add_u32_sdwa v106, s92, v106 dst_sel:DWORD dst_unused:UNUSED_PAD src0_sel:DWORD src1_sel:WORD_0\n\t"
    "s_waitcnt vmcnt(2) lgkmcnt(5)\n\t"
    "v_smfmac_f32_16x16x64_bf16 v[116:119], v[120:123], v[92:99], v124\n\t"
    "ds_read_b64 v[92:93], v106 offset:0\n\t"
    "ds_read_b64 v[94:95], v126 offset:0\n\t"
    "v_add_u32_sdwa v98, s92, v107 dst_sel:DWORD dst_unused:UNUSED_PAD src0_sel:DWORD src1_sel:WORD_1\n\t"
    "v_add_u32_sdwa v107, s92, v107 dst_sel:DWORD dst_unused:UNUSED_PAD src0_sel:DWORD src1_sel:WORD_0\n\t"
    "ds_read_b64 v[96:97], v107 offset:0\n\t"
    "ds_read_b64 v[98:99], v98 offset:0\n\t"
    "buffer_load_dwordx4 v[104:107], v127, %12, s88 offen offset:1024 sc1\n\t"
    "v_add_u32_sdwa v126, s92, v108 dst_sel:DWORD dst_unused:UNUSED_PAD src0_sel:DWORD src1_sel:WORD_1\n\t"
    "v_cvt_pk_bf16_f32 v20, v116, v117\n\t"
    "v_cvt_pk_bf16_f32 v21, v118, v119\n\t"
    "v_and_b32 v119, s67, v23\n\t"
    "v_lshlrev_b32 v118, 16, v23\n\t"
    "v_and_b32 v117, s67, v22\n\t"
    "v_lshlrev_b32 v116, 16, v22\n\t"
    "v_add_u32_sdwa v108, s92, v108 dst_sel:DWORD dst_unused:UNUSED_PAD src0_sel:DWORD src1_sel:WORD_0\n\t"
    "s_waitcnt lgkmcnt(4)\n\t"
    "v_smfmac_f32_16x16x64_bf16 v[116:119], v[120:123], v[84:91], v124\n\t"
    "ds_read_b64 v[84:85], v108 offset:0\n\t"
    "v_mfma_f32_16x16x32_bf16 v[80:83], v[64:67], v[188:191], 0\n\t"
    "ds_read_b64 v[86:87], v126 offset:0\n\t"
    "v_mfma_f32_16x16x32_bf16 v[80:83], v[68:71], v[192:195], v[80:83]\n\t"
    "v_add_u32_sdwa v90, s92, v109 dst_sel:DWORD dst_unused:UNUSED_PAD src0_sel:DWORD src1_sel:WORD_1\n\t"
    "v_add_u32_sdwa v109, s92, v109 dst_sel:DWORD dst_unused:UNUSED_PAD src0_sel:DWORD src1_sel:WORD_0\n\t"
    "ds_read_b64 v[88:89], v109 offset:0\n\t"
    "v_mfma_f32_16x16x32_bf16 v[80:83], v[72:75], v[244:247], v[80:83]\n\t"
    "ds_read_b64 v[90:91], v90 offset:0\n\t"
    "v_mfma_f32_16x16x32_bf16 v[80:83], v[76:79], v[248:251], v[80:83]\n\t"
    "v_add_u32_sdwa v126, s92, v110 dst_sel:DWORD dst_unused:UNUSED_PAD src0_sel:DWORD src1_sel:WORD_1\n\t"
    "v_add_u32_sdwa v110, s92, v110 dst_sel:DWORD dst_unused:UNUSED_PAD src0_sel:DWORD src1_sel:WORD_0\n\t"
    "s_waitcnt vmcnt(2) lgkmcnt(4)\n\t"
    "v_smfmac_f32_16x16x64_bf16 v[116:119], v[120:123], v[92:99], v124\n\t"
    "ds_read_b64 v[92:93], v110 offset:0\n\t"
    "ds_read_b64 v[94:95], v126 offset:0\n\t"
    "v_add_u32_sdwa v98, s92, v111 dst_sel:DWORD dst_unused:UNUSED_PAD src0_sel:DWORD src1_sel:WORD_1\n\t"
    "v_add_u32_sdwa v111, s92, v111 dst_sel:DWORD dst_unused:UNUSED_PAD src0_sel:DWORD src1_sel:WORD_0\n\t"
    "ds_read_b64 v[96:97], v111 offset:0\n\t"
    "ds_read_b64 v[98:99], v98 offset:0\n\t"
    "s_add_u32 s89, s89, s95\n\t"
    "buffer_load_dwordx4 v[108:111], v127, %12, s89 offen offset:0 sc1\n\t"
    "v_add_u32_sdwa v126, s92, v112 dst_sel:DWORD dst_unused:UNUSED_PAD src0_sel:DWORD src1_sel:WORD_1\n\t"
    "v_cvt_pk_bf16_f32 v22, v116, v117\n\t"
    "v_cvt_pk_bf16_f32 v23, v118, v119\n\t"
    "v_and_b32 v119, s67, v29\n\t"
    "v_lshlrev_b32 v118, 16, v29\n\t"
    "v_and_b32 v117, s67, v28\n\t"
    "v_lshlrev_b32 v116, 16, v28\n\t"
    "v_add_u32_sdwa v112, s92, v112 dst_sel:DWORD dst_unused:UNUSED_PAD src0_sel:DWORD src1_sel:WORD_0\n\t"
    "s_waitcnt lgkmcnt(4)\n\t"
    "v_smfmac_f32_16x16x64_bf16 v[116:119], v[120:123], v[84:91], v124\n\t"
    "ds_read_b64 v[84:85], v112 offset:0\n\t"
    "ds_read_b64 v[86:87], v126 offset:0\n\t"
    "v_cvt_pk_bf16_f32 v80, v80, v81\n\t"
    "v_cvt_pk_bf16_f32 v81, v82, v83\n\t"
    "ds_write_b64 v125, v[80:81] offset:384\n\t"
    "v_add_u32_sdwa v90, s92, v113 dst_sel:DWORD dst_unused:UNUSED_PAD src0_sel:DWORD src1_sel:WORD_1\n\t"
    "v_add_u32_sdwa v113, s92, v113 dst_sel:DWORD dst_unused:UNUSED_PAD src0_sel:DWORD src1_sel:WORD_0\n\t"
    "ds_read_b64 v[88:89], v113 offset:0\n\t"
    "ds_read_b64 v[90:91], v90 offset:0\n\t"
    "v_add_u32_sdwa v126, s92, v114 dst_sel:DWORD dst_unused:UNUSED_PAD src0_sel:DWORD src1_sel:WORD_1\n\t"
    "v_add_u32_sdwa v114, s92, v114 dst_sel:DWORD dst_unused:UNUSED_PAD src0_sel:DWORD src1_sel:WORD_0\n\t"
    "s_waitcnt vmcnt(2) lgkmcnt(5)\n\t"
    "v_smfmac_f32_16x16x64_bf16 v[116:119], v[120:123], v[92:99], v124\n\t"
    "ds_read_b64 v[92:93], v114 offset:0\n\t"
    "ds_read_b64 v[94:95], v126 offset:0\n\t"
    "v_add_u32_sdwa v98, s92, v115 dst_sel:DWORD dst_unused:UNUSED_PAD src0_sel:DWORD src1_sel:WORD_1\n\t"
    "v_add_u32_sdwa v115, s92, v115 dst_sel:DWORD dst_unused:UNUSED_PAD src0_sel:DWORD src1_sel:WORD_0\n\t"
    "ds_read_b64 v[96:97], v115 offset:0\n\t"
    "ds_read_b64 v[98:99], v98 offset:0\n\t"
    "buffer_load_dwordx4 v[112:115], v127, %12, s89 offen offset:1024 sc1\n\t"
    "v_add_u32_sdwa v126, s92, v100 dst_sel:DWORD dst_unused:UNUSED_PAD src0_sel:DWORD src1_sel:WORD_1\n\t"
    "v_cvt_pk_bf16_f32 v28, v116, v117\n\t"
    "v_cvt_pk_bf16_f32 v29, v118, v119\n\t"
    "v_and_b32 v119, s67, v31\n\t"
    "v_lshlrev_b32 v118, 16, v31\n\t"
    "v_and_b32 v117, s67, v30\n\t"
    "v_lshlrev_b32 v116, 16, v30\n\t"
    "v_add_u32_sdwa v100, s92, v100 dst_sel:DWORD dst_unused:UNUSED_PAD src0_sel:DWORD src1_sel:WORD_0\n\t"
    "s_waitcnt lgkmcnt(4)\n\t"
    "v_smfmac_f32_16x16x64_bf16 v[116:119], v[120:123], v[84:91], v124\n\t"
    "ds_read_b64 v[84:85], v100 offset:0\n\t"
    "v_mfma_f32_16x16x32_bf16 v[80:83], v[64:67], v[196:199], 0\n\t"
    "ds_read_b64 v[86:87], v126 offset:0\n\t"
    "v_mfma_f32_16x16x32_bf16 v[80:83], v[68:71], v[200:203], v[80:83]\n\t"
    "v_add_u32_sdwa v90, s92, v101 dst_sel:DWORD dst_unused:UNUSED_PAD src0_sel:DWORD src1_sel:WORD_1\n\t"
    "v_add_u32_sdwa v101, s92, v101 dst_sel:DWORD dst_unused:UNUSED_PAD src0_sel:DWORD src1_sel:WORD_0\n\t"
    "ds_read_b64 v[88:89], v101 offset:0\n\t"
    "v_mfma_f32_16x16x32_bf16 v[80:83], v[72:75], v[252:255], v[80:83]\n\t"
    "ds_read_b64 v[90:91], v90 offset:0\n\t"
    "v_mfma_f32_16x16x32_bf16 v[80:83], v[76:79], v[128:131], v[80:83]\n\t"
    "v_add_u32_sdwa v126, s92, v102 dst_sel:DWORD dst_unused:UNUSED_PAD src0_sel:DWORD src1_sel:WORD_1\n\t"
    "v_add_u32_sdwa v102, s92, v102 dst_sel:DWORD dst_unused:UNUSED_PAD src0_sel:DWORD src1_sel:WORD_0\n\t"
    "s_xor_b32 s50, s50, 0x2000\n\t"
    "s_add_u32 m0, s50, 128000\n\t"
    "s_add_u32 s63, s63, 0x4000\n\t"
    "buffer_load_dwordx4 v127, %12, s63 offen sc1 lds\n\t"
    "s_waitcnt vmcnt(3) lgkmcnt(4)\n\t"
    "v_smfmac_f32_16x16x64_bf16 v[116:119], v[120:123], v[92:99], v124\n\t"
    "ds_read_b64 v[92:93], v102 offset:0\n\t"
    "ds_read_b64 v[94:95], v126 offset:0\n\t"
    "v_add_u32_sdwa v98, s92, v103 dst_sel:DWORD dst_unused:UNUSED_PAD src0_sel:DWORD src1_sel:WORD_1\n\t"
    "v_add_u32_sdwa v103, s92, v103 dst_sel:DWORD dst_unused:UNUSED_PAD src0_sel:DWORD src1_sel:WORD_0\n\t"
    "ds_read_b64 v[96:97], v103 offset:0\n\t"
    "ds_read_b64 v[98:99], v98 offset:0\n\t"
    "s_add_u32 s90, s90, s95\n\t"
    "buffer_load_dwordx4 v[100:103], v127, %12, s90 offen offset:0 sc1\n\t"
    "v_add_u32_sdwa v126, s92, v104 dst_sel:DWORD dst_unused:UNUSED_PAD src0_sel:DWORD src1_sel:WORD_1\n\t"
    "v_cvt_pk_bf16_f32 v30, v116, v117\n\t"
    "v_cvt_pk_bf16_f32 v31, v118, v119\n\t"
    "v_and_b32 v119, s67, v37\n\t"
    "v_lshlrev_b32 v118, 16, v37\n\t"
    "v_and_b32 v117, s67, v36\n\t"
    "v_lshlrev_b32 v116, 16, v36\n\t"
    "v_add_u32_sdwa v104, s92, v104 dst_sel:DWORD dst_unused:UNUSED_PAD src0_sel:DWORD src1_sel:WORD_0\n\t"
    "s_waitcnt lgkmcnt(4)\n\t"
    "v_smfmac_f32_16x16x64_bf16 v[116:119], v[120:123], v[84:91], v124\n\t"
    "ds_read_b64 v[84:85], v104 offset:0\n\t"
    "ds_read_b64 v[86:87], v126 offset:0\n\t"
    "v_cvt_pk_bf16_f32 v80, v80, v81\n\t"
    "v_cvt_pk_bf16_f32 v81, v82, v83\n\t"
    "ds_write_b64 v125, v[80:81] offset:512\n\t"
    "v_add_u32_sdwa v90, s92, v105 dst_sel:DWORD dst_unused:UNUSED_PAD src0_sel:DWORD src1_sel:WORD_1\n\t"
    "v_add_u32_sdwa v105, s92, v105 dst_sel:DWORD dst_unused:UNUSED_PAD src0_sel:DWORD src1_sel:WORD_0\n\t"
    "ds_read_b64 v[88:89], v105 offset:0\n\t"
    "ds_read_b64 v[90:91], v90 offset:0\n\t"
    "v_add_u32_sdwa v126, s92, v106 dst_sel:DWORD dst_unused:UNUSED_PAD src0_sel:DWORD src1_sel:WORD_1\n\t"
    "v_add_u32_sdwa v106, s92, v106 dst_sel:DWORD dst_unused:UNUSED_PAD src0_sel:DWORD src1_sel:WORD_0\n\t"
    "s_waitcnt vmcnt(3) lgkmcnt(5)\n\t"
    "v_smfmac_f32_16x16x64_bf16 v[116:119], v[120:123], v[92:99], v124\n\t"
    "ds_read_b64 v[92:93], v106 offset:0\n\t"
    "ds_read_b64 v[94:95], v126 offset:0\n\t"
    "v_add_u32_sdwa v98, s92, v107 dst_sel:DWORD dst_unused:UNUSED_PAD src0_sel:DWORD src1_sel:WORD_1\n\t"
    "v_add_u32_sdwa v107, s92, v107 dst_sel:DWORD dst_unused:UNUSED_PAD src0_sel:DWORD src1_sel:WORD_0\n\t"
    "ds_read_b64 v[96:97], v107 offset:0\n\t"
    "ds_read_b64 v[98:99], v98 offset:0\n\t"
    "buffer_load_dwordx4 v[104:107], v127, %12, s90 offen offset:1024 sc1\n\t"
    "v_add_u32_sdwa v126, s92, v108 dst_sel:DWORD dst_unused:UNUSED_PAD src0_sel:DWORD src1_sel:WORD_1\n\t"
    "v_cvt_pk_bf16_f32 v36, v116, v117\n\t"
    "v_cvt_pk_bf16_f32 v37, v118, v119\n\t"
    "v_and_b32 v119, s67, v39\n\t"
    "v_lshlrev_b32 v118, 16, v39\n\t"
    "v_and_b32 v117, s67, v38\n\t"
    "v_lshlrev_b32 v116, 16, v38\n\t"
    "v_add_u32_sdwa v108, s92, v108 dst_sel:DWORD dst_unused:UNUSED_PAD src0_sel:DWORD src1_sel:WORD_0\n\t"
    "s_waitcnt lgkmcnt(4)\n\t"
    "v_smfmac_f32_16x16x64_bf16 v[116:119], v[120:123], v[84:91], v124\n\t"
    "ds_read_b64 v[84:85], v108 offset:0\n\t"
    "v_mfma_f32_16x16x32_bf16 v[80:83], v[64:67], v[204:207], 0\n\t"
    "ds_read_b64 v[86:87], v126 offset:0\n\t"
    "v_mfma_f32_16x16x32_bf16 v[80:83], v[68:71], v[208:211], v[80:83]\n\t"
    "v_add_u32_sdwa v90, s92, v109 dst_sel:DWORD dst_unused:UNUSED_PAD src0_sel:DWORD src1_sel:WORD_1\n\t"
    "v_add_u32_sdwa v109, s92, v109 dst_sel:DWORD dst_unused:UNUSED_PAD src0_sel:DWORD src1_sel:WORD_0\n\t"
    "ds_read_b64 v[88:89], v109 offset:0\n\t"
    "v_mfma_f32_16x16x32_bf16 v[80:83], v[72:75], v[132:135], v[80:83]\n\t"
    "ds_read_b64 v[90:91], v90 offset:0\n\t"
    "v_mfma_f32_16x16x32_bf16 v[80:83], v[76:79], v[136:139], v[80:83]\n\t"
    "v_add_u32_sdwa v126, s92, v110 dst_sel:DWORD dst_unused:UNUSED_PAD src0_sel:DWORD src1_sel:WORD_1\n\t"
    "v_add_u32_sdwa v110, s92, v110 dst_sel:DWORD dst_unused:UNUSED_PAD src0_sel:DWORD src1_sel:WORD_0\n\t"
    "s_cmp_lt_u32 %16, 4\n\t"
    "s_cbranch_scc1 L_tp19_%=\n\t"
    "s_setprio 0\n\t"
    "L_tp19_%=:\n\t"
    "s_waitcnt vmcnt(3) lgkmcnt(4)\n\t"
    "v_smfmac_f32_16x16x64_bf16 v[116:119], v[120:123], v[92:99], v124\n\t"
    "ds_read_b64 v[92:93], v110 offset:0\n\t"
    "ds_read_b64 v[94:95], v126 offset:0\n\t"
    "v_add_u32_sdwa v98, s92, v111 dst_sel:DWORD dst_unused:UNUSED_PAD src0_sel:DWORD src1_sel:WORD_1\n\t"
    "v_add_u32_sdwa v111, s92, v111 dst_sel:DWORD dst_unused:UNUSED_PAD src0_sel:DWORD src1_sel:WORD_0\n\t"
    "ds_read_b64 v[96:97], v111 offset:0\n\t"
    "ds_read_b64 v[98:99], v98 offset:0\n\t"
    "s_add_u32 s91, s91, s95\n\t"
    "buffer_load_dwordx4 v[108:111], v127, %12, s91 offen offset:0 sc1\n\t"
    "v_add_u32_sdwa v126, s92, v112 dst_sel:DWORD dst_unused:UNUSED_PAD src0_sel:DWORD src1_sel:WORD_1\n\t"
    "v_cvt_pk_bf16_f32 v38, v116, v117\n\t"
    "v_cvt_pk_bf16_f32 v39, v118, v119\n\t"
    "v_and_b32 v119, s67, v45\n\t"
    "v_lshlrev_b32 v118, 16, v45\n\t"
    "v_and_b32 v117, s67, v44\n\t"
    "v_lshlrev_b32 v116, 16, v44\n\t"
    "v_add_u32_sdwa v112, s92, v112 dst_sel:DWORD dst_unused:UNUSED_PAD src0_sel:DWORD src1_sel:WORD_0\n\t"
    "s_waitcnt lgkmcnt(4)\n\t"
    "v_smfmac_f32_16x16x64_bf16 v[116:119], v[120:123], v[84:91], v124\n\t"
    "ds_read_b64 v[84:85], v112 offset:0\n\t"
    "ds_read_b64 v[86:87], v126 offset:0\n\t"
    "v_cvt_pk_bf16_f32 v80, v80, v81\n\t"
    "v_cvt_pk_bf16_f32 v81, v82, v83\n\t"
    "ds_write_b64 v125, v[80:81] offset:640\n\t"
    "v_add_u32_sdwa v90, s92, v113 dst_sel:DWORD dst_unused:UNUSED_PAD src0_sel:DWORD src1_sel:WORD_1\n\t"
    "v_add_u32_sdwa v113, s92, v113 dst_sel:DWORD dst_unused:UNUSED_PAD src0_sel:DWORD src1_sel:WORD_0\n\t"
    "ds_read_b64 v[88:89], v113 offset:0\n\t"
    "ds_read_b64 v[90:91], v90 offset:0\n\t"
    "v_add_u32_sdwa v126, s92, v114 dst_sel:DWORD dst_unused:UNUSED_PAD src0_sel:DWORD src1_sel:WORD_1\n\t"
    "v_add_u32_sdwa v114, s92, v114 dst_sel:DWORD dst_unused:UNUSED_PAD src0_sel:DWORD src1_sel:WORD_0\n\t"
    "s_waitcnt vmcnt(2) lgkmcnt(5)\n\t"
    "v_smfmac_f32_16x16x64_bf16 v[116:119], v[120:123], v[92:99], v124\n\t"
    "ds_read_b64 v[92:93], v114 offset:0\n\t"
    "ds_read_b64 v[94:95], v126 offset:0\n\t"
    "v_add_u32_sdwa v98, s92, v115 dst_sel:DWORD dst_unused:UNUSED_PAD src0_sel:DWORD src1_sel:WORD_1\n\t"
    "v_add_u32_sdwa v115, s92, v115 dst_sel:DWORD dst_unused:UNUSED_PAD src0_sel:DWORD src1_sel:WORD_0\n\t"
    "ds_read_b64 v[96:97], v115 offset:0\n\t"
    "ds_read_b64 v[98:99], v98 offset:0\n\t"
    "buffer_load_dwordx4 v[112:115], v127, %12, s91 offen offset:1024 sc1\n\t"
    "v_add_u32_sdwa v126, s92, v100 dst_sel:DWORD dst_unused:UNUSED_PAD src0_sel:DWORD src1_sel:WORD_1\n\t"
    "v_cvt_pk_bf16_f32 v44, v116, v117\n\t"
    "v_cvt_pk_bf16_f32 v45, v118, v119\n\t"
    "v_and_b32 v119, s67, v47\n\t"
    "v_lshlrev_b32 v118, 16, v47\n\t"
    "v_and_b32 v117, s67, v46\n\t"
    "v_lshlrev_b32 v116, 16, v46\n\t"
    "v_add_u32_sdwa v100, s92, v100 dst_sel:DWORD dst_unused:UNUSED_PAD src0_sel:DWORD src1_sel:WORD_0\n\t"
    "s_waitcnt lgkmcnt(4)\n\t"
    "v_smfmac_f32_16x16x64_bf16 v[116:119], v[120:123], v[84:91], v124\n\t"
    "ds_read_b64 v[84:85], v100 offset:0\n\t"
    "v_mfma_f32_16x16x32_bf16 v[80:83], v[64:67], v[156:159], 0\n\t"
    "ds_read_b64 v[86:87], v126 offset:0\n\t"
    "v_mfma_f32_16x16x32_bf16 v[80:83], v[68:71], v[160:163], v[80:83]\n\t"
    "v_add_u32_sdwa v90, s92, v101 dst_sel:DWORD dst_unused:UNUSED_PAD src0_sel:DWORD src1_sel:WORD_1\n\t"
    "v_add_u32_sdwa v101, s92, v101 dst_sel:DWORD dst_unused:UNUSED_PAD src0_sel:DWORD src1_sel:WORD_0\n\t"
    "ds_read_b64 v[88:89], v101 offset:0\n\t"
    "v_mfma_f32_16x16x32_bf16 v[80:83], v[72:75], v[140:143], v[80:83]\n\t"
    "ds_read_b64 v[90:91], v90 offset:0\n\t"
    "v_mfma_f32_16x16x32_bf16 v[80:83], v[76:79], v[144:147], v[80:83]\n\t"
    "v_add_u32_sdwa v126, s92, v102 dst_sel:DWORD dst_unused:UNUSED_PAD src0_sel:DWORD src1_sel:WORD_1\n\t"
    "v_add_u32_sdwa v102, s92, v102 dst_sel:DWORD dst_unused:UNUSED_PAD src0_sel:DWORD src1_sel:WORD_0\n\t"
    "s_waitcnt vmcnt(2) lgkmcnt(4)\n\t"
    "v_smfmac_f32_16x16x64_bf16 v[116:119], v[120:123], v[92:99], v124\n\t"
    "ds_read_b64 v[92:93], v102 offset:0\n\t"
    "ds_read_b64 v[94:95], v126 offset:0\n\t"
    "v_add_u32_sdwa v98, s92, v103 dst_sel:DWORD dst_unused:UNUSED_PAD src0_sel:DWORD src1_sel:WORD_1\n\t"
    "v_add_u32_sdwa v103, s92, v103 dst_sel:DWORD dst_unused:UNUSED_PAD src0_sel:DWORD src1_sel:WORD_0\n\t"
    "ds_read_b64 v[96:97], v103 offset:0\n\t"
    "ds_read_b64 v[98:99], v98 offset:0\n\t"
    "s_add_u32 s84, s84, s94\n\t"
    "buffer_load_dwordx4 v[100:103], v127, %12, s84 offen offset:0 sc1\n\t"
    "v_add_u32_sdwa v126, s92, v104 dst_sel:DWORD dst_unused:UNUSED_PAD src0_sel:DWORD src1_sel:WORD_1\n\t"
    "v_cvt_pk_bf16_f32 v46, v116, v117\n\t"
    "v_cvt_pk_bf16_f32 v47, v118, v119\n\t"
    "v_and_b32 v119, s67, v53\n\t"
    "v_lshlrev_b32 v118, 16, v53\n\t"
    "v_and_b32 v117, s67, v52\n\t"
    "v_lshlrev_b32 v116, 16, v52\n\t"
    "v_add_u32_sdwa v104, s92, v104 dst_sel:DWORD dst_unused:UNUSED_PAD src0_sel:DWORD src1_sel:WORD_0\n\t"
    "s_waitcnt lgkmcnt(4)\n\t"
    "v_smfmac_f32_16x16x64_bf16 v[116:119], v[120:123], v[84:91], v124\n\t"
    "ds_read_b64 v[84:85], v104 offset:0\n\t"
    "ds_read_b64 v[86:87], v126 offset:0\n\t"
    "v_cvt_pk_bf16_f32 v80, v80, v81\n\t"
    "v_cvt_pk_bf16_f32 v81, v82, v83\n\t"
    "ds_write_b64 v125, v[80:81] offset:768\n\t"
    "v_add_u32_sdwa v90, s92, v105 dst_sel:DWORD dst_unused:UNUSED_PAD src0_sel:DWORD src1_sel:WORD_1\n\t"
    "v_add_u32_sdwa v105, s92, v105 dst_sel:DWORD dst_unused:UNUSED_PAD src0_sel:DWORD src1_sel:WORD_0\n\t"
    "ds_read_b64 v[88:89], v105 offset:0\n\t"
    "ds_read_b64 v[90:91], v90 offset:0\n\t"
    "v_add_u32_sdwa v126, s92, v106 dst_sel:DWORD dst_unused:UNUSED_PAD src0_sel:DWORD src1_sel:WORD_1\n\t"
    "v_add_u32_sdwa v106, s92, v106 dst_sel:DWORD dst_unused:UNUSED_PAD src0_sel:DWORD src1_sel:WORD_0\n\t"
    "s_waitcnt vmcnt(2) lgkmcnt(5)\n\t"
    "v_smfmac_f32_16x16x64_bf16 v[116:119], v[120:123], v[92:99], v124\n\t"
    "ds_read_b64 v[92:93], v106 offset:0\n\t"
    "ds_read_b64 v[94:95], v126 offset:0\n\t"
    "v_add_u32_sdwa v98, s92, v107 dst_sel:DWORD dst_unused:UNUSED_PAD src0_sel:DWORD src1_sel:WORD_1\n\t"
    "v_add_u32_sdwa v107, s92, v107 dst_sel:DWORD dst_unused:UNUSED_PAD src0_sel:DWORD src1_sel:WORD_0\n\t"
    "ds_read_b64 v[96:97], v107 offset:0\n\t"
    "ds_read_b64 v[98:99], v98 offset:0\n\t"
    "buffer_load_dwordx4 v[104:107], v127, %12, s84 offen offset:1024 sc1\n\t"
    "v_add_u32_sdwa v126, s92, v108 dst_sel:DWORD dst_unused:UNUSED_PAD src0_sel:DWORD src1_sel:WORD_1\n\t"
    "v_cvt_pk_bf16_f32 v52, v116, v117\n\t"
    "v_cvt_pk_bf16_f32 v53, v118, v119\n\t"
    "v_and_b32 v119, s67, v55\n\t"
    "v_lshlrev_b32 v118, 16, v55\n\t"
    "v_and_b32 v117, s67, v54\n\t"
    "v_lshlrev_b32 v116, 16, v54\n\t"
    "v_add_u32_sdwa v108, s92, v108 dst_sel:DWORD dst_unused:UNUSED_PAD src0_sel:DWORD src1_sel:WORD_0\n\t"
    "s_waitcnt lgkmcnt(4)\n\t"
    "v_smfmac_f32_16x16x64_bf16 v[116:119], v[120:123], v[84:91], v124\n\t"
    "ds_read_b64 v[84:85], v108 offset:0\n\t"
    "v_mfma_f32_16x16x32_bf16 v[80:83], v[64:67], v[212:215], 0\n\t"
    "ds_read_b64 v[86:87], v126 offset:0\n\t"
    "v_mfma_f32_16x16x32_bf16 v[80:83], v[68:71], v[216:219], v[80:83]\n\t"
    "v_add_u32_sdwa v90, s92, v109 dst_sel:DWORD dst_unused:UNUSED_PAD src0_sel:DWORD src1_sel:WORD_1\n\t"
    "v_add_u32_sdwa v109, s92, v109 dst_sel:DWORD dst_unused:UNUSED_PAD src0_sel:DWORD src1_sel:WORD_0\n\t"
    "ds_read_b64 v[88:89], v109 offset:0\n\t"
    "v_mfma_f32_16x16x32_bf16 v[80:83], v[72:75], v[148:151], v[80:83]\n\t"
    "ds_read_b64 v[90:91], v90 offset:0\n\t"
    "v_mfma_f32_16x16x32_bf16 v[80:83], v[76:79], v[152:155], v[80:83]\n\t"
    "v_add_u32 v79, s96, v127\n\t"
    "s_add_u32 s96, s96, 0x1000\n\t"
    "s_and_b32 s96, s96, 0x3000\n\t"
    "v_add_u32 v79, 128000, v79\n\t"
    "ds_read_b128 v[64:67], v79 offset:0\n\t"
    "ds_read_b128 v[68:71], v79 offset:1024\n\t"
    "ds_read_b128 v[72:75], v79 offset:2048\n\t"
    "ds_read_b128 v[76:79], v79 offset:3072\n\t"
    "v_add_u32_sdwa v126, s92, v110 dst_sel:DWORD dst_unused:UNUSED_PAD src0_sel:DWORD src1_sel:WORD_1\n\t"
    "v_add_u32_sdwa v110, s92, v110 dst_sel:DWORD dst_unused:UNUSED_PAD src0_sel:DWORD src1_sel:WORD_0\n\t"
    "s_waitcnt vmcnt(2) lgkmcnt(8)\n\t"
    "v_smfmac_f32_16x16x64_bf16 v[116:119], v[120:123], v[92:99], v124\n\t"
    "ds_read_b64 v[92:93], v110 offset:0\n\t"
    "ds_read_b64 v[94:95], v126 offset:0\n\t"
    "v_add_u32_sdwa v98, s92, v111 dst_sel:DWORD dst_unused:UNUSED_PAD src0_sel:DWORD src1_sel:WORD_1\n\t"
    "v_add_u32_sdwa v111, s92, v111 dst_sel:DWORD dst_unused:UNUSED_PAD src0_sel:DWORD src1_sel:WORD_0\n\t"
    "ds_read_b64 v[96:97], v111 offset:0\n\t"
    "ds_read_b64 v[98:99], v98 offset:0\n\t"
    "s_add_u32 s85, s85, s94\n\t"
    "buffer_load_dwordx4 v[108:111], v127, %12, s85 offen offset:0 sc1\n\t"
    "v_add_u32_sdwa v126, s92, v112 dst_sel:DWORD dst_unused:UNUSED_PAD src0_sel:DWORD src1_sel:WORD_1\n\t"
    "v_cvt_pk_bf16_f32 v54, v116, v117\n\t"
    "v_cvt_pk_bf16_f32 v55, v118, v119\n\t"
    "v_and_b32 v119, s67, v61\n\t"
    "v_lshlrev_b32 v118, 16, v61\n\t"
    "v_and_b32 v117, s67, v60\n\t"
    "v_lshlrev_b32 v116, 16, v60\n\t"
    "v_add_u32_sdwa v112, s92, v112 dst_sel:DWORD dst_unused:UNUSED_PAD src0_sel:DWORD src1_sel:WORD_0\n\t"
    "s_waitcnt lgkmcnt(8)\n\t"
    "v_smfmac_f32_16x16x64_bf16 v[116:119], v[120:123], v[84:91], v124\n\t"
    "ds_read_b64 v[84:85], v112 offset:0\n\t"
    "ds_read_b64 v[86:87], v126 offset:0\n\t"
    "v_cvt_pk_bf16_f32 v80, v80, v81\n\t"
    "v_cvt_pk_bf16_f32 v81, v82, v83\n\t"
    "ds_write_b64 v125, v[80:81] offset:872\n\t"
    "v_add_u32_sdwa v90, s92, v113 dst_sel:DWORD dst_unused:UNUSED_PAD src0_sel:DWORD src1_sel:WORD_1\n\t"
    "v_add_u32_sdwa v113, s92, v113 dst_sel:DWORD dst_unused:UNUSED_PAD src0_sel:DWORD src1_sel:WORD_0\n\t"
    "ds_read_b64 v[88:89], v113 offset:0\n\t"
    "ds_read_b64 v[90:91], v90 offset:0\n\t"
    "v_add_u32_sdwa v126, s92, v114 dst_sel:DWORD dst_unused:UNUSED_PAD src0_sel:DWORD src1_sel:WORD_1\n\t"
    "v_add_u32_sdwa v114, s92, v114 dst_sel:DWORD dst_unused:UNUSED_PAD src0_sel:DWORD src1_sel:WORD_0\n\t"
    "s_waitcnt vmcnt(2) lgkmcnt(5)\n\t"
    "v_smfmac_f32_16x16x64_bf16 v[116:119], v[120:123], v[92:99], v124\n\t"
    "ds_read_b64 v[92:93], v114 offset:0\n\t"
    "ds_read_b64 v[94:95], v126 offset:0\n\t"
    "v_add_u32_sdwa v98, s92, v115 dst_sel:DWORD dst_unused:UNUSED_PAD src0_sel:DWORD src1_sel:WORD_1\n\t"
    "v_add_u32_sdwa v115, s92, v115 dst_sel:DWORD dst_unused:UNUSED_PAD src0_sel:DWORD src1_sel:WORD_0\n\t"
    "ds_read_b64 v[96:97], v115 offset:0\n\t"
    "ds_read_b64 v[98:99], v98 offset:0\n\t"
    "buffer_load_dwordx4 v[112:115], v127, %12, s85 offen offset:1024 sc1\n\t"
    "v_add_u32_sdwa v126, s92, v100 dst_sel:DWORD dst_unused:UNUSED_PAD src0_sel:DWORD src1_sel:WORD_1\n\t"
    "v_cvt_pk_bf16_f32 v60, v116, v117\n\t"
    "v_cvt_pk_bf16_f32 v61, v118, v119\n\t"
    "v_and_b32 v119, s67, v63\n\t"
    "v_lshlrev_b32 v118, 16, v63\n\t"
    "v_and_b32 v117, s67, v62\n\t"
    "v_lshlrev_b32 v116, 16, v62\n\t"
    "v_add_u32_sdwa v100, s92, v100 dst_sel:DWORD dst_unused:UNUSED_PAD src0_sel:DWORD src1_sel:WORD_0\n\t"
    "s_waitcnt lgkmcnt(4)\n\t"
    "v_smfmac_f32_16x16x64_bf16 v[116:119], v[120:123], v[84:91], v124\n\t"
    "ds_read_b64 v[84:85], v100 offset:0\n\t"
    "ds_read_b64 v[86:87], v126 offset:0\n\t"
    "v_add_u32_sdwa v90, s92, v101 dst_sel:DWORD dst_unused:UNUSED_PAD src0_sel:DWORD src1_sel:WORD_1\n\t"
    "v_add_u32_sdwa v101, s92, v101 dst_sel:DWORD dst_unused:UNUSED_PAD src0_sel:DWORD src1_sel:WORD_0\n\t"
    "ds_read_b64 v[88:89], v101 offset:0\n\t"
    "ds_read_b64 v[90:91], v90 offset:0\n\t"
    "v_add_u32_sdwa v126, s92, v102 dst_sel:DWORD dst_unused:UNUSED_PAD src0_sel:DWORD src1_sel:WORD_1\n\t"
    "v_add_u32_sdwa v102, s92, v102 dst_sel:DWORD dst_unused:UNUSED_PAD src0_sel:DWORD src1_sel:WORD_0\n\t"
    "s_cmp_lt_u32 %16, 4\n\t"
    "s_cbranch_scc1 L_tp31_%=\n\t"
    "s_setprio 1\n\t"
    "L_tp31_%=:\n\t"
    "s_waitcnt vmcnt(2) lgkmcnt(4)\n\t"
    "v_smfmac_f32_16x16x64_bf16 v[116:119], v[120:123], v[92:99], v124\n\t"
    "ds_read_b64 v[92:93], v102 offset:0\n\t"
    "ds_read_b64 v[94:95], v126 offset:0\n\t"
    "v_add_u32_sdwa v98, s92, v103 dst_sel:DWORD dst_unused:UNUSED_PAD src0_sel:DWORD src1_sel:WORD_1\n\t"
    "v_add_u32_sdwa v103, s92, v103 dst_sel:DWORD dst_unused:UNUSED_PAD src0_sel:DWORD src1_sel:WORD_0\n\t"
    "ds_read_b64 v[96:97], v103 offset:0\n\t"
    "ds_read_b64 v[98:99], v98 offset:0\n\t"
    "s_add_u32 s86, s86, s94\n\t"
    "buffer_load_dwordx4 v[100:103], v127, %12, s86 offen offset:0 sc1\n\t"
    "v_add_u32_sdwa v126, s92, v104 dst_sel:DWORD dst_unused:UNUSED_PAD src0_sel:DWORD src1_sel:WORD_1\n\t"
    "v_cvt_pk_bf16_f32 v62, v116, v117\n\t"
    "v_cvt_pk_bf16_f32 v63, v118, v119\n\t"
    "v_and_b32 v119, s67, v1\n\t"
    "v_lshlrev_b32 v118, 16, v1\n\t"
    "v_and_b32 v117, s67, v0\n\t"
    "v_lshlrev_b32 v116, 16, v0\n\t"
    "v_add_u32_sdwa v104, s92, v104 dst_sel:DWORD dst_unused:UNUSED_PAD src0_sel:DWORD src1_sel:WORD_0\n\t"
    "s_waitcnt lgkmcnt(4)\n\t"
    "v_smfmac_f32_16x16x64_bf16 v[116:119], v[120:123], v[84:91], v124\n\t"
    "ds_read_b64 v[84:85], v104 offset:0\n\t"
    "v_mfma_f32_16x16x32_bf16 v[80:83], v[64:67], v[164:167], 0\n\t"
    "ds_read_b64 v[86:87], v126 offset:0\n\t"
    "v_mfma_f32_16x16x32_bf16 v[80:83], v[68:71], v[168:171], v[80:83]\n\t"
    "v_add_u32_sdwa v90, s92, v105 dst_sel:DWORD dst_unused:UNUSED_PAD src0_sel:DWORD src1_sel:WORD_1\n\t"
    "v_add_u32_sdwa v105, s92, v105 dst_sel:DWORD dst_unused:UNUSED_PAD src0_sel:DWORD src1_sel:WORD_0\n\t"
    "ds_read_b64 v[88:89], v105 offset:0\n\t"
    "v_mfma_f32_16x16x32_bf16 v[80:83], v[72:75], v[220:223], v[80:83]\n\t"
    "ds_read_b64 v[90:91], v90 offset:0\n\t"
    "v_mfma_f32_16x16x32_bf16 v[80:83], v[76:79], v[224:227], v[80:83]\n\t"
    "v_add_u32_sdwa v126, s92, v106 dst_sel:DWORD dst_unused:UNUSED_PAD src0_sel:DWORD src1_sel:WORD_1\n\t"
    "v_add_u32_sdwa v106, s92, v106 dst_sel:DWORD dst_unused:UNUSED_PAD src0_sel:DWORD src1_sel:WORD_0\n\t"
    "s_waitcnt vmcnt(2) lgkmcnt(4)\n\t"
    "v_smfmac_f32_16x16x64_bf16 v[116:119], v[120:123], v[92:99], v124\n\t"
    "ds_read_b64 v[92:93], v106 offset:0\n\t"
    "ds_read_b64 v[94:95], v126 offset:0\n\t"
    "v_add_u32_sdwa v98, s92, v107 dst_sel:DWORD dst_unused:UNUSED_PAD src0_sel:DWORD src1_sel:WORD_1\n\t"
    "v_add_u32_sdwa v107, s92, v107 dst_sel:DWORD dst_unused:UNUSED_PAD src0_sel:DWORD src1_sel:WORD_0\n\t"
    "ds_read_b64 v[96:97], v107 offset:0\n\t"
    "ds_read_b64 v[98:99], v98 offset:0\n\t"
    "buffer_load_dwordx4 v[104:107], v127, %12, s86 offen offset:1024 sc1\n\t"
    "v_add_u32_sdwa v126, s92, v108 dst_sel:DWORD dst_unused:UNUSED_PAD src0_sel:DWORD src1_sel:WORD_1\n\t"
    "v_cvt_pk_bf16_f32 v0, v116, v117\n\t"
    "v_cvt_pk_bf16_f32 v1, v118, v119\n\t"
    "v_and_b32 v119, s67, v3\n\t"
    "v_lshlrev_b32 v118, 16, v3\n\t"
    "v_and_b32 v117, s67, v2\n\t"
    "v_lshlrev_b32 v116, 16, v2\n\t"
    "v_add_u32_sdwa v108, s92, v108 dst_sel:DWORD dst_unused:UNUSED_PAD src0_sel:DWORD src1_sel:WORD_0\n\t"
    "s_waitcnt lgkmcnt(4)\n\t"
    "v_smfmac_f32_16x16x64_bf16 v[116:119], v[120:123], v[84:91], v124\n\t"
    "ds_read_b64 v[84:85], v108 offset:0\n\t"
    "ds_read_b64 v[86:87], v126 offset:0\n\t"
    "v_cvt_pk_bf16_f32 v80, v80, v81\n\t"
    "v_cvt_pk_bf16_f32 v81, v82, v83\n\t"
    "ds_write_b64 v125, v[80:81] offset:32000\n\t"
    "v_mfma_f32_16x16x32_bf16 v[80:83], v[64:67], v[172:175], 0\n\t"
    "v_add_u32_sdwa v90, s92, v109 dst_sel:DWORD dst_unused:UNUSED_PAD src0_sel:DWORD src1_sel:WORD_1\n\t"
    "v_add_u32_sdwa v109, s92, v109 dst_sel:DWORD dst_unused:UNUSED_PAD src0_sel:DWORD src1_sel:WORD_0\n\t"
    "ds_read_b64 v[88:89], v109 offset:0\n\t"
    "v_mfma_f32_16x16x32_bf16 v[80:83], v[68:71], v[176:179], v[80:83]\n\t"
    "ds_read_b64 v[90:91], v90 offset:0\n\t"
    "v_mfma_f32_16x16x32_bf16 v[80:83], v[72:75], v[228:231], v[80:83]\n\t"
    "v_mfma_f32_16x16x32_bf16 v[80:83], v[76:79], v[232:235], v[80:83]\n\t"
    "v_add_u32_sdwa v126, s92, v110 dst_sel:DWORD dst_unused:UNUSED_PAD src0_sel:DWORD src1_sel:WORD_1\n\t"
    "v_add_u32_sdwa v110, s92, v110 dst_sel:DWORD dst_unused:UNUSED_PAD src0_sel:DWORD src1_sel:WORD_0\n\t"
    "s_waitcnt vmcnt(2) lgkmcnt(5)\n\t"
    "v_smfmac_f32_16x16x64_bf16 v[116:119], v[120:123], v[92:99], v124\n\t"
    "ds_read_b64 v[92:93], v110 offset:0\n\t"
    "ds_read_b64 v[94:95], v126 offset:0\n\t"
    "v_add_u32_sdwa v98, s92, v111 dst_sel:DWORD dst_unused:UNUSED_PAD src0_sel:DWORD src1_sel:WORD_1\n\t"
    "v_add_u32_sdwa v111, s92, v111 dst_sel:DWORD dst_unused:UNUSED_PAD src0_sel:DWORD src1_sel:WORD_0\n\t"
    "ds_read_b64 v[96:97], v111 offset:0\n\t"
    "ds_read_b64 v[98:99], v98 offset:0\n\t"
    "s_add_u32 s87, s87, s94\n\t"
    "buffer_load_dwordx4 v[108:111], v127, %12, s87 offen offset:0 sc1\n\t"
    "v_add_u32_sdwa v126, s92, v112 dst_sel:DWORD dst_unused:UNUSED_PAD src0_sel:DWORD src1_sel:WORD_1\n\t"
    "v_cvt_pk_bf16_f32 v2, v116, v117\n\t"
    "v_cvt_pk_bf16_f32 v3, v118, v119\n\t"
    "v_and_b32 v119, s67, v9\n\t"
    "v_lshlrev_b32 v118, 16, v9\n\t"
    "v_and_b32 v117, s67, v8\n\t"
    "v_lshlrev_b32 v116, 16, v8\n\t"
    "v_add_u32_sdwa v112, s92, v112 dst_sel:DWORD dst_unused:UNUSED_PAD src0_sel:DWORD src1_sel:WORD_0\n\t"
    "s_waitcnt lgkmcnt(4)\n\t"
    "v_smfmac_f32_16x16x64_bf16 v[116:119], v[120:123], v[84:91], v124\n\t"
    "ds_read_b64 v[84:85], v112 offset:0\n\t"
    "ds_read_b64 v[86:87], v126 offset:0\n\t"
    "v_cvt_pk_bf16_f32 v80, v80, v81\n\t"
    "v_cvt_pk_bf16_f32 v81, v82, v83\n\t"
    "ds_write_b64 v125, v[80:81] offset:32128\n\t"
    "v_add_u32_sdwa v90, s92, v113 dst_sel:DWORD dst_unused:UNUSED_PAD src0_sel:DWORD src1_sel:WORD_1\n\t"
    "v_add_u32_sdwa v113, s92, v113 dst_sel:DWORD dst_unused:UNUSED_PAD src0_sel:DWORD src1_sel:WORD_0\n\t"
    "ds_read_b64 v[88:89], v113 offset:0\n\t"
    "ds_read_b64 v[90:91], v90 offset:0\n\t"
    "v_add_u32_sdwa v126, s92, v114 dst_sel:DWORD dst_unused:UNUSED_PAD src0_sel:DWORD src1_sel:WORD_1\n\t"
    "v_add_u32_sdwa v114, s92, v114 dst_sel:DWORD dst_unused:UNUSED_PAD src0_sel:DWORD src1_sel:WORD_0\n\t"
    "s_waitcnt vmcnt(2) lgkmcnt(5)\n\t"
    "v_smfmac_f32_16x16x64_bf16 v[116:119], v[120:123], v[92:99], v124\n\t"
    "ds_read_b64 v[92:93], v114 offset:0\n\t"
    "ds_read_b64 v[94:95], v126 offset:0\n\t"
    "v_add_u32_sdwa v98, s92, v115 dst_sel:DWORD dst_unused:UNUSED_PAD src0_sel:DWORD src1_sel:WORD_1\n\t"
    "v_add_u32_sdwa v115, s92, v115 dst_sel:DWORD dst_unused:UNUSED_PAD src0_sel:DWORD src1_sel:WORD_0\n\t"
    "ds_read_b64 v[96:97], v115 offset:0\n\t"
    "ds_read_b64 v[98:99], v98 offset:0\n\t"
    "buffer_load_dwordx4 v[112:115], v127, %12, s87 offen offset:1024 sc1\n\t"
    "v_add_u32_sdwa v126, s92, v100 dst_sel:DWORD dst_unused:UNUSED_PAD src0_sel:DWORD src1_sel:WORD_1\n\t"
    "v_cvt_pk_bf16_f32 v8, v116, v117\n\t"
    "v_cvt_pk_bf16_f32 v9, v118, v119\n\t"
    "v_and_b32 v119, s67, v11\n\t"
    "v_lshlrev_b32 v118, 16, v11\n\t"
    "v_and_b32 v117, s67, v10\n\t"
    "v_lshlrev_b32 v116, 16, v10\n\t"
    "v_add_u32_sdwa v100, s92, v100 dst_sel:DWORD dst_unused:UNUSED_PAD src0_sel:DWORD src1_sel:WORD_0\n\t"
    "s_waitcnt lgkmcnt(4)\n\t"
    "v_smfmac_f32_16x16x64_bf16 v[116:119], v[120:123], v[84:91], v124\n\t"
    "ds_read_b64 v[84:85], v100 offset:0\n\t"
    "v_mfma_f32_16x16x32_bf16 v[80:83], v[64:67], v[180:183], 0\n\t"
    "ds_read_b64 v[86:87], v126 offset:0\n\t"
    "v_mfma_f32_16x16x32_bf16 v[80:83], v[68:71], v[184:187], v[80:83]\n\t"
    "v_add_u32_sdwa v90, s92, v101 dst_sel:DWORD dst_unused:UNUSED_PAD src0_sel:DWORD src1_sel:WORD_1\n\t"
    "v_add_u32_sdwa v101, s92, v101 dst_sel:DWORD dst_unused:UNUSED_PAD src0_sel:DWORD src1_sel:WORD_0\n\t"
    "ds_read_b64 v[88:89], v101 offset:0\n\t"
    "v_mfma_f32_16x16x32_bf16 v[80:83], v[72:75], v[236:239], v[80:83]\n\t"
    "ds_read_b64 v[90:91], v90 offset:0\n\t"
    "v_mfma_f32_16x16x32_bf16 v[80:83], v[76:79], v[240:243], v[80:83]\n\t"
    "v_add_u32_sdwa v126, s92, v102 dst_sel:DWORD dst_unused:UNUSED_PAD src0_sel:DWORD src1_sel:WORD_1\n\t"
    "v_add_u32_sdwa v102, s92, v102 dst_sel:DWORD dst_unused:UNUSED_PAD src0_sel:DWORD src1_sel:WORD_0\n\t"
    "s_waitcnt vmcnt(2) lgkmcnt(4)\n\t"
    "v_smfmac_f32_16x16x64_bf16 v[116:119], v[120:123], v[92:99], v124\n\t"
    "ds_read_b64 v[92:93], v102 offset:0\n\t"
    "ds_read_b64 v[94:95], v126 offset:0\n\t"
    "v_add_u32_sdwa v98, s92, v103 dst_sel:DWORD dst_unused:UNUSED_PAD src0_sel:DWORD src1_sel:WORD_1\n\t"
    "v_add_u32_sdwa v103, s92, v103 dst_sel:DWORD dst_unused:UNUSED_PAD src0_sel:DWORD src1_sel:WORD_0\n\t"
    "ds_read_b64 v[96:97], v103 offset:0\n\t"
    "ds_read_b64 v[98:99], v98 offset:0\n\t"
    "s_add_u32 s88, s88, s94\n\t"
    "buffer_load_dwordx4 v[100:103], v127, %12, s88 offen offset:0 sc1\n\t"
    "v_add_u32_sdwa v126, s92, v104 dst_sel:DWORD dst_unused:UNUSED_PAD src0_sel:DWORD src1_sel:WORD_1\n\t"
    "v_cvt_pk_bf16_f32 v10, v116, v117\n\t"
    "v_cvt_pk_bf16_f32 v11, v118, v119\n\t"
    "v_and_b32 v119, s67, v17\n\t"
    "v_lshlrev_b32 v118, 16, v17\n\t"
    "v_and_b32 v117, s67, v16\n\t"
    "v_lshlrev_b32 v116, 16, v16\n\t"
    "v_add_u32_sdwa v104, s92, v104 dst_sel:DWORD dst_unused:UNUSED_PAD src0_sel:DWORD src1_sel:WORD_0\n\t"
    "s_waitcnt lgkmcnt(4)\n\t"
    "v_smfmac_f32_16x16x64_bf16 v[116:119], v[120:123], v[84:91], v124\n\t"
    "ds_read_b64 v[84:85], v104 offset:0\n\t"
    "ds_read_b64 v[86:87], v126 offset:0\n\t"
    "v_cvt_pk_bf16_f32 v80, v80, v81\n\t"
    "v_cvt_pk_bf16_f32 v81, v82, v83\n\t"
    "ds_write_b64 v125, v[80:81] offset:32256\n\t"
    "v_add_u32_sdwa v90, s92, v105 dst_sel:DWORD dst_unused:UNUSED_PAD src0_sel:DWORD src1_sel:WORD_1\n\t"
    "v_add_u32_sdwa v105, s92, v105 dst_sel:DWORD dst_unused:UNUSED_PAD src0_sel:DWORD src1_sel:WORD_0\n\t"
    "ds_read_b64 v[88:89], v105 offset:0\n\t"
    "ds_read_b64 v[90:91], v90 offset:0\n\t"
    "v_add_u32_sdwa v126, s92, v106 dst_sel:DWORD dst_unused:UNUSED_PAD src0_sel:DWORD src1_sel:WORD_1\n\t"
    "v_add_u32_sdwa v106, s92, v106 dst_sel:DWORD dst_unused:UNUSED_PAD src0_sel:DWORD src1_sel:WORD_0\n\t"
    "s_waitcnt vmcnt(2) lgkmcnt(5)\n\t"
    "v_smfmac_f32_16x16x64_bf16 v[116:119], v[120:123], v[92:99], v124\n\t"
    "ds_read_b64 v[92:93], v106 offset:0\n\t"
    "ds_read_b64 v[94:95], v126 offset:0\n\t"
    "v_add_u32_sdwa v98, s92, v107 dst_sel:DWORD dst_unused:UNUSED_PAD src0_sel:DWORD src1_sel:WORD_1\n\t"
    "v_add_u32_sdwa v107, s92, v107 dst_sel:DWORD dst_unused:UNUSED_PAD src0_sel:DWORD src1_sel:WORD_0\n\t"
    "ds_read_b64 v[96:97], v107 offset:0\n\t"
    "ds_read_b64 v[98:99], v98 offset:0\n\t"
    "buffer_load_dwordx4 v[104:107], v127, %12, s88 offen offset:1024 sc1\n\t"
    "v_add_u32_sdwa v126, s92, v108 dst_sel:DWORD dst_unused:UNUSED_PAD src0_sel:DWORD src1_sel:WORD_1\n\t"
    "v_cvt_pk_bf16_f32 v16, v116, v117\n\t"
    "v_cvt_pk_bf16_f32 v17, v118, v119\n\t"
    "v_and_b32 v119, s67, v19\n\t"
    "v_lshlrev_b32 v118, 16, v19\n\t"
    "v_and_b32 v117, s67, v18\n\t"
    "v_lshlrev_b32 v116, 16, v18\n\t"
    "v_add_u32_sdwa v108, s92, v108 dst_sel:DWORD dst_unused:UNUSED_PAD src0_sel:DWORD src1_sel:WORD_0\n\t"
    "s_waitcnt lgkmcnt(4)\n\t"
    "v_smfmac_f32_16x16x64_bf16 v[116:119], v[120:123], v[84:91], v124\n\t"
    "ds_read_b64 v[84:85], v108 offset:0\n\t"
    "v_mfma_f32_16x16x32_bf16 v[80:83], v[64:67], v[188:191], 0\n\t"
    "ds_read_b64 v[86:87], v126 offset:0\n\t"
    "v_mfma_f32_16x16x32_bf16 v[80:83], v[68:71], v[192:195], v[80:83]\n\t"
    "v_add_u32_sdwa v90, s92, v109 dst_sel:DWORD dst_unused:UNUSED_PAD src0_sel:DWORD src1_sel:WORD_1\n\t"
    "v_add_u32_sdwa v109, s92, v109 dst_sel:DWORD dst_unused:UNUSED_PAD src0_sel:DWORD src1_sel:WORD_0\n\t"
    "ds_read_b64 v[88:89], v109 offset:0\n\t"
    "v_mfma_f32_16x16x32_bf16 v[80:83], v[72:75], v[244:247], v[80:83]\n\t"
    "ds_read_b64 v[90:91], v90 offset:0\n\t"
    "v_mfma_f32_16x16x32_bf16 v[80:83], v[76:79], v[248:251], v[80:83]\n\t"
    "v_add_u32_sdwa v126, s92, v110 dst_sel:DWORD dst_unused:UNUSED_PAD src0_sel:DWORD src1_sel:WORD_1\n\t"
    "v_add_u32_sdwa v110, s92, v110 dst_sel:DWORD dst_unused:UNUSED_PAD src0_sel:DWORD src1_sel:WORD_0\n\t"
    "s_waitcnt vmcnt(2) lgkmcnt(4)\n\t"
    "v_smfmac_f32_16x16x64_bf16 v[116:119], v[120:123], v[92:99], v124\n\t"
    "ds_read_b64 v[92:93], v110 offset:0\n\t"
    "ds_read_b64 v[94:95], v126 offset:0\n\t"
    "v_add_u32_sdwa v98, s92, v111 dst_sel:DWORD dst_unused:UNUSED_PAD src0_sel:DWORD src1_sel:WORD_1\n\t"
    "v_add_u32_sdwa v111, s92, v111 dst_sel:DWORD dst_unused:UNUSED_PAD src0_sel:DWORD src1_sel:WORD_0\n\t"
    "ds_read_b64 v[96:97], v111 offset:0\n\t"
    "ds_read_b64 v[98:99], v98 offset:0\n\t"
    "s_add_u32 s89, s89, s94\n\t"
    "buffer_load_dwordx4 v[108:111], v127, %12, s89 offen offset:0 sc1\n\t"
    "v_add_u32_sdwa v126, s92, v112 dst_sel:DWORD dst_unused:UNUSED_PAD src0_sel:DWORD src1_sel:WORD_1\n\t"
    "v_cvt_pk_bf16_f32 v18, v116, v117\n\t"
    "v_cvt_pk_bf16_f32 v19, v118, v119\n\t"
    "v_and_b32 v119, s67, v25\n\t"
    "v_lshlrev_b32 v118, 16, v25\n\t"
    "v_and_b32 v117, s67, v24\n\t"
    "v_lshlrev_b32 v116, 16, v24\n\t"
    "v_add_u32_sdwa v112, s92, v112 dst_sel:DWORD dst_unused:UNUSED_PAD src0_sel:DWORD src1_sel:WORD_0\n\t"
    "s_waitcnt lgkmcnt(4)\n\t"
    "v_smfmac_f32_16x16x64_bf16 v[116:119], v[120:123], v[84:91], v124\n\t"
    "ds_read_b64 v[84:85], v112 offset:0\n\t"
    "ds_read_b64 v[86:87], v126 offset:0\n\t"
    "v_cvt_pk_bf16_f32 v80, v80, v81\n\t"
    "v_cvt_pk_bf16_f32 v81, v82, v83\n\t"
    "ds_write_b64 v125, v[80:81] offset:32384\n\t"
    "v_add_u32_sdwa v90, s92, v113 dst_sel:DWORD dst_unused:UNUSED_PAD src0_sel:DWORD src1_sel:WORD_1\n\t"
    "v_add_u32_sdwa v113, s92, v113 dst_sel:DWORD dst_unused:UNUSED_PAD src0_sel:DWORD src1_sel:WORD_0\n\t"
    "ds_read_b64 v[88:89], v113 offset:0\n\t"
    "ds_read_b64 v[90:91], v90 offset:0\n\t"
    "v_add_u32_sdwa v126, s92, v114 dst_sel:DWORD dst_unused:UNUSED_PAD src0_sel:DWORD src1_sel:WORD_1\n\t"
    "v_add_u32_sdwa v114, s92, v114 dst_sel:DWORD dst_unused:UNUSED_PAD src0_sel:DWORD src1_sel:WORD_0\n\t"
    "s_waitcnt vmcnt(2) lgkmcnt(5)\n\t"
    "v_smfmac_f32_16x16x64_bf16 v[116:119], v[120:123], v[92:99], v124\n\t"
    "ds_read_b64 v[92:93], v114 offset:0\n\t"
    "ds_read_b64 v[94:95], v126 offset:0\n\t"
    "v_add_u32_sdwa v98, s92, v115 dst_sel:DWORD dst_unused:UNUSED_PAD src0_sel:DWORD src1_sel:WORD_1\n\t"
    "v_add_u32_sdwa v115, s92, v115 dst_sel:DWORD dst_unused:UNUSED_PAD src0_sel:DWORD src1_sel:WORD_0\n\t"
    "ds_read_b64 v[96:97], v115 offset:0\n\t"
    "ds_read_b64 v[98:99], v98 offset:0\n\t"
    "buffer_load_dwordx4 v[112:115], v127, %12, s89 offen offset:1024 sc1\n\t"
    "v_add_u32_sdwa v126, s92, v100 dst_sel:DWORD dst_unused:UNUSED_PAD src0_sel:DWORD src1_sel:WORD_1\n\t"
    "v_cvt_pk_bf16_f32 v24, v116, v117\n\t"
    "v_cvt_pk_bf16_f32 v25, v118, v119\n\t"
    "v_and_b32 v119, s67, v27\n\t"
    "v_lshlrev_b32 v118, 16, v27\n\t"
    "v_and_b32 v117, s67, v26\n\t"
    "v_lshlrev_b32 v116, 16, v26\n\t"
    "v_add_u32_sdwa v100, s92, v100 dst_sel:DWORD dst_unused:UNUSED_PAD src0_sel:DWORD src1_sel:WORD_0\n\t"
    "s_waitcnt lgkmcnt(4)\n\t"
    "v_smfmac_f32_16x16x64_bf16 v[116:119], v[120:123], v[84:91], v124\n\t"
    "ds_read_b64 v[84:85], v100 offset:0\n\t"
    "v_mfma_f32_16x16x32_bf16 v[80:83], v[64:67], v[196:199], 0\n\t"
    "ds_read_b64 v[86:87], v126 offset:0\n\t"
    "v_mfma_f32_16x16x32_bf16 v[80:83], v[68:71], v[200:203], v[80:83]\n\t"
    "v_add_u32_sdwa v90, s92, v101 dst_sel:DWORD dst_unused:UNUSED_PAD src0_sel:DWORD src1_sel:WORD_1\n\t"
    "v_add_u32_sdwa v101, s92, v101 dst_sel:DWORD dst_unused:UNUSED_PAD src0_sel:DWORD src1_sel:WORD_0\n\t"
    "ds_read_b64 v[88:89], v101 offset:0\n\t"
    "v_mfma_f32_16x16x32_bf16 v[80:83], v[72:75], v[252:255], v[80:83]\n\t"
    "ds_read_b64 v[90:91], v90 offset:0\n\t"
    "v_mfma_f32_16x16x32_bf16 v[80:83], v[76:79], v[128:131], v[80:83]\n\t"
    "v_add_u32_sdwa v126, s92, v102 dst_sel:DWORD dst_unused:UNUSED_PAD src0_sel:DWORD src1_sel:WORD_1\n\t"
    "v_add_u32_sdwa v102, s92, v102 dst_sel:DWORD dst_unused:UNUSED_PAD src0_sel:DWORD src1_sel:WORD_0\n\t"
    "s_waitcnt vmcnt(2) lgkmcnt(4)\n\t"
    "v_smfmac_f32_16x16x64_bf16 v[116:119], v[120:123], v[92:99], v124\n\t"
    "ds_read_b64 v[92:93], v102 offset:0\n\t"
    "ds_read_b64 v[94:95], v126 offset:0\n\t"
    "v_add_u32_sdwa v98, s92, v103 dst_sel:DWORD dst_unused:UNUSED_PAD src0_sel:DWORD src1_sel:WORD_1\n\t"
    "v_add_u32_sdwa v103, s92, v103 dst_sel:DWORD dst_unused:UNUSED_PAD src0_sel:DWORD src1_sel:WORD_0\n\t"
    "ds_read_b64 v[96:97], v103 offset:0\n\t"
    "ds_read_b64 v[98:99], v98 offset:0\n\t"
    "s_add_u32 s90, s90, s94\n\t"
    "buffer_load_dwordx4 v[100:103], v127, %12, s90 offen offset:0 sc1\n\t"
    "v_add_u32_sdwa v126, s92, v104 dst_sel:DWORD dst_unused:UNUSED_PAD src0_sel:DWORD src1_sel:WORD_1\n\t"
    "v_cvt_pk_bf16_f32 v26, v116, v117\n\t"
    "v_cvt_pk_bf16_f32 v27, v118, v119\n\t"
    "v_and_b32 v119, s67, v33\n\t"
    "v_lshlrev_b32 v118, 16, v33\n\t"
    "v_and_b32 v117, s67, v32\n\t"
    "v_lshlrev_b32 v116, 16, v32\n\t"
    "v_add_u32_sdwa v104, s92, v104 dst_sel:DWORD dst_unused:UNUSED_PAD src0_sel:DWORD src1_sel:WORD_0\n\t"
    "s_waitcnt lgkmcnt(4)\n\t"
    "v_smfmac_f32_16x16x64_bf16 v[116:119], v[120:123], v[84:91], v124\n\t"
    "ds_read_b64 v[84:85], v104 offset:0\n\t"
    "ds_read_b64 v[86:87], v126 offset:0\n\t"
    "v_cvt_pk_bf16_f32 v80, v80, v81\n\t"
    "v_cvt_pk_bf16_f32 v81, v82, v83\n\t"
    "ds_write_b64 v125, v[80:81] offset:32512\n\t"
    "v_add_u32_sdwa v90, s92, v105 dst_sel:DWORD dst_unused:UNUSED_PAD src0_sel:DWORD src1_sel:WORD_1\n\t"
    "v_add_u32_sdwa v105, s92, v105 dst_sel:DWORD dst_unused:UNUSED_PAD src0_sel:DWORD src1_sel:WORD_0\n\t"
    "ds_read_b64 v[88:89], v105 offset:0\n\t"
    "ds_read_b64 v[90:91], v90 offset:0\n\t"
    "v_add_u32_sdwa v126, s92, v106 dst_sel:DWORD dst_unused:UNUSED_PAD src0_sel:DWORD src1_sel:WORD_1\n\t"
    "v_add_u32_sdwa v106, s92, v106 dst_sel:DWORD dst_unused:UNUSED_PAD src0_sel:DWORD src1_sel:WORD_0\n\t"
    "s_waitcnt vmcnt(2) lgkmcnt(5)\n\t"
    "v_smfmac_f32_16x16x64_bf16 v[116:119], v[120:123], v[92:99], v124\n\t"
    "ds_read_b64 v[92:93], v106 offset:0\n\t"
    "ds_read_b64 v[94:95], v126 offset:0\n\t"
    "v_add_u32_sdwa v98, s92, v107 dst_sel:DWORD dst_unused:UNUSED_PAD src0_sel:DWORD src1_sel:WORD_1\n\t"
    "v_add_u32_sdwa v107, s92, v107 dst_sel:DWORD dst_unused:UNUSED_PAD src0_sel:DWORD src1_sel:WORD_0\n\t"
    "ds_read_b64 v[96:97], v107 offset:0\n\t"
    "ds_read_b64 v[98:99], v98 offset:0\n\t"
    "buffer_load_dwordx4 v[104:107], v127, %12, s90 offen offset:1024 sc1\n\t"
    "v_add_u32_sdwa v126, s92, v108 dst_sel:DWORD dst_unused:UNUSED_PAD src0_sel:DWORD src1_sel:WORD_1\n\t"
    "v_cvt_pk_bf16_f32 v32, v116, v117\n\t"
    "v_cvt_pk_bf16_f32 v33, v118, v119\n\t"
    "v_and_b32 v119, s67, v35\n\t"
    "v_lshlrev_b32 v118, 16, v35\n\t"
    "v_and_b32 v117, s67, v34\n\t"
    "v_lshlrev_b32 v116, 16, v34\n\t"
    "v_add_u32_sdwa v108, s92, v108 dst_sel:DWORD dst_unused:UNUSED_PAD src0_sel:DWORD src1_sel:WORD_0\n\t"
    "s_waitcnt lgkmcnt(4)\n\t"
    "v_smfmac_f32_16x16x64_bf16 v[116:119], v[120:123], v[84:91], v124\n\t"
    "ds_read_b64 v[84:85], v108 offset:0\n\t"
    "v_mfma_f32_16x16x32_bf16 v[80:83], v[64:67], v[204:207], 0\n\t"
    "ds_read_b64 v[86:87], v126 offset:0\n\t"
    "v_mfma_f32_16x16x32_bf16 v[80:83], v[68:71], v[208:211], v[80:83]\n\t"
    "v_add_u32_sdwa v90, s92, v109 dst_sel:DWORD dst_unused:UNUSED_PAD src0_sel:DWORD src1_sel:WORD_1\n\t"
    "v_add_u32_sdwa v109, s92, v109 dst_sel:DWORD dst_unused:UNUSED_PAD src0_sel:DWORD src1_sel:WORD_0\n\t"
    "ds_read_b64 v[88:89], v109 offset:0\n\t"
    "v_mfma_f32_16x16x32_bf16 v[80:83], v[72:75], v[132:135], v[80:83]\n\t"
    "ds_read_b64 v[90:91], v90 offset:0\n\t"
    "v_mfma_f32_16x16x32_bf16 v[80:83], v[76:79], v[136:139], v[80:83]\n\t"
    "v_add_u32_sdwa v126, s92, v110 dst_sel:DWORD dst_unused:UNUSED_PAD src0_sel:DWORD src1_sel:WORD_1\n\t"
    "v_add_u32_sdwa v110, s92, v110 dst_sel:DWORD dst_unused:UNUSED_PAD src0_sel:DWORD src1_sel:WORD_0\n\t"
    "s_cmp_lt_u32 %16, 4\n\t"
    "s_cbranch_scc1 L_tp51_%=\n\t"
    "s_setprio 0\n\t"
    "L_tp51_%=:\n\t"
    "s_waitcnt vmcnt(2) lgkmcnt(4)\n\t"
    "v_smfmac_f32_16x16x64_bf16 v[116:119], v[120:123], v[92:99], v124\n\t"
    "ds_read_b64 v[92:93], v110 offset:0\n\t"
    "ds_read_b64 v[94:95], v126 offset:0\n\t"
    "v_add_u32_sdwa v98, s92, v111 dst_sel:DWORD dst_unused:UNUSED_PAD src0_sel:DWORD src1_sel:WORD_1\n\t"
    "v_add_u32_sdwa v111, s92, v111 dst_sel:DWORD dst_unused:UNUSED_PAD src0_sel:DWORD src1_sel:WORD_0\n\t"
    "ds_read_b64 v[96:97], v111 offset:0\n\t"
    "ds_read_b64 v[98:99], v98 offset:0\n\t"
    "s_add_u32 s91, s91, s94\n\t"
    "buffer_load_dwordx4 v[108:111], v127, %12, s91 offen offset:0 sc1\n\t"
    "v_add_u32_sdwa v126, s92, v112 dst_sel:DWORD dst_unused:UNUSED_PAD src0_sel:DWORD src1_sel:WORD_1\n\t"
    "v_cvt_pk_bf16_f32 v34, v116, v117\n\t"
    "v_cvt_pk_bf16_f32 v35, v118, v119\n\t"
    "v_and_b32 v119, s67, v41\n\t"
    "v_lshlrev_b32 v118, 16, v41\n\t"
    "v_and_b32 v117, s67, v40\n\t"
    "v_lshlrev_b32 v116, 16, v40\n\t"
    "v_add_u32_sdwa v112, s92, v112 dst_sel:DWORD dst_unused:UNUSED_PAD src0_sel:DWORD src1_sel:WORD_0\n\t"
    "s_waitcnt lgkmcnt(4)\n\t"
    "v_smfmac_f32_16x16x64_bf16 v[116:119], v[120:123], v[84:91], v124\n\t"
    "ds_read_b64 v[84:85], v112 offset:0\n\t"
    "ds_read_b64 v[86:87], v126 offset:0\n\t"
    "v_cvt_pk_bf16_f32 v80, v80, v81\n\t"
    "v_cvt_pk_bf16_f32 v81, v82, v83\n\t"
    "ds_write_b64 v125, v[80:81] offset:32640\n\t"
    "v_add_u32_sdwa v90, s92, v113 dst_sel:DWORD dst_unused:UNUSED_PAD src0_sel:DWORD src1_sel:WORD_1\n\t"
    "v_add_u32_sdwa v113, s92, v113 dst_sel:DWORD dst_unused:UNUSED_PAD src0_sel:DWORD src1_sel:WORD_0\n\t"
    "ds_read_b64 v[88:89], v113 offset:0\n\t"
    "ds_read_b64 v[90:91], v90 offset:0\n\t"
    "v_add_u32_sdwa v126, s92, v114 dst_sel:DWORD dst_unused:UNUSED_PAD src0_sel:DWORD src1_sel:WORD_1\n\t"
    "v_add_u32_sdwa v114, s92, v114 dst_sel:DWORD dst_unused:UNUSED_PAD src0_sel:DWORD src1_sel:WORD_0\n\t"
    "s_waitcnt vmcnt(2) lgkmcnt(5)\n\t"
    "v_smfmac_f32_16x16x64_bf16 v[116:119], v[120:123], v[92:99], v124\n\t"
    "ds_read_b64 v[92:93], v114 offset:0\n\t"
    "ds_read_b64 v[94:95], v126 offset:0\n\t"
    "v_add_u32_sdwa v98, s92, v115 dst_sel:DWORD dst_unused:UNUSED_PAD src0_sel:DWORD src1_sel:WORD_1\n\t"
    "v_add_u32_sdwa v115, s92, v115 dst_sel:DWORD dst_unused:UNUSED_PAD src0_sel:DWORD src1_sel:WORD_0\n\t"
    "ds_read_b64 v[96:97], v115 offset:0\n\t"
    "ds_read_b64 v[98:99], v98 offset:0\n\t"
    "buffer_load_dwordx4 v[112:115], v127, %12, s91 offen offset:1024 sc1\n\t"
    "v_add_u32_sdwa v126, s92, v100 dst_sel:DWORD dst_unused:UNUSED_PAD src0_sel:DWORD src1_sel:WORD_1\n\t"
    "v_cvt_pk_bf16_f32 v40, v116, v117\n\t"
    "v_cvt_pk_bf16_f32 v41, v118, v119\n\t"
    "v_and_b32 v119, s67, v43\n\t"
    "v_lshlrev_b32 v118, 16, v43\n\t"
    "v_and_b32 v117, s67, v42\n\t"
    "v_lshlrev_b32 v116, 16, v42\n\t"
    "v_add_u32_sdwa v100, s92, v100 dst_sel:DWORD dst_unused:UNUSED_PAD src0_sel:DWORD src1_sel:WORD_0\n\t"
    "s_waitcnt lgkmcnt(4)\n\t"
    "v_smfmac_f32_16x16x64_bf16 v[116:119], v[120:123], v[84:91], v124\n\t"
    "ds_read_b64 v[84:85], v100 offset:0\n\t"
    "v_mfma_f32_16x16x32_bf16 v[80:83], v[64:67], v[156:159], 0\n\t"
    "ds_read_b64 v[86:87], v126 offset:0\n\t"
    "v_mfma_f32_16x16x32_bf16 v[80:83], v[68:71], v[160:163], v[80:83]\n\t"
    "v_add_u32_sdwa v90, s92, v101 dst_sel:DWORD dst_unused:UNUSED_PAD src0_sel:DWORD src1_sel:WORD_1\n\t"
    "v_add_u32_sdwa v101, s92, v101 dst_sel:DWORD dst_unused:UNUSED_PAD src0_sel:DWORD src1_sel:WORD_0\n\t"
    "ds_read_b64 v[88:89], v101 offset:0\n\t"
    "v_mfma_f32_16x16x32_bf16 v[80:83], v[72:75], v[140:143], v[80:83]\n\t"
    "ds_read_b64 v[90:91], v90 offset:0\n\t"
    "v_mfma_f32_16x16x32_bf16 v[80:83], v[76:79], v[144:147], v[80:83]\n\t"
    "v_add_u32_sdwa v126, s92, v102 dst_sel:DWORD dst_unused:UNUSED_PAD src0_sel:DWORD src1_sel:WORD_1\n\t"
    "v_add_u32_sdwa v102, s92, v102 dst_sel:DWORD dst_unused:UNUSED_PAD src0_sel:DWORD src1_sel:WORD_0\n\t"
    "s_waitcnt vmcnt(2) lgkmcnt(4)\n\t"
    "v_smfmac_f32_16x16x64_bf16 v[116:119], v[120:123], v[92:99], v124\n\t"
    "ds_read_b64 v[92:93], v102 offset:0\n\t"
    "ds_read_b64 v[94:95], v126 offset:0\n\t"
    "v_add_u32_sdwa v98, s92, v103 dst_sel:DWORD dst_unused:UNUSED_PAD src0_sel:DWORD src1_sel:WORD_1\n\t"
    "v_add_u32_sdwa v103, s92, v103 dst_sel:DWORD dst_unused:UNUSED_PAD src0_sel:DWORD src1_sel:WORD_0\n\t"
    "ds_read_b64 v[96:97], v103 offset:0\n\t"
    "ds_read_b64 v[98:99], v98 offset:0\n\t"
    "s_add_u32 s84, s84, s95\n\t"
    "buffer_load_dwordx4 v[100:103], v127, %12, s84 offen offset:0 sc1\n\t"
    "v_add_u32_sdwa v126, s92, v104 dst_sel:DWORD dst_unused:UNUSED_PAD src0_sel:DWORD src1_sel:WORD_1\n\t"
    "v_cvt_pk_bf16_f32 v42, v116, v117\n\t"
    "v_cvt_pk_bf16_f32 v43, v118, v119\n\t"
    "v_and_b32 v119, s67, v49\n\t"
    "v_lshlrev_b32 v118, 16, v49\n\t"
    "v_and_b32 v117, s67, v48\n\t"
    "v_lshlrev_b32 v116, 16, v48\n\t"
    "v_add_u32_sdwa v104, s92, v104 dst_sel:DWORD dst_unused:UNUSED_PAD src0_sel:DWORD src1_sel:WORD_0\n\t"
    "s_waitcnt lgkmcnt(4)\n\t"
    "v_smfmac_f32_16x16x64_bf16 v[116:119], v[120:123], v[84:91], v124\n\t"
    "ds_read_b64 v[84:85], v104 offset:0\n\t"
    "ds_read_b64 v[86:87], v126 offset:0\n\t"
    "v_cvt_pk_bf16_f32 v80, v80, v81\n\t"
    "v_cvt_pk_bf16_f32 v81, v82, v83\n\t"
    "ds_write_b64 v125, v[80:81] offset:32768\n\t"
    "v_add_u32_sdwa v90, s92, v105 dst_sel:DWORD dst_unused:UNUSED_PAD src0_sel:DWORD src1_sel:WORD_1\n\t"
    "v_add_u32_sdwa v105, s92, v105 dst_sel:DWORD dst_unused:UNUSED_PAD src0_sel:DWORD src1_sel:WORD_0\n\t"
    "ds_read_b64 v[88:89], v105 offset:0\n\t"
    "ds_read_b64 v[90:91], v90 offset:0\n\t"
    "v_add_u32_sdwa v126, s92, v106 dst_sel:DWORD dst_unused:UNUSED_PAD src0_sel:DWORD src1_sel:WORD_1\n\t"
    "v_add_u32_sdwa v106, s92, v106 dst_sel:DWORD dst_unused:UNUSED_PAD src0_sel:DWORD src1_sel:WORD_0\n\t"
    "s_waitcnt vmcnt(2) lgkmcnt(5)\n\t"
    "v_smfmac_f32_16x16x64_bf16 v[116:119], v[120:123], v[92:99], v124\n\t"
    "ds_read_b64 v[92:93], v106 offset:0\n\t"
    "ds_read_b64 v[94:95], v126 offset:0\n\t"
    "v_add_u32_sdwa v98, s92, v107 dst_sel:DWORD dst_unused:UNUSED_PAD src0_sel:DWORD src1_sel:WORD_1\n\t"
    "v_add_u32_sdwa v107, s92, v107 dst_sel:DWORD dst_unused:UNUSED_PAD src0_sel:DWORD src1_sel:WORD_0\n\t"
    "ds_read_b64 v[96:97], v107 offset:0\n\t"
    "ds_read_b64 v[98:99], v98 offset:0\n\t"
    "buffer_load_dwordx4 v[104:107], v127, %12, s84 offen offset:1024 sc1\n\t"
    "v_add_u32_sdwa v126, s92, v108 dst_sel:DWORD dst_unused:UNUSED_PAD src0_sel:DWORD src1_sel:WORD_1\n\t"
    "v_cvt_pk_bf16_f32 v48, v116, v117\n\t"
    "v_cvt_pk_bf16_f32 v49, v118, v119\n\t"
    "v_and_b32 v119, s67, v51\n\t"
    "v_lshlrev_b32 v118, 16, v51\n\t"
    "v_and_b32 v117, s67, v50\n\t"
    "v_lshlrev_b32 v116, 16, v50\n\t"
    "v_add_u32_sdwa v108, s92, v108 dst_sel:DWORD dst_unused:UNUSED_PAD src0_sel:DWORD src1_sel:WORD_0\n\t"
    "s_waitcnt lgkmcnt(4)\n\t"
    "v_smfmac_f32_16x16x64_bf16 v[116:119], v[120:123], v[84:91], v124\n\t"
    "ds_read_b64 v[84:85], v108 offset:0\n\t"
    "v_mfma_f32_16x16x32_bf16 v[80:83], v[64:67], v[212:215], 0\n\t"
    "ds_read_b64 v[86:87], v126 offset:0\n\t"
    "v_mfma_f32_16x16x32_bf16 v[80:83], v[68:71], v[216:219], v[80:83]\n\t"
    "v_add_u32_sdwa v90, s92, v109 dst_sel:DWORD dst_unused:UNUSED_PAD src0_sel:DWORD src1_sel:WORD_1\n\t"
    "v_add_u32_sdwa v109, s92, v109 dst_sel:DWORD dst_unused:UNUSED_PAD src0_sel:DWORD src1_sel:WORD_0\n\t"
    "ds_read_b64 v[88:89], v109 offset:0\n\t"
    "v_mfma_f32_16x16x32_bf16 v[80:83], v[72:75], v[148:151], v[80:83]\n\t"
    "ds_read_b64 v[90:91], v90 offset:0\n\t"
    "v_mfma_f32_16x16x32_bf16 v[80:83], v[76:79], v[152:155], v[80:83]\n\t"
    "v_add_u32_sdwa v126, s92, v110 dst_sel:DWORD dst_unused:UNUSED_PAD src0_sel:DWORD src1_sel:WORD_1\n\t"
    "v_add_u32_sdwa v110, s92, v110 dst_sel:DWORD dst_unused:UNUSED_PAD src0_sel:DWORD src1_sel:WORD_0\n\t"
    "s_waitcnt vmcnt(2) lgkmcnt(4)\n\t"
    "v_smfmac_f32_16x16x64_bf16 v[116:119], v[120:123], v[92:99], v124\n\t"
    "ds_read_b64 v[92:93], v110 offset:0\n\t"
    "ds_read_b64 v[94:95], v126 offset:0\n\t"
    "v_add_u32_sdwa v98, s92, v111 dst_sel:DWORD dst_unused:UNUSED_PAD src0_sel:DWORD src1_sel:WORD_1\n\t"
    "v_add_u32_sdwa v111, s92, v111 dst_sel:DWORD dst_unused:UNUSED_PAD src0_sel:DWORD src1_sel:WORD_0\n\t"
    "ds_read_b64 v[96:97], v111 offset:0\n\t"
    "ds_read_b64 v[98:99], v98 offset:0\n\t"
    "s_add_u32 s85, s85, s95\n\t"
    "buffer_load_dwordx4 v[108:111], v127, %12, s85 offen offset:0 sc1\n\t"
    "v_add_u32_sdwa v126, s92, v112 dst_sel:DWORD dst_unused:UNUSED_PAD src0_sel:DWORD src1_sel:WORD_1\n\t"
    "v_cvt_pk_bf16_f32 v50, v116, v117\n\t"
    "v_cvt_pk_bf16_f32 v51, v118, v119\n\t"
    "v_and_b32 v119, s67, v57\n\t"
    "v_lshlrev_b32 v118, 16, v57\n\t"
    "v_and_b32 v117, s67, v56\n\t"
    "v_lshlrev_b32 v116, 16, v56\n\t"
    "v_add_u32_sdwa v112, s92, v112 dst_sel:DWORD dst_unused:UNUSED_PAD src0_sel:DWORD src1_sel:WORD_0\n\t"
    "s_waitcnt lgkmcnt(4)\n\t"
    "v_smfmac_f32_16x16x64_bf16 v[116:119], v[120:123], v[84:91], v124\n\t"
    "ds_read_b64 v[84:85], v112 offset:0\n\t"
    "ds_read_b64 v[86:87], v126 offset:0\n\t"
    "v_cvt_pk_bf16_f32 v80, v80, v81\n\t"
    "v_cvt_pk_bf16_f32 v81, v82, v83\n\t"
    "ds_write_b64 v125, v[80:81] offset:32872\n\t"
    "v_add_u32_sdwa v90, s92, v113 dst_sel:DWORD dst_unused:UNUSED_PAD src0_sel:DWORD src1_sel:WORD_1\n\t"
    "v_add_u32_sdwa v113, s92, v113 dst_sel:DWORD dst_unused:UNUSED_PAD src0_sel:DWORD src1_sel:WORD_0\n\t"
    "ds_read_b64 v[88:89], v113 offset:0\n\t"
    "ds_read_b64 v[90:91], v90 offset:0\n\t"
    "v_add_u32_sdwa v126, s92, v114 dst_sel:DWORD dst_unused:UNUSED_PAD src0_sel:DWORD src1_sel:WORD_1\n\t"
    "v_add_u32_sdwa v114, s92, v114 dst_sel:DWORD dst_unused:UNUSED_PAD src0_sel:DWORD src1_sel:WORD_0\n\t"
    "s_waitcnt vmcnt(2) lgkmcnt(5)\n\t"
    "v_smfmac_f32_16x16x64_bf16 v[116:119], v[120:123], v[92:99], v124\n\t"
    "ds_read_b64 v[92:93], v114 offset:0\n\t"
    "ds_read_b64 v[94:95], v126 offset:0\n\t"
    "v_add_u32_sdwa v98, s92, v115 dst_sel:DWORD dst_unused:UNUSED_PAD src0_sel:DWORD src1_sel:WORD_1\n\t"
    "v_add_u32_sdwa v115, s92, v115 dst_sel:DWORD dst_unused:UNUSED_PAD src0_sel:DWORD src1_sel:WORD_0\n\t"
    "ds_read_b64 v[96:97], v115 offset:0\n\t"
    "ds_read_b64 v[98:99], v98 offset:0\n\t"
    "buffer_load_dwordx4 v[112:115], v127, %12, s85 offen offset:1024 sc1\n\t"
    "s_sub_u32 s92, 64000, s92\n\t"
    "v_add_u32_sdwa v126, s92, v100 dst_sel:DWORD dst_unused:UNUSED_PAD src0_sel:DWORD src1_sel:WORD_1\n\t"
    "v_cvt_pk_bf16_f32 v56, v116, v117\n\t"
    "v_cvt_pk_bf16_f32 v57, v118, v119\n\t"
    "v_and_b32 v119, s67, v59\n\t"
    "v_lshlrev_b32 v118, 16, v59\n\t"
    "v_and_b32 v117, s67, v58\n\t"
    "v_lshlrev_b32 v116, 16, v58\n\t"
    "v_add_u32_sdwa v100, s92, v100 dst_sel:DWORD dst_unused:UNUSED_PAD src0_sel:DWORD src1_sel:WORD_0\n\t"
    "s_waitcnt lgkmcnt(0)\n\t"
    "s_barrier\n\t"
    "v_add_u32 v125, s93, v125\n\t"
    "s_sub_u32 s93, 0, s93\n\t"
    "v_smfmac_f32_16x16x64_bf16 v[116:119], v[120:123], v[84:91], v124\n\t"
    "ds_read_b64 v[84:85], v100 offset:0\n\t"
    "ds_read_b64 v[86:87], v126 offset:0\n\t"
    "v_add_u32_sdwa v90, s92, v101 dst_sel:DWORD dst_unused:UNUSED_PAD src0_sel:DWORD src1_sel:WORD_1\n\t"
    "v_add_u32_sdwa v101, s92, v101 dst_sel:DWORD dst_unused:UNUSED_PAD src0_sel:DWORD src1_sel:WORD_0\n\t"
    "ds_read_b64 v[88:89], v101 offset:0\n\t"
    "ds_read_b64 v[90:91], v90 offset:0\n\t"
    "v_add_u32_sdwa v126, s92, v102 dst_sel:DWORD dst_unused:UNUSED_PAD src0_sel:DWORD src1_sel:WORD_1\n\t"
    "v_add_u32_sdwa v102, s92, v102 dst_sel:DWORD dst_unused:UNUSED_PAD src0_sel:DWORD src1_sel:WORD_0\n\t"
    "s_cmp_lt_u32 %16, 4\n\t"
    "s_cbranch_scc1 L_tp63_%=\n\t"
    "s_setprio 1\n\t"
    "L_tp63_%=:\n\t"
    "s_waitcnt vmcnt(2)\n\t"
    "v_smfmac_f32_16x16x64_bf16 v[116:119], v[120:123], v[92:99], v124\n\t"
    "ds_read_b64 v[92:93], v102 offset:0\n\t"
    "ds_read_b64 v[94:95], v126 offset:0\n\t"
    "v_add_u32_sdwa v98, s92, v103 dst_sel:DWORD dst_unused:UNUSED_PAD src0_sel:DWORD src1_sel:WORD_1\n\t"
    "v_add_u32_sdwa v103, s92, v103 dst_sel:DWORD dst_unused:UNUSED_PAD src0_sel:DWORD src1_sel:WORD_0\n\t"
    "ds_read_b64 v[96:97], v103 offset:0\n\t"
    "ds_read_b64 v[98:99], v98 offset:0\n\t"
    "s_add_u32 s86, s86, s95\n\t"
    "buffer_load_dwordx4 v[100:103], v127, %12, s86 offen offset:0 sc1\n\t"
    "v_add_u32_sdwa v126, s92, v104 dst_sel:DWORD dst_unused:UNUSED_PAD src0_sel:DWORD src1_sel:WORD_1\n\t"
    "v_cvt_pk_bf16_f32 v58, v116, v117\n\t"
    "v_cvt_pk_bf16_f32 v59, v118, v119\n\t"
    "v_and_b32 v119, s67, v5\n\t"
    "v_lshlrev_b32 v118, 16, v5\n\t"
    "v_and_b32 v117, s67, v4\n\t"
    "v_lshlrev_b32 v116, 16, v4\n\t"
    "v_add_u32_sdwa v104, s92, v104 dst_sel:DWORD dst_unused:UNUSED_PAD src0_sel:DWORD src1_sel:WORD_0\n\t"
    "v_add_u32 v79, s96, v127\n\t"
    "s_add_u32 s96, s96, 0x1000\n\t"
    "s_and_b32 s96, s96, 0x3000\n\t"
    "v_add_u32 v79, 128000, v79\n\t"
    "ds_read_b128 v[64:67], v79 offset:0\n\t"
    "ds_read_b128 v[68:71], v79 offset:1024\n\t"
    "ds_read_b128 v[72:75], v79 offset:2048\n\t"
    "ds_read_b128 v[76:79], v79 offset:3072\n\t"
    "s_sub_u32 s66, s66, 1\n\t"
    "s_cmp_lg_u32 s66, 0\n\t"
    "s_cbranch_scc1 L_loop_%=\n\t"
    "s_cmp_eq_u32 %16, 0\n\t"
    "s_cbranch_scc1 L_pub_%=\n\t"
    "L_pubb_%=:\n\t"
    "s_mov_b32 s92, 0xffff\n\t"
    "s_waitcnt lgkmcnt(8)\n\t"
    "v_smfmac_f32_16x16x64_bf16 v[116:119], v[120:123], v[84:91], v124\n\t"
    "ds_read_b64 v[84:85], v104 offset:0\n\t"
    "ds_read_b64 v[86:87], v126 offset:0\n\t"
    "v_lshrrev_b32 v90, 16, v105\n\t"
    "v_and_b32 v105, s92, v105\n\t"
    "ds_read_b64 v[88:89], v105 offset:0\n\t"
    "ds_read_b64 v[90:91], v90 offset:0\n\t"
    "v_lshrrev_b32 v126, 16, v106\n\t"
    "v_and_b32 v106, s92, v106\n\t"
    "s_waitcnt vmcnt(2) lgkmcnt(8)\n\t"
    "v_smfmac_f32_16x16x64_bf16 v[116:119], v[120:123], v[92:99], v124\n\t"
    "ds_read_b64 v[92:93], v106 offset:0\n\t"
    "s_waitcnt lgkmcnt(5)\n\t"
    "v_mfma_f32_16x16x32_bf16 v[80:83], v[64:67], v[164:167], 0\n\t"
    "ds_read_b64 v[94:95], v126 offset:0\n\t"
    "v_mfma_f32_16x16x32_bf16 v[80:83], v[68:71], v[168:171], v[80:83]\n\t"
    "v_lshrrev_b32 v98, 16, v107\n\t"
    "v_and_b32 v107, s92, v107\n\t"
    "ds_read_b64 v[96:97], v107 offset:0\n\t"
    "v_mfma_f32_16x16x32_bf16 v[80:83], v[72:75], v[220:223], v[80:83]\n\t"
    "ds_read_b64 v[98:99], v98 offset:0\n\t"
    "v_mfma_f32_16x16x32_bf16 v[80:83], v[76:79], v[224:227], v[80:83]\n\t"
    "v_lshrrev_b32 v126, 16, v108\n\t"
    "v_cvt_pk_bf16_f32 v4, v116, v117\n\t"
    "v_cvt_pk_bf16_f32 v5, v118, v119\n\t"
    "v_and_b32 v119, s67, v7\n\t"
    "v_lshlrev_b32 v118, 16, v7\n\t"
    "v_and_b32 v117, s67, v6\n\t"
    "v_lshlrev_b32 v116, 16, v6\n\t"
    "v_and_b32 v108, s92, v108\n\t"
    "s_waitcnt lgkmcnt(4)\n\t"
    "v_smfmac_f32_16x16x64_bf16 v[116:119], v[120:123], v[84:91], v124\n\t"
    "ds_read_b64 v[84:85], v108 offset:0\n\t"
    "v_mfma_f32_16x16x32_bf16 v[104:107], v[64:67], v[172:175], 0\n\t"
    "ds_read_b64 v[86:87], v126 offset:0\n\t"
    "v_mfma_f32_16x16x32_bf16 v[104:107], v[68:71], v[176:179], v[104:107]\n\t"
    "v_lshrrev_b32 v90, 16, v109\n\t"
    "v_and_b32 v109, s92, v109\n\t"
    "ds_read_b64 v[88:89], v109 offset:0\n\t"
    "v_mfma_f32_16x16x32_bf16 v[104:107], v[72:75], v[228:231], v[104:107]\n\t"
    "ds_read_b64 v[90:91], v90 offset:0\n\t"
    "v_mfma_f32_16x16x32_bf16 v[104:107], v[76:79], v[232:235], v[104:107]\n\t"
    "v_lshrrev_b32 v126, 16, v110\n\t"
    "v_cvt_pk_bf16_f32 v80, v80, v81\n\t"
    "v_cvt_pk_bf16_f32 v81, v82, v83\n\t"
    "ds_write_b64 v125, v[80:81] offset:0\n\t"
    "v_mfma_f32_16x16x32_bf16 v[80:83], v[64:67], v[180:183], 0\n\t"
    "v_and_b32 v110, s92, v110\n\t"
    "v_mfma_f32_16x16x32_bf16 v[80:83], v[68:71], v[184:187], v[80:83]\n\t"
    "s_waitcnt vmcnt(1) lgkmcnt(5)\n\t"
    "v_smfmac_f32_16x16x64_bf16 v[116:119], v[120:123], v[92:99], v124\n\t"
    "ds_read_b64 v[92:93], v110 offset:0\n\t"
    "v_mfma_f32_16x16x32_bf16 v[80:83], v[72:75], v[236:239], v[80:83]\n\t"
    "ds_read_b64 v[94:95], v126 offset:0\n\t"
    "v_mfma_f32_16x16x32_bf16 v[80:83], v[76:79], v[240:243], v[80:83]\n\t"
    "v_lshrrev_b32 v98, 16, v111\n\t"
    "v_and_b32 v111, s92, v111\n\t"
    "ds_read_b64 v[96:97], v111 offset:0\n\t"
    "v_cvt_pk_bf16_f32 v104, v104, v105\n\t"
    "v_cvt_pk_bf16_f32 v105, v106, v107\n\t"
    "ds_write_b64 v125, v[104:105] offset:128\n\t"
    "v_mfma_f32_16x16x32_bf16 v[104:107], v[64:67], v[188:191], 0\n\t"
    "ds_read_b64 v[98:99], v98 offset:0\n\t"
    "v_mfma_f32_16x16x32_bf16 v[104:107], v[68:71], v[192:195], v[104:107]\n\t"
    "buffer_load_dwordx4 v[108:111], v127, %12, s86 offen offset:1024 sc1\n\t"
    "v_lshrrev_b32 v126, 16, v112\n\t"
    "v_mfma_f32_16x16x32_bf16 v[104:107], v[72:75], v[244:247], v[104:107]\n\t"
    "v_cvt_pk_bf16_f32 v6, v116, v117\n\t"
    "v_cvt_pk_bf16_f32 v7, v118, v119\n\t"
    "v_and_b32 v119, s67, v13\n\t"
    "v_lshlrev_b32 v118, 16, v13\n\t"
    "v_and_b32 v117, s67, v12\n\t"
    "v_lshlrev_b32 v116, 16, v12\n\t"
    "v_and_b32 v112, s92, v112\n\t"
    "s_waitcnt lgkmcnt(6)\n\t"
    "v_smfmac_f32_16x16x64_bf16 v[116:119], v[120:123], v[84:91], v124\n\t"
    "ds_read_b64 v[84:85], v112 offset:0\n\t"
    "v_mfma_f32_16x16x32_bf16 v[104:107], v[76:79], v[248:251], v[104:107]\n\t"
    "ds_read_b64 v[86:87], v126 offset:0\n\t"
    "v_cvt_pk_bf16_f32 v80, v80, v81\n\t"
    "v_cvt_pk_bf16_f32 v81, v82, v83\n\t"
    "ds_write_b64 v125, v[80:81] offset:256\n\t"
    "v_mfma_f32_16x16x32_bf16 v[80:83], v[64:67], v[196:199], 0\n\t"
    "v_lshrrev_b32 v90, 16, v113\n\t"
    "v_and_b32 v113, s92, v113\n\t"
    "ds_read_b64 v[88:89], v113 offset:0\n\t"
    "v_mfma_f32_16x16x32_bf16 v[80:83], v[68:71], v[200:203], v[80:83]\n\t"
    "ds_read_b64 v[90:91], v90 offset:0\n\t"
    "v_mfma_f32_16x16x32_bf16 v[80:83], v[72:75], v[252:255], v[80:83]\n\t"
    "v_lshrrev_b32 v126, 16, v114\n\t"
    "v_mfma_f32_16x16x32_bf16 v[80:83], v[76:79], v[128:131], v[80:83]\n\t"
    "v_and_b32 v114, s92, v114\n\t"
    "v_cvt_pk_bf16_f32 v104, v104, v105\n\t"
    "v_cvt_pk_bf16_f32 v105, v106, v107\n\t"
    "ds_write_b64 v125, v[104:105] offset:384\n\t"
    "v_mfma_f32_16x16x32_bf16 v[104:107], v[64:67], v[204:207], 0\n\t"
    "s_waitcnt vmcnt(1) lgkmcnt(6)\n\t"
    "v_smfmac_f32_16x16x64_bf16 v[116:119], v[120:123], v[92:99], v124\n\t"
    "ds_read_b64 v[92:93], v114 offset:0\n\t"
    "v_mfma_f32_16x16x32_bf16 v[104:107], v[68:71], v[208:211], v[104:107]\n\t"
    "ds_read_b64 v[94:95], v126 offset:0\n\t"
    "v_mfma_f32_16x16x32_bf16 v[104:107], v[72:75], v[132:135], v[104:107]\n\t"
    "v_lshrrev_b32 v98, 16, v115\n\t"
    "v_and_b32 v115, s92, v115\n\t"
    "ds_read_b64 v[96:97], v115 offset:0\n\t"
    "v_mfma_f32_16x16x32_bf16 v[104:107], v[76:79], v[136:139], v[104:107]\n\t"
    "ds_read_b64 v[98:99], v98 offset:0\n\t"
    "v_cvt_pk_bf16_f32 v80, v80, v81\n\t"
    "v_cvt_pk_bf16_f32 v81, v82, v83\n\t"
    "ds_write_b64 v125, v[80:81] offset:512\n\t"
    "v_mfma_f32_16x16x32_bf16 v[80:83], v[64:67], v[156:159], 0\n\t"
    "s_add_u32 s87, s87, s95\n\t"
    "buffer_load_dwordx4 v[112:115], v127, %12, s87 offen offset:0 sc1\n\t"
    "v_lshrrev_b32 v126, 16, v100\n\t"
    "v_mfma_f32_16x16x32_bf16 v[80:83], v[68:71], v[160:163], v[80:83]\n\t"
    "v_cvt_pk_bf16_f32 v12, v116, v117\n\t"
    "v_cvt_pk_bf16_f32 v13, v118, v119\n\t"
    "v_and_b32 v119, s67, v15\n\t"
    "v_lshlrev_b32 v118, 16, v15\n\t"
    "v_and_b32 v117, s67, v14\n\t"
    "v_lshlrev_b32 v116, 16, v14\n\t"
    "v_and_b32 v100, s92, v100\n\t"
    "s_waitcnt lgkmcnt(6)\n\t"
    "v_smfmac_f32_16x16x64_bf16 v[116:119], v[120:123], v[84:91], v124\n\t"
    "ds_read_b64 v[84:85], v100 offset:0\n\t"
    "v_mfma_f32_16x16x32_bf16 v[80:83], v[72:75], v[140:143], v[80:83]\n\t"
    "ds_read_b64 v[86:87], v126 offset:0\n\t"
    "v_mfma_f32_16x16x32_bf16 v[80:83], v[76:79], v[144:147], v[80:83]\n\t"
    "v_lshrrev_b32 v90, 16, v101\n\t"
    "v_and_b32 v101, s92, v101\n\t"
    "ds_read_b64 v[88:89], v101 offset:0\n\t"
    "v_cvt_pk_bf16_f32 v104, v104, v105\n\t"
    "v_cvt_pk_bf16_f32 v105, v106, v107\n\t"
    "ds_write_b64 v125, v[104:105] offset:640\n\t"
    "v_mfma_f32_16x16x32_bf16 v[104:107], v[64:67], v[212:215], 0\n\t"
    "ds_read_b64 v[90:91], v90 offset:0\n\t"
    "v_mfma_f32_16x16x32_bf16 v[104:107], v[68:71], v[216:219], v[104:107]\n\t"
    "v_lshrrev_b32 v126, 16, v102\n\t"
    "v_mfma_f32_16x16x32_bf16 v[104:107], v[72:75], v[148:151], v[104:107]\n\t"
    "v_and_b32 v102, s92, v102\n\t"
    "v_mfma_f32_16x16x32_bf16 v[104:107], v[76:79], v[152:155], v[104:107]\n\t"
    "v_add_u32 v79, s96, v127\n\t"
    "s_add_u32 s96, s96, 0x1000\n\t"
    "s_and_b32 s96, s96, 0x3000\n\t"
    "v_add_u32 v79, 128000, v79\n\t"
    "ds_read_b128 v[64:67], v79 offset:0\n\t"
    "ds_read_b128 v[68:71], v79 offset:1024\n\t"
    "ds_read_b128 v[72:75], v79 offset:2048\n\t"
    "ds_read_b128 v[76:79], v79 offset:3072\n\t"
    "v_cvt_pk_bf16_f32 v80, v80, v81\n\t"
    "v_cvt_pk_bf16_f32 v81, v82, v83\n\t"
    "ds_write_b64 v125, v[80:81] offset:768\n\t"
    "s_waitcnt vmcnt(1) lgkmcnt(11)\n\t"
    "v_smfmac_f32_16x16x64_bf16 v[116:119], v[120:123], v[92:99], v124\n\t"
    "ds_read_b64 v[92:93], v102 offset:0\n\t"
    "s_waitcnt lgkmcnt(2)\n\t"
    "v_mfma_f32_16x16x32_bf16 v[80:83], v[64:67], v[164:167], 0\n\t"
    "ds_read_b64 v[94:95], v126 offset:0\n\t"
    "v_mfma_f32_16x16x32_bf16 v[80:83], v[68:71], v[168:171], v[80:83]\n\t"
    "v_lshrrev_b32 v98, 16, v103\n\t"
    "v_and_b32 v103, s92, v103\n\t"
    "ds_read_b64 v[96:97], v103 offset:0\n\t"
    "v_mfma_f32_16x16x32_bf16 v[80:83], v[72:75], v[220:223], v[80:83]\n\t"
    "ds_read_b64 v[98:99], v98 offset:0\n\t"
    "buffer_load_dwordx4 v[100:103], v127, %12, s87 offen offset:1024 sc1\n\t"
    "v_lshrrev_b32 v126, 16, v108\n\t"
    "v_cvt_pk_bf16_f32 v14, v116, v117\n\t"
    "v_cvt_pk_bf16_f32 v15, v118, v119\n\t"
    "v_and_b32 v119, s67, v21\n\t"
    "v_lshlrev_b32 v118, 16, v21\n\t"
    "v_and_b32 v117, s67, v20\n\t"
    "v_lshlrev_b32 v116, 16, v20\n\t"
    "v_and_b32 v108, s92, v108\n\t"
    "s_nop 0\n\t"
    "v_smfmac_f32_16x16x64_bf16 v[116:119], v[120:123], v[84:91], v124\n\t"
    "ds_read_b64 v[84:85], v108 offset:0\n\t"
    "v_mfma_f32_16x16x32_bf16 v[80:83], v[76:79], v[224:227], v[80:83]\n\t"
    "ds_read_b64 v[86:87], v126 offset:0\n\t"
    "v_cvt_pk_bf16_f32 v104, v104, v105\n\t"
    "v_cvt_pk_bf16_f32 v105, v106, v107\n\t"
    "ds_write_b64 v125, v[104:105] offset:872\n\t"
    "v_mfma_f32_16x16x32_bf16 v[104:107], v[64:67], v[172:175], 0\n\t"
    "v_lshrrev_b32 v90, 16, v109\n\t"
    "v_and_b32 v109, s92, v109\n\t"
    "ds_read_b64 v[88:89], v109 offset:0\n\t"
    "v_mfma_f32_16x16x32_bf16 v[104:107], v[68:71], v[176:179], v[104:107]\n\t"
    "ds_read_b64 v[90:91], v90 offset:0\n\t"
    "v_mfma_f32_16x16x32_bf16 v[104:107], v[72:75], v[228:231], v[104:107]\n\t"
    "v_lshrrev_b32 v126, 16, v110\n\t"
    "v_mfma_f32_16x16x32_bf16 v[104:107], v[76:79], v[232:235], v[104:107]\n\t"
    "v_and_b32 v110, s92, v110\n\t"
    "v_cvt_pk_bf16_f32 v80, v80, v81\n\t"
    "v_cvt_pk_bf16_f32 v81, v82, v83\n\t"
    "ds_write_b64 v125, v[80:81] offset:32000\n\t"
    "v_mfma_f32_16x16x32_bf16 v[80:83], v[64:67], v[180:183], 0\n\t"
    "s_waitcnt vmcnt(1) lgkmcnt(6)\n\t"
    "v_smfmac_f32_16x16x64_bf16 v[116:119], v[120:123], v[92:99], v124\n\t"
    "ds_read_b64 v[92:93], v110 offset:0\n\t"
    "v_mfma_f32_16x16x32_bf16 v[80:83], v[68:71], v[184:187], v[80:83]\n\t"
    "ds_read_b64 v[94:95], v126 offset:0\n\t"
    "v_mfma_f32_16x16x32_bf16 v[80:83], v[72:75], v[236:239], v[80:83]\n\t"
    "v_lshrrev_b32 v98, 16, v111\n\t"
    "v_and_b32 v111, s92, v111\n\t"
    "ds_read_b64 v[96:97], v111 offset:0\n\t"
    "v_mfma_f32_16x16x32_bf16 v[80:83], v[76:79], v[240:243], v[80:83]\n\t"
    "ds_read_b64 v[98:99], v98 offset:0\n\t"
    "v_cvt_pk_bf16_f32 v104, v104, v105\n\t"
    "v_cvt_pk_bf16_f32 v105, v106, v107\n\t"
    "ds_write_b64 v125, v[104:105] offset:32128\n\t"
    "v_mfma_f32_16x16x32_bf16 v[104:107], v[64:67], v[188:191], 0\n\t"
    "s_add_u32 s84, s84, 0x8000\n\t"
    "buffer_load_dwordx4 v[108:111], v127, %12, s84 offen offset:0 sc1\n\t"
    "v_lshrrev_b32 v126, 16, v112\n\t"
    "v_mfma_f32_16x16x32_bf16 v[104:107], v[68:71], v[192:195], v[104:107]\n\t"
    "v_cvt_pk_bf16_f32 v20, v116, v117\n\t"
    "v_cvt_pk_bf16_f32 v21, v118, v119\n\t"
    "v_and_b32 v119, s67, v23\n\t"
    "v_lshlrev_b32 v118, 16, v23\n\t"
    "v_and_b32 v117, s67, v22\n\t"
    "v_lshlrev_b32 v116, 16, v22\n\t"
    "v_and_b32 v112, s92, v112\n\t"
    "s_waitcnt lgkmcnt(6)\n\t"
    "v_smfmac_f32_16x16x64_bf16 v[116:119], v[120:123], v[84:91], v124\n\t"
    "ds_read_b64 v[84:85], v112 offset:0\n\t"
    "v_mfma_f32_16x16x32_bf16 v[104:107], v[72:75], v[244:247], v[104:107]\n\t"
    "ds_read_b64 v[86:87], v126 offset:0\n\t"
    "v_mfma_f32_16x16x32_bf16 v[104:107], v[76:79], v[248:251], v[104:107]\n\t"
    "v_lshrrev_b32 v90, 16, v113\n\t"
    "v_and_b32 v113, s92, v113\n\t"
    "ds_read_b64 v[88:89], v113 offset:0\n\t"
    "v_cvt_pk_bf16_f32 v80, v80, v81\n\t"
    "v_cvt_pk_bf16_f32 v81, v82, v83\n\t"
    "ds_write_b64 v125, v[80:81] offset:32256\n\t"
    "v_mfma_f32_16x16x32_bf16 v[80:83], v[64:67], v[196:199], 0\n\t"
    "ds_read_b64 v[90:91], v90 offset:0\n\t"
    "v_mfma_f32_16x16x32_bf16 v[80:83], v[68:71], v[200:203], v[80:83]\n\t"
    "v_lshrrev_b32 v126, 16, v114\n\t"
    "v_mfma_f32_16x16x32_bf16 v[80:83], v[72:75], v[252:255], v[80:83]\n\t"
    "v_and_b32 v114, s92, v114\n\t"
    "v_mfma_f32_16x16x32_bf16 v[80:83], v[76:79], v[128:131], v[80:83]\n\t"
    "v_cvt_pk_bf16_f32 v104, v104, v105\n\t"
    "v_cvt_pk_bf16_f32 v105, v106, v107\n\t"
    "ds_write_b64 v125, v[104:105] offset:32384\n\t"
    "s_waitcnt vmcnt(1) lgkmcnt(7)\n\t"
    "v_smfmac_f32_16x16x64_bf16 v[116:119], v[120:123], v[92:99], v124\n\t"
    "ds_read_b64 v[92:93], v114 offset:0\n\t"
    "v_mfma_f32_16x16x32_bf16 v[104:107], v[64:67], v[204:207], 0\n\t"
    "ds_read_b64 v[94:95], v126 offset:0\n\t"
    "v_mfma_f32_16x16x32_bf16 v[104:107], v[68:71], v[208:211], v[104:107]\n\t"
    "v_lshrrev_b32 v98, 16, v115\n\t"
    "v_and_b32 v115, s92, v115\n\t"
    "ds_read_b64 v[96:97], v115 offset:0\n\t"
    "v_mfma_f32_16x16x32_bf16 v[104:107], v[72:75], v[132:135], v[104:107]\n\t"
    "ds_read_b64 v[98:99], v98 offset:0\n\t"
    "v_mfma_f32_16x16x32_bf16 v[104:107], v[76:79], v[136:139], v[104:107]\n\t"
    "buffer_load_dwordx4 v[112:115], v127, %12, s84 offen offset:1024 sc1\n\t"
    "v_lshrrev_b32 v126, 16, v100\n\t"
    "v_cvt_pk_bf16_f32 v80, v80, v81\n\t"
    "v_cvt_pk_bf16_f32 v81, v82, v83\n\t"
    "ds_write_b64 v125, v[80:81] offset:32512\n\t"
    "v_mfma_f32_16x16x32_bf16 v[80:83], v[64:67], v[156:159], 0\n\t"
    "v_cvt_pk_bf16_f32 v22, v116, v117\n\t"
    "v_cvt_pk_bf16_f32 v23, v118, v119\n\t"
    "v_and_b32 v119, s67, v29\n\t"
    "v_lshlrev_b32 v118, 16, v29\n\t"
    "v_and_b32 v117, s67, v28\n\t"
    "v_lshlrev_b32 v116, 16, v28\n\t"
    "v_and_b32 v100, s92, v100\n\t"
    "s_waitcnt lgkmcnt(6)\n\t"
    "v_smfmac_f32_16x16x64_bf16 v[116:119], v[120:123], v[84:91], v124\n\t"
    "ds_read_b64 v[84:85], v100 offset:0\n\t"
    "v_mfma_f32_16x16x32_bf16 v[80:83], v[68:71], v[160:163], v[80:83]\n\t"
    "ds_read_b64 v[86:87], v126 offset:0\n\t"
    "v_mfma_f32_16x16x32_bf16 v[80:83], v[72:75], v[140:143], v[80:83]\n\t"
    "v_lshrrev_b32 v90, 16, v101\n\t"
    "v_and_b32 v101, s92, v101\n\t"
    "ds_read_b64 v[88:89], v101 offset:0\n\t"
    "v_mfma_f32_16x16x32_bf16 v[80:83], v[76:79], v[144:147], v[80:83]\n\t"
    "ds_read_b64 v[90:91], v90 offset:0\n\t"
    "v_cvt_pk_bf16_f32 v104, v104, v105\n\t"
    "v_cvt_pk_bf16_f32 v105, v106, v107\n\t"
    "ds_write_b64 v125, v[104:105] offset:32640\n\t"
    "v_mfma_f32_16x16x32_bf16 v[104:107], v[64:67], v[212:215], 0\n\t"
    "v_lshrrev_b32 v126, 16, v102\n\t"
    "v_mfma_f32_16x16x32_bf16 v[104:107], v[68:71], v[216:219], v[104:107]\n\t"
    "v_and_b32 v102, s92, v102\n\t"
    "v_mfma_f32_16x16x32_bf16 v[104:107], v[72:75], v[148:151], v[104:107]\n\t"
    "s_waitcnt vmcnt(1) lgkmcnt(6)\n\t"
    "v_smfmac_f32_16x16x64_bf16 v[116:119], v[120:123], v[92:99], v124\n\t"
    "ds_read_b64 v[92:93], v102 offset:0\n\t"
    "v_mfma_f32_16x16x32_bf16 v[104:107], v[76:79], v[152:155], v[104:107]\n\t"
    "ds_read_b64 v[94:95], v126 offset:0\n\t"
    "v_cvt_pk_bf16_f32 v80, v80, v81\n\t"
    "v_cvt_pk_bf16_f32 v81, v82, v83\n\t"
    "ds_write_b64 v125, v[80:81] offset:32768\n\t"
    "s_nop 3\n\t"
    "v_cvt_pk_bf16_f32 v104, v104, v105\n\t"
    "v_cvt_pk_bf16_f32 v105, v106, v107\n\t"
    "ds_write_b64 v125, v[104:105] offset:32872\n\t"
    "v_lshrrev_b32 v98, 16, v103\n\t"
    "v_and_b32 v103, s92, v103\n\t"
    "ds_read_b64 v[96:97], v103 offset:0\n\t"
    "ds_read_b64 v[98:99], v98 offset:0\n\t"
    "s_add_u32 s85, s85, 0x8000\n\t"
    "buffer_load_dwordx4 v[100:103], v127, %12, s85 offen offset:0 sc1\n\t"
    "v_lshrrev_b32 v126, 16, v108\n\t"
    "v_cvt_pk_bf16_f32 v28, v116, v117\n\t"
    "v_cvt_pk_bf16_f32 v29, v118, v119\n\t"
    "v_and_b32 v119, s67, v31\n\t"
    "v_lshlrev_b32 v118, 16, v31\n\t"
    "v_and_b32 v117, s67, v30\n\t"
    "v_lshlrev_b32 v116, 16, v30\n\t"
    "v_and_b32 v108, s92, v108\n\t"
    "v_mov_b32 v155, 138896\n\t"
    "v_mov_b32 v144, 0\n\t"
    "v_mov_b32 v145, 0\n\t"
    "ds_write_b64 v155, v[144:145]\n\t"
    "s_waitcnt lgkmcnt(0)\n\t"
    "s_barrier\n\t"
    "v_smfmac_f32_16x16x64_bf16 v[116:119], v[120:123], v[84:91], v124\n\t"
    "ds_read_b64 v[84:85], v108 offset:64000\n\t"
    "ds_read_b64 v[86:87], v126 offset:64000\n\t"
    "v_lshrrev_b32 v90, 16, v109\n\t"
    "v_and_b32 v109, s92, v109\n\t"
    "ds_read_b64 v[88:89], v109 offset:64000\n\t"
    "ds_read_b64 v[90:91], v90 offset:64000\n\t"
    "buffer_load_dwordx4 v[104:107], v127, %12, s85 offen offset:1024 sc1\n\t"
    "v_lshrrev_b32 v126, 16, v110\n\t"
    "v_and_b32 v110, s92, v110\n\t"
    "s_waitcnt vmcnt(2)\n\t"
    "v_smfmac_f32_16x16x64_bf16 v[116:119], v[120:123], v[92:99], v124\n\t"
    "ds_read_b64 v[92:93], v110 offset:64000\n\t"
    "ds_read_b64 v[94:95], v126 offset:64000\n\t"
    "v_lshrrev_b32 v98, 16, v111\n\t"
    "v_and_b32 v111, s92, v111\n\t"
    "ds_read_b64 v[96:97], v111 offset:64000\n\t"
    "ds_read_b64 v[98:99], v98 offset:64000\n\t"
    "s_add_u32 s86, s86, 0x8000\n\t"
    "buffer_load_dwordx4 v[108:111], v127, %12, s86 offen offset:0 sc1\n\t"
    "v_lshrrev_b32 v252, 16, v112\n\t"
    "v_and_b32 v112, s92, v112\n\t"
    "v_lshrrev_b32 v253, 16, v113\n\t"
    "v_cvt_pk_bf16_f32 v30, v116, v117\n\t"
    "v_cvt_pk_bf16_f32 v31, v118, v119\n\t"
    "v_and_b32 v119, s67, v5\n\t"
    "v_lshlrev_b32 v118, 16, v5\n\t"
    "v_and_b32 v117, s67, v4\n\t"
    "v_lshlrev_b32 v116, 16, v4\n\t"
    "v_and_b32 v113, s92, v113\n\t"
    "v_mov_b32 v82, 0xbdd2d3e8\n\t"
    "v_mov_b32 v83, 0xbdd2d3e8\n\t"
    "v_mov_b32 v81, 0xc0135761\n\t"
    "v_lshrrev_b32 v125, 2, v127\n\t"
    "v_mov_b32 v80, 0xc0135761\n\t"
    "s_waitcnt lgkmcnt(4)\n\t"
    "v_smfmac_f32_16x16x64_bf16 v[116:119], v[120:123], v[84:91], v124\n\t"
    "ds_read_b64 v[84:85], v112 offset:64000\n\t"
    "ds_read_b64 v[86:87], v252 offset:64000\n\t"
    "ds_read_b64 v[88:89], v113 offset:64000\n\t"
    "ds_read_b64 v[90:91], v253 offset:64000\n\t"
    "v_lshrrev_b32 v254, 16, v114\n\t"
    "v_and_b32 v114, s92, v114\n\t"
    "v_lshrrev_b32 v255, 16, v115\n\t"
    "v_and_b32 v115, s92, v115\n\t"
    "s_lshl_b32 s65, %16, 10\n\t"
    "s_add_u32 s65, s65, 30408704\n\t"
    "s_xor_b32 s61, %14, 8\n\t"
    "s_mul_i32 s61, s61, 69632\n\t"
    "s_add_u32 s61, s61, s65\n\t"
    "s_mul_i32 s94, %14, 69632\n\t"
    "s_add_u32 s94, s94, s65\n\t"
    "s_lshl_b32 s65, %16, 9\n\t"
    "s_sub_u32 s65, 0x10000, s65\n\t"
    "s_add_u32 s93, s94, s65\n\t"
    "s_add_u32 s96, s61, s65\n\t"
    "v_mov_b32 v165, 1\n\t"
    "s_waitcnt vmcnt(2) lgkmcnt(4)\n\t"
    "v_smfmac_f32_16x16x64_bf16 v[116:119], v[120:123], v[92:99], v124\n\t"
    "ds_read_b64 v[92:93], v114 offset:64000\n\t"
    "ds_read_b64 v[94:95], v254 offset:64000\n\t"
    "ds_read_b64 v[96:97], v115 offset:64000\n\t"
    "ds_read_b64 v[98:99], v255 offset:64000\n\t"
    "buffer_load_dwordx4 v[172:175], v127, %12, s86 offen offset:1024 sc1\n\t"
    "v_lshrrev_b32 v252, 16, v100\n\t"
    "v_and_b32 v100, s92, v100\n\t"
    "v_lshrrev_b32 v253, 16, v101\n\t"
    "v_cvt_pk_bf16_f32 v4, v116, v117\n\t"
    "v_cvt_pk_bf16_f32 v5, v118, v119\n\t"
    "v_and_b32 v119, s67, v7\n\t"
    "v_lshlrev_b32 v118, 16, v7\n\t"
    "v_and_b32 v117, s67, v6\n\t"
    "v_lshlrev_b32 v116, 16, v6\n\t"
    "v_and_b32 v101, s92, v101\n\t"
    "v_mov_b32 v156, v120\n\t"
    "v_mov_b32 v160, 0\n\t"
    "v_mov_b32 v161, v121\n\t"
    "v_mov_b32 v157, 0\n\t"
    "v_mov_b32 v158, v122\n\t"
    "v_mov_b32 v162, 0\n\t"
    "v_mov_b32 v163, v123\n\t"
    "v_mov_b32 v159, 0\n\t"
    "s_waitcnt lgkmcnt(4)\n\t"
    "v_smfmac_f32_16x16x64_bf16 v[116:119], v[120:123], v[84:91], v124\n\t"
    "ds_read_b64 v[84:85], v100 offset:64000\n\t"
    "ds_read_b64 v[86:87], v252 offset:64000\n\t"
    "ds_read_b64 v[88:89], v101 offset:64000\n\t"
    "ds_read_b64 v[90:91], v253 offset:64000\n\t"
    "s_add_u32 s87, s87, 0x8000\n\t"
    "buffer_load_dwordx4 v[176:179], v127, %12, s87 offen offset:0 sc1\n\t"
    "v_lshrrev_b32 v254, 16, v102\n\t"
    "v_and_b32 v102, s92, v102\n\t"
    "v_lshrrev_b32 v255, 16, v103\n\t"
    "v_and_b32 v103, s92, v103\n\t"
    "s_cmp_lt_u32 %16, 4\n\t"
    "s_cbranch_scc1 L_tp163_%=\n\t"
    "s_setprio 0\n\t"
    "L_tp163_%=:\n\t"
    "s_waitcnt vmcnt(3) lgkmcnt(4)\n\t"
    "v_smfmac_f32_16x16x64_bf16 v[116:119], v[120:123], v[92:99], v124\n\t"
    "ds_read_b64 v[92:93], v102 offset:64000\n\t"
    "ds_read_b64 v[94:95], v254 offset:64000\n\t"
    "ds_read_b64 v[96:97], v103 offset:64000\n\t"
    "ds_read_b64 v[98:99], v255 offset:64000\n\t"
    "buffer_load_dwordx4 v[180:183], v127, %12, s87 offen offset:1024 sc1\n\t"
    "v_lshrrev_b32 v252, 16, v104\n\t"
    "v_and_b32 v104, s92, v104\n\t"
    "v_lshrrev_b32 v253, 16, v105\n\t"
    "v_cvt_pk_bf16_f32 v6, v116, v117\n\t"
    "v_cvt_pk_bf16_f32 v7, v118, v119\n\t"
    "v_and_b32 v119, s67, v13\n\t"
    "v_lshlrev_b32 v118, 16, v13\n\t"
    "v_and_b32 v117, s67, v12\n\t"
    "v_lshlrev_b32 v116, 16, v12\n\t"
    "v_and_b32 v105, s92, v105\n\t"
    "buffer_store_dwordx4 v[4:7], v127, %12, s61 offen sc1\n\t"
    "s_waitcnt lgkmcnt(4)\n\t"
    "v_smfmac_f32_16x16x64_bf16 v[116:119], v[120:123], v[84:91], v124\n\t"
    "ds_read_b64 v[84:85], v104 offset:64000\n\t"
    "ds_read_b64 v[86:87], v252 offset:64000\n\t"
    "ds_read_b64 v[88:89], v105 offset:64000\n\t"
    "ds_read_b64 v[90:91], v253 offset:64000\n\t"
    "s_add_u32 s88, s88, s95\n\t"
    "buffer_load_dwordx4 v[184:187], v127, %12, s88 offen offset:0 sc1\n\t"
    "v_lshrrev_b32 v254, 16, v106\n\t"
    "v_and_b32 v106, s92, v106\n\t"
    "v_lshrrev_b32 v255, 16, v107\n\t"
    "v_and_b32 v107, s92, v107\n\t"
    "s_waitcnt vmcnt(5) lgkmcnt(4)\n\t"
    "v_smfmac_f32_16x16x64_bf16 v[116:119], v[120:123], v[92:99], v124\n\t"
    "ds_read_b64 v[92:93], v106 offset:64000\n\t"
    "ds_read_b64 v[94:95], v254 offset:64000\n\t"
    "ds_read_b64 v[96:97], v107 offset:64000\n\t"
    "ds_read_b64 v[98:99], v255 offset:64000\n\t"
    "s_add_u32 s88, s88, 0x8000\n\t"
    "buffer_load_dwordx4 v[188:191], v127, %12, s88 offen offset:0 sc1\n\t"
    "v_lshrrev_b32 v252, 16, v108\n\t"
    "v_and_b32 v108, s92, v108\n\t"
    "v_lshrrev_b32 v253, 16, v109\n\t"
    "v_cvt_pk_bf16_f32 v12, v116, v117\n\t"
    "v_cvt_pk_bf16_f32 v13, v118, v119\n\t"
    "v_and_b32 v119, s67, v15\n\t"
    "v_lshlrev_b32 v118, 16, v15\n\t"
    "v_and_b32 v117, s67, v14\n\t"
    "v_lshlrev_b32 v116, 16, v14\n\t"
    "v_and_b32 v109, s92, v109\n\t"
    "s_waitcnt lgkmcnt(4)\n\t"
    "v_smfmac_f32_16x16x64_bf16 v[116:119], v[120:123], v[84:91], v124\n\t"
    "ds_read_b64 v[84:85], v108 offset:64000\n\t"
    "ds_read_b64 v[86:87], v252 offset:64000\n\t"
    "ds_read_b64 v[88:89], v109 offset:64000\n\t"
    "ds_read_b64 v[90:91], v253 offset:64000\n\t"
    "s_sub_u32 s88, s88, 0x8000\n\t"
    "buffer_load_dwordx4 v[192:195], v127, %12, s88 offen offset:1024 sc1\n\t"
    "v_lshrrev_b32 v254, 16, v110\n\t"
    "v_and_b32 v110, s92, v110\n\t"
    "v_lshrrev_b32 v255, 16, v111\n\t"
    "v_and_b32 v111, s92, v111\n\t"
    "s_waitcnt vmcnt(6) lgkmcnt(4)\n\t"
    "v_smfmac_f32_16x16x64_bf16 v[116:119], v[120:123], v[92:99], v124\n\t"
    "ds_read_b64 v[92:93], v110 offset:64000\n\t"
    "ds_read_b64 v[94:95], v254 offset:64000\n\t"
    "ds_read_b64 v[96:97], v111 offset:64000\n\t"
    "ds_read_b64 v[98:99], v255 offset:64000\n\t"
    "s_add_u32 s88, s88, 0x8000\n\t"
    "buffer_load_dwordx4 v[196:199], v127, %12, s88 offen offset:1024 sc1\n\t"
    "v_lshrrev_b32 v252, 16, v172\n\t"
    "v_and_b32 v172, s92, v172\n\t"
    "v_lshrrev_b32 v253, 16, v173\n\t"
    "v_cvt_pk_bf16_f32 v14, v116, v117\n\t"
    "v_cvt_pk_bf16_f32 v15, v118, v119\n\t"
    "v_and_b32 v119, s67, v21\n\t"
    "v_lshlrev_b32 v118, 16, v21\n\t"
    "v_and_b32 v117, s67, v20\n\t"
    "v_lshlrev_b32 v116, 16, v20\n\t"
    "v_and_b32 v173, s92, v173\n\t"
    "s_waitcnt lgkmcnt(4)\n\t"
    "v_smfmac_f32_16x16x64_bf16 v[116:119], v[120:123], v[84:91], v124\n\t"
    "ds_read_b64 v[84:85], v172 offset:64000\n\t"
    "ds_read_b64 v[86:87], v252 offset:64000\n\t"
    "ds_read_b64 v[88:89], v173 offset:64000\n\t"
    "ds_read_b64 v[90:91], v253 offset:64000\n\t"
    "v_lshrrev_b32 v254, 16, v174\n\t"
    "v_and_b32 v174, s92, v174\n\t"
    "v_lshrrev_b32 v255, 16, v175\n\t"
    "v_and_b32 v175, s92, v175\n\t"
    "s_waitcnt vmcnt(6) lgkmcnt(4)\n\t"
    "v_smfmac_f32_16x16x64_bf16 v[116:119], v[120:123], v[92:99], v124\n\t"
    "ds_read_b64 v[92:93], v174 offset:64000\n\t"
    "ds_read_b64 v[94:95], v254 offset:64000\n\t"
    "ds_read_b64 v[96:97], v175 offset:64000\n\t"
    "ds_read_b64 v[98:99], v255 offset:64000\n\t"
    "s_add_u32 s89, s89, s95\n\t"
    "buffer_load_dwordx4 v[200:203], v127, %12, s89 offen offset:0 sc1\n\t"
    "v_lshrrev_b32 v252, 16, v176\n\t"
    "v_and_b32 v176, s92, v176\n\t"
    "v_lshrrev_b32 v253, 16, v177\n\t"
    "v_cvt_pk_bf16_f32 v20, v116, v117\n\t"
    "v_cvt_pk_bf16_f32 v21, v118, v119\n\t"
    "v_and_b32 v119, s67, v23\n\t"
    "v_lshlrev_b32 v118, 16, v23\n\t"
    "v_and_b32 v117, s67, v22\n\t"
    "v_lshlrev_b32 v116, 16, v22\n\t"
    "v_and_b32 v177, s92, v177\n\t"
    "s_waitcnt lgkmcnt(4)\n\t"
    "v_smfmac_f32_16x16x64_bf16 v[116:119], v[120:123], v[84:91], v124\n\t"
    "ds_read_b64 v[84:85], v176 offset:64000\n\t"
    "ds_read_b64 v[86:87], v252 offset:64000\n\t"
    "ds_read_b64 v[88:89], v177 offset:64000\n\t"
    "ds_read_b64 v[90:91], v253 offset:64000\n\t"
    "v_lshrrev_b32 v254, 16, v178\n\t"
    "v_and_b32 v178, s92, v178\n\t"
    "v_lshrrev_b32 v255, 16, v179\n\t"
    "v_and_b32 v179, s92, v179\n\t"
    "s_waitcnt vmcnt(6) lgkmcnt(4)\n\t"
    "v_smfmac_f32_16x16x64_bf16 v[116:119], v[120:123], v[92:99], v124\n\t"
    "ds_read_b64 v[92:93], v178 offset:64000\n\t"
    "ds_read_b64 v[94:95], v254 offset:64000\n\t"
    "ds_read_b64 v[96:97], v179 offset:64000\n\t"
    "ds_read_b64 v[98:99], v255 offset:64000\n\t"
    "s_add_u32 s89, s89, 0x8000\n\t"
    "buffer_load_dwordx4 v[236:239], v127, %12, s89 offen offset:0 sc1\n\t"
    "v_lshrrev_b32 v252, 16, v180\n\t"
    "v_and_b32 v180, s92, v180\n\t"
    "v_lshrrev_b32 v253, 16, v181\n\t"
    "v_cvt_pk_bf16_f32 v22, v116, v117\n\t"
    "v_cvt_pk_bf16_f32 v23, v118, v119\n\t"
    "v_and_b32 v119, s67, v29\n\t"
    "v_lshlrev_b32 v118, 16, v29\n\t"
    "v_and_b32 v117, s67, v28\n\t"
    "v_lshlrev_b32 v116, 16, v28\n\t"
    "v_and_b32 v181, s92, v181\n\t"
    "s_add_u32 s61, s61, 0x2000\n\t"
    "buffer_store_dwordx4 v[12:15], v127, %12, s61 offen sc1\n\t"
    "s_waitcnt lgkmcnt(4)\n\t"
    "v_smfmac_f32_16x16x64_bf16 v[116:119], v[120:123], v[84:91], v124\n\t"
    "ds_read_b64 v[84:85], v180 offset:64000\n\t"
    "ds_read_b64 v[86:87], v252 offset:64000\n\t"
    "ds_read_b64 v[88:89], v181 offset:64000\n\t"
    "ds_read_b64 v[90:91], v253 offset:64000\n\t"
    "v_lshrrev_b32 v254, 16, v182\n\t"
    "v_and_b32 v182, s92, v182\n\t"
    "v_lshrrev_b32 v255, 16, v183\n\t"
    "v_and_b32 v183, s92, v183\n\t"
    "s_waitcnt vmcnt(6) lgkmcnt(4)\n\t"
    "v_smfmac_f32_16x16x64_bf16 v[116:119], v[120:123], v[92:99], v124\n\t"
    "ds_read_b64 v[92:93], v182 offset:64000\n\t"
    "ds_read_b64 v[94:95], v254 offset:64000\n\t"
    "ds_read_b64 v[96:97], v183 offset:64000\n\t"
    "ds_read_b64 v[98:99], v255 offset:64000\n\t"
    "s_sub_u32 s89, s89, 0x8000\n\t"
    "buffer_load_dwordx4 v[204:207], v127, %12, s89 offen offset:1024 sc1\n\t"
    "v_lshrrev_b32 v252, 16, v184\n\t"
    "v_and_b32 v184, s92, v184\n\t"
    "v_lshrrev_b32 v253, 16, v185\n\t"
    "v_cvt_pk_bf16_f32 v28, v116, v117\n\t"
    "v_cvt_pk_bf16_f32 v29, v118, v119\n\t"
    "v_and_b32 v119, s67, v31\n\t"
    "v_lshlrev_b32 v118, 16, v31\n\t"
    "v_and_b32 v117, s67, v30\n\t"
    "v_lshlrev_b32 v116, 16, v30\n\t"
    "v_and_b32 v185, s92, v185\n\t"
    "s_waitcnt lgkmcnt(4)\n\t"
    "v_smfmac_f32_16x16x64_bf16 v[116:119], v[120:123], v[84:91], v124\n\t"
    "ds_read_b64 v[84:85], v184 offset:0\n\t"
    "ds_read_b64 v[86:87], v252 offset:0\n\t"
    "ds_read_b64 v[88:89], v185 offset:0\n\t"
    "ds_read_b64 v[90:91], v253 offset:0\n\t"
    "v_lshrrev_b32 v254, 16, v186\n\t"
    "v_and_b32 v186, s92, v186\n\t"
    "v_lshrrev_b32 v255, 16, v187\n\t"
    "v_and_b32 v187, s92, v187\n\t"
    "s_cmp_lt_u32 %16, 4\n\t"
    "s_cbranch_scc1 L_tp175_%=\n\t"
    "s_setprio 1\n\t"
    "L_tp175_%=:\n\t"
    "s_waitcnt vmcnt(6) lgkmcnt(4)\n\t"
    "v_smfmac_f32_16x16x64_bf16 v[116:119], v[120:123], v[92:99], v124\n\t"
    "ds_read_b64 v[92:93], v186 offset:0\n\t"
    "ds_read_b64 v[94:95], v254 offset:0\n\t"
    "ds_read_b64 v[96:97], v187 offset:0\n\t"
    "ds_read_b64 v[98:99], v255 offset:0\n\t"
    "s_add_u32 s89, s89, 0x8000\n\t"
    "buffer_load_dwordx4 v[128:131], v127, %12, s89 offen offset:1024 sc1\n\t"
    "v_lshrrev_b32 v252, 16, v188\n\t"
    "v_and_b32 v188, s92, v188\n\t"
    "v_lshrrev_b32 v253, 16, v189\n\t"
    "v_cvt_pk_bf16_f32 v30, v116, v117\n\t"
    "v_cvt_pk_bf16_f32 v31, v118, v119\n\t"
    "v_and_b32 v119, s67, v37\n\t"
    "v_lshlrev_b32 v118, 16, v37\n\t"
    "v_and_b32 v117, s67, v36\n\t"
    "v_lshlrev_b32 v116, 16, v36\n\t"
    "v_and_b32 v189, s92, v189\n\t"
    "s_waitcnt lgkmcnt(4)\n\t"
    "v_smfmac_f32_16x16x64_bf16 v[116:119], v[120:123], v[84:91], v124\n\t"
    "ds_read_b64 v[84:85], v188 offset:64000\n\t"
    "ds_read_b64 v[86:87], v252 offset:64000\n\t"
    "ds_read_b64 v[88:89], v189 offset:64000\n\t"
    "ds_read_b64 v[90:91], v253 offset:64000\n\t"
    "v_lshrrev_b32 v254, 16, v190\n\t"
    "v_and_b32 v190, s92, v190\n\t"
    "v_lshrrev_b32 v255, 16, v191\n\t"
    "v_and_b32 v191, s92, v191\n\t"
    "s_waitcnt vmcnt(6) lgkmcnt(4)\n\t"
    "v_smfmac_f32_16x16x64_bf16 v[116:119], v[120:123], v[92:99], v124\n\t"
    "ds_read_b64 v[92:93], v190 offset:64000\n\t"
    "ds_read_b64 v[94:95], v254 offset:64000\n\t"
    "ds_read_b64 v[96:97], v191 offset:64000\n\t"
    "ds_read_b64 v[98:99], v255 offset:64000\n\t"
    "s_add_u32 s90, s90, s95\n\t"
    "buffer_load_dwordx4 v[132:135], v127, %12, s90 offen offset:0 sc1\n\t"
    "v_lshrrev_b32 v252, 16, v192\n\t"
    "v_and_b32 v192, s92, v192\n\t"
    "v_lshrrev_b32 v253, 16, v193\n\t"
    "v_and_b32 v193, s92, v193\n\t"
    "s_waitcnt lgkmcnt(4)\n\t"
    "v_smfmac_f32_16x16x64_bf16 v[116:119], v[120:123], v[84:91], v124\n\t"
    "ds_read_b64 v[84:85], v192 offset:0\n\t"
    "ds_read_b64 v[86:87], v252 offset:0\n\t"
    "ds_read_b64 v[88:89], v193 offset:0\n\t"
    "ds_read_b64 v[90:91], v253 offset:0\n\t"
    "v_lshrrev_b32 v254, 16, v194\n\t"
    "v_and_b32 v194, s92, v194\n\t"
    "v_lshrrev_b32 v255, 16, v195\n\t"
    "v_and_b32 v195, s92, v195\n\t"
    "s_waitcnt vmcnt(6) lgkmcnt(4)\n\t"
    "v_smfmac_f32_16x16x64_bf16 v[116:119], v[120:123], v[92:99], v124\n\t"
    "ds_read_b64 v[92:93], v194 offset:0\n\t"
    "ds_read_b64 v[94:95], v254 offset:0\n\t"
    "ds_read_b64 v[96:97], v195 offset:0\n\t"
    "ds_read_b64 v[98:99], v255 offset:0\n\t"
    "s_add_u32 s90, s90, 0x8000\n\t"
    "buffer_load_dwordx4 v[136:139], v127, %12, s90 offen offset:0 sc1\n\t"
    "v_lshrrev_b32 v252, 16, v196\n\t"
    "v_and_b32 v196, s92, v196\n\t"
    "v_lshrrev_b32 v253, 16, v197\n\t"
    "v_cvt_pk_bf16_f32 v36, v116, v117\n\t"
    "v_cvt_pk_bf16_f32 v37, v118, v119\n\t"
    "v_and_b32 v119, s67, v39\n\t"
    "v_lshlrev_b32 v118, 16, v39\n\t"
    "v_and_b32 v117, s67, v38\n\t"
    "v_lshlrev_b32 v116, 16, v38\n\t"
    "v_and_b32 v197, s92, v197\n\t"
    "s_waitcnt vmcnt(4)\n\t"
    "s_mov_b64 exec, 1\n\t"
    "buffer_store_dword v165, off, %12, s96 offset:0 sc1\n\t"
    "s_mov_b64 exec, -1\n\t"
    "s_add_u32 s61, s61, 0x2000\n\t"
    "buffer_store_dwordx4 v[20:23], v127, %12, s61 offen sc1\n\t"
    "s_waitcnt lgkmcnt(4)\n\t"
    "v_smfmac_f32_16x16x64_bf16 v[116:119], v[120:123], v[84:91], v124\n\t"
    "ds_read_b64 v[84:85], v196 offset:64000\n\t"
    "ds_read_b64 v[86:87], v252 offset:64000\n\t"
    "ds_read_b64 v[88:89], v197 offset:64000\n\t"
    "ds_read_b64 v[90:91], v253 offset:64000\n\t"
    "v_lshrrev_b32 v254, 16, v198\n\t"
    "v_and_b32 v198, s92, v198\n\t"
    "v_lshrrev_b32 v255, 16, v199\n\t"
    "v_and_b32 v199, s92, v199\n\t"
    "s_waitcnt lgkmcnt(4)\n\t"
    "v_smfmac_f32_16x16x64_bf16 v[116:119], v[120:123], v[92:99], v124\n\t"
    "ds_read_b64 v[92:93], v198 offset:64000\n\t"
    "ds_read_b64 v[94:95], v254 offset:64000\n\t"
    "ds_read_b64 v[96:97], v199 offset:64000\n\t"
    "ds_read_b64 v[98:99], v255 offset:64000\n\t"
    "s_sub_u32 s90, s90, 0x8000\n\t"
    "buffer_load_dwordx4 v[140:143], v127, %12, s90 offen offset:1024 sc1\n\t"
    "v_lshrrev_b32 v252, 16, v200\n\t"
    "v_and_b32 v200, s92, v200\n\t"
    "v_lshrrev_b32 v253, 16, v201\n\t"
    "v_and_b32 v201, s92, v201\n\t"
    "s_waitcnt lgkmcnt(4)\n\t"
    "v_smfmac_f32_16x16x64_bf16 v[116:119], v[120:123], v[84:91], v124\n\t"
    "ds_read_b64 v[84:85], v200 offset:0\n\t"
    "ds_read_b64 v[86:87], v252 offset:0\n\t"
    "ds_read_b64 v[88:89], v201 offset:0\n\t"
    "ds_read_b64 v[90:91], v253 offset:0\n\t"
    "v_lshrrev_b32 v254, 16, v202\n\t"
    "v_and_b32 v202, s92, v202\n\t"
    "v_lshrrev_b32 v255, 16, v203\n\t"
    "v_and_b32 v203, s92, v203\n\t"
    "s_waitcnt lgkmcnt(4)\n\t"
    "v_smfmac_f32_16x16x64_bf16 v[116:119], v[120:123], v[92:99], v124\n\t"
    "ds_read_b64 v[92:93], v202 offset:0\n\t"
    "ds_read_b64 v[94:95], v254 offset:0\n\t"
    "ds_read_b64 v[96:97], v203 offset:0\n\t"
    "ds_read_b64 v[98:99], v255 offset:0\n\t"
    "s_add_u32 s90, s90, 0x8000\n\t"
    "buffer_load_dwordx4 v[144:147], v127, %12, s90 offen offset:1024 sc1\n\t"
    "v_lshrrev_b32 v252, 16, v236\n\t"
    "v_and_b32 v236, s92, v236\n\t"
    "v_lshrrev_b32 v253, 16, v237\n\t"
    "v_cvt_pk_bf16_f32 v38, v116, v117\n\t"
    "v_cvt_pk_bf16_f32 v39, v118, v119\n\t"
    "v_and_b32 v119, s67, v45\n\t"
    "v_lshlrev_b32 v118, 16, v45\n\t"
    "v_and_b32 v117, s67, v44\n\t"
    "v_lshlrev_b32 v116, 16, v44\n\t"
    "v_and_b32 v237, s92, v237\n\t"
    "s_waitcnt lgkmcnt(4)\n\t"
    "v_smfmac_f32_16x16x64_bf16 v[116:119], v[120:123], v[84:91], v124\n\t"
    "ds_read_b64 v[84:85], v236 offset:64000\n\t"
    "ds_read_b64 v[86:87], v252 offset:64000\n\t"
    "ds_read_b64 v[88:89], v237 offset:64000\n\t"
    "ds_read_b64 v[90:91], v253 offset:64000\n\t"
    "v_lshrrev_b32 v254, 16, v238\n\t"
    "v_and_b32 v238, s92, v238\n\t"
    "v_lshrrev_b32 v255, 16, v239\n\t"
    "v_and_b32 v239, s92, v239\n\t"
    "s_waitcnt vmcnt(7) lgkmcnt(4)\n\t"
    "v_smfmac_f32_16x16x64_bf16 v[116:119], v[120:123], v[92:99], v124\n\t"
    "ds_read_b64 v[92:93], v238 offset:64000\n\t"
    "ds_read_b64 v[94:95], v254 offset:64000\n\t"
    "ds_read_b64 v[96:97], v239 offset:64000\n\t"
    "ds_read_b64 v[98:99], v255 offset:64000\n\t"
    "s_add_u32 s91, s91, s95\n\t"
    "buffer_load_dwordx4 v[148:151], v127, %12, s91 offen offset:0 sc1\n\t"
    "v_lshrrev_b32 v252, 16, v204\n\t"
    "v_and_b32 v204, s92, v204\n\t"
    "v_lshrrev_b32 v253, 16, v205\n\t"
    "v_and_b32 v205, s92, v205\n\t"
    "s_waitcnt lgkmcnt(4)\n\t"
    "v_smfmac_f32_16x16x64_bf16 v[116:119], v[120:123], v[84:91], v124\n\t"
    "ds_read_b64 v[84:85], v204 offset:0\n\t"
    "ds_read_b64 v[86:87], v252 offset:0\n\t"
    "ds_read_b64 v[88:89], v205 offset:0\n\t"
    "ds_read_b64 v[90:91], v253 offset:0\n\t"
    "v_lshrrev_b32 v254, 16, v206\n\t"
    "v_and_b32 v206, s92, v206\n\t"
    "v_lshrrev_b32 v255, 16, v207\n\t"
    "v_and_b32 v207, s92, v207\n\t"
    "s_waitcnt vmcnt(7) lgkmcnt(4)\n\t"
    "v_smfmac_f32_16x16x64_bf16 v[116:119], v[120:123], v[92:99], v124\n\t"
    "ds_read_b64 v[92:93], v206 offset:0\n\t"
    "ds_read_b64 v[94:95], v254 offset:0\n\t"
    "ds_read_b64 v[96:97], v207 offset:0\n\t"
    "ds_read_b64 v[98:99], v255 offset:0\n\t"
    "s_add_u32 s91, s91, 0x8000\n\t"
    "buffer_load_dwordx4 v[152:155], v127, %12, s91 offen offset:0 sc1\n\t"
    "v_lshrrev_b32 v252, 16, v128\n\t"
    "v_and_b32 v128, s92, v128\n\t"
    "v_lshrrev_b32 v253, 16, v129\n\t"
    "v_cvt_pk_bf16_f32 v44, v116, v117\n\t"
    "v_cvt_pk_bf16_f32 v45, v118, v119\n\t"
    "v_and_b32 v119, s67, v47\n\t"
    "v_lshlrev_b32 v118, 16, v47\n\t"
    "v_and_b32 v117, s67, v46\n\t"
    "v_lshlrev_b32 v116, 16, v46\n\t"
    "v_and_b32 v129, s92, v129\n\t"
    "s_add_u32 s61, s61, 0x2000\n\t"
    "buffer_store_dwordx4 v[28:31], v127, %12, s61 offen sc1\n\t"
    "s_waitcnt lgkmcnt(4)\n\t"
    "v_smfmac_f32_16x16x64_bf16 v[116:119], v[120:123], v[84:91], v124\n\t"
    "ds_read_b64 v[84:85], v128 offset:64000\n\t"
    "ds_read_b64 v[86:87], v252 offset:64000\n\t"
    "ds_read_b64 v[88:89], v129 offset:64000\n\t"
    "ds_read_b64 v[90:91], v253 offset:64000\n\t"
    "v_lshrrev_b32 v254, 16, v130\n\t"
    "v_and_b32 v130, s92, v130\n\t"
    "v_lshrrev_b32 v255, 16, v131\n\t"
    "v_and_b32 v131, s92, v131\n\t"
    "s_waitcnt vmcnt(8) lgkmcnt(4)\n\t"
    "v_smfmac_f32_16x16x64_bf16 v[116:119], v[120:123], v[92:99], v124\n\t"
    "ds_read_b64 v[92:93], v130 offset:64000\n\t"
    "ds_read_b64 v[94:95], v254 offset:64000\n\t"
    "ds_read_b64 v[96:97], v131 offset:64000\n\t"
    "ds_read_b64 v[98:99], v255 offset:64000\n\t"
    "s_sub_u32 s91, s91, 0x8000\n\t"
    "buffer_load_dwordx4 v[208:211], v127, %12, s91 offen offset:1024 sc1\n\t"
    "v_lshrrev_b32 v252, 16, v132\n\t"
    "v_and_b32 v132, s92, v132\n\t"
    "v_lshrrev_b32 v253, 16, v133\n\t"
    "v_and_b32 v133, s92, v133\n\t"
    "s_waitcnt lgkmcnt(4)\n\t"
    "v_smfmac_f32_16x16x64_bf16 v[116:119], v[120:123], v[84:91], v124\n\t"
    "ds_read_b64 v[84:85], v132 offset:0\n\t"
    "ds_read_b64 v[86:87], v252 offset:0\n\t"
    "ds_read_b64 v[88:89], v133 offset:0\n\t"
    "ds_read_b64 v[90:91], v253 offset:0\n\t"
    "v_lshrrev_b32 v254, 16, v134\n\t"
    "v_and_b32 v134, s92, v134\n\t"
    "v_lshrrev_b32 v255, 16, v135\n\t"
    "v_and_b32 v135, s92, v135\n\t"
    "s_waitcnt vmcnt(8) lgkmcnt(4)\n\t"
    "v_smfmac_f32_16x16x64_bf16 v[116:119], v[120:123], v[92:99], v124\n\t"
    "ds_read_b64 v[92:93], v134 offset:0\n\t"
    "ds_read_b64 v[94:95], v254 offset:0\n\t"
    "ds_read_b64 v[96:97], v135 offset:0\n\t"
    "ds_read_b64 v[98:99], v255 offset:0\n\t"
    "s_add_u32 s91, s91, 0x8000\n\t"
    "buffer_load_dwordx4 v[212:215], v127, %12, s91 offen offset:1024 sc1\n\t"
    "v_lshrrev_b32 v252, 16, v136\n\t"
    "v_and_b32 v136, s92, v136\n\t"
    "v_lshrrev_b32 v253, 16, v137\n\t"
    "v_cvt_pk_bf16_f32 v46, v116, v117\n\t"
    "v_cvt_pk_bf16_f32 v47, v118, v119\n\t"
    "v_and_b32 v119, s67, v53\n\t"
    "v_lshlrev_b32 v118, 16, v53\n\t"
    "v_and_b32 v117, s67, v52\n\t"
    "v_lshlrev_b32 v116, 16, v52\n\t"
    "v_and_b32 v137, s92, v137\n\t"
    "s_waitcnt lgkmcnt(4)\n\t"
    "v_smfmac_f32_16x16x64_bf16 v[116:119], v[120:123], v[84:91], v124\n\t"
    "ds_read_b64 v[84:85], v136 offset:64000\n\t"
    "ds_read_b64 v[86:87], v252 offset:64000\n\t"
    "ds_read_b64 v[88:89], v137 offset:64000\n\t"
    "ds_read_b64 v[90:91], v253 offset:64000\n\t"
    "v_lshrrev_b32 v254, 16, v138\n\t"
    "v_and_b32 v138, s92, v138\n\t"
    "v_lshrrev_b32 v255, 16, v139\n\t"
    "v_and_b32 v139, s92, v139\n\t"
    "s_waitcnt vmcnt(6) lgkmcnt(4)\n\t"
    "v_smfmac_f32_16x16x64_bf16 v[116:119], v[120:123], v[92:99], v124\n\t"
    "ds_read_b64 v[92:93], v138 offset:64000\n\t"
    "ds_read_b64 v[94:95], v254 offset:64000\n\t"
    "ds_read_b64 v[96:97], v139 offset:64000\n\t"
    "ds_read_b64 v[98:99], v255 offset:64000\n\t"
    "s_sub_u32 s84, s84, s95\n\t"
    "buffer_load_dwordx4 v[216:219], v127, %12, s84 offen offset:0 sc1\n\t"
    "v_lshrrev_b32 v252, 16, v140\n\t"
    "v_and_b32 v140, s92, v140\n\t"
    "v_lshrrev_b32 v253, 16, v141\n\t"
    "v_and_b32 v141, s92, v141\n\t"
    "s_waitcnt lgkmcnt(4)\n\t"
    "v_smfmac_f32_16x16x64_bf16 v[116:119], v[120:123], v[84:91], v124\n\t"
    "ds_read_b64 v[84:85], v140 offset:0\n\t"
    "ds_read_b64 v[86:87], v252 offset:0\n\t"
    "ds_read_b64 v[88:89], v141 offset:0\n\t"
    "ds_read_b64 v[90:91], v253 offset:0\n\t"
    "v_lshrrev_b32 v254, 16, v142\n\t"
    "v_and_b32 v142, s92, v142\n\t"
    "v_lshrrev_b32 v255, 16, v143\n\t"
    "v_and_b32 v143, s92, v143\n\t"
    "s_cmp_lt_u32 %16, 4\n\t"
    "s_cbranch_scc1 L_tp195_%=\n\t"
    "s_setprio 0\n\t"
    "L_tp195_%=:\n\t"
    "s_waitcnt vmcnt(6) lgkmcnt(4)\n\t"
    "v_smfmac_f32_16x16x64_bf16 v[116:119], v[120:123], v[92:99], v124\n\t"
    "ds_read_b64 v[92:93], v142 offset:0\n\t"
    "ds_read_b64 v[94:95], v254 offset:0\n\t"
    "ds_read_b64 v[96:97], v143 offset:0\n\t"
    "ds_read_b64 v[98:99], v255 offset:0\n\t"
    "buffer_load_dwordx4 v[240:243], v127, %12, s84 offen offset:1024 sc1\n\t"
    "v_lshrrev_b32 v252, 16, v144\n\t"
    "v_and_b32 v144, s92, v144\n\t"
    "v_lshrrev_b32 v253, 16, v145\n\t"
    "v_cvt_pk_bf16_f32 v52, v116, v117\n\t"
    "v_cvt_pk_bf16_f32 v53, v118, v119\n\t"
    "v_and_b32 v119, s67, v55\n\t"
    "v_lshlrev_b32 v118, 16, v55\n\t"
    "v_and_b32 v117, s67, v54\n\t"
    "v_lshlrev_b32 v116, 16, v54\n\t"
    "v_and_b32 v145, s92, v145\n\t"
    "s_waitcnt vmcnt(4)\n\t"
    "s_mov_b64 exec, 1\n\t"
    "buffer_store_dword v165, off, %12, s96 offset:128 sc1\n\t"
    "s_mov_b64 exec, -1\n\t"
    "s_add_u32 s61, s61, 0x2000\n\t"
    "buffer_store_dwordx4 v[36:39], v127, %12, s61 offen sc1\n\t"
    "s_waitcnt lgkmcnt(4)\n\t"
    "v_smfmac_f32_16x16x64_bf16 v[116:119], v[120:123], v[84:91], v124\n\t"
    "ds_read_b64 v[84:85], v144 offset:64000\n\t"
    "ds_read_b64 v[86:87], v252 offset:64000\n\t"
    "ds_read_b64 v[88:89], v145 offset:64000\n\t"
    "ds_read_b64 v[90:91], v253 offset:64000\n\t"
    "v_lshrrev_b32 v254, 16, v146\n\t"
    "v_and_b32 v146, s92, v146\n\t"
    "v_lshrrev_b32 v255, 16, v147\n\t"
    "v_and_b32 v147, s92, v147\n\t"
    "s_waitcnt lgkmcnt(4)\n\t"
    "v_smfmac_f32_16x16x64_bf16 v[116:119], v[120:123], v[92:99], v124\n\t"
    "ds_read_b64 v[92:93], v146 offset:64000\n\t"
    "ds_read_b64 v[94:95], v254 offset:64000\n\t"
    "ds_read_b64 v[96:97], v147 offset:64000\n\t"
    "ds_read_b64 v[98:99], v255 offset:64000\n\t"
    "s_sub_u32 s85, s85, s95\n\t"
    "buffer_load_dwordx4 v[220:223], v127, %12, s85 offen offset:0 sc1\n\t"
    "v_lshrrev_b32 v252, 16, v148\n\t"
    "v_and_b32 v148, s92, v148\n\t"
    "v_lshrrev_b32 v253, 16, v149\n\t"
    "v_and_b32 v149, s92, v149\n\t"
    "s_waitcnt lgkmcnt(4)\n\t"
    "v_smfmac_f32_16x16x64_bf16 v[116:119], v[120:123], v[84:91], v124\n\t"
    "ds_read_b64 v[84:85], v148 offset:0\n\t"
    "ds_read_b64 v[86:87], v252 offset:0\n\t"
    "ds_read_b64 v[88:89], v149 offset:0\n\t"
    "ds_read_b64 v[90:91], v253 offset:0\n\t"
    "v_lshrrev_b32 v254, 16, v150\n\t"
    "v_and_b32 v150, s92, v150\n\t"
    "v_lshrrev_b32 v255, 16, v151\n\t"
    "v_and_b32 v151, s92, v151\n\t"
    "s_waitcnt lgkmcnt(4)\n\t"
    "v_smfmac_f32_16x16x64_bf16 v[116:119], v[120:123], v[92:99], v124\n\t"
    "ds_read_b64 v[92:93], v150 offset:0\n\t"
    "ds_read_b64 v[94:95], v254 offset:0\n\t"
    "ds_read_b64 v[96:97], v151 offset:0\n\t"
    "ds_read_b64 v[98:99], v255 offset:0\n\t"
    "buffer_load_dwordx4 v[224:227], v127, %12, s85 offen offset:1024 sc1\n\t"
    "v_lshrrev_b32 v252, 16, v152\n\t"
    "v_and_b32 v152, s92, v152\n\t"
    "v_lshrrev_b32 v253, 16, v153\n\t"
    "v_cvt_pk_bf16_f32 v54, v116, v117\n\t"
    "v_cvt_pk_bf16_f32 v55, v118, v119\n\t"
    "v_and_b32 v119, s67, v61\n\t"
    "v_lshlrev_b32 v118, 16, v61\n\t"
    "v_and_b32 v117, s67, v60\n\t"
    "v_lshlrev_b32 v116, 16, v60\n\t"
    "v_and_b32 v153, s92, v153\n\t"
    "s_waitcnt lgkmcnt(4)\n\t"
    "v_smfmac_f32_16x16x64_bf16 v[116:119], v[120:123], v[84:91], v124\n\t"
    "ds_read_b64 v[84:85], v152 offset:64000\n\t"
    "ds_read_b64 v[86:87], v252 offset:64000\n\t"
    "ds_read_b64 v[88:89], v153 offset:64000\n\t"
    "ds_read_b64 v[90:91], v253 offset:64000\n\t"
    "v_lshrrev_b32 v254, 16, v154\n\t"
    "v_and_b32 v154, s92, v154\n\t"
    "v_lshrrev_b32 v255, 16, v155\n\t"
    "v_and_b32 v155, s92, v155\n\t"
    "s_waitcnt vmcnt(7) lgkmcnt(4)\n\t"
    "v_smfmac_f32_16x16x64_bf16 v[116:119], v[120:123], v[92:99], v124\n\t"
    "ds_read_b64 v[92:93], v154 offset:64000\n\t"
    "ds_read_b64 v[94:95], v254 offset:64000\n\t"
    "ds_read_b64 v[96:97], v155 offset:64000\n\t"
    "ds_read_b64 v[98:99], v255 offset:64000\n\t"
    "s_sub_u32 s86, s86, s95\n\t"
    "buffer_load_dwordx4 v[228:231], v127, %12, s86 offen offset:0 sc1\n\t"
    "v_lshrrev_b32 v252, 16, v208\n\t"
    "v_and_b32 v208, s92, v208\n\t"
    "v_lshrrev_b32 v253, 16, v209\n\t"
    "v_and_b32 v209, s92, v209\n\t"
    "s_waitcnt lgkmcnt(4)\n\t"
    "v_smfmac_f32_16x16x64_bf16 v[116:119], v[120:123], v[84:91], v124\n\t"
    "ds_read_b64 v[84:85], v208 offset:0\n\t"
    "ds_read_b64 v[86:87], v252 offset:0\n\t"
    "ds_read_b64 v[88:89], v209 offset:0\n\t"
    "ds_read_b64 v[90:91], v253 offset:0\n\t"
    "v_lshrrev_b32 v254, 16, v210\n\t"
    "v_and_b32 v210, s92, v210\n\t"
    "v_lshrrev_b32 v255, 16, v211\n\t"
    "v_and_b32 v211, s92, v211\n\t"
    "s_waitcnt vmcnt(7) lgkmcnt(4)\n\t"
    "v_smfmac_f32_16x16x64_bf16 v[116:119], v[120:123], v[92:99], v124\n\t"
    "ds_read_b64 v[92:93], v210 offset:0\n\t"
    "ds_read_b64 v[94:95], v254 offset:0\n\t"
    "ds_read_b64 v[96:97], v211 offset:0\n\t"
    "ds_read_b64 v[98:99], v255 offset:0\n\t"
    "buffer_load_dwordx4 v[232:235], v127, %12, s86 offen offset:1024 sc1\n\t"
    "v_lshrrev_b32 v252, 16, v212\n\t"
    "v_and_b32 v212, s92, v212\n\t"
    "v_lshrrev_b32 v253, 16, v213\n\t"
    "v_cvt_pk_bf16_f32 v60, v116, v117\n\t"
    "v_cvt_pk_bf16_f32 v61, v118, v119\n\t"
    "v_and_b32 v119, s67, v63\n\t"
    "v_lshlrev_b32 v118, 16, v63\n\t"
    "v_and_b32 v117, s67, v62\n\t"
    "v_lshlrev_b32 v116, 16, v62\n\t"
    "v_and_b32 v213, s92, v213\n\t"
    "s_add_u32 s61, s61, 0x2000\n\t"
    "buffer_store_dwordx4 v[44:47], v127, %12, s61 offen sc1\n\t"
    "s_waitcnt lgkmcnt(4)\n\t"
    "v_smfmac_f32_16x16x64_bf16 v[116:119], v[120:123], v[84:91], v124\n\t"
    "ds_read_b64 v[84:85], v212 offset:64000\n\t"
    "ds_read_b64 v[86:87], v252 offset:64000\n\t"
    "ds_read_b64 v[88:89], v213 offset:64000\n\t"
    "ds_read_b64 v[90:91], v253 offset:64000\n\t"
    "v_lshrrev_b32 v254, 16, v214\n\t"
    "v_and_b32 v214, s92, v214\n\t"
    "v_lshrrev_b32 v255, 16, v215\n\t"
    "v_and_b32 v215, s92, v215\n\t"
    "s_waitcnt vmcnt(8) lgkmcnt(4)\n\t"
    "v_smfmac_f32_16x16x64_bf16 v[116:119], v[120:123], v[92:99], v124\n\t"
    "ds_read_b64 v[92:93], v214 offset:64000\n\t"
    "ds_read_b64 v[94:95], v254 offset:64000\n\t"
    "ds_read_b64 v[96:97], v215 offset:64000\n\t"
    "ds_read_b64 v[98:99], v255 offset:64000\n\t"
    "s_sub_u32 s87, s87, s95\n\t"
    "buffer_load_dwordx4 v[244:247], v127, %12, s87 offen offset:0 sc1\n\t"
    "v_lshrrev_b32 v252, 16, v216\n\t"
    "v_and_b32 v216, s92, v216\n\t"
    "v_lshrrev_b32 v253, 16, v217\n\t"
    "v_and_b32 v217, s92, v217\n\t"
    "s_waitcnt lgkmcnt(4)\n\t"
    "v_smfmac_f32_16x16x64_bf16 v[116:119], v[120:123], v[84:91], v124\n\t"
    "ds_read_b64 v[84:85], v216 offset:0\n\t"
    "ds_read_b64 v[86:87], v252 offset:0\n\t"
    "ds_read_b64 v[88:89], v217 offset:0\n\t"
    "ds_read_b64 v[90:91], v253 offset:0\n\t"
    "v_lshrrev_b32 v254, 16, v218\n\t"
    "v_and_b32 v218, s92, v218\n\t"
    "v_lshrrev_b32 v255, 16, v219\n\t"
    "v_and_b32 v219, s92, v219\n\t"
    "s_cmp_lt_u32 %16, 4\n\t"
    "s_cbranch_scc1 L_tp207_%=\n\t"
    "s_setprio 1\n\t"
    "L_tp207_%=:\n\t"
    "s_waitcnt vmcnt(8) lgkmcnt(4)\n\t"
    "v_smfmac_f32_16x16x64_bf16 v[116:119], v[120:123], v[92:99], v124\n\t"
    "ds_read_b64 v[92:93], v218 offset:0\n\t"
    "ds_read_b64 v[94:95], v254 offset:0\n\t"
    "ds_read_b64 v[96:97], v219 offset:0\n\t"
    "ds_read_b64 v[98:99], v255 offset:0\n\t"
    "buffer_load_dwordx4 v[248:251], v127, %12, s87 offen offset:1024 sc1\n\t"
    "v_lshrrev_b32 v252, 16, v240\n\t"
    "v_and_b32 v240, s92, v240\n\t"
    "v_lshrrev_b32 v253, 16, v241\n\t"
    "v_cvt_pk_bf16_f32 v62, v116, v117\n\t"
    "v_cvt_pk_bf16_f32 v63, v118, v119\n\t"
    "v_mov_b64 v[172:173], s[70:71]\n\t"
    "v_mov_b64 v[174:175], s[72:73]\n\t"
    "v_and_b32 v241, s92, v241\n\t"
    "s_waitcnt lgkmcnt(4)\n\t"
    "v_smfmac_f32_16x16x64_bf16 v[172:175], v[120:123], v[84:91], v124\n\t"
    "ds_read_b64 v[84:85], v240 offset:0\n\t"
    "ds_read_b64 v[86:87], v252 offset:0\n\t"
    "ds_read_b64 v[88:89], v241 offset:0\n\t"
    "ds_read_b64 v[90:91], v253 offset:0\n\t"
    "v_lshrrev_b32 v254, 16, v242\n\t"
    "v_and_b32 v242, s92, v242\n\t"
    "v_lshrrev_b32 v255, 16, v243\n\t"
    "v_and_b32 v243, s92, v243\n\t"
    "s_waitcnt vmcnt(6) lgkmcnt(4)\n\t"
    "v_smfmac_f32_16x16x64_bf16 v[172:175], v[120:123], v[92:99], v124\n\t"
    "ds_read_b64 v[92:93], v242 offset:0\n\t"
    "ds_read_b64 v[94:95], v254 offset:0\n\t"
    "ds_read_b64 v[96:97], v243 offset:0\n\t"
    "ds_read_b64 v[98:99], v255 offset:0\n\t"
    "s_add_u32 s84, s84, 0x8000\n\t"
    "buffer_load_dwordx4 v[168:171], v127, %12, s84 offen offset:0 sc1\n\t"
    "v_lshrrev_b32 v252, 16, v220\n\t"
    "v_and_b32 v220, s92, v220\n\t"
    "v_lshrrev_b32 v253, 16, v221\n\t"
    "v_mov_b64 v[176:177], s[70:71]\n\t"
    "v_mov_b64 v[178:179], s[72:73]\n\t"
    "v_and_b32 v221, s92, v221\n\t"
    "s_waitcnt lgkmcnt(4)\n\t"
    "v_smfmac_f32_16x16x64_bf16 v[176:179], v[120:123], v[84:91], v124\n\t"
    "ds_read_b64 v[84:85], v220 offset:0\n\t"
    "ds_read_b64 v[86:87], v252 offset:0\n\t"
    "ds_read_b64 v[88:89], v221 offset:0\n\t"
    "ds_read_b64 v[90:91], v253 offset:0\n\t"
    "v_lshrrev_b32 v254, 16, v222\n\t"
    "v_and_b32 v222, s92, v222\n\t"
    "v_lshrrev_b32 v255, 16, v223\n\t"
    "v_and_b32 v223, s92, v223\n\t"
    "s_mov_b64 exec, 1\n\t"
    "buffer_load_dword v164, off, %12, s93 offset:0 sc1\n\t"
    "s_mov_b64 exec, -1\n\t"
    "s_waitcnt vmcnt(7) lgkmcnt(4)\n\t"
    "v_smfmac_f32_16x16x64_bf16 v[176:179], v[120:123], v[92:99], v124\n\t"
    "ds_read_b64 v[92:93], v222 offset:0\n\t"
    "ds_read_b64 v[94:95], v254 offset:0\n\t"
    "ds_read_b64 v[96:97], v223 offset:0\n\t"
    "ds_read_b64 v[98:99], v255 offset:0\n\t"
    "s_sub_u32 s88, s88, s95\n\t"
    "buffer_load_dwordx4 v[112:115], v127, %12, s88 offen offset:0 sc1\n\t"
    "v_lshrrev_b32 v252, 16, v224\n\t"
    "v_and_b32 v224, s92, v224\n\t"
    "v_lshrrev_b32 v253, 16, v225\n\t"
    "v_mov_b64 v[180:181], s[70:71]\n\t"
    "v_mov_b64 v[182:183], s[72:73]\n\t"
    "v_and_b32 v225, s92, v225\n\t"
    "s_waitcnt vmcnt(5)\n\t"
    "s_mov_b64 exec, 1\n\t"
    "buffer_store_dword v165, off, %12, s96 offset:256 sc1\n\t"
    "s_mov_b64 exec, -1\n\t"
    "s_add_u32 s61, s61, 0x2000\n\t"
    "buffer_store_dwordx4 v[52:55], v127, %12, s61 offen sc1\n\t"
    "s_waitcnt lgkmcnt(4)\n\t"
    "v_smfmac_f32_16x16x64_bf16 v[180:183], v[120:123], v[84:91], v124\n\t"
    "ds_read_b64 v[84:85], v224 offset:0\n\t"
    "ds_read_b64 v[86:87], v252 offset:0\n\t"
    "ds_read_b64 v[88:89], v225 offset:0\n\t"
    "ds_read_b64 v[90:91], v253 offset:0\n\t"
    "v_lshrrev_b32 v254, 16, v226\n\t"
    "v_and_b32 v226, s92, v226\n\t"
    "v_lshrrev_b32 v255, 16, v227\n\t"
    "v_and_b32 v227, s92, v227\n\t"
    "s_waitcnt lgkmcnt(4)\n\t"
    "v_smfmac_f32_16x16x64_bf16 v[180:183], v[120:123], v[92:99], v124\n\t"
    "ds_read_b64 v[92:93], v226 offset:0\n\t"
    "ds_read_b64 v[94:95], v254 offset:0\n\t"
    "ds_read_b64 v[96:97], v227 offset:0\n\t"
    "ds_read_b64 v[98:99], v255 offset:0\n\t"
    "buffer_load_dwordx4 v[100:103], v127, %12, s84 offen offset:1024 sc1\n\t"
    "v_lshrrev_b32 v252, 16, v228\n\t"
    "v_and_b32 v228, s92, v228\n\t"
    "v_lshrrev_b32 v253, 16, v229\n\t"
    "v_mov_b64 v[184:185], s[70:71]\n\t"
    "v_mov_b64 v[186:187], s[72:73]\n\t"
    "v_and_b32 v229, s92, v229\n\t"
    "s_waitcnt lgkmcnt(4)\n\t"
    "v_smfmac_f32_16x16x64_bf16 v[184:187], v[120:123], v[84:91], v124\n\t"
    "ds_read_b64 v[84:85], v228 offset:0\n\t"
    "ds_read_b64 v[86:87], v252 offset:0\n\t"
    "ds_read_b64 v[88:89], v229 offset:0\n\t"
    "ds_read_b64 v[90:91], v253 offset:0\n\t"
    "v_lshrrev_b32 v254, 16, v230\n\t"
    "v_and_b32 v230, s92, v230\n\t"
    "v_lshrrev_b32 v255, 16, v231\n\t"
    "v_and_b32 v231, s92, v231\n\t"
    "s_waitcnt lgkmcnt(4)\n\t"
    "v_smfmac_f32_16x16x64_bf16 v[184:187], v[120:123], v[92:99], v124\n\t"
    "ds_read_b64 v[92:93], v230 offset:0\n\t"
    "ds_read_b64 v[94:95], v254 offset:0\n\t"
    "ds_read_b64 v[96:97], v231 offset:0\n\t"
    "ds_read_b64 v[98:99], v255 offset:0\n\t"
    "buffer_load_dwordx4 v[104:107], v127, %12, s88 offen offset:1024 sc1\n\t"
    "v_lshrrev_b32 v252, 16, v232\n\t"
    "v_and_b32 v232, s92, v232\n\t"
    "v_lshrrev_b32 v253, 16, v233\n\t"
    "v_mov_b64 v[188:189], s[70:71]\n\t"
    "v_mov_b64 v[190:191], s[72:73]\n\t"
    "v_and_b32 v233, s92, v233\n\t"
    "s_waitcnt lgkmcnt(4)\n\t"
    "v_smfmac_f32_16x16x64_bf16 v[188:191], v[120:123], v[84:91], v124\n\t"
    "ds_read_b64 v[84:85], v232 offset:0\n\t"
    "ds_read_b64 v[86:87], v252 offset:0\n\t"
    "ds_read_b64 v[88:89], v233 offset:0\n\t"
    "ds_read_b64 v[90:91], v253 offset:0\n\t"
    "v_lshrrev_b32 v254, 16, v234\n\t"
    "v_and_b32 v234, s92, v234\n\t"
    "v_lshrrev_b32 v255, 16, v235\n\t"
    "v_and_b32 v235, s92, v235\n\t"
    "s_waitcnt vmcnt(5)\n\t"
    "v_readfirstlane_b32 s65, v164\n\t"
    "s_nop 0\n\t"
    "s_cmp_eq_u32 s65, 0\n\t"
    "s_cbranch_scc1 L_xw0_%=\n\t"
    "L_xwb0_%=:\n\t"
    "buffer_load_dwordx4 v[4:7], v127, %12, s94 offen sc1\n\t"
    "s_waitcnt lgkmcnt(4)\n\t"
    "v_smfmac_f32_16x16x64_bf16 v[188:191], v[120:123], v[92:99], v124\n\t"
    "ds_read_b64 v[92:93], v234 offset:0\n\t"
    "ds_read_b64 v[94:95], v254 offset:0\n\t"
    "ds_read_b64 v[96:97], v235 offset:0\n\t"
    "ds_read_b64 v[98:99], v255 offset:0\n\t"
    "s_add_u32 s85, s85, 0x8000\n\t"
    "buffer_load_dwordx4 v[108:111], v127, %12, s85 offen offset:0 sc1\n\t"
    "v_lshrrev_b32 v252, 16, v244\n\t"
    "v_and_b32 v244, s92, v244\n\t"
    "v_lshrrev_b32 v253, 16, v245\n\t"
    "v_mov_b64 v[192:193], s[70:71]\n\t"
    "v_mov_b64 v[194:195], s[72:73]\n\t"
    "v_and_b32 v245, s92, v245\n\t"
    "s_waitcnt lgkmcnt(4)\n\t"
    "v_smfmac_f32_16x16x64_bf16 v[192:195], v[120:123], v[84:91], v124\n\t"
    "ds_read_b64 v[84:85], v244 offset:0\n\t"
    "ds_read_b64 v[86:87], v252 offset:0\n\t"
    "ds_read_b64 v[88:89], v245 offset:0\n\t"
    "ds_read_b64 v[90:91], v253 offset:0\n\t"
    "v_lshrrev_b32 v254, 16, v246\n\t"
    "v_and_b32 v246, s92, v246\n\t"
    "v_lshrrev_b32 v255, 16, v247\n\t"
    "v_and_b32 v247, s92, v247\n\t"
    "s_waitcnt lgkmcnt(4)\n\t"
    "v_smfmac_f32_16x16x64_bf16 v[192:195], v[120:123], v[92:99], v124\n\t"
    "ds_read_b64 v[92:93], v246 offset:0\n\t"
    "ds_read_b64 v[94:95], v254 offset:0\n\t"
    "ds_read_b64 v[96:97], v247 offset:0\n\t"
    "ds_read_b64 v[98:99], v255 offset:0\n\t"
    "s_sub_u32 s89, s89, s95\n\t"
    "buffer_load_dwordx4 v[216:219], v127, %12, s89 offen offset:0 sc1\n\t"
    "v_lshrrev_b32 v252, 16, v248\n\t"
    "v_and_b32 v248, s92, v248\n\t"
    "v_lshrrev_b32 v253, 16, v249\n\t"
    "v_mov_b64 v[196:197], s[70:71]\n\t"
    "v_mov_b64 v[198:199], s[72:73]\n\t"
    "v_and_b32 v249, s92, v249\n\t"
    "s_add_u32 s61, s61, 0x2000\n\t"
    "buffer_store_dwordx4 v[60:63], v127, %12, s61 offen sc1\n\t"
    "s_waitcnt lgkmcnt(4)\n\t"
    "v_smfmac_f32_16x16x64_bf16 v[196:199], v[120:123], v[84:91], v124\n\t"
    "ds_read_b64 v[84:85], v248 offset:0\n\t"
    "ds_read_b64 v[86:87], v252 offset:0\n\t"
    "ds_read_b64 v[88:89], v249 offset:0\n\t"
    "ds_read_b64 v[90:91], v253 offset:0\n\t"
    "v_lshrrev_b32 v254, 16, v250\n\t"
    "v_and_b32 v250, s92, v250\n\t"
    "v_lshrrev_b32 v255, 16, v251\n\t"
    "v_and_b32 v251, s92, v251\n\t"
    "s_add_u32 s94, s94, 0x2000\n\t"
    "buffer_load_dwordx4 v[12:15], v127, %12, s94 offen sc1\n\t"
    "s_waitcnt lgkmcnt(4)\n\t"
    "v_smfmac_f32_16x16x64_bf16 v[196:199], v[120:123], v[92:99], v124\n\t"
    "ds_read_b64 v[92:93], v250 offset:0\n\t"
    "ds_read_b64 v[94:95], v254 offset:0\n\t"
    "ds_read_b64 v[96:97], v251 offset:0\n\t"
    "ds_read_b64 v[98:99], v255 offset:0\n\t"
    "buffer_load_dwordx4 v[116:119], v127, %12, s85 offen offset:1024 sc1\n\t"
    "v_lshrrev_b32 v252, 16, v168\n\t"
    "v_and_b32 v168, s92, v168\n\t"
    "v_lshrrev_b32 v253, 16, v169\n\t"
    "v_mov_b64 v[200:201], s[70:71]\n\t"
    "v_mov_b64 v[202:203], s[72:73]\n\t"
    "v_and_b32 v169, s92, v169\n\t"
    "s_waitcnt lgkmcnt(4)\n\t"
    "v_smfmac_f32_16x16x64_bf16 v[200:203], v[120:123], v[84:91], v124\n\t"
    "ds_read_b64 v[84:85], v168 offset:64000\n\t"
    "ds_read_b64 v[86:87], v252 offset:64000\n\t"
    "ds_read_b64 v[88:89], v169 offset:64000\n\t"
    "ds_read_b64 v[90:91], v253 offset:64000\n\t"
    "v_lshrrev_b32 v254, 16, v170\n\t"
    "v_and_b32 v170, s92, v170\n\t"
    "v_lshrrev_b32 v255, 16, v171\n\t"
    "v_and_b32 v171, s92, v171\n\t"
    "s_waitcnt vmcnt(10) lgkmcnt(4)\n\t"
    "v_smfmac_f32_16x16x64_bf16 v[200:203], v[120:123], v[92:99], v124\n\t"
    "ds_read_b64 v[92:93], v170 offset:64000\n\t"
    "ds_read_b64 v[94:95], v254 offset:64000\n\t"
    "ds_read_b64 v[96:97], v171 offset:64000\n\t"
    "ds_read_b64 v[98:99], v255 offset:64000\n\t"
    "buffer_load_dwordx4 v[240:243], v127, %12, s89 offen offset:1024 sc1\n\t"
    "v_lshrrev_b32 v252, 16, v112\n\t"
    "v_and_b32 v112, s92, v112\n\t"
    "v_lshrrev_b32 v253, 16, v113\n\t"
    "v_and_b32 v113, s92, v113\n\t"
    "s_waitcnt lgkmcnt(4)\n\t"
    "v_smfmac_f32_16x16x64_bf16 v[172:175], v[120:123], v[84:91], v124\n\t"
    "ds_read_b64 v[84:85], v112 offset:0\n\t"
    "ds_read_b64 v[86:87], v252 offset:0\n\t"
    "ds_read_b64 v[88:89], v113 offset:0\n\t"
    "ds_read_b64 v[90:91], v253 offset:0\n\t"
    "v_lshrrev_b32 v254, 16, v114\n\t"
    "v_and_b32 v114, s92, v114\n\t"
    "v_lshrrev_b32 v255, 16, v115\n\t"
    "v_and_b32 v115, s92, v115\n\t"
    "s_waitcnt vmcnt(6)\n\t"
    "v_smfmac_f32_16x16x64_bf16 v[172:175], v[156:159], v[0:7], v124\n\t"
    "s_waitcnt lgkmcnt(4)\n\t"
    "v_smfmac_f32_16x16x64_bf16 v[172:175], v[120:123], v[92:99], v124\n\t"
    "ds_read_b64 v[92:93], v114 offset:0\n\t"
    "ds_read_b64 v[94:95], v254 offset:0\n\t"
    "ds_read_b64 v[96:97], v115 offset:0\n\t"
    "ds_read_b64 v[98:99], v255 offset:0\n\t"
    "s_add_u32 s86, s86, 0x8000\n\t"
    "buffer_load_dwordx4 v[220:223], v127, %12, s86 offen offset:0 sc1\n\t"
    "v_lshrrev_b32 v252, 16, v100\n\t"
    "v_and_b32 v100, s92, v100\n\t"
    "v_lshrrev_b32 v253, 16, v101\n\t"
    "v_mov_b64 v[204:205], s[70:71]\n\t"
    "v_mov_b64 v[206:207], s[72:73]\n\t"
    "v_and_b32 v101, s92, v101\n\t"
    "v_smfmac_f32_16x16x64_bf16 v[176:179], v[160:163], v[0:7], v124\n\t"
    "v_pk_mul_f32 v[64:65], v[172:173], v[172:173]\n\t"
    "v_pk_mul_f32 v[66:67], v[174:175], v[174:175]\n\t"
    "v_pk_fma_f32 v[64:65], v[64:65], v[82:83], v[80:81]\n\t"
    "v_pk_fma_f32 v[66:67], v[66:67], v[82:83], v[80:81]\n\t"
    "v_pk_mul_f32 v[64:65], v[64:65], v[172:173]\n\t"
    "v_pk_mul_f32 v[66:67], v[66:67], v[174:175]\n\t"
    "v_exp_f32 v68, v64\n\t"
    "v_exp_f32 v69, v65\n\t"
    "s_waitcnt lgkmcnt(4)\n\t"
    "v_smfmac_f32_16x16x64_bf16 v[204:207], v[120:123], v[84:91], v124\n\t"
    "ds_read_b64 v[84:85], v100 offset:64000\n\t"
    "ds_read_b64 v[86:87], v252 offset:64000\n\t"
    "ds_read_b64 v[88:89], v101 offset:64000\n\t"
    "ds_read_b64 v[90:91], v253 offset:64000\n\t"
    "v_lshrrev_b32 v254, 16, v102\n\t"
    "v_and_b32 v102, s92, v102\n\t"
    "v_lshrrev_b32 v255, 16, v103\n\t"
    "v_and_b32 v103, s92, v103\n\t"
    "s_mov_b64 exec, 1\n\t"
    "buffer_load_dword v164, off, %12, s93 offset:128 sc1\n\t"
    "s_mov_b64 exec, -1\n\t"
    "v_exp_f32 v70, v66\n\t"
    "v_exp_f32 v71, v67\n\t"
    "v_pk_fma_f32 v[68:69], v[68:69], s[40:41], s[40:41]\n\t"
    "v_pk_fma_f32 v[70:71], v[70:71], s[42:43], s[42:43]\n\t"
    "v_rcp_f32 v68, v68\n\t"
    "v_rcp_f32 v69, v69\n\t"
    "v_rcp_f32 v70, v70\n\t"
    "v_rcp_f32 v71, v71\n\t"
    "s_cmp_lt_u32 %16, 4\n\t"
    "s_cbranch_scc1 L_tp227_%=\n\t"
    "s_setprio 0\n\t"
    "L_tp227_%=:\n\t"
    "s_waitcnt lgkmcnt(4)\n\t"
    "v_smfmac_f32_16x16x64_bf16 v[204:207], v[120:123], v[92:99], v124\n\t"
    "ds_read_b64 v[92:93], v102 offset:64000\n\t"
    "ds_read_b64 v[94:95], v254 offset:64000\n\t"
    "ds_read_b64 v[96:97], v103 offset:64000\n\t"
    "ds_read_b64 v[98:99], v255 offset:64000\n\t"
    "s_sub_u32 s90, s90, s95\n\t"
    "buffer_load_dwordx4 v[224:227], v127, %12, s90 offen offset:0 sc1\n\t"
    "v_lshrrev_b32 v252, 16, v104\n\t"
    "v_and_b32 v104, s92, v104\n\t"
    "v_lshrrev_b32 v253, 16, v105\n\t"
    "v_and_b32 v105, s92, v105\n\t"
    "s_waitcnt vmcnt(6)\n\t"
    "s_mov_b64 exec, 1\n\t"
    "buffer_store_dword v165, off, %12, s96 offset:384 sc1\n\t"
    "s_mov_b64 exec, -1\n\t"
    "v_pk_mul_f32 v[68:69], v[68:69], v[172:173]\n\t"
    "v_pk_fma_f32 v[68:69], v[70:71], v[174:175], v[68:69]\n\t"
    "v_add_f32 v236, v68, v69\n\t"
    "s_waitcnt lgkmcnt(4)\n\t"
    "v_smfmac_f32_16x16x64_bf16 v[176:179], v[120:123], v[84:91], v124\n\t"
    "ds_read_b64 v[84:85], v104 offset:0\n\t"
    "ds_read_b64 v[86:87], v252 offset:0\n\t"
    "ds_read_b64 v[88:89], v105 offset:0\n\t"
    "ds_read_b64 v[90:91], v253 offset:0\n\t"
    "v_lshrrev_b32 v254, 16, v106\n\t"
    "v_and_b32 v106, s92, v106\n\t"
    "v_lshrrev_b32 v255, 16, v107\n\t"
    "v_and_b32 v107, s92, v107\n\t"
    "s_waitcnt vmcnt(6)\n\t"
    "v_smfmac_f32_16x16x64_bf16 v[180:183], v[156:159], v[8:15], v124\n\t"
    "v_lshrrev_b32 v166, 7, v127\n\t"
    "v_and_b32 v167, 0x70, v127\n\t"
    "v_lshl_add_u32 v126, v166, 8, v167\n\t"
    "v_add_u32 v126, 128, v126\n\t"
    "s_bfe_u32 s46, %14, 0x30003\n\t"
    "s_min_u32 s46, s46, 6\n\t"
    "s_lshl_b32 s69, s46, 3\n\t"
    "s_add_u32 s69, s69, %16\n\t"
    "v_lshrrev_b32 v147, 4, v127\n\t"
    "v_add_u32 v148, 140288, v127\n\t"
    "s_mov_b32 s46, 48244736\n\t"
    "buffer_load_dwordx4 v[128:131], v127, %12, s46 offen sc1\n\t"
    "s_lshl_b32 s65, %14, 12\n\t"
    "s_add_u32 s65, s65, 16777216\n\t"
    "buffer_load_dwordx4 v[0:3], v127, %12, s65 offen offset:0 sc1\n\t"
    "s_waitcnt lgkmcnt(4)\n\t"
    "v_smfmac_f32_16x16x64_bf16 v[176:179], v[120:123], v[92:99], v124\n\t"
    "ds_read_b64 v[92:93], v106 offset:0\n\t"
    "ds_read_b64 v[94:95], v254 offset:0\n\t"
    "ds_read_b64 v[96:97], v107 offset:0\n\t"
    "ds_read_b64 v[98:99], v255 offset:0\n\t"
    "buffer_load_dwordx4 v[228:231], v127, %12, s86 offen offset:1024 sc1\n\t"
    "v_lshrrev_b32 v252, 16, v108\n\t"
    "v_and_b32 v108, s92, v108\n\t"
    "v_lshrrev_b32 v253, 16, v109\n\t"
    "v_mov_b64 v[208:209], s[70:71]\n\t"
    "v_mov_b64 v[210:211], s[72:73]\n\t"
    "v_and_b32 v109, s92, v109\n\t"
    "v_smfmac_f32_16x16x64_bf16 v[184:187], v[160:163], v[8:15], v124\n\t"
    "v_pk_mul_f32 v[72:73], v[176:177], v[176:177]\n\t"
    "v_pk_mul_f32 v[74:75], v[178:179], v[178:179]\n\t"
    "v_pk_fma_f32 v[72:73], v[72:73], v[82:83], v[80:81]\n\t"
    "v_pk_fma_f32 v[74:75], v[74:75], v[82:83], v[80:81]\n\t"
    "v_pk_mul_f32 v[72:73], v[72:73], v[176:177]\n\t"
    "v_pk_mul_f32 v[74:75], v[74:75], v[178:179]\n\t"
    "v_exp_f32 v76, v72\n\t"
    "v_exp_f32 v77, v73\n\t"
    "s_lshl_b32 s65, %14, 12\n\t"
    "s_add_u32 s65, s65, 16777216\n\t"
    "buffer_load_dwordx4 v[4:7], v127, %12, s65 offen offset:1024 sc1\n\t"
    "s_waitcnt lgkmcnt(4)\n\t"
    "v_smfmac_f32_16x16x64_bf16 v[208:211], v[120:123], v[84:91], v124\n\t"
    "ds_read_b64 v[84:85], v108 offset:64000\n\t"
    "ds_read_b64 v[86:87], v252 offset:64000\n\t"
    "ds_read_b64 v[88:89], v109 offset:64000\n\t"
    "ds_read_b64 v[90:91], v253 offset:64000\n\t"
    "v_lshrrev_b32 v254, 16, v110\n\t"
    "v_and_b32 v110, s92, v110\n\t"
    "v_lshrrev_b32 v255, 16, v111\n\t"
    "v_and_b32 v111, s92, v111\n\t"
    "v_exp_f32 v78, v74\n\t"
    "v_exp_f32 v79, v75\n\t"
    "v_pk_fma_f32 v[76:77], v[76:77], s[40:41], s[40:41]\n\t"
    "v_pk_fma_f32 v[78:79], v[78:79], s[42:43], s[42:43]\n\t"
    "v_rcp_f32 v76, v76\n\t"
    "v_rcp_f32 v77, v77\n\t"
    "v_rcp_f32 v78, v78\n\t"
    "v_rcp_f32 v79, v79\n\t"
    "s_lshl_b32 s65, %14, 12\n\t"
    "s_add_u32 s65, s65, 16777216\n\t"
    "buffer_load_dwordx4 v[8:11], v127, %12, s65 offen offset:2048 sc1\n\t"
    "s_waitcnt lgkmcnt(4)\n\t"
    "v_smfmac_f32_16x16x64_bf16 v[208:211], v[120:123], v[92:99], v124\n\t"
    "ds_read_b64 v[92:93], v110 offset:64000\n\t"
    "ds_read_b64 v[94:95], v254 offset:64000\n\t"
    "ds_read_b64 v[96:97], v111 offset:64000\n\t"
    "ds_read_b64 v[98:99], v255 offset:64000\n\t"
    "buffer_load_dwordx4 v[232:235], v127, %12, s90 offen offset:1024 sc1\n\t"
    "v_lshrrev_b32 v252, 16, v216\n\t"
    "v_and_b32 v216, s92, v216\n\t"
    "v_lshrrev_b32 v253, 16, v217\n\t"
    "v_and_b32 v217, s92, v217\n\t"
    "v_pk_mul_f32 v[76:77], v[76:77], v[176:177]\n\t"
    "v_pk_fma_f32 v[76:77], v[78:79], v[178:179], v[76:77]\n\t"
    "v_add_f32 v237, v76, v77\n\t"
    "s_lshl_b32 s65, %14, 12\n\t"
    "s_add_u32 s65, s65, 16777216\n\t"
    "buffer_load_dwordx4 v[12:15], v127, %12, s65 offen offset:3072 sc1\n\t"
    "s_waitcnt lgkmcnt(4)\n\t"
    "v_smfmac_f32_16x16x64_bf16 v[180:183], v[120:123], v[84:91], v124\n\t"
    "ds_read_b64 v[84:85], v216 offset:0\n\t"
    "ds_read_b64 v[86:87], v252 offset:0\n\t"
    "ds_read_b64 v[88:89], v217 offset:0\n\t"
    "ds_read_b64 v[90:91], v253 offset:0\n\t"
    "v_lshrrev_b32 v254, 16, v218\n\t"
    "v_and_b32 v218, s92, v218\n\t"
    "v_lshrrev_b32 v255, 16, v219\n\t"
    "v_and_b32 v219, s92, v219\n\t"
    "s_waitcnt vmcnt(9)\n\t"
    "v_readfirstlane_b32 s65, v164\n\t"
    "s_nop 0\n\t"
    "s_cmp_eq_u32 s65, 0\n\t"
    "s_cbranch_scc1 L_xw1_%=\n\t"
    "L_xwb1_%=:\n\t"
    "s_add_u32 s94, s94, 0x2000\n\t"
    "buffer_load_dwordx4 v[20:23], v127, %12, s94 offen sc1\n\t"
    "s_waitcnt lgkmcnt(4)\n\t"
    "v_smfmac_f32_16x16x64_bf16 v[180:183], v[120:123], v[92:99], v124\n\t"
    "ds_read_b64 v[92:93], v218 offset:0\n\t"
    "ds_read_b64 v[94:95], v254 offset:0\n\t"
    "ds_read_b64 v[96:97], v219 offset:0\n\t"
    "ds_read_b64 v[98:99], v255 offset:0\n\t"
    "s_add_u32 s87, s87, 0x8000\n\t"
    "buffer_load_dwordx4 v[244:247], v127, %12, s87 offen offset:0 sc1\n\t"
    "v_lshrrev_b32 v252, 16, v116\n\t"
    "v_and_b32 v116, s92, v116\n\t"
    "v_lshrrev_b32 v253, 16, v117\n\t"
    "v_mov_b64 v[212:213], s[70:71]\n\t"
    "v_mov_b64 v[214:215], s[72:73]\n\t"
    "v_and_b32 v117, s92, v117\n\t"
    "v_pk_mul_f32 v[64:65], v[180:181], v[180:181]\n\t"
    "v_pk_mul_f32 v[66:67], v[182:183], v[182:183]\n\t"
    "v_pk_fma_f32 v[64:65], v[64:65], v[82:83], v[80:81]\n\t"
    "v_pk_fma_f32 v[66:67], v[66:67], v[82:83], v[80:81]\n\t"
    "v_pk_mul_f32 v[64:65], v[64:65], v[180:181]\n\t"
    "v_pk_mul_f32 v[66:67], v[66:67], v[182:183]\n\t"
    "v_exp_f32 v68, v64\n\t"
    "v_exp_f32 v69, v65\n\t"
    "s_waitcnt lgkmcnt(4)\n\t"
    "v_smfmac_f32_16x16x64_bf16 v[212:215], v[120:123], v[84:91], v124\n\t"
    "ds_read_b64 v[84:85], v116 offset:64000\n\t"
    "ds_read_b64 v[86:87], v252 offset:64000\n\t"
    "ds_read_b64 v[88:89], v117 offset:64000\n\t"
    "ds_read_b64 v[90:91], v253 offset:64000\n\t"
    "v_lshrrev_b32 v254, 16, v118\n\t"
    "v_and_b32 v118, s92, v118\n\t"
    "v_lshrrev_b32 v255, 16, v119\n\t"
    "v_and_b32 v119, s92, v119\n\t"
    "v_exp_f32 v70, v66\n\t"
    "v_exp_f32 v71, v67\n\t"
    "v_pk_fma_f32 v[68:69], v[68:69], s[40:41], s[40:41]\n\t"
    "v_pk_fma_f32 v[70:71], v[70:71], s[42:43], s[42:43]\n\t"
    "v_rcp_f32 v68, v68\n\t"
    "v_rcp_f32 v69, v69\n\t"
    "v_rcp_f32 v70, v70\n\t"
    "v_rcp_f32 v71, v71\n\t"
    "s_waitcnt lgkmcnt(4)\n\t"
    "v_smfmac_f32_16x16x64_bf16 v[212:215], v[120:123], v[92:99], v124\n\t"
    "ds_read_b64 v[92:93], v118 offset:64000\n\t"
    "ds_read_b64 v[94:95], v254 offset:64000\n\t"
    "ds_read_b64 v[96:97], v119 offset:64000\n\t"
    "ds_read_b64 v[98:99], v255 offset:64000\n\t"
    "s_sub_u32 s91, s91, s95\n\t"
    "buffer_load_dwordx4 v[248:251], v127, %12, s91 offen offset:0 sc1\n\t"
    "v_lshrrev_b32 v252, 16, v240\n\t"
    "v_and_b32 v240, s92, v240\n\t"
    "v_lshrrev_b32 v253, 16, v241\n\t"
    "v_and_b32 v241, s92, v241\n\t"
    "v_pk_mul_f32 v[68:69], v[68:69], v[180:181]\n\t"
    "v_pk_fma_f32 v[68:69], v[70:71], v[182:183], v[68:69]\n\t"
    "v_add_f32 v238, v68, v69\n\t"
    "s_waitcnt lgkmcnt(4)\n\t"
    "v_smfmac_f32_16x16x64_bf16 v[184:187], v[120:123], v[84:91], v124\n\t"
    "ds_read_b64 v[84:85], v240 offset:0\n\t"
    "ds_read_b64 v[86:87], v252 offset:0\n\t"
    "ds_read_b64 v[88:89], v241 offset:0\n\t"
    "ds_read_b64 v[90:91], v253 offset:0\n\t"
    "v_lshrrev_b32 v254, 16, v242\n\t"
    "v_and_b32 v242, s92, v242\n\t"
    "v_lshrrev_b32 v255, 16, v243\n\t"
    "v_and_b32 v243, s92, v243\n\t"
    "s_add_u32 s94, s94, 0x2000\n\t"
    "buffer_load_dwordx4 v[28:31], v127, %12, s94 offen sc1\n\t"
    "s_waitcnt vmcnt(10)\n\t"
    "s_cmp_lg_u32 %16, 0\n\t"
    "s_cbranch_scc1 L_bend_%=\n\t"
    "s_mov_b32 s47, 1048576\n\t"
    "L_bchk_%=:\n\t"
    "v_min_u32 v132, v128, v129\n\t"
    "v_min3_u32 v132, v132, v130, v131\n\t"
    "v_cmp_eq_u32 vcc, 0, v132\n\t"
    "s_cbranch_vccz L_bok_%=\n\t"
    "s_sleep 16\n\t"
    "s_mov_b32 s46, 48244736\n\t"
    "buffer_load_dwordx4 v[128:131], v127, %12, s46 offen sc1\n\t"
    "s_sub_u32 s47, s47, 1\n\t"
    "s_waitcnt vmcnt(0)\n\t"
    "s_cmp_lg_u32 s47, 0\n\t"
    "s_cbranch_scc1 L_bchk_%=\n\t"
    "L_bok_%=:\n\t"
    "v_readfirstlane_b32 s46, v128\n\t"
    "v_lshrrev_b32 v135, 2, v127\n\t"
    "v_mov_b32 v138, -4096\n\t"
    "v_mov_b32 v139, 4096\n\t"
    "v_subrev_u32 v128, s46, v128\n\t"
    "v_subrev_u32 v129, s46, v129\n\t"
    "v_subrev_u32 v130, s46, v130\n\t"
    "v_subrev_u32 v131, s46, v131\n\t"
    "v_add3_u32 v132, v128, v129, v130\n\t"
    "v_add_u32 v132, v132, v131\n\t"
    "s_nop 1\n\t"
    "v_add_u32_dpp v132, v132, v132 row_shr:1 row_mask:0xf bank_mask:0xf\n\t"
    "s_nop 1\n\t"
    "v_add_u32_dpp v132, v132, v132 row_shr:2 row_mask:0xf bank_mask:0xf\n\t"
    "s_nop 1\n\t"
    "v_add_u32_dpp v132, v132, v132 row_shr:4 row_mask:0xf bank_mask:0xf\n\t"
    "s_nop 1\n\t"
    "v_add_u32_dpp v132, v132, v132 row_shr:8 row_mask:0xf bank_mask:0xf\n\t"
    "s_nop 1\n\t"
    "v_add_u32_dpp v132, v132, v132 row_bcast:15 row_mask:0xa bank_mask:0xf\n\t"
    "s_nop 1\n\t"
    "v_add_u32_dpp v132, v132, v132 row_bcast:31 row_mask:0xc bank_mask:0xf\n\t"
    "s_nop 0\n\t"
    "v_readlane_b32 s46, v132, 63\n\t"
    "s_movk_i32 s47, 19\n\t"
    "v_mov_b32 v134, 600\n\t"
    "s_ashr_i32 s46, s46, 8\n\t"
    "v_sub_u32 v128, s46, v128\n\t"
    "v_med3_i32 v128, v128, v138, v139\n\t"
    "v_mad_i32_i24 v128, v128, s47, v134\n\t"
    "v_ashrrev_i32 v128, 10, v128\n\t"
    "v_med3_i32 v128, v128, -8, 3\n\t"
    "v_sub_u32 v129, s46, v129\n\t"
    "v_med3_i32 v129, v129, v138, v139\n\t"
    "v_mad_i32_i24 v129, v129, s47, v134\n\t"
    "v_ashrrev_i32 v129, 10, v129\n\t"
    "v_med3_i32 v129, v129, -8, 3\n\t"
    "v_sub_u32 v130, s46, v130\n\t"
    "v_med3_i32 v130, v130, v138, v139\n\t"
    "v_mad_i32_i24 v130, v130, s47, v134\n\t"
    "v_ashrrev_i32 v130, 10, v130\n\t"
    "v_med3_i32 v130, v130, -8, 3\n\t"
    "v_sub_u32 v131, s46, v131\n\t"
    "v_med3_i32 v131, v131, v138, v139\n\t"
    "v_mad_i32_i24 v131, v131, s47, v134\n\t"
    "v_ashrrev_i32 v131, 10, v131\n\t"
    "v_med3_i32 v131, v131, -8, 3\n\t"
    "v_add3_u32 v132, v128, v129, v130\n\t"
    "v_add_u32 v132, v132, v131\n\t"
    "v_mov_b32 v133, 0\n\t"
    "v_mov_b32 v134, 0\n\t"
    "v_cmp_gt_u32 vcc, %14, v135\n\t"
    "v_cmp_ge_u32 s[46:47], %14, v135\n\t"
    "v_add_u32 v135, 1, v135\n\t"
    "v_cndmask_b32 v136, 0, v128, vcc\n\t"
    "v_cndmask_b32 v137, 0, v128, s[46:47]\n\t"
    "v_add_u32 v133, v133, v136\n\t"
    "v_add_u32 v134, v134, v137\n\t"
    "v_cmp_gt_u32 vcc, %14, v135\n\t"
    "v_cmp_ge_u32 s[46:47], %14, v135\n\t"
    "v_add_u32 v135, 1, v135\n\t"
    "v_cndmask_b32 v136, 0, v129, vcc\n\t"
    "v_cndmask_b32 v137, 0, v129, s[46:47]\n\t"
    "v_add_u32 v133, v133, v136\n\t"
    "v_add_u32 v134, v134, v137\n\t"
    "v_cmp_gt_u32 vcc, %14, v135\n\t"
    "v_cmp_ge_u32 s[46:47], %14, v135\n\t"
    "v_add_u32 v135, 1, v135\n\t"
    "v_cndmask_b32 v136, 0, v130, vcc\n\t"
    "v_cndmask_b32 v137, 0, v130, s[46:47]\n\t"
    "v_add_u32 v133, v133, v136\n\t"
    "v_add_u32 v134, v134, v137\n\t"
    "v_cmp_gt_u32 vcc, %14, v135\n\t"
    "v_cmp_ge_u32 s[46:47], %14, v135\n\t"
    "v_add_u32 v135, 1, v135\n\t"
    "v_cndmask_b32 v136, 0, v131, vcc\n\t"
    "v_cndmask_b32 v137, 0, v131, s[46:47]\n\t"
    "v_add_u32 v133, v133, v136\n\t"
    "v_add_u32 v134, v134, v137\n\t"
    "s_nop 0\n\t"
    "v_add_u32_dpp v132, v132, v132 row_shr:1 row_mask:0xf bank_mask:0xf\n\t"
    "v_add_u32_dpp v133, v133, v133 row_shr:1 row_mask:0xf bank_mask:0xf\n\t"
    "v_add_u32_dpp v134, v134, v134 row_shr:1 row_mask:0xf bank_mask:0xf\n\t"
    "v_add_u32_dpp v132, v132, v132 row_shr:2 row_mask:0xf bank_mask:0xf\n\t"
    "v_add_u32_dpp v133, v133, v133 row_shr:2 row_mask:0xf bank_mask:0xf\n\t"
    "v_add_u32_dpp v134, v134, v134 row_shr:2 row_mask:0xf bank_mask:0xf\n\t"
    "v_add_u32_dpp v132, v132, v132 row_shr:4 row_mask:0xf bank_mask:0xf\n\t"
    "v_add_u32_dpp v133, v133, v133 row_shr:4 row_mask:0xf bank_mask:0xf\n\t"
    "v_add_u32_dpp v134, v134, v134 row_shr:4 row_mask:0xf bank_mask:0xf\n\t"
    "v_add_u32_dpp v132, v132, v132 row_shr:8 row_mask:0xf bank_mask:0xf\n\t"
    "v_add_u32_dpp v133, v133, v133 row_shr:8 row_mask:0xf bank_mask:0xf\n\t"
    "v_add_u32_dpp v134, v134, v134 row_shr:8 row_mask:0xf bank_mask:0xf\n\t"
    "v_add_u32_dpp v132, v132, v132 row_bcast:15 row_mask:0xa bank_mask:0xf\n\t"
    "v_add_u32_dpp v133, v133, v133 row_bcast:15 row_mask:0xa bank_mask:0xf\n\t"
    "v_add_u32_dpp v134, v134, v134 row_bcast:15 row_mask:0xa bank_mask:0xf\n\t"
    "v_add_u32_dpp v132, v132, v132 row_bcast:31 row_mask:0xc bank_mask:0xf\n\t"
    "v_add_u32_dpp v133, v133, v133 row_bcast:31 row_mask:0xc bank_mask:0xf\n\t"
    "v_add_u32_dpp v134, v134, v134 row_bcast:31 row_mask:0xc bank_mask:0xf\n\t"
    "v_readlane_b32 s46, v132, 63\n\t"
    "v_readlane_b32 s47, v133, 63\n\t"
    "v_readlane_b32 s48, v134, 63\n\t"
    "s_add_u32 s44, %14, 1\n\t"
    "s_mul_i32 s49, %14, s46\n\t"
    "s_ashr_i32 s49, s49, 8\n\t"
    "s_sub_u32 s47, s47, s49\n\t"
    "s_lshl_b32 s47, s47, 2\n\t"
    "s_mul_i32 s49, s44, s46\n\t"
    "s_ashr_i32 s49, s49, 8\n\t"
    "s_sub_u32 s48, s48, s49\n\t"
    "s_lshl_b32 s48, s48, 2\n\t"
    "s_add_u32 s46, s46, 256\n\t"
    "s_cmp_le_u32 s46, 512\n\t"
    "s_cselect_b32 s47, s47, 0\n\t"
    "s_cselect_b32 s48, s48, 0\n\t"
    "s_lshl_b32 s49, %14, 6\n\t"
    "s_add_u32 s47, s47, s49\n\t"
    "s_add_u32 s48, s48, s49\n\t"
    "s_add_u32 s48, s48, 64\n\t"
    "v_mov_b32 v132, s47\n\t"
    "v_mov_b32 v133, s48\n\t"
    "v_mov_b32 v134, 138896\n\t"
    "ds_write_b64 v134, v[132:133]\n\t"
    "L_bend_%=:\n\t"
    "s_waitcnt lgkmcnt(4)\n\t"
    "v_smfmac_f32_16x16x64_bf16 v[184:187], v[120:123], v[92:99], v124\n\t"
    "ds_read_b64 v[92:93], v242 offset:0\n\t"
    "ds_read_b64 v[94:95], v254 offset:0\n\t"
    "ds_read_b64 v[96:97], v243 offset:0\n\t"
    "ds_read_b64 v[98:99], v255 offset:0\n\t"
    "buffer_load_dwordx4 v[168:171], v127, %12, s87 offen offset:1024 sc1\n\t"
    "v_lshrrev_b32 v252, 16, v220\n\t"
    "v_and_b32 v220, s92, v220\n\t"
    "v_lshrrev_b32 v253, 16, v221\n\t"
    "v_mov_b64 v[216:217], s[70:71]\n\t"
    "v_mov_b64 v[218:219], s[72:73]\n\t"
    "v_and_b32 v221, s92, v221\n\t"
    "v_pk_mul_f32 v[72:73], v[184:185], v[184:185]\n\t"
    "v_pk_mul_f32 v[74:75], v[186:187], v[186:187]\n\t"
    "v_pk_fma_f32 v[72:73], v[72:73], v[82:83], v[80:81]\n\t"
    "v_pk_fma_f32 v[74:75], v[74:75], v[82:83], v[80:81]\n\t"
    "v_pk_mul_f32 v[72:73], v[72:73], v[184:185]\n\t"
    "v_pk_mul_f32 v[74:75], v[74:75], v[186:187]\n\t"
    "v_exp_f32 v76, v72\n\t"
    "v_exp_f32 v77, v73\n\t"
    "s_waitcnt lgkmcnt(4)\n\t"
    "v_smfmac_f32_16x16x64_bf16 v[216:219], v[120:123], v[84:91], v124\n\t"
    "ds_read_b64 v[84:85], v220 offset:64000\n\t"
    "ds_read_b64 v[86:87], v252 offset:64000\n\t"
    "ds_read_b64 v[88:89], v221 offset:64000\n\t"
    "ds_read_b64 v[90:91], v253 offset:64000\n\t"
    "v_lshrrev_b32 v254, 16, v222\n\t"
    "v_and_b32 v222, s92, v222\n\t"
    "v_lshrrev_b32 v255, 16, v223\n\t"
    "v_and_b32 v223, s92, v223\n\t"
    "v_exp_f32 v78, v74\n\t"
    "v_exp_f32 v79, v75\n\t"
    "v_pk_fma_f32 v[76:77], v[76:77], s[40:41], s[40:41]\n\t"
    "v_pk_fma_f32 v[78:79], v[78:79], s[42:43], s[42:43]\n\t"
    "v_rcp_f32 v76, v76\n\t"
    "v_rcp_f32 v77, v77\n\t"
    "v_rcp_f32 v78, v78\n\t"
    "v_rcp_f32 v79, v79\n\t"
    "s_cmp_lt_u32 %16, 4\n\t"
    "s_cbranch_scc1 L_tp239_%=\n\t"
    "s_setprio 1\n\t"
    "L_tp239_%=:\n\t"
    "s_waitcnt lgkmcnt(4)\n\t"
    "v_smfmac_f32_16x16x64_bf16 v[216:219], v[120:123], v[92:99], v124\n\t"
    "ds_read_b64 v[92:93], v222 offset:64000\n\t"
    "ds_read_b64 v[94:95], v254 offset:64000\n\t"
    "ds_read_b64 v[96:97], v223 offset:64000\n\t"
    "ds_read_b64 v[98:99], v255 offset:64000\n\t"
    "buffer_load_dwordx4 v[112:115], v127, %12, s91 offen offset:1024 sc1\n\t"
    "v_lshrrev_b32 v252, 16, v224\n\t"
    "v_and_b32 v224, s92, v224\n\t"
    "v_lshrrev_b32 v253, 16, v225\n\t"
    "v_and_b32 v225, s92, v225\n\t"
    "v_pk_mul_f32 v[76:77], v[76:77], v[184:185]\n\t"
    "v_pk_fma_f32 v[76:77], v[78:79], v[186:187], v[76:77]\n\t"
    "v_add_f32 v239, v76, v77\n\t"
    "s_waitcnt lgkmcnt(4)\n\t"
    "v_smfmac_f32_16x16x64_bf16 v[188:191], v[120:123], v[84:91], v124\n\t"
    "ds_read_b64 v[84:85], v224 offset:0\n\t"
    "ds_read_b64 v[86:87], v252 offset:0\n\t"
    "ds_read_b64 v[88:89], v225 offset:0\n\t"
    "ds_read_b64 v[90:91], v253 offset:0\n\t"
    "v_lshrrev_b32 v254, 16, v226\n\t"
    "v_and_b32 v226, s92, v226\n\t"
    "v_lshrrev_b32 v255, 16, v227\n\t"
    "v_and_b32 v227, s92, v227\n\t"
    "s_waitcnt vmcnt(5)\n\t"
    "v_smfmac_f32_16x16x64_bf16 v[188:191], v[156:159], v[16:23], v124\n\t"
    "s_waitcnt lgkmcnt(4)\n\t"
    "v_smfmac_f32_16x16x64_bf16 v[188:191], v[120:123], v[92:99], v124\n\t"
    "ds_read_b64 v[92:93], v226 offset:0\n\t"
    "ds_read_b64 v[94:95], v254 offset:0\n\t"
    "ds_read_b64 v[96:97], v227 offset:0\n\t"
    "ds_read_b64 v[98:99], v255 offset:0\n\t"
    "s_add_u32 s88, s88, 0x8000\n\t"
    "buffer_load_dwordx4 v[100:103], v127, %12, s88 offen offset:0 sc1\n\t"
    "v_lshrrev_b32 v252, 16, v228\n\t"
    "v_and_b32 v228, s92, v228\n\t"
    "v_lshrrev_b32 v253, 16, v229\n\t"
    "v_mov_b64 v[220:221], s[70:71]\n\t"
    "v_mov_b64 v[222:223], s[72:73]\n\t"
    "v_and_b32 v229, s92, v229\n\t"
    "v_smfmac_f32_16x16x64_bf16 v[192:195], v[160:163], v[16:23], v124\n\t"
    "v_pk_mul_f32 v[64:65], v[188:189], v[188:189]\n\t"
    "v_pk_mul_f32 v[66:67], v[190:191], v[190:191]\n\t"
    "v_pk_fma_f32 v[64:65], v[64:65], v[82:83], v[80:81]\n\t"
    "v_pk_fma_f32 v[66:67], v[66:67], v[82:83], v[80:81]\n\t"
    "v_pk_mul_f32 v[64:65], v[64:65], v[188:189]\n\t"
    "v_pk_mul_f32 v[66:67], v[66:67], v[190:191]\n\t"
    "v_exp_f32 v68, v64\n\t"
    "v_exp_f32 v69, v65\n\t"
    "ds_read_b128 v[16:19], v148 offset:4096\n\t"
    "s_waitcnt lgkmcnt(5)\n\t"
    "v_smfmac_f32_16x16x64_bf16 v[220:223], v[120:123], v[84:91], v124\n\t"
    "ds_read_b64 v[84:85], v228 offset:64000\n\t"
    "ds_read_b64 v[86:87], v252 offset:64000\n\t"
    "ds_read_b64 v[88:89], v229 offset:64000\n\t"
    "ds_read_b64 v[90:91], v253 offset:64000\n\t"
    "v_lshrrev_b32 v254, 16, v230\n\t"
    "v_and_b32 v230, s92, v230\n\t"
    "v_lshrrev_b32 v255, 16, v231\n\t"
    "v_and_b32 v231, s92, v231\n\t"
    "s_mov_b64 exec, 1\n\t"
    "buffer_load_dword v164, off, %12, s93 offset:256 sc1\n\t"
    "s_mov_b64 exec, -1\n\t"
    "v_exp_f32 v70, v66\n\t"
    "v_exp_f32 v71, v67\n\t"
    "v_pk_fma_f32 v[68:69], v[68:69], s[40:41], s[40:41]\n\t"
    "v_pk_fma_f32 v[70:71], v[70:71], s[42:43], s[42:43]\n\t"
    "v_rcp_f32 v68, v68\n\t"
    "v_rcp_f32 v69, v69\n\t"
    "v_rcp_f32 v70, v70\n\t"
    "v_rcp_f32 v71, v71\n\t"
    "ds_read_b128 v[20:23], v148 offset:5120\n\t"
    "s_waitcnt lgkmcnt(6)\n\t"
    "v_smfmac_f32_16x16x64_bf16 v[220:223], v[120:123], v[92:99], v124\n\t"
    "ds_read_b64 v[92:93], v230 offset:64000\n\t"
    "ds_read_b64 v[94:95], v254 offset:64000\n\t"
    "ds_read_b64 v[96:97], v231 offset:64000\n\t"
    "ds_read_b64 v[98:99], v255 offset:64000\n\t"
    "buffer_load_dwordx4 v[104:107], v127, %12, s88 offen offset:1024 sc1\n\t"
    "v_lshrrev_b32 v252, 16, v232\n\t"
    "v_and_b32 v232, s92, v232\n\t"
    "v_lshrrev_b32 v253, 16, v233\n\t"
    "v_and_b32 v233, s92, v233\n\t"
    "v_pk_mul_f32 v[68:69], v[68:69], v[188:189]\n\t"
    "v_pk_fma_f32 v[68:69], v[70:71], v[190:191], v[68:69]\n\t"
    "v_add_f32 v240, v68, v69\n\t"
    "s_waitcnt lgkmcnt(5)\n\t"
    "v_smfmac_f32_16x16x64_bf16 v[192:195], v[120:123], v[84:91], v124\n\t"
    "ds_read_b64 v[84:85], v232 offset:0\n\t"
    "ds_read_b64 v[86:87], v252 offset:0\n\t"
    "ds_read_b64 v[88:89], v233 offset:0\n\t"
    "ds_read_b64 v[90:91], v253 offset:0\n\t"
    "v_lshrrev_b32 v254, 16, v234\n\t"
    "v_and_b32 v234, s92, v234\n\t"
    "v_lshrrev_b32 v255, 16, v235\n\t"
    "v_and_b32 v235, s92, v235\n\t"
    "s_waitcnt vmcnt(5)\n\t"
    "v_smfmac_f32_16x16x64_bf16 v[196:199], v[156:159], v[24:31], v124\n\t"
    "s_waitcnt lgkmcnt(4)\n\t"
    "v_smfmac_f32_16x16x64_bf16 v[192:195], v[120:123], v[92:99], v124\n\t"
    "ds_read_b64 v[92:93], v234 offset:0\n\t"
    "ds_read_b64 v[94:95], v254 offset:0\n\t"
    "ds_read_b64 v[96:97], v235 offset:0\n\t"
    "ds_read_b64 v[98:99], v255 offset:0\n\t"
    "s_add_u32 s89, s89, 0x8000\n\t"
    "buffer_load_dwordx4 v[108:111], v127, %12, s89 offen offset:0 sc1\n\t"
    "v_lshrrev_b32 v252, 16, v244\n\t"
    "v_and_b32 v244, s92, v244\n\t"
    "v_lshrrev_b32 v253, 16, v245\n\t"
    "v_mov_b64 v[224:225], s[70:71]\n\t"
    "v_mov_b64 v[226:227], s[72:73]\n\t"
    "v_and_b32 v245, s92, v245\n\t"
    "v_smfmac_f32_16x16x64_bf16 v[200:203], v[160:163], v[24:31], v124\n\t"
    "v_pk_mul_f32 v[72:73], v[192:193], v[192:193]\n\t"
    "v_pk_mul_f32 v[74:75], v[194:195], v[194:195]\n\t"
    "v_pk_fma_f32 v[72:73], v[72:73], v[82:83], v[80:81]\n\t"
    "v_pk_fma_f32 v[74:75], v[74:75], v[82:83], v[80:81]\n\t"
    "v_pk_mul_f32 v[72:73], v[72:73], v[192:193]\n\t"
    "v_pk_mul_f32 v[74:75], v[74:75], v[194:195]\n\t"
    "v_exp_f32 v76, v72\n\t"
    "v_exp_f32 v77, v73\n\t"
    "ds_read_b128 v[24:27], v148 offset:6144\n\t"
    "s_waitcnt lgkmcnt(5)\n\t"
    "v_smfmac_f32_16x16x64_bf16 v[224:227], v[120:123], v[84:91], v124\n\t"
    "ds_read_b64 v[84:85], v244 offset:64000\n\t"
    "ds_read_b64 v[86:87], v252 offset:64000\n\t"
    "ds_read_b64 v[88:89], v245 offset:64000\n\t"
    "ds_read_b64 v[90:91], v253 offset:64000\n\t"
    "v_lshrrev_b32 v254, 16, v246\n\t"
    "v_and_b32 v246, s92, v246\n\t"
    "v_lshrrev_b32 v255, 16, v247\n\t"
    "v_and_b32 v247, s92, v247\n\t"
    "v_exp_f32 v78, v74\n\t"
    "v_exp_f32 v79, v75\n\t"
    "v_pk_fma_f32 v[76:77], v[76:77], s[40:41], s[40:41]\n\t"
    "v_pk_fma_f32 v[78:79], v[78:79], s[42:43], s[42:43]\n\t"
    "v_rcp_f32 v76, v76\n\t"
    "v_rcp_f32 v77, v77\n\t"
    "v_rcp_f32 v78, v78\n\t"
    "v_rcp_f32 v79, v79\n\t"
    "ds_read_b128 v[28:31], v148 offset:7168\n\t"
    "s_waitcnt lgkmcnt(6)\n\t"
    "v_smfmac_f32_16x16x64_bf16 v[224:227], v[120:123], v[92:99], v124\n\t"
    "ds_read_b64 v[92:93], v246 offset:64000\n\t"
    "ds_read_b64 v[94:95], v254 offset:64000\n\t"
    "ds_read_b64 v[96:97], v247 offset:64000\n\t"
    "ds_read_b64 v[98:99], v255 offset:64000\n\t"
    "buffer_load_dwordx4 v[116:119], v127, %12, s89 offen offset:1024 sc1\n\t"
    "v_lshrrev_b32 v252, 16, v248\n\t"
    "v_and_b32 v248, s92, v248\n\t"
    "v_lshrrev_b32 v253, 16, v249\n\t"
    "v_and_b32 v249, s92, v249\n\t"
    "v_pk_mul_f32 v[76:77], v[76:77], v[192:193]\n\t"
    "v_pk_fma_f32 v[76:77], v[78:79], v[194:195], v[76:77]\n\t"
    "v_add_f32 v241, v76, v77\n\t"
    "s_waitcnt lgkmcnt(5)\n\t"
    "v_smfmac_f32_16x16x64_bf16 v[196:199], v[120:123], v[84:91], v124\n\t"
    "ds_read_b64 v[84:85], v248 offset:0\n\t"
    "ds_read_b64 v[86:87], v252 offset:0\n\t"
    "ds_read_b64 v[88:89], v249 offset:0\n\t"
    "ds_read_b64 v[90:91], v253 offset:0\n\t"
    "v_lshrrev_b32 v254, 16, v250\n\t"
    "v_and_b32 v250, s92, v250\n\t"
    "v_lshrrev_b32 v255, 16, v251\n\t"
    "v_and_b32 v251, s92, v251\n\t"
    "s_waitcnt vmcnt(3)\n\t"
    "v_readfirstlane_b32 s65, v164\n\t"
    "s_nop 0\n\t"
    "s_cmp_eq_u32 s65, 0\n\t"
    "s_cbranch_scc1 L_xw2_%=\n\t"
    "L_xwb2_%=:\n\t"
    "s_add_u32 s94, s94, 0x2000\n\t"
    "buffer_load_dwordx4 v[36:39], v127, %12, s94 offen sc1\n\t"
    "v_mov_b32 v155, 138896\n\t"
    "s_and_b32 s46, %16, 3\n\t"
    "s_mul_i32 s49, s46, 1040\n\t"
    "s_add_u32 s49, s49, 2064\n\t"
    "L_rng_%=:\n\t"
    "ds_read_b64 v[144:145], v155\n\t"
    "s_waitcnt lgkmcnt(0)\n\t"
    "v_readfirstlane_b32 s38, v145\n\t"
    "v_readfirstlane_b32 s37, v144\n\t"
    "s_cmp_eq_u32 s38, 0\n\t"
    "s_cbranch_scc1 L_rng_%=\n\t"
    "s_sub_u32 s38, s38, s37\n\t"
    "s_lshr_b32 s44, s38, 2\n\t"
    "s_mul_i32 s51, s46, s44\n\t"
    "s_add_u32 s48, s51, s44\n\t"
    "s_lshl_b32 s48, s48, 16\n\t"
    "s_add_u32 s48, s48, s51\n\t"
    "s_lshr_b32 s47, %16, 2\n\t"
    "s_add_u32 s48, s48, s47\n\t"
    "s_add_u32 s48, s48, 0x200\n\t"
    "s_mul_i32 s46, s37, 400\n\t"
    "s_add_u32 s96, %18, s46\n\t"
    "s_addc_u32 s97, %19, 0\n\t"
    "s_sub_u32 s98, 6553600, s46\n\t"
    "s_mov_b32 s99, 0x20000\n\t"
    "v_bfe_u32 v154, v147, 2, 2\n\t"
    "v_and_b32 v155, 3, v147\n\t"
    "v_mul_u32_u24 v154, 25, v154\n\t"
    "v_mad_u32_u24 v154, v155, 6, v154\n\t"
    "v_lshlrev_b32 v153, 2, v154\n\t"
    "s_and_b32 s53, s48, 0xff\n\t"
    "s_bfe_u32 s55, s48, 0x80008\n\t"
    "s_add_u32 s54, s53, s55\n\t"
    "s_add_u32 s55, s54, s55\n\t"
    "s_lshr_b32 s64, s48, 16\n\t"
    "s_mul_i32 s66, s53, 400\n\t"
    "buffer_load_dwordx3 v[128:130], v153, s[96:99], s66 offen\n\t"
    "buffer_load_dwordx4 v[132:135], v153, s[96:99], s66 offen offset:12\n\t"
    "s_mul_i32 s66, s54, 400\n\t"
    "buffer_load_dwordx3 v[136:138], v153, s[96:99], s66 offen\n\t"
    "buffer_load_dwordx4 v[140:143], v153, s[96:99], s66 offen offset:12\n\t"
    "v_cmp_eq_u32 s[62:63], 0, v155\n\t"
    "v_and_b32 v151, 48, v147\n\t"
    "v_or_b32 v155, 0x100000, v151\n\t"
    "v_cndmask_b32 v152, v155, v151, s[62:63]\n\t"
    "v_smfmac_f32_16x16x64_bf16 v[196:199], v[120:123], v[92:99], v124\n\t"
    "ds_read_b64 v[92:93], v250 offset:0\n\t"
    "ds_read_b64 v[94:95], v254 offset:0\n\t"
    "ds_read_b64 v[96:97], v251 offset:0\n\t"
    "ds_read_b64 v[98:99], v255 offset:0\n\t"
    "s_add_u32 s90, s90, 0x8000\n\t"
    "buffer_load_dwordx4 v[248:251], v127, %12, s90 offen offset:0 sc1\n\t"
    "v_lshrrev_b32 v252, 16, v168\n\t"
    "v_and_b32 v168, s92, v168\n\t"
    "v_lshrrev_b32 v253, 16, v169\n\t"
    "v_mov_b64 v[228:229], s[70:71]\n\t"
    "v_mov_b64 v[230:231], s[72:73]\n\t"
    "v_and_b32 v169, s92, v169\n\t"
    "v_pk_mul_f32 v[64:65], v[196:197], v[196:197]\n\t"
    "v_pk_mul_f32 v[66:67], v[198:199], v[198:199]\n\t"
    "v_pk_fma_f32 v[64:65], v[64:65], v[82:83], v[80:81]\n\t"
    "v_pk_fma_f32 v[66:67], v[66:67], v[82:83], v[80:81]\n\t"
    "v_pk_mul_f32 v[64:65], v[64:65], v[196:197]\n\t"
    "v_pk_mul_f32 v[66:67], v[66:67], v[198:199]\n\t"
    "v_exp_f32 v68, v64\n\t"
    "v_exp_f32 v69, v65\n\t"
    "v_smfmac_f32_16x16x64_bf16 v[228:231], v[120:123], v[84:91], v124\n\t"
    "ds_read_b64 v[84:85], v168 offset:64000\n\t"
    "ds_read_b64 v[86:87], v252 offset:64000\n\t"
    "ds_read_b64 v[88:89], v169 offset:64000\n\t"
    "ds_read_b64 v[90:91], v253 offset:64000\n\t"
    "v_lshrrev_b32 v254, 16, v170\n\t"
    "v_and_b32 v170, s92, v170\n\t"
    "v_lshrrev_b32 v255, 16, v171\n\t"
    "v_and_b32 v171, s92, v171\n\t"
    "v_exp_f32 v70, v66\n\t"
    "v_exp_f32 v71, v67\n\t"
    "v_pk_fma_f32 v[68:69], v[68:69], s[40:41], s[40:41]\n\t"
    "v_pk_fma_f32 v[70:71], v[70:71], s[42:43], s[42:43]\n\t"
    "v_rcp_f32 v68, v68\n\t"
    "v_rcp_f32 v69, v69\n\t"
    "v_rcp_f32 v70, v70\n\t"
    "v_rcp_f32 v71, v71\n\t"
    "s_waitcnt lgkmcnt(4)\n\t"
    "v_smfmac_f32_16x16x64_bf16 v[228:231], v[120:123], v[92:99], v124\n\t"
    "ds_read_b64 v[92:93], v170 offset:64000\n\t"
    "ds_read_b64 v[94:95], v254 offset:64000\n\t"
    "ds_read_b64 v[96:97], v171 offset:64000\n\t"
    "ds_read_b64 v[98:99], v255 offset:64000\n\t"
    "buffer_load_dwordx4 v[168:171], v127, %12, s90 offen offset:1024 sc1\n\t"
    "v_lshrrev_b32 v252, 16, v112\n\t"
    "v_and_b32 v112, s92, v112\n\t"
    "v_lshrrev_b32 v253, 16, v113\n\t"
    "v_and_b32 v113, s92, v113\n\t"
    "v_pk_mul_f32 v[68:69], v[68:69], v[196:197]\n\t"
    "v_pk_fma_f32 v[68:69], v[70:71], v[198:199], v[68:69]\n\t"
    "v_add_f32 v242, v68, v69\n\t"
    "s_waitcnt lgkmcnt(4)\n\t"
    "v_smfmac_f32_16x16x64_bf16 v[200:203], v[120:123], v[84:91], v124\n\t"
    "ds_read_b64 v[84:85], v112 offset:0\n\t"
    "ds_read_b64 v[86:87], v252 offset:0\n\t"
    "ds_read_b64 v[88:89], v113 offset:0\n\t"
    "ds_read_b64 v[90:91], v253 offset:0\n\t"
    "v_lshrrev_b32 v254, 16, v114\n\t"
    "v_and_b32 v114, s92, v114\n\t"
    "v_lshrrev_b32 v255, 16, v115\n\t"
    "v_and_b32 v115, s92, v115\n\t"
    "s_add_u32 s94, s94, 0x2000\n\t"
    "buffer_load_dwordx4 v[44:47], v127, %12, s94 offen sc1\n\t"
    "s_mov_b64 exec, 1\n\t"
    "buffer_load_dword v164, off, %12, s93 offset:384 sc1\n\t"
    "s_mov_b64 exec, -1\n\t"
    "s_waitcnt lgkmcnt(4)\n\t"
    "v_smfmac_f32_16x16x64_bf16 v[200:203], v[120:123], v[92:99], v124\n\t"
    "ds_read_b64 v[92:93], v114 offset:0\n\t"
    "ds_read_b64 v[94:95], v254 offset:0\n\t"
    "ds_read_b64 v[96:97], v115 offset:0\n\t"
    "ds_read_b64 v[98:99], v255 offset:0\n\t"
    "s_add_u32 s91, s91, 0x8000\n\t"
    "buffer_load_dwordx4 v[112:115], v127, %12, s91 offen offset:0 sc1\n\t"
    "v_lshrrev_b32 v252, 16, v100\n\t"
    "v_and_b32 v100, s92, v100\n\t"
    "v_lshrrev_b32 v253, 16, v101\n\t"
    "v_mov_b64 v[232:233], s[70:71]\n\t"
    "v_mov_b64 v[234:235], s[72:73]\n\t"
    "v_and_b32 v101, s92, v101\n\t"
    "v_pk_mul_f32 v[72:73], v[200:201], v[200:201]\n\t"
    "v_pk_mul_f32 v[74:75], v[202:203], v[202:203]\n\t"
    "v_pk_fma_f32 v[72:73], v[72:73], v[82:83], v[80:81]\n\t"
    "v_pk_fma_f32 v[74:75], v[74:75], v[82:83], v[80:81]\n\t"
    "v_pk_mul_f32 v[72:73], v[72:73], v[200:201]\n\t"
    "v_pk_mul_f32 v[74:75], v[74:75], v[202:203]\n\t"
    "v_exp_f32 v76, v72\n\t"
    "v_exp_f32 v77, v73\n\t"
    "s_waitcnt lgkmcnt(0)\n\t"
    "s_barrier\n\t"
    "v_smfmac_f32_16x16x64_bf16 v[232:235], v[120:123], v[84:91], v124\n\t"
    "ds_read_b64 v[84:85], v100 offset:64000\n\t"
    "ds_read_b64 v[86:87], v252 offset:64000\n\t"
    "ds_read_b64 v[88:89], v101 offset:64000\n\t"
    "ds_read_b64 v[90:91], v253 offset:64000\n\t"
    "v_lshrrev_b32 v254, 16, v102\n\t"
    "v_and_b32 v102, s92, v102\n\t"
    "v_lshrrev_b32 v255, 16, v103\n\t"
    "v_and_b32 v103, s92, v103\n\t"
    "v_exp_f32 v78, v74\n\t"
    "v_exp_f32 v79, v75\n\t"
    "v_pk_fma_f32 v[76:77], v[76:77], s[40:41], s[40:41]\n\t"
    "v_pk_fma_f32 v[78:79], v[78:79], s[42:43], s[42:43]\n\t"
    "v_rcp_f32 v76, v76\n\t"
    "v_rcp_f32 v77, v77\n\t"
    "v_rcp_f32 v78, v78\n\t"
    "v_rcp_f32 v79, v79\n\t"
    "s_mul_i32 s44, s69, 1040\n\t"
    "s_add_u32 m0, s44, 0\n\t"
    "s_lshl_b32 s44, s69, 11\n\t"
    "buffer_load_dwordx4 v126, %15, s44 offen lds\n\t"
    "s_add_u32 s69, s69, 8\n\t"
    "s_sub_u32 s44, s69, 61\n\t"
    "s_cmp_ge_u32 s69, 61\n\t"
    "s_cselect_b32 s69, s44, s69\n\t"
    "s_waitcnt vmcnt(13)\n\t"
    "v_smfmac_f32_16x16x64_bf16 v[232:235], v[120:123], v[92:99], v124\n\t"
    "ds_read_b64 v[92:93], v102 offset:64000\n\t"
    "ds_read_b64 v[94:95], v254 offset:64000\n\t"
    "ds_read_b64 v[96:97], v103 offset:64000\n\t"
    "ds_read_b64 v[98:99], v255 offset:64000\n\t"
    "buffer_load_dwordx4 v[100:103], v127, %12, s91 offen offset:1024 sc1\n\t"
    "v_lshrrev_b32 v252, 16, v104\n\t"
    "v_and_b32 v104, s92, v104\n\t"
    "v_lshrrev_b32 v253, 16, v105\n\t"
    "v_and_b32 v105, s92, v105\n\t"
    "v_pk_mul_f32 v[76:77], v[76:77], v[200:201]\n\t"
    "v_pk_fma_f32 v[76:77], v[78:79], v[202:203], v[76:77]\n\t"
    "v_add_f32 v243, v76, v77\n\t"
    "s_setprio 0\n\t"
    "s_waitcnt lgkmcnt(4)\n\t"
    "v_smfmac_f32_16x16x64_bf16 v[204:207], v[120:123], v[84:91], v124\n\t"
    "ds_read_b64 v[84:85], v104 offset:64000\n\t"
    "ds_read_b64 v[86:87], v252 offset:64000\n\t"
    "ds_read_b64 v[88:89], v105 offset:64000\n\t"
    "ds_read_b64 v[90:91], v253 offset:64000\n\t"
    "v_lshrrev_b32 v254, 16, v106\n\t"
    "v_and_b32 v106, s92, v106\n\t"
    "v_lshrrev_b32 v255, 16, v107\n\t"
    "v_and_b32 v107, s92, v107\n\t"
    "s_waitcnt vmcnt(11)\n\t"
    "v_smfmac_f32_16x16x64_bf16 v[204:207], v[156:159], v[32:39], v124\n\t"
    "s_mul_i32 s44, s69, 1040\n\t"
    "s_add_u32 m0, s44, 0\n\t"
    "s_lshl_b32 s44, s69, 11\n\t"
    "buffer_load_dwordx4 v126, %15, s44 offen lds\n\t"
    "s_add_u32 s69, s69, 8\n\t"
    "s_sub_u32 s44, s69, 61\n\t"
    "s_cmp_ge_u32 s69, 61\n\t"
    "s_cselect_b32 s69, s44, s69\n\t"
    "s_waitcnt lgkmcnt(4)\n\t"
    "v_smfmac_f32_16x16x64_bf16 v[204:207], v[120:123], v[92:99], v124\n\t"
    "ds_read_b64 v[92:93], v106 offset:64000\n\t"
    "ds_read_b64 v[94:95], v254 offset:64000\n\t"
    "ds_read_b64 v[96:97], v107 offset:64000\n\t"
    "ds_read_b64 v[98:99], v255 offset:64000\n\t"
    "v_lshrrev_b32 v252, 16, v108\n\t"
    "v_and_b32 v108, s92, v108\n\t"
    "v_lshrrev_b32 v253, 16, v109\n\t"
    "v_and_b32 v109, s92, v109\n\t"
    "v_smfmac_f32_16x16x64_bf16 v[208:211], v[160:163], v[32:39], v124\n\t"
    "v_pk_mul_f32 v[64:65], v[204:205], v[204:205]\n\t"
    "v_pk_mul_f32 v[66:67], v[206:207], v[206:207]\n\t"
    "v_pk_fma_f32 v[64:65], v[64:65], v[82:83], v[80:81]\n\t"
    "v_pk_fma_f32 v[66:67], v[66:67], v[82:83], v[80:81]\n\t"
    "v_pk_mul_f32 v[64:65], v[64:65], v[204:205]\n\t"
    "v_pk_mul_f32 v[66:67], v[66:67], v[206:207]\n\t"
    "v_exp_f32 v68, v64\n\t"
    "ds_read_b128 v[32:35], v148 offset:8192\n\t"
    "s_waitcnt lgkmcnt(5)\n\t"
    "v_smfmac_f32_16x16x64_bf16 v[208:211], v[120:123], v[84:91], v124\n\t"
    "ds_read_b64 v[84:85], v108 offset:64000\n\t"
    "ds_read_b64 v[86:87], v252 offset:64000\n\t"
    "ds_read_b64 v[88:89], v109 offset:64000\n\t"
    "ds_read_b64 v[90:91], v253 offset:64000\n\t"
    "v_lshrrev_b32 v254, 16, v110\n\t"
    "v_and_b32 v110, s92, v110\n\t"
    "v_lshrrev_b32 v255, 16, v111\n\t"
    "v_and_b32 v111, s92, v111\n\t"
    "s_waitcnt vmcnt(4)\n\t"
    "v_readfirstlane_b32 s65, v164\n\t"
    "s_nop 0\n\t"
    "s_cmp_eq_u32 s65, 0\n\t"
    "s_cbranch_scc1 L_xw3_%=\n\t"
    "L_xwb3_%=:\n\t"
    "s_add_u32 s94, s94, 0x2000\n\t"
    "buffer_load_dwordx4 v[52:55], v127, %12, s94 offen sc1\n\t"
    "v_exp_f32 v69, v65\n\t"
    "v_exp_f32 v70, v66\n\t"
    "v_exp_f32 v71, v67\n\t"
    "v_pk_fma_f32 v[68:69], v[68:69], s[40:41], s[40:41]\n\t"
    "v_pk_fma_f32 v[70:71], v[70:71], s[42:43], s[42:43]\n\t"
    "v_rcp_f32 v68, v68\n\t"
    "v_rcp_f32 v69, v69\n\t"
    "s_mul_i32 s44, s69, 1040\n\t"
    "s_add_u32 m0, s44, 0\n\t"
    "s_lshl_b32 s44, s69, 11\n\t"
    "buffer_load_dwordx4 v126, %15, s44 offen lds\n\t"
    "s_add_u32 s69, s69, 8\n\t"
    "s_sub_u32 s44, s69, 61\n\t"
    "s_cmp_ge_u32 s69, 61\n\t"
    "s_cselect_b32 s69, s44, s69\n\t"
    "ds_read_b128 v[36:39], v148 offset:9216\n\t"
    "s_waitcnt lgkmcnt(6)\n\t"
    "v_smfmac_f32_16x16x64_bf16 v[208:211], v[120:123], v[92:99], v124\n\t"
    "ds_read_b64 v[92:93], v110 offset:64000\n\t"
    "ds_read_b64 v[94:95], v254 offset:64000\n\t"
    "ds_read_b64 v[96:97], v111 offset:64000\n\t"
    "ds_read_b64 v[98:99], v255 offset:64000\n\t"
    "v_lshrrev_b32 v252, 16, v116\n\t"
    "v_and_b32 v116, s92, v116\n\t"
    "v_lshrrev_b32 v253, 16, v117\n\t"
    "v_and_b32 v117, s92, v117\n\t"
    "v_rcp_f32 v70, v70\n\t"
    "v_rcp_f32 v71, v71\n\t"
    "v_pk_mul_f32 v[68:69], v[68:69], v[204:205]\n\t"
    "v_pk_fma_f32 v[68:69], v[70:71], v[206:207], v[68:69]\n\t"
    "v_add_f32 v244, v68, v69\n\t"
    "v_pk_mul_f32 v[72:73], v[208:209], v[208:209]\n\t"
    "v_pk_mul_f32 v[74:75], v[210:211], v[210:211]\n\t"
    "v_lshlrev_b32 v146, 2, v147\n\t"
    "s_lshl_b32 s50, s37, 8\n\t"
    "s_add_u32 s50, s50, 26214400\n\t"
    "s_lshl_b32 s46, s53, 8\n\t"
    "s_add_u32 s46, s46, s50\n\t"
    "buffer_load_dword v144, v146, %12, s46 offen sc1\n\t"
    "buffer_load_dword v145, v146, %12, s46 offen offset:512 sc1\n\t"
    "v_mov_b32 v149, s49\n\t"
    "s_add_u32 s46, s51, 6\n\t"
    "v_mov_b32 v155, s46\n\t"
    "ds_write_b32 v149, v155\n\t"
    "s_mov_b32 s61, 0\n\t"
    "v_mov_b32 v149, 1024\n\t"
    "v_mov_b32 v150, 0\n\t"
    "ds_write_b32 v149, v150\n\t"
    "s_mov_b32 s45, 1\n\t"
    "s_waitcnt lgkmcnt(7)\n\t"
    "v_smfmac_f32_16x16x64_bf16 v[212:215], v[120:123], v[84:91], v124\n\t"
    "ds_read_b64 v[84:85], v116 offset:64000\n\t"
    "ds_read_b64 v[86:87], v252 offset:64000\n\t"
    "ds_read_b64 v[88:89], v117 offset:64000\n\t"
    "ds_read_b64 v[90:91], v253 offset:64000\n\t"
    "v_lshrrev_b32 v254, 16, v118\n\t"
    "v_and_b32 v118, s92, v118\n\t"
    "v_lshrrev_b32 v255, 16, v119\n\t"
    "v_and_b32 v119, s92, v119\n\t"
    "v_smfmac_f32_16x16x64_bf16 v[212:215], v[156:159], v[40:47], v124\n\t"
    "v_pk_fma_f32 v[72:73], v[72:73], v[82:83], v[80:81]\n\t"
    "v_pk_fma_f32 v[74:75], v[74:75], v[82:83], v[80:81]\n\t"
    "v_pk_mul_f32 v[72:73], v[72:73], v[208:209]\n\t"
    "v_pk_mul_f32 v[74:75], v[74:75], v[210:211]\n\t"
    "v_exp_f32 v76, v72\n\t"
    "v_exp_f32 v77, v73\n\t"
    "v_exp_f32 v78, v74\n\t"
    "s_mul_i32 s44, s69, 1040\n\t"
    "s_add_u32 m0, s44, 0\n\t"
    "s_lshl_b32 s44, s69, 11\n\t"
    "buffer_load_dwordx4 v126, %15, s44 offen lds\n\t"
    "s_add_u32 s69, s69, 8\n\t"
    "s_sub_u32 s44, s69, 61\n\t"
    "s_cmp_ge_u32 s69, 61\n\t"
    "s_cselect_b32 s69, s44, s69\n\t"
    "s_waitcnt lgkmcnt(6)\n\t"
    "v_smfmac_f32_16x16x64_bf16 v[212:215], v[120:123], v[92:99], v124\n\t"
    "ds_read_b64 v[92:93], v118 offset:64000\n\t"
    "ds_read_b64 v[94:95], v254 offset:64000\n\t"
    "ds_read_b64 v[96:97], v119 offset:64000\n\t"
    "ds_read_b64 v[98:99], v255 offset:64000\n\t"
    "v_lshrrev_b32 v252, 16, v248\n\t"
    "v_and_b32 v248, s92, v248\n\t"
    "v_lshrrev_b32 v253, 16, v249\n\t"
    "v_and_b32 v249, s92, v249\n\t"
    "v_smfmac_f32_16x16x64_bf16 v[216:219], v[160:163], v[40:47], v124\n\t"
    "v_exp_f32 v79, v75\n\t"
    "v_pk_fma_f32 v[76:77], v[76:77], s[40:41], s[40:41]\n\t"
    "v_pk_fma_f32 v[78:79], v[78:79], s[42:43], s[42:43]\n\t"
    "v_rcp_f32 v76, v76\n\t"
    "v_rcp_f32 v77, v77\n\t"
    "v_rcp_f32 v78, v78\n\t"
    "v_rcp_f32 v79, v79\n\t"
    "ds_read_b128 v[40:43], v148 offset:10240\n\t"
    "s_waitcnt lgkmcnt(5)\n\t"
    "v_smfmac_f32_16x16x64_bf16 v[216:219], v[120:123], v[84:91], v124\n\t"
    "ds_read_b64 v[84:85], v248 offset:64000\n\t"
    "ds_read_b64 v[86:87], v252 offset:64000\n\t"
    "ds_read_b64 v[88:89], v249 offset:64000\n\t"
    "ds_read_b64 v[90:91], v253 offset:64000\n\t"
    "v_lshrrev_b32 v254, 16, v250\n\t"
    "v_and_b32 v250, s92, v250\n\t"
    "v_lshrrev_b32 v255, 16, v251\n\t"
    "v_and_b32 v251, s92, v251\n\t"
    "s_add_u32 s94, s94, 0x2000\n\t"
    "buffer_load_dwordx4 v[60:63], v127, %12, s94 offen sc1\n\t"
    "v_pk_mul_f32 v[76:77], v[76:77], v[208:209]\n\t"
    "v_pk_fma_f32 v[76:77], v[78:79], v[210:211], v[76:77]\n\t"
    "v_add_f32 v245, v76, v77\n\t"
    "v_pk_mul_f32 v[64:65], v[212:213], v[212:213]\n\t"
    "v_pk_mul_f32 v[66:67], v[214:215], v[214:215]\n\t"
    "v_pk_fma_f32 v[64:65], v[64:65], v[82:83], v[80:81]\n\t"
    "v_pk_fma_f32 v[66:67], v[66:67], v[82:83], v[80:81]\n\t"
    "s_mul_i32 s44, s69, 1040\n\t"
    "s_add_u32 m0, s44, 0\n\t"
    "s_lshl_b32 s44, s69, 11\n\t"
    "buffer_load_dwordx4 v126, %15, s44 offen lds\n\t"
    "s_add_u32 s69, s69, 8\n\t"
    "s_sub_u32 s44, s69, 61\n\t"
    "s_cmp_ge_u32 s69, 61\n\t"
    "s_cselect_b32 s69, s44, s69\n\t"
    "ds_read_b128 v[44:47], v148 offset:11264\n\t"
    "s_waitcnt lgkmcnt(6)\n\t"
    "v_smfmac_f32_16x16x64_bf16 v[216:219], v[120:123], v[92:99], v124\n\t"
    "ds_read_b64 v[92:93], v250 offset:64000\n\t"
    "ds_read_b64 v[94:95], v254 offset:64000\n\t"
    "ds_read_b64 v[96:97], v251 offset:64000\n\t"
    "ds_read_b64 v[98:99], v255 offset:64000\n\t"
    "v_lshrrev_b32 v252, 16, v168\n\t"
    "v_and_b32 v168, s92, v168\n\t"
    "v_lshrrev_b32 v253, 16, v169\n\t"
    "v_and_b32 v169, s92, v169\n\t"
    "v_pk_mul_f32 v[64:65], v[64:65], v[212:213]\n\t"
    "v_pk_mul_f32 v[66:67], v[66:67], v[214:215]\n\t"
    "v_exp_f32 v68, v64\n\t"
    "v_exp_f32 v69, v65\n\t"
    "v_exp_f32 v70, v66\n\t"
    "v_exp_f32 v71, v67\n\t"
    "v_pk_fma_f32 v[68:69], v[68:69], s[40:41], s[40:41]\n\t"
    "s_waitcnt lgkmcnt(5)\n\t"
    "v_smfmac_f32_16x16x64_bf16 v[220:223], v[120:123], v[84:91], v124\n\t"
    "ds_read_b64 v[84:85], v168 offset:64000\n\t"
    "ds_read_b64 v[86:87], v252 offset:64000\n\t"
    "ds_read_b64 v[88:89], v169 offset:64000\n\t"
    "ds_read_b64 v[90:91], v253 offset:64000\n\t"
    "v_lshrrev_b32 v254, 16, v170\n\t"
    "v_and_b32 v170, s92, v170\n\t"
    "v_lshrrev_b32 v255, 16, v171\n\t"
    "v_and_b32 v171, s92, v171\n\t"
    "v_pk_fma_f32 v[70:71], v[70:71], s[42:43], s[42:43]\n\t"
    "v_rcp_f32 v68, v68\n\t"
    "v_rcp_f32 v69, v69\n\t"
    "v_rcp_f32 v70, v70\n\t"
    "v_rcp_f32 v71, v71\n\t"
    "v_pk_mul_f32 v[68:69], v[68:69], v[212:213]\n\t"
    "v_pk_fma_f32 v[68:69], v[70:71], v[214:215], v[68:69]\n\t"
    "s_mul_i32 s44, s69, 1040\n\t"
    "s_add_u32 m0, s44, 0\n\t"
    "s_lshl_b32 s44, s69, 11\n\t"
    "buffer_load_dwordx4 v126, %15, s44 offen lds\n\t"
    "s_add_u32 s69, s69, 8\n\t"
    "s_sub_u32 s44, s69, 61\n\t"
    "s_cmp_ge_u32 s69, 61\n\t"
    "s_cselect_b32 s69, s44, s69\n\t"
    "s_waitcnt vmcnt(11) lgkmcnt(4)\n\t"
    "v_smfmac_f32_16x16x64_bf16 v[220:223], v[120:123], v[92:99], v124\n\t"
    "ds_read_b64 v[92:93], v170 offset:64000\n\t"
    "ds_read_b64 v[94:95], v254 offset:64000\n\t"
    "ds_read_b64 v[96:97], v171 offset:64000\n\t"
    "ds_read_b64 v[98:99], v255 offset:64000\n\t"
    "v_lshrrev_b32 v252, 16, v112\n\t"
    "v_and_b32 v112, s92, v112\n\t"
    "v_lshrrev_b32 v253, 16, v113\n\t"
    "v_and_b32 v113, s92, v113\n\t"
    "v_add_f32 v246, v68, v69\n\t"
    "v_pk_mul_f32 v[72:73], v[216:217], v[216:217]\n\t"
    "v_pk_mul_f32 v[74:75], v[218:219], v[218:219]\n\t"
    "v_pk_fma_f32 v[72:73], v[72:73], v[82:83], v[80:81]\n\t"
    "v_pk_fma_f32 v[74:75], v[74:75], v[82:83], v[80:81]\n\t"
    "v_pk_mul_f32 v[72:73], v[72:73], v[216:217]\n\t"
    "v_pk_mul_f32 v[74:75], v[74:75], v[218:219]\n\t"
    "s_waitcnt lgkmcnt(4)\n\t"
    "v_smfmac_f32_16x16x64_bf16 v[224:227], v[120:123], v[84:91], v124\n\t"
    "ds_read_b64 v[84:85], v112 offset:64000\n\t"
    "ds_read_b64 v[86:87], v252 offset:64000\n\t"
    "ds_read_b64 v[88:89], v113 offset:64000\n\t"
    "ds_read_b64 v[90:91], v253 offset:64000\n\t"
    "v_lshrrev_b32 v254, 16, v114\n\t"
    "v_and_b32 v114, s92, v114\n\t"
    "v_lshrrev_b32 v255, 16, v115\n\t"
    "v_and_b32 v115, s92, v115\n\t"
    "s_waitcnt vmcnt(7)\n\t"
    "v_smfmac_f32_16x16x64_bf16 v[220:223], v[156:159], v[48:55], v124\n\t"
    "v_exp_f32 v76, v72\n\t"
    "v_exp_f32 v77, v73\n\t"
    "v_exp_f32 v78, v74\n\t"
    "v_exp_f32 v79, v75\n\t"
    "v_pk_fma_f32 v[76:77], v[76:77], s[40:41], s[40:41]\n\t"
    "v_pk_fma_f32 v[78:79], v[78:79], s[42:43], s[42:43]\n\t"
    "v_rcp_f32 v76, v76\n\t"
    "s_mul_i32 s44, s69, 1040\n\t"
    "s_add_u32 m0, s44, 0\n\t"
    "s_lshl_b32 s44, s69, 11\n\t"
    "buffer_load_dwordx4 v126, %15, s44 offen lds\n\t"
    "s_add_u32 s69, s69, 8\n\t"
    "s_sub_u32 s44, s69, 61\n\t"
    "s_cmp_ge_u32 s69, 61\n\t"
    "s_cselect_b32 s69, s44, s69\n\t"
    "s_waitcnt lgkmcnt(4)\n\t"
    "v_smfmac_f32_16x16x64_bf16 v[224:227], v[120:123], v[92:99], v124\n\t"
    "ds_read_b64 v[92:93], v114 offset:64000\n\t"
    "ds_read_b64 v[94:95], v254 offset:64000\n\t"
    "ds_read_b64 v[96:97], v115 offset:64000\n\t"
    "ds_read_b64 v[98:99], v255 offset:64000\n\t"
    "v_lshrrev_b32 v252, 16, v100\n\t"
    "v_and_b32 v100, s92, v100\n\t"
    "v_lshrrev_b32 v253, 16, v101\n\t"
    "v_and_b32 v101, s92, v101\n\t"
    "v_smfmac_f32_16x16x64_bf16 v[224:227], v[160:163], v[48:55], v124\n\t"
    "v_rcp_f32 v77, v77\n\t"
    "v_rcp_f32 v78, v78\n\t"
    "v_rcp_f32 v79, v79\n\t"
    "v_pk_mul_f32 v[76:77], v[76:77], v[216:217]\n\t"
    "v_pk_fma_f32 v[76:77], v[78:79], v[218:219], v[76:77]\n\t"
    "v_add_f32 v247, v76, v77\n\t"
    "v_pk_mul_f32 v[64:65], v[220:221], v[220:221]\n\t"
    "ds_read_b128 v[48:51], v148 offset:12288\n\t"
    "s_waitcnt lgkmcnt(5)\n\t"
    "v_smfmac_f32_16x16x64_bf16 v[228:231], v[120:123], v[84:91], v124\n\t"
    "ds_read_b64 v[84:85], v100 offset:64000\n\t"
    "ds_read_b64 v[86:87], v252 offset:64000\n\t"
    "ds_read_b64 v[88:89], v101 offset:64000\n\t"
    "ds_read_b64 v[90:91], v253 offset:64000\n\t"
    "v_lshrrev_b32 v254, 16, v102\n\t"
    "v_and_b32 v102, s92, v102\n\t"
    "v_lshrrev_b32 v255, 16, v103\n\t"
    "v_and_b32 v103, s92, v103\n\t"
    "v_pk_mul_f32 v[66:67], v[222:223], v[222:223]\n\t"
    "v_pk_fma_f32 v[64:65], v[64:65], v[82:83], v[80:81]\n\t"
    "v_pk_fma_f32 v[66:67], v[66:67], v[82:83], v[80:81]\n\t"
    "v_pk_mul_f32 v[64:65], v[64:65], v[220:221]\n\t"
    "v_pk_mul_f32 v[66:67], v[66:67], v[222:223]\n\t"
    "v_exp_f32 v68, v64\n\t"
    "v_exp_f32 v69, v65\n\t"
    "s_mul_i32 s44, s69, 1040\n\t"
    "s_add_u32 m0, s44, 0\n\t"
    "s_lshl_b32 s44, s69, 11\n\t"
    "buffer_load_dwordx4 v126, %15, s44 offen lds\n\t"
    "s_add_u32 s69, s69, 8\n\t"
    "s_sub_u32 s44, s69, 61\n\t"
    "s_cmp_ge_u32 s69, 61\n\t"
    "s_cselect_b32 s69, s44, s69\n\t"
    "ds_read_b128 v[52:55], v148 offset:13312\n\t"
    "s_waitcnt lgkmcnt(6)\n\t"
    "v_smfmac_f32_16x16x64_bf16 v[228:231], v[120:123], v[92:99], v124\n\t"
    "ds_read_b64 v[92:93], v102 offset:64000\n\t"
    "ds_read_b64 v[94:95], v254 offset:64000\n\t"
    "ds_read_b64 v[96:97], v103 offset:64000\n\t"
    "ds_read_b64 v[98:99], v255 offset:64000\n\t"
    "v_exp_f32 v70, v66\n\t"
    "v_exp_f32 v71, v67\n\t"
    "v_pk_fma_f32 v[68:69], v[68:69], s[40:41], s[40:41]\n\t"
    "v_pk_fma_f32 v[70:71], v[70:71], s[42:43], s[42:43]\n\t"
    "v_rcp_f32 v68, v68\n\t"
    "v_rcp_f32 v69, v69\n\t"
    "v_rcp_f32 v70, v70\n\t"
    "s_waitcnt lgkmcnt(5)\n\t"
    "v_smfmac_f32_16x16x64_bf16 v[232:235], v[120:123], v[84:91], v124\n\t"
    "s_waitcnt vmcnt(4)\n\t"
    "v_smfmac_f32_16x16x64_bf16 v[228:231], v[156:159], v[56:63], v124\n\t"
    "v_rcp_f32 v71, v71\n\t"
    "v_pk_mul_f32 v[68:69], v[68:69], v[220:221]\n\t"
    "v_pk_fma_f32 v[68:69], v[70:71], v[222:223], v[68:69]\n\t"
    "v_add_f32 v248, v68, v69\n\t"
    "v_pk_mul_f32 v[72:73], v[224:225], v[224:225]\n\t"
    "v_pk_mul_f32 v[74:75], v[226:227], v[226:227]\n\t"
    "v_pk_fma_f32 v[72:73], v[72:73], v[82:83], v[80:81]\n\t"
    "s_waitcnt lgkmcnt(0)\n\t"
    "v_smfmac_f32_16x16x64_bf16 v[232:235], v[120:123], v[92:99], v124\n\t"
    "v_smfmac_f32_16x16x64_bf16 v[232:235], v[160:163], v[56:63], v124\n\t"
    "v_pk_fma_f32 v[74:75], v[74:75], v[82:83], v[80:81]\n\t"
    "v_pk_mul_f32 v[72:73], v[72:73], v[224:225]\n\t"
    "v_pk_mul_f32 v[74:75], v[74:75], v[226:227]\n\t"
    "v_exp_f32 v76, v72\n\t"
    "v_exp_f32 v77, v73\n\t"
    "v_exp_f32 v78, v74\n\t"
    "v_exp_f32 v79, v75\n\t"
    "ds_read_b128 v[56:59], v148 offset:14336\n\t"
    "s_barrier\n\t"
    "v_add_u32 v126, 124928, v126\n\t"
    "s_bfe_u32 s46, %14, 0x30003\n\t"
    "s_min_u32 s46, s46, 6\n\t"
    "s_lshl_b32 s69, s46, 3\n\t"
    "s_add_u32 s69, s69, %16\n\t"
    "s_mul_i32 s44, s69, 1040\n\t"
    "s_add_u32 m0, s44, 63440\n\t"
    "s_lshl_b32 s44, s69, 11\n\t"
    "buffer_load_dwordx4 v126, %15, s44 offen lds\n\t"
    "s_add_u32 s69, s69, 8\n\t"
    "s_sub_u32 s44, s69, 64\n\t"
    "s_cmp_ge_u32 s69, 64\n\t"
    "s_cselect_b32 s69, s44, s69\n\t"
    "s_mul_i32 s44, s69, 1040\n\t"
    "s_add_u32 m0, s44, 63440\n\t"
    "s_lshl_b32 s44, s69, 11\n\t"
    "buffer_load_dwordx4 v126, %15, s44 offen lds\n\t"
    "s_add_u32 s69, s69, 8\n\t"
    "s_sub_u32 s44, s69, 64\n\t"
    "s_cmp_ge_u32 s69, 64\n\t"
    "s_cselect_b32 s69, s44, s69\n\t"
    "s_mul_i32 s44, s69, 1040\n\t"
    "s_add_u32 m0, s44, 63440\n\t"
    "s_lshl_b32 s44, s69, 11\n\t"
    "buffer_load_dwordx4 v126, %15, s44 offen lds\n\t"
    "s_add_u32 s69, s69, 8\n\t"
    "s_sub_u32 s44, s69, 64\n\t"
    "s_cmp_ge_u32 s69, 64\n\t"
    "s_cselect_b32 s69, s44, s69\n\t"
    "s_mul_i32 s44, s69, 1040\n\t"
    "s_add_u32 m0, s44, 63440\n\t"
    "s_lshl_b32 s44, s69, 11\n\t"
    "buffer_load_dwordx4 v126, %15, s44 offen lds\n\t"
    "s_add_u32 s69, s69, 8\n\t"
    "s_sub_u32 s44, s69, 64\n\t"
    "s_cmp_ge_u32 s69, 64\n\t"
    "s_cselect_b32 s69, s44, s69\n\t"
    "ds_read_b128 v[60:63], v148 offset:15360\n\t"
    "ds_read_b128 v[84:87], v148 offset:21504\n\t"
    "ds_read_b128 v[88:91], v148 offset:22528\n\t"
    "v_pk_fma_f32 v[76:77], v[76:77], s[40:41], s[40:41]\n\t"
    "v_pk_fma_f32 v[78:79], v[78:79], s[42:43], s[42:43]\n\t"
    "v_rcp_f32 v76, v76\n\t"
    "v_rcp_f32 v77, v77\n\t"
    "v_rcp_f32 v78, v78\n\t"
    "v_rcp_f32 v79, v79\n\t"
    "v_pk_mul_f32 v[76:77], v[76:77], v[224:225]\n\t"
    "v_pk_fma_f32 v[76:77], v[78:79], v[226:227], v[76:77]\n\t"
    "v_add_f32 v249, v76, v77\n\t"
    "s_mul_i32 s44, s69, 1040\n\t"
    "s_add_u32 m0, s44, 63440\n\t"
    "s_lshl_b32 s44, s69, 11\n\t"
    "buffer_load_dwordx4 v126, %15, s44 offen lds\n\t"
    "s_add_u32 s69, s69, 8\n\t"
    "s_sub_u32 s44, s69, 64\n\t"
    "s_cmp_ge_u32 s69, 64\n\t"
    "s_cselect_b32 s69, s44, s69\n\t"
    "s_mul_i32 s44, s69, 1040\n\t"
    "s_add_u32 m0, s44, 63440\n\t"
    "s_lshl_b32 s44, s69, 11\n\t"
    "buffer_load_dwordx4 v126, %15, s44 offen lds\n\t"
    "s_add_u32 s69, s69, 8\n\t"
    "s_sub_u32 s44, s69, 64\n\t"
    "s_cmp_ge_u32 s69, 64\n\t"
    "s_cselect_b32 s69, s44, s69\n\t"
    "v_pk_mul_f32 v[64:65], v[228:229], v[228:229]\n\t"
    "v_pk_mul_f32 v[66:67], v[230:231], v[230:231]\n\t"
    "v_pk_fma_f32 v[64:65], v[64:65], v[82:83], v[80:81]\n\t"
    "v_pk_fma_f32 v[66:67], v[66:67], v[82:83], v[80:81]\n\t"
    "v_pk_mul_f32 v[64:65], v[64:65], v[228:229]\n\t"
    "v_pk_mul_f32 v[66:67], v[66:67], v[230:231]\n\t"
    "v_exp_f32 v68, v64\n\t"
    "v_exp_f32 v69, v65\n\t"
    "v_exp_f32 v70, v66\n\t"
    "v_exp_f32 v71, v67\n\t"
    "v_pk_fma_f32 v[68:69], v[68:69], s[40:41], s[40:41]\n\t"
    "v_pk_fma_f32 v[70:71], v[70:71], s[42:43], s[42:43]\n\t"
    "v_rcp_f32 v68, v68\n\t"
    "v_rcp_f32 v69, v69\n\t"
    "v_rcp_f32 v70, v70\n\t"
    "v_rcp_f32 v71, v71\n\t"
    "v_pk_mul_f32 v[68:69], v[68:69], v[228:229]\n\t"
    "v_pk_fma_f32 v[68:69], v[70:71], v[230:231], v[68:69]\n\t"
    "v_add_f32 v250, v68, v69\n\t"
    "s_mul_i32 s44, s69, 1040\n\t"
    "s_add_u32 m0, s44, 63440\n\t"
    "s_lshl_b32 s44, s69, 11\n\t"
    "buffer_load_dwordx4 v126, %15, s44 offen lds\n\t"
    "s_add_u32 s69, s69, 8\n\t"
    "s_sub_u32 s44, s69, 64\n\t"
    "s_cmp_ge_u32 s69, 64\n\t"
    "s_cselect_b32 s69, s44, s69\n\t"
    "s_mul_i32 s44, s69, 1040\n\t"
    "s_add_u32 m0, s44, 63440\n\t"
    "s_lshl_b32 s44, s69, 11\n\t"
    "buffer_load_dwordx4 v126, %15, s44 offen lds\n\t"
    "s_add_u32 s69, s69, 8\n\t"
    "s_sub_u32 s44, s69, 64\n\t"
    "s_cmp_ge_u32 s69, 64\n\t"
    "s_cselect_b32 s69, s44, s69\n\t"
    "v_pk_mul_f32 v[72:73], v[232:233], v[232:233]\n\t"
    "v_pk_mul_f32 v[74:75], v[234:235], v[234:235]\n\t"
    "v_pk_fma_f32 v[72:73], v[72:73], v[82:83], v[80:81]\n\t"
    "v_pk_fma_f32 v[74:75], v[74:75], v[82:83], v[80:81]\n\t"
    "v_pk_mul_f32 v[72:73], v[72:73], v[232:233]\n\t"
    "v_pk_mul_f32 v[74:75], v[74:75], v[234:235]\n\t"
    "v_exp_f32 v76, v72\n\t"
    "v_exp_f32 v77, v73\n\t"
    "v_exp_f32 v78, v74\n\t"
    "v_exp_f32 v79, v75\n\t"
    "v_pk_fma_f32 v[76:77], v[76:77], s[40:41], s[40:41]\n\t"
    "v_pk_fma_f32 v[78:79], v[78:79], s[42:43], s[42:43]\n\t"
    "v_rcp_f32 v76, v76\n\t"
    "v_rcp_f32 v77, v77\n\t"
    "v_rcp_f32 v78, v78\n\t"
    "v_rcp_f32 v79, v79\n\t"
    "v_pk_mul_f32 v[76:77], v[76:77], v[232:233]\n\t"
    "v_pk_fma_f32 v[76:77], v[78:79], v[234:235], v[76:77]\n\t"
    "v_add_f32 v251, v76, v77\n\t"
    "ds_read_b128 v[64:67], v148 offset:16384\n\t"
    "ds_read_b128 v[68:71], v148 offset:17408\n\t"
    "ds_read_b128 v[72:75], v148 offset:18432\n\t"
    "ds_read_b128 v[76:79], v148 offset:19456\n\t"
    "ds_read_b128 v[80:83], v148 offset:20480\n\t"
    "s_waitcnt vmcnt(0) lgkmcnt(0)\n\t"
    "s_mov_b32 s68, 130\n\t"
    "v_mad_u32_u24 v128, v128, s68, v152\n\t"
    "v_and_b32 v128, -16, v128\n\t"
    "v_mad_u32_u24 v129, v129, s68, v151\n\t"
    "v_and_b32 v129, -16, v129\n\t"
    "v_mad_u32_u24 v130, v130, s68, v151\n\t"
    "v_and_b32 v130, -16, v130\n\t"
    "v_mad_u32_u24 v132, v132, s68, v151\n\t"
    "v_and_b32 v132, -16, v132\n\t"
    "v_mad_u32_u24 v133, v133, s68, v151\n\t"
    "v_and_b32 v133, -16, v133\n\t"
    "v_mad_u32_u24 v134, v134, s68, v151\n\t"
    "v_and_b32 v134, -16, v134\n\t"
    "v_mad_u32_u24 v135, v135, s68, v151\n\t"
    "v_and_b32 v135, -16, v135\n\t"
    "v_lshrrev_b32 v122, 4, v147\n\t"
    "v_and_b32 v155, 15, v147\n\t"
    "v_lshlrev_b32 v155, 4, v155\n\t"
    "v_lshl_add_u32 v122, v122, 16, v155\n\t"
    "v_mov_b32 v150, 1\n\t"
    "v_mov_b32 v123, 0\n\t"
    "v_lshlrev_b32 v149, 20, v147\n\t"
    "v_add_u32 v149, s49, v149\n\t"
    "s_mov_b32 s52, s53\n\t"
    "s_cmp_ge_u32 %16, 4\n\t"
    "s_cbranch_scc1 L_st4_%=\n\t"
    "s_bfe_u32 s65, s84, 0xc0002\n\t"
    "s_add_u32 s65, s65, s36\n\t"
    "buffer_store_dword v236, v125, %12, s65 offen offset:0 sc1\n\t"
    "buffer_store_dword v237, v125, %12, s65 offen offset:256 sc1\n\t"
    "s_bfe_u32 s65, s85, 0xc0002\n\t"
    "s_add_u32 s65, s65, s36\n\t"
    "buffer_store_dword v238, v125, %12, s65 offen offset:0 sc1\n\t"
    "buffer_store_dword v239, v125, %12, s65 offen offset:256 sc1\n\t"
    "s_bfe_u32 s65, s86, 0xc0002\n\t"
    "s_add_u32 s65, s65, s36\n\t"
    "buffer_store_dword v240, v125, %12, s65 offen offset:0 sc1\n\t"
    "buffer_store_dword v241, v125, %12, s65 offen offset:256 sc1\n\t"
    "s_bfe_u32 s65, s87, 0xc0002\n\t"
    "s_add_u32 s65, s65, s36\n\t"
    "buffer_store_dword v242, v125, %12, s65 offen offset:0 sc1\n\t"
    "buffer_store_dword v243, v125, %12, s65 offen offset:256 sc1\n\t"
    "s_bfe_u32 s65, s88, 0xc0002\n\t"
    "s_add_u32 s65, s65, s36\n\t"
    "buffer_store_dword v244, v125, %12, s65 offen offset:0 sc1\n\t"
    "buffer_store_dword v245, v125, %12, s65 offen offset:256 sc1\n\t"
    "s_bfe_u32 s65, s89, 0xc0002\n\t"
    "s_add_u32 s65, s65, s36\n\t"
    "buffer_store_dword v246, v125, %12, s65 offen offset:0 sc1\n\t"
    "buffer_store_dword v247, v125, %12, s65 offen offset:256 sc1\n\t"
    "s_bfe_u32 s65, s90, 0xc0002\n\t"
    "s_add_u32 s65, s65, s36\n\t"
    "buffer_store_dword v248, v125, %12, s65 offen offset:0 sc1\n\t"
    "buffer_store_dword v249, v125, %12, s65 offen offset:256 sc1\n\t"
    "s_bfe_u32 s65, s91, 0xc0002\n\t"
    "s_add_u32 s65, s65, s36\n\t"
    "buffer_store_dword v250, v125, %12, s65 offen offset:0 sc1\n\t"
    "buffer_store_dword v251, v125, %12, s65 offen offset:256 sc1\n\t"
    "s_sub_u32 s61, s64, 3\n\t"
    "L_st4_%=:\n\t"
    "s_barrier\n\t"
    "s_mov_b32 s68, 130\n\t"
    "ds_read_b128 v[156:159], v128 offset:0\n\t"
    "ds_read_b128 v[160:163], v128 offset:64\n\t"
    "ds_read_b128 v[164:167], v129 offset:0\n\t"
    "ds_read_b128 v[168:171], v129 offset:64\n\t"
    "ds_read_b128 v[172:175], v130 offset:0\n\t"
    "ds_read_b128 v[176:179], v130 offset:64\n\t"
    "ds_read_b128 v[180:183], v132 offset:0\n\t"
    "ds_read_b128 v[184:187], v132 offset:64\n\t"
    "ds_read_b128 v[188:191], v133 offset:0\n\t"
    "ds_read_b128 v[192:195], v133 offset:64\n\t"
    "ds_read_b128 v[196:199], v134 offset:0\n\t"
    "ds_read_b128 v[200:203], v134 offset:64\n\t"
    "ds_read_b128 v[204:207], v135 offset:0\n\t"
    "ds_read_b128 v[208:211], v135 offset:64\n\t"
    "s_setprio 1\n\t"
    "L_pnn_%=:\n\t"
    "s_mul_i32 s69, s52, 272\n\t"
    "s_add_u32 s69, s69, 130048\n\t"
    "s_mul_i32 s66, s55, 400\n\t"
    "s_waitcnt lgkmcnt(8)\n\t"
    "v_mfma_f32_16x16x32_bf16 v[92:95], v[156:159], v[156:159], 0\n\t"
    "v_add_u32 v147, s69, v146\n\t"
    "v_fmac_f32 v121, v108, v72\n\t"
    "ds_add_rtn_u32 v148, v149, v150\n\t"
    "v_mfma_f32_16x16x32_bf16 v[92:95], v[160:163], v[160:163], v[92:95]\n\t"
    "v_fmac_f32 v120, v109, v73\n\t"
    "v_fmac_f32 v121, v110, v74\n\t"
    "buffer_load_dwordx3 v[128:130], v153, s[96:99], s66 offen\n\t"
    "v_mfma_f32_16x16x32_bf16 v[96:99], v[164:167], v[164:167], 0\n\t"
    "v_fmac_f32 v120, v111, v75\n\t"
    "v_fmac_f32 v121, v112, v76\n\t"
    "v_mfma_f32_16x16x32_bf16 v[96:99], v[168:171], v[168:171], v[96:99]\n\t"
    "v_fmac_f32 v120, v113, v77\n\t"
    "v_fmac_f32 v121, v114, v78\n\t"
    "buffer_load_dwordx4 v[132:135], v153, s[96:99], s66 offen offset:12\n\t"
    "v_mfma_f32_16x16x32_bf16 v[100:103], v[164:167], v[172:175], 0\n\t"
    "v_fmac_f32 v120, v115, v79\n\t"
    "v_fmac_f32 v121, v116, v80\n\t"
    "v_mfma_f32_16x16x32_bf16 v[100:103], v[168:171], v[176:179], v[100:103]\n\t"
    "v_fmac_f32 v120, v117, v81\n\t"
    "v_fmac_f32 v121, v118, v82\n\t"
    "s_waitcnt vmcnt(3)\n\t"
    "s_cmp_ge_u32 s55, s61\n\t"
    "s_cbranch_scc1 L_req0_%=\n\t"
    "L_back0_%=:\n\t"
    "v_mfma_f32_16x16x32_bf16 v[104:107], v[172:175], v[172:175], 0\n\t"
    "v_mad_u32_u24 v136, v136, s68, v152\n\t"
    "v_and_b32 v136, -16, v136\n\t"
    "v_mfma_f32_16x16x32_bf16 v[104:107], v[176:179], v[176:179], v[104:107]\n\t"
    "v_fmac_f32 v120, v119, v83\n\t"
    "v_add_f32 v154, v120, v121\n\t"
    "ds_write_b32 v147, v154\n\t"
    "v_mfma_f32_16x16x32_bf16 v[108:111], v[156:159], v[164:167], 0\n\t"
    "v_fma_f32 v120, v92, v84, v144\n\t"
    "v_mul_f32 v121, v96, v0\n\t"
    "v_mfma_f32_16x16x32_bf16 v[108:111], v[160:163], v[168:171], v[108:111]\n\t"
    "v_fmac_f32 v120, v97, v1\n\t"
    "v_fmac_f32 v121, v98, v2\n\t"
    "s_waitcnt lgkmcnt(8)\n\t"
    "v_mfma_f32_16x16x32_bf16 v[112:115], v[164:167], v[180:183], 0\n\t"
    "v_fmac_f32 v120, v99, v3\n\t"
    "v_fmac_f32 v121, v100, v4\n\t"
    "v_mfma_f32_16x16x32_bf16 v[112:115], v[168:171], v[184:187], v[112:115]\n\t"
    "v_fmac_f32 v120, v101, v5\n\t"
    "v_fmac_f32 v121, v102, v6\n\t"
    "v_mfma_f32_16x16x32_bf16 v[116:119], v[172:175], v[180:183], 0\n\t"
    "v_fmac_f32 v120, v103, v7\n\t"
    "v_fmac_f32 v121, v104, v8\n\t"
    "v_mfma_f32_16x16x32_bf16 v[116:119], v[176:179], v[184:187], v[116:119]\n\t"
    "v_fmac_f32 v120, v105, v9\n\t"
    "v_fmac_f32 v121, v106, v10\n\t"
    "ds_read_b128 v[212:215], v136 offset:0\n\t"
    "ds_read_b128 v[216:219], v136 offset:64\n\t"
    "v_mfma_f32_16x16x32_bf16 v[92:95], v[180:183], v[180:183], 0\n\t"
    "v_fmac_f32 v120, v107, v11\n\t"
    "v_mad_u32_u24 v137, v137, s68, v151\n\t"
    "v_mfma_f32_16x16x32_bf16 v[92:95], v[184:187], v[184:187], v[92:95]\n\t"
    "v_and_b32 v137, -16, v137\n\t"
    "v_fmac_f32 v121, v108, v85\n\t"
    "v_mfma_f32_16x16x32_bf16 v[96:99], v[156:159], v[172:175], 0\n\t"
    "v_fmac_f32 v120, v112, v12\n\t"
    "v_fmac_f32 v121, v113, v13\n\t"
    "v_mfma_f32_16x16x32_bf16 v[96:99], v[160:163], v[176:179], v[96:99]\n\t"
    "v_fmac_f32 v120, v114, v14\n\t"
    "v_fmac_f32 v121, v115, v15\n\t"
    "s_lshl_b32 s51, s55, 8\n\t"
    "s_add_u32 s51, s51, s50\n\t"
    "s_waitcnt lgkmcnt(8)\n\t"
    "v_mfma_f32_16x16x32_bf16 v[100:103], v[164:167], v[188:191], 0\n\t"
    "v_mad_u32_u24 v138, v138, s68, v151\n\t"
    "v_and_b32 v138, -16, v138\n\t"
    "v_mfma_f32_16x16x32_bf16 v[100:103], v[168:171], v[192:195], v[100:103]\n\t"
    "v_fmac_f32 v120, v116, v16\n\t"
    "v_fmac_f32 v121, v117, v17\n\t"
    "buffer_load_dword v144, v146, %12, s51 offen sc1\n\t"
    "v_mfma_f32_16x16x32_bf16 v[104:107], v[172:175], v[188:191], 0\n\t"
    "v_fmac_f32 v120, v118, v18\n\t"
    "v_fmac_f32 v121, v119, v19\n\t"
    "v_mfma_f32_16x16x32_bf16 v[104:107], v[176:179], v[192:195], v[104:107]\n\t"
    "v_fmac_f32 v120, v92, v20\n\t"
    "v_fmac_f32 v121, v93, v21\n\t"
    "s_waitcnt lgkmcnt(6)\n\t"
    "v_mfma_f32_16x16x32_bf16 v[108:111], v[164:167], v[196:199], 0\n\t"
    "v_fmac_f32 v120, v94, v22\n\t"
    "v_fmac_f32 v121, v95, v23\n\t"
    "v_mfma_f32_16x16x32_bf16 v[108:111], v[168:171], v[200:203], v[108:111]\n\t"
    "v_fmac_f32 v120, v96, v86\n\t"
    "v_mad_u32_u24 v140, v140, s68, v151\n\t"
    "v_mfma_f32_16x16x32_bf16 v[112:115], v[156:159], v[180:183], 0\n\t"
    "v_fmac_f32 v121, v100, v24\n\t"
    "v_fmac_f32 v120, v101, v25\n\t"
    "v_mfma_f32_16x16x32_bf16 v[112:115], v[160:163], v[184:187], v[112:115]\n\t"
    "v_fmac_f32 v121, v102, v26\n\t"
    "v_fmac_f32 v120, v103, v27\n\t"
    "s_waitcnt lgkmcnt(3)\n\t"
    "v_mfma_f32_16x16x32_bf16 v[116:119], v[164:167], v[204:207], 0\n\t"
    "v_readfirstlane_b32 s60, v148\n\t"
    "v_and_b32 v140, -16, v140\n\t"
    "v_mfma_f32_16x16x32_bf16 v[116:119], v[168:171], v[208:211], v[116:119]\n\t"
    "v_fmac_f32 v121, v104, v28\n\t"
    "v_fmac_f32 v120, v105, v29\n\t"
    "ds_read_b128 v[164:167], v137 offset:0\n\t"
    "ds_read_b128 v[168:171], v137 offset:64\n\t"
    "v_mfma_f32_16x16x32_bf16 v[92:95], v[172:175], v[196:199], 0\n\t"
    "v_fmac_f32 v121, v106, v30\n\t"
    "v_fmac_f32 v120, v107, v31\n\t"
    "v_mfma_f32_16x16x32_bf16 v[92:95], v[176:179], v[200:203], v[92:95]\n\t"
    "v_fmac_f32 v121, v108, v32\n\t"
    "v_fmac_f32 v120, v109, v33\n\t"
    "v_mfma_f32_16x16x32_bf16 v[96:99], v[172:175], v[204:207], 0\n\t"
    "v_fmac_f32 v121, v110, v34\n\t"
    "v_fmac_f32 v120, v111, v35\n\t"
    "v_mfma_f32_16x16x32_bf16 v[96:99], v[176:179], v[208:211], v[96:99]\n\t"
    "v_fmac_f32 v121, v112, v87\n\t"
    "v_mad_u32_u24 v141, v141, s68, v151\n\t"
    "ds_read_b128 v[172:175], v138 offset:0\n\t"
    "ds_read_b128 v[176:179], v138 offset:64\n\t"
    "v_mfma_f32_16x16x32_bf16 v[100:103], v[156:159], v[188:191], 0\n\t"
    "v_fmac_f32 v120, v116, v36\n\t"
    "v_fmac_f32 v121, v117, v37\n\t"
    "v_mfma_f32_16x16x32_bf16 v[100:103], v[160:163], v[192:195], v[100:103]\n\t"
    "v_fmac_f32 v120, v118, v38\n\t"
    "v_fmac_f32 v121, v119, v39\n\t"
    "v_mfma_f32_16x16x32_bf16 v[104:107], v[180:183], v[188:191], 0\n\t"
    "v_and_b32 v141, -16, v141\n\t"
    "v_fmac_f32 v120, v92, v40\n\t"
    "v_mfma_f32_16x16x32_bf16 v[104:107], v[184:187], v[192:195], v[104:107]\n\t"
    "v_fmac_f32 v121, v93, v41\n\t"
    "v_fmac_f32 v120, v94, v42\n\t"
    "v_mfma_f32_16x16x32_bf16 v[108:111], v[180:183], v[196:199], 0\n\t"
    "v_fmac_f32 v121, v95, v43\n\t"
    "v_fmac_f32 v120, v96, v44\n\t"
    "v_mfma_f32_16x16x32_bf16 v[108:111], v[184:187], v[200:203], v[108:111]\n\t"
    "v_fmac_f32 v121, v97, v45\n\t"
    "v_fmac_f32 v120, v98, v46\n\t"
    "v_mfma_f32_16x16x32_bf16 v[112:115], v[180:183], v[204:207], 0\n\t"
    "v_fmac_f32 v121, v99, v47\n\t"
    "v_fmac_f32 v120, v100, v88\n\t"
    "v_mfma_f32_16x16x32_bf16 v[112:115], v[184:187], v[208:211], v[112:115]\n\t"
    "v_mad_u32_u24 v142, v142, s68, v151\n\t"
    "v_and_b32 v142, -16, v142\n\t"
    "ds_read_b128 v[180:183], v140 offset:0\n\t"
    "ds_read_b128 v[184:187], v140 offset:64\n\t"
    "s_cmp_eq_u32 s45, 1\n\t"
    "s_cbranch_scc1 L_sig_%=\n\t"
    "L_sigb_%=:\n\t"
    "v_mfma_f32_16x16x32_bf16 v[116:119], v[156:159], v[196:199], 0\n\t"
    "v_fmac_f32 v121, v104, v48\n\t"
    "v_fmac_f32 v120, v105, v49\n\t"
    "v_mfma_f32_16x16x32_bf16 v[116:119], v[160:163], v[200:203], v[116:119]\n\t"
    "v_fmac_f32 v121, v106, v50\n\t"
    "v_fmac_f32 v120, v107, v51\n\t"
    "v_mfma_f32_16x16x32_bf16 v[92:95], v[188:191], v[188:191], 0\n\t"
    "v_mad_u32_u24 v143, v143, s68, v151\n\t"
    "v_and_b32 v143, -16, v143\n\t"
    "v_mfma_f32_16x16x32_bf16 v[92:95], v[192:195], v[192:195], v[92:95]\n\t"
    "v_fmac_f32 v121, v108, v52\n\t"
    "v_fmac_f32 v120, v109, v53\n\t"
    "v_mfma_f32_16x16x32_bf16 v[96:99], v[188:191], v[196:199], 0\n\t"
    "v_fmac_f32 v121, v110, v54\n\t"
    "v_fmac_f32 v120, v111, v55\n\t"
    "v_mfma_f32_16x16x32_bf16 v[96:99], v[192:195], v[200:203], v[96:99]\n\t"
    "v_fmac_f32 v121, v112, v56\n\t"
    "v_fmac_f32 v120, v113, v57\n\t"
    "v_mfma_f32_16x16x32_bf16 v[100:103], v[188:191], v[204:207], 0\n\t"
    "v_fmac_f32 v121, v114, v58\n\t"
    "v_fmac_f32 v120, v115, v59\n\t"
    "v_mfma_f32_16x16x32_bf16 v[100:103], v[192:195], v[208:211], v[100:103]\n\t"
    "v_fmac_f32 v121, v116, v89\n\t"
    "ds_read_b128 v[188:191], v141 offset:0\n\t"
    "ds_read_b128 v[192:195], v141 offset:64\n\t"
    "v_mfma_f32_16x16x32_bf16 v[104:107], v[156:159], v[204:207], 0\n\t"
    "v_fmac_f32 v120, v92, v60\n\t"
    "v_fmac_f32 v121, v93, v61\n\t"
    "v_mfma_f32_16x16x32_bf16 v[104:107], v[160:163], v[208:211], v[104:107]\n\t"
    "v_fmac_f32 v120, v94, v62\n\t"
    "v_fmac_f32 v121, v95, v63\n\t"
    "v_mfma_f32_16x16x32_bf16 v[108:111], v[196:199], v[196:199], 0\n\t"
    "v_fmac_f32 v120, v96, v64\n\t"
    "v_fmac_f32 v121, v97, v65\n\t"
    "v_mfma_f32_16x16x32_bf16 v[108:111], v[200:203], v[200:203], v[108:111]\n\t"
    "v_fmac_f32 v120, v98, v66\n\t"
    "v_fmac_f32 v121, v99, v67\n\t"
    "v_mfma_f32_16x16x32_bf16 v[112:115], v[196:199], v[204:207], 0\n\t"
    "v_fmac_f32 v120, v100, v68\n\t"
    "v_fmac_f32 v121, v101, v69\n\t"
    "v_mfma_f32_16x16x32_bf16 v[112:115], v[200:203], v[208:211], v[112:115]\n\t"
    "v_fmac_f32 v120, v102, v70\n\t"
    "v_fmac_f32 v121, v103, v71\n\t"
    "ds_read_b128 v[196:199], v142 offset:0\n\t"
    "ds_read_b128 v[200:203], v142 offset:64\n\t"
    "v_mfma_f32_16x16x32_bf16 v[116:119], v[204:207], v[204:207], 0\n\t"
    "v_fmac_f32 v120, v104, v90\n\t"
    "v_mfma_f32_16x16x32_bf16 v[116:119], v[208:211], v[208:211], v[116:119]\n\t"
    "ds_read_b128 v[204:207], v143 offset:0\n\t"
    "ds_read_b128 v[208:211], v143 offset:64\n\t"
    "s_mov_b32 s52, s53\n\t"
    "s_mov_b32 s53, s54\n\t"
    "s_mov_b32 s54, s55\n\t"
    "s_mov_b32 s55, s60\n\t"
    "s_cmp_lt_u32 s54, s64\n\t"
    "s_cbranch_scc1 L_pr0_%=\n\t"
    "s_setprio 0\n\t"
    "L_pr0_%=:\n\t"
    "s_cmp_lt_u32 s53, s64\n\t"
    "s_cbranch_scc0 L_done_%=\n\t"
    "s_mul_i32 s69, s52, 272\n\t"
    "s_add_u32 s69, s69, 130048\n\t"
    "s_mul_i32 s66, s55, 400\n\t"
    "s_waitcnt lgkmcnt(8)\n\t"
    "v_mfma_f32_16x16x32_bf16 v[92:95], v[212:215], v[212:215], 0\n\t"
    "v_add_u32 v147, s69, v146\n\t"
    "v_fmac_f32 v121, v108, v72\n\t"
    "ds_add_rtn_u32 v148, v149, v150\n\t"
    "v_mfma_f32_16x16x32_bf16 v[92:95], v[216:219], v[216:219], v[92:95]\n\t"
    "v_fmac_f32 v120, v109, v73\n\t"
    "v_fmac_f32 v121, v110, v74\n\t"
    "buffer_load_dwordx3 v[136:138], v153, s[96:99], s66 offen\n\t"
    "v_mfma_f32_16x16x32_bf16 v[96:99], v[164:167], v[164:167], 0\n\t"
    "v_fmac_f32 v120, v111, v75\n\t"
    "v_fmac_f32 v121, v112, v76\n\t"
    "v_mfma_f32_16x16x32_bf16 v[96:99], v[168:171], v[168:171], v[96:99]\n\t"
    "v_fmac_f32 v120, v113, v77\n\t"
    "v_fmac_f32 v121, v114, v78\n\t"
    "buffer_load_dwordx4 v[140:143], v153, s[96:99], s66 offen offset:12\n\t"
    "v_mfma_f32_16x16x32_bf16 v[100:103], v[164:167], v[172:175], 0\n\t"
    "v_fmac_f32 v120, v115, v79\n\t"
    "v_fmac_f32 v121, v116, v80\n\t"
    "v_mfma_f32_16x16x32_bf16 v[100:103], v[168:171], v[176:179], v[100:103]\n\t"
    "v_fmac_f32 v120, v117, v81\n\t"
    "v_fmac_f32 v121, v118, v82\n\t"
    "s_waitcnt vmcnt(3)\n\t"
    "s_cmp_ge_u32 s55, s61\n\t"
    "s_cbranch_scc1 L_req1_%=\n\t"
    "L_back1_%=:\n\t"
    "v_mfma_f32_16x16x32_bf16 v[104:107], v[172:175], v[172:175], 0\n\t"
    "v_mad_u32_u24 v128, v128, s68, v152\n\t"
    "v_and_b32 v128, -16, v128\n\t"
    "v_mfma_f32_16x16x32_bf16 v[104:107], v[176:179], v[176:179], v[104:107]\n\t"
    "v_fmac_f32 v120, v119, v83\n\t"
    "v_add_f32 v154, v120, v121\n\t"
    "ds_write_b32 v147, v154\n\t"
    "v_mfma_f32_16x16x32_bf16 v[108:111], v[212:215], v[164:167], 0\n\t"
    "v_fma_f32 v120, v92, v84, v145\n\t"
    "v_mul_f32 v121, v96, v0\n\t"
    "v_mfma_f32_16x16x32_bf16 v[108:111], v[216:219], v[168:171], v[108:111]\n\t"
    "v_fmac_f32 v120, v97, v1\n\t"
    "v_fmac_f32 v121, v98, v2\n\t"
    "s_waitcnt lgkmcnt(8)\n\t"
    "v_mfma_f32_16x16x32_bf16 v[112:115], v[164:167], v[180:183], 0\n\t"
    "v_fmac_f32 v120, v99, v3\n\t"
    "v_fmac_f32 v121, v100, v4\n\t"
    "v_mfma_f32_16x16x32_bf16 v[112:115], v[168:171], v[184:187], v[112:115]\n\t"
    "v_fmac_f32 v120, v101, v5\n\t"
    "v_fmac_f32 v121, v102, v6\n\t"
    "v_mfma_f32_16x16x32_bf16 v[116:119], v[172:175], v[180:183], 0\n\t"
    "v_fmac_f32 v120, v103, v7\n\t"
    "v_fmac_f32 v121, v104, v8\n\t"
    "v_mfma_f32_16x16x32_bf16 v[116:119], v[176:179], v[184:187], v[116:119]\n\t"
    "v_fmac_f32 v120, v105, v9\n\t"
    "v_fmac_f32 v121, v106, v10\n\t"
    "ds_read_b128 v[156:159], v128 offset:0\n\t"
    "ds_read_b128 v[160:163], v128 offset:64\n\t"
    "v_mfma_f32_16x16x32_bf16 v[92:95], v[180:183], v[180:183], 0\n\t"
    "v_fmac_f32 v120, v107, v11\n\t"
    "v_mad_u32_u24 v129, v129, s68, v151\n\t"
    "v_mfma_f32_16x16x32_bf16 v[92:95], v[184:187], v[184:187], v[92:95]\n\t"
    "v_and_b32 v129, -16, v129\n\t"
    "v_fmac_f32 v121, v108, v85\n\t"
    "v_mfma_f32_16x16x32_bf16 v[96:99], v[212:215], v[172:175], 0\n\t"
    "v_fmac_f32 v120, v112, v12\n\t"
    "v_fmac_f32 v121, v113, v13\n\t"
    "v_mfma_f32_16x16x32_bf16 v[96:99], v[216:219], v[176:179], v[96:99]\n\t"
    "v_fmac_f32 v120, v114, v14\n\t"
    "v_fmac_f32 v121, v115, v15\n\t"
    "s_lshl_b32 s51, s55, 8\n\t"
    "s_add_u32 s51, s51, s50\n\t"
    "s_waitcnt lgkmcnt(8)\n\t"
    "v_mfma_f32_16x16x32_bf16 v[100:103], v[164:167], v[188:191], 0\n\t"
    "v_mad_u32_u24 v130, v130, s68, v151\n\t"
    "v_and_b32 v130, -16, v130\n\t"
    "v_mfma_f32_16x16x32_bf16 v[100:103], v[168:171], v[192:195], v[100:103]\n\t"
    "v_fmac_f32 v120, v116, v16\n\t"
    "v_fmac_f32 v121, v117, v17\n\t"
    "buffer_load_dword v145, v146, %12, s51 offen sc1\n\t"
    "v_mfma_f32_16x16x32_bf16 v[104:107], v[172:175], v[188:191], 0\n\t"
    "v_fmac_f32 v120, v118, v18\n\t"
    "v_fmac_f32 v121, v119, v19\n\t"
    "v_mfma_f32_16x16x32_bf16 v[104:107], v[176:179], v[192:195], v[104:107]\n\t"
    "v_fmac_f32 v120, v92, v20\n\t"
    "v_fmac_f32 v121, v93, v21\n\t"
    "s_waitcnt lgkmcnt(6)\n\t"
    "v_mfma_f32_16x16x32_bf16 v[108:111], v[164:167], v[196:199], 0\n\t"
    "v_fmac_f32 v120, v94, v22\n\t"
    "v_fmac_f32 v121, v95, v23\n\t"
    "v_mfma_f32_16x16x32_bf16 v[108:111], v[168:171], v[200:203], v[108:111]\n\t"
    "v_fmac_f32 v120, v96, v86\n\t"
    "v_mad_u32_u24 v132, v132, s68, v151\n\t"
    "v_mfma_f32_16x16x32_bf16 v[112:115], v[212:215], v[180:183], 0\n\t"
    "v_fmac_f32 v121, v100, v24\n\t"
    "v_fmac_f32 v120, v101, v25\n\t"
    "v_mfma_f32_16x16x32_bf16 v[112:115], v[216:219], v[184:187], v[112:115]\n\t"
    "v_fmac_f32 v121, v102, v26\n\t"
    "v_fmac_f32 v120, v103, v27\n\t"
    "s_waitcnt lgkmcnt(3)\n\t"
    "v_mfma_f32_16x16x32_bf16 v[116:119], v[164:167], v[204:207], 0\n\t"
    "v_readfirstlane_b32 s60, v148\n\t"
    "v_and_b32 v132, -16, v132\n\t"
    "v_mfma_f32_16x16x32_bf16 v[116:119], v[168:171], v[208:211], v[116:119]\n\t"
    "v_fmac_f32 v121, v104, v28\n\t"
    "v_fmac_f32 v120, v105, v29\n\t"
    "ds_read_b128 v[164:167], v129 offset:0\n\t"
    "ds_read_b128 v[168:171], v129 offset:64\n\t"
    "v_mfma_f32_16x16x32_bf16 v[92:95], v[172:175], v[196:199], 0\n\t"
    "v_fmac_f32 v121, v106, v30\n\t"
    "v_fmac_f32 v120, v107, v31\n\t"
    "v_mfma_f32_16x16x32_bf16 v[92:95], v[176:179], v[200:203], v[92:95]\n\t"
    "v_fmac_f32 v121, v108, v32\n\t"
    "v_fmac_f32 v120, v109, v33\n\t"
    "v_mfma_f32_16x16x32_bf16 v[96:99], v[172:175], v[204:207], 0\n\t"
    "v_fmac_f32 v121, v110, v34\n\t"
    "v_fmac_f32 v120, v111, v35\n\t"
    "v_mfma_f32_16x16x32_bf16 v[96:99], v[176:179], v[208:211], v[96:99]\n\t"
    "v_fmac_f32 v121, v112, v87\n\t"
    "v_mad_u32_u24 v133, v133, s68, v151\n\t"
    "ds_read_b128 v[172:175], v130 offset:0\n\t"
    "ds_read_b128 v[176:179], v130 offset:64\n\t"
    "v_mfma_f32_16x16x32_bf16 v[100:103], v[212:215], v[188:191], 0\n\t"
    "v_fmac_f32 v120, v116, v36\n\t"
    "v_fmac_f32 v121, v117, v37\n\t"
    "v_mfma_f32_16x16x32_bf16 v[100:103], v[216:219], v[192:195], v[100:103]\n\t"
    "v_fmac_f32 v120, v118, v38\n\t"
    "v_fmac_f32 v121, v119, v39\n\t"
    "v_mfma_f32_16x16x32_bf16 v[104:107], v[180:183], v[188:191], 0\n\t"
    "v_and_b32 v133, -16, v133\n\t"
    "v_fmac_f32 v120, v92, v40\n\t"
    "v_mfma_f32_16x16x32_bf16 v[104:107], v[184:187], v[192:195], v[104:107]\n\t"
    "v_fmac_f32 v121, v93, v41\n\t"
    "v_fmac_f32 v120, v94, v42\n\t"
    "v_mfma_f32_16x16x32_bf16 v[108:111], v[180:183], v[196:199], 0\n\t"
    "v_fmac_f32 v121, v95, v43\n\t"
    "v_fmac_f32 v120, v96, v44\n\t"
    "v_mfma_f32_16x16x32_bf16 v[108:111], v[184:187], v[200:203], v[108:111]\n\t"
    "v_fmac_f32 v121, v97, v45\n\t"
    "v_fmac_f32 v120, v98, v46\n\t"
    "v_mfma_f32_16x16x32_bf16 v[112:115], v[180:183], v[204:207], 0\n\t"
    "v_fmac_f32 v121, v99, v47\n\t"
    "v_fmac_f32 v120, v100, v88\n\t"
    "v_mfma_f32_16x16x32_bf16 v[112:115], v[184:187], v[208:211], v[112:115]\n\t"
    "v_mad_u32_u24 v134, v134, s68, v151\n\t"
    "v_and_b32 v134, -16, v134\n\t"
    "ds_read_b128 v[180:183], v132 offset:0\n\t"
    "ds_read_b128 v[184:187], v132 offset:64\n\t"
    "v_mfma_f32_16x16x32_bf16 v[116:119], v[212:215], v[196:199], 0\n\t"
    "v_fmac_f32 v121, v104, v48\n\t"
    "v_fmac_f32 v120, v105, v49\n\t"
    "v_mfma_f32_16x16x32_bf16 v[116:119], v[216:219], v[200:203], v[116:119]\n\t"
    "v_fmac_f32 v121, v106, v50\n\t"
    "v_fmac_f32 v120, v107, v51\n\t"
    "v_mfma_f32_16x16x32_bf16 v[92:95], v[188:191], v[188:191], 0\n\t"
    "v_mad_u32_u24 v135, v135, s68, v151\n\t"
    "v_and_b32 v135, -16, v135\n\t"
    "v_mfma_f32_16x16x32_bf16 v[92:95], v[192:195], v[192:195], v[92:95]\n\t"
    "v_fmac_f32 v121, v108, v52\n\t"
    "v_fmac_f32 v120, v109, v53\n\t"
    "v_mfma_f32_16x16x32_bf16 v[96:99], v[188:191], v[196:199], 0\n\t"
    "v_fmac_f32 v121, v110, v54\n\t"
    "v_fmac_f32 v120, v111, v55\n\t"
    "v_mfma_f32_16x16x32_bf16 v[96:99], v[192:195], v[200:203], v[96:99]\n\t"
    "v_fmac_f32 v121, v112, v56\n\t"
    "v_fmac_f32 v120, v113, v57\n\t"
    "v_mfma_f32_16x16x32_bf16 v[100:103], v[188:191], v[204:207], 0\n\t"
    "v_fmac_f32 v121, v114, v58\n\t"
    "v_fmac_f32 v120, v115, v59\n\t"
    "v_mfma_f32_16x16x32_bf16 v[100:103], v[192:195], v[208:211], v[100:103]\n\t"
    "v_fmac_f32 v121, v116, v89\n\t"
    "ds_read_b128 v[188:191], v133 offset:0\n\t"
    "ds_read_b128 v[192:195], v133 offset:64\n\t"
    "v_mfma_f32_16x16x32_bf16 v[104:107], v[212:215], v[204:207], 0\n\t"
    "v_fmac_f32 v120, v92, v60\n\t"
    "v_fmac_f32 v121, v93, v61\n\t"
    "v_mfma_f32_16x16x32_bf16 v[104:107], v[216:219], v[208:211], v[104:107]\n\t"
    "v_fmac_f32 v120, v94, v62\n\t"
    "v_fmac_f32 v121, v95, v63\n\t"
    "v_mfma_f32_16x16x32_bf16 v[108:111], v[196:199], v[196:199], 0\n\t"
    "v_fmac_f32 v120, v96, v64\n\t"
    "v_fmac_f32 v121, v97, v65\n\t"
    "v_mfma_f32_16x16x32_bf16 v[108:111], v[200:203], v[200:203], v[108:111]\n\t"
    "v_fmac_f32 v120, v98, v66\n\t"
    "v_fmac_f32 v121, v99, v67\n\t"
    "v_mfma_f32_16x16x32_bf16 v[112:115], v[196:199], v[204:207], 0\n\t"
    "v_fmac_f32 v120, v100, v68\n\t"
    "v_fmac_f32 v121, v101, v69\n\t"
    "v_mfma_f32_16x16x32_bf16 v[112:115], v[200:203], v[208:211], v[112:115]\n\t"
    "v_fmac_f32 v120, v102, v70\n\t"
    "v_fmac_f32 v121, v103, v71\n\t"
    "ds_read_b128 v[196:199], v134 offset:0\n\t"
    "ds_read_b128 v[200:203], v134 offset:64\n\t"
    "v_mfma_f32_16x16x32_bf16 v[116:119], v[204:207], v[204:207], 0\n\t"
    "v_fmac_f32 v120, v104, v90\n\t"
    "v_mfma_f32_16x16x32_bf16 v[116:119], v[208:211], v[208:211], v[116:119]\n\t"
    "ds_read_b128 v[204:207], v135 offset:0\n\t"
    "ds_read_b128 v[208:211], v135 offset:64\n\t"
    "s_mov_b32 s52, s53\n\t"
    "s_mov_b32 s53, s54\n\t"
    "s_mov_b32 s54, s55\n\t"
    "s_mov_b32 s55, s60\n\t"
    "s_cmp_lt_u32 s54, s64\n\t"
    "s_cbranch_scc1 L_pr1_%=\n\t"
    "s_setprio 0\n\t"
    "L_pr1_%=:\n\t"
    "s_cmp_lt_u32 s53, s64\n\t"
    "s_cbranch_scc1 L_pnn_%=\n\t"
    "L_done_%=:\n\t"
    "s_mul_i32 s69, s52, 272\n\t"
    "s_add_u32 s69, s69, 130048\n\t"
    "v_add_u32 v147, s69, v146\n\t"
    "v_fmac_f32 v121, v108, v72\n\t"
    "v_fmac_f32 v120, v109, v73\n\t"
    "v_fmac_f32 v121, v110, v74\n\t"
    "v_fmac_f32 v120, v111, v75\n\t"
    "v_fmac_f32 v121, v112, v76\n\t"
    "v_fmac_f32 v120, v113, v77\n\t"
    "v_fmac_f32 v121, v114, v78\n\t"
    "v_fmac_f32 v120, v115, v79\n\t"
    "v_fmac_f32 v121, v116, v80\n\t"
    "v_fmac_f32 v120, v117, v81\n\t"
    "v_fmac_f32 v121, v118, v82\n\t"
    "v_fmac_f32 v120, v119, v83\n\t"
    "v_add_f32 v154, v120, v121\n\t"
    "ds_write_b32 v147, v154\n\t"
    "v_lshrrev_b32 v147, 2, v146\n\t"
    "s_mov_b64 s[92:93], %17\n\t"
    "s_and_b32 s93, s93, 0xffff\n\t"
    "s_mov_b32 s94, 32768\n\t"
    "s_mov_b32 s95, 0x20000\n\t"
    "s_lshl_b32 s44, s37, 1\n\t"
    "v_and_b32 v155, 15, v147\n\t"
    "v_lshl_add_u32 v155, %16, 4, v155\n\t"
    "v_lshrrev_b32 v154, 4, v147\n\t"
    "v_mul_u32_u24 v122, 272, v155\n\t"
    "v_lshl_add_u32 v122, v154, 6, v122\n\t"
    "v_add_u32 v122, 130048, v122\n\t"
    "v_mul_u32_u24 v154, 2560, v154\n\t"
    "v_lshl_add_u32 v154, v155, 2, v154\n\t"
    "v_add_u32 v154, 151808, v154\n\t"
    "v_cmp_le_u32 vcc, 48, v147\n\t"
    "v_cmp_gt_u32 s[46:47], s38, v155\n\t"
    "v_lshlrev_b32 v155, 1, v155\n\t"
    "s_and_b64 vcc, vcc, s[46:47]\n\t"
    "s_waitcnt lgkmcnt(0)\n\t"
    "s_barrier\n\t"
    "s_cbranch_vccz L_fin_%=\n\t"
    "ds_read_b128 v[92:95], v122 offset:0\n\t"
    "ds_read_b128 v[96:99], v122 offset:16\n\t"
    "ds_read_b128 v[100:103], v122 offset:32\n\t"
    "ds_read_b128 v[104:107], v122 offset:48\n\t"
    "ds_read2_b32 v[108:109], v154 offset0:0 offset1:80\n\t"
    "ds_read2_b32 v[110:111], v154 offset0:160 offset1:240\n\t"
    "v_add_u32 v154, 1280, v154\n\t"
    "ds_read2_b32 v[112:113], v154 offset0:0 offset1:80\n\t"
    "ds_read2_b32 v[114:115], v154 offset0:160 offset1:240\n\t"
    "s_waitcnt lgkmcnt(6)\n\t"
    "v_add_f32 v92, v92, v96\n\t"
    "v_add_f32 v93, v93, v97\n\t"
    "v_add_f32 v94, v94, v98\n\t"
    "v_add_f32 v95, v95, v99\n\t"
    "s_waitcnt lgkmcnt(5)\n\t"
    "v_add_f32 v92, v92, v100\n\t"
    "v_add_f32 v93, v93, v101\n\t"
    "v_add_f32 v94, v94, v102\n\t"
    "v_add_f32 v95, v95, v103\n\t"
    "s_waitcnt lgkmcnt(4)\n\t"
    "v_add_f32 v92, v92, v104\n\t"
    "v_add_f32 v93, v93, v105\n\t"
    "v_add_f32 v94, v94, v106\n\t"
    "v_add_f32 v95, v95, v107\n\t"
    "s_waitcnt lgkmcnt(2)\n\t"
    "v_add_f32 v92, v92, v108\n\t"
    "v_add_f32 v93, v93, v109\n\t"
    "v_add_f32 v94, v94, v110\n\t"
    "v_add_f32 v95, v95, v111\n\t"
    "s_waitcnt lgkmcnt(0)\n\t"
    "v_add_f32 v92, v92, v112\n\t"
    "v_add_f32 v93, v93, v113\n\t"
    "v_add_f32 v94, v94, v114\n\t"
    "v_add_f32 v95, v95, v115\n\t"
    "v_add_f32 v92, v92, v93\n\t"
    "v_add_f32 v94, v94, v95\n\t"
    "v_add_f32 v92, v92, v94\n\t"
    "v_mov_b32 v93, v92\n\t"
    "s_nop 1\n\t"
    "v_permlane16_swap_b32 v93, v92\n\t"
    "s_nop 1\n\t"
    "v_add_f32 v92, v92, v93\n\t"
    "v_mov_b32 v93, v92\n\t"
    "s_nop 1\n\t"
    "v_permlane32_swap_b32 v93, v92\n\t"
    "s_nop 1\n\t"
    "v_add_f32 v92, v92, v93\n\t"
    "v_add_f32 v92, s39, v92\n\t"
    "v_cvt_pk_bf16_f32 v92, v92, v92\n\t"
    "s_mov_b64 exec, vcc\n\t"
    "buffer_store_short v92, v155, s[92:95], s44 offen\n\t"
    "s_mov_b64 exec, -1\n\t"
    "s_branch L_fin_%=\n\t"
    "L_req0_%=:\n\t"
    "s_cmp_lg_u32 s61, 0\n\t"
    "s_cbranch_scc1 L_req0b_%=\n\t"
    "s_bfe_u32 s65, s84, 0xc0002\n\t"
    "s_add_u32 s65, s65, s36\n\t"
    "buffer_store_dword v236, v125, %12, s65 offen offset:0 sc1\n\t"
    "buffer_store_dword v237, v125, %12, s65 offen offset:256 sc1\n\t"
    "s_bfe_u32 s65, s85, 0xc0002\n\t"
    "s_add_u32 s65, s65, s36\n\t"
    "buffer_store_dword v238, v125, %12, s65 offen offset:0 sc1\n\t"
    "buffer_store_dword v239, v125, %12, s65 offen offset:256 sc1\n\t"
    "s_bfe_u32 s65, s86, 0xc0002\n\t"
    "s_add_u32 s65, s65, s36\n\t"
    "buffer_store_dword v240, v125, %12, s65 offen offset:0 sc1\n\t"
    "buffer_store_dword v241, v125, %12, s65 offen offset:256 sc1\n\t"
    "s_bfe_u32 s65, s87, 0xc0002\n\t"
    "s_add_u32 s65, s65, s36\n\t"
    "buffer_store_dword v242, v125, %12, s65 offen offset:0 sc1\n\t"
    "buffer_store_dword v243, v125, %12, s65 offen offset:256 sc1\n\t"
    "s_bfe_u32 s65, s88, 0xc0002\n\t"
    "s_add_u32 s65, s65, s36\n\t"
    "buffer_store_dword v244, v125, %12, s65 offen offset:0 sc1\n\t"
    "buffer_store_dword v245, v125, %12, s65 offen offset:256 sc1\n\t"
    "s_bfe_u32 s65, s89, 0xc0002\n\t"
    "s_add_u32 s65, s65, s36\n\t"
    "buffer_store_dword v246, v125, %12, s65 offen offset:0 sc1\n\t"
    "buffer_store_dword v247, v125, %12, s65 offen offset:256 sc1\n\t"
    "s_bfe_u32 s65, s90, 0xc0002\n\t"
    "s_add_u32 s65, s65, s36\n\t"
    "buffer_store_dword v248, v125, %12, s65 offen offset:0 sc1\n\t"
    "buffer_store_dword v249, v125, %12, s65 offen offset:256 sc1\n\t"
    "s_bfe_u32 s65, s91, 0xc0002\n\t"
    "s_add_u32 s65, s65, s36\n\t"
    "buffer_store_dword v250, v125, %12, s65 offen offset:0 sc1\n\t"
    "buffer_store_dword v251, v125, %12, s65 offen offset:256 sc1\n\t"
    "s_sub_u32 s61, s64, 3\n\t"
    "s_branch L_back0_%=\n\t"
    "L_req0b_%=:\n\t"
    "s_cmp_lg_u32 s61, 1\n\t"
    "s_cbranch_scc1 L_req0c_%=\n\t"
    "v_lshrrev_b32 v154, 16, v122\n\t"
    "v_lshl_add_u32 v154, %16, 2, v154\n\t"
    "v_mul_u32_u24 v154, 320, v154\n\t"
    "v_and_b32 v122, 0xf0, v122\n\t"
    "v_cmp_gt_u32 vcc, 64, v122\n\t"
    "v_add3_u32 v122, v122, v154, s45\n\t"
    "s_mov_b32 s61, -1\n\t"
    "v_add_f32 v124, v220, v224\n\t"
    "v_add_f32 v154, v228, v232\n\t"
    "v_add_f32 v124, v124, v154\n\t"
    "v_add_f32 v125, v221, v225\n\t"
    "v_add_f32 v154, v229, v233\n\t"
    "v_add_f32 v125, v125, v154\n\t"
    "v_add_f32 v126, v222, v226\n\t"
    "v_add_f32 v154, v230, v234\n\t"
    "v_add_f32 v126, v126, v154\n\t"
    "v_add_f32 v127, v223, v227\n\t"
    "v_add_f32 v154, v231, v235\n\t"
    "v_add_f32 v127, v127, v154\n\t"
    "ds_write_b128 v122, v[124:127] offset:0\n\t"
    "s_cmp_le_u32 s38, 64\n\t"
    "s_cbranch_scc1 L_back0_%=\n\t"
    "v_add_f32 v124, v236, v240\n\t"
    "v_add_f32 v154, v244, v248\n\t"
    "v_add_f32 v124, v124, v154\n\t"
    "v_add_f32 v125, v237, v241\n\t"
    "v_add_f32 v154, v245, v249\n\t"
    "v_add_f32 v125, v125, v154\n\t"
    "v_add_f32 v126, v238, v242\n\t"
    "v_add_f32 v154, v246, v250\n\t"
    "v_add_f32 v126, v126, v154\n\t"
    "v_add_f32 v127, v239, v243\n\t"
    "v_add_f32 v154, v247, v251\n\t"
    "v_add_f32 v127, v127, v154\n\t"
    "s_and_saveexec_b64 s[46:47], vcc\n\t"
    "ds_write_b128 v122, v[124:127] offset:256\n\t"
    "s_mov_b64 exec, s[46:47]\n\t"
    "s_branch L_back0_%=\n\t"
    "L_req0c_%=:\n\t"
    "s_lshl_b32 s46, %14, 7\n\t"
    "s_add_u32 s46, s46, 48245760\n\t"
    "s_mov_b32 s47, 1048576\n\t"
    "s_mov_b64 exec, 1\n\t"
    "s_cmp_ge_u32 s55, s64\n\t"
    "s_cbranch_scc1 L_pq0_%=\n\t"
    "buffer_load_dword v123, off, %12, s46 sc1\n\t"
    "s_mov_b64 exec, -1\n\t"
    "s_branch L_back0_%=\n\t"
    "L_pq0_%=:\n\t"
    "v_cmp_eq_u32 vcc, 0, v123\n\t"
    "s_cbranch_vccz L_in0_%=\n\t"
    "L_poll0_%=:\n\t"
    "buffer_load_dword v155, off, %12, s46 sc1\n\t"
    "s_sub_u32 s47, s47, 1\n\t"
    "s_cmp_eq_u32 s47, 0\n\t"
    "s_cbranch_scc1 L_in0_%=\n\t"
    "s_waitcnt vmcnt(0)\n\t"
    "v_cmp_eq_u32 vcc, 0, v155\n\t"
    "s_cbranch_vccz L_in0_%=\n\t"
    "s_sleep 8\n\t"
    "s_branch L_poll0_%=\n\t"
    "L_in0_%=:\n\t"
    "s_mov_b64 exec, -1\n\t"
    "s_lshl_b32 s46, s37, 2\n\t"
    "s_lshl_b32 s47, %16, 20\n\t"
    "s_add_u32 s46, s46, s47\n\t"
    "s_add_u32 s46, s46, 17825792\n\t"
    "v_and_b32 v155, 0xc0, v122\n\t"
    "v_lshl_add_u32 v155, v155, 24, v122\n\t"
    "buffer_load_dwordx4 v[220:223], v122, %12, s46 offen sc1\n\t"
    "s_add_u32 s46, s46, 0x40000\n\t"
    "buffer_load_dwordx4 v[224:227], v122, %12, s46 offen sc1\n\t"
    "s_add_u32 s46, s46, 0x40000\n\t"
    "buffer_load_dwordx4 v[228:231], v122, %12, s46 offen sc1\n\t"
    "s_add_u32 s46, s46, 0x40000\n\t"
    "buffer_load_dwordx4 v[232:235], v122, %12, s46 offen sc1\n\t"
    "s_add_u32 s46, s46, 0x40000\n\t"
    "s_mov_b32 s61, 1\n\t"
    "s_cmp_le_u32 s38, 64\n\t"
    "s_cbranch_scc1 L_back0_%=\n\t"
    "s_sub_u32 s46, s46, 0x40000\n\t"
    "buffer_load_dwordx4 v[248:251], v155, %12, s46 offen offset:256 sc1\n\t"
    "s_sub_u32 s46, s46, 0x40000\n\t"
    "buffer_load_dwordx4 v[244:247], v155, %12, s46 offen offset:256 sc1\n\t"
    "s_sub_u32 s46, s46, 0x40000\n\t"
    "buffer_load_dwordx4 v[240:243], v155, %12, s46 offen offset:256 sc1\n\t"
    "s_sub_u32 s46, s46, 0x40000\n\t"
    "buffer_load_dwordx4 v[236:239], v155, %12, s46 offen offset:256 sc1\n\t"
    "s_branch L_back0_%=\n\t"
    "L_req1_%=:\n\t"
    "s_cmp_lg_u32 s61, 1\n\t"
    "s_cbranch_scc1 L_req1c_%=\n\t"
    "v_lshrrev_b32 v154, 16, v122\n\t"
    "v_lshl_add_u32 v154, %16, 2, v154\n\t"
    "v_mul_u32_u24 v154, 320, v154\n\t"
    "v_and_b32 v122, 0xf0, v122\n\t"
    "v_cmp_gt_u32 vcc, 64, v122\n\t"
    "v_add3_u32 v122, v122, v154, s45\n\t"
    "s_mov_b32 s61, -1\n\t"
    "v_add_f32 v124, v220, v224\n\t"
    "v_add_f32 v154, v228, v232\n\t"
    "v_add_f32 v124, v124, v154\n\t"
    "v_add_f32 v125, v221, v225\n\t"
    "v_add_f32 v154, v229, v233\n\t"
    "v_add_f32 v125, v125, v154\n\t"
    "v_add_f32 v126, v222, v226\n\t"
    "v_add_f32 v154, v230, v234\n\t"
    "v_add_f32 v126, v126, v154\n\t"
    "v_add_f32 v127, v223, v227\n\t"
    "v_add_f32 v154, v231, v235\n\t"
    "v_add_f32 v127, v127, v154\n\t"
    "ds_write_b128 v122, v[124:127] offset:0\n\t"
    "s_cmp_le_u32 s38, 64\n\t"
    "s_cbranch_scc1 L_back1_%=\n\t"
    "v_add_f32 v124, v236, v240\n\t"
    "v_add_f32 v154, v244, v248\n\t"
    "v_add_f32 v124, v124, v154\n\t"
    "v_add_f32 v125, v237, v241\n\t"
    "v_add_f32 v154, v245, v249\n\t"
    "v_add_f32 v125, v125, v154\n\t"
    "v_add_f32 v126, v238, v242\n\t"
    "v_add_f32 v154, v246, v250\n\t"
    "v_add_f32 v126, v126, v154\n\t"
    "v_add_f32 v127, v239, v243\n\t"
    "v_add_f32 v154, v247, v251\n\t"
    "v_add_f32 v127, v127, v154\n\t"
    "s_and_saveexec_b64 s[46:47], vcc\n\t"
    "ds_write_b128 v122, v[124:127] offset:256\n\t"
    "s_mov_b64 exec, s[46:47]\n\t"
    "s_branch L_back1_%=\n\t"
    "L_req1c_%=:\n\t"
    "s_lshl_b32 s46, %14, 7\n\t"
    "s_add_u32 s46, s46, 48245760\n\t"
    "s_mov_b32 s47, 1048576\n\t"
    "s_mov_b64 exec, 1\n\t"
    "s_cmp_ge_u32 s55, s64\n\t"
    "s_cbranch_scc1 L_pq1_%=\n\t"
    "buffer_load_dword v123, off, %12, s46 sc1\n\t"
    "s_mov_b64 exec, -1\n\t"
    "s_branch L_back1_%=\n\t"
    "L_pq1_%=:\n\t"
    "v_cmp_eq_u32 vcc, 0, v123\n\t"
    "s_cbranch_vccz L_in1_%=\n\t"
    "L_poll1_%=:\n\t"
    "buffer_load_dword v155, off, %12, s46 sc1\n\t"
    "s_sub_u32 s47, s47, 1\n\t"
    "s_cmp_eq_u32 s47, 0\n\t"
    "s_cbranch_scc1 L_in1_%=\n\t"
    "s_waitcnt vmcnt(0)\n\t"
    "v_cmp_eq_u32 vcc, 0, v155\n\t"
    "s_cbranch_vccz L_in1_%=\n\t"
    "s_sleep 8\n\t"
    "s_branch L_poll1_%=\n\t"
    "L_in1_%=:\n\t"
    "s_mov_b64 exec, -1\n\t"
    "s_lshl_b32 s46, s37, 2\n\t"
    "s_lshl_b32 s47, %16, 20\n\t"
    "s_add_u32 s46, s46, s47\n\t"
    "s_add_u32 s46, s46, 17825792\n\t"
    "v_and_b32 v155, 0xc0, v122\n\t"
    "v_lshl_add_u32 v155, v155, 24, v122\n\t"
    "buffer_load_dwordx4 v[220:223], v122, %12, s46 offen sc1\n\t"
    "s_add_u32 s46, s46, 0x40000\n\t"
    "buffer_load_dwordx4 v[224:227], v122, %12, s46 offen sc1\n\t"
    "s_add_u32 s46, s46, 0x40000\n\t"
    "buffer_load_dwordx4 v[228:231], v122, %12, s46 offen sc1\n\t"
    "s_add_u32 s46, s46, 0x40000\n\t"
    "buffer_load_dwordx4 v[232:235], v122, %12, s46 offen sc1\n\t"
    "s_add_u32 s46, s46, 0x40000\n\t"
    "s_mov_b32 s61, 1\n\t"
    "s_cmp_le_u32 s38, 64\n\t"
    "s_cbranch_scc1 L_back1_%=\n\t"
    "s_sub_u32 s46, s46, 0x40000\n\t"
    "buffer_load_dwordx4 v[248:251], v155, %12, s46 offen offset:256 sc1\n\t"
    "s_sub_u32 s46, s46, 0x40000\n\t"
    "buffer_load_dwordx4 v[244:247], v155, %12, s46 offen offset:256 sc1\n\t"
    "s_sub_u32 s46, s46, 0x40000\n\t"
    "buffer_load_dwordx4 v[240:243], v155, %12, s46 offen offset:256 sc1\n\t"
    "s_sub_u32 s46, s46, 0x40000\n\t"
    "buffer_load_dwordx4 v[236:239], v155, %12, s46 offen offset:256 sc1\n\t"
    "s_branch L_back1_%=\n\t"
    "L_sig_%=:\n\t"
    "v_subrev_u32 v155, s49, v149\n\t"
    "v_add_u32 v155, 1024, v155\n\t"
    "s_waitcnt vmcnt(0)\n\t"
    "ds_add_rtn_u32 v148, v155, v150\n\t"
    "s_mov_b32 s45, 151808\n\t"
    "s_mov_b32 s46, 48234496\n\t"
    "v_mov_b32 v155, 1\n\t"
    "s_waitcnt lgkmcnt(0)\n\t"
    "v_readfirstlane_b32 s47, v148\n\t"
    "s_nop 0\n\t"
    "s_cmp_lg_u32 s47, 7\n\t"
    "s_cbranch_scc1 L_sigb_%=\n\t"
    "s_mov_b64 exec, 1\n\t"
    "buffer_atomic_add v155, off, %12, s46 sc0 sc1\n\t"
    "s_mov_b64 exec, -1\n\t"
    "s_waitcnt vmcnt(0)\n\t"
    "v_readfirstlane_b32 s47, v155\n\t"
    "v_lshlrev_b32 v155, 5, v146\n\t"
    "s_cmp_lg_u32 s47, 255\n\t"
    "s_cbranch_scc1 L_sigb_%=\n\t"
    "s_mov_b32 s46, 48245760\n\t"
    "buffer_store_dword v150, v155, %12, s46 offen sc1\n\t"
    "s_add_u32 s46, s46, 0x2000\n\t"
    "buffer_store_dword v150, v155, %12, s46 offen sc1\n\t"
    "s_add_u32 s46, s46, 0x2000\n\t"
    "buffer_store_dword v150, v155, %12, s46 offen sc1\n\t"
    "s_add_u32 s46, s46, 0x2000\n\t"
    "buffer_store_dword v150, v155, %12, s46 offen sc1\n\t"
    "s_add_u32 s46, s46, 0x2000\n\t"
    "s_branch L_sigb_%=\n\t"
    "L_fin_%=:\n\t"
    "s_branch L_pend_%=\n\t"
    "L_pub_%=:\n\t"
    "s_memrealtime s[46:47]\n\t"
    "s_lshl_b32 s49, %14, 2\n\t"
    "s_add_u32 s49, s49, 48244736\n\t"
    "s_waitcnt lgkmcnt(0)\n\t"
    "s_sub_u32 s47, s46, s44\n\t"
    "s_mul_i32 s47, s47, 236\n\t"
    "s_lshr_b32 s47, s47, 8\n\t"
    "s_add_u32 s46, s46, s47\n\t"
    "s_or_b32 s46, s46, 1\n\t"
    "v_mov_b32 v83, s46\n\t"
    "s_mov_b64 exec, 1\n\t"
    "buffer_store_dword v83, off, %12, s49 sc1\n\t"
    "s_mov_b64 exec, -1\n\t"
    "s_branch L_pubb_%=\n\t"
    "L_xw0_%=:\n\t"
    "s_mov_b32 s68, 1048576\n\t"
    "L_xv0_%=:\n\t"
    "s_sleep 4\n\t"
    "s_mov_b64 exec, 1\n\t"
    "buffer_load_dword v164, off, %12, s93 offset:0 sc1\n\t"
    "s_mov_b64 exec, -1\n\t"
    "s_sub_u32 s68, s68, 1\n\t"
    "s_cmp_eq_u32 s68, 0\n\t"
    "s_cbranch_scc1 L_xwb0_%=\n\t"
    "s_waitcnt vmcnt(0)\n\t"
    "v_readfirstlane_b32 s65, v164\n\t"
    "s_nop 0\n\t"
    "s_cmp_eq_u32 s65, 0\n\t"
    "s_cbranch_scc1 L_xv0_%=\n\t"
    "s_branch L_xwb0_%=\n\t"
    "L_xw1_%=:\n\t"
    "s_mov_b32 s68, 1048576\n\t"
    "L_xv1_%=:\n\t"
    "s_sleep 4\n\t"
    "s_mov_b64 exec, 1\n\t"
    "buffer_load_dword v164, off, %12, s93 offset:128 sc1\n\t"
    "s_mov_b64 exec, -1\n\t"
    "s_sub_u32 s68, s68, 1\n\t"
    "s_cmp_eq_u32 s68, 0\n\t"
    "s_cbranch_scc1 L_xwb1_%=\n\t"
    "s_waitcnt vmcnt(0)\n\t"
    "v_readfirstlane_b32 s65, v164\n\t"
    "s_nop 0\n\t"
    "s_cmp_eq_u32 s65, 0\n\t"
    "s_cbranch_scc1 L_xv1_%=\n\t"
    "s_branch L_xwb1_%=\n\t"
    "L_xw2_%=:\n\t"
    "s_mov_b32 s68, 1048576\n\t"
    "L_xv2_%=:\n\t"
    "s_sleep 4\n\t"
    "s_mov_b64 exec, 1\n\t"
    "buffer_load_dword v164, off, %12, s93 offset:256 sc1\n\t"
    "s_mov_b64 exec, -1\n\t"
    "s_sub_u32 s68, s68, 1\n\t"
    "s_cmp_eq_u32 s68, 0\n\t"
    "s_cbranch_scc1 L_xwb2_%=\n\t"
    "s_waitcnt vmcnt(0)\n\t"
    "v_readfirstlane_b32 s65, v164\n\t"
    "s_nop 0\n\t"
    "s_cmp_eq_u32 s65, 0\n\t"
    "s_cbranch_scc1 L_xv2_%=\n\t"
    "s_branch L_xwb2_%=\n\t"
    "L_xw3_%=:\n\t"
    "s_mov_b32 s68, 1048576\n\t"
    "L_xv3_%=:\n\t"
    "s_sleep 4\n\t"
    "s_mov_b64 exec, 1\n\t"
    "buffer_load_dword v164, off, %12, s93 offset:384 sc1\n\t"
    "s_mov_b64 exec, -1\n\t"
    "s_sub_u32 s68, s68, 1\n\t"
    "s_cmp_eq_u32 s68, 0\n\t"
    "s_cbranch_scc1 L_xwb3_%=\n\t"
    "s_waitcnt vmcnt(0)\n\t"
    "v_readfirstlane_b32 s65, v164\n\t"
    "s_nop 0\n\t"
    "s_cmp_eq_u32 s65, 0\n\t"
    "s_cbranch_scc1 L_xv3_%=\n\t"
    "s_branch L_xwb3_%=\n\t"
    "L_mp2_%=:\n\t"
    "v_lshrrev_b32 v57, 4, v127\n\t"
    "v_add_u32 v57, 32, v57\n\t"
    "s_and_b32 s65, %14, 7\n\t"
    "s_add_u32 s65, s65, 24\n\t"
    "v_writelane_b32 v57, s65, 16\n\t"
    "v_mov_b32 v58, 16\n\t"
    "v_lshlrev_b32 v57, 7, v57\n\t"
    "v_writelane_b32 v58, 32, 16\n\t"
    "s_mov_b32 s65, 48234496\n\t"
    "v_mov_b32 v56, 32\n\t"
    "s_mov_b32 s66, 1048576\n\t"
    "s_mov_b32 s65, 48234496\n\t"
    "s_mov_b64 exec, 0x1ffff\n\t"
    "buffer_load_dword v56, v57, %12, s65 offen sc1\n\t"
    "s_mov_b64 exec, -1\n\t"
    "s_branch L_mp2b_%=\n\t"
    "L_mp3_%=:\n\t"
    "v_cmp_gt_u32 vcc, v58, v56\n\t"
    "s_cbranch_vccz L_mp3b_%=\n\t"
    "s_sub_u32 s66, s66, 1\n\t"
    "s_cmp_eq_u32 s66, 0\n\t"
    "s_cbranch_scc1 L_mp3b_%=\n\t"
    "s_sleep 8\n\t"
    "s_mov_b32 s65, 48234496\n\t"
    "s_mov_b64 exec, 0x1ffff\n\t"
    "buffer_load_dword v56, v57, %12, s65 offen sc1\n\t"
    "s_mov_b64 exec, -1\n\t"
    "s_waitcnt vmcnt(0)\n\t"
    "s_branch L_mp3_%=\n\t"
    "L_pend_%=:\n\t"
    ".Ldcp_end:\n\t"
    :: "s"(s_idx), "s"(p_wp), "s"(p_w1), "n"(0), "n"(0), "n"(0), "s"(pb1), "s"(pw2), "s"(pb2), "s"(b2s),
       "n"(0),
       "n"(0), "s"(s_ws), "n"(0), "s"(bid), "s"(s_tab), "s"(w), "s"(p_out),
       "s"((unsigned)(unsigned long long)idx), "s"((unsigned)((unsigned long long)idx >> 32) & 0xFFFFu)
    : "memory", "vcc", "scc", "m0", "s36", "s37", "s38", "s39", "s40", "s41", "s42", "s43", "s44", "s45", "s46", "s47", "s48", "s49", "s50", "s51", "s52", "s53", "s54", "s55", "s56", "s57", "s58", "s59", "s60", "s61", "s62", "s63", "s64", "s65", "s66", "s67", "s68", "s69", "s70", "s71", "s72", "s73", "s74", "s75", "s76", "s77", "s78", "s79", "s80", "s81", "s82", "s83", "s84", "s85", "s86", "s87", "s88", "s89", "s90", "s91", "s92", "s93", "s94", "s95", "s96", "s97", "s98", "s99", "v0", "v1", "v2", "v3", "v4", "v5", "v6", "v7", "v8", "v9", "v10", "v11", "v12", "v13", "v14", "v15", "v16", "v17", "v18", "v19", "v20", "v21", "v22", "v23", "v24", "v25", "v26", "v27", "v28", "v29", "v30", "v31", "v32", "v33", "v34", "v35", "v36", "v37", "v38", "v39", "v40", "v41", "v42", "v43", "v44", "v45", "v46", "v47", "v48", "v49", "v50", "v51", "v52", "v53", "v54", "v55", "v56", "v57", "v58", "v59", "v60", "v61", "v62", "v63", "v64", "v65", "v66", "v67", "v68", "v69", "v70", "v71", "v72", "v73", "v74", "v75", "v76", "v77", "v78", "v79", "v80", "v81", "v82", "v83", "v84", "v85", "v86", "v87", "v88", "v89", "v90", "v91", "v92", "v93", "v94", "v95", "v96", "v97", "v98", "v99", "v100", "v101", "v102", "v103", "v104", "v105", "v106", "v107", "v108", "v109", "v110", "v111", "v112", "v113", "v114", "v115", "v116", "v117", "v118", "v119", "v120", "v121", "v122", "v123", "v124", "v125", "v126", "v127", "v128", "v129", "v130", "v131", "v132", "v133", "v134", "v135", "v136", "v137", "v138", "v139", "v140", "v141", "v142", "v143", "v144", "v145", "v146", "v147", "v148", "v149", "v150", "v151", "v152", "v153", "v154", "v155", "v156", "v157", "v158", "v159", "v160", "v161", "v162", "v163", "v164", "v165", "v166", "v167", "v168", "v169", "v170", "v171", "v172", "v173", "v174", "v175", "v176", "v177", "v178", "v179", "v180", "v181", "v182", "v183", "v184", "v185", "v186", "v187", "v188", "v189", "v190", "v191", "v192", "v193", "v194", "v195", "v196", "v197", "v198", "v199", "v200", "v201", "v202", "v203", "v204", "v205", "v206", "v207", "v208", "v209", "v210", "v211", "v212", "v213", "v214", "v215", "v216", "v217", "v218", "v219", "v220", "v221", "v222", "v223", "v224", "v225", "v226", "v227", "v228", "v229", "v230", "v231", "v232", "v233", "v234", "v235", "v236", "v237", "v238", "v239", "v240", "v241", "v242", "v243", "v244", "v245", "v246", "v247", "v248", "v249", "v250", "v251", "v252", "v253", "v254", "v255");
}

extern "C" void kernel_launch(void* const* d_in, const int* in_sizes, int n_in, void* d_out, int out_size, void* d_ws,
                              size_t ws_size, hipStream_t stream) {
  const int* idx = (const int*)d_in[0];
  const unsigned short* table = (const unsigned short*)d_in[1];
  const unsigned short* w_p = (const unsigned short*)d_in[2];
  const unsigned short* w1 = (const unsigned short*)d_in[3];
  const unsigned short* b1 = (const unsigned short*)d_in[4];
  const unsigned short* w2 = (const unsigned short*)d_in[5];
  const unsigned short* b2 = (const unsigned short*)d_in[6];
  char* ws = (char*)d_ws;
  dcp_all<<<dim3(256), dim3(512), 0, stream>>>(idx, table, w_p, w1, b1, w2, b2, ws, (__hip_bfloat16*)d_out);
}
```

```cpp
#include <hip/hip_runtime.h>
#include <hip/hip_bf16.h>
#include <stdint.h>

typedef __attribute__((ext_vector_type(2))) unsigned u32x2;

constexpr int BATCH = 16384, NF = 100, DIM = 128, VOC = 1000, HID = 512;
constexpr size_t NWIN = 13;
constexpr size_t WS_IDX = 0;
constexpr size_t IDX_STREAM = 212992;
constexpr size_t WS_W1F = 3407872;
constexpr size_t W1F_SLAB = 102400;
constexpr size_t WS_TF = 16515072;
constexpr size_t WS_WT = 16777216;
constexpr size_t WS_PART = 17825792;
constexpr size_t WS_P0 = 26214400;
constexpr size_t WS_XCH = 30408704;
constexpr size_t XCH_PITCH = 69632;
constexpr size_t WS_CNT = 48234496;
constexpr size_t WS_CH = 48242688;
constexpr size_t WS_RP = 48243712;
constexpr size_t WS_TS = 48244736;
constexpr size_t WS_RDY = 48245760;
constexpr size_t WS_DBG = 48278528;
constexpr size_t WS_END = 48671744;

__device__ __forceinline__ __amdgpu_buffer_rsrc_t mk_srd(const void* p, unsigned bytes) {
  return __builtin_amdgcn_make_buffer_rsrc((void*)p, (short)0, (int)bytes, 0x00020000);
}

__global__ __launch_bounds__(512, 2) void dcp_all(const int* __restrict__ idx, const unsigned short* __restrict__ table,
                                                  const unsigned short* __restrict__ w_p, const unsigned short* __restrict__ w1,
                                                  const unsigned short* __restrict__ b1, const unsigned short* __restrict__ w2,
                                                  const unsigned short* __restrict__ b2, char* __restrict__ ws,
                                                  __hip_bfloat16* __restrict__ out) {
  __shared__ __attribute__((aligned(1024))) char smem[163840];
  const int w = __builtin_amdgcn_readfirstlane(threadIdx.x >> 6) + (int)(uintptr_t)smem;
  const int bid = blockIdx.x;
  asm volatile(
    "s_getpc_b64 s[36:37]\n\t"
    ".Ldcp_pc:\n\t"
    "s_bfe_u32 s38, %0, 0x40003\n\t"
    "s_lshl_b32 s38, s38, 13\n\t"
    "v_and_b32 v122, 63, %1\n\t"
    "v_lshlrev_b32 v122, 7, v122\n\t"
    "v_add_u32 v122, s38, v122\n\t"
    "v_min_u32 v122, .Ldcp_end-.Ldcp_pc-4, v122\n\t"
    "s_cmp_lg_u32 %2, 3\n\t"
    "s_cbranch_scc1 L_nopf_%=\n\t"
    "global_load_dword v123, v122, s[36:37]\n\t"
    "L_nopf_%=:\n\t"
    :: "s"(bid), "v"(threadIdx.x), "s"(w) : "memory", "scc", "s36", "s37", "s38", "s74", "s75", "v122", "v123");
  const int xcd = bid & 7, loc = bid >> 3;
  const int cg = xcd * 16 + (loc >> 1);
  const __amdgpu_buffer_rsrc_t s_idx = mk_srd(idx + (size_t)bid * 64 * NF, (unsigned)((BATCH - bid * 64) * NF * 4));
  const __amdgpu_buffer_rsrc_t s_tab = mk_srd(table, VOC * DIM * 2);
  const __amdgpu_buffer_rsrc_t s_ws = mk_srd(ws, (unsigned)WS_END);
  const unsigned short* pb1 = b1 + 4 * cg;
  const unsigned short* pw2 = w2 + 4 * cg;
  const unsigned long long pb2 = (unsigned long long)b2 & ~3ull;
  const unsigned b2s = ((unsigned)(unsigned long long)b2 & 2u) * 8u;
  const unsigned long long p_wp = (unsigned long long)w_p & 0xFFFFFFFFFFFFull, p_w1 = (unsigned long long)w1 & 0xFFFFFFFFFFFFull;
  const unsigned long long p_out = (unsigned long long)out & 0xFFFFFFFFFFFFull;
  asm volatile(
    "v_mbcnt_lo_u32_b32 v147, -1, 0\n\t"
    "v_mbcnt_hi_u32_b32 v147, -1, v147\n\t"
    "s_mov_b64 s[92:93], %1\n\t"
    "s_mov_b32 s94, 20000\n\t"
    "s_mov_b32 s95, 0x20000\n\t"
    "s_lshr_b32 s46, %14, 3\n\t"
    "s_lshl_b32 s51, s46, 2\n\t"
    "v_lshlrev_b32 v122, 4, v147\n\t"
    "s_cmp_ge_u32 %16, 2\n\t"
    "s_cbranch_scc1 L_aimg_%=\n\t"
    "s_lshl_b32 s44, %16, 10\n\t"
    "s_add_u32 m0, s44, 140288\n\t"
    "s_nop 0\n\t"
    "buffer_load_dwordx4 v122, s[92:95], s44 offen lds\n\t"
    "s_add_u32 s44, s44, 0x800\n\t"
    "s_add_u32 m0, s44, 140288\n\t"
    "s_nop 0\n\t"
    "buffer_load_dwordx4 v122, s[92:95], s44 offen lds\n\t"
    "s_add_u32 s44, s44, 0x800\n\t"
    "s_add_u32 m0, s44, 140288\n\t"
    "s_nop 0\n\t"
    "buffer_load_dwordx4 v122, s[92:95], s44 offen lds\n\t"
    "s_add_u32 s44, s44, 0x800\n\t"
    "s_add_u32 m0, s44, 140288\n\t"
    "s_nop 0\n\t"
    "buffer_load_dwordx4 v122, s[92:95], s44 offen lds\n\t"
    "s_add_u32 s44, s44, 0x800\n\t"
    "s_add_u32 m0, s44, 140288\n\t"
    "s_nop 0\n\t"
    "buffer_load_dwordx4 v122, s[92:95], s44 offen lds\n\t"
    "s_add_u32 s44, s44, 0x800\n\t"
    "s_add_u32 m0, s44, 140288\n\t"
    "s_nop 0\n\t"
    "buffer_load_dwordx4 v122, s[92:95], s44 offen lds\n\t"
    "s_add_u32 s44, s44, 0x800\n\t"
    "s_add_u32 m0, s44, 140288\n\t"
    "s_nop 0\n\t"
    "buffer_load_dwordx4 v122, s[92:95], s44 offen lds\n\t"
    "s_add_u32 s44, s44, 0x800\n\t"
    "s_add_u32 m0, s44, 140288\n\t"
    "s_nop 0\n\t"
    "buffer_load_dwordx4 v122, s[92:95], s44 offen lds\n\t"
    "s_add_u32 s44, s44, 0x800\n\t"
    "s_add_u32 m0, s44, 140288\n\t"
    "s_nop 0\n\t"
    "buffer_load_dwordx4 v122, s[92:95], s44 offen lds\n\t"
    "s_add_u32 s44, s44, 0x800\n\t"
    "s_add_u32 m0, s44, 140288\n\t"
    "s_nop 0\n\t"
    "buffer_load_dwordx4 v122, s[92:95], s44 offen lds\n\t"
    "s_add_u32 s44, s44, 0x800\n\t"
    "s_mov_b32 s98, 13107200\n\t"
    "s_mov_b32 s99, 0x20000\n\t"
    "s_cmp_eq_u32 %16, 0\n\t"
    "s_cbranch_scc1 L_aside_%=\n\t"
    "s_branch L_ahelp_%=\n\t"
    "L_aimg_%=:\n\t"
    "s_cmp_lg_u32 %16, 2\n\t"
    "s_cbranch_scc1 L_anoinit_%=\n\t"
    "v_mov_b32 v0, 0\n\t"
    "v_mov_b32 v1, 0\n\t"
    "v_mov_b32 v2, 0\n\t"
    "v_mov_b32 v3, 0\n\t"
    "v_lshlrev_b32 v8, 4, v147\n\t"
    "v_add_u32 v8, 130048, v8\n\t"
    "v_mov_b32 v9, 1024\n\t"
    "ds_write_b128 v9, v[0:3]\n\t"
    "ds_write_b128 v8, v[0:3] offset:0\n\t"
    "ds_write_b128 v8, v[0:3] offset:1024\n\t"
    "ds_write_b128 v8, v[0:3] offset:2048\n\t"
    "ds_write_b32 v9, v0 offset:2084\n\t"
    "ds_write_b32 v9, v0 offset:2088\n\t"
    "ds_write_b32 v9, v0 offset:1044\n\t"
    "v_mov_b32 v4, 3\n\t"
    "ds_write_b32 v9, v4 offset:1040\n\t"
    "v_mov_b32 v5, 16\n\t"
    "ds_write_b32 v9, v5 offset:2080\n\t"
    "v_mov_b32 v6, 33\n\t"
    "ds_write_b32 v9, v6 offset:3120\n\t"
    "v_mov_b32 v7, 52\n\t"
    "ds_write_b32 v9, v7 offset:4160\n\t"
    "L_anoinit_%=:\n\t"
    "s_and_b32 s46, %16, 3\n\t"
    "s_mul_i32 s49, s46, 1040\n\t"
    "s_add_u32 s49, s49, 2064\n\t"
    "s_lshl_b32 s46, s46, 3\n\t"
    "s_lshr_b32 s47, 1076763402, s46\n\t"
    "s_and_b32 s47, s47, 0xff\n\t"
    "s_lshl_b32 s48, s47, 16\n\t"
    "s_lshr_b32 s47, 773523968, s46\n\t"
    "s_and_b32 s47, s47, 0xff\n\t"
    "s_add_u32 s48, s48, s47\n\t"
    "s_lshr_b32 s47, %16, 2\n\t"
    "s_add_u32 s47, s47, 0x200\n\t"
    "s_cmp_eq_u32 s46, 0\n\t"
    "s_cselect_b32 s47, 0x100, s47\n\t"
    "s_add_u32 s48, s48, s47\n\t"
    "v_lshrrev_b32 v126, 3, v147\n\t"
    "v_and_b32 v154, 7, v147\n\t"
    "v_lshlrev_b32 v126, 8, v126\n\t"
    "v_lshl_add_u32 v126, v154, 4, v126\n\t"
    "v_lshlrev_b32 v117, 20, v147\n\t"
    "v_add_u32 v117, 1024, v117\n\t"
    "v_mov_b32 v118, 1\n\t"
    "s_and_b32 s46, %16, 6\n\t"
    "s_cmp_eq_u32 s46, 4\n\t"
    "s_cbranch_scc1 L_adma_%=\n\t"
    "s_lshr_b32 s61, %16, 2\n\t"
    "s_lshl_b32 s61, s61, 1\n\t"
    "s_and_b32 s46, %16, 1\n\t"
    "s_add_u32 s61, s61, s46\n\t"
    "s_add_u32 s46, s61, 84\n\t"
    "s_add_u32 s69, s51, s46\n\t"
    "s_sub_u32 s44, s69, 125\n\t"
    "s_cmp_ge_u32 s69, 125\n\t"
    "s_cselect_b32 s69, s44, s69\n\t"
    "s_mul_i32 m0, s69, 1040\n\t"
    "s_lshl_b32 s44, s69, 11\n\t"
    "buffer_load_dwordx4 v126, %15, s44 offen lds\n\t"
    "s_add_u32 s69, s69, 4\n\t"
    "s_sub_u32 s44, s69, 125\n\t"
    "s_cmp_ge_u32 s69, 125\n\t"
    "s_cselect_b32 s69, s44, s69\n\t"
    "s_mul_i32 m0, s69, 1040\n\t"
    "s_lshl_b32 s44, s69, 11\n\t"
    "buffer_load_dwordx4 v126, %15, s44 offen lds\n\t"
    "s_add_u32 s69, s69, 4\n\t"
    "s_sub_u32 s44, s69, 125\n\t"
    "s_cmp_ge_u32 s69, 125\n\t"
    "s_cselect_b32 s69, s44, s69\n\t"
    "s_waitcnt lgkmcnt(0)\n\t"
    "s_barrier\n\t"
    "s_load_dwordx2 s[72:73], %6, 0\n\t"
    "s_load_dwordx2 s[88:89], %7, 0\n\t"
    "s_load_dword s91, %8, 0\n\t"
    "s_mul_i32 m0, s69, 1040\n\t"
    "s_lshl_b32 s44, s69, 11\n\t"
    "buffer_load_dwordx4 v126, %15, s44 offen lds\n\t"
    "s_add_u32 s69, s69, 4\n\t"
    "s_sub_u32 s44, s69, 125\n\t"
    "s_cmp_ge_u32 s69, 125\n\t"
    "s_cselect_b32 s69, s44, s69\n\t"
    "s_mul_i32 m0, s69, 1040\n\t"
    "s_lshl_b32 s44, s69, 11\n\t"
    "buffer_load_dwordx4 v126, %15, s44 offen lds\n\t"
    "s_add_u32 s69, s69, 4\n\t"
    "s_sub_u32 s44, s69, 125\n\t"
    "s_cmp_ge_u32 s69, 125\n\t"
    "s_cselect_b32 s69, s44, s69\n\t"
    "s_mul_i32 m0, s69, 1040\n\t"
    "s_lshl_b32 s44, s69, 11\n\t"
    "buffer_load_dwordx4 v126, %15, s44 offen lds\n\t"
    "s_add_u32 s69, s69, 4\n\t"
    "s_sub_u32 s44, s69, 125\n\t"
    "s_cmp_ge_u32 s69, 125\n\t"
    "s_cselect_b32 s69, s44, s69\n\t"
    "s_mul_i32 m0, s69, 1040\n\t"
    "s_lshl_b32 s44, s69, 11\n\t"
    "buffer_load_dwordx4 v126, %15, s44 offen lds\n\t"
    "s_add_u32 s69, s69, 4\n\t"
    "s_sub_u32 s44, s69, 125\n\t"
    "s_cmp_ge_u32 s69, 125\n\t"
    "s_cselect_b32 s69, s44, s69\n\t"
    "v_bfe_u32 v154, v147, 2, 2\n\t"
    "v_and_b32 v155, 3, v147\n\t"
    "v_mul_u32_u24 v154, 25, v154\n\t"
    "v_mad_u32_u24 v154, v155, 6, v154\n\t"
    "v_lshlrev_b32 v153, 2, v154\n\t"
    "s_and_b32 s53, s48, 0xff\n\t"
    "s_bfe_u32 s55, s48, 0x80008\n\t"
    "s_add_u32 s54, s53, s55\n\t"
    "s_add_u32 s55, s54, s55\n\t"
    "s_lshr_b32 s64, s48, 16\n\t"
    "v_cmp_eq_u32 s[62:63], 0, v155\n\t"
    "v_and_b32 v151, 48, v147\n\t"
    "v_or_b32 v155, 0x100000, v151\n\t"
    "v_cndmask_b32 v152, v155, v151, s[62:63]\n\t"
    "v_lshrrev_b32 v94, 4, v147\n\t"
    "v_mul_u32_u24 v92, 0x1388, v94\n\t"
    "v_lshl_add_u32 v92, v154, 1, v92\n\t"
    "v_add_u32 v92, 140288, v92\n\t"
    "v_mul_u32_u24 v93, 200, v154\n\t"
    "v_mad_u32_u24 v93, v94, 50, v93\n\t"
    "v_add_u32 v93, 140288, v93\n\t"
    "v_add_u32 v95, 140288, v122\n\t"
    "v_mov_b32 v0, 0\n\t"
    "v_mov_b32 v28, 0\n\t"
    "v_mov_b32 v1, 0\n\t"
    "v_mov_b32 v29, 0\n\t"
    "v_mov_b32 v2, 0\n\t"
    "v_mov_b32 v30, 0\n\t"
    "v_mov_b32 v3, 0\n\t"
    "v_mov_b32 v31, 0\n\t"
    "v_mov_b32 v4, 0\n\t"
    "v_mov_b32 v32, 0\n\t"
    "v_mov_b32 v5, 0\n\t"
    "v_mov_b32 v33, 0\n\t"
    "v_mov_b32 v6, 0\n\t"
    "v_mov_b32 v34, 0\n\t"
    "v_mov_b32 v7, 0\n\t"
    "v_mov_b32 v35, 0\n\t"
    "v_mov_b32 v8, 0\n\t"
    "v_mov_b32 v36, 0\n\t"
    "v_mov_b32 v9, 0\n\t"
    "v_mov_b32 v37, 0\n\t"
    "v_mov_b32 v10, 0\n\t"
    "v_mov_b32 v38, 0\n\t"
    "v_mov_b32 v11, 0\n\t"
    "v_mov_b32 v39, 0\n\t"
    "v_mov_b32 v12, 0\n\t"
    "v_mov_b32 v40, 0\n\t"
    "v_mov_b32 v13, 0\n\t"
    "v_mov_b32 v41, 0\n\t"
    "v_mov_b32 v14, 0\n\t"
    "v_mov_b32 v42, 0\n\t"
    "v_mov_b32 v15, 0\n\t"
    "v_mov_b32 v43, 0\n\t"
    "v_mov_b32 v16, 0\n\t"
    "v_mov_b32 v44, 0\n\t"
    "v_mov_b32 v17, 0\n\t"
    "v_mov_b32 v45, 0\n\t"
    "v_mov_b32 v18, 0\n\t"
    "v_mov_b32 v46, 0\n\t"
    "v_mov_b32 v19, 0\n\t"
    "v_mov_b32 v47, 0\n\t"
    "v_mov_b32 v20, 0\n\t"
    "v_mov_b32 v48, 0\n\t"
    "v_mov_b32 v21, 0\n\t"
    "v_mov_b32 v49, 0\n\t"
    "v_mov_b32 v22, 0\n\t"
    "v_mov_b32 v50, 0\n\t"
    "v_mov_b32 v23, 0\n\t"
    "v_mov_b32 v51, 0\n\t"
    "s_cmp_eq_u32 s61, 1\n\t"
    "s_cbranch_scc1 L_afold1_%=\n\t"
    "s_cmp_eq_u32 s61, 2\n\t"
    "s_cbranch_scc1 L_afold2_%=\n\t"
    "s_cmp_eq_u32 s61, 3\n\t"
    "s_cbranch_scc1 L_afold3_%=\n\t"
    "L_afold0_%=:\n\t"
    "L_asp0_%=:\n\t"
    "ds_read_b32 v119, v117 offset:1044\n\t"
    "s_waitcnt lgkmcnt(0)\n\t"
    "v_readfirstlane_b32 s46, v119\n\t"
    "s_nop 0\n\t"
    "s_cmp_ge_u32 s46, 2\n\t"
    "s_cbranch_scc1 L_ago0_%=\n\t"
    "s_sleep 1\n\t"
    "s_branch L_asp0_%=\n\t"
    "L_ago0_%=:\n\t"
    "ds_read_u16_d16_hi v0, v92 offset:204\n\t"
    "ds_read_u16_d16_hi v28, v93 offset:402\n\t"
    "ds_read_u16_d16_hi v1, v92 offset:1404\n\t"
    "ds_read_u16_d16_hi v29, v93 offset:414\n\t"
    "ds_read_u16_d16_hi v2, v92 offset:2604\n\t"
    "ds_read_u16_d16_hi v30, v93 offset:426\n\t"
    "ds_read_u16_d16_hi v3, v92 offset:3804\n\t"
    "ds_read_u16_d16_hi v31, v93 offset:438\n\t"
    "ds_read_u16_d16_hi v4, v92 offset:408\n\t"
    "ds_read_u16_d16_hi v32, v93 offset:804\n\t"
    "ds_read_u16_d16_hi v5, v92 offset:1608\n\t"
    "ds_read_u16_d16_hi v33, v93 offset:816\n\t"
    "ds_read_u16_d16_hi v6, v92 offset:2808\n\t"
    "ds_read_u16_d16_hi v34, v93 offset:828\n\t"
    "ds_read_u16_d16_hi v7, v92 offset:4008\n\t"
    "ds_read_u16_d16_hi v35, v93 offset:840\n\t"
    "ds_read_u16_d16_hi v8, v92 offset:412\n\t"
    "ds_read_u16_d16_hi v36, v93 offset:1204\n\t"
    "ds_read_u16_d16_hi v9, v92 offset:1612\n\t"
    "ds_read_u16_d16_hi v37, v93 offset:1216\n\t"
    "ds_read_u16_d16_hi v10, v92 offset:2812\n\t"
    "ds_read_u16_d16_hi v38, v93 offset:1228\n\t"
    "ds_read_u16_d16_hi v11, v92 offset:4012\n\t"
    "ds_read_u16_d16_hi v39, v93 offset:1240\n\t"
    "ds_read_u16_d16_hi v12, v92 offset:810\n\t"
    "ds_read_u16_d16_hi v40, v93 offset:1008\n\t"
    "ds_read_u16_d16_hi v13, v92 offset:2010\n\t"
    "ds_read_u16_d16_hi v41, v93 offset:1020\n\t"
    "ds_read_u16_d16_hi v14, v92 offset:3210\n\t"
    "ds_read_u16_d16_hi v42, v93 offset:1032\n\t"
    "ds_read_u16_d16_hi v15, v92 offset:4410\n\t"
    "ds_read_u16_d16_hi v43, v93 offset:1044\n\t"
    "ds_read_u16_d16_hi v16, v92 offset:202\n\t"
    "ds_read_u16_d16_hi v17, v92 offset:1402\n\t"
    "ds_read_u16_d16_hi v18, v92 offset:2602\n\t"
    "ds_read_u16_d16_hi v19, v92 offset:3802\n\t"
    "ds_read_u16_d16_hi v20, v92 offset:808\n\t"
    "ds_read_u16_d16_hi v21, v92 offset:2008\n\t"
    "ds_read_u16_d16_hi v22, v92 offset:3208\n\t"
    "ds_read_u16_d16_hi v23, v92 offset:4408\n\t"
    "ds_add_u32 v117, v118 offset:0\n\t"
    "s_waitcnt lgkmcnt(15)\n\t"
    "v_add_f32 v0, v0, v28\n\t"
    "v_add_f32 v1, v1, v29\n\t"
    "v_add_f32 v2, v2, v30\n\t"
    "v_add_f32 v3, v3, v31\n\t"
    "v_add_f32 v4, v4, v32\n\t"
    "v_add_f32 v5, v5, v33\n\t"
    "v_add_f32 v6, v6, v34\n\t"
    "v_add_f32 v7, v7, v35\n\t"
    "v_add_f32 v8, v8, v36\n\t"
    "v_add_f32 v9, v9, v37\n\t"
    "v_add_f32 v10, v10, v38\n\t"
    "v_add_f32 v11, v11, v39\n\t"
    "s_waitcnt lgkmcnt(9)\n\t"
    "v_add_f32 v12, v12, v40\n\t"
    "v_add_f32 v13, v13, v41\n\t"
    "v_add_f32 v14, v14, v42\n\t"
    "v_add_f32 v15, v15, v43\n\t"
    "s_waitcnt lgkmcnt(5)\n\t"
    "s_waitcnt lgkmcnt(1)\n\t"
    "L_asp1_%=:\n\t"
    "ds_read_b32 v119, v117 offset:0\n\t"
    "s_waitcnt lgkmcnt(0)\n\t"
    "v_readfirstlane_b32 s46, v119\n\t"
    "s_nop 0\n\t"
    "s_cmp_ge_u32 s46, 4\n\t"
    "s_cbranch_scc1 L_ago1_%=\n\t"
    "s_sleep 1\n\t"
    "s_branch L_asp1_%=\n\t"
    "L_ago1_%=:\n\t"
    "ds_write_b128 v95, v[0:3] offset:1024\n\t"
    "ds_write_b128 v95, v[4:7] offset:7168\n\t"
    "ds_write_b128 v95, v[8:11] offset:11264\n\t"
    "ds_write_b128 v95, v[12:15] offset:16384\n\t"
    "ds_write_b128 v95, v[16:19] offset:0\n\t"
    "ds_write_b128 v95, v[20:23] offset:15360\n\t"
    "ds_add_u32 v117, v118 offset:4\n\t"
    "s_branch L_ab0_%=\n\t"
    "L_afold1_%=:\n\t"
    "L_asp2_%=:\n\t"
    "ds_read_b32 v119, v117 offset:1044\n\t"
    "s_waitcnt lgkmcnt(0)\n\t"
    "v_readfirstlane_b32 s46, v119\n\t"
    "s_nop 0\n\t"
    "s_cmp_ge_u32 s46, 2\n\t"
    "s_cbranch_scc1 L_ago2_%=\n\t"
    "s_sleep 1\n\t"
    "s_branch L_asp2_%=\n\t"
    "L_ago2_%=:\n\t"
    "ds_read_u16_d16_hi v0, v92 offset:206\n\t"
    "ds_read_u16_d16_hi v28, v93 offset:602\n\t"
    "ds_read_u16_d16_hi v1, v92 offset:1406\n\t"
    "ds_read_u16_d16_hi v29, v93 offset:614\n\t"
    "ds_read_u16_d16_hi v2, v92 offset:2606\n\t"
    "ds_read_u16_d16_hi v30, v93 offset:626\n\t"
    "ds_read_u16_d16_hi v3, v92 offset:3806\n\t"
    "ds_read_u16_d16_hi v31, v93 offset:638\n\t"
    "ds_read_u16_d16_hi v4, v92 offset:210\n\t"
    "ds_read_u16_d16_hi v32, v93 offset:1002\n\t"
    "ds_read_u16_d16_hi v5, v92 offset:1410\n\t"
    "ds_read_u16_d16_hi v33, v93 offset:1014\n\t"
    "ds_read_u16_d16_hi v6, v92 offset:2610\n\t"
    "ds_read_u16_d16_hi v34, v93 offset:1026\n\t"
    "ds_read_u16_d16_hi v7, v92 offset:3810\n\t"
    "ds_read_u16_d16_hi v35, v93 offset:1038\n\t"
    "ds_read_u16_d16_hi v8, v92 offset:608\n\t"
    "ds_read_u16_d16_hi v36, v93 offset:806\n\t"
    "ds_read_u16_d16_hi v9, v92 offset:1808\n\t"
    "ds_read_u16_d16_hi v37, v93 offset:818\n\t"
    "ds_read_u16_d16_hi v10, v92 offset:3008\n\t"
    "ds_read_u16_d16_hi v38, v93 offset:830\n\t"
    "ds_read_u16_d16_hi v11, v92 offset:4208\n\t"
    "ds_read_u16_d16_hi v39, v93 offset:842\n\t"
    "ds_read_u16_d16_hi v12, v92 offset:812\n\t"
    "ds_read_u16_d16_hi v40, v93 offset:1208\n\t"
    "ds_read_u16_d16_hi v13, v92 offset:2012\n\t"
    "ds_read_u16_d16_hi v41, v93 offset:1220\n\t"
    "ds_read_u16_d16_hi v14, v92 offset:3212\n\t"
    "ds_read_u16_d16_hi v42, v93 offset:1232\n\t"
    "ds_read_u16_d16_hi v15, v92 offset:4412\n\t"
    "ds_read_u16_d16_hi v43, v93 offset:1244\n\t"
    "ds_read_u16_d16_hi v16, v92 offset:404\n\t"
    "ds_read_u16_d16_hi v17, v92 offset:1604\n\t"
    "ds_read_u16_d16_hi v18, v92 offset:2804\n\t"
    "ds_read_u16_d16_hi v19, v92 offset:4004\n\t"
    "ds_read_u16_d16_hi v20, v92 offset:1010\n\t"
    "ds_read_u16_d16_hi v21, v92 offset:2210\n\t"
    "ds_read_u16_d16_hi v22, v92 offset:3410\n\t"
    "ds_read_u16_d16_hi v23, v92 offset:4610\n\t"
    "ds_add_u32 v117, v118 offset:0\n\t"
    "s_waitcnt lgkmcnt(15)\n\t"
    "v_add_f32 v0, v0, v28\n\t"
    "v_add_f32 v1, v1, v29\n\t"
    "v_add_f32 v2, v2, v30\n\t"
    "v_add_f32 v3, v3, v31\n\t"
    "v_add_f32 v4, v4, v32\n\t"
    "v_add_f32 v5, v5, v33\n\t"
    "v_add_f32 v6, v6, v34\n\t"
    "v_add_f32 v7, v7, v35\n\t"
    "v_add_f32 v8, v8, v36\n\t"
    "v_add_f32 v9, v9, v37\n\t"
    "v_add_f32 v10, v10, v38\n\t"
    "v_add_f32 v11, v11, v39\n\t"
    "s_waitcnt lgkmcnt(9)\n\t"
    "v_add_f32 v12, v12, v40\n\t"
    "v_add_f32 v13, v13, v41\n\t"
    "v_add_f32 v14, v14, v42\n\t"
    "v_add_f32 v15, v15, v43\n\t"
    "s_waitcnt lgkmcnt(5)\n\t"
    "s_waitcnt lgkmcnt(1)\n\t"
    "L_asp3_%=:\n\t"
    "ds_read_b32 v119, v117 offset:0\n\t"
    "s_waitcnt lgkmcnt(0)\n\t"
    "v_readfirstlane_b32 s46, v119\n\t"
    "s_nop 0\n\t"
    "s_cmp_ge_u32 s46, 4\n\t"
    "s_cbranch_scc1 L_ago3_%=\n\t"
    "s_sleep 1\n\t"
    "s_branch L_asp3_%=\n\t"
    "L_ago3_%=:\n\t"
    "ds_write_b128 v95, v[0:3] offset:3072\n\t"
    "ds_write_b128 v95, v[4:7] offset:8192\n\t"
    "ds_write_b128 v95, v[8:11] offset:12288\n\t"
    "ds_write_b128 v95, v[12:15] offset:17408\n\t"
    "ds_write_b128 v95, v[16:19] offset:2048\n\t"
    "ds_write_b128 v95, v[20:23] offset:18432\n\t"
    "ds_add_u32 v117, v118 offset:4\n\t"
    "s_branch L_ab0_%=\n\t"
    "L_afold2_%=:\n\t"
    "L_asp4_%=:\n\t"
    "ds_read_b32 v119, v117 offset:1044\n\t"
    "s_waitcnt lgkmcnt(0)\n\t"
    "v_readfirstlane_b32 s46, v119\n\t"
    "s_nop 0\n\t"
    "s_cmp_ge_u32 s46, 2\n\t"
    "s_cbranch_scc1 L_ago4_%=\n\t"
    "s_sleep 1\n\t"
    "s_branch L_asp4_%=\n\t"
    "L_ago4_%=:\n\t"
    "ds_read_u16_d16_hi v0, v92 offset:406\n\t"
    "ds_read_u16_d16_hi v28, v93 offset:604\n\t"
    "ds_read_u16_d16_hi v1, v92 offset:1606\n\t"
    "ds_read_u16_d16_hi v29, v93 offset:616\n\t"
    "ds_read_u16_d16_hi v2, v92 offset:2806\n\t"
    "ds_read_u16_d16_hi v30, v93 offset:628\n\t"
    "ds_read_u16_d16_hi v3, v92 offset:4006\n\t"
    "ds_read_u16_d16_hi v31, v93 offset:640\n\t"
    "ds_read_u16_d16_hi v4, v92 offset:212\n\t"
    "ds_read_u16_d16_hi v32, v93 offset:1202\n\t"
    "ds_read_u16_d16_hi v5, v92 offset:1412\n\t"
    "ds_read_u16_d16_hi v33, v93 offset:1214\n\t"
    "ds_read_u16_d16_hi v6, v92 offset:2612\n\t"
    "ds_read_u16_d16_hi v34, v93 offset:1226\n\t"
    "ds_read_u16_d16_hi v7, v92 offset:3812\n\t"
    "ds_read_u16_d16_hi v35, v93 offset:1238\n\t"
    "ds_read_u16_d16_hi v8, v92 offset:610\n\t"
    "ds_read_u16_d16_hi v36, v93 offset:1006\n\t"
    "ds_read_u16_d16_hi v9, v92 offset:1810\n\t"
    "ds_read_u16_d16_hi v37, v93 offset:1018\n\t"
    "ds_read_u16_d16_hi v10, v92 offset:3010\n\t"
    "ds_read_u16_d16_hi v38, v93 offset:1030\n\t"
    "ds_read_u16_d16_hi v11, v92 offset:4210\n\t"
    "ds_read_u16_d16_hi v39, v93 offset:1042\n\t"
    "ds_read_u16_d16_hi v12, v92 offset:1012\n\t"
    "ds_read_u16_d16_hi v40, v93 offset:1210\n\t"
    "ds_read_u16_d16_hi v13, v92 offset:2212\n\t"
    "ds_read_u16_d16_hi v41, v93 offset:1222\n\t"
    "ds_read_u16_d16_hi v14, v92 offset:3412\n\t"
    "ds_read_u16_d16_hi v42, v93 offset:1234\n\t"
    "ds_read_u16_d16_hi v15, v92 offset:4612\n\t"
    "ds_read_u16_d16_hi v43, v93 offset:1246\n\t"
    "ds_read_u16_d16_hi v16, v92 offset:606\n\t"
    "ds_read_u16_d16_hi v17, v92 offset:1806\n\t"
    "ds_read_u16_d16_hi v18, v92 offset:3006\n\t"
    "ds_read_u16_d16_hi v19, v92 offset:4206\n\t"
    "ds_read_u16_d16_hi v20, v92 offset:1212\n\t"
    "ds_read_u16_d16_hi v21, v92 offset:2412\n\t"
    "ds_read_u16_d16_hi v22, v92 offset:3612\n\t"
    "ds_read_u16_d16_hi v23, v92 offset:4812\n\t"
    "ds_add_u32 v117, v118 offset:0\n\t"
    "s_waitcnt lgkmcnt(15)\n\t"
    "v_add_f32 v0, v0, v28\n\t"
    "v_add_f32 v1, v1, v29\n\t"
    "v_add_f32 v2, v2, v30\n\t"
    "v_add_f32 v3, v3, v31\n\t"
    "v_add_f32 v4, v4, v32\n\t"
    "v_add_f32 v5, v5, v33\n\t"
    "v_add_f32 v6, v6, v34\n\t"
    "v_add_f32 v7, v7, v35\n\t"
    "v_add_f32 v8, v8, v36\n\t"
    "v_add_f32 v9, v9, v37\n\t"
    "v_add_f32 v10, v10, v38\n\t"
    "v_add_f32 v11, v11, v39\n\t"
    "s_waitcnt lgkmcnt(9)\n\t"
    "v_add_f32 v12, v12, v40\n\t"
    "v_add_f32 v13, v13, v41\n\t"
    "v_add_f32 v14, v14, v42\n\t"
    "v_add_f32 v15, v15, v43\n\t"
    "s_waitcnt lgkmcnt(5)\n\t"
    "s_waitcnt lgkmcnt(1)\n\t"
    "L_asp5_%=:\n\t"
    "ds_read_b32 v119, v117 offset:0\n\t"
    "s_waitcnt lgkmcnt(0)\n\t"
    "v_readfirstlane_b32 s46, v119\n\t"
    "s_nop 0\n\t"
    "s_cmp_ge_u32 s46, 4\n\t"
    "s_cbranch_scc1 L_ago5_%=\n\t"
    "s_sleep 1\n\t"
    "s_branch L_asp5_%=\n\t"
    "L_ago5_%=:\n\t"
    "ds_write_b128 v95, v[0:3] offset:4096\n\t"
    "ds_write_b128 v95, v[4:7] offset:9216\n\t"
    "ds_write_b128 v95, v[8:11] offset:13312\n\t"
    "ds_write_b128 v95, v[12:15] offset:19456\n\t"
    "ds_write_b128 v95, v[16:19] offset:5120\n\t"
    "ds_write_b128 v95, v[20:23] offset:20480\n\t"
    "ds_add_u32 v117, v118 offset:4\n\t"
    "s_branch L_ab0_%=\n\t"
    "L_afold3_%=:\n\t"
    "L_asp6_%=:\n\t"
    "ds_read_b32 v119, v117 offset:1044\n\t"
    "s_waitcnt lgkmcnt(0)\n\t"
    "v_readfirstlane_b32 s46, v119\n\t"
    "s_nop 0\n\t"
    "s_cmp_ge_u32 s46, 2\n\t"
    "s_cbranch_scc1 L_ago6_%=\n\t"
    "s_sleep 1\n\t"
    "s_branch L_asp6_%=\n\t"
    "L_ago6_%=:\n\t"
    "ds_read_u16_d16_hi v0, v92 offset:208\n\t"
    "ds_read_u16_d16_hi v28, v93 offset:802\n\t"
    "ds_read_u16_d16_hi v1, v92 offset:1408\n\t"
    "ds_read_u16_d16_hi v29, v93 offset:814\n\t"
    "ds_read_u16_d16_hi v2, v92 offset:2608\n\t"
    "ds_read_u16_d16_hi v30, v93 offset:826\n\t"
    "ds_read_u16_d16_hi v3, v92 offset:3808\n\t"
    "ds_read_u16_d16_hi v31, v93 offset:838\n\t"
    "ds_read_u16_d16_hi v4, v92 offset:410\n\t"
    "ds_read_u16_d16_hi v32, v93 offset:1004\n\t"
    "ds_read_u16_d16_hi v5, v92 offset:1610\n\t"
    "ds_read_u16_d16_hi v33, v93 offset:1016\n\t"
    "ds_read_u16_d16_hi v6, v92 offset:2810\n\t"
    "ds_read_u16_d16_hi v34, v93 offset:1028\n\t"
    "ds_read_u16_d16_hi v7, v92 offset:4010\n\t"
    "ds_read_u16_d16_hi v35, v93 offset:1040\n\t"
    "ds_read_u16_d16_hi v8, v92 offset:612\n\t"
    "ds_read_u16_d16_hi v36, v93 offset:1206\n\t"
    "ds_read_u16_d16_hi v9, v92 offset:1812\n\t"
    "ds_read_u16_d16_hi v37, v93 offset:1218\n\t"
    "ds_read_u16_d16_hi v10, v92 offset:3012\n\t"
    "ds_read_u16_d16_hi v38, v93 offset:1230\n\t"
    "ds_read_u16_d16_hi v11, v92 offset:4212\n\t"
    "ds_read_u16_d16_hi v39, v93 offset:1242\n\t"
    "ds_read_u16_d16_hi v12, v92 offset:0\n\t"
    "ds_read_u16_d16_hi v13, v92 offset:2\n\t"
    "ds_read_u16_d16_hi v41, v93 offset:200\n\t"
    "ds_read_u16_d16_hi v14, v92 offset:4\n\t"
    "ds_read_u16_d16_hi v42, v93 offset:400\n\t"
    "ds_read_u16_d16_hi v15, v92 offset:6\n\t"
    "ds_read_u16_d16_hi v43, v93 offset:600\n\t"
    "ds_read_u16_d16_hi v16, v92 offset:8\n\t"
    "ds_read_u16_d16_hi v44, v93 offset:800\n\t"
    "ds_read_u16_d16_hi v17, v92 offset:10\n\t"
    "ds_read_u16_d16_hi v45, v93 offset:1000\n\t"
    "ds_read_u16_d16_hi v18, v92 offset:12\n\t"
    "ds_read_u16_d16_hi v46, v93 offset:1200\n\t"
    "ds_add_u32 v117, v118 offset:0\n\t"
    "s_waitcnt lgkmcnt(15)\n\t"
    "v_add_f32 v0, v0, v28\n\t"
    "v_add_f32 v1, v1, v29\n\t"
    "v_add_f32 v2, v2, v30\n\t"
    "v_add_f32 v3, v3, v31\n\t"
    "v_add_f32 v4, v4, v32\n\t"
    "v_add_f32 v5, v5, v33\n\t"
    "v_add_f32 v6, v6, v34\n\t"
    "v_add_f32 v7, v7, v35\n\t"
    "s_waitcnt lgkmcnt(14)\n\t"
    "v_add_f32 v8, v8, v36\n\t"
    "v_add_f32 v9, v9, v37\n\t"
    "v_add_f32 v10, v10, v38\n\t"
    "v_add_f32 v11, v11, v39\n\t"
    "s_waitcnt lgkmcnt(7)\n\t"
    "v_cndmask_b32 v12, 0, v12, s[62:63]\n\t"
    "v_add_f32 v13, v13, v41\n\t"
    "v_add_f32 v14, v14, v42\n\t"
    "v_add_f32 v15, v15, v43\n\t"
    "s_waitcnt lgkmcnt(1)\n\t"
    "v_add_f32 v16, v16, v44\n\t"
    "v_add_f32 v17, v17, v45\n\t"
    "v_add_f32 v18, v18, v46\n\t"
    "L_asp7_%=:\n\t"
    "ds_read_b32 v119, v117 offset:0\n\t"
    "s_waitcnt lgkmcnt(0)\n\t"
    "v_readfirstlane_b32 s46, v119\n\t"
    "s_nop 0\n\t"
    "s_cmp_ge_u32 s46, 4\n\t"
    "s_cbranch_scc1 L_ago7_%=\n\t"
    "s_sleep 1\n\t"
    "s_branch L_asp7_%=\n\t"
    "L_ago7_%=:\n\t"
    "ds_write_b128 v95, v[0:3] offset:6144\n\t"
    "ds_write_b128 v95, v[4:7] offset:10240\n\t"
    "ds_write_b128 v95, v[8:11] offset:14336\n\t"
    "ds_write_b128 v95, v[12:15] offset:21504\n\t"
    "ds_write_b128 v95, v[16:19] offset:22528\n\t"
    "ds_add_u32 v117, v118 offset:4\n\t"
    "L_ab0_%=:\n\t"
    "s_mul_i32 m0, s69, 1040\n\t"
    "s_lshl_b32 s44, s69, 11\n\t"
    "buffer_load_dwordx4 v126, %15, s44 offen lds\n\t"
    "s_add_u32 s69, s69, 4\n\t"
    "s_sub_u32 s44, s69, 125\n\t"
    "s_cmp_ge_u32 s69, 125\n\t"
    "s_cselect_b32 s69, s44, s69\n\t"
    "s_mul_i32 m0, s69, 1040\n\t"
    "s_lshl_b32 s44, s69, 11\n\t"
    "buffer_load_dwordx4 v126, %15, s44 offen lds\n\t"
    "s_add_u32 s69, s69, 4\n\t"
    "s_sub_u32 s44, s69, 125\n\t"
    "s_cmp_ge_u32 s69, 125\n\t"
    "s_cselect_b32 s69, s44, s69\n\t"
    "s_mul_i32 m0, s69, 1040\n\t"
    "s_lshl_b32 s44, s69, 11\n\t"
    "buffer_load_dwordx4 v126, %15, s44 offen lds\n\t"
    "s_add_u32 s69, s69, 4\n\t"
    "s_sub_u32 s44, s69, 125\n\t"
    "s_cmp_ge_u32 s69, 125\n\t"
    "s_cselect_b32 s69, s44, s69\n\t"
    "s_mul_i32 m0, s69, 1040\n\t"
    "s_lshl_b32 s44, s69, 11\n\t"
    "buffer_load_dwordx4 v126, %15, s44 offen lds\n\t"
    "s_add_u32 s69, s69, 4\n\t"
    "s_sub_u32 s44, s69, 125\n\t"
    "s_cmp_ge_u32 s69, 125\n\t"
    "s_cselect_b32 s69, s44, s69\n\t"
    "s_mul_i32 m0, s69, 1040\n\t"
    "s_lshl_b32 s44, s69, 11\n\t"
    "buffer_load_dwordx4 v126, %15, s44 offen lds\n\t"
    "s_add_u32 s69, s69, 4\n\t"
    "s_sub_u32 s44, s69, 125\n\t"
    "s_cmp_ge_u32 s69, 125\n\t"
    "s_cselect_b32 s69, s44, s69\n\t"
    "s_branch L_ab_%=\n\t"
    "L_adma_%=:\n\t"
    "s_sub_u32 s46, %16, 4\n\t"
    "s_add_u32 s69, s51, s46\n\t"
    "s_sub_u32 s44, s69, 125\n\t"
    "s_cmp_ge_u32 s69, 125\n\t"
    "s_cselect_b32 s69, s44, s69\n\t"
    "s_mul_i32 m0, s69, 1040\n\t"
    "s_lshl_b32 s44, s69, 11\n\t"
    "buffer_load_dwordx4 v126, %15, s44 offen lds\n\t"
    "s_add_u32 s69, s69, 2\n\t"
    "s_sub_u32 s44, s69, 125\n\t"
    "s_cmp_ge_u32 s69, 125\n\t"
    "s_cselect_b32 s69, s44, s69\n\t"
    "s_mul_i32 m0, s69, 1040\n\t"
    "s_lshl_b32 s44, s69, 11\n\t"
    "buffer_load_dwordx4 v126, %15, s44 offen lds\n\t"
    "s_add_u32 s69, s69, 2\n\t"
    "s_sub_u32 s44, s69, 125\n\t"
    "s_cmp_ge_u32 s69, 125\n\t"
    "s_cselect_b32 s69, s44, s69\n\t"
    "s_barrier\n\t"
    "s_load_dwordx2 s[72:73], %6, 0\n\t"
    "s_load_dwordx2 s[88:89], %7, 0\n\t"
    "s_load_dword s91, %8, 0\n\t"
    "s_mul_i32 m0, s69, 1040\n\t"
    "s_lshl_b32 s44, s69, 11\n\t"
    "buffer_load_dwordx4 v126, %15, s44 offen lds\n\t"
    "s_add_u32 s69, s69, 2\n\t"
    "s_sub_u32 s44, s69, 125\n\t"
    "s_cmp_ge_u32 s69, 125\n\t"
    "s_cselect_b32 s69, s44, s69\n\t"
    "s_mul_i32 m0, s69, 1040\n\t"
    "s_lshl_b32 s44, s69, 11\n\t"
    "buffer_load_dwordx4 v126, %15, s44 offen lds\n\t"
    "s_add_u32 s69, s69, 2\n\t"
    "s_sub_u32 s44, s69, 125\n\t"
    "s_cmp_ge_u32 s69, 125\n\t"
    "s_cselect_b32 s69, s44, s69\n\t"
    "s_mul_i32 m0, s69, 1040\n\t"
    "s_lshl_b32 s44, s69, 11\n\t"
    "buffer_load_dwordx4 v126, %15, s44 offen lds\n\t"
    "s_add_u32 s69, s69, 2\n\t"
    "s_sub_u32 s44, s69, 125\n\t"
    "s_cmp_ge_u32 s69, 125\n\t"
    "s_cselect_b32 s69, s44, s69\n\t"
    "s_mul_i32 m0, s69, 1040\n\t"
    "s_lshl_b32 s44, s69, 11\n\t"
    "buffer_load_dwordx4 v126, %15, s44 offen lds\n\t"
    "s_add_u32 s69, s69, 2\n\t"
    "s_sub_u32 s44, s69, 125\n\t"
    "s_cmp_ge_u32 s69, 125\n\t"
    "s_cselect_b32 s69, s44, s69\n\t"
    "s_mul_i32 m0, s69, 1040\n\t"
    "s_lshl_b32 s44, s69, 11\n\t"
    "buffer_load_dwordx4 v126, %15, s44 offen lds\n\t"
    "s_add_u32 s69, s69, 2\n\t"
    "s_sub_u32 s44, s69, 125\n\t"
    "s_cmp_ge_u32 s69, 125\n\t"
    "s_cselect_b32 s69, s44, s69\n\t"
    "s_mul_i32 m0, s69, 1040\n\t"
    "s_lshl_b32 s44, s69, 11\n\t"
    "buffer_load_dwordx4 v126, %15, s44 offen lds\n\t"
    "s_add_u32 s69, s69, 2\n\t"
    "s_sub_u32 s44, s69, 125\n\t"
    "s_cmp_ge_u32 s69, 125\n\t"
    "s_cselect_b32 s69, s44, s69\n\t"
    "s_mul_i32 m0, s69, 1040\n\t"
    "s_lshl_b32 s44, s69, 11\n\t"
    "buffer_load_dwordx4 v126, %15, s44 offen lds\n\t"
    "s_add_u32 s69, s69, 2\n\t"
    "s_sub_u32 s44, s69, 125\n\t"
    "s_cmp_ge_u32 s69, 125\n\t"
    "s_cselect_b32 s69, s44, s69\n\t"
    "s_mul_i32 m0, s69, 1040\n\t"
    "s_lshl_b32 s44, s69, 11\n\t"
    "buffer_load_dwordx4 v126, %15, s44 offen lds\n\t"
    "s_add_u32 s69, s69, 2\n\t"
    "s_sub_u32 s44, s69, 125\n\t"
    "s_cmp_ge_u32 s69, 125\n\t"
    "s_cselect_b32 s69, s44, s69\n\t"
    "s_mul_i32 m0, s69, 1040\n\t"
    "s_lshl_b32 s44, s69, 11\n\t"
    "buffer_load_dwordx4 v126, %15, s44 offen lds\n\t"
    "s_add_u32 s69, s69, 2\n\t"
    "s_sub_u32 s44, s69, 125\n\t"
    "s_cmp_ge_u32 s69, 125\n\t"
    "s_cselect_b32 s69, s44, s69\n\t"
    "s_mul_i32 m0, s69, 1040\n\t"
    "s_lshl_b32 s44, s69, 11\n\t"
    "buffer_load_dwordx4 v126, %15, s44 offen lds\n\t"
    "s_add_u32 s69, s69, 2\n\t"
    "s_sub_u32 s44, s69, 125\n\t"
    "s_cmp_ge_u32 s69, 125\n\t"
    "s_cselect_b32 s69, s44, s69\n\t"
    "v_bfe_u32 v154, v147, 2, 2\n\t"
    "v_and_b32 v155, 3, v147\n\t"
    "v_mul_u32_u24 v154, 25, v154\n\t"
    "v_mad_u32_u24 v154, v155, 6, v154\n\t"
    "v_lshlrev_b32 v153, 2, v154\n\t"
    "s_and_b32 s53, s48, 0xff\n\t"
    "s_bfe_u32 s55, s48, 0x80008\n\t"
    "s_add_u32 s54, s53, s55\n\t"
    "s_add_u32 s55, s54, s55\n\t"
    "s_lshr_b32 s64, s48, 16\n\t"
    "v_cmp_eq_u32 s[62:63], 0, v155\n\t"
    "v_and_b32 v151, 48, v147\n\t"
    "v_or_b32 v155, 0x100000, v151\n\t"
    "v_cndmask_b32 v152, v155, v151, s[62:63]\n\t"
    "s_mul_i32 m0, s69, 1040\n\t"
    "s_lshl_b32 s44, s69, 11\n\t"
    "buffer_load_dwordx4 v126, %15, s44 offen lds\n\t"
    "s_add_u32 s69, s69, 2\n\t"
    "s_sub_u32 s44, s69, 125\n\t"
    "s_cmp_ge_u32 s69, 125\n\t"
    "s_cselect_b32 s69, s44, s69\n\t"
    "s_mul_i32 m0, s69, 1040\n\t"
    "s_lshl_b32 s44, s69, 11\n\t"
    "buffer_load_dwordx4 v126, %15, s44 offen lds\n\t"
    "s_add_u32 s69, s69, 2\n\t"
    "s_sub_u32 s44, s69, 125\n\t"
    "s_cmp_ge_u32 s69, 125\n\t"
    "s_cselect_b32 s69, s44, s69\n\t"
    "s_mul_i32 m0, s69, 1040\n\t"
    "s_lshl_b32 s44, s69, 11\n\t"
    "buffer_load_dwordx4 v126, %15, s44 offen lds\n\t"
    "s_add_u32 s69, s69, 2\n\t"
    "s_sub_u32 s44, s69, 125\n\t"
    "s_cmp_ge_u32 s69, 125\n\t"
    "s_cselect_b32 s69, s44, s69\n\t"
    "s_mul_i32 m0, s69, 1040\n\t"
    "s_lshl_b32 s44, s69, 11\n\t"
    "buffer_load_dwordx4 v126, %15, s44 offen lds\n\t"
    "s_add_u32 s69, s69, 2\n\t"
    "s_sub_u32 s44, s69, 125\n\t"
    "s_cmp_ge_u32 s69, 125\n\t"
    "s_cselect_b32 s69, s44, s69\n\t"
    "s_mul_i32 m0, s69, 1040\n\t"
    "s_lshl_b32 s44, s69, 11\n\t"
    "buffer_load_dwordx4 v126, %15, s44 offen lds\n\t"
    "s_add_u32 s69, s69, 2\n\t"
    "s_sub_u32 s44, s69, 125\n\t"
    "s_cmp_ge_u32 s69, 125\n\t"
    "s_cselect_b32 s69, s44, s69\n\t"
    "s_mul_i32 m0, s69, 1040\n\t"
    "s_lshl_b32 s44, s69, 11\n\t"
    "buffer_load_dwordx4 v126, %15, s44 offen lds\n\t"
    "s_add_u32 s69, s69, 2\n\t"
    "s_sub_u32 s44, s69, 125\n\t"
    "s_cmp_ge_u32 s69, 125\n\t"
    "s_cselect_b32 s69, s44, s69\n\t"
    "s_mul_i32 m0, s69, 1040\n\t"
    "s_lshl_b32 s44, s69, 11\n\t"
    "buffer_load_dwordx4 v126, %15, s44 offen lds\n\t"
    "s_add_u32 s69, s69, 2\n\t"
    "s_sub_u32 s44, s69, 125\n\t"
    "s_cmp_ge_u32 s69, 125\n\t"
    "s_cselect_b32 s69, s44, s69\n\t"
    "s_mul_i32 m0, s69, 1040\n\t"
    "s_lshl_b32 s44, s69, 11\n\t"
    "buffer_load_dwordx4 v126, %15, s44 offen lds\n\t"
    "s_add_u32 s69, s69, 2\n\t"
    "s_sub_u32 s44, s69, 125\n\t"
    "s_cmp_ge_u32 s69, 125\n\t"
    "s_cselect_b32 s69, s44, s69\n\t"
    "s_mul_i32 m0, s69, 1040\n\t"
    "s_lshl_b32 s44, s69, 11\n\t"
    "buffer_load_dwordx4 v126, %15, s44 offen lds\n\t"
    "s_add_u32 s69, s69, 2\n\t"
    "s_sub_u32 s44, s69, 125\n\t"
    "s_cmp_ge_u32 s69, 125\n\t"
    "s_cselect_b32 s69, s44, s69\n\t"
    "s_mul_i32 m0, s69, 1040\n\t"
    "s_lshl_b32 s44, s69, 11\n\t"
    "buffer_load_dwordx4 v126, %15, s44 offen lds\n\t"
    "s_add_u32 s69, s69, 2\n\t"
    "s_sub_u32 s44, s69, 125\n\t"
    "s_cmp_ge_u32 s69, 125\n\t"
    "s_cselect_b32 s69, s44, s69\n\t"
    "s_mul_i32 m0, s69, 1040\n\t"
    "s_lshl_b32 s44, s69, 11\n\t"
    "buffer_load_dwordx4 v126, %15, s44 offen lds\n\t"
    "s_add_u32 s69, s69, 2\n\t"
    "s_sub_u32 s44, s69, 125\n\t"
    "s_cmp_ge_u32 s69, 125\n\t"
    "s_cselect_b32 s69, s44, s69\n\t"
    "s_mul_i32 m0, s69, 1040\n\t"
    "s_lshl_b32 s44, s69, 11\n\t"
    "buffer_load_dwordx4 v126, %15, s44 offen lds\n\t"
    "s_add_u32 s69, s69, 2\n\t"
    "s_sub_u32 s44, s69, 125\n\t"
    "s_cmp_ge_u32 s69, 125\n\t"
    "s_cselect_b32 s69, s44, s69\n\t"
    "s_mul_i32 m0, s69, 1040\n\t"
    "s_lshl_b32 s44, s69, 11\n\t"
    "buffer_load_dwordx4 v126, %15, s44 offen lds\n\t"
    "s_add_u32 s69, s69, 2\n\t"
    "s_sub_u32 s44, s69, 125\n\t"
    "s_cmp_ge_u32 s69, 125\n\t"
    "s_cselect_b32 s69, s44, s69\n\t"
    "s_mul_i32 m0, s69, 1040\n\t"
    "s_lshl_b32 s44, s69, 11\n\t"
    "buffer_load_dwordx4 v126, %15, s44 offen lds\n\t"
    "s_add_u32 s69, s69, 2\n\t"
    "s_sub_u32 s44, s69, 125\n\t"
    "s_cmp_ge_u32 s69, 125\n\t"
    "s_cselect_b32 s69, s44, s69\n\t"
    "s_mul_i32 m0, s69, 1040\n\t"
    "s_lshl_b32 s44, s69, 11\n\t"
    "buffer_load_dwordx4 v126, %15, s44 offen lds\n\t"
    "s_add_u32 s69, s69, 2\n\t"
    "s_sub_u32 s44, s69, 125\n\t"
    "s_cmp_ge_u32 s69, 125\n\t"
    "s_cselect_b32 s69, s44, s69\n\t"
    "s_mul_i32 m0, s69, 1040\n\t"
    "s_lshl_b32 s44, s69, 11\n\t"
    "buffer_load_dwordx4 v126, %15, s44 offen lds\n\t"
    "s_add_u32 s69, s69, 2\n\t"
    "s_sub_u32 s44, s69, 125\n\t"
    "s_cmp_ge_u32 s69, 125\n\t"
    "s_cselect_b32 s69, s44, s69\n\t"
    "s_mul_i32 m0, s69, 1040\n\t"
    "s_lshl_b32 s44, s69, 11\n\t"
    "buffer_load_dwordx4 v126, %15, s44 offen lds\n\t"
    "s_add_u32 s69, s69, 2\n\t"
    "s_sub_u32 s44, s69, 125\n\t"
    "s_cmp_ge_u32 s69, 125\n\t"
    "s_cselect_b32 s69, s44, s69\n\t"
    "s_mul_i32 m0, s69, 1040\n\t"
    "s_lshl_b32 s44, s69, 11\n\t"
    "buffer_load_dwordx4 v126, %15, s44 offen lds\n\t"
    "s_add_u32 s69, s69, 2\n\t"
    "s_sub_u32 s44, s69, 125\n\t"
    "s_cmp_ge_u32 s69, 125\n\t"
    "s_cselect_b32 s69, s44, s69\n\t"
    "s_mul_i32 m0, s69, 1040\n\t"
    "s_lshl_b32 s44, s69, 11\n\t"
    "buffer_load_dwordx4 v126, %15, s44 offen lds\n\t"
    "s_add_u32 s69, s69, 2\n\t"
    "s_sub_u32 s44, s69, 125\n\t"
    "s_cmp_ge_u32 s69, 125\n\t"
    "s_cselect_b32 s69, s44, s69\n\t"
    "s_mul_i32 m0, s69, 1040\n\t"
    "s_lshl_b32 s44, s69, 11\n\t"
    "buffer_load_dwordx4 v126, %15, s44 offen lds\n\t"
    "s_add_u32 s69, s69, 2\n\t"
    "s_sub_u32 s44, s69, 125\n\t"
    "s_cmp_ge_u32 s69, 125\n\t"
    "s_cselect_b32 s69, s44, s69\n\t"
    "s_mul_i32 m0, s69, 1040\n\t"
    "s_lshl_b32 s44, s69, 11\n\t"
    "buffer_load_dwordx4 v126, %15, s44 offen lds\n\t"
    "s_add_u32 s69, s69, 2\n\t"
    "s_sub_u32 s44, s69, 125\n\t"
    "s_cmp_ge_u32 s69, 125\n\t"
    "s_cselect_b32 s69, s44, s69\n\t"
    "s_mul_i32 m0, s69, 1040\n\t"
    "s_lshl_b32 s44, s69, 11\n\t"
    "buffer_load_dwordx4 v126, %15, s44 offen lds\n\t"
    "s_add_u32 s69, s69, 2\n\t"
    "s_sub_u32 s44, s69, 125\n\t"
    "s_cmp_ge_u32 s69, 125\n\t"
    "s_cselect_b32 s69, s44, s69\n\t"
    "s_mul_i32 m0, s69, 1040\n\t"
    "s_lshl_b32 s44, s69, 11\n\t"
    "buffer_load_dwordx4 v126, %15, s44 offen lds\n\t"
    "s_add_u32 s69, s69, 2\n\t"
    "s_sub_u32 s44, s69, 125\n\t"
    "s_cmp_ge_u32 s69, 125\n\t"
    "s_cselect_b32 s69, s44, s69\n\t"
    "s_mul_i32 m0, s69, 1040\n\t"
    "s_lshl_b32 s44, s69, 11\n\t"
    "buffer_load_dwordx4 v126, %15, s44 offen lds\n\t"
    "s_add_u32 s69, s69, 2\n\t"
    "s_sub_u32 s44, s69, 125\n\t"
    "s_cmp_ge_u32 s69, 125\n\t"
    "s_cselect_b32 s69, s44, s69\n\t"
    "s_mul_i32 m0, s69, 1040\n\t"
    "s_lshl_b32 s44, s69, 11\n\t"
    "buffer_load_dwordx4 v126, %15, s44 offen lds\n\t"
    "s_add_u32 s69, s69, 2\n\t"
    "s_sub_u32 s44, s69, 125\n\t"
    "s_cmp_ge_u32 s69, 125\n\t"
    "s_cselect_b32 s69, s44, s69\n\t"
    "s_mul_i32 m0, s69, 1040\n\t"
    "s_lshl_b32 s44, s69, 11\n\t"
    "buffer_load_dwordx4 v126, %15, s44 offen lds\n\t"
    "s_add_u32 s69, s69, 2\n\t"
    "s_sub_u32 s44, s69, 125\n\t"
    "s_cmp_ge_u32 s69, 125\n\t"
    "s_cselect_b32 s69, s44, s69\n\t"
    "s_mul_i32 m0, s69, 1040\n\t"
    "s_lshl_b32 s44, s69, 11\n\t"
    "buffer_load_dwordx4 v126, %15, s44 offen lds\n\t"
    "s_add_u32 s69, s69, 2\n\t"
    "s_sub_u32 s44, s69, 125\n\t"
    "s_cmp_ge_u32 s69, 125\n\t"
    "s_cselect_b32 s69, s44, s69\n\t"
    "s_mul_i32 m0, s69, 1040\n\t"
    "s_lshl_b32 s44, s69, 11\n\t"
    "buffer_load_dwordx4 v126, %15, s44 offen lds\n\t"
    "s_add_u32 s69, s69, 2\n\t"
    "s_sub_u32 s44, s69, 125\n\t"
    "s_cmp_ge_u32 s69, 125\n\t"
    "s_cselect_b32 s69, s44, s69\n\t"
    "s_mul_i32 m0, s69, 1040\n\t"
    "s_lshl_b32 s44, s69, 11\n\t"
    "buffer_load_dwordx4 v126, %15, s44 offen lds\n\t"
    "s_add_u32 s69, s69, 2\n\t"
    "s_sub_u32 s44, s69, 125\n\t"
    "s_cmp_ge_u32 s69, 125\n\t"
    "s_cselect_b32 s69, s44, s69\n\t"
    "s_mul_i32 m0, s69, 1040\n\t"
    "s_lshl_b32 s44, s69, 11\n\t"
    "buffer_load_dwordx4 v126, %15, s44 offen lds\n\t"
    "s_add_u32 s69, s69, 2\n\t"
    "s_sub_u32 s44, s69, 125\n\t"
    "s_cmp_ge_u32 s69, 125\n\t"
    "s_cselect_b32 s69, s44, s69\n\t"
    "L_ab_%=:\n\t"
    "s_mul_i32 s66, s53, 400\n\t"
    "buffer_load_dwordx3 v[128:130], v153, %0, s66 offen\n\t"
    "buffer_load_dwordx4 v[132:135], v153, %0, s66 offen offset:12\n\t"
    "s_mul_i32 s66, s54, 400\n\t"
    "buffer_load_dwordx3 v[136:138], v153, %0, s66 offen\n\t"
    "buffer_load_dwordx4 v[140:143], v153, %0, s66 offen offset:12\n\t"
    "v_add_u32 v95, 140288, v122\n\t"
    "v_mbcnt_lo_u32_b32 v100, -1, 0\n\t"
    "v_mbcnt_hi_u32_b32 v100, -1, v100\n\t"
    "s_lshl_b32 s61, %16, 15\n\t"
    "s_add_u32 s61, s61, 16515072\n\t"
    "v_lshlrev_b32 v127, 4, v100\n\t"
    "s_lshr_b32 s37, %14, 4\n\t"
    "s_and_b32 s39, %14, 7\n\t"
    "v_and_b32 v101, 15, v100\n\t"
    "s_lshl_b32 s39, s39, 4\n\t"
    "s_add_u32 s39, s39, s37\n\t"
    "s_mul_i32 s65, %16, 125\n\t"
    "v_add_u32 v155, s65, v101\n\t"
    "s_mul_i32 s63, s39, 102400\n\t"
    "s_add_u32 s63, s63, 3407872\n\t"
    "v_lshrrev_b32 v102, 2, v101\n\t"
    "s_and_b32 s37, s37, 7\n\t"
    "s_lshl_b32 s37, s37, 11\n\t"
    "v_lshrrev_b32 v103, 3, v100\n\t"
    "s_lshl_b32 s36, s39, 16\n\t"
    "s_bfe_u32 s65, %14, 0x10003\n\t"
    "v_and_b32 v103, 2, v103\n\t"
    "s_lshl_b32 s38, s65, 15\n\t"
    "s_add_u32 s36, s36, s38\n\t"
    "v_and_b32 v104, 1, v100\n\t"
    "s_lshl_b32 s38, %16, 12\n\t"
    "s_add_u32 s36, s36, s38\n\t"
    "v_lshlrev_b32 v104, 4, v104\n\t"
    "s_add_u32 s36, s36, 17825792\n\t"
    "v_mov_b32 v101, 0x3f80\n\t"
    "v_lshlrev_b32 v104, v104, v101\n\t"
    "v_cmp_eq_u32 vcc, v102, v103\n\t"
    "s_waitcnt lgkmcnt(0)\n\t"
    "s_lshr_b32 s39, s91, %9\n\t"
    "s_lshl_b32 s39, s39, 16\n\t"
    "v_cndmask_b32 v120, 0, v104, vcc\n\t"
    "v_cndmask_b32 v121, 0, v104, vcc\n\t"
    "v_add_u32 v103, 1, v103\n\t"
    "v_cmp_eq_u32 vcc, v102, v103\n\t"
    "s_lshl_b32 s40, s88, 16\n\t"
    "s_and_b32 s41, s88, 0xffff0000\n\t"
    "v_cndmask_b32 v122, 0, v104, vcc\n\t"
    "v_cndmask_b32 v123, 0, v104, vcc\n\t"
    "v_and_b32 v101, 2, v100\n\t"
    "v_mov_b32 v102, 0x44444444\n\t"
    "v_mov_b32 v103, 0xeeeeeeee\n\t"
    "v_cmp_eq_u32 vcc, 0, v101\n\t"
    "s_lshl_b32 s42, s89, 16\n\t"
    "s_and_b32 s43, s89, 0xffff0000\n\t"
    "v_cndmask_b32 v124, v103, v102, vcc\n\t"
    "v_lshrrev_b32 v101, 4, v100\n\t"
    "v_mul_u32_u24 v101, 8000, v101\n\t"
    "v_lshl_add_u32 v125, v155, 3, v101\n\t"
    "v_add_u32 v125, 64000, v125\n\t"
    "s_lshl_b32 s70, s72, 16\n\t"
    "s_and_b32 s71, s72, 0xffff0000\n\t"
    "s_lshl_b32 s72, s73, 16\n\t"
    "s_and_b32 s73, s73, 0xffff0000\n\t"
    "s_mov_b32 s67, 0xffff0000\n\t"
    "s_and_b32 s65, %14, 8\n\t"
    "s_add_u32 s38, s65, %16\n\t"
    "s_mul_i32 s38, s38, 212992\n\t"
    "s_add_u32 s38, s38, 196608\n\t"
    "s_add_u32 s84, s37, s38\n\t"
    "s_add_u32 s37, s37, 0x800\n\t"
    "s_and_b32 s37, s37, 0x3800\n\t"
    "s_add_u32 s85, s37, s38\n\t"
    "s_add_u32 s37, s37, 0x800\n\t"
    "s_and_b32 s37, s37, 0x3800\n\t"
    "s_add_u32 s86, s37, s38\n\t"
    "s_add_u32 s37, s37, 0x800\n\t"
    "s_and_b32 s37, s37, 0x3800\n\t"
    "s_add_u32 s87, s37, s38\n\t"
    "s_add_u32 s37, s37, 0x800\n\t"
    "s_and_b32 s37, s37, 0x3800\n\t"
    "s_add_u32 s88, s37, s38\n\t"
    "s_add_u32 s37, s37, 0x800\n\t"
    "s_and_b32 s37, s37, 0x3800\n\t"
    "s_add_u32 s89, s37, s38\n\t"
    "s_add_u32 s37, s37, 0x800\n\t"
    "s_and_b32 s37, s37, 0x3800\n\t"
    "s_add_u32 s90, s37, s38\n\t"
    "s_add_u32 s37, s37, 0x800\n\t"
    "s_and_b32 s37, s37, 0x3800\n\t"
    "s_add_u32 s91, s37, s38\n\t"
    "s_mul_i32 s94, s65, 425984\n\t"
    "s_sub_u32 s94, s94, 1703936\n\t"
    "s_sub_u32 s95, 0x8000, s94\n\t"
    "s_lshl_b32 s97, s65, 11\n\t"
    "s_sub_u32 s97, s97, 229376\n\t"
    "s_mov_b32 s92, 64000\n\t"
    "s_mov_b32 s93, -64000\n\t"
    "s_mov_b32 s96, 0x2000\n\t"
    "s_mov_b32 s62, 2\n\t"
    "v_rcp_f32 v101, s40\n\t"
    "v_rcp_f32 v102, s41\n\t"
    "v_rcp_f32 v103, s42\n\t"
    "v_rcp_f32 v104, s43\n\t"
    "v_mov_b32 v100, 0x7f7fffff\n\t"
    "v_med3_f32 v101, v101, -v100, v100\n\t"
    "v_med3_f32 v102, v102, -v100, v100\n\t"
    "v_med3_f32 v103, v103, -v100, v100\n\t"
    "v_med3_f32 v104, v104, -v100, v100\n\t"
    "v_readfirstlane_b32 s40, v101\n\t"
    "v_readfirstlane_b32 s41, v102\n\t"
    "v_readfirstlane_b32 s42, v103\n\t"
    "v_readfirstlane_b32 s43, v104\n\t"
    "L_asp8_%=:\n\t"
    "ds_read_b32 v119, v117 offset:4\n\t"
    "s_waitcnt lgkmcnt(0)\n\t"
    "v_readfirstlane_b32 s46, v119\n\t"
    "s_nop 0\n\t"
    "s_cmp_ge_u32 s46, 4\n\t"
    "s_cbranch_scc1 L_ago8_%=\n\t"
    "s_sleep 1\n\t"
    "s_branch L_asp8_%=\n\t"
    "L_ago8_%=:\n\t"
    "ds_read_b128 v[0:3], v95 offset:0\n\t"
    "ds_read_b128 v[4:7], v95 offset:1024\n\t"
    "ds_read_b128 v[8:11], v95 offset:2048\n\t"
    "ds_read_b128 v[12:15], v95 offset:3072\n\t"
    "ds_read_b128 v[16:19], v95 offset:4096\n\t"
    "ds_read_b128 v[20:23], v95 offset:5120\n\t"
    "ds_read_b128 v[24:27], v95 offset:6144\n\t"
    "ds_read_b128 v[28:31], v95 offset:7168\n\t"
    "ds_read_b128 v[32:35], v95 offset:8192\n\t"
    "ds_read_b128 v[36:39], v95 offset:9216\n\t"
    "ds_read_b128 v[40:43], v95 offset:10240\n\t"
    "ds_read_b128 v[44:47], v95 offset:11264\n\t"
    "ds_read_b128 v[48:51], v95 offset:12288\n\t"
    "ds_read_b128 v[52:55], v95 offset:13312\n\t"
    "ds_read_b128 v[56:59], v95 offset:14336\n\t"
    "ds_read_b128 v[60:63], v95 offset:15360\n\t"
    "ds_read_b128 v[64:67], v95 offset:16384\n\t"
    "ds_read_b128 v[68:71], v95 offset:17408\n\t"
    "ds_read_b128 v[72:75], v95 offset:18432\n\t"
    "ds_read_b128 v[76:79], v95 offset:19456\n\t"
    "ds_read_b128 v[80:83], v95 offset:20480\n\t"
    "ds_read_b128 v[84:87], v95 offset:21504\n\t"
    "ds_read_b128 v[88:91], v95 offset:22528\n\t"
    "s_waitcnt vmcnt(4)\n\t"
    "ds_add_u32 v117, v118 offset:8\n\t"
    "s_waitcnt vmcnt(0)\n\t"
    "s_branch L_awd_%=\n\t"
    "s_waitcnt vmcnt(4)\n\t"
    "ds_add_u32 v117, v118 offset:8\n\t"
    "s_branch L_await_%=\n\t"
    "L_aside_%=:\n\t"
    "s_setprio 3\n\t"
    "v_cmp_gt_u32 vcc, 48, v147\n\t"
    "v_mov_b32 v133, 0x40000000\n\t"
    "v_lshrrev_b32 v134, 3, v147\n\t"
    "v_bfe_u32 v135, v147, 2, 1\n\t"
    "v_and_b32 v130, 3, v147\n\t"
    "v_lshl_add_u32 v132, v135, 5, v130\n\t"
    "v_mul_u32_u24 v64, 400, v132\n\t"
    "v_lshl_add_u32 v64, v134, 5, v64\n\t"
    "s_movk_i32 s50, 0\n\t"
    "buffer_load_dwordx4 v[0:3], v64, %0, s50 offen offset:16\n\t"
    "buffer_load_dwordx4 v[4:7], v64, %0, s50 offen offset:32\n\t"
    "v_lshlrev_b32 v66, 14, v134\n\t"
    "v_lshl_add_u32 v66, v132, 4, v66\n\t"
    "v_lshlrev_b32 v68, 8, v134\n\t"
    "v_lshl_add_u32 v68, v135, 2, v68\n\t"
    "v_add_u32 v68, 130048, v68\n\t"
    "v_cndmask_b32 v68, v133, v68, vcc\n\t"
    "v_cndmask_b32 v66, v133, v66, vcc\n\t"
    "v_lshrrev_b32 v134, 2, v147\n\t"
    "v_bfe_u32 v135, v147, 1, 1\n\t"
    "v_and_b32 v130, 1, v147\n\t"
    "v_lshl_add_u32 v132, v135, 5, v130\n\t"
    "v_mul_u32_u24 v65, 400, v132\n\t"
    "v_lshl_add_u32 v65, v134, 5, v65\n\t"
    "v_lshlrev_b32 v67, 14, v134\n\t"
    "v_lshl_add_u32 v67, v132, 4, v67\n\t"
    "v_lshlrev_b32 v69, 8, v134\n\t"
    "v_lshl_add_u32 v69, v135, 2, v69\n\t"
    "v_add_u32 v69, 130048, v69\n\t"
    "v_cndmask_b32 v69, v133, v69, vcc\n\t"
    "v_cndmask_b32 v67, v133, v67, vcc\n\t"
    "s_barrier\n\t"
    "s_load_dwordx2 s[72:73], %6, 0\n\t"
    "s_load_dwordx2 s[88:89], %7, 0\n\t"
    "s_load_dword s91, %8, 0\n\t"
    "v_lshlrev_b32 v128, 4, v147\n\t"
    "v_add_u32 v128, 133120, v128\n\t"
    "v_and_b32 v130, 1, v147\n\t"
    "v_cndmask_b32 v128, v133, v128, vcc\n\t"
    "v_mul_u32_u24 v127, 7, v130\n\t"
    "v_cmp_eq_u32 vcc, 1, v130\n\t"
    "v_mov_b32 v126, 0x07060100\n\t"
    "v_mov_b32 v133, 0x05040100\n\t"
    "v_mov_b32 v136, 1\n\t"
    "v_cndmask_b32 v126, v126, v133, vcc\n\t"
    "s_mov_b32 s36, 0\n\t"
    "s_mov_b32 s37, 8000\n\t"
    "s_mov_b32 s38, 16000\n\t"
    "s_mov_b32 s39, 24000\n\t"
    "s_mov_b32 s40, 32000\n\t"
    "s_mov_b32 s41, 40000\n\t"
    "s_mov_b32 s42, 48000\n\t"
    "s_mov_b32 s43, 56000\n\t"
    "s_movk_i32 s44, 0xf8\n\t"
    "s_mov_b32 s45, 0x01010000\n\t"
    "s_mov_b32 s46, 0x020c000c\n\t"
    "s_lshr_b32 s49, %14, 4\n\t"
    "s_mul_i32 s49, s49, 212992\n\t"
    "s_and_b32 s50, %14, 15\n\t"
    "s_lshl_b32 s50, s50, 10\n\t"
    "s_add_u32 s49, s49, s50\n\t"
    "v_lshrrev_b32 v134, 4, v147\n\t"
    "v_bfe_u32 v135, v147, 2, 2\n\t"
    "v_and_b32 v130, 3, v147\n\t"
    "v_lshlrev_b32 v137, 17, v134\n\t"
    "v_lshl_add_u32 v137, v135, 10, v137\n\t"
    "v_lshl_add_u32 v137, v130, 4, v137\n\t"
    "v_add_u32 v138, 0x1000, v137\n\t"
    "v_mul_u32_u24 v141, 0x210, v134\n\t"
    "v_lshl_add_u32 v141, v135, 6, v141\n\t"
    "v_lshl_add_u32 v141, v130, 4, v141\n\t"
    "v_add_u32 v141, 134656, v141\n\t"
    "v_mul_u32_u24 v142, 0x210, v130\n\t"
    "v_lshl_add_u32 v142, v135, 6, v142\n\t"
    "v_lshl_add_u32 v142, v134, 3, v142\n\t"
    "v_add_u32 v142, 134656, v142\n\t"
    "s_mov_b32 s50, 102400\n\t"
    "v_mul_u32_u24 v139, s50, v134\n\t"
    "v_and_b32 v132, 15, v147\n\t"
    "v_lshl_add_u32 v139, v132, 4, v139\n\t"
    "v_add_u32 v140, 409600, v139\n\t"
    "s_lshr_b32 s50, %14, 4\n\t"
    "s_lshl_b32 s47, s50, 13\n\t"
    "s_lshl_b32 s48, s50, 8\n\t"
    "s_and_b32 s50, %14, 7\n\t"
    "s_lshl_b32 s51, s50, 7\n\t"
    "s_add_u32 s47, s47, s51\n\t"
    "s_mul_i32 s51, s50, 1638400\n\t"
    "s_add_u32 s48, s48, s51\n\t"
    "s_bfe_u32 s50, %14, 0x10003\n\t"
    "s_lshl_b32 s51, s50, 19\n\t"
    "s_add_u32 s47, s47, s51\n\t"
    "s_lshl_b32 s51, s50, 12\n\t"
    "s_add_u32 s48, s48, s51\n\t"
    "s_add_u32 s48, s48, 3407872\n\t"
    "s_waitcnt vmcnt(2)\n\t"
    "v_lshlrev_b32 v134, 20, v147\n\t"
    "v_add_u32 v134, 2068, v134\n\t"
    "ds_add_u32 v134, v136\n\t"
    "v_mov_b32 v110, 65664\n\t"
    "v_mov_b32 v111, 131136\n\t"
    "v_mov_b32 v112, 262176\n\t"
    "v_mov_b32 v113, 524304\n\t"
    "v_mov_b32 v114, 1048584\n\t"
    "v_mov_b32 v115, 2097156\n\t"
    "v_mov_b32 v116, 4194306\n\t"
    "v_mov_b32 v117, 8388609\n\t"
    "s_waitcnt vmcnt(0)\n\t"
    "v_lshl_add_u32 v76, v0, 3, s36\n\t"
    "v_lshl_add_u32 v77, v1, 3, s37\n\t"
    "v_lshl_add_u32 v78, v2, 3, s38\n\t"
    "v_lshl_add_u32 v79, v3, 3, s39\n\t"
    "v_lshl_add_u32 v80, v4, 3, s40\n\t"
    "v_lshl_add_u32 v81, v5, 3, s41\n\t"
    "v_lshl_add_u32 v82, v6, 3, s42\n\t"
    "v_lshl_add_u32 v83, v7, 3, s43\n\t"
    "v_and_or_b32 v92, v76, s44, v68\n\t"
    "ds_or_rtn_b32 v118, v92, v110\n\t"
    "v_and_or_b32 v93, v77, s44, v68\n\t"
    "ds_or_rtn_b32 v119, v93, v111\n\t"
    "v_and_or_b32 v94, v78, s44, v68\n\t"
    "ds_or_rtn_b32 v120, v94, v112\n\t"
    "v_and_or_b32 v95, v79, s44, v68\n\t"
    "ds_or_rtn_b32 v121, v95, v113\n\t"
    "v_and_or_b32 v96, v80, s44, v68\n\t"
    "ds_or_rtn_b32 v122, v96, v114\n\t"
    "v_and_or_b32 v97, v81, s44, v68\n\t"
    "ds_or_rtn_b32 v123, v97, v115\n\t"
    "v_and_or_b32 v98, v82, s44, v68\n\t"
    "ds_or_rtn_b32 v124, v98, v116\n\t"
    "v_and_or_b32 v99, v83, s44, v68\n\t"
    "ds_or_rtn_b32 v125, v99, v117\n\t"
    "v_lshl_or_b32 v164, v77, 16, v76\n\t"
    "v_lshl_or_b32 v165, v79, 16, v78\n\t"
    "v_lshl_or_b32 v166, v81, 16, v80\n\t"
    "v_lshl_or_b32 v167, v83, 16, v82\n\t"
    "s_movk_i32 s50, 0\n\t"
    "buffer_load_dwordx4 v[8:11], v64, %0, s50 offen offset:1616\n\t"
    "buffer_load_dwordx4 v[12:15], v64, %0, s50 offen offset:1632\n\t"
    "s_movk_i32 s50, 3200\n\t"
    "buffer_load_dwordx4 v[16:19], v64, %0, s50 offen offset:16\n\t"
    "buffer_load_dwordx4 v[20:23], v64, %0, s50 offen offset:32\n\t"
    "s_waitcnt vmcnt(2)\n\t"
    "v_lshl_add_u32 v84, v8, 3, s36\n\t"
    "v_lshl_add_u32 v85, v9, 3, s37\n\t"
    "v_lshl_add_u32 v86, v10, 3, s38\n\t"
    "v_lshl_add_u32 v87, v11, 3, s39\n\t"
    "v_lshl_add_u32 v88, v12, 3, s40\n\t"
    "v_lshl_add_u32 v89, v13, 3, s41\n\t"
    "v_lshl_add_u32 v90, v14, 3, s42\n\t"
    "v_lshl_add_u32 v91, v15, 3, s43\n\t"
    "s_waitcnt lgkmcnt(4)\n\t"
    "v_and_b32 v118, v118, v110\n\t"
    "v_perm_b32 v118, v118, v118, s46\n\t"
    "ds_or_b32 v92, v118\n\t"
    "v_and_b32 v119, v119, v111\n\t"
    "v_perm_b32 v119, v119, v119, s46\n\t"
    "ds_or_b32 v93, v119\n\t"
    "v_and_b32 v120, v120, v112\n\t"
    "v_perm_b32 v120, v120, v120, s46\n\t"
    "ds_or_b32 v94, v120\n\t"
    "v_and_b32 v121, v121, v113\n\t"
    "v_perm_b32 v121, v121, v121, s46\n\t"
    "ds_or_b32 v95, v121\n\t"
    "s_waitcnt lgkmcnt(4)\n\t"
    "v_and_b32 v122, v122, v114\n\t"
    "v_perm_b32 v122, v122, v122, s46\n\t"
    "ds_or_b32 v96, v122\n\t"
    "v_and_b32 v123, v123, v115\n\t"
    "v_perm_b32 v123, v123, v123, s46\n\t"
    "ds_or_b32 v97, v123\n\t"
    "v_and_b32 v124, v124, v116\n\t"
    "v_perm_b32 v124, v124, v124, s46\n\t"
    "ds_or_b32 v98, v124\n\t"
    "v_and_b32 v125, v125, v117\n\t"
    "v_perm_b32 v125, v125, v125, s46\n\t"
    "ds_or_b32 v99, v125\n\t"
    "buffer_store_dwordx4 v[164:167], v66, %12, s49 offen offset:0 sc1\n\t"
    "v_and_or_b32 v100, v84, s44, v68\n\t"
    "ds_read_b32 v100, v100\n\t"
    "v_and_or_b32 v101, v85, s44, v68\n\t"
    "ds_read_b32 v101, v101\n\t"
    "v_and_or_b32 v102, v86, s44, v68\n\t"
    "ds_read_b32 v102, v102\n\t"
    "v_and_or_b32 v103, v87, s44, v68\n\t"
    "ds_read_b32 v103, v103\n\t"
    "v_and_or_b32 v104, v88, s44, v68\n\t"
    "ds_read_b32 v104, v104\n\t"
    "v_and_or_b32 v105, v89, s44, v68\n\t"
    "ds_read_b32 v105, v105\n\t"
    "v_and_or_b32 v106, v90, s44, v68\n\t"
    "ds_read_b32 v106, v106\n\t"
    "v_and_or_b32 v107, v91, s44, v68\n\t"
    "ds_read_b32 v107, v107\n\t"
    "s_waitcnt vmcnt(1)\n\t"
    "v_lshl_add_u32 v76, v16, 3, s36\n\t"
    "v_lshl_add_u32 v77, v17, 3, s37\n\t"
    "v_lshl_add_u32 v78, v18, 3, s38\n\t"
    "v_lshl_add_u32 v79, v19, 3, s39\n\t"
    "v_lshl_add_u32 v80, v20, 3, s40\n\t"
    "v_lshl_add_u32 v81, v21, 3, s41\n\t"
    "v_lshl_add_u32 v82, v22, 3, s42\n\t"
    "v_lshl_add_u32 v83, v23, 3, s43\n\t"
    "s_movk_i32 s50, 3200\n\t"
    "buffer_load_dwordx4 v[24:27], v64, %0, s50 offen offset:1616\n\t"
    "buffer_load_dwordx4 v[28:31], v64, %0, s50 offen offset:1632\n\t"
    "s_movk_i32 s50, 6400\n\t"
    "buffer_load_dwordx4 v[32:35], v64, %0, s50 offen offset:16\n\t"
    "buffer_load_dwordx4 v[36:39], v64, %0, s50 offen offset:32\n\t"
    "v_mov_b32 v129, 0xffff0000\n\t"
    "s_waitcnt lgkmcnt(4)\n\t"
    "v_perm_b32 v100, v100, v84, v126\n\t"
    "v_perm_b32 v101, v101, v85, v126\n\t"
    "v_perm_b32 v102, v102, v86, v126\n\t"
    "v_perm_b32 v103, v103, v87, v126\n\t"
    "s_waitcnt lgkmcnt(0)\n\t"
    "v_perm_b32 v104, v104, v88, v126\n\t"
    "v_perm_b32 v105, v105, v89, v126\n\t"
    "v_perm_b32 v106, v106, v90, v126\n\t"
    "v_perm_b32 v107, v107, v91, v126\n\t"
    "v_max_u32 v108, v100, v101\n\t"
    "v_min_u32 v101, v100, v101\n\t"
    "v_max_u32 v100, v102, v103\n\t"
    "v_min_u32 v103, v102, v103\n\t"
    "v_max_u32 v102, v104, v105\n\t"
    "v_min_u32 v105, v104, v105\n\t"
    "v_max_u32 v104, v106, v107\n\t"
    "v_min_u32 v107, v106, v107\n\t"
    "v_max_u32 v106, v108, v100\n\t"
    "v_min_u32 v100, v108, v100\n\t"
    "v_max_u32 v108, v101, v103\n\t"
    "v_min_u32 v103, v101, v103\n\t"
    "v_max_u32 v101, v102, v104\n\t"
    "v_min_u32 v104, v102, v104\n\t"
    "v_max_u32 v102, v105, v107\n\t"
    "v_min_u32 v107, v105, v107\n\t"
    "v_bfi_b32 v130, v106, 0, v129\n\t"
    "v_cmp_eq_u32 vcc, 0, v130\n\t"
    "v_and_or_b32 v92, v106, s44, v68\n\t"
    "s_nop 0\n\t"
    "v_cndmask_b32 v130, v130, v129, vcc\n\t"
    "v_ffbl_b32 v131, v130\n\t"
    "v_and_b32 v134, 7, v131\n\t"
    "v_lshlrev_b32 v134, v134, s45\n\t"
    "v_xor_b32 v129, v129, v134\n\t"
    "v_xor_b32 v132, v131, v127\n\t"
    "v_bfi_b32 v130, v101, 0, v129\n\t"
    "v_cmp_eq_u32 vcc, 0, v130\n\t"
    "v_and_or_b32 v93, v101, s44, v68\n\t"
    "v_xor_b32 v133, 23, v132\n\t"
    "v_lshlrev_b32 v110, v132, v136\n\t"
    "v_and_b32 v132, 7, v132\n\t"
    "v_lshl_or_b32 v110, v136, v133, v110\n\t"
    "v_lshl_add_u32 v132, v132, 1, v128\n\t"
    "ds_or_rtn_b32 v118, v92, v110\n\t"
    "ds_write_b16 v132, v106\n\t"
    "v_cndmask_b32 v130, v130, v129, vcc\n\t"
    "v_ffbl_b32 v131, v130\n\t"
    "v_and_b32 v134, 7, v131\n\t"
    "v_lshlrev_b32 v134, v134, s45\n\t"
    "v_xor_b32 v129, v129, v134\n\t"
    "v_xor_b32 v132, v131, v127\n\t"
    "v_bfi_b32 v130, v108, 0, v129\n\t"
    "v_cmp_eq_u32 vcc, 0, v130\n\t"
    "v_and_or_b32 v94, v108, s44, v68\n\t"
    "v_xor_b32 v133, 23, v132\n\t"
    "v_lshlrev_b32 v111, v132, v136\n\t"
    "v_and_b32 v132, 7, v132\n\t"
    "v_lshl_or_b32 v111, v136, v133, v111\n\t"
    "v_lshl_add_u32 v132, v132, 1, v128\n\t"
    "ds_or_rtn_b32 v119, v93, v111\n\t"
    "ds_write_b16 v132, v101\n\t"
    "v_cndmask_b32 v130, v130, v129, vcc\n\t"
    "v_ffbl_b32 v131, v130\n\t"
    "v_and_b32 v134, 7, v131\n\t"
    "v_lshlrev_b32 v134, v134, s45\n\t"
    "v_xor_b32 v129, v129, v134\n\t"
    "v_xor_b32 v132, v131, v127\n\t"
    "v_bfi_b32 v130, v100, 0, v129\n\t"
    "v_cmp_eq_u32 vcc, 0, v130\n\t"
    "v_and_or_b32 v95, v100, s44, v68\n\t"
    "v_xor_b32 v133, 23, v132\n\t"
    "v_lshlrev_b32 v112, v132, v136\n\t"
    "v_and_b32 v132, 7, v132\n\t"
    "v_lshl_or_b32 v112, v136, v133, v112\n\t"
    "v_lshl_add_u32 v132, v132, 1, v128\n\t"
    "ds_or_rtn_b32 v120, v94, v112\n\t"
    "ds_write_b16 v132, v108\n\t"
    "v_cndmask_b32 v130, v130, v129, vcc\n\t"
    "v_ffbl_b32 v131, v130\n\t"
    "v_and_b32 v134, 7, v131\n\t"
    "v_lshlrev_b32 v134, v134, s45\n\t"
    "v_xor_b32 v129, v129, v134\n\t"
    "v_xor_b32 v132, v131, v127\n\t"
    "v_bfi_b32 v130, v102, 0, v129\n\t"
    "v_cmp_eq_u32 vcc, 0, v130\n\t"
    "v_and_or_b32 v96, v102, s44, v68\n\t"
    "v_xor_b32 v133, 23, v132\n\t"
    "v_lshlrev_b32 v113, v132, v136\n\t"
    "v_and_b32 v132, 7, v132\n\t"
    "v_lshl_or_b32 v113, v136, v133, v113\n\t"
    "v_lshl_add_u32 v132, v132, 1, v128\n\t"
    "ds_or_rtn_b32 v121, v95, v113\n\t"
    "ds_write_b16 v132, v100\n\t"
    "v_cndmask_b32 v130, v130, v129, vcc\n\t"
    "v_ffbl_b32 v131, v130\n\t"
    "v_and_b32 v134, 7, v131\n\t"
    "v_lshlrev_b32 v134, v134, s45\n\t"
    "v_xor_b32 v129, v129, v134\n\t"
    "v_xor_b32 v132, v131, v127\n\t"
    "s_waitcnt lgkmcnt(7)\n\t"
    "v_and_b32 v118, v118, v110\n\t"
    "v_perm_b32 v118, v118, v118, s46\n\t"
    "ds_or_b32 v92, v118\n\t"
    "v_bfi_b32 v130, v104, 0, v129\n\t"
    "v_cmp_eq_u32 vcc, 0, v130\n\t"
    "v_and_or_b32 v97, v104, s44, v68\n\t"
    "v_xor_b32 v133, 23, v132\n\t"
    "v_lshlrev_b32 v114, v132, v136\n\t"
    "v_and_b32 v132, 7, v132\n\t"
    "v_lshl_or_b32 v114, v136, v133, v114\n\t"
    "v_lshl_add_u32 v132, v132, 1, v128\n\t"
    "ds_or_rtn_b32 v122, v96, v114\n\t"
    "ds_write_b16 v132, v102\n\t"
    "v_cndmask_b32 v130, v130, v129, vcc\n\t"
    "v_ffbl_b32 v131, v130\n\t"
    "v_and_b32 v134, 7, v131\n\t"
    "v_lshlrev_b32 v134, v134, s45\n\t"
    "v_xor_b32 v129, v129, v134\n\t"
    "v_xor_b32 v132, v131, v127\n\t"
    "s_waitcnt lgkmcnt(8)\n\t"
    "v_and_b32 v119, v119, v111\n\t"
    "v_perm_b32 v119, v119, v119, s46\n\t"
    "ds_or_b32 v93, v119\n\t"
    "v_bfi_b32 v130, v103, 0, v129\n\t"
    "v_cmp_eq_u32 vcc, 0, v130\n\t"
    "v_and_or_b32 v98, v103, s44, v68\n\t"
    "v_xor_b32 v133, 23, v132\n\t"
    "v_lshlrev_b32 v115, v132, v136\n\t"
    "v_and_b32 v132, 7, v132\n\t"
    "v_lshl_or_b32 v115, v136, v133, v115\n\t"
    "v_lshl_add_u32 v132, v132, 1, v128\n\t"
    "ds_or_rtn_b32 v123, v97, v115\n\t"
    "ds_write_b16 v132, v104\n\t"
    "v_cndmask_b32 v130, v130, v129, vcc\n\t"
    "v_ffbl_b32 v131, v130\n\t"
    "v_and_b32 v134, 7, v131\n\t"
    "v_lshlrev_b32 v134, v134, s45\n\t"
    "v_xor_b32 v129, v129, v134\n\t"
    "v_xor_b32 v132, v131, v127\n\t"
    "s_waitcnt lgkmcnt(9)\n\t"
    "v_and_b32 v120, v120, v112\n\t"
    "v_perm_b32 v120, v120, v120, s46\n\t"
    "ds_or_b32 v94, v120\n\t"
    "v_bfi_b32 v130, v107, 0, v129\n\t"
    "v_cmp_eq_u32 vcc, 0, v130\n\t"
    "v_and_or_b32 v99, v107, s44, v68\n\t"
    "v_xor_b32 v133, 23, v132\n\t"
    "v_lshlrev_b32 v116, v132, v136\n\t"
    "v_and_b32 v132, 7, v132\n\t"
    "v_lshl_or_b32 v116, v136, v133, v116\n\t"
    "v_lshl_add_u32 v132, v132, 1, v128\n\t"
    "ds_or_rtn_b32 v124, v98, v116\n\t"
    "ds_write_b16 v132, v103\n\t"
    "v_cndmask_b32 v130, v130, v129, vcc\n\t"
    "v_ffbl_b32 v131, v130\n\t"
    "v_xor_b32 v132, v131, v127\n\t"
    "s_waitcnt lgkmcnt(10)\n\t"
    "v_and_b32 v121, v121, v113\n\t"
    "v_perm_b32 v121, v121, v121, s46\n\t"
    "ds_or_b32 v95, v121\n\t"
    "v_xor_b32 v133, 23, v132\n\t"
    "v_lshlrev_b32 v117, v132, v136\n\t"
    "v_and_b32 v132, 7, v132\n\t"
    "v_lshl_or_b32 v117, v136, v133, v117\n\t"
    "v_lshl_add_u32 v132, v132, 1, v128\n\t"
    "ds_or_rtn_b32 v125, v99, v117\n\t"
    "ds_write_b16 v132, v107\n\t"
    "s_waitcnt lgkmcnt(10)\n\t"
    "v_and_b32 v122, v122, v114\n\t"
    "v_perm_b32 v122, v122, v122, s46\n\t"
    "ds_or_b32 v96, v122\n\t"
    "ds_read_b128 v[72:75], v128\n\t"
    "v_and_or_b32 v100, v76, s44, v68\n\t"
    "ds_read_b32 v100, v100\n\t"
    "v_and_or_b32 v101, v77, s44, v68\n\t"
    "ds_read_b32 v101, v101\n\t"
    "v_and_or_b32 v102, v78, s44, v68\n\t"
    "ds_read_b32 v102, v102\n\t"
    "v_and_or_b32 v103, v79, s44, v68\n\t"
    "ds_read_b32 v103, v103\n\t"
    "v_and_or_b32 v104, v80, s44, v68\n\t"
    "ds_read_b32 v104, v104\n\t"
    "v_and_or_b32 v105, v81, s44, v68\n\t"
    "ds_read_b32 v105, v105\n\t"
    "v_and_or_b32 v106, v82, s44, v68\n\t"
    "ds_read_b32 v106, v106\n\t"
    "v_and_or_b32 v107, v83, s44, v68\n\t"
    "ds_read_b32 v107, v107\n\t"
    "s_waitcnt vmcnt(2)\n\t"
    "v_lshl_add_u32 v84, v24, 3, s36\n\t"
    "v_lshl_add_u32 v85, v25, 3, s37\n\t"
    "v_lshl_add_u32 v86, v26, 3, s38\n\t"
    "v_lshl_add_u32 v87, v27, 3, s39\n\t"
    "v_lshl_add_u32 v88, v28, 3, s40\n\t"
    "v_lshl_add_u32 v89, v29, 3, s41\n\t"
    "v_lshl_add_u32 v90, v30, 3, s42\n\t"
    "v_lshl_add_u32 v91, v31, 3, s43\n\t"
    "s_movk_i32 s50, 6400\n\t"
    "buffer_load_dwordx4 v[40:43], v65, %0, s50 offen offset:1616\n\t"
    "buffer_load_dwordx4 v[44:47], v65, %0, s50 offen offset:1632\n\t"
    "s_movk_i32 s50, 6400\n\t"
    "buffer_load_dwordx4 v[48:51], v65, %0, s50 offen offset:2416\n\t"
    "buffer_load_dwordx4 v[52:55], v65, %0, s50 offen offset:2432\n\t"
    "s_movk_i32 s50, 9600\n\t"
    "buffer_load_dwordx4 v[56:59], v65, %0, s50 offen offset:16\n\t"
    "buffer_load_dwordx4 v[60:63], v65, %0, s50 offen offset:32\n\t"
    "s_movk_i32 s50, 9600\n\t"
    "buffer_load_dwordx4 v[0:3], v65, %0, s50 offen offset:816\n\t"
    "buffer_load_dwordx4 v[4:7], v65, %0, s50 offen offset:832\n\t"
    "s_movk_i32 s50, 9600\n\t"
    "buffer_load_dwordx4 v[8:11], v65, %0, s50 offen offset:1616\n\t"
    "buffer_load_dwordx4 v[12:15], v65, %0, s50 offen offset:1632\n\t"
    "s_waitcnt lgkmcnt(11)\n\t"
    "v_and_b32 v123, v123, v115\n\t"
    "v_perm_b32 v123, v123, v123, s46\n\t"
    "ds_or_b32 v97, v123\n\t"
    "v_and_b32 v124, v124, v116\n\t"
    "v_perm_b32 v124, v124, v124, s46\n\t"
    "ds_or_b32 v98, v124\n\t"
    "v_and_b32 v125, v125, v117\n\t"
    "v_perm_b32 v125, v125, v125, s46\n\t"
    "ds_or_b32 v99, v125\n\t"
    "s_waitcnt lgkmcnt(11)\n\t"
    "buffer_store_dwordx4 v[72:75], v66, %12, s49 offen offset:64 sc1\n\t"
    "v_mov_b32 v129, 0xffff0000\n\t"
    "s_waitcnt lgkmcnt(7)\n\t"
    "v_perm_b32 v100, v100, v76, v126\n\t"
    "v_perm_b32 v101, v101, v77, v126\n\t"
    "v_perm_b32 v102, v102, v78, v126\n\t"
    "v_perm_b32 v103, v103, v79, v126\n\t"
    "s_waitcnt lgkmcnt(3)\n\t"
    "v_perm_b32 v104, v104, v80, v126\n\t"
    "v_perm_b32 v105, v105, v81, v126\n\t"
    "v_perm_b32 v106, v106, v82, v126\n\t"
    "v_perm_b32 v107, v107, v83, v126\n\t"
    "v_max_u32 v108, v100, v101\n\t"
    "v_min_u32 v101, v100, v101\n\t"
    "v_max_u32 v100, v102, v103\n\t"
    "v_min_u32 v103, v102, v103\n\t"
    "v_max_u32 v102, v104, v105\n\t"
    "v_min_u32 v105, v104, v105\n\t"
    "v_max_u32 v104, v106, v107\n\t"
    "v_min_u32 v107, v106, v107\n\t"
    "v_max_u32 v106, v108, v100\n\t"
    "v_min_u32 v100, v108, v100\n\t"
    "v_max_u32 v108, v101, v103\n\t"
    "v_min_u32 v103, v101, v103\n\t"
    "v_max_u32 v101, v102, v104\n\t"
    "v_min_u32 v104, v102, v104\n\t"
    "v_max_u32 v102, v105, v107\n\t"
    "v_min_u32 v107, v105, v107\n\t"
    "v_bfi_b32 v130, v106, 0, v129\n\t"
    "v_cmp_eq_u32 vcc, 0, v130\n\t"
    "v_and_or_b32 v92, v106, s44, v68\n\t"
    "s_nop 0\n\t"
    "v_cndmask_b32 v130, v130, v129, vcc\n\t"
    "v_ffbl_b32 v131, v130\n\t"
    "v_and_b32 v134, 7, v131\n\t"
    "v_lshlrev_b32 v134, v134, s45\n\t"
    "v_xor_b32 v129, v129, v134\n\t"
    "v_xor_b32 v132, v131, v127\n\t"
    "v_bfi_b32 v130, v101, 0, v129\n\t"
    "v_cmp_eq_u32 vcc, 0, v130\n\t"
    "v_and_or_b32 v93, v101, s44, v68\n\t"
    "v_xor_b32 v133, 23, v132\n\t"
    "v_lshlrev_b32 v110, v132, v136\n\t"
    "v_and_b32 v132, 7, v132\n\t"
    "v_lshl_or_b32 v110, v136, v133, v110\n\t"
    "v_lshl_add_u32 v132, v132, 1, v128\n\t"
    "ds_or_rtn_b32 v118, v92, v110\n\t"
    "ds_write_b16 v132, v106\n\t"
    "v_cndmask_b32 v130, v130, v129, vcc\n\t"
    "v_ffbl_b32 v131, v130\n\t"
    "v_and_b32 v134, 7, v131\n\t"
    "v_lshlrev_b32 v134, v134, s45\n\t"
    "v_xor_b32 v129, v129, v134\n\t"
    "v_xor_b32 v132, v131, v127\n\t"
    "v_bfi_b32 v130, v108, 0, v129\n\t"
    "v_cmp_eq_u32 vcc, 0, v130\n\t"
    "v_and_or_b32 v94, v108, s44, v68\n\t"
    "v_xor_b32 v133, 23, v132\n\t"
    "v_lshlrev_b32 v111, v132, v136\n\t"
    "v_and_b32 v132, 7, v132\n\t"
    "v_lshl_or_b32 v111, v136, v133, v111\n\t"
    "v_lshl_add_u32 v132, v132, 1, v128\n\t"
    "ds_or_rtn_b32 v119, v93, v111\n\t"
    "ds_write_b16 v132, v101\n\t"
    "v_cndmask_b32 v130, v130, v129, vcc\n\t"
    "v_ffbl_b32 v131, v130\n\t"
    "v_and_b32 v134, 7, v131\n\t"
    "v_lshlrev_b32 v134, v134, s45\n\t"
    "v_xor_b32 v129, v129, v134\n\t"
    "v_xor_b32 v132, v131, v127\n\t"
    "v_bfi_b32 v130, v100, 0, v129\n\t"
    "v_cmp_eq_u32 vcc, 0, v130\n\t"
    "v_and_or_b32 v95, v100, s44, v68\n\t"
    "v_xor_b32 v133, 23, v132\n\t"
    "v_lshlrev_b32 v112, v132, v136\n\t"
    "v_and_b32 v132, 7, v132\n\t"
    "v_lshl_or_b32 v112, v136, v133, v112\n\t"
    "v_lshl_add_u32 v132, v132, 1, v128\n\t"
    "ds_or_rtn_b32 v120, v94, v112\n\t"
    "ds_write_b16 v132, v108\n\t"
    "v_cndmask_b32 v130, v130, v129, vcc\n\t"
    "v_ffbl_b32 v131, v130\n\t"
    "v_and_b32 v134, 7, v131\n\t"
    "v_lshlrev_b32 v134, v134, s45\n\t"
    "v_xor_b32 v129, v129, v134\n\t"
    "v_xor_b32 v132, v131, v127\n\t"
    "v_bfi_b32 v130, v102, 0, v129\n\t"
    "v_cmp_eq_u32 vcc, 0, v130\n\t"
    "v_and_or_b32 v96, v102, s44, v68\n\t"
    "v_xor_b32 v133, 23, v132\n\t"
    "v_lshlrev_b32 v113, v132, v136\n\t"
    "v_and_b32 v132, 7, v132\n\t"
    "v_lshl_or_b32 v113, v136, v133, v113\n\t"
    "v_lshl_add_u32 v132, v132, 1, v128\n\t"
    "ds_or_rtn_b32 v121, v95, v113\n\t"
    "ds_write_b16 v132, v100\n\t"
    "v_cndmask_b32 v130, v130, v129, vcc\n\t"
    "v_ffbl_b32 v131, v130\n\t"
    "v_and_b32 v134, 7, v131\n\t"
    "v_lshlrev_b32 v134, v134, s45\n\t"
    "v_xor_b32 v129, v129, v134\n\t"
    "v_xor_b32 v132, v131, v127\n\t"
    "s_waitcnt lgkmcnt(7)\n\t"
    "v_and_b32 v118, v118, v110\n\t"
    "v_perm_b32 v118, v118, v118, s46\n\t"
    "ds_or_b32 v92, v118\n\t"
    "v_bfi_b32 v130, v104, 0, v129\n\t"
    "v_cmp_eq_u32 vcc, 0, v130\n\t"
    "v_and_or_b32 v97, v104, s44, v68\n\t"
    "v_xor_b32 v133, 23, v132\n\t"
    "v_lshlrev_b32 v114, v132, v136\n\t"
    "v_and_b32 v132, 7, v132\n\t"
    "v_lshl_or_b32 v114, v136, v133, v114\n\t"
    "v_lshl_add_u32 v132, v132, 1, v128\n\t"
    "ds_or_rtn_b32 v122, v96, v114\n\t"
    "ds_write_b16 v132, v102\n\t"
    "v_cndmask_b32 v130, v130, v129, vcc\n\t"
    "v_ffbl_b32 v131, v130\n\t"
    "v_and_b32 v134, 7, v131\n\t"
    "v_lshlrev_b32 v134, v134, s45\n\t"
    "v_xor_b32 v129, v129, v134\n\t"
    "v_xor_b32 v132, v131, v127\n\t"
    "s_waitcnt lgkmcnt(8)\n\t"
    "v_and_b32 v119, v119, v111\n\t"
    "v_perm_b32 v119, v119, v119, s46\n\t"
    "ds_or_b32 v93, v119\n\t"
    "v_bfi_b32 v130, v103, 0, v129\n\t"
    "v_cmp_eq_u32 vcc, 0, v130\n\t"
    "v_and_or_b32 v98, v103, s44, v68\n\t"
    "v_xor_b32 v133, 23, v132\n\t"
    "v_lshlrev_b32 v115, v132, v136\n\t"
    "v_and_b32 v132, 7, v132\n\t"
    "v_lshl_or_b32 v115, v136, v133, v115\n\t"
    "v_lshl_add_u32 v132, v132, 1, v128\n\t"
    "ds_or_rtn_b32 v123, v97, v115\n\t"
    "ds_write_b16 v132, v104\n\t"
    "v_cndmask_b32 v130, v130, v129, vcc\n\t"
    "v_ffbl_b32 v131, v130\n\t"
    "v_and_b32 v134, 7, v131\n\t"
    "v_lshlrev_b32 v134, v134, s45\n\t"
    "v_xor_b32 v129, v129, v134\n\t"
    "v_xor_b32 v132, v131, v127\n\t"
    "s_waitcnt lgkmcnt(9)\n\t"
    "v_and_b32 v120, v120, v112\n\t"
    "v_perm_b32 v120, v120, v120, s46\n\t"
    "ds_or_b32 v94, v120\n\t"
    "v_bfi_b32 v130, v107, 0, v129\n\t"
    "v_cmp_eq_u32 vcc, 0, v130\n\t"
    "v_and_or_b32 v99, v107, s44, v68\n\t"
    "v_xor_b32 v133, 23, v132\n\t"
    "v_lshlrev_b32 v116, v132, v136\n\t"
    "v_and_b32 v132, 7, v132\n\t"
    "v_lshl_or_b32 v116, v136, v133, v116\n\t"
    "v_lshl_add_u32 v132, v132, 1, v128\n\t"
    "ds_or_rtn_b32 v124, v98, v116\n\t"
    "ds_write_b16 v132, v103\n\t"
    "v_cndmask_b32 v130, v130, v129, vcc\n\t"
    "v_ffbl_b32 v131, v130\n\t"
    "v_xor_b32 v132, v131, v127\n\t"
    "s_waitcnt lgkmcnt(10)\n\t"
    "v_and_b32 v121, v121, v113\n\t"
    "v_perm_b32 v121, v121, v121, s46\n\t"
    "ds_or_b32 v95, v121\n\t"
    "v_xor_b32 v133, 23, v132\n\t"
    "v_lshlrev_b32 v117, v132, v136\n\t"
    "v_and_b32 v132, 7, v132\n\t"
    "v_lshl_or_b32 v117, v136, v133, v117\n\t"
    "v_lshl_add_u32 v132, v132, 1, v128\n\t"
    "ds_or_rtn_b32 v125, v99, v117\n\t"
    "ds_write_b16 v132, v107\n\t"
    "s_waitcnt lgkmcnt(10)\n\t"
    "v_and_b32 v122, v122, v114\n\t"
    "v_perm_b32 v122, v122, v122, s46\n\t"
    "ds_or_b32 v96, v122\n\t"
    "ds_read_b128 v[72:75], v128\n\t"
    "v_and_or_b32 v100, v84, s44, v68\n\t"
    "ds_read_b32 v100, v100\n\t"
    "v_and_or_b32 v101, v85, s44, v68\n\t"
    "ds_read_b32 v101, v101\n\t"
    "v_and_or_b32 v102, v86, s44, v68\n\t"
    "ds_read_b32 v102, v102\n\t"
    "v_and_or_b32 v103, v87, s44, v68\n\t"
    "ds_read_b32 v103, v103\n\t"
    "v_and_or_b32 v104, v88, s44, v68\n\t"
    "ds_read_b32 v104, v104\n\t"
    "v_and_or_b32 v105, v89, s44, v68\n\t"
    "ds_read_b32 v105, v105\n\t"
    "v_and_or_b32 v106, v90, s44, v68\n\t"
    "ds_read_b32 v106, v106\n\t"
    "v_and_or_b32 v107, v91, s44, v68\n\t"
    "ds_read_b32 v107, v107\n\t"
    "s_waitcnt vmcnt(11)\n\t"
    "v_lshl_add_u32 v76, v32, 3, s36\n\t"
    "v_lshl_add_u32 v77, v33, 3, s37\n\t"
    "v_lshl_add_u32 v78, v34, 3, s38\n\t"
    "v_lshl_add_u32 v79, v35, 3, s39\n\t"
    "v_lshl_add_u32 v80, v36, 3, s40\n\t"
    "v_lshl_add_u32 v81, v37, 3, s41\n\t"
    "v_lshl_add_u32 v82, v38, 3, s42\n\t"
    "v_lshl_add_u32 v83, v39, 3, s43\n\t"
    "s_movk_i32 s50, 9600\n\t"
    "buffer_load_dwordx4 v[16:19], v65, %0, s50 offen offset:2416\n\t"
    "buffer_load_dwordx4 v[20:23], v65, %0, s50 offen offset:2432\n\t"
    "v_mov_b32 v134, 1032\n\t"
    "L_asvsp8_%=:\n\t"
    "ds_read_b32 v130, v134\n\t"
    "s_waitcnt lgkmcnt(0)\n\t"
    "v_readfirstlane_b32 s50, v130\n\t"
    "s_nop 0\n\t"
    "s_cmp_lt_u32 s50, 6\n\t"
    "s_cbranch_scc1 L_asvsp8_%=\n\t"
    "s_mov_b64 s[96:97], %2\n\t"
    "s_and_b32 s97, s97, 0xffff\n\t"
    "buffer_load_dwordx4 v[156:159], v137, s[96:99], s47 offen offset:0\n\t"
    "buffer_load_dwordx4 v[160:163], v138, s[96:99], s47 offen offset:0\n\t"
    "buffer_load_dwordx4 v[164:167], v137, s[96:99], s47 offen offset:64\n\t"
    "buffer_load_dwordx4 v[168:171], v138, s[96:99], s47 offen offset:64\n\t"
    "s_add_u32 s47, s47, 0x100000\n\t"
    "buffer_load_dwordx4 v[172:175], v137, s[96:99], s47 offen offset:0\n\t"
    "buffer_load_dwordx4 v[176:179], v138, s[96:99], s47 offen offset:0\n\t"
    "buffer_load_dwordx4 v[180:183], v137, s[96:99], s47 offen offset:64\n\t"
    "buffer_load_dwordx4 v[184:187], v138, s[96:99], s47 offen offset:64\n\t"
    "s_add_u32 s47, s47, 0x100000\n\t"
    "buffer_load_dwordx4 v[188:191], v137, s[96:99], s47 offen offset:0\n\t"
    "buffer_load_dwordx4 v[192:195], v138, s[96:99], s47 offen offset:0\n\t"
    "buffer_load_dwordx4 v[196:199], v137, s[96:99], s47 offen offset:64\n\t"
    "buffer_load_dwordx4 v[200:203], v138, s[96:99], s47 offen offset:64\n\t"
    "s_add_u32 s47, s47, 0x100000\n\t"
    "v_and_b32 v123, v123, v115\n\t"
    "v_perm_b32 v123, v123, v123, s46\n\t"
    "ds_or_b32 v97, v123\n\t"
    "v_and_b32 v124, v124, v116\n\t"
    "v_perm_b32 v124, v124, v124, s46\n\t"
    "ds_or_b32 v98, v124\n\t"
    "v_and_b32 v125, v125, v117\n\t"
    "v_perm_b32 v125, v125, v125, s46\n\t"
    "ds_or_b32 v99, v125\n\t"
    "buffer_store_dwordx4 v[72:75], v66, %12, s49 offen offset:128 sc1\n\t"
    "v_mov_b32 v129, 0xffff0000\n\t"
    "v_perm_b32 v100, v100, v84, v126\n\t"
    "v_perm_b32 v101, v101, v85, v126\n\t"
    "v_perm_b32 v102, v102, v86, v126\n\t"
    "v_perm_b32 v103, v103, v87, v126\n\t"
    "v_perm_b32 v104, v104, v88, v126\n\t"
    "v_perm_b32 v105, v105, v89, v126\n\t"
    "v_perm_b32 v106, v106, v90, v126\n\t"
    "v_perm_b32 v107, v107, v91, v126\n\t"
    "v_max_u32 v108, v100, v101\n\t"
    "v_min_u32 v101, v100, v101\n\t"
    "v_max_u32 v100, v102, v103\n\t"
    "v_min_u32 v103, v102, v103\n\t"
    "v_max_u32 v102, v104, v105\n\t"
    "v_min_u32 v105, v104, v105\n\t"
    "v_max_u32 v104, v106, v107\n\t"
    "v_min_u32 v107, v106, v107\n\t"
    "v_max_u32 v106, v108, v100\n\t"
    "v_min_u32 v100, v108, v100\n\t"
    "v_max_u32 v108, v101, v103\n\t"
    "v_min_u32 v103, v101, v103\n\t"
    "v_max_u32 v101, v102, v104\n\t"
    "v_min_u32 v104, v102, v104\n\t"
    "v_max_u32 v102, v105, v107\n\t"
    "v_min_u32 v107, v105, v107\n\t"
    "v_bfi_b32 v130, v106, 0, v129\n\t"
    "v_cmp_eq_u32 vcc, 0, v130\n\t"
    "v_and_or_b32 v92, v106, s44, v68\n\t"
    "s_nop 0\n\t"
    "v_cndmask_b32 v130, v130, v129, vcc\n\t"
    "v_ffbl_b32 v131, v130\n\t"
    "v_and_b32 v134, 7, v131\n\t"
    "v_lshlrev_b32 v134, v134, s45\n\t"
    "v_xor_b32 v129, v129, v134\n\t"
    "v_xor_b32 v132, v131, v127\n\t"
    "v_bfi_b32 v130, v101, 0, v129\n\t"
    "v_cmp_eq_u32 vcc, 0, v130\n\t"
    "v_and_or_b32 v93, v101, s44, v68\n\t"
    "v_xor_b32 v133, 23, v132\n\t"
    "v_lshlrev_b32 v110, v132, v136\n\t"
    "v_and_b32 v132, 7, v132\n\t"
    "v_lshl_or_b32 v110, v136, v133, v110\n\t"
    "v_lshl_add_u32 v132, v132, 1, v128\n\t"
    "ds_or_rtn_b32 v118, v92, v110\n\t"
    "ds_write_b16 v132, v106\n\t"
    "v_cndmask_b32 v130, v130, v129, vcc\n\t"
    "v_ffbl_b32 v131, v130\n\t"
    "v_and_b32 v134, 7, v131\n\t"
    "v_lshlrev_b32 v134, v134, s45\n\t"
    "v_xor_b32 v129, v129, v134\n\t"
    "v_xor_b32 v132, v131, v127\n\t"
    "v_bfi_b32 v130, v108, 0, v129\n\t"
    "v_cmp_eq_u32 vcc, 0, v130\n\t"
    "v_and_or_b32 v94, v108, s44, v68\n\t"
    "v_xor_b32 v133, 23, v132\n\t"
    "v_lshlrev_b32 v111, v132, v136\n\t"
    "v_and_b32 v132, 7, v132\n\t"
    "v_lshl_or_b32 v111, v136, v133, v111\n\t"
    "v_lshl_add_u32 v132, v132, 1, v128\n\t"
    "ds_or_rtn_b32 v119, v93, v111\n\t"
    "ds_write_b16 v132, v101\n\t"
    "v_cndmask_b32 v130, v130, v129, vcc\n\t"
    "v_ffbl_b32 v131, v130\n\t"
    "v_and_b32 v134, 7, v131\n\t"
    "v_lshlrev_b32 v134, v134, s45\n\t"
    "v_xor_b32 v129, v129, v134\n\t"
    "v_xor_b32 v132, v131, v127\n\t"
    "v_bfi_b32 v130, v100, 0, v129\n\t"
    "v_cmp_eq_u32 vcc, 0, v130\n\t"
    "v_and_or_b32 v95, v100, s44, v68\n\t"
    "v_xor_b32 v133, 23, v132\n\t"
    "v_lshlrev_b32 v112, v132, v136\n\t"
    "v_and_b32 v132, 7, v132\n\t"
    "v_lshl_or_b32 v112, v136, v133, v112\n\t"
    "v_lshl_add_u32 v132, v132, 1, v128\n\t"
    "ds_or_rtn_b32 v120, v94, v112\n\t"
    "ds_write_b16 v132, v108\n\t"
    "v_cndmask_b32 v130, v130, v129, vcc\n\t"
    "v_ffbl_b32 v131, v130\n\t"
    "v_and_b32 v134, 7, v131\n\t"
    "v_lshlrev_b32 v134, v134, s45\n\t"
    "v_xor_b32 v129, v129, v134\n\t"
    "v_xor_b32 v132, v131, v127\n\t"
    "v_bfi_b32 v130, v102, 0, v129\n\t"
    "v_cmp_eq_u32 vcc, 0, v130\n\t"
    "v_and_or_b32 v96, v102, s44, v68\n\t"
    "v_xor_b32 v133, 23, v132\n\t"
    "v_lshlrev_b32 v113, v132, v136\n\t"
    "v_and_b32 v132, 7, v132\n\t"
    "v_lshl_or_b32 v113, v136, v133, v113\n\t"
    "v_lshl_add_u32 v132, v132, 1, v128\n\t"
    "ds_or_rtn_b32 v121, v95, v113\n\t"
    "ds_write_b16 v132, v100\n\t"
    "v_cndmask_b32 v130, v130, v129, vcc\n\t"
    "v_ffbl_b32 v131, v130\n\t"
    "v_and_b32 v134, 7, v131\n\t"
    "v_lshlrev_b32 v134, v134, s45\n\t"
    "v_xor_b32 v129, v129, v134\n\t"
    "v_xor_b32 v132, v131, v127\n\t"
    "s_waitcnt lgkmcnt(7)\n\t"
    "v_and_b32 v118, v118, v110\n\t"
    "v_perm_b32 v118, v118, v118, s46\n\t"
    "ds_or_b32 v92, v118\n\t"
    "v_bfi_b32 v130, v104, 0, v129\n\t"
    "v_cmp_eq_u32 vcc, 0, v130\n\t"
    "v_and_or_b32 v97, v104, s44, v68\n\t"
    "v_xor_b32 v133, 23, v132\n\t"
    "v_lshlrev_b32 v114, v132, v136\n\t"
    "v_and_b32 v132, 7, v132\n\t"
    "v_lshl_or_b32 v114, v136, v133, v114\n\t"
    "v_lshl_add_u32 v132, v132, 1, v128\n\t"
    "ds_or_rtn_b32 v122, v96, v114\n\t"
    "ds_write_b16 v132, v102\n\t"
    "v_cndmask_b32 v130, v130, v129, vcc\n\t"
    "v_ffbl_b32 v131, v130\n\t"
    "v_and_b32 v134, 7, v131\n\t"
    "v_lshlrev_b32 v134, v134, s45\n\t"
    "v_xor_b32 v129, v129, v134\n\t"
    "v_xor_b32 v132, v131, v127\n\t"
    "s_waitcnt lgkmcnt(8)\n\t"
    "v_and_b32 v119, v119, v111\n\t"
    "v_perm_b32 v119, v119, v119, s46\n\t"
    "ds_or_b32 v93, v119\n\t"
    "v_bfi_b32 v130, v103, 0, v129\n\t"
    "v_cmp_eq_u32 vcc, 0, v130\n\t"
    "v_and_or_b32 v98, v103, s44, v68\n\t"
    "v_xor_b32 v133, 23, v132\n\t"
    "v_lshlrev_b32 v115, v132, v136\n\t"
    "v_and_b32 v132, 7, v132\n\t"
    "v_lshl_or_b32 v115, v136, v133, v115\n\t"
    "v_lshl_add_u32 v132, v132, 1, v128\n\t"
    "ds_or_rtn_b32 v123, v97, v115\n\t"
    "ds_write_b16 v132, v104\n\t"
    "v_cndmask_b32 v130, v130, v129, vcc\n\t"
    "v_ffbl_b32 v131, v130\n\t"
    "v_and_b32 v134, 7, v131\n\t"
    "v_lshlrev_b32 v134, v134, s45\n\t"
    "v_xor_b32 v129, v129, v134\n\t"
    "v_xor_b32 v132, v131, v127\n\t"
    "s_waitcnt lgkmcnt(9)\n\t"
    "v_and_b32 v120, v120, v112\n\t"
    "v_perm_b32 v120, v120, v120, s46\n\t"
    "ds_or_b32 v94, v120\n\t"
    "v_bfi_b32 v130, v107, 0, v129\n\t"
    "v_cmp_eq_u32 vcc, 0, v130\n\t"
    "v_and_or_b32 v99, v107, s44, v68\n\t"
    "v_xor_b32 v133, 23, v132\n\t"
    "v_lshlrev_b32 v116, v132, v136\n\t"
    "v_and_b32 v132, 7, v132\n\t"
    "v_lshl_or_b32 v116, v136, v133, v116\n\t"
    "v_lshl_add_u32 v132, v132, 1, v128\n\t"
    "ds_or_rtn_b32 v124, v98, v116\n\t"
    "ds_write_b16 v132, v103\n\t"
    "v_cndmask_b32 v130, v130, v129, vcc\n\t"
    "v_ffbl_b32 v131, v130\n\t"
    "v_xor_b32 v132, v131, v127\n\t"
    "s_waitcnt lgkmcnt(10)\n\t"
    "v_and_b32 v121, v121, v113\n\t"
    "v_perm_b32 v121, v121, v121, s46\n\t"
    "ds_or_b32 v95, v121\n\t"
    "v_xor_b32 v133, 23, v132\n\t"
    "v_lshlrev_b32 v117, v132, v136\n\t"
    "v_and_b32 v132, 7, v132\n\t"
    "v_lshl_or_b32 v117, v136, v133, v117\n\t"
    "v_lshl_add_u32 v132, v132, 1, v128\n\t"
    "ds_or_rtn_b32 v125, v99, v117\n\t"
    "ds_write_b16 v132, v107\n\t"
    "s_waitcnt lgkmcnt(10)\n\t"
    "v_and_b32 v122, v122, v114\n\t"
    "v_perm_b32 v122, v122, v122, s46\n\t"
    "ds_or_b32 v96, v122\n\t"
    "ds_read_b128 v[72:75], v128\n\t"
    "v_and_or_b32 v100, v76, s44, v68\n\t"
    "ds_read_b32 v100, v100\n\t"
    "v_and_or_b32 v101, v77, s44, v68\n\t"
    "ds_read_b32 v101, v101\n\t"
    "v_and_or_b32 v102, v78, s44, v68\n\t"
    "ds_read_b32 v102, v102\n\t"
    "v_and_or_b32 v103, v79, s44, v68\n\t"
    "ds_read_b32 v103, v103\n\t"
    "v_and_or_b32 v104, v80, s44, v68\n\t"
    "ds_read_b32 v104, v104\n\t"
    "v_and_or_b32 v105, v81, s44, v68\n\t"
    "ds_read_b32 v105, v105\n\t"
    "v_and_or_b32 v106, v82, s44, v68\n\t"
    "ds_read_b32 v106, v106\n\t"
    "v_and_or_b32 v107, v83, s44, v68\n\t"
    "ds_read_b32 v107, v107\n\t"
    "s_waitcnt vmcnt(24)\n\t"
    "v_lshl_add_u32 v84, v40, 3, s36\n\t"
    "v_lshl_add_u32 v85, v41, 3, s37\n\t"
    "v_lshl_add_u32 v86, v42, 3, s38\n\t"
    "v_lshl_add_u32 v87, v43, 3, s39\n\t"
    "v_lshl_add_u32 v88, v44, 3, s40\n\t"
    "v_lshl_add_u32 v89, v45, 3, s41\n\t"
    "v_lshl_add_u32 v90, v46, 3, s42\n\t"
    "v_lshl_add_u32 v91, v47, 3, s43\n\t"
    "buffer_load_dwordx4 v[204:207], v137, s[96:99], s47 offen offset:0\n\t"
    "buffer_load_dwordx4 v[208:211], v138, s[96:99], s47 offen offset:0\n\t"
    "buffer_load_dwordx4 v[212:215], v137, s[96:99], s47 offen offset:64\n\t"
    "buffer_load_dwordx4 v[216:219], v138, s[96:99], s47 offen offset:64\n\t"
    "s_add_u32 s47, s47, 0x100000\n\t"
    "buffer_load_dwordx4 v[220:223], v137, s[96:99], s47 offen offset:0\n\t"
    "buffer_load_dwordx4 v[224:227], v138, s[96:99], s47 offen offset:0\n\t"
    "buffer_load_dwordx4 v[228:231], v137, s[96:99], s47 offen offset:64\n\t"
    "buffer_load_dwordx4 v[232:235], v138, s[96:99], s47 offen offset:64\n\t"
    "s_add_u32 s47, s47, 0x100000\n\t"
    "buffer_load_dwordx4 v[236:239], v137, s[96:99], s47 offen offset:0\n\t"
    "buffer_load_dwordx4 v[240:243], v138, s[96:99], s47 offen offset:0\n\t"
    "buffer_load_dwordx4 v[244:247], v137, s[96:99], s47 offen offset:64\n\t"
    "buffer_load_dwordx4 v[248:251], v138, s[96:99], s47 offen offset:64\n\t"
    "s_add_u32 s47, s47, 0x100000\n\t"
    "s_waitcnt lgkmcnt(11)\n\t"
    "v_and_b32 v123, v123, v115\n\t"
    "v_perm_b32 v123, v123, v123, s46\n\t"
    "ds_or_b32 v97, v123\n\t"
    "v_and_b32 v124, v124, v116\n\t"
    "v_perm_b32 v124, v124, v124, s46\n\t"
    "ds_or_b32 v98, v124\n\t"
    "v_and_b32 v125, v125, v117\n\t"
    "v_perm_b32 v125, v125, v125, s46\n\t"
    "ds_or_b32 v99, v125\n\t"
    "s_waitcnt lgkmcnt(11)\n\t"
    "buffer_store_dwordx4 v[72:75], v66, %12, s49 offen offset:192 sc1\n\t"
    "v_mov_b32 v129, 0xffff0000\n\t"
    "s_waitcnt lgkmcnt(7)\n\t"
    "v_perm_b32 v100, v100, v76, v126\n\t"
    "v_perm_b32 v101, v101, v77, v126\n\t"
    "v_perm_b32 v102, v102, v78, v126\n\t"
    "v_perm_b32 v103, v103, v79, v126\n\t"
    "s_waitcnt lgkmcnt(3)\n\t"
    "v_perm_b32 v104, v104, v80, v126\n\t"
    "v_perm_b32 v105, v105, v81, v126\n\t"
    "v_perm_b32 v106, v106, v82, v126\n\t"
    "v_perm_b32 v107, v107, v83, v126\n\t"
    "v_max_u32 v108, v100, v101\n\t"
    "v_min_u32 v101, v100, v101\n\t"
    "v_max_u32 v100, v102, v103\n\t"
    "v_min_u32 v103, v102, v103\n\t"
    "v_max_u32 v102, v104, v105\n\t"
    "v_min_u32 v105, v104, v105\n\t"
    "v_max_u32 v104, v106, v107\n\t"
    "v_min_u32 v107, v106, v107\n\t"
    "v_max_u32 v106, v108, v100\n\t"
    "v_min_u32 v100, v108, v100\n\t"
    "v_max_u32 v108, v101, v103\n\t"
    "v_min_u32 v103, v101, v103\n\t"
    "v_max_u32 v101, v102, v104\n\t"
    "v_min_u32 v104, v102, v104\n\t"
    "v_max_u32 v102, v105, v107\n\t"
    "v_min_u32 v107, v105, v107\n\t"
    "v_bfi_b32 v130, v106, 0, v129\n\t"
    "v_cmp_eq_u32 vcc, 0, v130\n\t"
    "v_and_or_b32 v92, v106, s44, v68\n\t"
    "s_nop 0\n\t"
    "v_cndmask_b32 v130, v130, v129, vcc\n\t"
    "v_ffbl_b32 v131, v130\n\t"
    "v_and_b32 v134, 7, v131\n\t"
    "v_lshlrev_b32 v134, v134, s45\n\t"
    "v_xor_b32 v129, v129, v134\n\t"
    "v_xor_b32 v132, v131, v127\n\t"
    "v_bfi_b32 v130, v101, 0, v129\n\t"
    "v_cmp_eq_u32 vcc, 0, v130\n\t"
    "v_and_or_b32 v93, v101, s44, v68\n\t"
    "v_xor_b32 v133, 23, v132\n\t"
    "v_lshlrev_b32 v110, v132, v136\n\t"
    "v_and_b32 v132, 7, v132\n\t"
    "v_lshl_or_b32 v110, v136, v133, v110\n\t"
    "v_lshl_add_u32 v132, v132, 1, v128\n\t"
    "ds_or_rtn_b32 v118, v92, v110\n\t"
    "ds_write_b16 v132, v106\n\t"
    "v_cndmask_b32 v130, v130, v129, vcc\n\t"
    "v_ffbl_b32 v131, v130\n\t"
    "v_and_b32 v134, 7, v131\n\t"
    "v_lshlrev_b32 v134, v134, s45\n\t"
    "v_xor_b32 v129, v129, v134\n\t"
    "v_xor_b32 v132, v131, v127\n\t"
    "v_bfi_b32 v130, v108, 0, v129\n\t"
    "v_cmp_eq_u32 vcc, 0, v130\n\t"
    "v_and_or_b32 v94, v108, s44, v68\n\t"
    "v_xor_b32 v133, 23, v132\n\t"
    "v_lshlrev_b32 v111, v132, v136\n\t"
    "v_and_b32 v132, 7, v132\n\t"
    "v_lshl_or_b32 v111, v136, v133, v111\n\t"
    "v_lshl_add_u32 v132, v132, 1, v128\n\t"
    "ds_or_rtn_b32 v119, v93, v111\n\t"
    "ds_write_b16 v132, v101\n\t"
    "v_cndmask_b32 v130, v130, v129, vcc\n\t"
    "v_ffbl_b32 v131, v130\n\t"
    "v_and_b32 v134, 7, v131\n\t"
    "v_lshlrev_b32 v134, v134, s45\n\t"
    "v_xor_b32 v129, v129, v134\n\t"
    "v_xor_b32 v132, v131, v127\n\t"
    "v_bfi_b32 v130, v100, 0, v129\n\t"
    "v_cmp_eq_u32 vcc, 0, v130\n\t"
    "v_and_or_b32 v95, v100, s44, v68\n\t"
    "v_xor_b32 v133, 23, v132\n\t"
    "v_lshlrev_b32 v112, v132, v136\n\t"
    "v_and_b32 v132, 7, v132\n\t"
    "v_lshl_or_b32 v112, v136, v133, v112\n\t"
    "v_lshl_add_u32 v132, v132, 1, v128\n\t"
    "ds_or_rtn_b32 v120, v94, v112\n\t"
    "ds_write_b16 v132, v108\n\t"
    "v_cndmask_b32 v130, v130, v129, vcc\n\t"
    "v_ffbl_b32 v131, v130\n\t"
    "v_and_b32 v134, 7, v131\n\t"
    "v_lshlrev_b32 v134, v134, s45\n\t"
    "v_xor_b32 v129, v129, v134\n\t"
    "v_xor_b32 v132, v131, v127\n\t"
    "v_bfi_b32 v130, v102, 0, v129\n\t"
    "v_cmp_eq_u32 vcc, 0, v130\n\t"
    "v_and_or_b32 v96, v102, s44, v68\n\t"
    "v_xor_b32 v133, 23, v132\n\t"
    "v_lshlrev_b32 v113, v132, v136\n\t"
    "v_and_b32 v132, 7, v132\n\t"
    "v_lshl_or_b32 v113, v136, v133, v113\n\t"
    "v_lshl_add_u32 v132, v132, 1, v128\n\t"
    "ds_or_rtn_b32 v121, v95, v113\n\t"
    "ds_write_b16 v132, v100\n\t"
    "v_cndmask_b32 v130, v130, v129, vcc\n\t"
    "v_ffbl_b32 v131, v130\n\t"
    "v_and_b32 v134, 7, v131\n\t"
    "v_lshlrev_b32 v134, v134, s45\n\t"
    "v_xor_b32 v129, v129, v134\n\t"
    "v_xor_b32 v132, v131, v127\n\t"
    "s_waitcnt lgkmcnt(7)\n\t"
    "v_and_b32 v118, v118, v110\n\t"
    "v_perm_b32 v118, v118, v118, s46\n\t"
    "ds_or_b32 v92, v118\n\t"
    "v_bfi_b32 v130, v104, 0, v129\n\t"
    "v_cmp_eq_u32 vcc, 0, v130\n\t"
    "v_and_or_b32 v97, v104, s44, v68\n\t"
    "v_xor_b32 v133, 23, v132\n\t"
    "v_lshlrev_b32 v114, v132, v136\n\t"
    "v_and_b32 v132, 7, v132\n\t"
    "v_lshl_or_b32 v114, v136, v133, v114\n\t"
    "v_lshl_add_u32 v132, v132, 1, v128\n\t"
    "ds_or_rtn_b32 v122, v96, v114\n\t"
    "ds_write_b16 v132, v102\n\t"
    "v_cndmask_b32 v130, v130, v129, vcc\n\t"
    "v_ffbl_b32 v131, v130\n\t"
    "v_and_b32 v134, 7, v131\n\t"
    "v_lshlrev_b32 v134, v134, s45\n\t"
    "v_xor_b32 v129, v129, v134\n\t"
    "v_xor_b32 v132, v131, v127\n\t"
    "s_waitcnt lgkmcnt(8)\n\t"
    "v_and_b32 v119, v119, v111\n\t"
    "v_perm_b32 v119, v119, v119, s46\n\t"
    "ds_or_b32 v93, v119\n\t"
    "v_bfi_b32 v130, v103, 0, v129\n\t"
    "v_cmp_eq_u32 vcc, 0, v130\n\t"
    "v_and_or_b32 v98, v103, s44, v68\n\t"
    "v_xor_b32 v133, 23, v132\n\t"
    "v_lshlrev_b32 v115, v132, v136\n\t"
    "v_and_b32 v132, 7, v132\n\t"
    "v_lshl_or_b32 v115, v136, v133, v115\n\t"
    "v_lshl_add_u32 v132, v132, 1, v128\n\t"
    "ds_or_rtn_b32 v123, v97, v115\n\t"
    "ds_write_b16 v132, v104\n\t"
    "v_cndmask_b32 v130, v130, v129, vcc\n\t"
    "v_ffbl_b32 v131, v130\n\t"
    "v_and_b32 v134, 7, v131\n\t"
    "v_lshlrev_b32 v134, v134, s45\n\t"
    "v_xor_b32 v129, v129, v134\n\t"
    "v_xor_b32 v132, v131, v127\n\t"
    "s_waitcnt lgkmcnt(9)\n\t"
    "v_and_b32 v120, v120, v112\n\t"
    "v_perm_b32 v120, v120, v120, s46\n\t"
    "ds_or_b32 v94, v120\n\t"
    "v_bfi_b32 v130, v107, 0, v129\n\t"
    "v_cmp_eq_u32 vcc, 0, v130\n\t"
    "v_and_or_b32 v99, v107, s44, v68\n\t"
    "v_xor_b32 v133, 23, v132\n\t"
    "v_lshlrev_b32 v116, v132, v136\n\t"
    "v_and_b32 v132, 7, v132\n\t"
    "v_lshl_or_b32 v116, v136, v133, v116\n\t"
    "v_lshl_add_u32 v132, v132, 1, v128\n\t"
    "ds_or_rtn_b32 v124, v98, v116\n\t"
    "ds_write_b16 v132, v103\n\t"
    "v_cndmask_b32 v130, v130, v129, vcc\n\t"
    "v_ffbl_b32 v131, v130\n\t"
    "v_xor_b32 v132, v131, v127\n\t"
    "s_waitcnt lgkmcnt(10)\n\t"
    "v_and_b32 v121, v121, v113\n\t"
    "v_perm_b32 v121, v121, v121, s46\n\t"
    "ds_or_b32 v95, v121\n\t"
    "v_xor_b32 v133, 23, v132\n\t"
    "v_lshlrev_b32 v117, v132, v136\n\t"
    "v_and_b32 v132, 7, v132\n\t"
    "v_lshl_or_b32 v117, v136, v133, v117\n\t"
    "v_lshl_add_u32 v132, v132, 1, v128\n\t"
    "ds_or_rtn_b32 v125, v99, v117\n\t"
    "ds_write_b16 v132, v107\n\t"
    "s_waitcnt lgkmcnt(10)\n\t"
    "v_and_b32 v122, v122, v114\n\t"
    "v_perm_b32 v122, v122, v122, s46\n\t"
    "ds_or_b32 v96, v122\n\t"
    "ds_read_b128 v[72:75], v128\n\t"
    "v_mov_b32 v134, 1036\n\t"
    "L_asvsp12_%=:\n\t"
    "ds_read_b32 v130, v134\n\t"
    "s_waitcnt lgkmcnt(0)\n\t"
    "v_readfirstlane_b32 s50, v130\n\t"
    "s_nop 0\n\t"
    "s_cmp_lt_u32 s50, 1\n\t"
    "s_cbranch_scc1 L_asvsp12_%=\n\t"
    "v_and_or_b32 v100, v84, s44, v69\n\t"
    "ds_read_b32 v100, v100\n\t"
    "v_and_or_b32 v101, v85, s44, v69\n\t"
    "ds_read_b32 v101, v101\n\t"
    "v_and_or_b32 v102, v86, s44, v69\n\t"
    "ds_read_b32 v102, v102\n\t"
    "v_and_or_b32 v103, v87, s44, v69\n\t"
    "ds_read_b32 v103, v103\n\t"
    "v_and_or_b32 v104, v88, s44, v69\n\t"
    "ds_read_b32 v104, v104\n\t"
    "v_and_or_b32 v105, v89, s44, v69\n\t"
    "ds_read_b32 v105, v105\n\t"
    "v_and_or_b32 v106, v90, s44, v69\n\t"
    "ds_read_b32 v106, v106\n\t"
    "v_and_or_b32 v107, v91, s44, v69\n\t"
    "ds_read_b32 v107, v107\n\t"
    "s_waitcnt vmcnt(35)\n\t"
    "v_lshl_add_u32 v76, v48, 3, s36\n\t"
    "v_lshl_add_u32 v77, v49, 3, s37\n\t"
    "v_lshl_add_u32 v78, v50, 3, s38\n\t"
    "v_lshl_add_u32 v79, v51, 3, s39\n\t"
    "v_lshl_add_u32 v80, v52, 3, s40\n\t"
    "v_lshl_add_u32 v81, v53, 3, s41\n\t"
    "v_lshl_add_u32 v82, v54, 3, s42\n\t"
    "v_lshl_add_u32 v83, v55, 3, s43\n\t"
    "s_lshl_b32 s52, %14, 2\n\t"
    "s_add_u32 s52, s52, 48242688\n\t"
    "v_mov_b32 v70, 2072\n\t"
    "buffer_load_dword v71, off, %12, s52 offset:1024 sc1\n\t"
    "ds_read_b32 v70, v70\n\t"
    "buffer_load_dwordx4 v[24:27], v137, s[96:99], s47 offen offset:0\n\t"
    "buffer_load_dwordx4 v[28:31], v138, s[96:99], s47 offen offset:0\n\t"
    "buffer_load_dwordx4 v[32:35], v137, s[96:99], s47 offen offset:64\n\t"
    "buffer_load_dwordx4 v[36:39], v138, s[96:99], s47 offen offset:64\n\t"
    "s_add_u32 s47, s47, 0x100000\n\t"
    "buffer_load_dwordx4 v[40:43], v137, s[96:99], s47 offen offset:0\n\t"
    "buffer_load_dwordx4 v[44:47], v138, s[96:99], s47 offen offset:0\n\t"
    "buffer_load_dwordx4 v[48:51], v137, s[96:99], s47 offen offset:64\n\t"
    "buffer_load_dwordx4 v[52:55], v138, s[96:99], s47 offen offset:64\n\t"
    "s_add_u32 s47, s47, 0x100000\n\t"
    "v_and_b32 v123, v123, v115\n\t"
    "v_perm_b32 v123, v123, v123, s46\n\t"
    "ds_or_b32 v97, v123\n\t"
    "v_and_b32 v124, v124, v116\n\t"
    "v_perm_b32 v124, v124, v124, s46\n\t"
    "ds_or_b32 v98, v124\n\t"
    "v_and_b32 v125, v125, v117\n\t"
    "v_perm_b32 v125, v125, v125, s46\n\t"
    "ds_or_b32 v99, v125\n\t"
    "buffer_store_dwordx4 v[72:75], v66, %12, s49 offen offset:256 sc1\n\t"
    "v_mov_b32 v129, 0xffff0000\n\t"
    "s_waitcnt lgkmcnt(8)\n\t"
    "v_perm_b32 v100, v100, v84, v126\n\t"
    "v_perm_b32 v101, v101, v85, v126\n\t"
    "v_perm_b32 v102, v102, v86, v126\n\t"
    "v_perm_b32 v103, v103, v87, v126\n\t"
    "s_waitcnt lgkmcnt(4)\n\t"
    "v_perm_b32 v104, v104, v88, v126\n\t"
    "v_perm_b32 v105, v105, v89, v126\n\t"
    "v_perm_b32 v106, v106, v90, v126\n\t"
    "v_perm_b32 v107, v107, v91, v126\n\t"
    "s_waitcnt vmcnt(34)\n\t"
    "ds_write_b128 v141, v[156:159] offset:0\n\t"
    "ds_write_b128 v141, v[160:163] offset:256\n\t"
    "ds_read_b64_tr_b16 v[156:157], v142 offset:0\n\t"
    "ds_read_b64_tr_b16 v[158:159], v142 offset:256\n\t"
    "ds_read_b64_tr_b16 v[160:161], v142 offset:32\n\t"
    "ds_read_b64_tr_b16 v[162:163], v142 offset:288\n\t"
    "s_waitcnt vmcnt(32)\n\t"
    "ds_write_b128 v141, v[164:167] offset:2112\n\t"
    "ds_write_b128 v141, v[168:171] offset:2368\n\t"
    "ds_read_b64_tr_b16 v[164:165], v142 offset:2112\n\t"
    "ds_read_b64_tr_b16 v[166:167], v142 offset:2368\n\t"
    "ds_read_b64_tr_b16 v[168:169], v142 offset:2144\n\t"
    "ds_read_b64_tr_b16 v[170:171], v142 offset:2400\n\t"
    "v_max_u32 v108, v100, v101\n\t"
    "v_min_u32 v101, v100, v101\n\t"
    "v_max_u32 v100, v102, v103\n\t"
    "v_min_u32 v103, v102, v103\n\t"
    "v_max_u32 v102, v104, v105\n\t"
    "v_min_u32 v105, v104, v105\n\t"
    "v_max_u32 v104, v106, v107\n\t"
    "v_min_u32 v107, v106, v107\n\t"
    "v_max_u32 v106, v108, v100\n\t"
    "v_min_u32 v100, v108, v100\n\t"
    "v_max_u32 v108, v101, v103\n\t"
    "v_min_u32 v103, v101, v103\n\t"
    "v_max_u32 v101, v102, v104\n\t"
    "v_min_u32 v104, v102, v104\n\t"
    "v_max_u32 v102, v105, v107\n\t"
    "v_min_u32 v107, v105, v107\n\t"
    "v_bfi_b32 v130, v106, 0, v129\n\t"
    "v_cmp_eq_u32 vcc, 0, v130\n\t"
    "v_and_or_b32 v92, v106, s44, v69\n\t"
    "s_nop 0\n\t"
    "v_cndmask_b32 v130, v130, v129, vcc\n\t"
    "v_ffbl_b32 v131, v130\n\t"
    "v_and_b32 v134, 7, v131\n\t"
    "v_lshlrev_b32 v134, v134, s45\n\t"
    "v_xor_b32 v129, v129, v134\n\t"
    "v_xor_b32 v132, v131, v127\n\t"
    "v_bfi_b32 v130, v101, 0, v129\n\t"
    "v_cmp_eq_u32 vcc, 0, v130\n\t"
    "v_and_or_b32 v93, v101, s44, v69\n\t"
    "v_xor_b32 v133, 23, v132\n\t"
    "v_lshlrev_b32 v110, v132, v136\n\t"
    "v_and_b32 v132, 7, v132\n\t"
    "v_lshl_or_b32 v110, v136, v133, v110\n\t"
    "v_lshl_add_u32 v132, v132, 1, v128\n\t"
    "ds_or_rtn_b32 v118, v92, v110\n\t"
    "ds_write_b16 v132, v106\n\t"
    "v_cndmask_b32 v130, v130, v129, vcc\n\t"
    "v_ffbl_b32 v131, v130\n\t"
    "v_and_b32 v134, 7, v131\n\t"
    "v_lshlrev_b32 v134, v134, s45\n\t"
    "v_xor_b32 v129, v129, v134\n\t"
    "v_xor_b32 v132, v131, v127\n\t"
    "v_bfi_b32 v130, v108, 0, v129\n\t"
    "v_cmp_eq_u32 vcc, 0, v130\n\t"
    "v_and_or_b32 v94, v108, s44, v69\n\t"
    "v_xor_b32 v133, 23, v132\n\t"
    "v_lshlrev_b32 v111, v132, v136\n\t"
    "v_and_b32 v132, 7, v132\n\t"
    "v_lshl_or_b32 v111, v136, v133, v111\n\t"
    "v_lshl_add_u32 v132, v132, 1, v128\n\t"
    "ds_or_rtn_b32 v119, v93, v111\n\t"
    "ds_write_b16 v132, v101\n\t"
    "v_cndmask_b32 v130, v130, v129, vcc\n\t"
    "v_ffbl_b32 v131, v130\n\t"
    "v_and_b32 v134, 7, v131\n\t"
    "v_lshlrev_b32 v134, v134, s45\n\t"
    "v_xor_b32 v129, v129, v134\n\t"
    "v_xor_b32 v132, v131, v127\n\t"
    "s_waitcnt lgkmcnt(10)\n\t"
    "buffer_store_dwordx4 v[156:159], v139, %12, s48 offen sc1\n\t"
    "buffer_store_dwordx4 v[160:163], v140, %12, s48 offen sc1\n\t"
    "s_add_u32 s48, s48, 819200\n\t"
    "s_waitcnt lgkmcnt(4)\n\t"
    "buffer_store_dwordx4 v[164:167], v139, %12, s48 offen sc1\n\t"
    "buffer_store_dwordx4 v[168:171], v140, %12, s48 offen sc1\n\t"
    "s_add_u32 s48, s48, -811008\n\t"
    "s_waitcnt vmcnt(34)\n\t"
    "ds_write_b128 v141, v[172:175] offset:0\n\t"
    "ds_write_b128 v141, v[176:179] offset:256\n\t"
    "ds_read_b64_tr_b16 v[172:173], v142 offset:0\n\t"
    "ds_read_b64_tr_b16 v[174:175], v142 offset:256\n\t"
    "ds_read_b64_tr_b16 v[176:177], v142 offset:32\n\t"
    "ds_read_b64_tr_b16 v[178:179], v142 offset:288\n\t"
    "s_waitcnt vmcnt(32)\n\t"
    "ds_write_b128 v141, v[180:183] offset:2112\n\t"
    "ds_write_b128 v141, v[184:187] offset:2368\n\t"
    "ds_read_b64_tr_b16 v[180:181], v142 offset:2112\n\t"
    "ds_read_b64_tr_b16 v[182:183], v142 offset:2368\n\t"
    "ds_read_b64_tr_b16 v[184:185], v142 offset:2144\n\t"
    "ds_read_b64_tr_b16 v[186:187], v142 offset:2400\n\t"
    "v_bfi_b32 v130, v100, 0, v129\n\t"
    "v_cmp_eq_u32 vcc, 0, v130\n\t"
    "v_and_or_b32 v95, v100, s44, v69\n\t"
    "v_xor_b32 v133, 23, v132\n\t"
    "v_lshlrev_b32 v112, v132, v136\n\t"
    "v_and_b32 v132, 7, v132\n\t"
    "v_lshl_or_b32 v112, v136, v133, v112\n\t"
    "v_lshl_add_u32 v132, v132, 1, v128\n\t"
    "ds_or_rtn_b32 v120, v94, v112\n\t"
    "ds_write_b16 v132, v108\n\t"
    "v_cndmask_b32 v130, v130, v129, vcc\n\t"
    "v_ffbl_b32 v131, v130\n\t"
    "v_and_b32 v134, 7, v131\n\t"
    "v_lshlrev_b32 v134, v134, s45\n\t"
    "v_xor_b32 v129, v129, v134\n\t"
    "v_xor_b32 v132, v131, v127\n\t"
    "v_bfi_b32 v130, v102, 0, v129\n\t"
    "v_cmp_eq_u32 vcc, 0, v130\n\t"
    "v_and_or_b32 v96, v102, s44, v69\n\t"
    "v_xor_b32 v133, 23, v132\n\t"
    "v_lshlrev_b32 v113, v132, v136\n\t"
    "v_and_b32 v132, 7, v132\n\t"
    "v_lshl_or_b32 v113, v136, v133, v113\n\t"
    "v_lshl_add_u32 v132, v132, 1, v128\n\t"
    "ds_or_rtn_b32 v121, v95, v113\n\t"
    "ds_write_b16 v132, v100\n\t"
    "v_cndmask_b32 v130, v130, v129, vcc\n\t"
    "v_ffbl_b32 v131, v130\n\t"
    "v_and_b32 v134, 7, v131\n\t"
    "v_lshlrev_b32 v134, v134, s45\n\t"
    "v_xor_b32 v129, v129, v134\n\t"
    "v_xor_b32 v132, v131, v127\n\t"
    "s_waitcnt lgkmcnt(15)\n\t"
    "v_and_b32 v118, v118, v110\n\t"
    "v_perm_b32 v118, v118, v118, s46\n\t"
    "ds_or_b32 v92, v118\n\t"
    "v_bfi_b32 v130, v104, 0, v129\n\t"
    "v_cmp_eq_u32 vcc, 0, v130\n\t"
    "v_and_or_b32 v97, v104, s44, v69\n\t"
    "v_xor_b32 v133, 23, v132\n\t"
    "v_lshlrev_b32 v114, v132, v136\n\t"
    "v_and_b32 v132, 7, v132\n\t"
    "v_lshl_or_b32 v114, v136, v133, v114\n\t"
    "v_lshl_add_u32 v132, v132, 1, v128\n\t"
    "ds_or_rtn_b32 v122, v96, v114\n\t"
    "ds_write_b16 v132, v102\n\t"
    "v_cndmask_b32 v130, v130, v129, vcc\n\t"
    "v_ffbl_b32 v131, v130\n\t"
    "v_and_b32 v134, 7, v131\n\t"
    "v_lshlrev_b32 v134, v134, s45\n\t"
    "v_xor_b32 v129, v129, v134\n\t"
    "v_xor_b32 v132, v131, v127\n\t"
    "v_and_b32 v119, v119, v111\n\t"
    "v_perm_b32 v119, v119, v119, s46\n\t"
    "ds_or_b32 v93, v119\n\t"
    "s_waitcnt lgkmcnt(14)\n\t"
    "buffer_store_dwordx4 v[172:175], v139, %12, s48 offen sc1\n\t"
    "buffer_store_dwordx4 v[176:179], v140, %12, s48 offen sc1\n\t"
    "s_add_u32 s48, s48, 819200\n\t"
    "s_waitcnt lgkmcnt(8)\n\t"
    "buffer_store_dwordx4 v[180:183], v139, %12, s48 offen sc1\n\t"
    "buffer_store_dwordx4 v[184:187], v140, %12, s48 offen sc1\n\t"
    "s_add_u32 s48, s48, -811008\n\t"
    "s_waitcnt vmcnt(34)\n\t"
    "ds_write_b128 v141, v[188:191] offset:0\n\t"
    "ds_write_b128 v141, v[192:195] offset:256\n\t"
    "ds_read_b64_tr_b16 v[188:189], v142 offset:0\n\t"
    "ds_read_b64_tr_b16 v[190:191], v142 offset:256\n\t"
    "ds_read_b64_tr_b16 v[192:193], v142 offset:32\n\t"
    "ds_read_b64_tr_b16 v[194:195], v142 offset:288\n\t"
    "s_waitcnt vmcnt(32)\n\t"
    "ds_write_b128 v141, v[196:199] offset:2112\n\t"
    "ds_write_b128 v141, v[200:203] offset:2368\n\t"
    "ds_read_b64_tr_b16 v[196:197], v142 offset:2112\n\t"
    "ds_read_b64_tr_b16 v[198:199], v142 offset:2368\n\t"
    "ds_read_b64_tr_b16 v[200:201], v142 offset:2144\n\t"
    "ds_read_b64_tr_b16 v[202:203], v142 offset:2400\n\t"
    "v_bfi_b32 v130, v103, 0, v129\n\t"
    "v_cmp_eq_u32 vcc, 0, v130\n\t"
    "v_and_or_b32 v98, v103, s44, v69\n\t"
    "v_xor_b32 v133, 23, v132\n\t"
    "v_lshlrev_b32 v115, v132, v136\n\t"
    "v_and_b32 v132, 7, v132\n\t"
    "v_lshl_or_b32 v115, v136, v133, v115\n\t"
    "v_lshl_add_u32 v132, v132, 1, v128\n\t"
    "ds_or_rtn_b32 v123, v97, v115\n\t"
    "ds_write_b16 v132, v104\n\t"
    "v_cndmask_b32 v130, v130, v129, vcc\n\t"
    "v_ffbl_b32 v131, v130\n\t"
    "v_and_b32 v134, 7, v131\n\t"
    "v_lshlrev_b32 v134, v134, s45\n\t"
    "v_xor_b32 v129, v129, v134\n\t"
    "v_xor_b32 v132, v131, v127\n\t"
    "s_waitcnt lgkmcnt(15)\n\t"
    "v_and_b32 v120, v120, v112\n\t"
    "v_perm_b32 v120, v120, v120, s46\n\t"
    "ds_or_b32 v94, v120\n\t"
    "v_bfi_b32 v130, v107, 0, v129\n\t"
    "v_cmp_eq_u32 vcc, 0, v130\n\t"
    "v_and_or_b32 v99, v107, s44, v69\n\t"
    "v_xor_b32 v133, 23, v132\n\t"
    "v_lshlrev_b32 v116, v132, v136\n\t"
    "v_and_b32 v132, 7, v132\n\t"
    "v_lshl_or_b32 v116, v136, v133, v116\n\t"
    "v_lshl_add_u32 v132, v132, 1, v128\n\t"
    "ds_or_rtn_b32 v124, v98, v116\n\t"
    "ds_write_b16 v132, v103\n\t"
    "v_cndmask_b32 v130, v130, v129, vcc\n\t"
    "v_ffbl_b32 v131, v130\n\t"
    "v_xor_b32 v132, v131, v127\n\t"
    "v_and_b32 v121, v121, v113\n\t"
    "v_perm_b32 v121, v121, v121, s46\n\t"
    "ds_or_b32 v95, v121\n\t"
    "v_xor_b32 v133, 23, v132\n\t"
    "v_lshlrev_b32 v117, v132, v136\n\t"
    "v_and_b32 v132, 7, v132\n\t"
    "v_lshl_or_b32 v117, v136, v133, v117\n\t"
    "v_lshl_add_u32 v132, v132, 1, v128\n\t"
    "ds_or_rtn_b32 v125, v99, v117\n\t"
    "ds_write_b16 v132, v107\n\t"
    "v_and_b32 v122, v122, v114\n\t"
    "v_perm_b32 v122, v122, v122, s46\n\t"
    "ds_or_b32 v96, v122\n\t"
    "ds_read_b128 v[72:75], v128\n\t"
    "s_waitcnt lgkmcnt(15)\n\t"
    "buffer_store_dwordx4 v[188:191], v139, %12, s48 offen sc1\n\t"
    "buffer_store_dwordx4 v[192:195], v140, %12, s48 offen sc1\n\t"
    "s_add_u32 s48, s48, 819200\n\t"
    "s_waitcnt lgkmcnt(10)\n\t"
    "buffer_store_dwordx4 v[196:199], v139, %12, s48 offen sc1\n\t"
    "buffer_store_dwordx4 v[200:203], v140, %12, s48 offen sc1\n\t"
    "s_add_u32 s48, s48, -811008\n\t"
    "v_and_or_b32 v100, v76, s44, v69\n\t"
    "ds_read_b32 v100, v100\n\t"
    "v_and_or_b32 v101, v77, s44, v69\n\t"
    "ds_read_b32 v101, v101\n\t"
    "v_and_or_b32 v102, v78, s44, v69\n\t"
    "ds_read_b32 v102, v102\n\t"
    "v_and_or_b32 v103, v79, s44, v69\n\t"
    "ds_read_b32 v103, v103\n\t"
    "v_and_or_b32 v104, v80, s44, v69\n\t"
    "ds_read_b32 v104, v104\n\t"
    "v_and_or_b32 v105, v81, s44, v69\n\t"
    "ds_read_b32 v105, v105\n\t"
    "v_and_or_b32 v106, v82, s44, v69\n\t"
    "ds_read_b32 v106, v106\n\t"
    "v_and_or_b32 v107, v83, s44, v69\n\t"
    "ds_read_b32 v107, v107\n\t"
    "v_lshl_add_u32 v84, v56, 3, s36\n\t"
    "v_lshl_add_u32 v85, v57, 3, s37\n\t"
    "v_lshl_add_u32 v86, v58, 3, s38\n\t"
    "v_lshl_add_u32 v87, v59, 3, s39\n\t"
    "v_lshl_add_u32 v88, v60, 3, s40\n\t"
    "v_lshl_add_u32 v89, v61, 3, s41\n\t"
    "v_lshl_add_u32 v90, v62, 3, s42\n\t"
    "v_lshl_add_u32 v91, v63, 3, s43\n\t"
    "v_mov_b32 v134, 1036\n\t"
    "L_asvsp12b_%=:\n\t"
    "ds_read_b32 v130, v134\n\t"
    "s_waitcnt lgkmcnt(0)\n\t"
    "v_readfirstlane_b32 s50, v130\n\t"
    "s_nop 0\n\t"
    "s_cmp_lt_u32 s50, 3\n\t"
    "s_cbranch_scc1 L_asvsp12b_%=\n\t"
    "s_and_b32 s50, %14, 7\n\t"
    "s_lshr_b32 s51, %14, 5\n\t"
    "s_add_u32 s50, s50, 8\n\t"
    "s_lshl_b32 s50, s50, 7\n\t"
    "s_lshl_b32 s51, s51, 7\n\t"
    "v_mov_b32 v132, s51\n\t"
    "v_writelane_b32 v132, s50, 0\n\t"
    "s_waitcnt vmcnt(0)\n\t"
    "v_cmp_ne_u32 vcc, v71, v70\n\t"
    "s_cbranch_vccz L_ahsok_%=\n\t"
    "s_mov_b32 s51, 1048576\n\t"
    "L_ahs_%=:\n\t"
    "buffer_load_dword v71, off, %12, s52 offset:1024 sc1\n\t"
    "s_sub_u32 s51, s51, 1\n\t"
    "s_waitcnt vmcnt(0)\n\t"
    "s_cmp_eq_u32 s51, 0\n\t"
    "s_cbranch_scc1 L_ahsok_%=\n\t"
    "v_cmp_ne_u32 vcc, v71, v70\n\t"
    "s_cbranch_vccnz L_ahs_%=\n\t"
    "L_ahsok_%=:\n\t"
    "s_mov_b32 s50, 48235520\n\t"
    "s_mov_b64 exec, 3\n\t"
    "buffer_atomic_add v136, v132, %12, s50 offen sc1\n\t"
    "s_mov_b64 exec, -1\n\t"
    "buffer_load_dwordx4 v[156:159], v137, s[96:99], s47 offen offset:0\n\t"
    "buffer_load_dwordx4 v[160:163], v138, s[96:99], s47 offen offset:0\n\t"
    "buffer_load_dwordx4 v[164:167], v137, s[96:99], s47 offen offset:64\n\t"
    "buffer_load_dwordx4 v[168:171], v138, s[96:99], s47 offen offset:64\n\t"
    "s_add_u32 s47, s47, 0x100000\n\t"
    "buffer_load_dwordx4 v[172:175], v137, s[96:99], s47 offen offset:0\n\t"
    "buffer_load_dwordx4 v[176:179], v138, s[96:99], s47 offen offset:0\n\t"
    "buffer_load_dwordx4 v[180:183], v137, s[96:99], s47 offen offset:64\n\t"
    "buffer_load_dwordx4 v[184:187], v138, s[96:99], s47 offen offset:64\n\t"
    "s_add_u32 s47, s47, 0x100000\n\t"
    "buffer_load_dwordx4 v[188:191], v137, s[96:99], s47 offen offset:0\n\t"
    "buffer_load_dwordx4 v[192:195], v138, s[96:99], s47 offen offset:0\n\t"
    "buffer_load_dwordx4 v[196:199], v137, s[96:99], s47 offen offset:64\n\t"
    "buffer_load_dwordx4 v[200:203], v138, s[96:99], s47 offen offset:64\n\t"
    "s_add_u32 s47, s47, 0x100000\n\t"
    "v_and_b32 v123, v123, v115\n\t"
    "v_perm_b32 v123, v123, v123, s46\n\t"
    "ds_or_b32 v97, v123\n\t"
    "v_and_b32 v124, v124, v116\n\t"
    "v_perm_b32 v124, v124, v124, s46\n\t"
    "ds_or_b32 v98, v124\n\t"
    "v_and_b32 v125, v125, v117\n\t"
    "v_perm_b32 v125, v125, v125, s46\n\t"
    "ds_or_b32 v99, v125\n\t"
    "buffer_store_dwordx4 v[72:75], v67, %12, s49 offen offset:320 sc1\n\t"
    "v_mov_b32 v129, 0xffff0000\n\t"
    "v_perm_b32 v100, v100, v76, v126\n\t"
    "v_perm_b32 v101, v101, v77, v126\n\t"
    "v_perm_b32 v102, v102, v78, v126\n\t"
    "v_perm_b32 v103, v103, v79, v126\n\t"
    "v_perm_b32 v104, v104, v80, v126\n\t"
    "v_perm_b32 v105, v105, v81, v126\n\t"
    "v_perm_b32 v106, v106, v82, v126\n\t"
    "v_perm_b32 v107, v107, v83, v126\n\t"
    "ds_write_b128 v141, v[204:207] offset:0\n\t"
    "ds_write_b128 v141, v[208:211] offset:256\n\t"
    "ds_read_b64_tr_b16 v[204:205], v142 offset:0\n\t"
    "ds_read_b64_tr_b16 v[206:207], v142 offset:256\n\t"
    "ds_read_b64_tr_b16 v[208:209], v142 offset:32\n\t"
    "ds_read_b64_tr_b16 v[210:211], v142 offset:288\n\t"
    "ds_write_b128 v141, v[212:215] offset:2112\n\t"
    "ds_write_b128 v141, v[216:219] offset:2368\n\t"
    "ds_read_b64_tr_b16 v[212:213], v142 offset:2112\n\t"
    "ds_read_b64_tr_b16 v[214:215], v142 offset:2368\n\t"
    "ds_read_b64_tr_b16 v[216:217], v142 offset:2144\n\t"
    "ds_read_b64_tr_b16 v[218:219], v142 offset:2400\n\t"
    "v_max_u32 v108, v100, v101\n\t"
    "v_min_u32 v101, v100, v101\n\t"
    "v_max_u32 v100, v102, v103\n\t"
    "v_min_u32 v103, v102, v103\n\t"
    "v_max_u32 v102, v104, v105\n\t"
    "v_min_u32 v105, v104, v105\n\t"
    "v_max_u32 v104, v106, v107\n\t"
    "v_min_u32 v107, v106, v107\n\t"
    "v_max_u32 v106, v108, v100\n\t"
    "v_min_u32 v100, v108, v100\n\t"
    "v_max_u32 v108, v101, v103\n\t"
    "v_min_u32 v103, v101, v103\n\t"
    "v_max_u32 v101, v102, v104\n\t"
    "v_min_u32 v104, v102, v104\n\t"
    "v_max_u32 v102, v105, v107\n\t"
    "v_min_u32 v107, v105, v107\n\t"
    "v_bfi_b32 v130, v106, 0, v129\n\t"
    "v_cmp_eq_u32 vcc, 0, v130\n\t"
    "v_and_or_b32 v92, v106, s44, v69\n\t"
    "s_nop 0\n\t"
    "v_cndmask_b32 v130, v130, v129, vcc\n\t"
    "v_ffbl_b32 v131, v130\n\t"
    "v_and_b32 v134, 7, v131\n\t"
    "v_lshlrev_b32 v134, v134, s45\n\t"
    "v_xor_b32 v129, v129, v134\n\t"
    "v_xor_b32 v132, v131, v127\n\t"
    "v_bfi_b32 v130, v101, 0, v129\n\t"
    "v_cmp_eq_u32 vcc, 0, v130\n\t"
    "v_and_or_b32 v93, v101, s44, v69\n\t"
    "v_xor_b32 v133, 23, v132\n\t"
    "v_lshlrev_b32 v110, v132, v136\n\t"
    "v_and_b32 v132, 7, v132\n\t"
    "v_lshl_or_b32 v110, v136, v133, v110\n\t"
    "v_lshl_add_u32 v132, v132, 1, v128\n\t"
    "ds_or_rtn_b32 v118, v92, v110\n\t"
    "ds_write_b16 v132, v106\n\t"
    "v_cndmask_b32 v130, v130, v129, vcc\n\t"
    "v_ffbl_b32 v131, v130\n\t"
    "v_and_b32 v134, 7, v131\n\t"
    "v_lshlrev_b32 v134, v134, s45\n\t"
    "v_xor_b32 v129, v129, v134\n\t"
    "v_xor_b32 v132, v131, v127\n\t"
    "v_bfi_b32 v130, v108, 0, v129\n\t"
    "v_cmp_eq_u32 vcc, 0, v130\n\t"
    "v_and_or_b32 v94, v108, s44, v69\n\t"
    "v_xor_b32 v133, 23, v132\n\t"
    "v_lshlrev_b32 v111, v132, v136\n\t"
    "v_and_b32 v132, 7, v132\n\t"
    "v_lshl_or_b32 v111, v136, v133, v111\n\t"
    "v_lshl_add_u32 v132, v132, 1, v128\n\t"
    "ds_or_rtn_b32 v119, v93, v111\n\t"
    "ds_write_b16 v132, v101\n\t"
    "v_cndmask_b32 v130, v130, v129, vcc\n\t"
    "v_ffbl_b32 v131, v130\n\t"
    "v_and_b32 v134, 7, v131\n\t"
    "v_lshlrev_b32 v134, v134, s45\n\t"
    "v_xor_b32 v129, v129, v134\n\t"
    "v_xor_b32 v132, v131, v127\n\t"
    "s_waitcnt lgkmcnt(10)\n\t"
    "buffer_store_dwordx4 v[204:207], v139, %12, s48 offen sc1\n\t"
    "buffer_store_dwordx4 v[208:211], v140, %12, s48 offen sc1\n\t"
    "s_add_u32 s48, s48, 819200\n\t"
    "s_waitcnt lgkmcnt(4)\n\t"
    "buffer_store_dwordx4 v[212:215], v139, %12, s48 offen sc1\n\t"
    "buffer_store_dwordx4 v[216:219], v140, %12, s48 offen sc1\n\t"
    "s_add_u32 s48, s48, -811008\n\t"
    "ds_write_b128 v141, v[220:223] offset:0\n\t"
    "ds_write_b128 v141, v[224:227] offset:256\n\t"
    "ds_read_b64_tr_b16 v[220:221], v142 offset:0\n\t"
    "ds_read_b64_tr_b16 v[222:223], v142 offset:256\n\t"
    "ds_read_b64_tr_b16 v[224:225], v142 offset:32\n\t"
    "ds_read_b64_tr_b16 v[226:227], v142 offset:288\n\t"
    "ds_write_b128 v141, v[228:231] offset:2112\n\t"
    "ds_write_b128 v141, v[232:235] offset:2368\n\t"
    "ds_read_b64_tr_b16 v[228:229], v142 offset:2112\n\t"
    "ds_read_b64_tr_b16 v[230:231], v142 offset:2368\n\t"
    "ds_read_b64_tr_b16 v[232:233], v142 offset:2144\n\t"
    "ds_read_b64_tr_b16 v[234:235], v142 offset:2400\n\t"
    "v_bfi_b32 v130, v100, 0, v129\n\t"
    "v_cmp_eq_u32 vcc, 0, v130\n\t"
    "v_and_or_b32 v95, v100, s44, v69\n\t"
    "v_xor_b32 v133, 23, v132\n\t"
    "v_lshlrev_b32 v112, v132, v136\n\t"
    "v_and_b32 v132, 7, v132\n\t"
    "v_lshl_or_b32 v112, v136, v133, v112\n\t"
    "v_lshl_add_u32 v132, v132, 1, v128\n\t"
    "ds_or_rtn_b32 v120, v94, v112\n\t"
    "ds_write_b16 v132, v108\n\t"
    "v_cndmask_b32 v130, v130, v129, vcc\n\t"
    "v_ffbl_b32 v131, v130\n\t"
    "v_and_b32 v134, 7, v131\n\t"
    "v_lshlrev_b32 v134, v134, s45\n\t"
    "v_xor_b32 v129, v129, v134\n\t"
    "v_xor_b32 v132, v131, v127\n\t"
    "v_bfi_b32 v130, v102, 0, v129\n\t"
    "v_cmp_eq_u32 vcc, 0, v130\n\t"
    "v_and_or_b32 v96, v102, s44, v69\n\t"
    "v_xor_b32 v133, 23, v132\n\t"
    "v_lshlrev_b32 v113, v132, v136\n\t"
    "v_and_b32 v132, 7, v132\n\t"
    "v_lshl_or_b32 v113, v136, v133, v113\n\t"
    "v_lshl_add_u32 v132, v132, 1, v128\n\t"
    "ds_or_rtn_b32 v121, v95, v113\n\t"
    "ds_write_b16 v132, v100\n\t"
    "v_cndmask_b32 v130, v130, v129, vcc\n\t"
    "v_ffbl_b32 v131, v130\n\t"
    "v_and_b32 v134, 7, v131\n\t"
    "v_lshlrev_b32 v134, v134, s45\n\t"
    "v_xor_b32 v129, v129, v134\n\t"
    "v_xor_b32 v132, v131, v127\n\t"
    "s_waitcnt lgkmcnt(15)\n\t"
    "v_and_b32 v118, v118, v110\n\t"
    "v_perm_b32 v118, v118, v118, s46\n\t"
    "ds_or_b32 v92, v118\n\t"
    "v_bfi_b32 v130, v104, 0, v129\n\t"
    "v_cmp_eq_u32 vcc, 0, v130\n\t"
    "v_and_or_b32 v97, v104, s44, v69\n\t"
    "v_xor_b32 v133, 23, v132\n\t"
    "v_lshlrev_b32 v114, v132, v136\n\t"
    "v_and_b32 v132, 7, v132\n\t"
    "v_lshl_or_b32 v114, v136, v133, v114\n\t"
    "v_lshl_add_u32 v132, v132, 1, v128\n\t"
    "ds_or_rtn_b32 v122, v96, v114\n\t"
    "ds_write_b16 v132, v102\n\t"
    "v_cndmask_b32 v130, v130, v129, vcc\n\t"
    "v_ffbl_b32 v131, v130\n\t"
    "v_and_b32 v134, 7, v131\n\t"
    "v_lshlrev_b32 v134, v134, s45\n\t"
    "v_xor_b32 v129, v129, v134\n\t"
    "v_xor_b32 v132, v131, v127\n\t"
    "v_and_b32 v119, v119, v111\n\t"
    "v_perm_b32 v119, v119, v119, s46\n\t"
    "ds_or_b32 v93, v119\n\t"
    "s_waitcnt lgkmcnt(14)\n\t"
    "buffer_store_dwordx4 v[220:223], v139, %12, s48 offen sc1\n\t"
    "buffer_store_dwordx4 v[224:227], v140, %12, s48 offen sc1\n\t"
    "s_add_u32 s48, s48, 819200\n\t"
    "s_waitcnt lgkmcnt(8)\n\t"
    "buffer_store_dwordx4 v[228:231], v139, %12, s48 offen sc1\n\t"
    "buffer_store_dwordx4 v[232:235], v140, %12, s48 offen sc1\n\t"
    "s_add_u32 s48, s48, -811008\n\t"
    "ds_write_b128 v141, v[236:239] offset:0\n\t"
    "ds_write_b128 v141, v[240:243] offset:256\n\t"
    "ds_read_b64_tr_b16 v[236:237], v142 offset:0\n\t"
    "ds_read_b64_tr_b16 v[238:239], v142 offset:256\n\t"
    "ds_read_b64_tr_b16 v[240:241], v142 offset:32\n\t"
    "ds_read_b64_tr_b16 v[242:243], v142 offset:288\n\t"
    "ds_write_b128 v141, v[244:247] offset:2112\n\t"
    "ds_write_b128 v141, v[248:251] offset:2368\n\t"
    "ds_read_b64_tr_b16 v[244:245], v142 offset:2112\n\t"
    "ds_read_b64_tr_b16 v[246:247], v142 offset:2368\n\t"
    "ds_read_b64_tr_b16 v[248:249], v142 offset:2144\n\t"
    "ds_read_b64_tr_b16 v[250:251], v142 offset:2400\n\t"
    "v_bfi_b32 v130, v103, 0, v129\n\t"
    "v_cmp_eq_u32 vcc, 0, v130\n\t"
    "v_and_or_b32 v98, v103, s44, v69\n\t"
    "v_xor_b32 v133, 23, v132\n\t"
    "v_lshlrev_b32 v115, v132, v136\n\t"
    "v_and_b32 v132, 7, v132\n\t"
    "v_lshl_or_b32 v115, v136, v133, v115\n\t"
    "v_lshl_add_u32 v132, v132, 1, v128\n\t"
    "ds_or_rtn_b32 v123, v97, v115\n\t"
    "ds_write_b16 v132, v104\n\t"
    "v_cndmask_b32 v130, v130, v129, vcc\n\t"
    "v_ffbl_b32 v131, v130\n\t"
    "v_and_b32 v134, 7, v131\n\t"
    "v_lshlrev_b32 v134, v134, s45\n\t"
    "v_xor_b32 v129, v129, v134\n\t"
    "v_xor_b32 v132, v131, v127\n\t"
    "s_waitcnt lgkmcnt(15)\n\t"
    "v_and_b32 v120, v120, v112\n\t"
    "v_perm_b32 v120, v120, v120, s46\n\t"
    "ds_or_b32 v94, v120\n\t"
    "v_bfi_b32 v130, v107, 0, v129\n\t"
    "v_cmp_eq_u32 vcc, 0, v130\n\t"
    "v_and_or_b32 v99, v107, s44, v69\n\t"
    "v_xor_b32 v133, 23, v132\n\t"
    "v_lshlrev_b32 v116, v132, v136\n\t"
    "v_and_b32 v132, 7, v132\n\t"
    "v_lshl_or_b32 v116, v136, v133, v116\n\t"
    "v_lshl_add_u32 v132, v132, 1, v128\n\t"
    "ds_or_rtn_b32 v124, v98, v116\n\t"
    "ds_write_b16 v132, v103\n\t"
    "v_cndmask_b32 v130, v130, v129, vcc\n\t"
    "v_ffbl_b32 v131, v130\n\t"
    "v_xor_b32 v132, v131, v127\n\t"
    "v_and_b32 v121, v121, v113\n\t"
    "v_perm_b32 v121, v121, v121, s46\n\t"
    "ds_or_b32 v95, v121\n\t"
    "v_xor_b32 v133, 23, v132\n\t"
    "v_lshlrev_b32 v117, v132, v136\n\t"
    "v_and_b32 v132, 7, v132\n\t"
    "v_lshl_or_b32 v117, v136, v133, v117\n\t"
    "v_lshl_add_u32 v132, v132, 1, v128\n\t"
    "ds_or_rtn_b32 v125, v99, v117\n\t"
    "ds_write_b16 v132, v107\n\t"
    "v_and_b32 v122, v122, v114\n\t"
    "v_perm_b32 v122, v122, v122, s46\n\t"
    "ds_or_b32 v96, v122\n\t"
    "ds_read_b128 v[72:75], v128\n\t"
    "v_and_or_b32 v100, v84, s44, v69\n\t"
    "ds_read_b32 v100, v100\n\t"
    "v_and_or_b32 v101, v85, s44, v69\n\t"
    "ds_read_b32 v101, v101\n\t"
    "v_and_or_b32 v102, v86, s44, v69\n\t"
    "ds_read_b32 v102, v102\n\t"
    "v_and_or_b32 v103, v87, s44, v69\n\t"
    "ds_read_b32 v103, v103\n\t"
    "v_and_or_b32 v104, v88, s44, v69\n\t"
    "ds_read_b32 v104, v104\n\t"
    "v_and_or_b32 v105, v89, s44, v69\n\t"
    "ds_read_b32 v105, v105\n\t"
    "v_and_or_b32 v106, v90, s44, v69\n\t"
    "ds_read_b32 v106, v106\n\t"
    "v_and_or_b32 v107, v91, s44, v69\n\t"
    "ds_read_b32 v107, v107\n\t"
    "v_lshl_add_u32 v76, v0, 3, s36\n\t"
    "v_lshl_add_u32 v77, v1, 3, s37\n\t"
    "v_lshl_add_u32 v78, v2, 3, s38\n\t"
    "v_lshl_add_u32 v79, v3, 3, s39\n\t"
    "v_lshl_add_u32 v80, v4, 3, s40\n\t"
    "v_lshl_add_u32 v81, v5, 3, s41\n\t"
    "v_lshl_add_u32 v82, v6, 3, s42\n\t"
    "v_lshl_add_u32 v83, v7, 3, s43\n\t"
    "s_waitcnt lgkmcnt(15)\n\t"
    "buffer_store_dwordx4 v[236:239], v139, %12, s48 offen sc1\n\t"
    "buffer_store_dwordx4 v[240:243], v140, %12, s48 offen sc1\n\t"
    "s_add_u32 s48, s48, 819200\n\t"
    "buffer_store_dwordx4 v[244:247], v139, %12, s48 offen sc1\n\t"
    "buffer_store_dwordx4 v[248:251], v140, %12, s48 offen sc1\n\t"
    "s_add_u32 s48, s48, -811008\n\t"
    "buffer_load_dwordx4 v[56:59], v137, s[96:99], s47 offen offset:0\n\t"
    "buffer_load_dwordx4 v[60:63], v138, s[96:99], s47 offen offset:0\n\t"
    "buffer_load_dwordx4 v[204:207], v137, s[96:99], s47 offen offset:64\n\t"
    "buffer_load_dwordx4 v[208:211], v138, s[96:99], s47 offen offset:64\n\t"
    "s_add_u32 s47, s47, 0x100000\n\t"
    "buffer_load_dwordx4 v[212:215], v137, s[96:99], s47 offen offset:0\n\t"
    "buffer_load_dwordx4 v[216:219], v138, s[96:99], s47 offen offset:0\n\t"
    "buffer_load_dwordx4 v[220:223], v137, s[96:99], s47 offen offset:64\n\t"
    "buffer_load_dwordx4 v[224:227], v138, s[96:99], s47 offen offset:64\n\t"
    "s_add_u32 s47, s47, 0x100000\n\t"
    "s_waitcnt lgkmcnt(11)\n\t"
    "v_and_b32 v123, v123, v115\n\t"
    "v_perm_b32 v123, v123, v123, s46\n\t"
    "ds_or_b32 v97, v123\n\t"
    "v_and_b32 v124, v124, v116\n\t"
    "v_perm_b32 v124, v124, v124, s46\n\t"
    "ds_or_b32 v98, v124\n\t"
    "v_and_b32 v125, v125, v117\n\t"
    "v_perm_b32 v125, v125, v125, s46\n\t"
    "ds_or_b32 v99, v125\n\t"
    "s_waitcnt lgkmcnt(11)\n\t"
    "buffer_store_dwordx4 v[72:75], v67, %12, s49 offen offset:352 sc1\n\t"
    "v_mov_b32 v129, 0xffff0000\n\t"
    "s_waitcnt lgkmcnt(7)\n\t"
    "v_perm_b32 v100, v100, v84, v126\n\t"
    "v_perm_b32 v101, v101, v85, v126\n\t"
    "v_perm_b32 v102, v102, v86, v126\n\t"
    "v_perm_b32 v103, v103, v87, v126\n\t"
    "s_waitcnt lgkmcnt(3)\n\t"
    "v_perm_b32 v104, v104, v88, v126\n\t"
    "v_perm_b32 v105, v105, v89, v126\n\t"
    "v_perm_b32 v106, v106, v90, v126\n\t"
    "v_perm_b32 v107, v107, v91, v126\n\t"
    "ds_write_b128 v141, v[24:27] offset:0\n\t"
    "ds_write_b128 v141, v[28:31] offset:256\n\t"
    "ds_read_b64_tr_b16 v[24:25], v142 offset:0\n\t"
    "ds_read_b64_tr_b16 v[26:27], v142 offset:256\n\t"
    "ds_read_b64_tr_b16 v[28:29], v142 offset:32\n\t"
    "ds_read_b64_tr_b16 v[30:31], v142 offset:288\n\t"
    "ds_write_b128 v141, v[32:35] offset:2112\n\t"
    "ds_write_b128 v141, v[36:39] offset:2368\n\t"
    "ds_read_b64_tr_b16 v[32:33], v142 offset:2112\n\t"
    "ds_read_b64_tr_b16 v[34:35], v142 offset:2368\n\t"
    "ds_read_b64_tr_b16 v[36:37], v142 offset:2144\n\t"
    "ds_read_b64_tr_b16 v[38:39], v142 offset:2400\n\t"
    "v_max_u32 v108, v100, v101\n\t"
    "v_min_u32 v101, v100, v101\n\t"
    "v_max_u32 v100, v102, v103\n\t"
    "v_min_u32 v103, v102, v103\n\t"
    "v_max_u32 v102, v104, v105\n\t"
    "v_min_u32 v105, v104, v105\n\t"
    "v_max_u32 v104, v106, v107\n\t"
    "v_min_u32 v107, v106, v107\n\t"
    "v_max_u32 v106, v108, v100\n\t"
    "v_min_u32 v100, v108, v100\n\t"
    "v_max_u32 v108, v101, v103\n\t"
    "v_min_u32 v103, v101, v103\n\t"
    "v_max_u32 v101, v102, v104\n\t"
    "v_min_u32 v104, v102, v104\n\t"
    "v_max_u32 v102, v105, v107\n\t"
    "v_min_u32 v107, v105, v107\n\t"
    "v_bfi_b32 v130, v106, 0, v129\n\t"
    "v_cmp_eq_u32 vcc, 0, v130\n\t"
    "v_and_or_b32 v92, v106, s44, v69\n\t"
    "s_nop 0\n\t"
    "v_cndmask_b32 v130, v130, v129, vcc\n\t"
    "v_ffbl_b32 v131, v130\n\t"
    "v_and_b32 v134, 7, v131\n\t"
    "v_lshlrev_b32 v134, v134, s45\n\t"
    "v_xor_b32 v129, v129, v134\n\t"
    "v_xor_b32 v132, v131, v127\n\t"
    "v_bfi_b32 v130, v101, 0, v129\n\t"
    "v_cmp_eq_u32 vcc, 0, v130\n\t"
    "v_and_or_b32 v93, v101, s44, v69\n\t"
    "v_xor_b32 v133, 23, v132\n\t"
    "v_lshlrev_b32 v110, v132, v136\n\t"
    "v_and_b32 v132, 7, v132\n\t"
    "v_lshl_or_b32 v110, v136, v133, v110\n\t"
    "v_lshl_add_u32 v132, v132, 1, v128\n\t"
    "ds_or_b32 v92, v110\n\t"
    "ds_write_b16 v132, v106\n\t"
    "v_cndmask_b32 v130, v130, v129, vcc\n\t"
    "v_ffbl_b32 v131, v130\n\t"
    "v_and_b32 v134, 7, v131\n\t"
    "v_lshlrev_b32 v134, v134, s45\n\t"
    "v_xor_b32 v129, v129, v134\n\t"
    "v_xor_b32 v132, v131, v127\n\t"
    "v_bfi_b32 v130, v108, 0, v129\n\t"
    "v_cmp_eq_u32 vcc, 0, v130\n\t"
    "v_and_or_b32 v94, v108, s44, v69\n\t"
    "v_xor_b32 v133, 23, v132\n\t"
    "v_lshlrev_b32 v111, v132, v136\n\t"
    "v_and_b32 v132, 7, v132\n\t"
    "v_lshl_or_b32 v111, v136, v133, v111\n\t"
    "v_lshl_add_u32 v132, v132, 1, v128\n\t"
    "ds_or_b32 v93, v111\n\t"
    "ds_write_b16 v132, v101\n\t"
    "v_cndmask_b32 v130, v130, v129, vcc\n\t"
    "v_ffbl_b32 v131, v130\n\t"
    "v_and_b32 v134, 7, v131\n\t"
    "v_lshlrev_b32 v134, v134, s45\n\t"
    "v_xor_b32 v129, v129, v134\n\t"
    "v_xor_b32 v132, v131, v127\n\t"
    "s_waitcnt lgkmcnt(10)\n\t"
    "buffer_store_dwordx4 v[24:27], v139, %12, s48 offen sc1\n\t"
    "buffer_store_dwordx4 v[28:31], v140, %12, s48 offen sc1\n\t"
    "s_add_u32 s48, s48, 819200\n\t"
    "s_waitcnt lgkmcnt(4)\n\t"
    "buffer_store_dwordx4 v[32:35], v139, %12, s48 offen sc1\n\t"
    "buffer_store_dwordx4 v[36:39], v140, %12, s48 offen sc1\n\t"
    "s_add_u32 s48, s48, -811008\n\t"
    "ds_write_b128 v141, v[40:43] offset:0\n\t"
    "ds_write_b128 v141, v[44:47] offset:256\n\t"
    "ds_read_b64_tr_b16 v[40:41], v142 offset:0\n\t"
    "ds_read_b64_tr_b16 v[42:43], v142 offset:256\n\t"
    "ds_read_b64_tr_b16 v[44:45], v142 offset:32\n\t"
    "ds_read_b64_tr_b16 v[46:47], v142 offset:288\n\t"
    "ds_write_b128 v141, v[48:51] offset:2112\n\t"
    "ds_write_b128 v141, v[52:55] offset:2368\n\t"
    "ds_read_b64_tr_b16 v[48:49], v142 offset:2112\n\t"
    "ds_read_b64_tr_b16 v[50:51], v142 offset:2368\n\t"
    "ds_read_b64_tr_b16 v[52:53], v142 offset:2144\n\t"
    "ds_read_b64_tr_b16 v[54:55], v142 offset:2400\n\t"
    "v_bfi_b32 v130, v100, 0, v129\n\t"
    "v_cmp_eq_u32 vcc, 0, v130\n\t"
    "v_and_or_b32 v95, v100, s44, v69\n\t"
    "v_xor_b32 v133, 23, v132\n\t"
    "v_lshlrev_b32 v112, v132, v136\n\t"
    "v_and_b32 v132, 7, v132\n\t"
    "v_lshl_or_b32 v112, v136, v133, v112\n\t"
    "v_lshl_add_u32 v132, v132, 1, v128\n\t"
    "ds_or_b32 v94, v112\n\t"
    "ds_write_b16 v132, v108\n\t"
    "v_cndmask_b32 v130, v130, v129, vcc\n\t"
    "v_ffbl_b32 v131, v130\n\t"
    "v_and_b32 v134, 7, v131\n\t"
    "v_lshlrev_b32 v134, v134, s45\n\t"
    "v_xor_b32 v129, v129, v134\n\t"
    "v_xor_b32 v132, v131, v127\n\t"
    "v_bfi_b32 v130, v102, 0, v129\n\t"
    "v_cmp_eq_u32 vcc, 0, v130\n\t"
    "v_and_or_b32 v96, v102, s44, v69\n\t"
    "v_xor_b32 v133, 23, v132\n\t"
    "v_lshlrev_b32 v113, v132, v136\n\t"
    "v_and_b32 v132, 7, v132\n\t"
    "v_lshl_or_b32 v113, v136, v133, v113\n\t"
    "v_lshl_add_u32 v132, v132, 1, v128\n\t"
    "ds_or_b32 v95, v113\n\t"
    "ds_write_b16 v132, v100\n\t"
    "v_cndmask_b32 v130, v130, v129, vcc\n\t"
    "v_ffbl_b32 v131, v130\n\t"
    "v_and_b32 v134, 7, v131\n\t"
    "v_lshlrev_b32 v134, v134, s45\n\t"
    "v_xor_b32 v129, v129, v134\n\t"
    "v_xor_b32 v132, v131, v127\n\t"
    "v_mbcnt_lo_u32_b32 v70, -1, 0\n\t"
    "v_mbcnt_hi_u32_b32 v70, -1, v70\n\t"
    "v_add_u32 v70, 8, v70\n\t"
    "s_and_b32 s50, %14, 7\n\t"
    "s_add_u32 s50, s50, 16\n\t"
    "v_writelane_b32 v70, s50, 8\n\t"
    "v_mov_b32 v148, 32\n\t"
    "v_lshlrev_b32 v70, 7, v70\n\t"
    "s_mov_b32 s52, 48234496\n\t"
    "s_mov_b64 exec, 0x1ff\n\t"
    "buffer_load_dword v71, v70, %12, s52 offen sc1\n\t"
    "s_mov_b64 exec, -1\n\t"
    "v_bfi_b32 v130, v104, 0, v129\n\t"
    "v_cmp_eq_u32 vcc, 0, v130\n\t"
    "v_and_or_b32 v97, v104, s44, v69\n\t"
    "v_xor_b32 v133, 23, v132\n\t"
    "v_lshlrev_b32 v114, v132, v136\n\t"
    "v_and_b32 v132, 7, v132\n\t"
    "v_lshl_or_b32 v114, v136, v133, v114\n\t"
    "v_lshl_add_u32 v132, v132, 1, v128\n\t"
    "ds_or_b32 v96, v114\n\t"
    "ds_write_b16 v132, v102\n\t"
    "v_cndmask_b32 v130, v130, v129, vcc\n\t"
    "v_ffbl_b32 v131, v130\n\t"
    "v_and_b32 v134, 7, v131\n\t"
    "v_lshlrev_b32 v134, v134, s45\n\t"
    "v_xor_b32 v129, v129, v134\n\t"
    "v_xor_b32 v132, v131, v127\n\t"
    "s_waitcnt lgkmcnt(12)\n\t"
    "buffer_store_dwordx4 v[40:43], v139, %12, s48 offen sc1\n\t"
    "buffer_store_dwordx4 v[44:47], v140, %12, s48 offen sc1\n\t"
    "s_add_u32 s48, s48, 819200\n\t"
    "s_waitcnt lgkmcnt(6)\n\t"
    "buffer_store_dwordx4 v[48:51], v139, %12, s48 offen sc1\n\t"
    "buffer_store_dwordx4 v[52:55], v140, %12, s48 offen sc1\n\t"
    "s_add_u32 s48, s48, -811008\n\t"
    "v_bfi_b32 v130, v103, 0, v129\n\t"
    "v_cmp_eq_u32 vcc, 0, v130\n\t"
    "v_and_or_b32 v98, v103, s44, v69\n\t"
    "v_xor_b32 v133, 23, v132\n\t"
    "v_lshlrev_b32 v115, v132, v136\n\t"
    "v_and_b32 v132, 7, v132\n\t"
    "v_lshl_or_b32 v115, v136, v133, v115\n\t"
    "v_lshl_add_u32 v132, v132, 1, v128\n\t"
    "ds_or_b32 v97, v115\n\t"
    "ds_write_b16 v132, v104\n\t"
    "v_cndmask_b32 v130, v130, v129, vcc\n\t"
    "v_ffbl_b32 v131, v130\n\t"
    "v_and_b32 v134, 7, v131\n\t"
    "v_lshlrev_b32 v134, v134, s45\n\t"
    "v_xor_b32 v129, v129, v134\n\t"
    "v_xor_b32 v132, v131, v127\n\t"
    "v_bfi_b32 v130, v107, 0, v129\n\t"
    "v_cmp_eq_u32 vcc, 0, v130\n\t"
    "v_and_or_b32 v99, v107, s44, v69\n\t"
    "v_xor_b32 v133, 23, v132\n\t"
    "v_lshlrev_b32 v116, v132, v136\n\t"
    "v_and_b32 v132, 7, v132\n\t"
    "v_lshl_or_b32 v116, v136, v133, v116\n\t"
    "v_lshl_add_u32 v132, v132, 1, v128\n\t"
    "ds_or_b32 v98, v116\n\t"
    "ds_write_b16 v132, v103\n\t"
    "v_cndmask_b32 v130, v130, v129, vcc\n\t"
    "v_ffbl_b32 v131, v130\n\t"
    "v_xor_b32 v132, v131, v127\n\t"
    "v_xor_b32 v133, 23, v132\n\t"
    "v_lshlrev_b32 v117, v132, v136\n\t"
    "v_and_b32 v132, 7, v132\n\t"
    "v_lshl_or_b32 v117, v136, v133, v117\n\t"
    "v_lshl_add_u32 v132, v132, 1, v128\n\t"
    "ds_or_b32 v99, v117\n\t"
    "ds_write_b16 v132, v107\n\t"
    "ds_read_b128 v[72:75], v128\n\t"
    "v_and_or_b32 v100, v76, s44, v69\n\t"
    "ds_read_b32 v100, v100\n\t"
    "v_and_or_b32 v101, v77, s44, v69\n\t"
    "ds_read_b32 v101, v101\n\t"
    "v_and_or_b32 v102, v78, s44, v69\n\t"
    "ds_read_b32 v102, v102\n\t"
    "v_and_or_b32 v103, v79, s44, v69\n\t"
    "ds_read_b32 v103, v103\n\t"
    "v_and_or_b32 v104, v80, s44, v69\n\t"
    "ds_read_b32 v104, v104\n\t"
    "v_and_or_b32 v105, v81, s44, v69\n\t"
    "ds_read_b32 v105, v105\n\t"
    "v_and_or_b32 v106, v82, s44, v69\n\t"
    "ds_read_b32 v106, v106\n\t"
    "v_and_or_b32 v107, v83, s44, v69\n\t"
    "ds_read_b32 v107, v107\n\t"
    "v_lshl_add_u32 v84, v8, 3, s36\n\t"
    "v_lshl_add_u32 v85, v9, 3, s37\n\t"
    "v_lshl_add_u32 v86, v10, 3, s38\n\t"
    "v_lshl_add_u32 v87, v11, 3, s39\n\t"
    "v_lshl_add_u32 v88, v12, 3, s40\n\t"
    "v_lshl_add_u32 v89, v13, 3, s41\n\t"
    "v_lshl_add_u32 v90, v14, 3, s42\n\t"
    "v_lshl_add_u32 v91, v15, 3, s43\n\t"
    "s_waitcnt vmcnt(4)\n\t"
    "s_mov_b32 s51, 1048576\n\t"
    "s_mov_b64 exec, 0x1ff\n\t"
    "L_af1_%=:\n\t"
    "v_cmp_gt_u32 vcc, v148, v71\n\t"
    "s_cbranch_vccz L_af1ok_%=\n\t"
    "s_sleep 8\n\t"
    "buffer_load_dword v71, v70, %12, s52 offen sc1\n\t"
    "s_sub_u32 s51, s51, 1\n\t"
    "s_waitcnt vmcnt(0)\n\t"
    "s_cmp_lg_u32 s51, 0\n\t"
    "s_cbranch_scc1 L_af1_%=\n\t"
    "L_af1ok_%=:\n\t"
    "s_mov_b64 exec, -1\n\t"
    "v_mov_b32 v134, 3112\n\t"
    "ds_write_b32 v134, v136\n\t"
    "v_mbcnt_lo_u32_b32 v70, -1, 0\n\t"
    "v_mbcnt_hi_u32_b32 v70, -1, v70\n\t"
    "s_and_b32 s50, %14, 7\n\t"
    "s_lshl_b32 s50, s50, 4\n\t"
    "s_lshr_b32 s51, %14, 4\n\t"
    "s_add_u32 s50, s50, s51\n\t"
    "s_mul_i32 s50, s50, 102400\n\t"
    "s_add_u32 s50, s50, 3407872\n\t"
    "v_lshlrev_b32 v148, 7, v70\n\t"
    "buffer_load_dword v71, v148, %12, s50 offen sc1\n\t"
    "s_and_b32 s51, %14, 8\n\t"
    "s_lshl_b32 s51, s51, 10\n\t"
    "s_add_u32 s50, s50, s51\n\t"
    "s_add_u32 s50, s50, 0x1000\n\t"
    "v_lshrrev_b32 v149, 4, v70\n\t"
    "v_lshl_add_u32 v149, v149, 11, v148\n\t"
    "buffer_load_dword v71, v148, %12, s50 offen sc1\n\t"
    "s_bfe_u32 s50, %14, 0x40003\n\t"
    "s_lshl_b32 s50, s50, 14\n\t"
    "s_add_u32 s50, s50, 16517120\n\t"
    "v_lshrrev_b32 v150, 3, v70\n\t"
    "v_lshl_add_u32 v150, v150, 10, v148\n\t"
    "buffer_load_dword v71, v149, %12, s50 offen sc1\n\t"
    "s_and_b32 s50, %14, 8\n\t"
    "s_bfe_u32 s51, %14, 0x30004\n\t"
    "s_add_u32 s50, s50, s51\n\t"
    "s_mul_i32 s50, s50, 212992\n\t"
    "s_add_u32 s50, s50, 196608\n\t"
    "buffer_load_dword v71, v150, %12, s50 offen sc1\n\t"
    "s_waitcnt lgkmcnt(9)\n\t"
    "buffer_store_dwordx4 v[72:75], v67, %12, s49 offen offset:384 sc1\n\t"
    "v_mov_b32 v129, 0xffff0000\n\t"
    "s_waitcnt lgkmcnt(5)\n\t"
    "v_perm_b32 v100, v100, v76, v126\n\t"
    "v_perm_b32 v101, v101, v77, v126\n\t"
    "v_perm_b32 v102, v102, v78, v126\n\t"
    "v_perm_b32 v103, v103, v79, v126\n\t"
    "s_waitcnt lgkmcnt(1)\n\t"
    "v_perm_b32 v104, v104, v80, v126\n\t"
    "v_perm_b32 v105, v105, v81, v126\n\t"
    "v_perm_b32 v106, v106, v82, v126\n\t"
    "v_perm_b32 v107, v107, v83, v126\n\t"
    "ds_write_b128 v141, v[156:159] offset:0\n\t"
    "ds_write_b128 v141, v[160:163] offset:256\n\t"
    "ds_read_b64_tr_b16 v[156:157], v142 offset:0\n\t"
    "ds_read_b64_tr_b16 v[158:159], v142 offset:256\n\t"
    "ds_read_b64_tr_b16 v[160:161], v142 offset:32\n\t"
    "ds_read_b64_tr_b16 v[162:163], v142 offset:288\n\t"
    "ds_write_b128 v141, v[164:167] offset:2112\n\t"
    "ds_write_b128 v141, v[168:171] offset:2368\n\t"
    "ds_read_b64_tr_b16 v[164:165], v142 offset:2112\n\t"
    "ds_read_b64_tr_b16 v[166:167], v142 offset:2368\n\t"
    "ds_read_b64_tr_b16 v[168:169], v142 offset:2144\n\t"
    "ds_read_b64_tr_b16 v[170:171], v142 offset:2400\n\t"
    "v_max_u32 v108, v100, v101\n\t"
    "v_min_u32 v101, v100, v101\n\t"
    "v_max_u32 v100, v102, v103\n\t"
    "v_min_u32 v103, v102, v103\n\t"
    "v_max_u32 v102, v104, v105\n\t"
    "v_min_u32 v105, v104, v105\n\t"
    "v_max_u32 v104, v106, v107\n\t"
    "v_min_u32 v107, v106, v107\n\t"
    "v_max_u32 v106, v108, v100\n\t"
    "v_min_u32 v100, v108, v100\n\t"
    "v_max_u32 v108, v101, v103\n\t"
    "v_min_u32 v103, v101, v103\n\t"
    "v_max_u32 v101, v102, v104\n\t"
    "v_min_u32 v104, v102, v104\n\t"
    "v_max_u32 v102, v105, v107\n\t"
    "v_min_u32 v107, v105, v107\n\t"
    "v_bfi_b32 v130, v106, 0, v129\n\t"
    "v_cmp_eq_u32 vcc, 0, v130\n\t"
    "v_and_or_b32 v92, v106, s44, v69\n\t"
    "s_nop 0\n\t"
    "v_cndmask_b32 v130, v130, v129, vcc\n\t"
    "v_ffbl_b32 v131, v130\n\t"
    "v_and_b32 v134, 7, v131\n\t"
    "v_lshlrev_b32 v134, v134, s45\n\t"
    "v_xor_b32 v129, v129, v134\n\t"
    "v_xor_b32 v132, v131, v127\n\t"
    "v_bfi_b32 v130, v101, 0, v129\n\t"
    "v_cmp_eq_u32 vcc, 0, v130\n\t"
    "v_and_or_b32 v93, v101, s44, v69\n\t"
    "v_xor_b32 v133, 23, v132\n\t"
    "v_lshlrev_b32 v110, v132, v136\n\t"
    "v_and_b32 v132, 7, v132\n\t"
    "v_lshl_or_b32 v110, v136, v133, v110\n\t"
    "v_lshl_add_u32 v132, v132, 1, v128\n\t"
    "ds_or_b32 v92, v110\n\t"
    "ds_write_b16 v132, v106\n\t"
    "v_cndmask_b32 v130, v130, v129, vcc\n\t"
    "v_ffbl_b32 v131, v130\n\t"
    "v_and_b32 v134, 7, v131\n\t"
    "v_lshlrev_b32 v134, v134, s45\n\t"
    "v_xor_b32 v129, v129, v134\n\t"
    "v_xor_b32 v132, v131, v127\n\t"
    "v_bfi_b32 v130, v108, 0, v129\n\t"
    "v_cmp_eq_u32 vcc, 0, v130\n\t"
    "v_and_or_b32 v94, v108, s44, v69\n\t"
    "v_xor_b32 v133, 23, v132\n\t"
    "v_lshlrev_b32 v111, v132, v136\n\t"
    "v_and_b32 v132, 7, v132\n\t"
    "v_lshl_or_b32 v111, v136, v133, v111\n\t"
    "v_lshl_add_u32 v132, v132, 1, v128\n\t"
    "ds_or_b32 v93, v111\n\t"
    "ds_write_b16 v132, v101\n\t"
    "v_cndmask_b32 v130, v130, v129, vcc\n\t"
    "v_ffbl_b32 v131, v130\n\t"
    "v_and_b32 v134, 7, v131\n\t"
    "v_lshlrev_b32 v134, v134, s45\n\t"
    "v_xor_b32 v129, v129, v134\n\t"
    "v_xor_b32 v132, v131, v127\n\t"
    "s_waitcnt lgkmcnt(10)\n\t"
    "buffer_store_dwordx4 v[156:159], v139, %12, s48 offen sc1\n\t"
    "buffer_store_dwordx4 v[160:163], v140, %12, s48 offen sc1\n\t"
    "s_add_u32 s48, s48, 819200\n\t"
    "s_waitcnt lgkmcnt(4)\n\t"
    "buffer_store_dwordx4 v[164:167], v139, %12, s48 offen sc1\n\t"
    "buffer_store_dwordx4 v[168:171], v140, %12, s48 offen sc1\n\t"
    "s_add_u32 s48, s48, -811008\n\t"
    "ds_write_b128 v141, v[172:175] offset:0\n\t"
    "ds_write_b128 v141, v[176:179] offset:256\n\t"
    "ds_read_b64_tr_b16 v[172:173], v142 offset:0\n\t"
    "ds_read_b64_tr_b16 v[174:175], v142 offset:256\n\t"
    "ds_read_b64_tr_b16 v[176:177], v142 offset:32\n\t"
    "ds_read_b64_tr_b16 v[178:179], v142 offset:288\n\t"
    "ds_write_b128 v141, v[180:183] offset:2112\n\t"
    "ds_write_b128 v141, v[184:187] offset:2368\n\t"
    "ds_read_b64_tr_b16 v[180:181], v142 offset:2112\n\t"
    "ds_read_b64_tr_b16 v[182:183], v142 offset:2368\n\t"
    "ds_read_b64_tr_b16 v[184:185], v142 offset:2144\n\t"
    "ds_read_b64_tr_b16 v[186:187], v142 offset:2400\n\t"
    "v_bfi_b32 v130, v100, 0, v129\n\t"
    "v_cmp_eq_u32 vcc, 0, v130\n\t"
    "v_and_or_b32 v95, v100, s44, v69\n\t"
    "v_xor_b32 v133, 23, v132\n\t"
    "v_lshlrev_b32 v112, v132, v136\n\t"
    "v_and_b32 v132, 7, v132\n\t"
    "v_lshl_or_b32 v112, v136, v133, v112\n\t"
    "v_lshl_add_u32 v132, v132, 1, v128\n\t"
    "ds_or_b32 v94, v112\n\t"
    "ds_write_b16 v132, v108\n\t"
    "v_cndmask_b32 v130, v130, v129, vcc\n\t"
    "v_ffbl_b32 v131, v130\n\t"
    "v_and_b32 v134, 7, v131\n\t"
    "v_lshlrev_b32 v134, v134, s45\n\t"
    "v_xor_b32 v129, v129, v134\n\t"
    "v_xor_b32 v132, v131, v127\n\t"
    "v_bfi_b32 v130, v102, 0, v129\n\t"
    "v_cmp_eq_u32 vcc, 0, v130\n\t"
    "v_and_or_b32 v96, v102, s44, v69\n\t"
    "v_xor_b32 v133, 23, v132\n\t"
    "v_lshlrev_b32 v113, v132, v136\n\t"
    "v_and_b32 v132, 7, v132\n\t"
    "v_lshl_or_b32 v113, v136, v133, v113\n\t"
    "v_lshl_add_u32 v132, v132, 1, v128\n\t"
    "ds_or_b32 v95, v113\n\t"
    "ds_write_b16 v132, v100\n\t"
    "v_cndmask_b32 v130, v130, v129, vcc\n\t"
    "v_ffbl_b32 v131, v130\n\t"
    "v_and_b32 v134, 7, v131\n\t"
    "v_lshlrev_b32 v134, v134, s45\n\t"
    "v_xor_b32 v129, v129, v134\n\t"
    "v_xor_b32 v132, v131, v127\n\t"
    "v_bfi_b32 v130, v104, 0, v129\n\t"
    "v_cmp_eq_u32 vcc, 0, v130\n\t"
    "v_and_or_b32 v97, v104, s44, v69\n\t"
    "v_xor_b32 v133, 23, v132\n\t"
    "v_lshlrev_b32 v114, v132, v136\n\t"
    "v_and_b32 v132, 7, v132\n\t"
    "v_lshl_or_b32 v114, v136, v133, v114\n\t"
    "v_lshl_add_u32 v132, v132, 1, v128\n\t"
    "ds_or_b32 v96, v114\n\t"
    "ds_write_b16 v132, v102\n\t"
    "v_cndmask_b32 v130, v130, v129, vcc\n\t"
    "v_ffbl_b32 v131, v130\n\t"
    "v_and_b32 v134, 7, v131\n\t"
    "v_lshlrev_b32 v134, v134, s45\n\t"
    "v_xor_b32 v129, v129, v134\n\t"
    "v_xor_b32 v132, v131, v127\n\t"
    "s_waitcnt lgkmcnt(12)\n\t"
    "buffer_store_dwordx4 v[172:175], v139, %12, s48 offen sc1\n\t"
    "buffer_store_dwordx4 v[176:179], v140, %12, s48 offen sc1\n\t"
    "s_add_u32 s48, s48, 819200\n\t"
    "s_waitcnt lgkmcnt(6)\n\t"
    "buffer_store_dwordx4 v[180:183], v139, %12, s48 offen sc1\n\t"
    "buffer_store_dwordx4 v[184:187], v140, %12, s48 offen sc1\n\t"
    "s_add_u32 s48, s48, -811008\n\t"
    "ds_write_b128 v141, v[188:191] offset:0\n\t"
    "ds_write_b128 v141, v[192:195] offset:256\n\t"
    "ds_read_b64_tr_b16 v[188:189], v142 offset:0\n\t"
    "ds_read_b64_tr_b16 v[190:191], v142 offset:256\n\t"
    "ds_read_b64_tr_b16 v[192:193], v142 offset:32\n\t"
    "ds_read_b64_tr_b16 v[194:195], v142 offset:288\n\t"
    "ds_write_b128 v141, v[196:199] offset:2112\n\t"
    "ds_write_b128 v141, v[200:203] offset:2368\n\t"
    "ds_read_b64_tr_b16 v[196:197], v142 offset:2112\n\t"
    "ds_read_b64_tr_b16 v[198:199], v142 offset:2368\n\t"
    "ds_read_b64_tr_b16 v[200:201], v142 offset:2144\n\t"
    "ds_read_b64_tr_b16 v[202:203], v142 offset:2400\n\t"
    "v_bfi_b32 v130, v103, 0, v129\n\t"
    "v_cmp_eq_u32 vcc, 0, v130\n\t"
    "v_and_or_b32 v98, v103, s44, v69\n\t"
    "v_xor_b32 v133, 23, v132\n\t"
    "v_lshlrev_b32 v115, v132, v136\n\t"
    "v_and_b32 v132, 7, v132\n\t"
    "v_lshl_or_b32 v115, v136, v133, v115\n\t"
    "v_lshl_add_u32 v132, v132, 1, v128\n\t"
    "ds_or_b32 v97, v115\n\t"
    "ds_write_b16 v132, v104\n\t"
    "v_cndmask_b32 v130, v130, v129, vcc\n\t"
    "v_ffbl_b32 v131, v130\n\t"
    "v_and_b32 v134, 7, v131\n\t"
    "v_lshlrev_b32 v134, v134, s45\n\t"
    "v_xor_b32 v129, v129, v134\n\t"
    "v_xor_b32 v132, v131, v127\n\t"
    "v_bfi_b32 v130, v107, 0, v129\n\t"
    "v_cmp_eq_u32 vcc, 0, v130\n\t"
    "v_and_or_b32 v99, v107, s44, v69\n\t"
    "v_xor_b32 v133, 23, v132\n\t"
    "v_lshlrev_b32 v116, v132, v136\n\t"
    "v_and_b32 v132, 7, v132\n\t"
    "v_lshl_or_b32 v116, v136, v133, v116\n\t"
    "v_lshl_add_u32 v132, v132, 1, v128\n\t"
    "ds_or_b32 v98, v116\n\t"
    "ds_write_b16 v132, v103\n\t"
    "v_cndmask_b32 v130, v130, v129, vcc\n\t"
    "v_ffbl_b32 v131, v130\n\t"
    "v_xor_b32 v132, v131, v127\n\t"
    "v_xor_b32 v133, 23, v132\n\t"
    "v_lshlrev_b32 v117, v132, v136\n\t"
    "v_and_b32 v132, 7, v132\n\t"
    "v_lshl_or_b32 v117, v136, v133, v117\n\t"
    "v_lshl_add_u32 v132, v132, 1, v128\n\t"
    "ds_or_b32 v99, v117\n\t"
    "ds_write_b16 v132, v107\n\t"
    "ds_read_b128 v[72:75], v128\n\t"
    "v_and_or_b32 v100, v84, s44, v69\n\t"
    "ds_read_b32 v100, v100\n\t"
    "v_and_or_b32 v101, v85, s44, v69\n\t"
    "ds_read_b32 v101, v101\n\t"
    "v_and_or_b32 v102, v86, s44, v69\n\t"
    "ds_read_b32 v102, v102\n\t"
    "v_and_or_b32 v103, v87, s44, v69\n\t"
    "ds_read_b32 v103, v103\n\t"
    "v_and_or_b32 v104, v88, s44, v69\n\t"
    "ds_read_b32 v104, v104\n\t"
    "v_and_or_b32 v105, v89, s44, v69\n\t"
    "ds_read_b32 v105, v105\n\t"
    "v_and_or_b32 v106, v90, s44, v69\n\t"
    "ds_read_b32 v106, v106\n\t"
    "v_and_or_b32 v107, v91, s44, v69\n\t"
    "ds_read_b32 v107, v107\n\t"
    "v_lshl_add_u32 v76, v16, 3, s36\n\t"
    "v_lshl_add_u32 v77, v17, 3, s37\n\t"
    "v_lshl_add_u32 v78, v18, 3, s38\n\t"
    "v_lshl_add_u32 v79, v19, 3, s39\n\t"
    "v_lshl_add_u32 v80, v20, 3, s40\n\t"
    "v_lshl_add_u32 v81, v21, 3, s41\n\t"
    "v_lshl_add_u32 v82, v22, 3, s42\n\t"
    "v_lshl_add_u32 v83, v23, 3, s43\n\t"
    "s_waitcnt lgkmcnt(15)\n\t"
    "buffer_store_dwordx4 v[188:191], v139, %12, s48 offen sc1\n\t"
    "buffer_store_dwordx4 v[192:195], v140, %12, s48 offen sc1\n\t"
    "s_add_u32 s48, s48, 819200\n\t"
    "buffer_store_dwordx4 v[196:199], v139, %12, s48 offen sc1\n\t"
    "buffer_store_dwordx4 v[200:203], v140, %12, s48 offen sc1\n\t"
    "s_add_u32 s48, s48, -811008\n\t"
    "s_waitcnt lgkmcnt(8)\n\t"
    "buffer_store_dwordx4 v[72:75], v67, %12, s49 offen offset:416 sc1\n\t"
    "v_mov_b32 v129, 0xffff0000\n\t"
    "s_waitcnt lgkmcnt(4)\n\t"
    "v_perm_b32 v100, v100, v84, v126\n\t"
    "v_perm_b32 v101, v101, v85, v126\n\t"
    "v_perm_b32 v102, v102, v86, v126\n\t"
    "v_perm_b32 v103, v103, v87, v126\n\t"
    "s_waitcnt lgkmcnt(0)\n\t"
    "v_perm_b32 v104, v104, v88, v126\n\t"
    "v_perm_b32 v105, v105, v89, v126\n\t"
    "v_perm_b32 v106, v106, v90, v126\n\t"
    "v_perm_b32 v107, v107, v91, v126\n\t"
    "ds_write_b128 v141, v[56:59] offset:0\n\t"
    "ds_write_b128 v141, v[60:63] offset:256\n\t"
    "ds_read_b64_tr_b16 v[56:57], v142 offset:0\n\t"
    "ds_read_b64_tr_b16 v[58:59], v142 offset:256\n\t"
    "ds_read_b64_tr_b16 v[60:61], v142 offset:32\n\t"
    "ds_read_b64_tr_b16 v[62:63], v142 offset:288\n\t"
    "ds_write_b128 v141, v[204:207] offset:2112\n\t"
    "ds_write_b128 v141, v[208:211] offset:2368\n\t"
    "ds_read_b64_tr_b16 v[204:205], v142 offset:2112\n\t"
    "ds_read_b64_tr_b16 v[206:207], v142 offset:2368\n\t"
    "ds_read_b64_tr_b16 v[208:209], v142 offset:2144\n\t"
    "ds_read_b64_tr_b16 v[210:211], v142 offset:2400\n\t"
    "v_max_u32 v108, v100, v101\n\t"
    "v_min_u32 v101, v100, v101\n\t"
    "v_max_u32 v100, v102, v103\n\t"
    "v_min_u32 v103, v102, v103\n\t"
    "v_max_u32 v102, v104, v105\n\t"
    "v_min_u32 v105, v104, v105\n\t"
    "v_max_u32 v104, v106, v107\n\t"
    "v_min_u32 v107, v106, v107\n\t"
    "v_max_u32 v106, v108, v100\n\t"
    "v_min_u32 v100, v108, v100\n\t"
    "v_max_u32 v108, v101, v103\n\t"
    "v_min_u32 v103, v101, v103\n\t"
    "v_max_u32 v101, v102, v104\n\t"
    "v_min_u32 v104, v102, v104\n\t"
    "v_max_u32 v102, v105, v107\n\t"
    "v_min_u32 v107, v105, v107\n\t"
    "v_bfi_b32 v130, v106, 0, v129\n\t"
    "v_cmp_eq_u32 vcc, 0, v130\n\t"
    "v_and_or_b32 v92, v106, s44, v69\n\t"
    "s_nop 0\n\t"
    "v_cndmask_b32 v130, v130, v129, vcc\n\t"
    "v_ffbl_b32 v131, v130\n\t"
    "v_and_b32 v134, 7, v131\n\t"
    "v_lshlrev_b32 v134, v134, s45\n\t"
    "v_xor_b32 v129, v129, v134\n\t"
    "v_xor_b32 v132, v131, v127\n\t"
    "v_bfi_b32 v130, v101, 0, v129\n\t"
    "v_cmp_eq_u32 vcc, 0, v130\n\t"
    "v_and_or_b32 v93, v101, s44, v69\n\t"
    "v_xor_b32 v133, 23, v132\n\t"
    "v_lshlrev_b32 v110, v132, v136\n\t"
    "v_and_b32 v132, 7, v132\n\t"
    "v_lshl_or_b32 v110, v136, v133, v110\n\t"
    "v_lshl_add_u32 v132, v132, 1, v128\n\t"
    "ds_or_b32 v92, v110\n\t"
    "ds_write_b16 v132, v106\n\t"
    "v_cndmask_b32 v130, v130, v129, vcc\n\t"
    "v_ffbl_b32 v131, v130\n\t"
    "v_and_b32 v134, 7, v131\n\t"
    "v_lshlrev_b32 v134, v134, s45\n\t"
    "v_xor_b32 v129, v129, v134\n\t"
    "v_xor_b32 v132, v131, v127\n\t"
    "v_bfi_b32 v130, v108, 0, v129\n\t"
    "v_cmp_eq_u32 vcc, 0, v130\n\t"
    "v_and_or_b32 v94, v108, s44, v69\n\t"
    "v_xor_b32 v133, 23, v132\n\t"
    "v_lshlrev_b32 v111, v132, v136\n\t"
    "v_and_b32 v132, 7, v132\n\t"
    "v_lshl_or_b32 v111, v136, v133, v111\n\t"
    "v_lshl_add_u32 v132, v132, 1, v128\n\t"
    "ds_or_b32 v93, v111\n\t"
    "ds_write_b16 v132, v101\n\t"
    "v_cndmask_b32 v130, v130, v129, vcc\n\t"
    "v_ffbl_b32 v131, v130\n\t"
    "v_and_b32 v134, 7, v131\n\t"
    "v_lshlrev_b32 v134, v134, s45\n\t"
    "v_xor_b32 v129, v129, v134\n\t"
    "v_xor_b32 v132, v131, v127\n\t"
    "s_waitcnt lgkmcnt(10)\n\t"
    "buffer_store_dwordx4 v[56:59], v139, %12, s48 offen sc1\n\t"
    "buffer_store_dwordx4 v[60:63], v140, %12, s48 offen sc1\n\t"
    "s_add_u32 s48, s48, 819200\n\t"
    "s_waitcnt lgkmcnt(4)\n\t"
    "buffer_store_dwordx4 v[204:207], v139, %12, s48 offen sc1\n\t"
    "buffer_store_dwordx4 v[208:211], v140, %12, s48 offen sc1\n\t"
    "s_add_u32 s48, s48, -811008\n\t"
    "ds_write_b128 v141, v[212:215] offset:0\n\t"
    "ds_write_b128 v141, v[216:219] offset:256\n\t"
    "ds_read_b64_tr_b16 v[212:213], v142 offset:0\n\t"
    "ds_read_b64_tr_b16 v[214:215], v142 offset:256\n\t"
    "ds_read_b64_tr_b16 v[216:217], v142 offset:32\n\t"
    "ds_read_b64_tr_b16 v[218:219], v142 offset:288\n\t"
    "ds_write_b128 v141, v[220:223] offset:2112\n\t"
    "ds_write_b128 v141, v[224:227] offset:2368\n\t"
    "ds_read_b64_tr_b16 v[220:221], v142 offset:2112\n\t"
    "ds_read_b64_tr_b16 v[222:223], v142 offset:2368\n\t"
    "ds_read_b64_tr_b16 v[224:225], v142 offset:2144\n\t"
    "ds_read_b64_tr_b16 v[226:227], v142 offset:2400\n\t"
    "v_bfi_b32 v130, v100, 0, v129\n\t"
    "v_cmp_eq_u32 vcc, 0, v130\n\t"
    "v_and_or_b32 v95, v100, s44, v69\n\t"
    "v_xor_b32 v133, 23, v132\n\t"
    "v_lshlrev_b32 v112, v132, v136\n\t"
    "v_and_b32 v132, 7, v132\n\t"
    "v_lshl_or_b32 v112, v136, v133, v112\n\t"
    "v_lshl_add_u32 v132, v132, 1, v128\n\t"
    "ds_or_b32 v94, v112\n\t"
    "ds_write_b16 v132, v108\n\t"
    "v_cndmask_b32 v130, v130, v129, vcc\n\t"
    "v_ffbl_b32 v131, v130\n\t"
    "v_and_b32 v134, 7, v131\n\t"
    "v_lshlrev_b32 v134, v134, s45\n\t"
    "v_xor_b32 v129, v129, v134\n\t"
    "v_xor_b32 v132, v131, v127\n\t"
    "v_bfi_b32 v130, v102, 0, v129\n\t"
    "v_cmp_eq_u32 vcc, 0, v130\n\t"
    "v_and_or_b32 v96, v102, s44, v69\n\t"
    "v_xor_b32 v133, 23, v132\n\t"
    "v_lshlrev_b32 v113, v132, v136\n\t"
    "v_and_b32 v132, 7, v132\n\t"
    "v_lshl_or_b32 v113, v136, v133, v113\n\t"
    "v_lshl_add_u32 v132, v132, 1, v128\n\t"
    "ds_or_b32 v95, v113\n\t"
    "ds_write_b16 v132, v100\n\t"
    "v_cndmask_b32 v130, v130, v129, vcc\n\t"
    "v_ffbl_b32 v131, v130\n\t"
    "v_and_b32 v134, 7, v131\n\t"
    "v_lshlrev_b32 v134, v134, s45\n\t"
    "v_xor_b32 v129, v129, v134\n\t"
    "v_xor_b32 v132, v131, v127\n\t"
    "v_bfi_b32 v130, v104, 0, v129\n\t"
    "v_cmp_eq_u32 vcc, 0, v130\n\t"
    "v_and_or_b32 v97, v104, s44, v69\n\t"
    "v_xor_b32 v133, 23, v132\n\t"
    "v_lshlrev_b32 v114, v132, v136\n\t"
    "v_and_b32 v132, 7, v132\n\t"
    "v_lshl_or_b32 v114, v136, v133, v114\n\t"
    "v_lshl_add_u32 v132, v132, 1, v128\n\t"
    "ds_or_b32 v96, v114\n\t"
    "ds_write_b16 v132, v102\n\t"
    "v_cndmask_b32 v130, v130, v129, vcc\n\t"
    "v_ffbl_b32 v131, v130\n\t"
    "v_and_b32 v134, 7, v131\n\t"
    "v_lshlrev_b32 v134, v134, s45\n\t"
    "v_xor_b32 v129, v129, v134\n\t"
    "v_xor_b32 v132, v131, v127\n\t"
    "s_waitcnt lgkmcnt(12)\n\t"
    "s_bitcmp1_b32 %14, 3\n\t"
    "s_cselect_b32 s48, 0x40000000, s48\n\t"
    "buffer_store_dwordx4 v[212:215], v139, %12, s48 offen sc1\n\t"
    "buffer_store_dwordx4 v[216:219], v140, %12, s48 offen sc1\n\t"
    "s_add_u32 s48, s48, 819200\n\t"
    "s_waitcnt lgkmcnt(6)\n\t"
    "buffer_store_dwordx4 v[220:223], v139, %12, s48 offen sc1\n\t"
    "buffer_store_dwordx4 v[224:227], v140, %12, s48 offen sc1\n\t"
    "s_add_u32 s48, s48, -811008\n\t"
    "v_bfi_b32 v130, v103, 0, v129\n\t"
    "v_cmp_eq_u32 vcc, 0, v130\n\t"
    "v_and_or_b32 v98, v103, s44, v69\n\t"
    "v_xor_b32 v133, 23, v132\n\t"
    "v_lshlrev_b32 v115, v132, v136\n\t"
    "v_and_b32 v132, 7, v132\n\t"
    "v_lshl_or_b32 v115, v136, v133, v115\n\t"
    "v_lshl_add_u32 v132, v132, 1, v128\n\t"
    "ds_or_b32 v97, v115\n\t"
    "ds_write_b16 v132, v104\n\t"
    "v_cndmask_b32 v130, v130, v129, vcc\n\t"
    "v_ffbl_b32 v131, v130\n\t"
    "v_and_b32 v134, 7, v131\n\t"
    "v_lshlrev_b32 v134, v134, s45\n\t"
    "v_xor_b32 v129, v129, v134\n\t"
    "v_xor_b32 v132, v131, v127\n\t"
    "v_bfi_b32 v130, v107, 0, v129\n\t"
    "v_cmp_eq_u32 vcc, 0, v130\n\t"
    "v_and_or_b32 v99, v107, s44, v69\n\t"
    "v_xor_b32 v133, 23, v132\n\t"
    "v_lshlrev_b32 v116, v132, v136\n\t"
    "v_and_b32 v132, 7, v132\n\t"
    "v_lshl_or_b32 v116, v136, v133, v116\n\t"
    "v_lshl_add_u32 v132, v132, 1, v128\n\t"
    "ds_or_b32 v98, v116\n\t"
    "ds_write_b16 v132, v103\n\t"
    "v_cndmask_b32 v130, v130, v129, vcc\n\t"
    "v_ffbl_b32 v131, v130\n\t"
    "v_xor_b32 v132, v131, v127\n\t"
    "v_xor_b32 v133, 23, v132\n\t"
    "v_lshlrev_b32 v117, v132, v136\n\t"
    "v_and_b32 v132, 7, v132\n\t"
    "v_lshl_or_b32 v117, v136, v133, v117\n\t"
    "v_lshl_add_u32 v132, v132, 1, v128\n\t"
    "ds_or_b32 v99, v117\n\t"
    "ds_write_b16 v132, v107\n\t"
    "ds_read_b128 v[72:75], v128\n\t"
    "v_and_or_b32 v100, v76, s44, v69\n\t"
    "ds_read_b32 v100, v100\n\t"
    "v_and_or_b32 v101, v77, s44, v69\n\t"
    "ds_read_b32 v101, v101\n\t"
    "v_and_or_b32 v102, v78, s44, v69\n\t"
    "ds_read_b32 v102, v102\n\t"
    "v_and_or_b32 v103, v79, s44, v69\n\t"
    "ds_read_b32 v103, v103\n\t"
    "v_and_or_b32 v104, v80, s44, v69\n\t"
    "ds_read_b32 v104, v104\n\t"
    "v_and_or_b32 v105, v81, s44, v69\n\t"
    "ds_read_b32 v105, v105\n\t"
    "v_and_or_b32 v106, v82, s44, v69\n\t"
    "ds_read_b32 v106, v106\n\t"
    "v_and_or_b32 v107, v83, s44, v69\n\t"
    "ds_read_b32 v107, v107\n\t"
    "s_waitcnt lgkmcnt(8)\n\t"
    "buffer_store_dwordx4 v[72:75], v67, %12, s49 offen offset:448 sc1\n\t"
    "v_mov_b32 v129, 0xffff0000\n\t"
    "s_waitcnt lgkmcnt(4)\n\t"
    "v_perm_b32 v100, v100, v76, v126\n\t"
    "v_perm_b32 v101, v101, v77, v126\n\t"
    "v_perm_b32 v102, v102, v78, v126\n\t"
    "v_perm_b32 v103, v103, v79, v126\n\t"
    "s_waitcnt lgkmcnt(0)\n\t"
    "v_perm_b32 v104, v104, v80, v126\n\t"
    "v_perm_b32 v105, v105, v81, v126\n\t"
    "v_perm_b32 v106, v106, v82, v126\n\t"
    "v_perm_b32 v107, v107, v83, v126\n\t"
    "v_max_u32 v108, v100, v101\n\t"
    "v_min_u32 v101, v100, v101\n\t"
    "v_max_u32 v100, v102, v103\n\t"
    "v_min_u32 v103, v102, v103\n\t"
    "v_max_u32 v102, v104, v105\n\t"
    "v_min_u32 v105, v104, v105\n\t"
    "v_max_u32 v104, v106, v107\n\t"
    "v_min_u32 v107, v106, v107\n\t"
    "v_max_u32 v106, v108, v100\n\t"
    "v_min_u32 v100, v108, v100\n\t"
    "v_max_u32 v108, v101, v103\n\t"
    "v_min_u32 v103, v101, v103\n\t"
    "v_max_u32 v101, v102, v104\n\t"
    "v_min_u32 v104, v102, v104\n\t"
    "v_max_u32 v102, v105, v107\n\t"
    "v_min_u32 v107, v105, v107\n\t"
    "v_bfi_b32 v130, v106, 0, v129\n\t"
    "v_cmp_eq_u32 vcc, 0, v130\n\t"
    "v_and_or_b32 v92, v106, s44, v69\n\t"
    "s_nop 0\n\t"
    "v_cndmask_b32 v130, v130, v129, vcc\n\t"
    "v_ffbl_b32 v131, v130\n\t"
    "v_and_b32 v134, 7, v131\n\t"
    "v_lshlrev_b32 v134, v134, s45\n\t"
    "v_xor_b32 v129, v129, v134\n\t"
    "v_xor_b32 v132, v131, v127\n\t"
    "v_bfi_b32 v130, v101, 0, v129\n\t"
    "v_cmp_eq_u32 vcc, 0, v130\n\t"
    "v_and_b32 v132, 7, v132\n\t"
    "v_lshl_add_u32 v132, v132, 1, v128\n\t"
    "ds_write_b16 v132, v106\n\t"
    "v_cndmask_b32 v130, v130, v129, vcc\n\t"
    "v_ffbl_b32 v131, v130\n\t"
    "v_and_b32 v134, 7, v131\n\t"
    "v_lshlrev_b32 v134, v134, s45\n\t"
    "v_xor_b32 v129, v129, v134\n\t"
    "v_xor_b32 v132, v131, v127\n\t"
    "v_bfi_b32 v130, v108, 0, v129\n\t"
    "v_cmp_eq_u32 vcc, 0, v130\n\t"
    "v_and_b32 v132, 7, v132\n\t"
    "v_lshl_add_u32 v132, v132, 1, v128\n\t"
    "ds_write_b16 v132, v101\n\t"
    "v_cndmask_b32 v130, v130, v129, vcc\n\t"
    "v_ffbl_b32 v131, v130\n\t"
    "v_and_b32 v134, 7, v131\n\t"
    "v_lshlrev_b32 v134, v134, s45\n\t"
    "v_xor_b32 v129, v129, v134\n\t"
    "v_xor_b32 v132, v131, v127\n\t"
    "v_bfi_b32 v130, v100, 0, v129\n\t"
    "v_cmp_eq_u32 vcc, 0, v130\n\t"
    "v_and_b32 v132, 7, v132\n\t"
    "v_lshl_add_u32 v132, v132, 1, v128\n\t"
    "ds_write_b16 v132, v108\n\t"
    "v_cndmask_b32 v130, v130, v129, vcc\n\t"
    "v_ffbl_b32 v131, v130\n\t"
    "v_and_b32 v134, 7, v131\n\t"
    "v_lshlrev_b32 v134, v134, s45\n\t"
    "v_xor_b32 v129, v129, v134\n\t"
    "v_xor_b32 v132, v131, v127\n\t"
    "v_bfi_b32 v130, v102, 0, v129\n\t"
    "v_cmp_eq_u32 vcc, 0, v130\n\t"
    "v_and_b32 v132, 7, v132\n\t"
    "v_lshl_add_u32 v132, v132, 1, v128\n\t"
    "ds_write_b16 v132, v100\n\t"
    "v_cndmask_b32 v130, v130, v129, vcc\n\t"
    "v_ffbl_b32 v131, v130\n\t"
    "v_and_b32 v134, 7, v131\n\t"
    "v_lshlrev_b32 v134, v134, s45\n\t"
    "v_xor_b32 v129, v129, v134\n\t"
    "v_xor_b32 v132, v131, v127\n\t"
    "v_bfi_b32 v130, v104, 0, v129\n\t"
    "v_cmp_eq_u32 vcc, 0, v130\n\t"
    "v_and_b32 v132, 7, v132\n\t"
    "v_lshl_add_u32 v132, v132, 1, v128\n\t"
    "ds_write_b16 v132, v102\n\t"
    "v_cndmask_b32 v130, v130, v129, vcc\n\t"
    "v_ffbl_b32 v131, v130\n\t"
    "v_and_b32 v134, 7, v131\n\t"
    "v_lshlrev_b32 v134, v134, s45\n\t"
    "v_xor_b32 v129, v129, v134\n\t"
    "v_xor_b32 v132, v131, v127\n\t"
    "v_bfi_b32 v130, v103, 0, v129\n\t"
    "v_cmp_eq_u32 vcc, 0, v130\n\t"
    "v_and_b32 v132, 7, v132\n\t"
    "v_lshl_add_u32 v132, v132, 1, v128\n\t"
    "ds_write_b16 v132, v104\n\t"
    "v_cndmask_b32 v130, v130, v129, vcc\n\t"
    "v_ffbl_b32 v131, v130\n\t"
    "v_and_b32 v134, 7, v131\n\t"
    "v_lshlrev_b32 v134, v134, s45\n\t"
    "v_xor_b32 v129, v129, v134\n\t"
    "v_xor_b32 v132, v131, v127\n\t"
    "v_bfi_b32 v130, v107, 0, v129\n\t"
    "v_cmp_eq_u32 vcc, 0, v130\n\t"
    "v_and_b32 v132, 7, v132\n\t"
    "v_lshl_add_u32 v132, v132, 1, v128\n\t"
    "ds_write_b16 v132, v103\n\t"
    "v_cndmask_b32 v130, v130, v129, vcc\n\t"
    "v_ffbl_b32 v131, v130\n\t"
    "v_xor_b32 v132, v131, v127\n\t"
    "v_and_b32 v132, 7, v132\n\t"
    "v_lshl_add_u32 v132, v132, 1, v128\n\t"
    "ds_write_b16 v132, v107\n\t"
    "ds_read_b128 v[72:75], v128\n\t"
    "s_waitcnt lgkmcnt(0)\n\t"
    "buffer_store_dwordx4 v[72:75], v67, %12, s49 offen offset:480 sc1\n\t"
    "s_setprio 0\n\t"
    "v_mov_b32 v134, 1036\n\t"
    "L_asvsp12d_%=:\n\t"
    "ds_read_b32 v130, v134\n\t"
    "s_waitcnt lgkmcnt(0)\n\t"
    "v_readfirstlane_b32 s50, v130\n\t"
    "s_nop 0\n\t"
    "s_cmp_lt_u32 s50, 4\n\t"
    "s_cbranch_scc1 L_asvsp12d_%=\n\t"
    "v_mbcnt_lo_u32_b32 v100, -1, 0\n\t"
    "v_mbcnt_hi_u32_b32 v100, -1, v100\n\t"
    "s_lshl_b32 s61, %16, 15\n\t"
    "s_add_u32 s61, s61, 16515072\n\t"
    "v_lshlrev_b32 v127, 4, v100\n\t"
    "s_lshr_b32 s37, %14, 4\n\t"
    "s_and_b32 s39, %14, 7\n\t"
    "v_and_b32 v101, 15, v100\n\t"
    "s_lshl_b32 s39, s39, 4\n\t"
    "s_add_u32 s39, s39, s37\n\t"
    "s_mul_i32 s65, %16, 125\n\t"
    "v_add_u32 v155, s65, v101\n\t"
    "s_mul_i32 s63, s39, 102400\n\t"
    "s_add_u32 s63, s63, 3407872\n\t"
    "v_lshrrev_b32 v102, 2, v101\n\t"
    "s_and_b32 s37, s37, 7\n\t"
    "s_lshl_b32 s37, s37, 11\n\t"
    "v_lshrrev_b32 v103, 3, v100\n\t"
    "s_lshl_b32 s36, s39, 16\n\t"
    "s_bfe_u32 s65, %14, 0x10003\n\t"
    "v_and_b32 v103, 2, v103\n\t"
    "s_lshl_b32 s38, s65, 15\n\t"
    "s_add_u32 s36, s36, s38\n\t"
    "v_and_b32 v104, 1, v100\n\t"
    "s_lshl_b32 s38, %16, 12\n\t"
    "s_add_u32 s36, s36, s38\n\t"
    "v_lshlrev_b32 v104, 4, v104\n\t"
    "s_add_u32 s36, s36, 17825792\n\t"
    "v_mov_b32 v101, 0x3f80\n\t"
    "v_lshlrev_b32 v104, v104, v101\n\t"
    "v_cmp_eq_u32 vcc, v102, v103\n\t"
    "s_waitcnt lgkmcnt(0)\n\t"
    "s_lshr_b32 s39, s91, %9\n\t"
    "s_lshl_b32 s39, s39, 16\n\t"
    "v_cndmask_b32 v120, 0, v104, vcc\n\t"
    "v_cndmask_b32 v121, 0, v104, vcc\n\t"
    "v_add_u32 v103, 1, v103\n\t"
    "v_cmp_eq_u32 vcc, v102, v103\n\t"
    "s_lshl_b32 s40, s88, 16\n\t"
    "s_and_b32 s41, s88, 0xffff0000\n\t"
    "v_cndmask_b32 v122, 0, v104, vcc\n\t"
    "v_cndmask_b32 v123, 0, v104, vcc\n\t"
    "v_and_b32 v101, 2, v100\n\t"
    "v_mov_b32 v102, 0x44444444\n\t"
    "v_mov_b32 v103, 0xeeeeeeee\n\t"
    "v_cmp_eq_u32 vcc, 0, v101\n\t"
    "s_lshl_b32 s42, s89, 16\n\t"
    "s_and_b32 s43, s89, 0xffff0000\n\t"
    "v_cndmask_b32 v124, v103, v102, vcc\n\t"
    "v_lshrrev_b32 v101, 4, v100\n\t"
    "v_mul_u32_u24 v101, 8000, v101\n\t"
    "v_lshl_add_u32 v125, v155, 3, v101\n\t"
    "v_add_u32 v125, 64000, v125\n\t"
    "s_lshl_b32 s70, s72, 16\n\t"
    "s_and_b32 s71, s72, 0xffff0000\n\t"
    "s_lshl_b32 s72, s73, 16\n\t"
    "s_and_b32 s73, s73, 0xffff0000\n\t"
    "s_mov_b32 s67, 0xffff0000\n\t"
    "s_and_b32 s65, %14, 8\n\t"
    "s_add_u32 s38, s65, %16\n\t"
    "s_mul_i32 s38, s38, 212992\n\t"
    "s_add_u32 s38, s38, 196608\n\t"
    "s_add_u32 s84, s37, s38\n\t"
    "s_add_u32 s37, s37, 0x800\n\t"
    "s_and_b32 s37, s37, 0x3800\n\t"
    "s_add_u32 s85, s37, s38\n\t"
    "s_add_u32 s37, s37, 0x800\n\t"
    "s_and_b32 s37, s37, 0x3800\n\t"
    "s_add_u32 s86, s37, s38\n\t"
    "s_add_u32 s37, s37, 0x800\n\t"
    "s_and_b32 s37, s37, 0x3800\n\t"
    "s_add_u32 s87, s37, s38\n\t"
    "s_add_u32 s37, s37, 0x800\n\t"
    "s_and_b32 s37, s37, 0x3800\n\t"
    "s_add_u32 s88, s37, s38\n\t"
    "s_add_u32 s37, s37, 0x800\n\t"
    "s_and_b32 s37, s37, 0x3800\n\t"
    "s_add_u32 s89, s37, s38\n\t"
    "s_add_u32 s37, s37, 0x800\n\t"
    "s_and_b32 s37, s37, 0x3800\n\t"
    "s_add_u32 s90, s37, s38\n\t"
    "s_add_u32 s37, s37, 0x800\n\t"
    "s_and_b32 s37, s37, 0x3800\n\t"
    "s_add_u32 s91, s37, s38\n\t"
    "s_mul_i32 s94, s65, 425984\n\t"
    "s_sub_u32 s94, s94, 1703936\n\t"
    "s_sub_u32 s95, 0x8000, s94\n\t"
    "s_lshl_b32 s97, s65, 11\n\t"
    "s_sub_u32 s97, s97, 229376\n\t"
    "s_mov_b32 s92, 64000\n\t"
    "s_mov_b32 s93, -64000\n\t"
    "s_mov_b32 s96, 0x2000\n\t"
    "s_mov_b32 s62, 2\n\t"
    "v_rcp_f32 v101, s40\n\t"
    "v_rcp_f32 v102, s41\n\t"
    "v_rcp_f32 v103, s42\n\t"
    "v_rcp_f32 v104, s43\n\t"
    "v_mov_b32 v100, 0x7f7fffff\n\t"
    "v_med3_f32 v101, v101, -v100, v100\n\t"
    "v_med3_f32 v102, v102, -v100, v100\n\t"
    "v_med3_f32 v103, v103, -v100, v100\n\t"
    "v_med3_f32 v104, v104, -v100, v100\n\t"
    "v_readfirstlane_b32 s40, v101\n\t"
    "v_readfirstlane_b32 s41, v102\n\t"
    "v_readfirstlane_b32 s42, v103\n\t"
    "v_readfirstlane_b32 s43, v104\n\t"
    "v_lshrrev_b32 v105, 4, v127\n\t"
    "v_and_b32 v105, 48, v105\n\t"
    "s_movk_i32 s65, 130\n\t"
    "v_mad_u32_u24 v104, v155, s65, v105\n\t"
    "v_add_u32 v105, 14170, v104\n\t"
    "v_and_b32 v104, -16, v104\n\t"
    "v_and_b32 v105, -16, v105\n\t"
    "ds_read_b128 v[164:167], v104 offset:0\n\t"
    "ds_read_b128 v[168:171], v104 offset:64\n\t"
    "ds_read_b128 v[172:175], v104 offset:2080\n\t"
    "ds_read_b128 v[176:179], v104 offset:2144\n\t"
    "ds_read_b128 v[180:183], v104 offset:4160\n\t"
    "ds_read_b128 v[184:187], v104 offset:4224\n\t"
    "ds_read_b128 v[188:191], v104 offset:6240\n\t"
    "ds_read_b128 v[192:195], v104 offset:6304\n\t"
    "ds_read_b128 v[196:199], v104 offset:8320\n\t"
    "ds_read_b128 v[200:203], v104 offset:8384\n\t"
    "ds_read_b128 v[204:207], v104 offset:10400\n\t"
    "ds_read_b128 v[208:211], v104 offset:10464\n\t"
    "s_cmp_lt_u32 s62, 2\n\t"
    "s_cbranch_scc1 L_mel0_%=\n\t"
    "buffer_load_dwordx4 v[220:223], v127, %12, s61 offen offset:2048 sc1\n\t"
    "buffer_load_dwordx4 v[224:227], v127, %12, s61 offen offset:3072 sc1\n\t"
    "s_add_u32 s61, s61, 0x1000\n\t"
    "buffer_load_dwordx4 v[228:231], v127, %12, s61 offen offset:2048 sc1\n\t"
    "buffer_load_dwordx4 v[232:235], v127, %12, s61 offen offset:3072 sc1\n\t"
    "s_add_u32 s61, s61, 0x1000\n\t"
    "buffer_load_dwordx4 v[236:239], v127, %12, s61 offen offset:2048 sc1\n\t"
    "buffer_load_dwordx4 v[240:243], v127, %12, s61 offen offset:3072 sc1\n\t"
    "s_add_u32 s61, s61, 0x1000\n\t"
    "buffer_load_dwordx4 v[244:247], v127, %12, s61 offen offset:2048 sc1\n\t"
    "buffer_load_dwordx4 v[248:251], v127, %12, s61 offen offset:3072 sc1\n\t"
    "s_add_u32 s61, s61, 0x1000\n\t"
    "buffer_load_dwordx4 v[252:255], v127, %12, s61 offen offset:2048 sc1\n\t"
    "s_mov_b32 s62, 1\n\t"
    "L_mel0_%=:\n\t"
    "buffer_load_dwordx4 v[0:3], v127, %12, s63 offen offset:0 sc1\n\t"
    "buffer_load_dwordx4 v[4:7], v127, %12, s63 offen offset:1024 sc1\n\t"
    "buffer_load_dwordx4 v[8:11], v127, %12, s63 offen offset:2048 sc1\n\t"
    "buffer_load_dwordx4 v[12:15], v127, %12, s63 offen offset:3072 sc1\n\t"
    "s_add_u32 s63, s63, 0x1000\n\t"
    "s_mov_b32 s62, 0\n\t"
    "s_and_b32 s65, %14, 8\n\t"
    "s_lshl_b32 s65, s65, 10\n\t"
    "s_add_u32 s65, s65, s63\n\t"
    "s_mov_b32 m0, 136192\n\t"
    "s_add_u32 s65, s65, 0\n\t"
    "buffer_load_dwordx4 v127, %12, s65 offen offset:0 sc1 lds\n\t"
    "buffer_load_dwordx4 v127, %12, s65 offen offset:1024 sc1 lds\n\t"
    "buffer_load_dwordx4 v127, %12, s65 offen offset:2048 sc1 lds\n\t"
    "buffer_load_dwordx4 v127, %12, s65 offen offset:3072 sc1 lds\n\t"
    "s_mov_b32 m0, 140288\n\t"
    "s_add_u32 s65, s65, 4096\n\t"
    "buffer_load_dwordx4 v127, %12, s65 offen offset:0 sc1 lds\n\t"
    "buffer_load_dwordx4 v127, %12, s65 offen offset:1024 sc1 lds\n\t"
    "buffer_load_dwordx4 v127, %12, s65 offen offset:2048 sc1 lds\n\t"
    "buffer_load_dwordx4 v127, %12, s65 offen offset:3072 sc1 lds\n\t"
    "s_waitcnt vmcnt(21)\n\t"
    "s_and_b32 s50, %14, 7\n\t"
    "s_lshr_b32 s51, %14, 4\n\t"
    "s_add_u32 s51, s51, 8\n\t"
    "s_lshl_b32 s50, s50, 7\n\t"
    "s_lshl_b32 s51, s51, 7\n\t"
    "v_mov_b32 v132, s51\n\t"
    "v_writelane_b32 v132, s50, 0\n\t"
    "s_mov_b32 s50, 48237568\n\t"
    "s_mov_b64 exec, 3\n\t"
    "buffer_atomic_add v136, v132, %12, s50 offen sc1\n\t"
    "s_mov_b64 exec, -1\n\t"
    "s_mov_b32 s69, 0x40000000\n\t"
    "s_branch L_aend2_%=\n\t"
    "L_ahelp_%=:\n\t"
    "s_setprio 3\n\t"
    "v_cmp_gt_u32 vcc, 48, v147\n\t"
    "v_mov_b32 v133, 0x40000000\n\t"
    "v_lshrrev_b32 v134, 3, v147\n\t"
    "v_add_u32 v134, 6, v134\n\t"
    "v_bfe_u32 v135, v147, 2, 1\n\t"
    "v_and_b32 v130, 3, v147\n\t"
    "v_lshl_add_u32 v132, v135, 5, v130\n\t"
    "v_mul_u32_u24 v64, 400, v132\n\t"
    "v_lshl_add_u32 v64, v134, 5, v64\n\t"
    "s_movk_i32 s50, 0\n\t"
    "buffer_load_dwordx4 v[0:3], v64, %0, s50 offen offset:16\n\t"
    "buffer_load_dwordx4 v[4:7], v64, %0, s50 offen offset:32\n\t"
    "v_lshlrev_b32 v66, 14, v134\n\t"
    "v_lshl_add_u32 v66, v132, 4, v66\n\t"
    "v_lshlrev_b32 v68, 8, v134\n\t"
    "v_lshl_add_u32 v68, v135, 2, v68\n\t"
    "v_add_u32 v68, 130048, v68\n\t"
    "v_cndmask_b32 v68, v133, v68, vcc\n\t"
    "v_cndmask_b32 v66, v133, v66, vcc\n\t"
    "s_barrier\n\t"
    "s_load_dwordx2 s[72:73], %6, 0\n\t"
    "s_load_dwordx2 s[88:89], %7, 0\n\t"
    "s_load_dword s91, %8, 0\n\t"
    "v_lshlrev_b32 v128, 4, v147\n\t"
    "v_add_u32 v128, 133888, v128\n\t"
    "v_and_b32 v130, 1, v147\n\t"
    "v_cndmask_b32 v128, v133, v128, vcc\n\t"
    "v_mul_u32_u24 v127, 7, v130\n\t"
    "v_cmp_eq_u32 vcc, 1, v130\n\t"
    "v_mov_b32 v126, 0x07060100\n\t"
    "v_mov_b32 v133, 0x05040100\n\t"
    "v_mov_b32 v136, 1\n\t"
    "v_cndmask_b32 v126, v126, v133, vcc\n\t"
    "s_mov_b32 s36, 0\n\t"
    "s_mov_b32 s37, 8000\n\t"
    "s_mov_b32 s38, 16000\n\t"
    "s_mov_b32 s39, 24000\n\t"
    "s_mov_b32 s40, 32000\n\t"
    "s_mov_b32 s41, 40000\n\t"
    "s_mov_b32 s42, 48000\n\t"
    "s_mov_b32 s43, 56000\n\t"
    "s_movk_i32 s44, 0xf8\n\t"
    "s_mov_b32 s45, 0x01010000\n\t"
    "s_mov_b32 s46, 0x020c000c\n\t"
    "s_lshr_b32 s49, %14, 4\n\t"
    "s_mul_i32 s49, s49, 212992\n\t"
    "s_and_b32 s50, %14, 15\n\t"
    "s_lshl_b32 s50, s50, 10\n\t"
    "s_add_u32 s49, s49, s50\n\t"
    "s_lshl_b32 s52, %14, 2\n\t"
    "s_add_u32 s52, s52, 48242688\n\t"
    "buffer_load_dword v70, off, %12, s52 offset:1024 sc1\n\t"
    "s_cmp_eq_u32 %14, 0\n\t"
    "s_cselect_b32 s47, 48242688, 0x40000000\n\t"
    "s_cselect_b32 s48, 48234496, 0x40000000\n\t"
    "v_lshlrev_b32 v152, 4, v147\n\t"
    "v_lshlrev_b32 v132, 7, v147\n\t"
    "v_mov_b32 v71, 0\n\t"
    "buffer_store_dword v71, v132, %12, s48 offen sc1\n\t"
    "v_mov_b32 v148, 0\n\t"
    "v_mov_b32 v149, 0\n\t"
    "v_mov_b32 v150, 0\n\t"
    "v_mov_b32 v151, 0\n\t"
    "buffer_store_dwordx4 v[148:151], v152, %12, s47 offen offset:2048 sc1\n\t"
    "s_add_u32 s50, s48, 0x1000\n\t"
    "s_waitcnt vmcnt(5)\n\t"
    "v_lshlrev_b32 v134, 20, v147\n\t"
    "v_add_u32 v134, 2068, v134\n\t"
    "ds_add_u32 v134, v136\n\t"
    "v_mov_b32 v110, 65664\n\t"
    "v_mov_b32 v111, 131136\n\t"
    "v_mov_b32 v112, 262176\n\t"
    "v_mov_b32 v113, 524304\n\t"
    "v_mov_b32 v114, 1048584\n\t"
    "v_mov_b32 v115, 2097156\n\t"
    "v_mov_b32 v116, 4194306\n\t"
    "v_mov_b32 v117, 8388609\n\t"
    "s_waitcnt vmcnt(3)\n\t"
    "v_lshl_add_u32 v76, v0, 3, s36\n\t"
    "v_lshl_add_u32 v77, v1, 3, s37\n\t"
    "v_lshl_add_u32 v78, v2, 3, s38\n\t"
    "v_lshl_add_u32 v79, v3, 3, s39\n\t"
    "v_lshl_add_u32 v80, v4, 3, s40\n\t"
    "v_lshl_add_u32 v81, v5, 3, s41\n\t"
    "v_lshl_add_u32 v82, v6, 3, s42\n\t"
    "v_lshl_add_u32 v83, v7, 3, s43\n\t"
    "v_and_or_b32 v92, v76, s44, v68\n\t"
    "ds_or_rtn_b32 v118, v92, v110\n\t"
    "v_and_or_b32 v93, v77, s44, v68\n\t"
    "ds_or_rtn_b32 v119, v93, v111\n\t"
    "v_and_or_b32 v94, v78, s44, v68\n\t"
    "ds_or_rtn_b32 v120, v94, v112\n\t"
    "v_and_or_b32 v95, v79, s44, v68\n\t"
    "ds_or_rtn_b32 v121, v95, v113\n\t"
    "v_and_or_b32 v96, v80, s44, v68\n\t"
    "ds_or_rtn_b32 v122, v96, v114\n\t"
    "v_and_or_b32 v97, v81, s44, v68\n\t"
    "ds_or_rtn_b32 v123, v97, v115\n\t"
    "v_and_or_b32 v98, v82, s44, v68\n\t"
    "ds_or_rtn_b32 v124, v98, v116\n\t"
    "v_and_or_b32 v99, v83, s44, v68\n\t"
    "ds_or_rtn_b32 v125, v99, v117\n\t"
    "v_lshl_or_b32 v164, v77, 16, v76\n\t"
    "v_lshl_or_b32 v165, v79, 16, v78\n\t"
    "v_lshl_or_b32 v166, v81, 16, v80\n\t"
    "v_lshl_or_b32 v167, v83, 16, v82\n\t"
    "s_movk_i32 s50, 0\n\t"
    "buffer_load_dwordx4 v[8:11], v64, %0, s50 offen offset:1616\n\t"
    "buffer_load_dwordx4 v[12:15], v64, %0, s50 offen offset:1632\n\t"
    "s_movk_i32 s50, 3200\n\t"
    "buffer_load_dwordx4 v[16:19], v64, %0, s50 offen offset:16\n\t"
    "buffer_load_dwordx4 v[20:23], v64, %0, s50 offen offset:32\n\t"
    "s_waitcnt vmcnt(6)\n\t"
    "v_add_u32 v70, 1, v70\n\t"
    "buffer_store_dword v70, off, %12, s52 sc1\n\t"
    "s_waitcnt vmcnt(3)\n\t"
    "v_lshl_add_u32 v84, v8, 3, s36\n\t"
    "v_lshl_add_u32 v85, v9, 3, s37\n\t"
    "v_lshl_add_u32 v86, v10, 3, s38\n\t"
    "v_lshl_add_u32 v87, v11, 3, s39\n\t"
    "v_lshl_add_u32 v88, v12, 3, s40\n\t"
    "v_lshl_add_u32 v89, v13, 3, s41\n\t"
    "v_lshl_add_u32 v90, v14, 3, s42\n\t"
    "v_lshl_add_u32 v91, v15, 3, s43\n\t"
    "s_waitcnt lgkmcnt(4)\n\t"
    "v_and_b32 v118, v118, v110\n\t"
    "v_perm_b32 v118, v118, v118, s46\n\t"
    "ds_or_b32 v92, v118\n\t"
    "v_and_b32 v119, v119, v111\n\t"
    "v_perm_b32 v119, v119, v119, s46\n\t"
    "ds_or_b32 v93, v119\n\t"
    "v_and_b32 v120, v120, v112\n\t"
    "v_perm_b32 v120, v120, v120, s46\n\t"
    "ds_or_b32 v94, v120\n\t"
    "v_and_b32 v121, v121, v113\n\t"
    "v_perm_b32 v121, v121, v121, s46\n\t"
    "ds_or_b32 v95, v121\n\t"
    "s_waitcnt lgkmcnt(4)\n\t"
    "v_and_b32 v122, v122, v114\n\t"
    "v_perm_b32 v122, v122, v122, s46\n\t"
    "ds_or_b32 v96, v122\n\t"
    "v_and_b32 v123, v123, v115\n\t"
    "v_perm_b32 v123, v123, v123, s46\n\t"
    "ds_or_b32 v97, v123\n\t"
    "v_and_b32 v124, v124, v116\n\t"
    "v_perm_b32 v124, v124, v124, s46\n\t"
    "ds_or_b32 v98, v124\n\t"
    "v_and_b32 v125, v125, v117\n\t"
    "v_perm_b32 v125, v125, v125, s46\n\t"
    "ds_or_b32 v99, v125\n\t"
    "buffer_store_dwordx4 v[164:167], v66, %12, s49 offen offset:0 sc1\n\t"
    "v_and_or_b32 v100, v84, s44, v68\n\t"
    "ds_read_b32 v100, v100\n\t"
    "v_and_or_b32 v101, v85, s44, v68\n\t"
    "ds_read_b32 v101, v101\n\t"
    "v_and_or_b32 v102, v86, s44, v68\n\t"
    "ds_read_b32 v102, v102\n\t"
    "v_and_or_b32 v103, v87, s44, v68\n\t"
    "ds_read_b32 v103, v103\n\t"
    "v_and_or_b32 v104, v88, s44, v68\n\t"
    "ds_read_b32 v104, v104\n\t"
    "v_and_or_b32 v105, v89, s44, v68\n\t"
    "ds_read_b32 v105, v105\n\t"
    "v_and_or_b32 v106, v90, s44, v68\n\t"
    "ds_read_b32 v106, v106\n\t"
    "v_and_or_b32 v107, v91, s44, v68\n\t"
    "ds_read_b32 v107, v107\n\t"
    "s_waitcnt vmcnt(2)\n\t"
    "v_lshl_add_u32 v76, v16, 3, s36\n\t"
    "v_lshl_add_u32 v77, v17, 3, s37\n\t"
    "v_lshl_add_u32 v78, v18, 3, s38\n\t"
    "v_lshl_add_u32 v79, v19, 3, s39\n\t"
    "v_lshl_add_u32 v80, v20, 3, s40\n\t"
    "v_lshl_add_u32 v81, v21, 3, s41\n\t"
    "v_lshl_add_u32 v82, v22, 3, s42\n\t"
    "v_lshl_add_u32 v83, v23, 3, s43\n\t"
    "s_movk_i32 s50, 3200\n\t"
    "buffer_load_dwordx4 v[24:27], v64, %0, s50 offen offset:1616\n\t"
    "buffer_load_dwordx4 v[28:31], v64, %0, s50 offen offset:1632\n\t"
    "s_movk_i32 s50, 6400\n\t"
    "buffer_load_dwordx4 v[32:35], v64, %0, s50 offen offset:16\n\t"
    "buffer_load_dwordx4 v[36:39], v64, %0, s50 offen offset:32\n\t"
    "s_cmp_lg_u32 %14, 0\n\t"
    "s_cbranch_scc1 L_ahe181_%=\n\t"
    "buffer_load_dwordx4 v[148:151], v152, %12, s47 offen sc1\n\t"
    "L_ahe181_%=:\n\t"
    "v_mov_b32 v129, 0xffff0000\n\t"
    "s_waitcnt lgkmcnt(4)\n\t"
    "v_perm_b32 v100, v100, v84, v126\n\t"
    "v_perm_b32 v101, v101, v85, v126\n\t"
    "v_perm_b32 v102, v102, v86, v126\n\t"
    "v_perm_b32 v103, v103, v87, v126\n\t"
    "s_waitcnt lgkmcnt(0)\n\t"
    "v_perm_b32 v104, v104, v88, v126\n\t"
    "v_perm_b32 v105, v105, v89, v126\n\t"
    "v_perm_b32 v106, v106, v90, v126\n\t"
    "v_perm_b32 v107, v107, v91, v126\n\t"
    "v_max_u32 v108, v100, v101\n\t"
    "v_min_u32 v101, v100, v101\n\t"
    "v_max_u32 v100, v102, v103\n\t"
    "v_min_u32 v103, v102, v103\n\t"
    "v_max_u32 v102, v104, v105\n\t"
    "v_min_u32 v105, v104, v105\n\t"
    "v_max_u32 v104, v106, v107\n\t"
    "v_min_u32 v107, v106, v107\n\t"
    "v_max_u32 v106, v108, v100\n\t"
    "v_min_u32 v100, v108, v100\n\t"
    "v_max_u32 v108, v101, v103\n\t"
    "v_min_u32 v103, v101, v103\n\t"
    "v_max_u32 v101, v102, v104\n\t"
    "v_min_u32 v104, v102, v104\n\t"
    "v_max_u32 v102, v105, v107\n\t"
    "v_min_u32 v107, v105, v107\n\t"
    "v_bfi_b32 v130, v106, 0, v129\n\t"
    "v_cmp_eq_u32 vcc, 0, v130\n\t"
    "v_and_or_b32 v92, v106, s44, v68\n\t"
    "s_nop 0\n\t"
    "v_cndmask_b32 v130, v130, v129, vcc\n\t"
    "v_ffbl_b32 v131, v130\n\t"
    "v_and_b32 v134, 7, v131\n\t"
    "v_lshlrev_b32 v134, v134, s45\n\t"
    "v_xor_b32 v129, v129, v134\n\t"
    "v_xor_b32 v132, v131, v127\n\t"
    "v_bfi_b32 v130, v101, 0, v129\n\t"
    "v_cmp_eq_u32 vcc, 0, v130\n\t"
    "v_and_or_b32 v93, v101, s44, v68\n\t"
    "v_xor_b32 v133, 23, v132\n\t"
    "v_lshlrev_b32 v110, v132, v136\n\t"
    "v_and_b32 v132, 7, v132\n\t"
    "v_lshl_or_b32 v110, v136, v133, v110\n\t"
    "v_lshl_add_u32 v132, v132, 1, v128\n\t"
    "ds_or_rtn_b32 v118, v92, v110\n\t"
    "ds_write_b16 v132, v106\n\t"
    "v_cndmask_b32 v130, v130, v129, vcc\n\t"
    "v_ffbl_b32 v131, v130\n\t"
    "v_and_b32 v134, 7, v131\n\t"
    "v_lshlrev_b32 v134, v134, s45\n\t"
    "v_xor_b32 v129, v129, v134\n\t"
    "v_xor_b32 v132, v131, v127\n\t"
    "v_bfi_b32 v130, v108, 0, v129\n\t"
    "v_cmp_eq_u32 vcc, 0, v130\n\t"
    "v_and_or_b32 v94, v108, s44, v68\n\t"
    "v_xor_b32 v133, 23, v132\n\t"
    "v_lshlrev_b32 v111, v132, v136\n\t"
    "v_and_b32 v132, 7, v132\n\t"
    "v_lshl_or_b32 v111, v136, v133, v111\n\t"
    "v_lshl_add_u32 v132, v132, 1, v128\n\t"
    "ds_or_rtn_b32 v119, v93, v111\n\t"
    "ds_write_b16 v132, v101\n\t"
    "v_cndmask_b32 v130, v130, v129, vcc\n\t"
    "v_ffbl_b32 v131, v130\n\t"
    "v_and_b32 v134, 7, v131\n\t"
    "v_lshlrev_b32 v134, v134, s45\n\t"
    "v_xor_b32 v129, v129, v134\n\t"
    "v_xor_b32 v132, v131, v127\n\t"
    "v_bfi_b32 v130, v100, 0, v129\n\t"
    "v_cmp_eq_u32 vcc, 0, v130\n\t"
    "v_and_or_b32 v95, v100, s44, v68\n\t"
    "v_xor_b32 v133, 23, v132\n\t"
    "v_lshlrev_b32 v112, v132, v136\n\t"
    "v_and_b32 v132, 7, v132\n\t"
    "v_lshl_or_b32 v112, v136, v133, v112\n\t"
    "v_lshl_add_u32 v132, v132, 1, v128\n\t"
    "ds_or_rtn_b32 v120, v94, v112\n\t"
    "ds_write_b16 v132, v108\n\t"
    "v_cndmask_b32 v130, v130, v129, vcc\n\t"
    "v_ffbl_b32 v131, v130\n\t"
    "v_and_b32 v134, 7, v131\n\t"
    "v_lshlrev_b32 v134, v134, s45\n\t"
    "v_xor_b32 v129, v129, v134\n\t"
    "v_xor_b32 v132, v131, v127\n\t"
    "v_bfi_b32 v130, v102, 0, v129\n\t"
    "v_cmp_eq_u32 vcc, 0, v130\n\t"
    "v_and_or_b32 v96, v102, s44, v68\n\t"
    "v_xor_b32 v133, 23, v132\n\t"
    "v_lshlrev_b32 v113, v132, v136\n\t"
    "v_and_b32 v132, 7, v132\n\t"
    "v_lshl_or_b32 v113, v136, v133, v113\n\t"
    "v_lshl_add_u32 v132, v132, 1, v128\n\t"
    "ds_or_rtn_b32 v121, v95, v113\n\t"
    "ds_write_b16 v132, v100\n\t"
    "v_cndmask_b32 v130, v130, v129, vcc\n\t"
    "v_ffbl_b32 v131, v130\n\t"
    "v_and_b32 v134, 7, v131\n\t"
    "v_lshlrev_b32 v134, v134, s45\n\t"
    "v_xor_b32 v129, v129, v134\n\t"
    "v_xor_b32 v132, v131, v127\n\t"
    "s_waitcnt lgkmcnt(7)\n\t"
    "v_and_b32 v118, v118, v110\n\t"
    "v_perm_b32 v118, v118, v118, s46\n\t"
    "ds_or_b32 v92, v118\n\t"
    "v_bfi_b32 v130, v104, 0, v129\n\t"
    "v_cmp_eq_u32 vcc, 0, v130\n\t"
    "v_and_or_b32 v97, v104, s44, v68\n\t"
    "v_xor_b32 v133, 23, v132\n\t"
    "v_lshlrev_b32 v114, v132, v136\n\t"
    "v_and_b32 v132, 7, v132\n\t"
    "v_lshl_or_b32 v114, v136, v133, v114\n\t"
    "v_lshl_add_u32 v132, v132, 1, v128\n\t"
    "ds_or_rtn_b32 v122, v96, v114\n\t"
    "ds_write_b16 v132, v102\n\t"
    "v_cndmask_b32 v130, v130, v129, vcc\n\t"
    "v_ffbl_b32 v131, v130\n\t"
    "v_and_b32 v134, 7, v131\n\t"
    "v_lshlrev_b32 v134, v134, s45\n\t"
    "v_xor_b32 v129, v129, v134\n\t"
    "v_xor_b32 v132, v131, v127\n\t"
    "s_waitcnt lgkmcnt(8)\n\t"
    "v_and_b32 v119, v119, v111\n\t"
    "v_perm_b32 v119, v119, v119, s46\n\t"
    "ds_or_b32 v93, v119\n\t"
    "v_bfi_b32 v130, v103, 0, v129\n\t"
    "v_cmp_eq_u32 vcc, 0, v130\n\t"
    "v_and_or_b32 v98, v103, s44, v68\n\t"
    "v_xor_b32 v133, 23, v132\n\t"
    "v_lshlrev_b32 v115, v132, v136\n\t"
    "v_and_b32 v132, 7, v132\n\t"
    "v_lshl_or_b32 v115, v136, v133, v115\n\t"
    "v_lshl_add_u32 v132, v132, 1, v128\n\t"
    "ds_or_rtn_b32 v123, v97, v115\n\t"
    "ds_write_b16 v132, v104\n\t"
    "v_cndmask_b32 v130, v130, v129, vcc\n\t"
    "v_ffbl_b32 v131, v130\n\t"
    "v_and_b32 v134, 7, v131\n\t"
    "v_lshlrev_b32 v134, v134, s45\n\t"
    "v_xor_b32 v129, v129, v134\n\t"
    "v_xor_b32 v132, v131, v127\n\t"
    "s_waitcnt lgkmcnt(9)\n\t"
    "v_and_b32 v120, v120, v112\n\t"
    "v_perm_b32 v120, v120, v120, s46\n\t"
    "ds_or_b32 v94, v120\n\t"
    "v_bfi_b32 v130, v107, 0, v129\n\t"
    "v_cmp_eq_u32 vcc, 0, v130\n\t"
    "v_and_or_b32 v99, v107, s44, v68\n\t"
    "v_xor_b32 v133, 23, v132\n\t"
    "v_lshlrev_b32 v116, v132, v136\n\t"
    "v_and_b32 v132, 7, v132\n\t"
    "v_lshl_or_b32 v116, v136, v133, v116\n\t"
    "v_lshl_add_u32 v132, v132, 1, v128\n\t"
    "ds_or_rtn_b32 v124, v98, v116\n\t"
    "ds_write_b16 v132, v103\n\t"
    "v_cndmask_b32 v130, v130, v129, vcc\n\t"
    "v_ffbl_b32 v131, v130\n\t"
    "v_xor_b32 v132, v131, v127\n\t"
    "s_waitcnt lgkmcnt(10)\n\t"
    "v_and_b32 v121, v121, v113\n\t"
    "v_perm_b32 v121, v121, v121, s46\n\t"
    "ds_or_b32 v95, v121\n\t"
    "v_xor_b32 v133, 23, v132\n\t"
    "v_lshlrev_b32 v117, v132, v136\n\t"
    "v_and_b32 v132, 7, v132\n\t"
    "v_lshl_or_b32 v117, v136, v133, v117\n\t"
    "v_lshl_add_u32 v132, v132, 1, v128\n\t"
    "ds_or_rtn_b32 v125, v99, v117\n\t"
    "ds_write_b16 v132, v107\n\t"
    "s_waitcnt lgkmcnt(10)\n\t"
    "v_and_b32 v122, v122, v114\n\t"
    "v_perm_b32 v122, v122, v122, s46\n\t"
    "ds_or_b32 v96, v122\n\t"
    "ds_read_b128 v[72:75], v128\n\t"
    "v_and_or_b32 v100, v76, s44, v68\n\t"
    "ds_read_b32 v100, v100\n\t"
    "v_and_or_b32 v101, v77, s44, v68\n\t"
    "ds_read_b32 v101, v101\n\t"
    "v_and_or_b32 v102, v78, s44, v68\n\t"
    "ds_read_b32 v102, v102\n\t"
    "v_and_or_b32 v103, v79, s44, v68\n\t"
    "ds_read_b32 v103, v103\n\t"
    "v_and_or_b32 v104, v80, s44, v68\n\t"
    "ds_read_b32 v104, v104\n\t"
    "v_and_or_b32 v105, v81, s44, v68\n\t"
    "ds_read_b32 v105, v105\n\t"
    "v_and_or_b32 v106, v82, s44, v68\n\t"
    "ds_read_b32 v106, v106\n\t"
    "v_and_or_b32 v107, v83, s44, v68\n\t"
    "ds_read_b32 v107, v107\n\t"
    "s_cmp_lg_u32 %14, 0\n\t"
    "s_cbranch_scc1 L_ahe375_%=\n\t"
    "s_waitcnt vmcnt(0)\n\t"
    "buffer_store_dwordx4 v[148:151], v152, %12, s47 offen offset:1024 sc1\n\t"
    "L_ahe375_%=:\n\t"
    "s_waitcnt vmcnt(2)\n\t"
    "v_lshl_add_u32 v84, v24, 3, s36\n\t"
    "v_lshl_add_u32 v85, v25, 3, s37\n\t"
    "v_lshl_add_u32 v86, v26, 3, s38\n\t"
    "v_lshl_add_u32 v87, v27, 3, s39\n\t"
    "v_lshl_add_u32 v88, v28, 3, s40\n\t"
    "v_lshl_add_u32 v89, v29, 3, s41\n\t"
    "v_lshl_add_u32 v90, v30, 3, s42\n\t"
    "v_lshl_add_u32 v91, v31, 3, s43\n\t"
    "v_mul_u32_u24 v58, 400, v147\n\t"
    "buffer_load_dwordx4 v[48:51], v58, %0, 0 offen\n\t"
    "s_lshr_b32 s50, %14, 5\n\t"
    "s_mul_i32 s50, s50, 125\n\t"
    "s_bfe_u32 s51, %14, 0x30002\n\t"
    "s_lshl_b32 s51, s51, 4\n\t"
    "s_min_u32 s51, s51, 109\n\t"
    "s_add_u32 s50, s50, s51\n\t"
    "s_lshl_b32 s50, s50, 8\n\t"
    "s_and_b32 s51, %14, 3\n\t"
    "s_lshl_b32 s51, s51, 6\n\t"
    "s_add_u32 s50, s50, s51\n\t"
    "s_bitcmp1_b32 %14, 1\n\t"
    "s_cselect_b32 s50, s50, 0x40000000\n\t"
    "v_and_b32 v59, 48, v147\n\t"
    "v_and_b32 v60, 15, v147\n\t"
    "v_lshl_add_u32 v59, v60, 8, v59\n\t"
    "buffer_load_dwordx4 v[52:55], v59, %15, s50 offen\n\t"
    "s_cmp_lg_u32 %14, 0\n\t"
    "s_cbranch_scc1 L_ahe407_%=\n\t"
    "buffer_load_dwordx4 v[148:151], v152, %12, s47 offen sc1\n\t"
    "L_ahe407_%=:\n\t"
    "s_waitcnt lgkmcnt(11)\n\t"
    "v_and_b32 v123, v123, v115\n\t"
    "v_perm_b32 v123, v123, v123, s46\n\t"
    "ds_or_b32 v97, v123\n\t"
    "v_and_b32 v124, v124, v116\n\t"
    "v_perm_b32 v124, v124, v124, s46\n\t"
    "ds_or_b32 v98, v124\n\t"
    "v_and_b32 v125, v125, v117\n\t"
    "v_perm_b32 v125, v125, v125, s46\n\t"
    "ds_or_b32 v99, v125\n\t"
    "s_waitcnt lgkmcnt(11)\n\t"
    "buffer_store_dwordx4 v[72:75], v66, %12, s49 offen offset:64 sc1\n\t"
    "v_mov_b32 v129, 0xffff0000\n\t"
    "s_waitcnt lgkmcnt(7)\n\t"
    "v_perm_b32 v100, v100, v76, v126\n\t"
    "v_perm_b32 v101, v101, v77, v126\n\t"
    "v_perm_b32 v102, v102, v78, v126\n\t"
    "v_perm_b32 v103, v103, v79, v126\n\t"
    "s_waitcnt lgkmcnt(3)\n\t"
    "v_perm_b32 v104, v104, v80, v126\n\t"
    "v_perm_b32 v105, v105, v81, v126\n\t"
    "v_perm_b32 v106, v106, v82, v126\n\t"
    "v_perm_b32 v107, v107, v83, v126\n\t"
    "v_max_u32 v108, v100, v101\n\t"
    "v_min_u32 v101, v100, v101\n\t"
    "v_max_u32 v100, v102, v103\n\t"
    "v_min_u32 v103, v102, v103\n\t"
    "v_max_u32 v102, v104, v105\n\t"
    "v_min_u32 v105, v104, v105\n\t"
    "v_max_u32 v104, v106, v107\n\t"
    "v_min_u32 v107, v106, v107\n\t"
    "v_max_u32 v106, v108, v100\n\t"
    "v_min_u32 v100, v108, v100\n\t"
    "v_max_u32 v108, v101, v103\n\t"
    "v_min_u32 v103, v101, v103\n\t"
    "v_max_u32 v101, v102, v104\n\t"
    "v_min_u32 v104, v102, v104\n\t"
    "v_max_u32 v102, v105, v107\n\t"
    "v_min_u32 v107, v105, v107\n\t"
    "v_bfi_b32 v130, v106, 0, v129\n\t"
    "v_cmp_eq_u32 vcc, 0, v130\n\t"
    "v_and_or_b32 v92, v106, s44, v68\n\t"
    "s_nop 0\n\t"
    "v_cndmask_b32 v130, v130, v129, vcc\n\t"
    "v_ffbl_b32 v131, v130\n\t"
    "v_and_b32 v134, 7, v131\n\t"
    "v_lshlrev_b32 v134, v134, s45\n\t"
    "v_xor_b32 v129, v129, v134\n\t"
    "v_xor_b32 v132, v131, v127\n\t"
    "v_bfi_b32 v130, v101, 0, v129\n\t"
    "v_cmp_eq_u32 vcc, 0, v130\n\t"
    "v_and_or_b32 v93, v101, s44, v68\n\t"
    "v_xor_b32 v133, 23, v132\n\t"
    "v_lshlrev_b32 v110, v132, v136\n\t"
    "v_and_b32 v132, 7, v132\n\t"
    "v_lshl_or_b32 v110, v136, v133, v110\n\t"
    "v_lshl_add_u32 v132, v132, 1, v128\n\t"
    "ds_or_rtn_b32 v118, v92, v110\n\t"
    "ds_write_b16 v132, v106\n\t"
    "v_cndmask_b32 v130, v130, v129, vcc\n\t"
    "v_ffbl_b32 v131, v130\n\t"
    "v_and_b32 v134, 7, v131\n\t"
    "v_lshlrev_b32 v134, v134, s45\n\t"
    "v_xor_b32 v129, v129, v134\n\t"
    "v_xor_b32 v132, v131, v127\n\t"
    "v_bfi_b32 v130, v108, 0, v129\n\t"
    "v_cmp_eq_u32 vcc, 0, v130\n\t"
    "v_and_or_b32 v94, v108, s44, v68\n\t"
    "v_xor_b32 v133, 23, v132\n\t"
    "v_lshlrev_b32 v111, v132, v136\n\t"
    "v_and_b32 v132, 7, v132\n\t"
    "v_lshl_or_b32 v111, v136, v133, v111\n\t"
    "v_lshl_add_u32 v132, v132, 1, v128\n\t"
    "ds_or_rtn_b32 v119, v93, v111\n\t"
    "ds_write_b16 v132, v101\n\t"
    "v_cndmask_b32 v130, v130, v129, vcc\n\t"
    "v_ffbl_b32 v131, v130\n\t"
    "v_and_b32 v134, 7, v131\n\t"
    "v_lshlrev_b32 v134, v134, s45\n\t"
    "v_xor_b32 v129, v129, v134\n\t"
    "v_xor_b32 v132, v131, v127\n\t"
    "v_bfi_b32 v130, v100, 0, v129\n\t"
    "v_cmp_eq_u32 vcc, 0, v130\n\t"
    "v_and_or_b32 v95, v100, s44, v68\n\t"
    "v_xor_b32 v133, 23, v132\n\t"
    "v_lshlrev_b32 v112, v132, v136\n\t"
    "v_and_b32 v132, 7, v132\n\t"
    "v_lshl_or_b32 v112, v136, v133, v112\n\t"
    "v_lshl_add_u32 v132, v132, 1, v128\n\t"
    "ds_or_rtn_b32 v120, v94, v112\n\t"
    "ds_write_b16 v132, v108\n\t"
    "v_cndmask_b32 v130, v130, v129, vcc\n\t"
    "v_ffbl_b32 v131, v130\n\t"
    "v_and_b32 v134, 7, v131\n\t"
    "v_lshlrev_b32 v134, v134, s45\n\t"
    "v_xor_b32 v129, v129, v134\n\t"
    "v_xor_b32 v132, v131, v127\n\t"
    "v_bfi_b32 v130, v102, 0, v129\n\t"
    "v_cmp_eq_u32 vcc, 0, v130\n\t"
    "v_and_or_b32 v96, v102, s44, v68\n\t"
    "v_xor_b32 v133, 23, v132\n\t"
    "v_lshlrev_b32 v113, v132, v136\n\t"
    "v_and_b32 v132, 7, v132\n\t"
    "v_lshl_or_b32 v113, v136, v133, v113\n\t"
    "v_lshl_add_u32 v132, v132, 1, v128\n\t"
    "ds_or_rtn_b32 v121, v95, v113\n\t"
    "ds_write_b16 v132, v100\n\t"
    "v_cndmask_b32 v130, v130, v129, vcc\n\t"
    "v_ffbl_b32 v131, v130\n\t"
    "v_and_b32 v134, 7, v131\n\t"
    "v_lshlrev_b32 v134, v134, s45\n\t"
    "v_xor_b32 v129, v129, v134\n\t"
    "v_xor_b32 v132, v131, v127\n\t"
    "s_waitcnt lgkmcnt(7)\n\t"
    "v_and_b32 v118, v118, v110\n\t"
    "v_perm_b32 v118, v118, v118, s46\n\t"
    "ds_or_b32 v92, v118\n\t"
    "v_bfi_b32 v130, v104, 0, v129\n\t"
    "v_cmp_eq_u32 vcc, 0, v130\n\t"
    "v_and_or_b32 v97, v104, s44, v68\n\t"
    "v_xor_b32 v133, 23, v132\n\t"
    "v_lshlrev_b32 v114, v132, v136\n\t"
    "v_and_b32 v132, 7, v132\n\t"
    "v_lshl_or_b32 v114, v136, v133, v114\n\t"
    "v_lshl_add_u32 v132, v132, 1, v128\n\t"
    "ds_or_rtn_b32 v122, v96, v114\n\t"
    "ds_write_b16 v132, v102\n\t"
    "v_cndmask_b32 v130, v130, v129, vcc\n\t"
    "v_ffbl_b32 v131, v130\n\t"
    "v_and_b32 v134, 7, v131\n\t"
    "v_lshlrev_b32 v134, v134, s45\n\t"
    "v_xor_b32 v129, v129, v134\n\t"
    "v_xor_b32 v132, v131, v127\n\t"
    "s_waitcnt lgkmcnt(8)\n\t"
    "v_and_b32 v119, v119, v111\n\t"
    "v_perm_b32 v119, v119, v119, s46\n\t"
    "ds_or_b32 v93, v119\n\t"
    "v_bfi_b32 v130, v103, 0, v129\n\t"
    "v_cmp_eq_u32 vcc, 0, v130\n\t"
    "v_and_or_b32 v98, v103, s44, v68\n\t"
    "v_xor_b32 v133, 23, v132\n\t"
    "v_lshlrev_b32 v115, v132, v136\n\t"
    "v_and_b32 v132, 7, v132\n\t"
    "v_lshl_or_b32 v115, v136, v133, v115\n\t"
    "v_lshl_add_u32 v132, v132, 1, v128\n\t"
    "ds_or_rtn_b32 v123, v97, v115\n\t"
    "ds_write_b16 v132, v104\n\t"
    "v_cndmask_b32 v130, v130, v129, vcc\n\t"
    "v_ffbl_b32 v131, v130\n\t"
    "v_and_b32 v134, 7, v131\n\t"
    "v_lshlrev_b32 v134, v134, s45\n\t"
    "v_xor_b32 v129, v129, v134\n\t"
    "v_xor_b32 v132, v131, v127\n\t"
    "s_waitcnt lgkmcnt(9)\n\t"
    "v_and_b32 v120, v120, v112\n\t"
    "v_perm_b32 v120, v120, v120, s46\n\t"
    "ds_or_b32 v94, v120\n\t"
    "v_bfi_b32 v130, v107, 0, v129\n\t"
    "v_cmp_eq_u32 vcc, 0, v130\n\t"
    "v_and_or_b32 v99, v107, s44, v68\n\t"
    "v_xor_b32 v133, 23, v132\n\t"
    "v_lshlrev_b32 v116, v132, v136\n\t"
    "v_and_b32 v132, 7, v132\n\t"
    "v_lshl_or_b32 v116, v136, v133, v116\n\t"
    "v_lshl_add_u32 v132, v132, 1, v128\n\t"
    "ds_or_rtn_b32 v124, v98, v116\n\t"
    "ds_write_b16 v132, v103\n\t"
    "v_cndmask_b32 v130, v130, v129, vcc\n\t"
    "v_ffbl_b32 v131, v130\n\t"
    "v_xor_b32 v132, v131, v127\n\t"
    "s_waitcnt lgkmcnt(10)\n\t"
    "v_and_b32 v121, v121, v113\n\t"
    "v_perm_b32 v121, v121, v121, s46\n\t"
    "ds_or_b32 v95, v121\n\t"
    "v_xor_b32 v133, 23, v132\n\t"
    "v_lshlrev_b32 v117, v132, v136\n\t"
    "v_and_b32 v132, 7, v132\n\t"
    "v_lshl_or_b32 v117, v136, v133, v117\n\t"
    "v_lshl_add_u32 v132, v132, 1, v128\n\t"
    "ds_or_rtn_b32 v125, v99, v117\n\t"
    "ds_write_b16 v132, v107\n\t"
    "s_waitcnt lgkmcnt(10)\n\t"
    "v_and_b32 v122, v122, v114\n\t"
    "v_perm_b32 v122, v122, v122, s46\n\t"
    "ds_or_b32 v96, v122\n\t"
    "ds_read_b128 v[72:75], v128\n\t"
    "v_and_or_b32 v100, v84, s44, v68\n\t"
    "ds_read_b32 v100, v100\n\t"
    "v_and_or_b32 v101, v85, s44, v68\n\t"
    "ds_read_b32 v101, v101\n\t"
    "v_and_or_b32 v102, v86, s44, v68\n\t"
    "ds_read_b32 v102, v102\n\t"
    "v_and_or_b32 v103, v87, s44, v68\n\t"
    "ds_read_b32 v103, v103\n\t"
    "v_and_or_b32 v104, v88, s44, v68\n\t"
    "ds_read_b32 v104, v104\n\t"
    "v_and_or_b32 v105, v89, s44, v68\n\t"
    "ds_read_b32 v105, v105\n\t"
    "v_and_or_b32 v106, v90, s44, v68\n\t"
    "ds_read_b32 v106, v106\n\t"
    "v_and_or_b32 v107, v91, s44, v68\n\t"
    "ds_read_b32 v107, v107\n\t"
    "s_cmp_lg_u32 %14, 0\n\t"
    "s_cbranch_scc1 L_ahe613_%=\n\t"
    "s_waitcnt vmcnt(0)\n\t"
    "buffer_store_dwordx4 v[148:151], v152, %12, s47 offen offset:1024 sc1\n\t"
    "L_ahe613_%=:\n\t"
    "s_waitcnt vmcnt(3)\n\t"
    "v_lshl_add_u32 v76, v32, 3, s36\n\t"
    "v_lshl_add_u32 v77, v33, 3, s37\n\t"
    "v_lshl_add_u32 v78, v34, 3, s38\n\t"
    "v_lshl_add_u32 v79, v35, 3, s39\n\t"
    "v_lshl_add_u32 v80, v36, 3, s40\n\t"
    "v_lshl_add_u32 v81, v37, 3, s41\n\t"
    "v_lshl_add_u32 v82, v38, 3, s42\n\t"
    "v_lshl_add_u32 v83, v39, 3, s43\n\t"
    "s_waitcnt vmcnt(1)\n\t"
    "v_lshl_add_u32 v48, v48, 3, s36\n\t"
    "v_lshl_add_u32 v49, v49, 3, s37\n\t"
    "v_lshl_add_u32 v50, v50, 3, s38\n\t"
    "v_lshl_add_u32 v51, v51, 3, s39\n\t"
    "v_lshl_or_b32 v56, v49, 16, v48\n\t"
    "v_lshl_or_b32 v57, v51, 16, v50\n\t"
    "s_and_b32 s51, %14, 1\n\t"
    "s_mul_i32 s51, s51, 1016\n\t"
    "s_sub_u32 s50, s49, s51\n\t"
    "s_add_u32 s50, s50, 196608\n\t"
    "buffer_store_dwordx2 v[56:57], v152, %12, s50 offen sc1\n\t"
    "s_lshl_b32 s50, %14, 10\n\t"
    "s_add_u32 s50, s50, 16515072\n\t"
    "s_bitcmp1_b32 %14, 1\n\t"
    "s_cselect_b32 s50, s50, 0x40000000\n\t"
    "buffer_store_dwordx4 v[52:55], v152, %12, s50 offen sc1\n\t"
    "s_mul_i32 s50, %14, 69632\n\t"
    "s_add_u32 s50, s50, 30474240\n\t"
    "v_mov_b32 v60, 0\n\t"
    "v_lshlrev_b32 v61, 3, v152\n\t"
    "s_mov_b64 exec, 0xffffffff\n\t"
    "buffer_store_dword v60, v61, %12, s50 offen sc1\n\t"
    "s_lshl_b32 s50, %14, 7\n\t"
    "s_add_u32 s50, s50, 48245760\n\t"
    "v_lshrrev_b32 v62, 2, v152\n\t"
    "buffer_store_dword v60, v62, %12, s50 offen sc1\n\t"
    "s_mov_b64 exec, -1\n\t"
    "s_cmp_lg_u32 %14, 0\n\t"
    "s_cbranch_scc1 L_ahe655_%=\n\t"
    "buffer_load_dwordx4 v[148:151], v152, %12, s47 offen sc1\n\t"
    "L_ahe655_%=:\n\t"
    "s_waitcnt lgkmcnt(11)\n\t"
    "v_and_b32 v123, v123, v115\n\t"
    "v_perm_b32 v123, v123, v123, s46\n\t"
    "ds_or_b32 v97, v123\n\t"
    "v_and_b32 v124, v124, v116\n\t"
    "v_perm_b32 v124, v124, v124, s46\n\t"
    "ds_or_b32 v98, v124\n\t"
    "v_and_b32 v125, v125, v117\n\t"
    "v_perm_b32 v125, v125, v125, s46\n\t"
    "ds_or_b32 v99, v125\n\t"
    "s_waitcnt lgkmcnt(11)\n\t"
    "buffer_store_dwordx4 v[72:75], v66, %12, s49 offen offset:128 sc1\n\t"
    "v_mov_b32 v129, 0xffff0000\n\t"
    "s_waitcnt lgkmcnt(7)\n\t"
    "v_perm_b32 v100, v100, v84, v126\n\t"
    "v_perm_b32 v101, v101, v85, v126\n\t"
    "v_perm_b32 v102, v102, v86, v126\n\t"
    "v_perm_b32 v103, v103, v87, v126\n\t"
    "s_waitcnt lgkmcnt(3)\n\t"
    "v_perm_b32 v104, v104, v88, v126\n\t"
    "v_perm_b32 v105, v105, v89, v126\n\t"
    "v_perm_b32 v106, v106, v90, v126\n\t"
    "v_perm_b32 v107, v107, v91, v126\n\t"
    "v_max_u32 v108, v100, v101\n\t"
    "v_min_u32 v101, v100, v101\n\t"
    "v_max_u32 v100, v102, v103\n\t"
    "v_min_u32 v103, v102, v103\n\t"
    "v_max_u32 v102, v104, v105\n\t"
    "v_min_u32 v105, v104, v105\n\t"
    "v_max_u32 v104, v106, v107\n\t"
    "v_min_u32 v107, v106, v107\n\t"
    "v_max_u32 v106, v108, v100\n\t"
    "v_min_u32 v100, v108, v100\n\t"
    "v_max_u32 v108, v101, v103\n\t"
    "v_min_u32 v103, v101, v103\n\t"
    "v_max_u32 v101, v102, v104\n\t"
    "v_min_u32 v104, v102, v104\n\t"
    "v_max_u32 v102, v105, v107\n\t"
    "v_min_u32 v107, v105, v107\n\t"
    "v_bfi_b32 v130, v106, 0, v129\n\t"
    "v_cmp_eq_u32 vcc, 0, v130\n\t"
    "v_and_or_b32 v92, v106, s44, v68\n\t"
    "s_nop 0\n\t"
    "v_cndmask_b32 v130, v130, v129, vcc\n\t"
    "v_ffbl_b32 v131, v130\n\t"
    "v_and_b32 v134, 7, v131\n\t"
    "v_lshlrev_b32 v134, v134, s45\n\t"
    "v_xor_b32 v129, v129, v134\n\t"
    "v_xor_b32 v132, v131, v127\n\t"
    "v_bfi_b32 v130, v101, 0, v129\n\t"
    "v_cmp_eq_u32 vcc, 0, v130\n\t"
    "v_and_or_b32 v93, v101, s44, v68\n\t"
    "v_xor_b32 v133, 23, v132\n\t"
    "v_lshlrev_b32 v110, v132, v136\n\t"
    "v_and_b32 v132, 7, v132\n\t"
    "v_lshl_or_b32 v110, v136, v133, v110\n\t"
    "v_lshl_add_u32 v132, v132, 1, v128\n\t"
    "ds_or_rtn_b32 v118, v92, v110\n\t"
    "ds_write_b16 v132, v106\n\t"
    "v_cndmask_b32 v130, v130, v129, vcc\n\t"
    "v_ffbl_b32 v131, v130\n\t"
    "v_and_b32 v134, 7, v131\n\t"
    "v_lshlrev_b32 v134, v134, s45\n\t"
    "v_xor_b32 v129, v129, v134\n\t"
    "v_xor_b32 v132, v131, v127\n\t"
    "v_bfi_b32 v130, v108, 0, v129\n\t"
    "v_cmp_eq_u32 vcc, 0, v130\n\t"
    "v_and_or_b32 v94, v108, s44, v68\n\t"
    "v_xor_b32 v133, 23, v132\n\t"
    "v_lshlrev_b32 v111, v132, v136\n\t"
    "v_and_b32 v132, 7, v132\n\t"
    "v_lshl_or_b32 v111, v136, v133, v111\n\t"
    "v_lshl_add_u32 v132, v132, 1, v128\n\t"
    "ds_or_rtn_b32 v119, v93, v111\n\t"
    "ds_write_b16 v132, v101\n\t"
    "v_cndmask_b32 v130, v130, v129, vcc\n\t"
    "v_ffbl_b32 v131, v130\n\t"
    "v_and_b32 v134, 7, v131\n\t"
    "v_lshlrev_b32 v134, v134, s45\n\t"
    "v_xor_b32 v129, v129, v134\n\t"
    "v_xor_b32 v132, v131, v127\n\t"
    "v_bfi_b32 v130, v100, 0, v129\n\t"
    "v_cmp_eq_u32 vcc, 0, v130\n\t"
    "v_and_or_b32 v95, v100, s44, v68\n\t"
    "v_xor_b32 v133, 23, v132\n\t"
    "v_lshlrev_b32 v112, v132, v136\n\t"
    "v_and_b32 v132, 7, v132\n\t"
    "v_lshl_or_b32 v112, v136, v133, v112\n\t"
    "v_lshl_add_u32 v132, v132, 1, v128\n\t"
    "ds_or_rtn_b32 v120, v94, v112\n\t"
    "ds_write_b16 v132, v108\n\t"
    "v_cndmask_b32 v130, v130, v129, vcc\n\t"
    "v_ffbl_b32 v131, v130\n\t"
    "v_and_b32 v134, 7, v131\n\t"
    "v_lshlrev_b32 v134, v134, s45\n\t"
    "v_xor_b32 v129, v129, v134\n\t"
    "v_xor_b32 v132, v131, v127\n\t"
    "v_bfi_b32 v130, v102, 0, v129\n\t"
    "v_cmp_eq_u32 vcc, 0, v130\n\t"
    "v_and_or_b32 v96, v102, s44, v68\n\t"
    "v_xor_b32 v133, 23, v132\n\t"
    "v_lshlrev_b32 v113, v132, v136\n\t"
    "v_and_b32 v132, 7, v132\n\t"
    "v_lshl_or_b32 v113, v136, v133, v113\n\t"
    "v_lshl_add_u32 v132, v132, 1, v128\n\t"
    "ds_or_rtn_b32 v121, v95, v113\n\t"
    "ds_write_b16 v132, v100\n\t"
    "v_cndmask_b32 v130, v130, v129, vcc\n\t"
    "v_ffbl_b32 v131, v130\n\t"
    "v_and_b32 v134, 7, v131\n\t"
    "v_lshlrev_b32 v134, v134, s45\n\t"
    "v_xor_b32 v129, v129, v134\n\t"
    "v_xor_b32 v132, v131, v127\n\t"
    "s_waitcnt lgkmcnt(7)\n\t"
    "v_and_b32 v118, v118, v110\n\t"
    "v_perm_b32 v118, v118, v118, s46\n\t"
    "ds_or_b32 v92, v118\n\t"
    "v_bfi_b32 v130, v104, 0, v129\n\t"
    "v_cmp_eq_u32 vcc, 0, v130\n\t"
    "v_and_or_b32 v97, v104, s44, v68\n\t"
    "v_xor_b32 v133, 23, v132\n\t"
    "v_lshlrev_b32 v114, v132, v136\n\t"
    "v_and_b32 v132, 7, v132\n\t"
    "v_lshl_or_b32 v114, v136, v133, v114\n\t"
    "v_lshl_add_u32 v132, v132, 1, v128\n\t"
    "ds_or_rtn_b32 v122, v96, v114\n\t"
    "ds_write_b16 v132, v102\n\t"
    "v_cndmask_b32 v130, v130, v129, vcc\n\t"
    "v_ffbl_b32 v131, v130\n\t"
    "v_and_b32 v134, 7, v131\n\t"
    "v_lshlrev_b32 v134, v134, s45\n\t"
    "v_xor_b32 v129, v129, v134\n\t"
    "v_xor_b32 v132, v131, v127\n\t"
    "s_waitcnt lgkmcnt(8)\n\t"
    "v_and_b32 v119, v119, v111\n\t"
    "v_perm_b32 v119, v119, v119, s46\n\t"
    "ds_or_b32 v93, v119\n\t"
    "v_bfi_b32 v130, v103, 0, v129\n\t"
    "v_cmp_eq_u32 vcc, 0, v130\n\t"
    "v_and_or_b32 v98, v103, s44, v68\n\t"
    "v_xor_b32 v133, 23, v132\n\t"
    "v_lshlrev_b32 v115, v132, v136\n\t"
    "v_and_b32 v132, 7, v132\n\t"
    "v_lshl_or_b32 v115, v136, v133, v115\n\t"
    "v_lshl_add_u32 v132, v132, 1, v128\n\t"
    "ds_or_rtn_b32 v123, v97, v115\n\t"
    "ds_write_b16 v132, v104\n\t"
    "v_cndmask_b32 v130, v130, v129, vcc\n\t"
    "v_ffbl_b32 v131, v130\n\t"
    "v_and_b32 v134, 7, v131\n\t"
    "v_lshlrev_b32 v134, v134, s45\n\t"
    "v_xor_b32 v129, v129, v134\n\t"
    "v_xor_b32 v132, v131, v127\n\t"
    "s_waitcnt lgkmcnt(9)\n\t"
    "v_and_b32 v120, v120, v112\n\t"
    "v_perm_b32 v120, v120, v120, s46\n\t"
    "ds_or_b32 v94, v120\n\t"
    "v_bfi_b32 v130, v107, 0, v129\n\t"
    "v_cmp_eq_u32 vcc, 0, v130\n\t"
    "v_and_or_b32 v99, v107, s44, v68\n\t"
    "v_xor_b32 v133, 23, v132\n\t"
    "v_lshlrev_b32 v116, v132, v136\n\t"
    "v_and_b32 v132, 7, v132\n\t"
    "v_lshl_or_b32 v116, v136, v133, v116\n\t"
    "v_lshl_add_u32 v132, v132, 1, v128\n\t"
    "ds_or_rtn_b32 v124, v98, v116\n\t"
    "ds_write_b16 v132, v103\n\t"
    "v_cndmask_b32 v130, v130, v129, vcc\n\t"
    "v_ffbl_b32 v131, v130\n\t"
    "v_xor_b32 v132, v131, v127\n\t"
    "s_waitcnt lgkmcnt(10)\n\t"
    "v_and_b32 v121, v121, v113\n\t"
    "v_perm_b32 v121, v121, v121, s46\n\t"
    "ds_or_b32 v95, v121\n\t"
    "v_xor_b32 v133, 23, v132\n\t"
    "v_lshlrev_b32 v117, v132, v136\n\t"
    "v_and_b32 v132, 7, v132\n\t"
    "v_lshl_or_b32 v117, v136, v133, v117\n\t"
    "v_lshl_add_u32 v132, v132, 1, v128\n\t"
    "ds_or_rtn_b32 v125, v99, v117\n\t"
    "ds_write_b16 v132, v107\n\t"
    "s_waitcnt lgkmcnt(10)\n\t"
    "v_and_b32 v122, v122, v114\n\t"
    "v_perm_b32 v122, v122, v122, s46\n\t"
    "ds_or_b32 v96, v122\n\t"
    "ds_read_b128 v[72:75], v128\n\t"
    "v_and_or_b32 v100, v76, s44, v68\n\t"
    "ds_read_b32 v100, v100\n\t"
    "v_and_or_b32 v101, v77, s44, v68\n\t"
    "ds_read_b32 v101, v101\n\t"
    "v_and_or_b32 v102, v78, s44, v68\n\t"
    "ds_read_b32 v102, v102\n\t"
    "v_and_or_b32 v103, v79, s44, v68\n\t"
    "ds_read_b32 v103, v103\n\t"
    "v_and_or_b32 v104, v80, s44, v68\n\t"
    "ds_read_b32 v104, v104\n\t"
    "v_and_or_b32 v105, v81, s44, v68\n\t"
    "ds_read_b32 v105, v105\n\t"
    "v_and_or_b32 v106, v82, s44, v68\n\t"
    "ds_read_b32 v106, v106\n\t"
    "v_and_or_b32 v107, v83, s44, v68\n\t"
    "ds_read_b32 v107, v107\n\t"
    "s_cmp_lg_u32 %14, 0\n\t"
    "s_cbranch_scc1 L_ahe861_%=\n\t"
    "s_waitcnt vmcnt(0)\n\t"
    "buffer_store_dwordx4 v[148:151], v152, %12, s47 offen offset:1024 sc1\n\t"
    "L_ahe861_%=:\n\t"
    "s_cmp_lg_u32 %14, 0\n\t"
    "s_cbranch_scc1 L_ahe866_%=\n\t"
    "buffer_load_dwordx4 v[148:151], v152, %12, s47 offen sc1\n\t"
    "L_ahe866_%=:\n\t"
    "s_waitcnt lgkmcnt(11)\n\t"
    "v_and_b32 v123, v123, v115\n\t"
    "v_perm_b32 v123, v123, v123, s46\n\t"
    "ds_or_b32 v97, v123\n\t"
    "v_and_b32 v124, v124, v116\n\t"
    "v_perm_b32 v124, v124, v124, s46\n\t"
    "ds_or_b32 v98, v124\n\t"
    "v_and_b32 v125, v125, v117\n\t"
    "v_perm_b32 v125, v125, v125, s46\n\t"
    "ds_or_b32 v99, v125\n\t"
    "s_waitcnt lgkmcnt(11)\n\t"
    "buffer_store_dwordx4 v[72:75], v66, %12, s49 offen offset:192 sc1\n\t"
    "v_mov_b32 v129, 0xffff0000\n\t"
    "s_waitcnt lgkmcnt(7)\n\t"
    "v_perm_b32 v100, v100, v76, v126\n\t"
    "v_perm_b32 v101, v101, v77, v126\n\t"
    "v_perm_b32 v102, v102, v78, v126\n\t"
    "v_perm_b32 v103, v103, v79, v126\n\t"
    "s_waitcnt lgkmcnt(3)\n\t"
    "v_perm_b32 v104, v104, v80, v126\n\t"
    "v_perm_b32 v105, v105, v81, v126\n\t"
    "v_perm_b32 v106, v106, v82, v126\n\t"
    "v_perm_b32 v107, v107, v83, v126\n\t"
    "v_max_u32 v108, v100, v101\n\t"
    "v_min_u32 v101, v100, v101\n\t"
    "v_max_u32 v100, v102, v103\n\t"
    "v_min_u32 v103, v102, v103\n\t"
    "v_max_u32 v102, v104, v105\n\t"
    "v_min_u32 v105, v104, v105\n\t"
    "v_max_u32 v104, v106, v107\n\t"
    "v_min_u32 v107, v106, v107\n\t"
    "v_max_u32 v106, v108, v100\n\t"
    "v_min_u32 v100, v108, v100\n\t"
    "v_max_u32 v108, v101, v103\n\t"
    "v_min_u32 v103, v101, v103\n\t"
    "v_max_u32 v101, v102, v104\n\t"
    "v_min_u32 v104, v102, v104\n\t"
    "v_max_u32 v102, v105, v107\n\t"
    "v_min_u32 v107, v105, v107\n\t"
    "v_bfi_b32 v130, v106, 0, v129\n\t"
    "v_cmp_eq_u32 vcc, 0, v130\n\t"
    "v_and_or_b32 v92, v106, s44, v68\n\t"
    "s_nop 0\n\t"
    "v_cndmask_b32 v130, v130, v129, vcc\n\t"
    "v_ffbl_b32 v131, v130\n\t"
    "v_and_b32 v134, 7, v131\n\t"
    "v_lshlrev_b32 v134, v134, s45\n\t"
    "v_xor_b32 v129, v129, v134\n\t"
    "v_xor_b32 v132, v131, v127\n\t"
    "v_bfi_b32 v130, v101, 0, v129\n\t"
    "v_cmp_eq_u32 vcc, 0, v130\n\t"
    "v_and_or_b32 v93, v101, s44, v68\n\t"
    "v_xor_b32 v133, 23, v132\n\t"
    "v_lshlrev_b32 v110, v132, v136\n\t"
    "v_and_b32 v132, 7, v132\n\t"
    "v_lshl_or_b32 v110, v136, v133, v110\n\t"
    "v_lshl_add_u32 v132, v132, 1, v128\n\t"
    "ds_or_rtn_b32 v118, v92, v110\n\t"
    "ds_write_b16 v132, v106\n\t"
    "v_cndmask_b32 v130, v130, v129, vcc\n\t"
    "v_ffbl_b32 v131, v130\n\t"
    "v_and_b32 v134, 7, v131\n\t"
    "v_lshlrev_b32 v134, v134, s45\n\t"
    "v_xor_b32 v129, v129, v134\n\t"
    "v_xor_b32 v132, v131, v127\n\t"
    "v_bfi_b32 v130, v108, 0, v129\n\t"
    "v_cmp_eq_u32 vcc, 0, v130\n\t"
    "v_and_or_b32 v94, v108, s44, v68\n\t"
    "v_xor_b32 v133, 23, v132\n\t"
    "v_lshlrev_b32 v111, v132, v136\n\t"
    "v_and_b32 v132, 7, v132\n\t"
    "v_lshl_or_b32 v111, v136, v133, v111\n\t"
    "v_lshl_add_u32 v132, v132, 1, v128\n\t"
    "ds_or_rtn_b32 v119, v93, v111\n\t"
    "ds_write_b16 v132, v101\n\t"
    "v_cndmask_b32 v130, v130, v129, vcc\n\t"
    "v_ffbl_b32 v131, v130\n\t"
    "v_and_b32 v134, 7, v131\n\t"
    "v_lshlrev_b32 v134, v134, s45\n\t"
    "v_xor_b32 v129, v129, v134\n\t"
    "v_xor_b32 v132, v131, v127\n\t"
    "v_bfi_b32 v130, v100, 0, v129\n\t"
    "v_cmp_eq_u32 vcc, 0, v130\n\t"
    "v_and_or_b32 v95, v100, s44, v68\n\t"
    "v_xor_b32 v133, 23, v132\n\t"
    "v_lshlrev_b32 v112, v132, v136\n\t"
    "v_and_b32 v132, 7, v132\n\t"
    "v_lshl_or_b32 v112, v136, v133, v112\n\t"
    "v_lshl_add_u32 v132, v132, 1, v128\n\t"
    "ds_or_rtn_b32 v120, v94, v112\n\t"
    "ds_write_b16 v132, v108\n\t"
    "v_cndmask_b32 v130, v130, v129, vcc\n\t"
    "v_ffbl_b32 v131, v130\n\t"
    "v_and_b32 v134, 7, v131\n\t"
    "v_lshlrev_b32 v134, v134, s45\n\t"
    "v_xor_b32 v129, v129, v134\n\t"
    "v_xor_b32 v132, v131, v127\n\t"
    "v_bfi_b32 v130, v102, 0, v129\n\t"
    "v_cmp_eq_u32 vcc, 0, v130\n\t"
    "v_and_or_b32 v96, v102, s44, v68\n\t"
    "v_xor_b32 v133, 23, v132\n\t"
    "v_lshlrev_b32 v113, v132, v136\n\t"
    "v_and_b32 v132, 7, v132\n\t"
    "v_lshl_or_b32 v113, v136, v133, v113\n\t"
    "v_lshl_add_u32 v132, v132, 1, v128\n\t"
    "ds_or_rtn_b32 v121, v95, v113\n\t"
    "ds_write_b16 v132, v100\n\t"
    "v_cndmask_b32 v130, v130, v129, vcc\n\t"
    "v_ffbl_b32 v131, v130\n\t"
    "v_and_b32 v134, 7, v131\n\t"
    "v_lshlrev_b32 v134, v134, s45\n\t"
    "v_xor_b32 v129, v129, v134\n\t"
    "v_xor_b32 v132, v131, v127\n\t"
    "s_waitcnt lgkmcnt(7)\n\t"
    "v_and_b32 v118, v118, v110\n\t"
    "v_perm_b32 v118, v118, v118, s46\n\t"
    "ds_or_b32 v92, v118\n\t"
    "v_bfi_b32 v130, v104, 0, v129\n\t"
    "v_cmp_eq_u32 vcc, 0, v130\n\t"
    "v_and_or_b32 v97, v104, s44, v68\n\t"
    "v_xor_b32 v133, 23, v132\n\t"
    "v_lshlrev_b32 v114, v132, v136\n\t"
    "v_and_b32 v132, 7, v132\n\t"
    "v_lshl_or_b32 v114, v136, v133, v114\n\t"
    "v_lshl_add_u32 v132, v132, 1, v128\n\t"
    "ds_or_rtn_b32 v122, v96, v114\n\t"
    "ds_write_b16 v132, v102\n\t"
    "v_cndmask_b32 v130, v130, v129, vcc\n\t"
    "v_ffbl_b32 v131, v130\n\t"
    "v_and_b32 v134, 7, v131\n\t"
    "v_lshlrev_b32 v134, v134, s45\n\t"
    "v_xor_b32 v129, v129, v134\n\t"
    "v_xor_b32 v132, v131, v127\n\t"
    "s_waitcnt lgkmcnt(8)\n\t"
    "v_and_b32 v119, v119, v111\n\t"
    "v_perm_b32 v119, v119, v119, s46\n\t"
    "ds_or_b32 v93, v119\n\t"
    "v_bfi_b32 v130, v103, 0, v129\n\t"
    "v_cmp_eq_u32 vcc, 0, v130\n\t"
    "v_and_or_b32 v98, v103, s44, v68\n\t"
    "v_xor_b32 v133, 23, v132\n\t"
    "v_lshlrev_b32 v115, v132, v136\n\t"
    "v_and_b32 v132, 7, v132\n\t"
    "v_lshl_or_b32 v115, v136, v133, v115\n\t"
    "v_lshl_add_u32 v132, v132, 1, v128\n\t"
    "ds_or_rtn_b32 v123, v97, v115\n\t"
    "ds_write_b16 v132, v104\n\t"
    "v_cndmask_b32 v130, v130, v129, vcc\n\t"
    "v_ffbl_b32 v131, v130\n\t"
    "v_and_b32 v134, 7, v131\n\t"
    "v_lshlrev_b32 v134, v134, s45\n\t"
    "v_xor_b32 v129, v129, v134\n\t"
    "v_xor_b32 v132, v131, v127\n\t"
    "s_waitcnt lgkmcnt(9)\n\t"
    "v_and_b32 v120, v120, v112\n\t"
    "v_perm_b32 v120, v120, v120, s46\n\t"
    "ds_or_b32 v94, v120\n\t"
    "v_bfi_b32 v130, v107, 0, v129\n\t"
    "v_cmp_eq_u32 vcc, 0, v130\n\t"
    "v_and_or_b32 v99, v107, s44, v68\n\t"
    "v_xor_b32 v133, 23, v132\n\t"
    "v_lshlrev_b32 v116, v132, v136\n\t"
    "v_and_b32 v132, 7, v132\n\t"
    "v_lshl_or_b32 v116, v136, v133, v116\n\t"
    "v_lshl_add_u32 v132, v132, 1, v128\n\t"
    "ds_or_rtn_b32 v124, v98, v116\n\t"
    "ds_write_b16 v132, v103\n\t"
    "v_cndmask_b32 v130, v130, v129, vcc\n\t"
    "v_ffbl_b32 v131, v130\n\t"
    "v_xor_b32 v132, v131, v127\n\t"
    "s_waitcnt lgkmcnt(10)\n\t"
    "v_and_b32 v121, v121, v113\n\t"
    "v_perm_b32 v121, v121, v121, s46\n\t"
    "ds_or_b32 v95, v121\n\t"
    "v_xor_b32 v133, 23, v132\n\t"
    "v_lshlrev_b32 v117, v132, v136\n\t"
    "v_and_b32 v132, 7, v132\n\t"
    "v_lshl_or_b32 v117, v136, v133, v117\n\t"
    "v_lshl_add_u32 v132, v132, 1, v128\n\t"
    "ds_or_rtn_b32 v125, v99, v117\n\t"
    "ds_write_b16 v132, v107\n\t"
    "s_waitcnt lgkmcnt(10)\n\t"
    "v_and_b32 v122, v122, v114\n\t"
    "v_perm_b32 v122, v122, v122, s46\n\t"
    "ds_or_b32 v96, v122\n\t"
    "ds_read_b128 v[72:75], v128\n\t"
    "s_waitcnt lgkmcnt(3)\n\t"
    "v_and_b32 v123, v123, v115\n\t"
    "v_perm_b32 v123, v123, v123, s46\n\t"
    "ds_or_b32 v97, v123\n\t"
    "v_and_b32 v124, v124, v116\n\t"
    "v_perm_b32 v124, v124, v124, s46\n\t"
    "ds_or_b32 v98, v124\n\t"
    "v_and_b32 v125, v125, v117\n\t"
    "v_perm_b32 v125, v125, v125, s46\n\t"
    "ds_or_b32 v99, v125\n\t"
    "s_waitcnt lgkmcnt(3)\n\t"
    "buffer_store_dwordx4 v[72:75], v66, %12, s49 offen offset:256 sc1\n\t"
    "v_mov_b32 v134, 1036\n\t"
    "ds_write_b32 v134, v70 offset:1036\n\t"
    "ds_write_b32 v134, v136\n\t"
    "s_cmp_lg_u32 %14, 0\n\t"
    "s_cbranch_scc1 L_ahsend_%=\n\t"
    "v_mov_b32 v132, 3\n\t"
    "s_waitcnt vmcnt(0)\n\t"
    "ds_write_b32 v134, v132\n\t"
    "buffer_store_dwordx4 v[148:151], v152, %12, s47 offen offset:1024 sc1\n\t"
    "s_setprio 0\n\t"
    "v_and_b32 v132, 0x70, v152\n\t"
    "v_lshlrev_b32 v132, 3, v132\n\t"
    "s_mov_b32 s51, 1048576\n\t"
    "L_ahs2_%=:\n\t"
    "s_sleep 8\n\t"
    "buffer_load_dword v71, v132, %12, s48 offen offset:1024 sc1\n\t"
    "buffer_load_dwordx4 v[148:151], v152, %12, s47 offen sc1\n\t"
    "s_sub_u32 s51, s51, 1\n\t"
    "s_cmp_eq_u32 s51, 0\n\t"
    "s_cbranch_scc1 L_ahsend_%=\n\t"
    "s_waitcnt vmcnt(0)\n\t"
    "buffer_store_dwordx4 v[148:151], v152, %12, s47 offen offset:1024 sc1\n\t"
    "v_cmp_gt_u32 vcc, 32, v71\n\t"
    "s_cbranch_vccnz L_ahs2_%=\n\t"
    "L_ahsend_%=:\n\t"
    "s_setprio 0\n\t"
    "s_and_b32 s46, %16, 3\n\t"
    "s_mul_i32 s49, s46, 1040\n\t"
    "s_add_u32 s49, s49, 2064\n\t"
    "s_lshl_b32 s46, s46, 3\n\t"
    "s_lshr_b32 s47, 1076763402, s46\n\t"
    "s_and_b32 s47, s47, 0xff\n\t"
    "s_lshl_b32 s48, s47, 16\n\t"
    "s_lshr_b32 s47, 773523968, s46\n\t"
    "s_and_b32 s47, s47, 0xff\n\t"
    "s_add_u32 s48, s48, s47\n\t"
    "s_lshr_b32 s47, %16, 2\n\t"
    "s_add_u32 s47, s47, 0x200\n\t"
    "s_cmp_eq_u32 s46, 0\n\t"
    "s_cselect_b32 s47, 0x100, s47\n\t"
    "s_add_u32 s48, s48, s47\n\t"
    "v_bfe_u32 v154, v147, 2, 2\n\t"
    "v_and_b32 v155, 3, v147\n\t"
    "v_mul_u32_u24 v154, 25, v154\n\t"
    "v_mad_u32_u24 v154, v155, 6, v154\n\t"
    "v_lshlrev_b32 v153, 2, v154\n\t"
    "s_and_b32 s53, s48, 0xff\n\t"
    "s_bfe_u32 s55, s48, 0x80008\n\t"
    "s_add_u32 s54, s53, s55\n\t"
    "s_add_u32 s55, s54, s55\n\t"
    "s_lshr_b32 s64, s48, 16\n\t"
    "s_mul_i32 s66, s53, 400\n\t"
    "buffer_load_dwordx3 v[128:130], v153, %0, s66 offen\n\t"
    "buffer_load_dwordx4 v[132:135], v153, %0, s66 offen offset:12\n\t"
    "s_mul_i32 s66, s54, 400\n\t"
    "buffer_load_dwordx3 v[136:138], v153, %0, s66 offen\n\t"
    "buffer_load_dwordx4 v[140:143], v153, %0, s66 offen offset:12\n\t"
    "v_cmp_eq_u32 s[62:63], 0, v155\n\t"
    "v_and_b32 v151, 48, v147\n\t"
    "v_or_b32 v155, 0x100000, v151\n\t"
    "v_cndmask_b32 v152, v155, v151, s[62:63]\n\t"
    "v_lshlrev_b32 v117, 20, v147\n\t"
    "v_add_u32 v117, 1024, v117\n\t"
    "v_mov_b32 v118, 1\n\t"
    "v_lshlrev_b32 v95, 4, v147\n\t"
    "v_add_u32 v95, 140288, v95\n\t"
    "v_mbcnt_lo_u32_b32 v100, -1, 0\n\t"
    "v_mbcnt_hi_u32_b32 v100, -1, v100\n\t"
    "s_lshl_b32 s61, %16, 15\n\t"
    "s_add_u32 s61, s61, 16515072\n\t"
    "v_lshlrev_b32 v127, 4, v100\n\t"
    "s_lshr_b32 s37, %14, 4\n\t"
    "s_and_b32 s39, %14, 7\n\t"
    "v_and_b32 v101, 15, v100\n\t"
    "s_lshl_b32 s39, s39, 4\n\t"
    "s_add_u32 s39, s39, s37\n\t"
    "s_mul_i32 s65, %16, 125\n\t"
    "v_add_u32 v155, s65, v101\n\t"
    "s_mul_i32 s63, s39, 102400\n\t"
    "s_add_u32 s63, s63, 3407872\n\t"
    "v_lshrrev_b32 v102, 2, v101\n\t"
    "s_and_b32 s37, s37, 7\n\t"
    "s_lshl_b32 s37, s37, 11\n\t"
    "v_lshrrev_b32 v103, 3, v100\n\t"
    "s_lshl_b32 s36, s39, 16\n\t"
    "s_bfe_u32 s65, %14, 0x10003\n\t"
    "v_and_b32 v103, 2, v103\n\t"
    "s_lshl_b32 s38, s65, 15\n\t"
    "s_add_u32 s36, s36, s38\n\t"
    "v_and_b32 v104, 1, v100\n\t"
    "s_lshl_b32 s38, %16, 12\n\t"
    "s_add_u32 s36, s36, s38\n\t"
    "v_lshlrev_b32 v104, 4, v104\n\t"
    "s_add_u32 s36, s36, 17825792\n\t"
    "v_mov_b32 v101, 0x3f80\n\t"
    "v_lshlrev_b32 v104, v104, v101\n\t"
    "v_cmp_eq_u32 vcc, v102, v103\n\t"
    "s_waitcnt lgkmcnt(0)\n\t"
    "s_lshr_b32 s39, s91, %9\n\t"
    "s_lshl_b32 s39, s39, 16\n\t"
    "v_cndmask_b32 v120, 0, v104, vcc\n\t"
    "v_cndmask_b32 v121, 0, v104, vcc\n\t"
    "v_add_u32 v103, 1, v103\n\t"
    "v_cmp_eq_u32 vcc, v102, v103\n\t"
    "s_lshl_b32 s40, s88, 16\n\t"
    "s_and_b32 s41, s88, 0xffff0000\n\t"
    "v_cndmask_b32 v122, 0, v104, vcc\n\t"
    "v_cndmask_b32 v123, 0, v104, vcc\n\t"
    "v_and_b32 v101, 2, v100\n\t"
    "v_mov_b32 v102, 0x44444444\n\t"
    "v_mov_b32 v103, 0xeeeeeeee\n\t"
    "v_cmp_eq_u32 vcc, 0, v101\n\t"
    "s_lshl_b32 s42, s89, 16\n\t"
    "s_and_b32 s43, s89, 0xffff0000\n\t"
    "v_cndmask_b32 v124, v103, v102, vcc\n\t"
    "v_lshrrev_b32 v101, 4, v100\n\t"
    "v_mul_u32_u24 v101, 8000, v101\n\t"
    "v_lshl_add_u32 v125, v155, 3, v101\n\t"
    "v_add_u32 v125, 64000, v125\n\t"
    "s_lshl_b32 s70, s72, 16\n\t"
    "s_and_b32 s71, s72, 0xffff0000\n\t"
    "s_lshl_b32 s72, s73, 16\n\t"
    "s_and_b32 s73, s73, 0xffff0000\n\t"
    "s_mov_b32 s67, 0xffff0000\n\t"
    "s_and_b32 s65, %14, 8\n\t"
    "s_add_u32 s38, s65, %16\n\t"
    "s_mul_i32 s38, s38, 212992\n\t"
    "s_add_u32 s38, s38, 196608\n\t"
    "s_add_u32 s84, s37, s38\n\t"
    "s_add_u32 s37, s37, 0x800\n\t"
    "s_and_b32 s37, s37, 0x3800\n\t"
    "s_add_u32 s85, s37, s38\n\t"
    "s_add_u32 s37, s37, 0x800\n\t"
    "s_and_b32 s37, s37, 0x3800\n\t"
    "s_add_u32 s86, s37, s38\n\t"
    "s_add_u32 s37, s37, 0x800\n\t"
    "s_and_b32 s37, s37, 0x3800\n\t"
    "s_add_u32 s87, s37, s38\n\t"
    "s_add_u32 s37, s37, 0x800\n\t"
    "s_and_b32 s37, s37, 0x3800\n\t"
    "s_add_u32 s88, s37, s38\n\t"
    "s_add_u32 s37, s37, 0x800\n\t"
    "s_and_b32 s37, s37, 0x3800\n\t"
    "s_add_u32 s89, s37, s38\n\t"
    "s_add_u32 s37, s37, 0x800\n\t"
    "s_and_b32 s37, s37, 0x3800\n\t"
    "s_add_u32 s90, s37, s38\n\t"
    "s_add_u32 s37, s37, 0x800\n\t"
    "s_and_b32 s37, s37, 0x3800\n\t"
    "s_add_u32 s91, s37, s38\n\t"
    "s_mul_i32 s94, s65, 425984\n\t"
    "s_sub_u32 s94, s94, 1703936\n\t"
    "s_sub_u32 s95, 0x8000, s94\n\t"
    "s_lshl_b32 s97, s65, 11\n\t"
    "s_sub_u32 s97, s97, 229376\n\t"
    "s_mov_b32 s92, 64000\n\t"
    "s_mov_b32 s93, -64000\n\t"
    "s_mov_b32 s96, 0x2000\n\t"
    "s_mov_b32 s62, 2\n\t"
    "v_rcp_f32 v101, s40\n\t"
    "v_rcp_f32 v102, s41\n\t"
    "v_rcp_f32 v103, s42\n\t"
    "v_rcp_f32 v104, s43\n\t"
    "v_mov_b32 v100, 0x7f7fffff\n\t"
    "v_med3_f32 v101, v101, -v100, v100\n\t"
    "v_med3_f32 v102, v102, -v100, v100\n\t"
    "v_med3_f32 v103, v103, -v100, v100\n\t"
    "v_med3_f32 v104, v104, -v100, v100\n\t"
    "v_readfirstlane_b32 s40, v101\n\t"
    "v_readfirstlane_b32 s41, v102\n\t"
    "v_readfirstlane_b32 s42, v103\n\t"
    "v_readfirstlane_b32 s43, v104\n\t"
    "L_await_%=:\n\t"
    "L_asp9_%=:\n\t"
    "ds_read_b32 v118, v117 offset:8\n\t"
    "ds_read_b32 v119, v117 offset:4\n\t"
    "s_waitcnt lgkmcnt(0)\n\t"
    "v_readfirstlane_b32 s46, v119\n\t"
    "s_nop 0\n\t"
    "s_cmp_ge_u32 s46, 4\n\t"
    "s_cbranch_scc1 L_ago9_%=\n\t"
    "s_sleep 1\n\t"
    "s_branch L_asp9_%=\n\t"
    "L_ago9_%=:\n\t"
    "v_readfirstlane_b32 s47, v118\n\t"
    "ds_read_b128 v[0:3], v95 offset:0\n\t"
    "ds_read_b128 v[4:7], v95 offset:1024\n\t"
    "ds_read_b128 v[8:11], v95 offset:2048\n\t"
    "ds_read_b128 v[12:15], v95 offset:3072\n\t"
    "ds_read_b128 v[16:19], v95 offset:4096\n\t"
    "ds_read_b128 v[20:23], v95 offset:5120\n\t"
    "ds_read_b128 v[24:27], v95 offset:6144\n\t"
    "ds_read_b128 v[28:31], v95 offset:7168\n\t"
    "ds_read_b128 v[32:35], v95 offset:8192\n\t"
    "ds_read_b128 v[36:39], v95 offset:9216\n\t"
    "ds_read_b128 v[40:43], v95 offset:10240\n\t"
    "ds_read_b128 v[44:47], v95 offset:11264\n\t"
    "ds_read_b128 v[48:51], v95 offset:12288\n\t"
    "ds_read_b128 v[52:55], v95 offset:13312\n\t"
    "ds_read_b128 v[56:59], v95 offset:14336\n\t"
    "ds_read_b128 v[60:63], v95 offset:15360\n\t"
    "ds_read_b128 v[64:67], v95 offset:16384\n\t"
    "ds_read_b128 v[68:71], v95 offset:17408\n\t"
    "ds_read_b128 v[72:75], v95 offset:18432\n\t"
    "ds_read_b128 v[76:79], v95 offset:19456\n\t"
    "ds_read_b128 v[80:83], v95 offset:20480\n\t"
    "ds_read_b128 v[84:87], v95 offset:21504\n\t"
    "ds_read_b128 v[88:91], v95 offset:22528\n\t"
    "s_waitcnt vmcnt(0)\n\t"
    "s_cmp_lg_u32 %16, 1\n\t"
    "s_cbranch_scc1 L_anh3_%=\n\t"
    "v_mov_b32 v119, 4\n\t"
    "ds_write_b32 v117, v119 offset:12\n\t"
    "s_lshl_b32 s65, %14, 12\n\t"
    "s_add_u32 s65, s65, 16777216\n\t"
    "s_waitcnt lgkmcnt(15)\n\t"
    "buffer_store_dwordx4 v[0:3], v127, %12, s65 offen offset:0 sc1\n\t"
    "buffer_store_dwordx4 v[4:7], v127, %12, s65 offen offset:1024 sc1\n\t"
    "buffer_store_dwordx4 v[8:11], v127, %12, s65 offen offset:2048 sc1\n\t"
    "buffer_store_dwordx4 v[12:15], v127, %12, s65 offen offset:3072 sc1\n\t"
    "L_anh3_%=:\n\t"
    "s_cmp_ge_u32 s47, 6\n\t"
    "s_cbranch_scc1 L_adok_%=\n\t"
    "L_awd_%=:\n\t"
    "L_asp10_%=:\n\t"
    "ds_read_b32 v119, v117 offset:8\n\t"
    "s_waitcnt lgkmcnt(0)\n\t"
    "v_readfirstlane_b32 s46, v119\n\t"
    "s_nop 0\n\t"
    "s_cmp_ge_u32 s46, 6\n\t"
    "s_cbranch_scc1 L_ago10_%=\n\t"
    "s_sleep 1\n\t"
    "s_branch L_asp10_%=\n\t"
    "L_ago10_%=:\n\t"
    "L_adok_%=:\n\t"
    "L_amain_%=:\n\t"
    "s_mov_b32 s68, 130\n\t"
    "v_mad_u32_u24 v128, v128, s68, v152\n\t"
    "v_and_b32 v128, -16, v128\n\t"
    "ds_read_b128 v[156:159], v128 offset:0\n\t"
    "ds_read_b128 v[160:163], v128 offset:64\n\t"
    "v_mad_u32_u24 v129, v129, s68, v151\n\t"
    "v_and_b32 v129, -16, v129\n\t"
    "ds_read_b128 v[164:167], v129 offset:0\n\t"
    "ds_read_b128 v[168:171], v129 offset:64\n\t"
    "v_mad_u32_u24 v130, v130, s68, v151\n\t"
    "v_and_b32 v130, -16, v130\n\t"
    "ds_read_b128 v[172:175], v130 offset:0\n\t"
    "ds_read_b128 v[176:179], v130 offset:64\n\t"
    "s_mul_i32 s66, s55, 400\n\t"
    "buffer_load_dwordx3 v[128:130], v153, %0, s66 offen\n\t"
    "v_mad_u32_u24 v132, v132, s68, v151\n\t"
    "v_and_b32 v132, -16, v132\n\t"
    "ds_read_b128 v[180:183], v132 offset:0\n\t"
    "ds_read_b128 v[184:187], v132 offset:64\n\t"
    "v_mad_u32_u24 v133, v133, s68, v151\n\t"
    "v_and_b32 v133, -16, v133\n\t"
    "ds_read_b128 v[188:191], v133 offset:0\n\t"
    "ds_read_b128 v[192:195], v133 offset:64\n\t"
    "v_mad_u32_u24 v134, v134, s68, v151\n\t"
    "v_and_b32 v134, -16, v134\n\t"
    "ds_read_b128 v[196:199], v134 offset:0\n\t"
    "ds_read_b128 v[200:203], v134 offset:64\n\t"
    "v_mad_u32_u24 v135, v135, s68, v151\n\t"
    "v_and_b32 v135, -16, v135\n\t"
    "ds_read_b128 v[204:207], v135 offset:0\n\t"
    "ds_read_b128 v[208:211], v135 offset:64\n\t"
    "v_mov_b32 v150, 1\n\t"
    "v_lshlrev_b32 v149, 20, v147\n\t"
    "v_add_u32 v149, s49, v149\n\t"
    "v_lshlrev_b32 v146, 2, v147\n\t"
    "s_mov_b32 s52, s53\n\t"
    "s_lshl_b32 s50, %14, 14\n\t"
    "s_add_u32 s50, s50, 26214400\n\t"
    "L_apnn_%=:\n\t"
    "s_mul_i32 s69, s52, 256\n\t"
    "s_add_u32 s69, s69, s50\n\t"
    "s_waitcnt lgkmcnt(8)\n\t"
    "v_mfma_f32_16x16x32_bf16 v[92:95], v[156:159], v[156:159], 0\n\t"
    "v_fmac_f32 v145, v108, v72\n\t"
    "v_fmac_f32 v144, v109, v73\n\t"
    "ds_add_rtn_u32 v148, v149, v150\n\t"
    "v_mfma_f32_16x16x32_bf16 v[92:95], v[160:163], v[160:163], v[92:95]\n\t"
    "v_fmac_f32 v145, v110, v74\n\t"
    "v_fmac_f32 v144, v111, v75\n\t"
    "buffer_load_dwordx4 v[132:135], v153, %0, s66 offen offset:12\n\t"
    "v_mfma_f32_16x16x32_bf16 v[96:99], v[164:167], v[164:167], 0\n\t"
    "v_fmac_f32 v145, v112, v76\n\t"
    "v_fmac_f32 v144, v113, v77\n\t"
    "v_mfma_f32_16x16x32_bf16 v[96:99], v[168:171], v[168:171], v[96:99]\n\t"
    "v_fmac_f32 v145, v114, v78\n\t"
    "v_fmac_f32 v144, v115, v79\n\t"
    "v_mfma_f32_16x16x32_bf16 v[100:103], v[164:167], v[172:175], 0\n\t"
    "v_fmac_f32 v145, v116, v80\n\t"
    "v_fmac_f32 v144, v117, v81\n\t"
    "v_mfma_f32_16x16x32_bf16 v[100:103], v[168:171], v[176:179], v[100:103]\n\t"
    "v_fmac_f32 v145, v118, v82\n\t"
    "v_fmac_f32 v144, v119, v83\n\t"
    "s_waitcnt vmcnt(4)\n\t"
    "s_cmp_ge_u32 s55, s64\n\t"
    "s_cbranch_scc1 L_ahk00_%=\n\t"
    "L_ahb00_%=:\n\t"
    "v_mfma_f32_16x16x32_bf16 v[104:107], v[172:175], v[172:175], 0\n\t"
    "v_mad_u32_u24 v136, v136, s68, v152\n\t"
    "v_and_b32 v136, -16, v136\n\t"
    "v_mfma_f32_16x16x32_bf16 v[104:107], v[176:179], v[176:179], v[104:107]\n\t"
    "v_add_f32 v154, v144, v145\n\t"
    "buffer_store_dword v154, v146, %12, s69 offen sc1\n\t"
    "v_mul_f32 v144, v92, v84\n\t"
    "v_mfma_f32_16x16x32_bf16 v[108:111], v[156:159], v[164:167], 0\n\t"
    "v_mul_f32 v145, v96, v0\n\t"
    "v_fmac_f32 v144, v97, v1\n\t"
    "v_mfma_f32_16x16x32_bf16 v[108:111], v[160:163], v[168:171], v[108:111]\n\t"
    "v_fmac_f32 v145, v98, v2\n\t"
    "v_fmac_f32 v144, v99, v3\n\t"
    "s_waitcnt lgkmcnt(7)\n\t"
    "v_mfma_f32_16x16x32_bf16 v[112:115], v[164:167], v[180:183], 0\n\t"
    "v_fmac_f32 v145, v100, v4\n\t"
    "v_fmac_f32 v144, v101, v5\n\t"
    "v_mfma_f32_16x16x32_bf16 v[112:115], v[168:171], v[184:187], v[112:115]\n\t"
    "v_fmac_f32 v145, v102, v6\n\t"
    "v_fmac_f32 v144, v103, v7\n\t"
    "v_mfma_f32_16x16x32_bf16 v[116:119], v[172:175], v[180:183], 0\n\t"
    "v_fmac_f32 v145, v104, v8\n\t"
    "v_fmac_f32 v144, v105, v9\n\t"
    "v_mfma_f32_16x16x32_bf16 v[116:119], v[176:179], v[184:187], v[116:119]\n\t"
    "v_fmac_f32 v145, v106, v10\n\t"
    "v_fmac_f32 v144, v107, v11\n\t"
    "ds_read_b128 v[212:215], v136 offset:0\n\t"
    "ds_read_b128 v[216:219], v136 offset:64\n\t"
    "v_mfma_f32_16x16x32_bf16 v[92:95], v[180:183], v[180:183], 0\n\t"
    "v_mad_u32_u24 v137, v137, s68, v151\n\t"
    "v_and_b32 v137, -16, v137\n\t"
    "v_mfma_f32_16x16x32_bf16 v[92:95], v[184:187], v[184:187], v[92:95]\n\t"
    "v_fmac_f32 v145, v108, v85\n\t"
    "v_mad_u32_u24 v138, v138, s68, v151\n\t"
    "v_mfma_f32_16x16x32_bf16 v[96:99], v[156:159], v[172:175], 0\n\t"
    "v_fmac_f32 v144, v112, v12\n\t"
    "v_fmac_f32 v145, v113, v13\n\t"
    "v_mfma_f32_16x16x32_bf16 v[96:99], v[160:163], v[176:179], v[96:99]\n\t"
    "v_fmac_f32 v144, v114, v14\n\t"
    "v_fmac_f32 v145, v115, v15\n\t"
    "s_waitcnt lgkmcnt(7)\n\t"
    "v_mfma_f32_16x16x32_bf16 v[100:103], v[164:167], v[188:191], 0\n\t"
    "v_and_b32 v138, -16, v138\n\t"
    "v_fmac_f32 v144, v116, v16\n\t"
    "v_mfma_f32_16x16x32_bf16 v[100:103], v[168:171], v[192:195], v[100:103]\n\t"
    "v_fmac_f32 v145, v117, v17\n\t"
    "v_fmac_f32 v144, v118, v18\n\t"
    "v_mfma_f32_16x16x32_bf16 v[104:107], v[172:175], v[188:191], 0\n\t"
    "v_fmac_f32 v145, v119, v19\n\t"
    "v_fmac_f32 v144, v92, v20\n\t"
    "v_mfma_f32_16x16x32_bf16 v[104:107], v[176:179], v[192:195], v[104:107]\n\t"
    "v_fmac_f32 v145, v93, v21\n\t"
    "v_fmac_f32 v144, v94, v22\n\t"
    "s_waitcnt lgkmcnt(5)\n\t"
    "v_mfma_f32_16x16x32_bf16 v[108:111], v[164:167], v[196:199], 0\n\t"
    "v_fmac_f32 v145, v95, v23\n\t"
    "v_fmac_f32 v144, v96, v86\n\t"
    "v_mfma_f32_16x16x32_bf16 v[108:111], v[168:171], v[200:203], v[108:111]\n\t"
    "v_mfma_f32_16x16x32_bf16 v[112:115], v[156:159], v[180:183], 0\n\t"
    "v_fmac_f32 v145, v100, v24\n\t"
    "v_fmac_f32 v144, v101, v25\n\t"
    "v_mfma_f32_16x16x32_bf16 v[112:115], v[160:163], v[184:187], v[112:115]\n\t"
    "v_fmac_f32 v145, v102, v26\n\t"
    "v_fmac_f32 v144, v103, v27\n\t"
    "s_waitcnt lgkmcnt(2)\n\t"
    "v_mfma_f32_16x16x32_bf16 v[116:119], v[164:167], v[204:207], 0\n\t"
    "v_readfirstlane_b32 s60, v148\n\t"
    "v_fmac_f32 v145, v104, v28\n\t"
    "v_mfma_f32_16x16x32_bf16 v[116:119], v[168:171], v[208:211], v[116:119]\n\t"
    "v_fmac_f32 v144, v105, v29\n\t"
    "v_fmac_f32 v145, v106, v30\n\t"
    "ds_read_b128 v[164:167], v137 offset:0\n\t"
    "ds_read_b128 v[168:171], v137 offset:64\n\t"
    "v_mfma_f32_16x16x32_bf16 v[92:95], v[172:175], v[196:199], 0\n\t"
    "v_fmac_f32 v144, v107, v31\n\t"
    "v_fmac_f32 v145, v108, v32\n\t"
    "v_mfma_f32_16x16x32_bf16 v[92:95], v[176:179], v[200:203], v[92:95]\n\t"
    "v_fmac_f32 v144, v109, v33\n\t"
    "v_fmac_f32 v145, v110, v34\n\t"
    "s_waitcnt vmcnt(4)\n\t"
    "s_cmp_ge_u32 s55, s64\n\t"
    "s_cbranch_scc1 L_ahk10_%=\n\t"
    "L_ahb10_%=:\n\t"
    "v_mfma_f32_16x16x32_bf16 v[96:99], v[172:175], v[204:207], 0\n\t"
    "v_mad_u32_u24 v140, v140, s68, v151\n\t"
    "v_and_b32 v140, -16, v140\n\t"
    "v_mfma_f32_16x16x32_bf16 v[96:99], v[176:179], v[208:211], v[96:99]\n\t"
    "v_fmac_f32 v144, v111, v35\n\t"
    "v_fmac_f32 v145, v112, v87\n\t"
    "ds_read_b128 v[172:175], v138 offset:0\n\t"
    "ds_read_b128 v[176:179], v138 offset:64\n\t"
    "s_mul_i32 s66, s60, 400\n\t"
    "v_mfma_f32_16x16x32_bf16 v[100:103], v[156:159], v[188:191], 0\n\t"
    "v_fmac_f32 v144, v116, v36\n\t"
    "v_fmac_f32 v145, v117, v37\n\t"
    "v_mfma_f32_16x16x32_bf16 v[100:103], v[160:163], v[192:195], v[100:103]\n\t"
    "v_fmac_f32 v144, v118, v38\n\t"
    "v_fmac_f32 v145, v119, v39\n\t"
    "buffer_load_dwordx3 v[136:138], v153, %0, s66 offen\n\t"
    "v_mfma_f32_16x16x32_bf16 v[104:107], v[180:183], v[188:191], 0\n\t"
    "v_mad_u32_u24 v141, v141, s68, v151\n\t"
    "v_and_b32 v141, -16, v141\n\t"
    "v_mfma_f32_16x16x32_bf16 v[104:107], v[184:187], v[192:195], v[104:107]\n\t"
    "v_fmac_f32 v144, v92, v40\n\t"
    "v_fmac_f32 v145, v93, v41\n\t"
    "v_mfma_f32_16x16x32_bf16 v[108:111], v[180:183], v[196:199], 0\n\t"
    "v_fmac_f32 v144, v94, v42\n\t"
    "v_fmac_f32 v145, v95, v43\n\t"
    "v_mfma_f32_16x16x32_bf16 v[108:111], v[184:187], v[200:203], v[108:111]\n\t"
    "v_fmac_f32 v144, v96, v44\n\t"
    "v_fmac_f32 v145, v97, v45\n\t"
    "v_mfma_f32_16x16x32_bf16 v[112:115], v[180:183], v[204:207], 0\n\t"
    "v_fmac_f32 v144, v98, v46\n\t"
    "v_fmac_f32 v145, v99, v47\n\t"
    "v_mfma_f32_16x16x32_bf16 v[112:115], v[184:187], v[208:211], v[112:115]\n\t"
    "v_fmac_f32 v144, v100, v88\n\t"
    "v_mad_u32_u24 v142, v142, s68, v151\n\t"
    "ds_read_b128 v[180:183], v140 offset:0\n\t"
    "ds_read_b128 v[184:187], v140 offset:64\n\t"
    "v_mfma_f32_16x16x32_bf16 v[116:119], v[156:159], v[196:199], 0\n\t"
    "v_and_b32 v142, -16, v142\n\t"
    "v_fmac_f32 v145, v104, v48\n\t"
    "v_mfma_f32_16x16x32_bf16 v[116:119], v[160:163], v[200:203], v[116:119]\n\t"
    "v_fmac_f32 v144, v105, v49\n\t"
    "v_fmac_f32 v145, v106, v50\n\t"
    "v_mfma_f32_16x16x32_bf16 v[92:95], v[188:191], v[188:191], 0\n\t"
    "v_fmac_f32 v144, v107, v51\n\t"
    "v_mad_u32_u24 v143, v143, s68, v151\n\t"
    "v_mfma_f32_16x16x32_bf16 v[92:95], v[192:195], v[192:195], v[92:95]\n\t"
    "v_and_b32 v143, -16, v143\n\t"
    "v_fmac_f32 v145, v108, v52\n\t"
    "v_mfma_f32_16x16x32_bf16 v[96:99], v[188:191], v[196:199], 0\n\t"
    "v_fmac_f32 v144, v109, v53\n\t"
    "v_fmac_f32 v145, v110, v54\n\t"
    "v_mfma_f32_16x16x32_bf16 v[96:99], v[192:195], v[200:203], v[96:99]\n\t"
    "v_fmac_f32 v144, v111, v55\n\t"
    "v_fmac_f32 v145, v112, v56\n\t"
    "v_mfma_f32_16x16x32_bf16 v[100:103], v[188:191], v[204:207], 0\n\t"
    "v_fmac_f32 v144, v113, v57\n\t"
    "v_fmac_f32 v145, v114, v58\n\t"
    "v_mfma_f32_16x16x32_bf16 v[100:103], v[192:195], v[208:211], v[100:103]\n\t"
    "v_fmac_f32 v144, v115, v59\n\t"
    "v_fmac_f32 v145, v116, v89\n\t"
    "ds_read_b128 v[188:191], v141 offset:0\n\t"
    "ds_read_b128 v[192:195], v141 offset:64\n\t"
    "v_mfma_f32_16x16x32_bf16 v[104:107], v[156:159], v[204:207], 0\n\t"
    "v_fmac_f32 v144, v92, v60\n\t"
    "v_fmac_f32 v145, v93, v61\n\t"
    "v_mfma_f32_16x16x32_bf16 v[104:107], v[160:163], v[208:211], v[104:107]\n\t"
    "v_fmac_f32 v144, v94, v62\n\t"
    "v_fmac_f32 v145, v95, v63\n\t"
    "v_mfma_f32_16x16x32_bf16 v[108:111], v[196:199], v[196:199], 0\n\t"
    "v_fmac_f32 v144, v96, v64\n\t"
    "v_fmac_f32 v145, v97, v65\n\t"
    "v_mfma_f32_16x16x32_bf16 v[108:111], v[200:203], v[200:203], v[108:111]\n\t"
    "v_fmac_f32 v144, v98, v66\n\t"
    "v_fmac_f32 v145, v99, v67\n\t"
    "v_mfma_f32_16x16x32_bf16 v[112:115], v[196:199], v[204:207], 0\n\t"
    "v_fmac_f32 v144, v100, v68\n\t"
    "v_fmac_f32 v145, v101, v69\n\t"
    "v_mfma_f32_16x16x32_bf16 v[112:115], v[200:203], v[208:211], v[112:115]\n\t"
    "v_fmac_f32 v144, v102, v70\n\t"
    "v_fmac_f32 v145, v103, v71\n\t"
    "ds_read_b128 v[196:199], v142 offset:0\n\t"
    "ds_read_b128 v[200:203], v142 offset:64\n\t"
    "v_mfma_f32_16x16x32_bf16 v[116:119], v[204:207], v[204:207], 0\n\t"
    "v_fmac_f32 v144, v104, v90\n\t"
    "v_mfma_f32_16x16x32_bf16 v[116:119], v[208:211], v[208:211], v[116:119]\n\t"
    "ds_read_b128 v[204:207], v143 offset:0\n\t"
    "ds_read_b128 v[208:211], v143 offset:64\n\t"
    "s_mov_b32 s52, s53\n\t"
    "s_mov_b32 s53, s54\n\t"
    "s_mov_b32 s54, s55\n\t"
    "s_mov_b32 s55, s60\n\t"
    "s_cmp_lt_u32 s53, s64\n\t"
    "s_cbranch_scc0 L_adone_%=\n\t"
    "s_mul_i32 s69, s52, 256\n\t"
    "s_add_u32 s69, s69, s50\n\t"
    "s_waitcnt lgkmcnt(8)\n\t"
    "v_mfma_f32_16x16x32_bf16 v[92:95], v[212:215], v[212:215], 0\n\t"
    "v_fmac_f32 v145, v108, v72\n\t"
    "v_fmac_f32 v144, v109, v73\n\t"
    "ds_add_rtn_u32 v148, v149, v150\n\t"
    "v_mfma_f32_16x16x32_bf16 v[92:95], v[216:219], v[216:219], v[92:95]\n\t"
    "v_fmac_f32 v145, v110, v74\n\t"
    "v_fmac_f32 v144, v111, v75\n\t"
    "buffer_load_dwordx4 v[140:143], v153, %0, s66 offen offset:12\n\t"
    "v_mfma_f32_16x16x32_bf16 v[96:99], v[164:167], v[164:167], 0\n\t"
    "v_fmac_f32 v145, v112, v76\n\t"
    "v_fmac_f32 v144, v113, v77\n\t"
    "v_mfma_f32_16x16x32_bf16 v[96:99], v[168:171], v[168:171], v[96:99]\n\t"
    "v_fmac_f32 v145, v114, v78\n\t"
    "v_fmac_f32 v144, v115, v79\n\t"
    "v_mfma_f32_16x16x32_bf16 v[100:103], v[164:167], v[172:175], 0\n\t"
    "v_fmac_f32 v145, v116, v80\n\t"
    "v_fmac_f32 v144, v117, v81\n\t"
    "v_mfma_f32_16x16x32_bf16 v[100:103], v[168:171], v[176:179], v[100:103]\n\t"
    "v_fmac_f32 v145, v118, v82\n\t"
    "v_fmac_f32 v144, v119, v83\n\t"
    "s_waitcnt vmcnt(4)\n\t"
    "s_cmp_ge_u32 s55, s64\n\t"
    "s_cbranch_scc1 L_ahk01_%=\n\t"
    "L_ahb01_%=:\n\t"
    "v_mfma_f32_16x16x32_bf16 v[104:107], v[172:175], v[172:175], 0\n\t"
    "v_mad_u32_u24 v128, v128, s68, v152\n\t"
    "v_and_b32 v128, -16, v128\n\t"
    "v_mfma_f32_16x16x32_bf16 v[104:107], v[176:179], v[176:179], v[104:107]\n\t"
    "v_add_f32 v154, v144, v145\n\t"
    "buffer_store_dword v154, v146, %12, s69 offen sc1\n\t"
    "v_mul_f32 v144, v92, v84\n\t"
    "v_mfma_f32_16x16x32_bf16 v[108:111], v[212:215], v[164:167], 0\n\t"
    "v_mul_f32 v145, v96, v0\n\t"
    "v_fmac_f32 v144, v97, v1\n\t"
    "v_mfma_f32_16x16x32_bf16 v[108:111], v[216:219], v[168:171], v[108:111]\n\t"
    "v_fmac_f32 v145, v98, v2\n\t"
    "v_fmac_f32 v144, v99, v3\n\t"
    "s_waitcnt lgkmcnt(7)\n\t"
    "v_mfma_f32_16x16x32_bf16 v[112:115], v[164:167], v[180:183], 0\n\t"
    "v_fmac_f32 v145, v100, v4\n\t"
    "v_fmac_f32 v144, v101, v5\n\t"
    "v_mfma_f32_16x16x32_bf16 v[112:115], v[168:171], v[184:187], v[112:115]\n\t"
    "v_fmac_f32 v145, v102, v6\n\t"
    "v_fmac_f32 v144, v103, v7\n\t"
    "v_mfma_f32_16x16x32_bf16 v[116:119], v[172:175], v[180:183], 0\n\t"
    "v_fmac_f32 v145, v104, v8\n\t"
    "v_fmac_f32 v144, v105, v9\n\t"
    "v_mfma_f32_16x16x32_bf16 v[116:119], v[176:179], v[184:187], v[116:119]\n\t"
    "v_fmac_f32 v145, v106, v10\n\t"
    "v_fmac_f32 v144, v107, v11\n\t"
    "ds_read_b128 v[156:159], v128 offset:0\n\t"
    "ds_read_b128 v[160:163], v128 offset:64\n\t"
    "v_mfma_f32_16x16x32_bf16 v[92:95], v[180:183], v[180:183], 0\n\t"
    "v_mad_u32_u24 v129, v129, s68, v151\n\t"
    "v_and_b32 v129, -16, v129\n\t"
    "v_mfma_f32_16x16x32_bf16 v[92:95], v[184:187], v[184:187], v[92:95]\n\t"
    "v_fmac_f32 v145, v108, v85\n\t"
    "v_mad_u32_u24 v130, v130, s68, v151\n\t"
    "v_mfma_f32_16x16x32_bf16 v[96:99], v[212:215], v[172:175], 0\n\t"
    "v_fmac_f32 v144, v112, v12\n\t"
    "v_fmac_f32 v145, v113, v13\n\t"
    "v_mfma_f32_16x16x32_bf16 v[96:99], v[216:219], v[176:179], v[96:99]\n\t"
    "v_fmac_f32 v144, v114, v14\n\t"
    "v_fmac_f32 v145, v115, v15\n\t"
    "s_waitcnt lgkmcnt(7)\n\t"
    "v_mfma_f32_16x16x32_bf16 v[100:103], v[164:167], v[188:191], 0\n\t"
    "v_and_b32 v130, -16, v130\n\t"
    "v_fmac_f32 v144, v116, v16\n\t"
    "v_mfma_f32_16x16x32_bf16 v[100:103], v[168:171], v[192:195], v[100:103]\n\t"
    "v_fmac_f32 v145, v117, v17\n\t"
    "v_fmac_f32 v144, v118, v18\n\t"
    "v_mfma_f32_16x16x32_bf16 v[104:107], v[172:175], v[188:191], 0\n\t"
    "v_fmac_f32 v145, v119, v19\n\t"
    "v_fmac_f32 v144, v92, v20\n\t"
    "v_mfma_f32_16x16x32_bf16 v[104:107], v[176:179], v[192:195], v[104:107]\n\t"
    "v_fmac_f32 v145, v93, v21\n\t"
    "v_fmac_f32 v144, v94, v22\n\t"
    "s_waitcnt lgkmcnt(5)\n\t"
    "v_mfma_f32_16x16x32_bf16 v[108:111], v[164:167], v[196:199], 0\n\t"
    "v_fmac_f32 v145, v95, v23\n\t"
    "v_fmac_f32 v144, v96, v86\n\t"
    "v_mfma_f32_16x16x32_bf16 v[108:111], v[168:171], v[200:203], v[108:111]\n\t"
    "v_mfma_f32_16x16x32_bf16 v[112:115], v[212:215], v[180:183], 0\n\t"
    "v_fmac_f32 v145, v100, v24\n\t"
    "v_fmac_f32 v144, v101, v25\n\t"
    "v_mfma_f32_16x16x32_bf16 v[112:115], v[216:219], v[184:187], v[112:115]\n\t"
    "v_fmac_f32 v145, v102, v26\n\t"
    "v_fmac_f32 v144, v103, v27\n\t"
    "s_waitcnt lgkmcnt(2)\n\t"
    "v_mfma_f32_16x16x32_bf16 v[116:119], v[164:167], v[204:207], 0\n\t"
    "v_readfirstlane_b32 s60, v148\n\t"
    "v_fmac_f32 v145, v104, v28\n\t"
    "v_mfma_f32_16x16x32_bf16 v[116:119], v[168:171], v[208:211], v[116:119]\n\t"
    "v_fmac_f32 v144, v105, v29\n\t"
    "v_fmac_f32 v145, v106, v30\n\t"
    "ds_read_b128 v[164:167], v129 offset:0\n\t"
    "ds_read_b128 v[168:171], v129 offset:64\n\t"
    "v_mfma_f32_16x16x32_bf16 v[92:95], v[172:175], v[196:199], 0\n\t"
    "v_fmac_f32 v144, v107, v31\n\t"
    "v_fmac_f32 v145, v108, v32\n\t"
    "v_mfma_f32_16x16x32_bf16 v[92:95], v[176:179], v[200:203], v[92:95]\n\t"
    "v_fmac_f32 v144, v109, v33\n\t"
    "v_fmac_f32 v145, v110, v34\n\t"
    "s_waitcnt vmcnt(4)\n\t"
    "s_cmp_ge_u32 s55, s64\n\t"
    "s_cbranch_scc1 L_ahk11_%=\n\t"
    "L_ahb11_%=:\n\t"
    "v_mfma_f32_16x16x32_bf16 v[96:99], v[172:175], v[204:207], 0\n\t"
    "v_mad_u32_u24 v132, v132, s68, v151\n\t"
    "v_and_b32 v132, -16, v132\n\t"
    "v_mfma_f32_16x16x32_bf16 v[96:99], v[176:179], v[208:211], v[96:99]\n\t"
    "v_fmac_f32 v144, v111, v35\n\t"
    "v_fmac_f32 v145, v112, v87\n\t"
    "ds_read_b128 v[172:175], v130 offset:0\n\t"
    "ds_read_b128 v[176:179], v130 offset:64\n\t"
    "s_mul_i32 s66, s60, 400\n\t"
    "v_mfma_f32_16x16x32_bf16 v[100:103], v[212:215], v[188:191], 0\n\t"
    "v_fmac_f32 v144, v116, v36\n\t"
    "v_fmac_f32 v145, v117, v37\n\t"
    "v_mfma_f32_16x16x32_bf16 v[100:103], v[216:219], v[192:195], v[100:103]\n\t"
    "v_fmac_f32 v144, v118, v38\n\t"
    "v_fmac_f32 v145, v119, v39\n\t"
    "buffer_load_dwordx3 v[128:130], v153, %0, s66 offen\n\t"
    "v_mfma_f32_16x16x32_bf16 v[104:107], v[180:183], v[188:191], 0\n\t"
    "v_mad_u32_u24 v133, v133, s68, v151\n\t"
    "v_and_b32 v133, -16, v133\n\t"
    "v_mfma_f32_16x16x32_bf16 v[104:107], v[184:187], v[192:195], v[104:107]\n\t"
    "v_fmac_f32 v144, v92, v40\n\t"
    "v_fmac_f32 v145, v93, v41\n\t"
    "v_mfma_f32_16x16x32_bf16 v[108:111], v[180:183], v[196:199], 0\n\t"
    "v_fmac_f32 v144, v94, v42\n\t"
    "v_fmac_f32 v145, v95, v43\n\t"
    "v_mfma_f32_16x16x32_bf16 v[108:111], v[184:187], v[200:203], v[108:111]\n\t"
    "v_fmac_f32 v144, v96, v44\n\t"
    "v_fmac_f32 v145, v97, v45\n\t"
    "v_mfma_f32_16x16x32_bf16 v[112:115], v[180:183], v[204:207], 0\n\t"
    "v_fmac_f32 v144, v98, v46\n\t"
    "v_fmac_f32 v145, v99, v47\n\t"
    "v_mfma_f32_16x16x32_bf16 v[112:115], v[184:187], v[208:211], v[112:115]\n\t"
    "v_fmac_f32 v144, v100, v88\n\t"
    "v_mad_u32_u24 v134, v134, s68, v151\n\t"
    "ds_read_b128 v[180:183], v132 offset:0\n\t"
    "ds_read_b128 v[184:187], v132 offset:64\n\t"
    "v_mfma_f32_16x16x32_bf16 v[116:119], v[212:215], v[196:199], 0\n\t"
    "v_and_b32 v134, -16, v134\n\t"
    "v_fmac_f32 v145, v104, v48\n\t"
    "v_mfma_f32_16x16x32_bf16 v[116:119], v[216:219], v[200:203], v[116:119]\n\t"
    "v_fmac_f32 v144, v105, v49\n\t"
    "v_fmac_f32 v145, v106, v50\n\t"
    "v_mfma_f32_16x16x32_bf16 v[92:95], v[188:191], v[188:191], 0\n\t"
    "v_fmac_f32 v144, v107, v51\n\t"
    "v_mad_u32_u24 v135, v135, s68, v151\n\t"
    "v_mfma_f32_16x16x32_bf16 v[92:95], v[192:195], v[192:195], v[92:95]\n\t"
    "v_and_b32 v135, -16, v135\n\t"
    "v_fmac_f32 v145, v108, v52\n\t"
    "v_mfma_f32_16x16x32_bf16 v[96:99], v[188:191], v[196:199], 0\n\t"
    "v_fmac_f32 v144, v109, v53\n\t"
    "v_fmac_f32 v145, v110, v54\n\t"
    "v_mfma_f32_16x16x32_bf16 v[96:99], v[192:195], v[200:203], v[96:99]\n\t"
    "v_fmac_f32 v144, v111, v55\n\t"
    "v_fmac_f32 v145, v112, v56\n\t"
    "v_mfma_f32_16x16x32_bf16 v[100:103], v[188:191], v[204:207], 0\n\t"
    "v_fmac_f32 v144, v113, v57\n\t"
    "v_fmac_f32 v145, v114, v58\n\t"
    "v_mfma_f32_16x16x32_bf16 v[100:103], v[192:195], v[208:211], v[100:103]\n\t"
    "v_fmac_f32 v144, v115, v59\n\t"
    "v_fmac_f32 v145, v116, v89\n\t"
    "ds_read_b128 v[188:191], v133 offset:0\n\t"
    "ds_read_b128 v[192:195], v133 offset:64\n\t"
    "v_mfma_f32_16x16x32_bf16 v[104:107], v[212:215], v[204:207], 0\n\t"
    "v_fmac_f32 v144, v92, v60\n\t"
    "v_fmac_f32 v145, v93, v61\n\t"
    "v_mfma_f32_16x16x32_bf16 v[104:107], v[216:219], v[208:211], v[104:107]\n\t"
    "v_fmac_f32 v144, v94, v62\n\t"
    "v_fmac_f32 v145, v95, v63\n\t"
    "v_mfma_f32_16x16x32_bf16 v[108:111], v[196:199], v[196:199], 0\n\t"
    "v_fmac_f32 v144, v96, v64\n\t"
    "v_fmac_f32 v145, v97, v65\n\t"
    "v_mfma_f32_16x16x32_bf16 v[108:111], v[200:203], v[200:203], v[108:111]\n\t"
    "v_fmac_f32 v144, v98, v66\n\t"
    "v_fmac_f32 v145, v99, v67\n\t"
    "v_mfma_f32_16x16x32_bf16 v[112:115], v[196:199], v[204:207], 0\n\t"
    "v_fmac_f32 v144, v100, v68\n\t"
    "v_fmac_f32 v145, v101, v69\n\t"
    "v_mfma_f32_16x16x32_bf16 v[112:115], v[200:203], v[208:211], v[112:115]\n\t"
    "v_fmac_f32 v144, v102, v70\n\t"
    "v_fmac_f32 v145, v103, v71\n\t"
    "ds_read_b128 v[196:199], v134 offset:0\n\t"
    "ds_read_b128 v[200:203], v134 offset:64\n\t"
    "v_mfma_f32_16x16x32_bf16 v[116:119], v[204:207], v[204:207], 0\n\t"
    "v_fmac_f32 v144, v104, v90\n\t"
    "v_mfma_f32_16x16x32_bf16 v[116:119], v[208:211], v[208:211], v[116:119]\n\t"
    "ds_read_b128 v[204:207], v135 offset:0\n\t"
    "ds_read_b128 v[208:211], v135 offset:64\n\t"
    "s_mov_b32 s52, s53\n\t"
    "s_mov_b32 s53, s54\n\t"
    "s_mov_b32 s54, s55\n\t"
    "s_mov_b32 s55, s60\n\t"
    "s_cmp_lt_u32 s53, s64\n\t"
    "s_cbranch_scc1 L_apnn_%=\n\t"
    "L_adone_%=:\n\t"
    "s_mul_i32 s69, s52, 256\n\t"
    "s_add_u32 s69, s69, s50\n\t"
    "v_fmac_f32 v145, v108, v72\n\t"
    "v_fmac_f32 v144, v109, v73\n\t"
    "v_fmac_f32 v145, v110, v74\n\t"
    "v_fmac_f32 v144, v111, v75\n\t"
    "v_fmac_f32 v145, v112, v76\n\t"
    "v_fmac_f32 v144, v113, v77\n\t"
    "v_fmac_f32 v145, v114, v78\n\t"
    "v_fmac_f32 v144, v115, v79\n\t"
    "v_fmac_f32 v145, v116, v80\n\t"
    "v_fmac_f32 v144, v117, v81\n\t"
    "v_fmac_f32 v145, v118, v82\n\t"
    "v_fmac_f32 v144, v119, v83\n\t"
    "v_add_f32 v40, v144, v145\n\t"
    "L_aend_%=:\n\t"
    "s_branch L_aend2_%=\n\t"
    "L_ahk00_%=:\n\t"
    "v_mov_b32 v147, 3112\n\t"
    "s_cmp_lt_u32 s54, s64\n\t"
    "ds_read_b32 v147, v147\n\t"
    "s_cbranch_scc1 L_ahb00_%=\n\t"
    "s_waitcnt vmcnt(3)\n\t"
    "v_add_u32 v137, 0, v155\n\t"
    "v_add_u32 v138, 16, v155\n\t"
    "v_add_u32 v140, 32, v155\n\t"
    "v_add_u32 v141, 48, v155\n\t"
    "v_add_u32 v142, 64, v155\n\t"
    "v_add_u32 v143, 80, v155\n\t"
    "s_branch L_ahb00_%=\n\t"
    "L_ahk10_%=:\n\t"
    "s_waitcnt lgkmcnt(4)\n\t"
    "v_readfirstlane_b32 s65, v147\n\t"
    "s_nop 0\n\t"
    "s_cmp_eq_u32 s65, 0\n\t"
    "s_cbranch_scc1 L_ahb10_%=\n\t"
    "s_cmp_lt_u32 s62, 2\n\t"
    "s_cbranch_scc1 L_mel1_%=\n\t"
    "buffer_load_dwordx4 v[220:223], v127, %12, s61 offen offset:2048 sc1\n\t"
    "buffer_load_dwordx4 v[224:227], v127, %12, s61 offen offset:3072 sc1\n\t"
    "s_add_u32 s61, s61, 0x1000\n\t"
    "buffer_load_dwordx4 v[228:231], v127, %12, s61 offen offset:2048 sc1\n\t"
    "buffer_load_dwordx4 v[232:235], v127, %12, s61 offen offset:3072 sc1\n\t"
    "s_add_u32 s61, s61, 0x1000\n\t"
    "buffer_load_dwordx4 v[236:239], v127, %12, s61 offen offset:2048 sc1\n\t"
    "buffer_load_dwordx4 v[240:243], v127, %12, s61 offen offset:3072 sc1\n\t"
    "s_add_u32 s61, s61, 0x1000\n\t"
    "buffer_load_dwordx4 v[244:247], v127, %12, s61 offen offset:2048 sc1\n\t"
    "buffer_load_dwordx4 v[248:251], v127, %12, s61 offen offset:3072 sc1\n\t"
    "s_add_u32 s61, s61, 0x1000\n\t"
    "buffer_load_dwordx4 v[252:255], v127, %12, s61 offen offset:2048 sc1\n\t"
    "s_mov_b32 s62, 1\n\t"
    "L_mel1_%=:\n\t"
    "s_cmp_lt_u32 s54, s64\n\t"
    "s_cbranch_scc1 L_ahb10_%=\n\t"
    "buffer_load_dwordx4 v[0:3], v127, %12, s63 offen offset:0 sc1\n\t"
    "buffer_load_dwordx4 v[4:7], v127, %12, s63 offen offset:1024 sc1\n\t"
    "buffer_load_dwordx4 v[8:11], v127, %12, s63 offen offset:2048 sc1\n\t"
    "buffer_load_dwordx4 v[12:15], v127, %12, s63 offen offset:3072 sc1\n\t"
    "s_add_u32 s63, s63, 0x1000\n\t"
    "s_mov_b32 s62, 0\n\t"
    "s_branch L_ahb10_%=\n\t"
    "L_ahk01_%=:\n\t"
    "v_mov_b32 v147, 3112\n\t"
    "s_cmp_lt_u32 s54, s64\n\t"
    "ds_read_b32 v147, v147\n\t"
    "s_cbranch_scc1 L_ahb01_%=\n\t"
    "s_waitcnt vmcnt(3)\n\t"
    "v_add_u32 v129, 0, v155\n\t"
    "v_add_u32 v130, 16, v155\n\t"
    "v_add_u32 v132, 32, v155\n\t"
    "v_add_u32 v133, 48, v155\n\t"
    "v_add_u32 v134, 64, v155\n\t"
    "v_add_u32 v135, 80, v155\n\t"
    "s_branch L_ahb01_%=\n\t"
    "L_ahk11_%=:\n\t"
    "s_waitcnt lgkmcnt(4)\n\t"
    "v_readfirstlane_b32 s65, v147\n\t"
    "s_nop 0\n\t"
    "s_cmp_eq_u32 s65, 0\n\t"
    "s_cbranch_scc1 L_ahb11_%=\n\t"
    "s_cmp_lt_u32 s62, 2\n\t"
    "s_cbranch_scc1 L_mel3_%=\n\t"
    "buffer_load_dwordx4 v[220:223], v127, %12, s61 offen offset:2048 sc1\n\t"
    "buffer_load_dwordx4 v[224:227], v127, %12, s61 offen offset:3072 sc1\n\t"
    "s_add_u32 s61, s61, 0x1000\n\t"
    "buffer_load_dwordx4 v[228:231], v127, %12, s61 offen offset:2048 sc1\n\t"
    "buffer_load_dwordx4 v[232:235], v127, %12, s61 offen offset:3072 sc1\n\t"
    "s_add_u32 s61, s61, 0x1000\n\t"
    "buffer_load_dwordx4 v[236:239], v127, %12, s61 offen offset:2048 sc1\n\t"
    "buffer_load_dwordx4 v[240:243], v127, %12, s61 offen offset:3072 sc1\n\t"
    "s_add_u32 s61, s61, 0x1000\n\t"
    "buffer_load_dwordx4 v[244:247], v127, %12, s61 offen offset:2048 sc1\n\t"
    "buffer_load_dwordx4 v[248:251], v127, %12, s61 offen offset:3072 sc1\n\t"
    "s_add_u32 s61, s61, 0x1000\n\t"
    "buffer_load_dwordx4 v[252:255], v127, %12, s61 offen offset:2048 sc1\n\t"
    "s_mov_b32 s62, 1\n\t"
    "L_mel3_%=:\n\t"
    "s_cmp_lt_u32 s54, s64\n\t"
    "s_cbranch_scc1 L_ahb11_%=\n\t"
    "buffer_load_dwordx4 v[0:3], v127, %12, s63 offen offset:0 sc1\n\t"
    "buffer_load_dwordx4 v[4:7], v127, %12, s63 offen offset:1024 sc1\n\t"
    "buffer_load_dwordx4 v[8:11], v127, %12, s63 offen offset:2048 sc1\n\t"
    "buffer_load_dwordx4 v[12:15], v127, %12, s63 offen offset:3072 sc1\n\t"
    "s_add_u32 s63, s63, 0x1000\n\t"
    "s_mov_b32 s62, 0\n\t"
    "s_branch L_ahb11_%=\n\t"
    "L_aend2_%=:\n\t"
    "s_setprio 0\n\t"
    "v_lshlrev_b32 v113, 16, v127\n\t"
    "v_add_u32 v113, 3108, v113\n\t"
    "s_cmp_eq_u32 s62, 0\n\t"
    "s_cbranch_scc1 L_mpf_%=\n\t"
    "L_ml0_%=:\n\t"
    "ds_read_b32 v114, v113 offset:4\n\t"
    "s_waitcnt lgkmcnt(0)\n\t"
    "v_readfirstlane_b32 s65, v114\n\t"
    "s_nop 0\n\t"
    "s_cmp_lg_u32 s65, 0\n\t"
    "s_cbranch_scc1 L_mm0_%=\n\t"
    "s_sleep 2\n\t"
    "s_branch L_ml0_%=\n\t"
    "L_mm0_%=:\n\t"
    "s_cmp_lt_u32 s62, 2\n\t"
    "s_cbranch_scc1 L_mel5_%=\n\t"
    "buffer_load_dwordx4 v[220:223], v127, %12, s61 offen offset:2048 sc1\n\t"
    "buffer_load_dwordx4 v[224:227], v127, %12, s61 offen offset:3072 sc1\n\t"
    "s_add_u32 s61, s61, 0x1000\n\t"
    "buffer_load_dwordx4 v[228:231], v127, %12, s61 offen offset:2048 sc1\n\t"
    "buffer_load_dwordx4 v[232:235], v127, %12, s61 offen offset:3072 sc1\n\t"
    "s_add_u32 s61, s61, 0x1000\n\t"
    "buffer_load_dwordx4 v[236:239], v127, %12, s61 offen offset:2048 sc1\n\t"
    "buffer_load_dwordx4 v[240:243], v127, %12, s61 offen offset:3072 sc1\n\t"
    "s_add_u32 s61, s61, 0x1000\n\t"
    "buffer_load_dwordx4 v[244:247], v127, %12, s61 offen offset:2048 sc1\n\t"
    "buffer_load_dwordx4 v[248:251], v127, %12, s61 offen offset:3072 sc1\n\t"
    "s_add_u32 s61, s61, 0x1000\n\t"
    "buffer_load_dwordx4 v[252:255], v127, %12, s61 offen offset:2048 sc1\n\t"
    "s_mov_b32 s62, 1\n\t"
    "L_mel5_%=:\n\t"
    "buffer_load_dwordx4 v[0:3], v127, %12, s63 offen offset:0 sc1\n\t"
    "buffer_load_dwordx4 v[4:7], v127, %12, s63 offen offset:1024 sc1\n\t"
    "buffer_load_dwordx4 v[8:11], v127, %12, s63 offen offset:2048 sc1\n\t"
    "buffer_load_dwordx4 v[12:15], v127, %12, s63 offen offset:3072 sc1\n\t"
    "s_add_u32 s63, s63, 0x1000\n\t"
    "s_mov_b32 s62, 0\n\t"
    "L_mpf_%=:\n\t"
    "v_lshrrev_b32 v105, 4, v127\n\t"
    "v_and_b32 v105, 48, v105\n\t"
    "s_movk_i32 s65, 130\n\t"
    "v_mad_u32_u24 v104, v155, s65, v105\n\t"
    "v_add_u32 v105, 14170, v104\n\t"
    "v_and_b32 v104, -16, v104\n\t"
    "v_and_b32 v105, -16, v105\n\t"
    "buffer_load_dwordx4 v[128:131], v127, %12, s61 offen offset:3072 sc1\n\t"
    "s_add_u32 s61, s61, 0x1000\n\t"
    "buffer_load_dwordx4 v[132:135], v127, %12, s61 offen offset:2048 sc1\n\t"
    "ds_read_b128 v[156:159], v104 offset:12480\n\t"
    "ds_read_b128 v[160:163], v104 offset:12544\n\t"
    "buffer_load_dwordx4 v[136:139], v127, %12, s61 offen offset:3072 sc1\n\t"
    "s_add_u32 s61, s61, 0x1000\n\t"
    "buffer_load_dwordx4 v[140:143], v127, %12, s61 offen offset:2048 sc1\n\t"
    "ds_read_b128 v[212:215], v105 offset:0\n\t"
    "ds_read_b128 v[216:219], v105 offset:64\n\t"
    "buffer_load_dwordx4 v[144:147], v127, %12, s61 offen offset:3072 sc1\n\t"
    "s_add_u32 s61, s61, 0x1000\n\t"
    "buffer_load_dwordx4 v[148:151], v127, %12, s61 offen offset:2048 sc1\n\t"
    "buffer_load_dwordx4 v[152:155], v127, %12, s61 offen offset:3072 sc1\n\t"
    "s_add_u32 s61, s61, 0x1000\n\t"
    "v_mov_b32 v114, 1\n\t"
    "ds_add_u32 v113, v114\n\t"
    "s_waitcnt lgkmcnt(5)\n\t"
    "s_waitcnt vmcnt(7)\n\t"
    "v_mfma_f32_16x16x32_bf16 v[16:19], v[0:3], v[164:167], 0\n\t"
    "v_mfma_f32_16x16x32_bf16 v[16:19], v[4:7], v[168:171], v[16:19]\n\t"
    "v_mfma_f32_16x16x32_bf16 v[16:19], v[8:11], v[220:223], v[16:19]\n\t"
    "v_mfma_f32_16x16x32_bf16 v[16:19], v[12:15], v[224:227], v[16:19]\n\t"
    "v_mfma_f32_16x16x32_bf16 v[20:23], v[0:3], v[172:175], 0\n\t"
    "v_mfma_f32_16x16x32_bf16 v[20:23], v[4:7], v[176:179], v[20:23]\n\t"
    "v_mfma_f32_16x16x32_bf16 v[20:23], v[8:11], v[228:231], v[20:23]\n\t"
    "v_mfma_f32_16x16x32_bf16 v[20:23], v[12:15], v[232:235], v[20:23]\n\t"
    "v_mfma_f32_16x16x32_bf16 v[80:83], v[0:3], v[180:183], 0\n\t"
    "v_mfma_f32_16x16x32_bf16 v[80:83], v[4:7], v[184:187], v[80:83]\n\t"
    "v_mfma_f32_16x16x32_bf16 v[80:83], v[8:11], v[236:239], v[80:83]\n\t"
    "v_mfma_f32_16x16x32_bf16 v[80:83], v[12:15], v[240:243], v[80:83]\n\t"
    "v_cvt_pk_bf16_f32 v24, v16, v17\n\t"
    "v_cvt_pk_bf16_f32 v25, v18, v19\n\t"
    "v_mfma_f32_16x16x32_bf16 v[16:19], v[0:3], v[188:191], 0\n\t"
    "v_mfma_f32_16x16x32_bf16 v[16:19], v[4:7], v[192:195], v[16:19]\n\t"
    "v_mfma_f32_16x16x32_bf16 v[16:19], v[8:11], v[244:247], v[16:19]\n\t"
    "v_mfma_f32_16x16x32_bf16 v[16:19], v[12:15], v[248:251], v[16:19]\n\t"
    "v_cvt_pk_bf16_f32 v26, v20, v21\n\t"
    "v_cvt_pk_bf16_f32 v27, v22, v23\n\t"
    "s_waitcnt vmcnt(6)\n\t"
    "v_mfma_f32_16x16x32_bf16 v[20:23], v[0:3], v[196:199], 0\n\t"
    "v_mfma_f32_16x16x32_bf16 v[20:23], v[4:7], v[200:203], v[20:23]\n\t"
    "v_mfma_f32_16x16x32_bf16 v[20:23], v[8:11], v[252:255], v[20:23]\n\t"
    "v_mfma_f32_16x16x32_bf16 v[20:23], v[12:15], v[128:131], v[20:23]\n\t"
    "v_cvt_pk_bf16_f32 v28, v80, v81\n\t"
    "v_cvt_pk_bf16_f32 v29, v82, v83\n\t"
    "s_waitcnt lgkmcnt(0)\n\t"
    "s_waitcnt vmcnt(4)\n\t"
    "v_mfma_f32_16x16x32_bf16 v[80:83], v[0:3], v[204:207], 0\n\t"
    "v_mfma_f32_16x16x32_bf16 v[80:83], v[4:7], v[208:211], v[80:83]\n\t"
    "v_mfma_f32_16x16x32_bf16 v[80:83], v[8:11], v[132:135], v[80:83]\n\t"
    "v_mfma_f32_16x16x32_bf16 v[80:83], v[12:15], v[136:139], v[80:83]\n\t"
    "v_cvt_pk_bf16_f32 v30, v16, v17\n\t"
    "v_cvt_pk_bf16_f32 v31, v18, v19\n\t"
    "s_waitcnt vmcnt(2)\n\t"
    "v_mfma_f32_16x16x32_bf16 v[16:19], v[0:3], v[156:159], 0\n\t"
    "buffer_load_dwordx4 v[100:103], v127, %12, s84 offen offset:0 sc1\n\t"
    "s_add_u32 s84, s84, s97\n\t"
    "v_mfma_f32_16x16x32_bf16 v[16:19], v[4:7], v[160:163], v[16:19]\n\t"
    "buffer_load_dwordx4 v[104:107], v127, %12, s85 offen offset:0 sc1\n\t"
    "s_add_u32 s85, s85, s97\n\t"
    "v_mfma_f32_16x16x32_bf16 v[16:19], v[8:11], v[140:143], v[16:19]\n\t"
    "v_mfma_f32_16x16x32_bf16 v[16:19], v[12:15], v[144:147], v[16:19]\n\t"
    "v_cvt_pk_bf16_f32 v32, v20, v21\n\t"
    "v_cvt_pk_bf16_f32 v33, v22, v23\n\t"
    "s_waitcnt vmcnt(2)\n\t"
    "v_mfma_f32_16x16x32_bf16 v[20:23], v[0:3], v[212:215], 0\n\t"
    "buffer_load_dwordx4 v[108:111], v127, %12, s86 offen offset:0 sc1\n\t"
    "s_add_u32 s86, s86, s97\n\t"
    "v_mfma_f32_16x16x32_bf16 v[20:23], v[4:7], v[216:219], v[20:23]\n\t"
    "buffer_load_dwordx4 v[112:115], v127, %12, s87 offen offset:0 sc1\n\t"
    "s_add_u32 s87, s87, s97\n\t"
    "v_mfma_f32_16x16x32_bf16 v[20:23], v[8:11], v[148:151], v[20:23]\n\t"
    "v_mfma_f32_16x16x32_bf16 v[20:23], v[12:15], v[152:155], v[20:23]\n\t"
    "v_cvt_pk_bf16_f32 v34, v80, v81\n\t"
    "v_cvt_pk_bf16_f32 v35, v82, v83\n\t"
    "v_cvt_pk_bf16_f32 v36, v16, v17\n\t"
    "v_cvt_pk_bf16_f32 v37, v18, v19\n\t"
    "s_nop 3\n\t"
    "v_cvt_pk_bf16_f32 v38, v20, v21\n\t"
    "v_cvt_pk_bf16_f32 v39, v22, v23\n\t"
    "v_lshrrev_b32 v41, 2, v127\n\t"
    "s_and_b32 s65, %14, 8\n\t"
    "s_lshl_b32 s65, s65, 10\n\t"
    "s_add_u32 s63, s63, s65\n\t"
    "buffer_store_dword v40, v41, %12, s69 offen sc1\n\t"
    "v_mov_b32 v82, 3108\n\t"
    "s_cmp_eq_u32 %16, 0\n\t"
    "s_cselect_b32 s47, 0x1ff, 0\n\t"
    "L_my_%=:\n\t"
    "ds_read_b32 v81, v82\n\t"
    "s_waitcnt lgkmcnt(0)\n\t"
    "v_cmp_gt_u32 vcc, 8, v81\n\t"
    "s_cbranch_vccz L_mz_%=\n\t"
    "s_sleep 2\n\t"
    "s_branch L_my_%=\n\t"
    "L_mz_%=:\n\t"
    "ds_write_b64 v125, v[24:25] offset:0\n\t"
    "v_mov_b64 v[0:1], 0\n\t"
    "ds_write_b64 v125, v[26:27] offset:128\n\t"
    "v_mov_b64 v[2:3], 0\n\t"
    "ds_write_b64 v125, v[28:29] offset:256\n\t"
    "v_mov_b64 v[4:5], 0\n\t"
    "ds_write_b64 v125, v[30:31] offset:384\n\t"
    "v_mov_b64 v[6:7], 0\n\t"
    "ds_write_b64 v125, v[32:33] offset:512\n\t"
    "ds_write_b64 v125, v[34:35] offset:640\n\t"
    "ds_write_b64 v125, v[36:37] offset:768\n\t"
    "ds_write_b64 v125, v[38:39] offset:872\n\t"
    "v_add_u32 v125, s93, v125\n\t"
    "s_sub_u32 s93, 0, s93\n\t"
    "s_waitcnt lgkmcnt(0)\n\t"
    "s_barrier\n\t"
    "s_memrealtime s[44:45]\n\t"
    "s_waitcnt lgkmcnt(0)\n\t"
    "s_waitcnt vmcnt(4)\n\t"
    "v_add_u32_sdwa v126, s92, v100 dst_sel:DWORD dst_unused:UNUSED_PAD src0_sel:DWORD src1_sel:WORD_1\n\t"
    "v_add_u32_sdwa v100, s92, v100 dst_sel:DWORD dst_unused:UNUSED_PAD src0_sel:DWORD src1_sel:WORD_0\n\t"
    "ds_read_b64 v[84:85], v100 offset:0\n\t"
    "ds_read_b64 v[86:87], v126 offset:0\n\t"
    "v_add_u32_sdwa v90, s92, v101 dst_sel:DWORD dst_unused:UNUSED_PAD src0_sel:DWORD src1_sel:WORD_1\n\t"
    "v_add_u32_sdwa v101, s92, v101 dst_sel:DWORD dst_unused:UNUSED_PAD src0_sel:DWORD src1_sel:WORD_0\n\t"
    "ds_read_b64 v[88:89], v101 offset:0\n\t"
    "ds_read_b64 v[90:91], v90 offset:0\n\t"
    "v_add_u32 v63, s96, v127\n\t"
    "s_add_u32 s96, s96, 0x1000\n\t"
    "s_and_b32 s96, s96, 0x3000\n\t"
    "v_add_u32 v63, 128000, v63\n\t"
    "ds_read_b128 v[48:51], v63 offset:0\n\t"
    "ds_read_b128 v[52:55], v63 offset:1024\n\t"
    "ds_read_b128 v[56:59], v63 offset:2048\n\t"
    "ds_read_b128 v[60:63], v63 offset:3072\n\t"
    "v_add_u32_sdwa v126, s92, v102 dst_sel:DWORD dst_unused:UNUSED_PAD src0_sel:DWORD src1_sel:WORD_1\n\t"
    "v_add_u32_sdwa v102, s92, v102 dst_sel:DWORD dst_unused:UNUSED_PAD src0_sel:DWORD src1_sel:WORD_0\n\t"
    "ds_read_b64 v[92:93], v102 offset:0\n\t"
    "ds_read_b64 v[94:95], v126 offset:0\n\t"
    "v_add_u32_sdwa v98, s92, v103 dst_sel:DWORD dst_unused:UNUSED_PAD src0_sel:DWORD src1_sel:WORD_1\n\t"
    "v_add_u32_sdwa v103, s92, v103 dst_sel:DWORD dst_unused:UNUSED_PAD src0_sel:DWORD src1_sel:WORD_0\n\t"
    "ds_read_b64 v[96:97], v103 offset:0\n\t"
    "ds_read_b64 v[98:99], v98 offset:0\n\t"
    "buffer_load_dwordx4 v[100:103], v127, %12, s88 offen offset:0 sc1\n\t"
    "s_add_u32 s88, s88, s97\n\t"
    "s_waitcnt vmcnt(4)\n\t"
    "v_add_u32_sdwa v126, s92, v104 dst_sel:DWORD dst_unused:UNUSED_PAD src0_sel:DWORD src1_sel:WORD_1\n\t"
    "v_add_u32_sdwa v104, s92, v104 dst_sel:DWORD dst_unused:UNUSED_PAD src0_sel:DWORD src1_sel:WORD_0\n\t"
    "v_mov_b64 v[8:9], 0\n\t"
    "v_mov_b64 v[10:11], 0\n\t"
    "v_mov_b64 v[12:13], 0\n\t"
    "v_mov_b64 v[14:15], 0\n\t"
    "v_mov_b64 v[16:17], 0\n\t"
    "v_mov_b64 v[18:19], 0\n\t"
    "s_waitcnt lgkmcnt(8)\n\t"
    "v_smfmac_f32_16x16x64_bf16 v[0:3], v[120:123], v[84:91], v124\n\t"
    "ds_read_b64 v[84:85], v104 offset:0\n\t"
    "s_waitcnt lgkmcnt(5)\n\t"
    "v_mfma_f32_16x16x32_bf16 v[116:119], v[48:51], v[164:167], 0\n\t"
    "ds_read_b64 v[86:87], v126 offset:0\n\t"
    "v_mfma_f32_16x16x32_bf16 v[116:119], v[52:55], v[168:171], v[116:119]\n\t"
    "v_add_u32_sdwa v90, s92, v105 dst_sel:DWORD dst_unused:UNUSED_PAD src0_sel:DWORD src1_sel:WORD_1\n\t"
    "v_add_u32_sdwa v105, s92, v105 dst_sel:DWORD dst_unused:UNUSED_PAD src0_sel:DWORD src1_sel:WORD_0\n\t"
    "ds_read_b64 v[88:89], v105 offset:0\n\t"
    "v_mfma_f32_16x16x32_bf16 v[116:119], v[56:59], v[220:223], v[116:119]\n\t"
    "ds_read_b64 v[90:91], v90 offset:0\n\t"
    "v_mfma_f32_16x16x32_bf16 v[116:119], v[60:63], v[224:227], v[116:119]\n\t"
    "v_add_u32_sdwa v126, s92, v106 dst_sel:DWORD dst_unused:UNUSED_PAD src0_sel:DWORD src1_sel:WORD_1\n\t"
    "v_add_u32_sdwa v106, s92, v106 dst_sel:DWORD dst_unused:UNUSED_PAD src0_sel:DWORD src1_sel:WORD_0\n\t"
    "v_mfma_f32_16x16x32_bf16 v[80:83], v[48:51], v[172:175], 0\n\t"
    "v_mov_b64 v[20:21], 0\n\t"
    "v_mov_b64 v[22:23], 0\n\t"
    "v_mov_b64 v[24:25], 0\n\t"
    "v_mov_b64 v[26:27], 0\n\t"
    "v_mov_b64 v[28:29], 0\n\t"
    "v_mov_b64 v[30:31], 0\n\t"
    "s_waitcnt vmcnt(3) lgkmcnt(4)\n\t"
    "v_smfmac_f32_16x16x64_bf16 v[4:7], v[120:123], v[92:99], v124\n\t"
    "ds_read_b64 v[92:93], v106 offset:0\n\t"
    "v_mfma_f32_16x16x32_bf16 v[80:83], v[52:55], v[176:179], v[80:83]\n\t"
    "ds_read_b64 v[94:95], v126 offset:0\n\t"
    "v_mfma_f32_16x16x32_bf16 v[80:83], v[56:59], v[228:231], v[80:83]\n\t"
    "v_add_u32_sdwa v98, s92, v107 dst_sel:DWORD dst_unused:UNUSED_PAD src0_sel:DWORD src1_sel:WORD_1\n\t"
    "v_add_u32_sdwa v107, s92, v107 dst_sel:DWORD dst_unused:UNUSED_PAD src0_sel:DWORD src1_sel:WORD_0\n\t"
    "ds_read_b64 v[96:97], v107 offset:0\n\t"
    "v_mfma_f32_16x16x32_bf16 v[80:83], v[60:63], v[232:235], v[80:83]\n\t"
    "ds_read_b64 v[98:99], v98 offset:0\n\t"
    "v_cvt_pk_bf16_f32 v116, v116, v117\n\t"
    "v_cvt_pk_bf16_f32 v117, v118, v119\n\t"
    "ds_write_b64 v125, v[116:117] offset:0\n\t"
    "v_mfma_f32_16x16x32_bf16 v[116:119], v[48:51], v[180:183], 0\n\t"
    "buffer_load_dwordx4 v[104:107], v127, %12, s89 offen offset:0 sc1\n\t"
    "s_add_u32 s89, s89, s97\n\t"
    "v_add_u32_sdwa v126, s92, v108 dst_sel:DWORD dst_unused:UNUSED_PAD src0_sel:DWORD src1_sel:WORD_1\n\t"
    "v_add_u32_sdwa v108, s92, v108 dst_sel:DWORD dst_unused:UNUSED_PAD src0_sel:DWORD src1_sel:WORD_0\n\t"
    "v_mfma_f32_16x16x32_bf16 v[116:119], v[52:55], v[184:187], v[116:119]\n\t"
    "v_mov_b64 v[32:33], 0\n\t"
    "v_mov_b64 v[34:35], 0\n\t"
    "v_mov_b64 v[36:37], 0\n\t"
    "v_mov_b64 v[38:39], 0\n\t"
    "v_mov_b64 v[40:41], 0\n\t"
    "v_mov_b64 v[42:43], 0\n\t"
    "v_mov_b64 v[44:45], 0\n\t"
    "v_mov_b64 v[46:47], 0\n\t"
    "s_waitcnt lgkmcnt(5)\n\t"
    "v_smfmac_f32_16x16x64_bf16 v[8:11], v[120:123], v[84:91], v124\n\t"
    "ds_read_b64 v[84:85], v108 offset:0\n\t"
    "v_mfma_f32_16x16x32_bf16 v[116:119], v[56:59], v[236:239], v[116:119]\n\t"
    "ds_read_b64 v[86:87], v126 offset:0\n\t"
    "v_mfma_f32_16x16x32_bf16 v[116:119], v[60:63], v[240:243], v[116:119]\n\t"
    "v_add_u32_sdwa v90, s92, v109 dst_sel:DWORD dst_unused:UNUSED_PAD src0_sel:DWORD src1_sel:WORD_1\n\t"
    "v_add_u32_sdwa v109, s92, v109 dst_sel:DWORD dst_unused:UNUSED_PAD src0_sel:DWORD src1_sel:WORD_0\n\t"
    "ds_read_b64 v[88:89], v109 offset:0\n\t"
    "v_cvt_pk_bf16_f32 v80, v80, v81\n\t"
    "v_cvt_pk_bf16_f32 v81, v82, v83\n\t"
    "ds_write_b64 v125, v[80:81] offset:128\n\t"
    "v_mfma_f32_16x16x32_bf16 v[80:83], v[48:51], v[188:191], 0\n\t"
    "ds_read_b64 v[90:91], v90 offset:0\n\t"
    "v_mfma_f32_16x16x32_bf16 v[80:83], v[52:55], v[192:195], v[80:83]\n\t"
    "v_add_u32_sdwa v126, s92, v110 dst_sel:DWORD dst_unused:UNUSED_PAD src0_sel:DWORD src1_sel:WORD_1\n\t"
    "v_add_u32_sdwa v110, s92, v110 dst_sel:DWORD dst_unused:UNUSED_PAD src0_sel:DWORD src1_sel:WORD_0\n\t"
    "v_mfma_f32_16x16x32_bf16 v[80:83], v[56:59], v[244:247], v[80:83]\n\t"
    "s_waitcnt vmcnt(3) lgkmcnt(6)\n\t"
    "v_smfmac_f32_16x16x64_bf16 v[12:15], v[120:123], v[92:99], v124\n\t"
    "ds_read_b64 v[92:93], v110 offset:0\n\t"
    "v_mfma_f32_16x16x32_bf16 v[80:83], v[60:63], v[248:251], v[80:83]\n\t"
    "ds_read_b64 v[94:95], v126 offset:0\n\t"
    "v_cvt_pk_bf16_f32 v116, v116, v117\n\t"
    "v_cvt_pk_bf16_f32 v117, v118, v119\n\t"
    "ds_write_b64 v125, v[116:117] offset:256\n\t"
    "v_mfma_f32_16x16x32_bf16 v[116:119], v[48:51], v[196:199], 0\n\t"
    "v_add_u32_sdwa v98, s92, v111 dst_sel:DWORD dst_unused:UNUSED_PAD src0_sel:DWORD src1_sel:WORD_1\n\t"
    "v_add_u32_sdwa v111, s92, v111 dst_sel:DWORD dst_unused:UNUSED_PAD src0_sel:DWORD src1_sel:WORD_0\n\t"
    "ds_read_b64 v[96:97], v111 offset:0\n\t"
    "v_mfma_f32_16x16x32_bf16 v[116:119], v[52:55], v[200:203], v[116:119]\n\t"
    "ds_read_b64 v[98:99], v98 offset:0\n\t"
    "v_mfma_f32_16x16x32_bf16 v[116:119], v[56:59], v[252:255], v[116:119]\n\t"
    "buffer_load_dwordx4 v[108:111], v127, %12, s90 offen offset:0 sc1\n\t"
    "s_add_u32 s90, s90, s97\n\t"
    "v_add_u32_sdwa v126, s92, v112 dst_sel:DWORD dst_unused:UNUSED_PAD src0_sel:DWORD src1_sel:WORD_1\n\t"
    "v_add_u32_sdwa v112, s92, v112 dst_sel:DWORD dst_unused:UNUSED_PAD src0_sel:DWORD src1_sel:WORD_0\n\t"
    "v_mfma_f32_16x16x32_bf16 v[116:119], v[60:63], v[128:131], v[116:119]\n\t"
    "v_cvt_pk_bf16_f32 v0, v0, v1\n\t"
    "v_cvt_pk_bf16_f32 v1, v2, v3\n\t"
    "v_cvt_pk_bf16_f32 v80, v80, v81\n\t"
    "v_cvt_pk_bf16_f32 v81, v82, v83\n\t"
    "ds_write_b64 v125, v[80:81] offset:384\n\t"
    "v_add_u32 v79, s96, v127\n\t"
    "s_add_u32 s96, s96, 0x1000\n\t"
    "s_and_b32 s96, s96, 0x3000\n\t"
    "v_add_u32 v79, 128000, v79\n\t"
    "ds_read_b128 v[64:67], v79 offset:0\n\t"
    "ds_read_b128 v[68:71], v79 offset:1024\n\t"
    "ds_read_b128 v[72:75], v79 offset:2048\n\t"
    "ds_read_b128 v[76:79], v79 offset:3072\n\t"
    "s_waitcnt lgkmcnt(10)\n\t"
    "v_smfmac_f32_16x16x64_bf16 v[16:19], v[120:123], v[84:91], v124\n\t"
    "ds_read_b64 v[84:85], v112 offset:0\n\t"
    "v_mfma_f32_16x16x32_bf16 v[80:83], v[48:51], v[204:207], 0\n\t"
    "ds_read_b64 v[86:87], v126 offset:0\n\t"
    "v_mfma_f32_16x16x32_bf16 v[80:83], v[52:55], v[208:211], v[80:83]\n\t"
    "v_add_u32_sdwa v90, s92, v113 dst_sel:DWORD dst_unused:UNUSED_PAD src0_sel:DWORD src1_sel:WORD_1\n\t"
    "v_add_u32_sdwa v113, s92, v113 dst_sel:DWORD dst_unused:UNUSED_PAD src0_sel:DWORD src1_sel:WORD_0\n\t"
    "ds_read_b64 v[88:89], v113 offset:0\n\t"
    "v_mfma_f32_16x16x32_bf16 v[80:83], v[56:59], v[132:135], v[80:83]\n\t"
    "ds_read_b64 v[90:91], v90 offset:0\n\t"
    "v_mfma_f32_16x16x32_bf16 v[80:83], v[60:63], v[136:139], v[80:83]\n\t"
    "v_add_u32_sdwa v126, s92, v114 dst_sel:DWORD dst_unused:UNUSED_PAD src0_sel:DWORD src1_sel:WORD_1\n\t"
    "v_add_u32_sdwa v114, s92, v114 dst_sel:DWORD dst_unused:UNUSED_PAD src0_sel:DWORD src1_sel:WORD_0\n\t"
    "v_cvt_pk_bf16_f32 v116, v116, v117\n\t"
    "v_cvt_pk_bf16_f32 v117, v118, v119\n\t"
    "ds_write_b64 v125, v[116:117] offset:512\n\t"
    "v_mfma_f32_16x16x32_bf16 v[116:119], v[48:51], v[156:159], 0\n\t"
    "v_cvt_pk_bf16_f32 v2, v4, v5\n\t"
    "v_cvt_pk_bf16_f32 v3, v6, v7\n\t"
    "v_mov_b64 v[4:5], 0\n\t"
    "v_mov_b64 v[6:7], 0\n\t"
    "s_waitcnt vmcnt(2) lgkmcnt(10)\n\t"
    "v_smfmac_f32_16x16x64_bf16 v[20:23], v[120:123], v[92:99], v124\n\t"
    "ds_read_b64 v[92:93], v114 offset:0\n\t"
    "v_mfma_f32_16x16x32_bf16 v[116:119], v[52:55], v[160:163], v[116:119]\n\t"
    "ds_read_b64 v[94:95], v126 offset:0\n\t"
    "v_mfma_f32_16x16x32_bf16 v[116:119], v[56:59], v[140:143], v[116:119]\n\t"
    "v_add_u32_sdwa v98, s92, v115 dst_sel:DWORD dst_unused:UNUSED_PAD src0_sel:DWORD src1_sel:WORD_1\n\t"
    "v_add_u32_sdwa v115, s92, v115 dst_sel:DWORD dst_unused:UNUSED_PAD src0_sel:DWORD src1_sel:WORD_0\n\t"
    "ds_read_b64 v[96:97], v115 offset:0\n\t"
    "v_mfma_f32_16x16x32_bf16 v[116:119], v[60:63], v[144:147], v[116:119]\n\t"
    "ds_read_b64 v[98:99], v98 offset:0\n\t"
    "v_cvt_pk_bf16_f32 v80, v80, v81\n\t"
    "v_cvt_pk_bf16_f32 v81, v82, v83\n\t"
    "ds_write_b64 v125, v[80:81] offset:640\n\t"
    "v_mfma_f32_16x16x32_bf16 v[80:83], v[48:51], v[212:215], 0\n\t"
    "buffer_load_dwordx4 v[112:115], v127, %12, s91 offen offset:0 sc1\n\t"
    "s_add_u32 s91, s91, s97\n\t"
    "v_add_u32_sdwa v126, s92, v100 dst_sel:DWORD dst_unused:UNUSED_PAD src0_sel:DWORD src1_sel:WORD_1\n\t"
    "v_add_u32_sdwa v100, s92, v100 dst_sel:DWORD dst_unused:UNUSED_PAD src0_sel:DWORD src1_sel:WORD_0\n\t"
    "v_mfma_f32_16x16x32_bf16 v[80:83], v[52:55], v[216:219], v[80:83]\n\t"
    "v_cvt_pk_bf16_f32 v8, v8, v9\n\t"
    "v_cvt_pk_bf16_f32 v9, v10, v11\n\t"
    "s_waitcnt lgkmcnt(6)\n\t"
    "v_smfmac_f32_16x16x64_bf16 v[24:27], v[120:123], v[84:91], v124\n\t"
    "ds_read_b64 v[84:85], v100 offset:0\n\t"
    "v_mfma_f32_16x16x32_bf16 v[80:83], v[56:59], v[148:151], v[80:83]\n\t"
    "ds_read_b64 v[86:87], v126 offset:0\n\t"
    "v_mfma_f32_16x16x32_bf16 v[80:83], v[60:63], v[152:155], v[80:83]\n\t"
    "v_add_u32_sdwa v90, s92, v101 dst_sel:DWORD dst_unused:UNUSED_PAD src0_sel:DWORD src1_sel:WORD_1\n\t"
    "v_add_u32_sdwa v101, s92, v101 dst_sel:DWORD dst_unused:UNUSED_PAD src0_sel:DWORD src1_sel:WORD_0\n\t"
    "ds_read_b64 v[88:89], v101 offset:0\n\t"
    "v_cvt_pk_bf16_f32 v116, v116, v117\n\t"
    "v_cvt_pk_bf16_f32 v117, v118, v119\n\t"
    "ds_write_b64 v125, v[116:117] offset:768\n\t"
    "v_mfma_f32_16x16x32_bf16 v[116:119], v[64:67], v[164:167], 0\n\t"
    "ds_read_b64 v[90:91], v90 offset:0\n\t"
    "v_mfma_f32_16x16x32_bf16 v[116:119], v[68:71], v[168:171], v[116:119]\n\t"
    "v_add_u32_sdwa v126, s92, v102 dst_sel:DWORD dst_unused:UNUSED_PAD src0_sel:DWORD src1_sel:WORD_1\n\t"
    "v_add_u32_sdwa v102, s92, v102 dst_sel:DWORD dst_unused:UNUSED_PAD src0_sel:DWORD src1_sel:WORD_0\n\t"
    "v_mfma_f32_16x16x32_bf16 v[116:119], v[72:75], v[220:223], v[116:119]\n\t"
    "v_cvt_pk_bf16_f32 v10, v12, v13\n\t"
    "v_cvt_pk_bf16_f32 v11, v14, v15\n\t"
    "v_mov_b64 v[12:13], 0\n\t"
    "v_mov_b64 v[14:15], 0\n\t"
    "s_cmp_lg_u32 s47, 0\n\t"
    "s_cbranch_scc1 L_mp2_%=\n\t"
    "L_mp2b_%=:\n\t"
    "s_waitcnt vmcnt(2) lgkmcnt(6)\n\t"
    "v_smfmac_f32_16x16x64_bf16 v[28:31], v[120:123], v[92:99], v124\n\t"
    "ds_read_b64 v[92:93], v102 offset:0\n\t"
    "v_mfma_f32_16x16x32_bf16 v[116:119], v[76:79], v[224:227], v[116:119]\n\t"
    "ds_read_b64 v[94:95], v126 offset:0\n\t"
    "v_cvt_pk_bf16_f32 v80, v80, v81\n\t"
    "v_cvt_pk_bf16_f32 v81, v82, v83\n\t"
    "ds_write_b64 v125, v[80:81] offset:872\n\t"
    "v_mfma_f32_16x16x32_bf16 v[80:83], v[64:67], v[172:175], 0\n\t"
    "v_add_u32_sdwa v98, s92, v103 dst_sel:DWORD dst_unused:UNUSED_PAD src0_sel:DWORD src1_sel:WORD_1\n\t"
    "v_add_u32_sdwa v103, s92, v103 dst_sel:DWORD dst_unused:UNUSED_PAD src0_sel:DWORD src1_sel:WORD_0\n\t"
    "ds_read_b64 v[96:97], v103 offset:0\n\t"
    "v_mfma_f32_16x16x32_bf16 v[80:83], v[68:71], v[176:179], v[80:83]\n\t"
    "ds_read_b64 v[98:99], v98 offset:0\n\t"
    "v_mfma_f32_16x16x32_bf16 v[80:83], v[72:75], v[228:231], v[80:83]\n\t"
    "v_add_u32_sdwa v126, s92, v104 dst_sel:DWORD dst_unused:UNUSED_PAD src0_sel:DWORD src1_sel:WORD_1\n\t"
    "v_add_u32_sdwa v104, s92, v104 dst_sel:DWORD dst_unused:UNUSED_PAD src0_sel:DWORD src1_sel:WORD_0\n\t"
    "v_mfma_f32_16x16x32_bf16 v[80:83], v[76:79], v[232:235], v[80:83]\n\t"
    "v_cvt_pk_bf16_f32 v16, v16, v17\n\t"
    "v_cvt_pk_bf16_f32 v17, v18, v19\n\t"
    "v_cvt_pk_bf16_f32 v116, v116, v117\n\t"
    "v_cvt_pk_bf16_f32 v117, v118, v119\n\t"
    "ds_write_b64 v125, v[116:117] offset:32000\n\t"
    "s_waitcnt lgkmcnt(6)\n\t"
    "v_smfmac_f32_16x16x64_bf16 v[32:35], v[120:123], v[84:91], v124\n\t"
    "ds_read_b64 v[84:85], v104 offset:0\n\t"
    "v_mfma_f32_16x16x32_bf16 v[116:119], v[64:67], v[180:183], 0\n\t"
    "ds_read_b64 v[86:87], v126 offset:0\n\t"
    "v_mfma_f32_16x16x32_bf16 v[116:119], v[68:71], v[184:187], v[116:119]\n\t"
    "v_add_u32_sdwa v90, s92, v105 dst_sel:DWORD dst_unused:UNUSED_PAD src0_sel:DWORD src1_sel:WORD_1\n\t"
    "v_add_u32_sdwa v105, s92, v105 dst_sel:DWORD dst_unused:UNUSED_PAD src0_sel:DWORD src1_sel:WORD_0\n\t"
    "ds_read_b64 v[88:89], v105 offset:0\n\t"
    "v_mfma_f32_16x16x32_bf16 v[116:119], v[72:75], v[236:239], v[116:119]\n\t"
    "ds_read_b64 v[90:91], v90 offset:0\n\t"
    "v_mfma_f32_16x16x32_bf16 v[116:119], v[76:79], v[240:243], v[116:119]\n\t"
    "v_add_u32_sdwa v126, s92, v106 dst_sel:DWORD dst_unused:UNUSED_PAD src0_sel:DWORD src1_sel:WORD_1\n\t"
    "v_add_u32_sdwa v106, s92, v106 dst_sel:DWORD dst_unused:UNUSED_PAD src0_sel:DWORD src1_sel:WORD_0\n\t"
    "v_cvt_pk_bf16_f32 v80, v80, v81\n\t"
    "v_cvt_pk_bf16_f32 v81, v82, v83\n\t"
    "ds_write_b64 v125, v[80:81] offset:32128\n\t"
    "v_mfma_f32_16x16x32_bf16 v[80:83], v[64:67], v[188:191], 0\n\t"
    "v_cvt_pk_bf16_f32 v18, v20, v21\n\t"
    "v_cvt_pk_bf16_f32 v19, v22, v23\n\t"
    "v_mov_b64 v[20:21], 0\n\t"
    "v_mov_b64 v[22:23], 0\n\t"
    "s_waitcnt vmcnt(0)\n\t"
    "s_cmp_lg_u32 s47, 0\n\t"
    "s_cbranch_scc1 L_mp3_%=\n\t"
    "L_mp3b_%=:\n\t"
    "s_barrier\n\t"
    "s_add_u32 s84, s84, s95\n\t"
    "buffer_load_dwordx4 v[100:103], v127, %12, s84 offen offset:0 sc1\n\t"
    "s_lshl_b32 s50, %16, 10\n\t"
    "s_add_u32 s63, s63, s50\n\t"
    "s_add_u32 s50, s50, 0x2000\n\t"
    "s_xor_b32 s50, s50, 0x2000\n\t"
    "s_add_u32 m0, s50, 128000\n\t"
    "s_add_u32 s63, s63, 0x4000\n\t"
    "buffer_load_dwordx4 v127, %12, s63 offen sc1 lds\n\t"
    "s_cmp_lt_u32 %16, 4\n\t"
    "s_cbranch_scc1 L_sp1_%=\n\t"
    "s_setprio 1\n\t"
    "L_sp1_%=:\n\t"
    "s_waitcnt lgkmcnt(6)\n\t"
    "v_smfmac_f32_16x16x64_bf16 v[36:39], v[120:123], v[92:99], v124\n\t"
    "ds_read_b64 v[92:93], v106 offset:0\n\t"
    "v_mfma_f32_16x16x32_bf16 v[80:83], v[68:71], v[192:195], v[80:83]\n\t"
    "ds_read_b64 v[94:95], v126 offset:0\n\t"
    "v_mfma_f32_16x16x32_bf16 v[80:83], v[72:75], v[244:247], v[80:83]\n\t"
    "v_add_u32_sdwa v98, s92, v107 dst_sel:DWORD dst_unused:UNUSED_PAD src0_sel:DWORD src1_sel:WORD_1\n\t"
    "v_add_u32_sdwa v107, s92, v107 dst_sel:DWORD dst_unused:UNUSED_PAD src0_sel:DWORD src1_sel:WORD_0\n\t"
    "ds_read_b64 v[96:97], v107 offset:0\n\t"
    "v_mfma_f32_16x16x32_bf16 v[80:83], v[76:79], v[248:251], v[80:83]\n\t"
    "ds_read_b64 v[98:99], v98 offset:0\n\t"
    "v_cvt_pk_bf16_f32 v116, v116, v117\n\t"
    "v_cvt_pk_bf16_f32 v117, v118, v119\n\t"
    "ds_write_b64 v125, v[116:117] offset:32256\n\t"
    "v_mfma_f32_16x16x32_bf16 v[116:119], v[64:67], v[196:199], 0\n\t"
    "buffer_load_dwordx4 v[104:107], v127, %12, s84 offen offset:1024 sc1\n\t"
    "v_add_u32_sdwa v126, s92, v108 dst_sel:DWORD dst_unused:UNUSED_PAD src0_sel:DWORD src1_sel:WORD_1\n\t"
    "v_add_u32_sdwa v108, s92, v108 dst_sel:DWORD dst_unused:UNUSED_PAD src0_sel:DWORD src1_sel:WORD_0\n\t"
    "v_mfma_f32_16x16x32_bf16 v[116:119], v[68:71], v[200:203], v[116:119]\n\t"
    "v_cvt_pk_bf16_f32 v24, v24, v25\n\t"
    "v_cvt_pk_bf16_f32 v25, v26, v27\n\t"
    "v_mov_b64 v[48:49], 0\n\t"
    "v_mov_b64 v[50:51], 0\n\t"
    "v_mov_b64 v[52:53], 0\n\t"
    "v_mov_b64 v[54:55], 0\n\t"
    "s_waitcnt lgkmcnt(6)\n\t"
    "v_smfmac_f32_16x16x64_bf16 v[40:43], v[120:123], v[84:91], v124\n\t"
    "ds_read_b64 v[84:85], v108 offset:0\n\t"
    "v_mfma_f32_16x16x32_bf16 v[116:119], v[72:75], v[252:255], v[116:119]\n\t"
    "ds_read_b64 v[86:87], v126 offset:0\n\t"
    "v_mfma_f32_16x16x32_bf16 v[116:119], v[76:79], v[128:131], v[116:119]\n\t"
    "v_add_u32_sdwa v90, s92, v109 dst_sel:DWORD dst_unused:UNUSED_PAD src0_sel:DWORD src1_sel:WORD_1\n\t"
    "v_add_u32_sdwa v109, s92, v109 dst_sel:DWORD dst_unused:UNUSED_PAD src0_sel:DWORD src1_sel:WORD_0\n\t"
    "ds_read_b64 v[88:89], v109 offset:0\n\t"
    "v_cvt_pk_bf16_f32 v80, v80, v81\n\t"
    "v_cvt_pk_bf16_f32 v81, v82, v83\n\t"
    "ds_write_b64 v125, v[80:81] offset:32384\n\t"
    "v_mfma_f32_16x16x32_bf16 v[80:83], v[64:67], v[204:207], 0\n\t"
    "ds_read_b64 v[90:91], v90 offset:0\n\t"
    "v_mfma_f32_16x16x32_bf16 v[80:83], v[68:71], v[208:211], v[80:83]\n\t"
    "v_add_u32_sdwa v126, s92, v110 dst_sel:DWORD dst_unused:UNUSED_PAD src0_sel:DWORD src1_sel:WORD_1\n\t"
    "v_add_u32_sdwa v110, s92, v110 dst_sel:DWORD dst_unused:UNUSED_PAD src0_sel:DWORD src1_sel:WORD_0\n\t"
    "v_mfma_f32_16x16x32_bf16 v[80:83], v[72:75], v[132:135], v[80:83]\n\t"
    "v_cvt_pk_bf16_f32 v26, v28, v29\n\t"
    "v_cvt_pk_bf16_f32 v27, v30, v31\n\t"
    "v_mov_b64 v[28:29], 0\n\t"
    "v_mov_b64 v[30:31], 0\n\t"
    "s_waitcnt lgkmcnt(6)\n\t"
    "v_smfmac_f32_16x16x64_bf16 v[44:47], v[120:123], v[92:99], v124\n\t"
    "ds_read_b64 v[92:93], v110 offset:0\n\t"
    "v_mfma_f32_16x16x32_bf16 v[80:83], v[76:79], v[136:139], v[80:83]\n\t"
    "ds_read_b64 v[94:95], v126 offset:0\n\t"
    "v_cvt_pk_bf16_f32 v116, v116, v117\n\t"
    "v_cvt_pk_bf16_f32 v117, v118, v119\n\t"
    "ds_write_b64 v125, v[116:117] offset:32512\n\t"
    "v_mfma_f32_16x16x32_bf16 v[116:119], v[64:67], v[156:159], 0\n\t"
    "v_add_u32_sdwa v98, s92, v111 dst_sel:DWORD dst_unused:UNUSED_PAD src0_sel:DWORD src1_sel:WORD_1\n\t"
    "v_add_u32_sdwa v111, s92, v111 dst_sel:DWORD dst_unused:UNUSED_PAD src0_sel:DWORD src1_sel:WORD_0\n\t"
    "ds_read_b64 v[96:97], v111 offset:0\n\t"
    "v_mfma_f32_16x16x32_bf16 v[116:119], v[68:71], v[160:163], v[116:119]\n\t"
    "ds_read_b64 v[98:99], v98 offset:0\n\t"
    "v_mfma_f32_16x16x32_bf16 v[116:119], v[72:75], v[140:143], v[116:119]\n\t"
    "s_add_u32 s85, s85, s95\n\t"
    "buffer_load_dwordx4 v[108:111], v127, %12, s85 offen offset:0 sc1\n\t"
    "v_add_u32_sdwa v126, s92, v112 dst_sel:DWORD dst_unused:UNUSED_PAD src0_sel:DWORD src1_sel:WORD_1\n\t"
    "v_add_u32_sdwa v112, s92, v112 dst_sel:DWORD dst_unused:UNUSED_PAD src0_sel:DWORD src1_sel:WORD_0\n\t"
    "v_mfma_f32_16x16x32_bf16 v[116:119], v[76:79], v[144:147], v[116:119]\n\t"
    "v_cvt_pk_bf16_f32 v32, v32, v33\n\t"
    "v_cvt_pk_bf16_f32 v33, v34, v35\n\t"
    "v_cvt_pk_bf16_f32 v80, v80, v81\n\t"
    "v_cvt_pk_bf16_f32 v81, v82, v83\n\t"
    "ds_write_b64 v125, v[80:81] offset:32640\n\t"
    "v_mov_b64 v[56:57], 0\n\t"
    "v_mov_b64 v[58:59], 0\n\t"
    "v_mov_b64 v[60:61], 0\n\t"
    "v_mov_b64 v[62:63], 0\n\t"
    "s_waitcnt lgkmcnt(6)\n\t"
    "v_smfmac_f32_16x16x64_bf16 v[48:51], v[120:123], v[84:91], v124\n\t"
    "ds_read_b64 v[84:85], v112 offset:0\n\t"
    "v_mfma_f32_16x16x32_bf16 v[80:83], v[64:67], v[212:215], 0\n\t"
    "ds_read_b64 v[86:87], v126 offset:0\n\t"
    "v_mfma_f32_16x16x32_bf16 v[80:83], v[68:71], v[216:219], v[80:83]\n\t"
    "v_add_u32_sdwa v90, s92, v113 dst_sel:DWORD dst_unused:UNUSED_PAD src0_sel:DWORD src1_sel:WORD_1\n\t"
    "v_add_u32_sdwa v113, s92, v113 dst_sel:DWORD dst_unused:UNUSED_PAD src0_sel:DWORD src1_sel:WORD_0\n\t"
    "ds_read_b64 v[88:89], v113 offset:0\n\t"
    "v_mfma_f32_16x16x32_bf16 v[80:83], v[72:75], v[148:151], v[80:83]\n\t"
    "ds_read_b64 v[90:91], v90 offset:0\n\t"
    "v_mfma_f32_16x16x32_bf16 v[80:83], v[76:79], v[152:155], v[80:83]\n\t"
    "v_add_u32_sdwa v126, s92, v114 dst_sel:DWORD dst_unused:UNUSED_PAD src0_sel:DWORD src1_sel:WORD_1\n\t"
    "v_add_u32_sdwa v114, s92, v114 dst_sel:DWORD dst_unused:UNUSED_PAD src0_sel:DWORD src1_sel:WORD_0\n\t"
    "v_cvt_pk_bf16_f32 v116, v116, v117\n\t"
    "v_cvt_pk_bf16_f32 v117, v118, v119\n\t"
    "ds_write_b64 v125, v[116:117] offset:32768\n\t"
    "s_nop 2\n\t"
    "v_cvt_pk_bf16_f32 v80, v80, v81\n\t"
    "v_cvt_pk_bf16_f32 v81, v82, v83\n\t"
    "ds_write_b64 v125, v[80:81] offset:32872\n\t"
    "v_cvt_pk_bf16_f32 v34, v36, v37\n\t"
    "v_cvt_pk_bf16_f32 v35, v38, v39\n\t"
    "v_mov_b64 v[36:37], 0\n\t"
    "v_mov_b64 v[38:39], 0\n\t"
    "v_cvt_pk_bf16_f32 v40, v40, v41\n\t"
    "v_cvt_pk_bf16_f32 v41, v42, v43\n\t"
    "v_cvt_pk_bf16_f32 v42, v44, v45\n\t"
    "v_cvt_pk_bf16_f32 v43, v46, v47\n\t"
    "v_mov_b64 v[44:45], 0\n\t"
    "v_mov_b64 v[46:47], 0\n\t"
    "s_waitcnt vmcnt(3) lgkmcnt(7)\n\t"
    "v_smfmac_f32_16x16x64_bf16 v[52:55], v[120:123], v[92:99], v124\n\t"
    "ds_read_b64 v[92:93], v114 offset:0\n\t"
    "ds_read_b64 v[94:95], v126 offset:0\n\t"
    "v_add_u32_sdwa v98, s92, v115 dst_sel:DWORD dst_unused:UNUSED_PAD src0_sel:DWORD src1_sel:WORD_1\n\t"
    "v_add_u32_sdwa v115, s92, v115 dst_sel:DWORD dst_unused:UNUSED_PAD src0_sel:DWORD src1_sel:WORD_0\n\t"
    "ds_read_b64 v[96:97], v115 offset:0\n\t"
    "ds_read_b64 v[98:99], v98 offset:0\n\t"
    "buffer_load_dwordx4 v[112:115], v127, %12, s85 offen offset:1024 sc1\n\t"
    "s_sub_u32 s92, 64000, s92\n\t"
    "v_add_u32_sdwa v126, s92, v100 dst_sel:DWORD dst_unused:UNUSED_PAD src0_sel:DWORD src1_sel:WORD_1\n\t"
    "v_add_u32_sdwa v100, s92, v100 dst_sel:DWORD dst_unused:UNUSED_PAD src0_sel:DWORD src1_sel:WORD_0\n\t"
    "v_cvt_pk_bf16_f32 v48, v48, v49\n\t"
    "v_cvt_pk_bf16_f32 v49, v50, v51\n\t"
    "s_waitcnt lgkmcnt(4)\n\t"
    "s_waitcnt lgkmcnt(0)\n\t"
    "s_waitcnt vmcnt(3)\n\t"
    "s_barrier\n\t"
    "v_add_u32 v125, s93, v125\n\t"
    "s_sub_u32 s93, 0, s93\n\t"
    "v_smfmac_f32_16x16x64_bf16 v[56:59], v[120:123], v[84:91], v124\n\t"
    "ds_read_b64 v[84:85], v100 offset:0\n\t"
    "ds_read_b64 v[86:87], v126 offset:0\n\t"
    "v_add_u32_sdwa v90, s92, v101 dst_sel:DWORD dst_unused:UNUSED_PAD src0_sel:DWORD src1_sel:WORD_1\n\t"
    "v_add_u32_sdwa v101, s92, v101 dst_sel:DWORD dst_unused:UNUSED_PAD src0_sel:DWORD src1_sel:WORD_0\n\t"
    "ds_read_b64 v[88:89], v101 offset:0\n\t"
    "ds_read_b64 v[90:91], v90 offset:0\n\t"
    "v_add_u32_sdwa v126, s92, v102 dst_sel:DWORD dst_unused:UNUSED_PAD src0_sel:DWORD src1_sel:WORD_1\n\t"
    "v_add_u32_sdwa v102, s92, v102 dst_sel:DWORD dst_unused:UNUSED_PAD src0_sel:DWORD src1_sel:WORD_0\n\t"
    "v_cvt_pk_bf16_f32 v50, v52, v53\n\t"
    "v_cvt_pk_bf16_f32 v51, v54, v55\n\t"
    "v_mov_b64 v[52:53], 0\n\t"
    "v_mov_b64 v[54:55], 0\n\t"
    "s_waitcnt vmcnt(2)\n\t"
    "v_smfmac_f32_16x16x64_bf16 v[60:63], v[120:123], v[92:99], v124\n\t"
    "ds_read_b64 v[92:93], v102 offset:0\n\t"
    "ds_read_b64 v[94:95], v126 offset:0\n\t"
    "v_add_u32_sdwa v98, s92, v103 dst_sel:DWORD dst_unused:UNUSED_PAD src0_sel:DWORD src1_sel:WORD_1\n\t"
    "v_add_u32_sdwa v103, s92, v103 dst_sel:DWORD dst_unused:UNUSED_PAD src0_sel:DWORD src1_sel:WORD_0\n\t"
    "ds_read_b64 v[96:97], v103 offset:0\n\t"
    "ds_read_b64 v[98:99], v98 offset:0\n\t"
    "s_add_u32 s86, s86, s95\n\t"
    "buffer_load_dwordx4 v[100:103], v127, %12, s86 offen offset:0 sc1\n\t"
    "v_add_u32_sdwa v126, s92, v104 dst_sel:DWORD dst_unused:UNUSED_PAD src0_sel:DWORD src1_sel:WORD_1\n\t"
    "v_add_u32_sdwa v104, s92, v104 dst_sel:DWORD dst_unused:UNUSED_PAD src0_sel:DWORD src1_sel:WORD_0\n\t"
    "v_cvt_pk_bf16_f32 v56, v56, v57\n\t"
    "v_cvt_pk_bf16_f32 v57, v58, v59\n\t"
    "v_add_u32 v79, s96, v127\n\t"
    "s_add_u32 s96, s96, 0x1000\n\t"
    "s_and_b32 s96, s96, 0x3000\n\t"
    "v_add_u32 v79, 128000, v79\n\t"
    "ds_read_b128 v[64:67], v79 offset:0\n\t"
    "ds_read_b128 v[68:71], v79 offset:1024\n\t"
    "ds_read_b128 v[72:75], v79 offset:2048\n\t"
    "ds_read_b128 v[76:79], v79 offset:3072\n\t"
    "v_mov_b64 v[116:117], 0\n\t"
    "v_mov_b64 v[118:119], 0\n\t"
    "v_cvt_pk_bf16_f32 v58, v60, v61\n\t"
    "v_cvt_pk_bf16_f32 v59, v62, v63\n\t"
    "v_mov_b64 v[60:61], 0\n\t"
    "v_mov_b64 v[62:63], 0\n\t"
    "s_mov_b32 s66, 4\n\t"
    "s_branch L_loop_%=\n\t"
    ".p2align 9\n\t"
    "s_nop 0\n\t"
    "s_nop 0\n\t"
    "s_nop 0\n\t"
    "s_nop 0\n\t"
    "s_nop 0\n\t"
    "s_nop 0\n\t"
    "s_nop 0\n\t"
    "s_nop 0\n\t"
    "s_nop 0\n\t"
    "s_nop 0\n\t"
    "s_nop 0\n\t"
    "s_nop 0\n\t"
    "s_nop 0\n\t"
    "s_nop 0\n\t"
    "s_nop 0\n\t"
    "s_nop 0\n\t"
    "s_nop 0\n\t"
    "s_nop 0\n\t"
    "s_nop 0\n\t"
    "s_nop 0\n\t"
    "s_nop 0\n\t"
    "s_nop 0\n\t"
    "s_nop 0\n\t"
    "s_nop 0\n\t"
    "s_nop 0\n\t"
    "s_nop 0\n\t"
    "s_nop 0\n\t"
    "s_nop 0\n\t"
    "s_nop 0\n\t"
    "s_nop 0\n\t"
    "s_nop 0\n\t"
    "s_nop 0\n\t"
    "s_nop 0\n\t"
    "L_loop_%=:\n\t"
    "s_waitcnt lgkmcnt(8)\n\t"
    "v_smfmac_f32_16x16x64_bf16 v[116:119], v[120:123], v[84:91], v124\n\t"
    "ds_read_b64 v[84:85], v104 offset:0\n\t"
    "ds_read_b64 v[86:87], v126 offset:0\n\t"
    "v_add_u32_sdwa v90, s92, v105 dst_sel:DWORD dst_unused:UNUSED_PAD src0_sel:DWORD src1_sel:WORD_1\n\t"
    "v_add_u32_sdwa v105, s92, v105 dst_sel:DWORD dst_unused:UNUSED_PAD src0_sel:DWORD src1_sel:WORD_0\n\t"
    "ds_read_b64 v[88:89], v105 offset:0\n\t"
    "ds_read_b64 v[90:91], v90 offset:0\n\t"
    "v_add_u32_sdwa v126, s92, v106 dst_sel:DWORD dst_unused:UNUSED_PAD src0_sel:DWORD src1_sel:WORD_1\n\t"
    "v_add_u32_sdwa v106, s92, v106 dst_sel:DWORD dst_unused:UNUSED_PAD src0_sel:DWORD src1_sel:WORD_0\n\t"
    "s_waitcnt vmcnt(2) lgkmcnt(8)\n\t"
    "v_smfmac_f32_16x16x64_bf16 v[116:119], v[120:123], v[92:99], v124\n\t"
    "ds_read_b64 v[92:93], v106 offset:0\n\t"
    "ds_read_b64 v[94:95], v126 offset:0\n\t"
    "v_add_u32_sdwa v98, s92, v107 dst_sel:DWORD dst_unused:UNUSED_PAD src0_sel:DWORD src1_sel:WORD_1\n\t"
    "v_add_u32_sdwa v107, s92, v107 dst_sel:DWORD dst_unused:UNUSED_PAD src0_sel:DWORD src1_sel:WORD_0\n\t"
    "ds_read_b64 v[96:97], v107 offset:0\n\t"
    "ds_read_b64 v[98:99], v98 offset:0\n\t"
    "buffer_load_dwordx4 v[104:107], v127, %12, s86 offen offset:1024 sc1\n\t"
    "v_add_u32_sdwa v126, s92, v108 dst_sel:DWORD dst_unused:UNUSED_PAD src0_sel:DWORD src1_sel:WORD_1\n\t"
    "v_cvt_pk_bf16_f32 v4, v116, v117\n\t"
    "v_cvt_pk_bf16_f32 v5, v118, v119\n\t"
    "v_and_b32 v119, s67, v7\n\t"
    "v_lshlrev_b32 v118, 16, v7\n\t"
    "v_and_b32 v117, s67, v6\n\t"
    "v_lshlrev_b32 v116, 16, v6\n\t"
    "v_add_u32_sdwa v108, s92, v108 dst_sel:DWORD dst_unused:UNUSED_PAD src0_sel:DWORD src1_sel:WORD_0\n\t"
    "s_waitcnt lgkmcnt(4)\n\t"
    "v_smfmac_f32_16x16x64_bf16 v[116:119], v[120:123], v[84:91], v124\n\t"
    "ds_read_b64 v[84:85], v108 offset:0\n\t"
    "v_mfma_f32_16x16x32_bf16 v[80:83], v[64:67], v[164:167], 0\n\t"
    "ds_read_b64 v[86:87], v126 offset:0\n\t"
    "v_mfma_f32_16x16x32_bf16 v[80:83], v[68:71], v[168:171], v[80:83]\n\t"
    "v_add_u32_sdwa v90, s92, v109 dst_sel:DWORD dst_unused:UNUSED_PAD src0_sel:DWORD src1_sel:WORD_1\n\t"
    "v_add_u32_sdwa v109, s92, v109 dst_sel:DWORD dst_unused:UNUSED_PAD src0_sel:DWORD src1_sel:WORD_0\n\t"
    "ds_read_b64 v[88:89], v109 offset:0\n\t"
    "v_mfma_f32_16x16x32_bf16 v[80:83], v[72:75], v[220:223], v[80:83]\n\t"
    "ds_read_b64 v[90:91], v90 offset:0\n\t"
    "v_mfma_f32_16x16x32_bf16 v[80:83], v[76:79], v[224:227], v[80:83]\n\t"
    "v_add_u32_sdwa v126, s92, v110 dst_sel:DWORD dst_unused:UNUSED_PAD src0_sel:DWORD src1_sel:WORD_1\n\t"
    "v_add_u32_sdwa v110, s92, v110 dst_sel:DWORD dst_unused:UNUSED_PAD src0_sel:DWORD src1_sel:WORD_0\n\t"
    "s_waitcnt vmcnt(2) lgkmcnt(4)\n\t"
    "v_smfmac_f32_16x16x64_bf16 v[116:119], v[120:123], v[92:99], v124\n\t"
    "ds_read_b64 v[92:93], v110 offset:0\n\t"
    "ds_read_b64 v[94:95], v126 offset:0\n\t"
    "v_add_u32_sdwa v98, s92, v111 dst_sel:DWORD dst_unused:UNUSED_PAD src0_sel:DWORD src1_sel:WORD_1\n\t"
    "v_add_u32_sdwa v111, s92, v111 dst_sel:DWORD dst_unused:UNUSED_PAD src0_sel:DWORD src1_sel:WORD_0\n\t"
    "ds_read_b64 v[96:97], v111 offset:0\n\t"
    "ds_read_b64 v[98:99], v98 offset:0\n\t"
    "s_add_u32 s87, s87, s95\n\t"
    "buffer_load_dwordx4 v[108:111], v127, %12, s87 offen offset:0 sc1\n\t"
    "v_add_u32_sdwa v126, s92, v112 dst_sel:DWORD dst_unused:UNUSED_PAD src0_sel:DWORD src1_sel:WORD_1\n\t"
    "v_cvt_pk_bf16_f32 v6, v116, v117\n\t"
    "v_cvt_pk_bf16_f32 v7, v118, v119\n\t"
    "v_and_b32 v119, s67, v13\n\t"
    "v_lshlrev_b32 v118, 16, v13\n\t"
    "v_and_b32 v117, s67, v12\n\t"
    "v_lshlrev_b32 v116, 16, v12\n\t"
    "v_add_u32_sdwa v112, s92, v112 dst_sel:DWORD dst_unused:UNUSED_PAD src0_sel:DWORD src1_sel:WORD_0\n\t"
    "s_waitcnt lgkmcnt(4)\n\t"
    "v_smfmac_f32_16x16x64_bf16 v[116:119], v[120:123], v[84:91], v124\n\t"
    "ds_read_b64 v[84:85], v112 offset:0\n\t"
    "ds_read_b64 v[86:87], v126 offset:0\n\t"
    "v_cvt_pk_bf16_f32 v80, v80, v81\n\t"
    "v_cvt_pk_bf16_f32 v81, v82, v83\n\t"
    "ds_write_b64 v125, v[80:81] offset:0\n\t"
    "v_mfma_f32_16x16x32_bf16 v[80:83], v[64:67], v[172:175], 0\n\t"
    "v_add_u32_sdwa v90, s92, v113 dst_sel:DWORD dst_unused:UNUSED_PAD src0_sel:DWORD src1_sel:WORD_1\n\t"
    "v_add_u32_sdwa v113, s92, v113 dst_sel:DWORD dst_unused:UNUSED_PAD src0_sel:DWORD src1_sel:WORD_0\n\t"
    "ds_read_b64 v[88:89], v113 offset:0\n\t"
    "v_mfma_f32_16x16x32_bf16 v[80:83], v[68:71], v[176:179], v[80:83]\n\t"
    "ds_read_b64 v[90:91], v90 offset:0\n\t"
    "v_mfma_f32_16x16x32_bf16 v[80:83], v[72:75], v[228:231], v[80:83]\n\t"
    "v_mfma_f32_16x16x32_bf16 v[80:83], v[76:79], v[232:235], v[80:83]\n\t"
    "v_add_u32_sdwa v126, s92, v114 dst_sel:DWORD dst_unused:UNUSED_PAD src0_sel:DWORD src1_sel:WORD_1\n\t"
    "v_add_u32_sdwa v114, s92, v114 dst_sel:DWORD dst_unused:UNUSED_PAD src0_sel:DWORD src1_sel:WORD_0\n\t"
    "s_waitcnt vmcnt(2) lgkmcnt(5)\n\t"
    "v_smfmac_f32_16x16x64_bf16 v[116:119], v[120:123], v[92:99], v124\n\t"
    "ds_read_b64 v[92:93], v114 offset:0\n\t"
    "ds_read_b64 v[94:95], v126 offset:0\n\t"
    "v_add_u32_sdwa v98, s92, v115 dst_sel:DWORD dst_unused:UNUSED_PAD src0_sel:DWORD src1_sel:WORD_1\n\t"
    "v_add_u32_sdwa v115, s92, v115 dst_sel:DWORD dst_unused:UNUSED_PAD src0_sel:DWORD src1_sel:WORD_0\n\t"
    "ds_read_b64 v[96:97], v115 offset:0\n\t"
    "ds_read_b64 v[98:99], v98 offset:0\n\t"
    "buffer_load_dwordx4 v[112:115], v127, %12, s87 offen offset:1024 sc1\n\t"
    "v_add_u32_sdwa v126, s92, v100 dst_sel:DWORD dst_unused:UNUSED_PAD src0_sel:DWORD src1_sel:WORD_1\n\t"
    "v_cvt_pk_bf16_f32 v12, v116, v117\n\t"
    "v_cvt_pk_bf16_f32 v13, v118, v119\n\t"
    "v_and_b32 v119, s67, v15\n\t"
    "v_lshlrev_b32 v118, 16, v15\n\t"
    "v_and_b32 v117, s67, v14\n\t"
    "v_lshlrev_b32 v116, 16, v14\n\t"
    "v_add_u32_sdwa v100, s92, v100 dst_sel:DWORD dst_unused:UNUSED_PAD src0_sel:DWORD src1_sel:WORD_0\n\t"
    "s_waitcnt lgkmcnt(4)\n\t"
    "v_smfmac_f32_16x16x64_bf16 v[116:119], v[120:123], v[84:91], v124\n\t"
    "ds_read_b64 v[84:85], v100 offset:0\n\t"
    "ds_read_b64 v[86:87], v126 offset:0\n\t"
    "v_cvt_pk_bf16_f32 v80, v80, v81\n\t"
    "v_cvt_pk_bf16_f32 v81, v82, v83\n\t"
    "ds_write_b64 v125, v[80:81] offset:128\n\t"
    "v_mfma_f32_16x16x32_bf16 v[80:83], v[64:67], v[180:183], 0\n\t"
    "v_add_u32_sdwa v90, s92, v101 dst_sel:DWORD dst_unused:UNUSED_PAD src0_sel:DWORD src1_sel:WORD_1\n\t"
    "v_add_u32_sdwa v101, s92, v101 dst_sel:DWORD dst_unused:UNUSED_PAD src0_sel:DWORD src1_sel:WORD_0\n\t"
    "ds_read_b64 v[88:89], v101 offset:0\n\t"
    "v_mfma_f32_16x16x32_bf16 v[80:83], v[68:71], v[184:187], v[80:83]\n\t"
    "ds_read_b64 v[90:91], v90 offset:0\n\t"
    "v_mfma_f32_16x16x32_bf16 v[80:83], v[72:75], v[236:239], v[80:83]\n\t"
    "v_mfma_f32_16x16x32_bf16 v[80:83], v[76:79], v[240:243], v[80:83]\n\t"
    "v_add_u32_sdwa v126, s92, v102 dst_sel:DWORD dst_unused:UNUSED_PAD src0_sel:DWORD src1_sel:WORD_1\n\t"
    "v_add_u32_sdwa v102, s92, v102 dst_sel:DWORD dst_unused:UNUSED_PAD src0_sel:DWORD src1_sel:WORD_0\n\t"
    "s_waitcnt vmcnt(2) lgkmcnt(5)\n\t"
    "v_smfmac_f32_16x16x64_bf16 v[116:119], v[120:123], v[92:99], v124\n\t"
    "ds_read_b64 v[92:93], v102 offset:0\n\t"
    "ds_read_b64 v[94:95], v126 offset:0\n\t"
    "v_add_u32_sdwa v98, s92, v103 dst_sel:DWORD dst_unused:UNUSED_PAD src0_sel:DWORD src1_sel:WORD_1\n\t"
    "v_add_u32_sdwa v103, s92, v103 dst_sel:DWORD dst_unused:UNUSED_PAD src0_sel:DWORD src1_sel:WORD_0\n\t"
    "ds_read_b64 v[96:97], v103 offset:0\n\t"
    "ds_read_b64 v[98:99], v98 offset:0\n\t"
    "s_add_u32 s88, s88, s95\n\t"
    "buffer_load_dwordx4 v[100:103], v127, %12, s88 offen offset:0 sc1\n\t"
    "v_add_u32_sdwa v126, s92, v104 dst_sel:DWORD dst_unused:UNUSED_PAD src0_sel:DWORD src1_sel:WORD_1\n\t"
    "v_cvt_pk_bf16_f32 v14, v116, v117\n\t"
    "v_cvt_pk_bf16_f32 v15, v118, v119\n\t"
    "v_and_b32 v119, s67, v21\n\t"
    "v_lshlrev_b32 v118, 16, v21\n\t"
    "v_and_b32 v117, s67, v20\n\t"
    "v_lshlrev_b32 v116, 16, v20\n\t"
    "v_add_u32_sdwa v104, s92, v104 dst_sel:DWORD dst_unused:UNUSED_PAD src0_sel:DWORD src1_sel:WORD_0\n\t"
    "s_waitcnt lgkmcnt(4)\n\t"
    "v_smfmac_f32_16x16x64_bf16 v[116:119], v[120:123], v[84:91], v124\n\t"
    "ds_read_b64 v[84:85], v104 offset:0\n\t"
    "ds_read_b64 v[86:87], v126 offset:0\n\t"
    "v_cvt_pk_bf16_f32 v80, v80, v81\n\t"
    "v_cvt_pk_bf16_f32 v81, v82, v83\n\t"
    "ds_write_b64 v125, v[80:81] offset:256\n\t"
    "v_add_u32_sdwa v90, s92, v105 dst_sel:DWORD dst_unused:UNUSED_PAD src0_sel:DWORD src1_sel:WORD_1\n\t"
    "v_add_u32_sdwa v105, s92, v105 dst_sel:DWORD dst_unused:UNUSED_PAD src0_sel:DWORD src1_sel:WORD_0\n\t"
    "ds_read_b64 v[88:89], v105 offset:0\n\t"
    "ds_read_b64 v[90:91], v90 offset:0\n\t"
    "v_add_u32_sdwa v126, s92, v106 dst_sel:DWORD dst_unused:UNUSED_PAD src0_sel:DWORD src1_sel:WORD_1\n\t"
    "v_add_u32_sdwa v106, s92, v106 dst_sel:DWORD dst_unused:UNUSED_PAD src0_sel:DWORD src1_sel:WORD_0\n\t"
    "s_waitcnt vmcnt(2) lgkmcnt(5)\n\t"
    "v_smfmac_f32_16x16x64_bf16 v[116:119], v[120:123], v[92:99], v124\n\t"
    "ds_read_b64 v[92:93], v106 offset:0\n\t"
    "ds_read_b64 v[94:95], v126 offset:0\n\t"
    "v_add_u32_sdwa v98, s92, v107 dst_sel:DWORD dst_unused:UNUSED_PAD src0_sel:DWORD src1_sel:WORD_1\n\t"
    "v_add_u32_sdwa v107, s92, v107 dst_sel:DWORD dst_unused:UNUSED_PAD src0_sel:DWORD src1_sel:WORD_0\n\t"
    "ds_read_b64 v[96:97], v107 offset:0\n\t"
    "ds_read_b64 v[98:99], v98 offset:0\n\t"
    "buffer_load_dwordx4 v[104:107], v127, %12, s88 offen offset:1024 sc1\n\t"
    "v_add_u32_sdwa v126, s92, v108 dst_sel:DWORD dst_unused:UNUSED_PAD src0_sel:DWORD src1_sel:WORD_1\n\t"
    "v_cvt_pk_bf16_f32 v20, v116, v117\n\t"
    "v_cvt_pk_bf16_f32 v21, v118, v119\n\t"
    "v_and_b32 v119, s67, v23\n\t"
    "v_lshlrev_b32 v118, 16, v23\n\t"
    "v_and_b32 v117, s67, v22\n\t"
    "v_lshlrev_b32 v116, 16, v22\n\t"
    "v_add_u32_sdwa v108, s92, v108 dst_sel:DWORD dst_unused:UNUSED_PAD src0_sel:DWORD src1_sel:WORD_0\n\t"
    "s_waitcnt lgkmcnt(4)\n\t"
    "v_smfmac_f32_16x16x64_bf16 v[116:119], v[120:123], v[84:91], v124\n\t"
    "ds_read_b64 v[84:85], v108 offset:0\n\t"
    "v_mfma_f32_16x16x32_bf16 v[80:83], v[64:67], v[188:191], 0\n\t"
    "ds_read_b64 v[86:87], v126 offset:0\n\t"
    "v_mfma_f32_16x16x32_bf16 v[80:83], v[68:71], v[192:195], v[80:83]\n\t"
    "v_add_u32_sdwa v90, s92, v109 dst_sel:DWORD dst_unused:UNUSED_PAD src0_sel:DWORD src1_sel:WORD_1\n\t"
    "v_add_u32_sdwa v109, s92, v109 dst_sel:DWORD dst_unused:UNUSED_PAD src0_sel:DWORD src1_sel:WORD_0\n\t"
    "ds_read_b64 v[88:89], v109 offset:0\n\t"
    "v_mfma_f32_16x16x32_bf16 v[80:83], v[72:75], v[244:247], v[80:83]\n\t"
    "ds_read_b64 v[90:91], v90 offset:0\n\t"
    "v_mfma_f32_16x16x32_bf16 v[80:83], v[76:79], v[248:251], v[80:83]\n\t"
    "v_add_u32_sdwa v126, s92, v110 dst_sel:DWORD dst_unused:UNUSED_PAD src0_sel:DWORD src1_sel:WORD_1\n\t"
    "v_add_u32_sdwa v110, s92, v110 dst_sel:DWORD dst_unused:UNUSED_PAD src0_sel:DWORD src1_sel:WORD_0\n\t"
    "s_waitcnt vmcnt(2) lgkmcnt(4)\n\t"
    "v_smfmac_f32_16x16x64_bf16 v[116:119], v[120:123], v[92:99], v124\n\t"
    "ds_read_b64 v[92:93], v110 offset:0\n\t"
    "ds_read_b64 v[94:95], v126 offset:0\n\t"
    "v_add_u32_sdwa v98, s92, v111 dst_sel:DWORD dst_unused:UNUSED_PAD src0_sel:DWORD src1_sel:WORD_1\n\t"
    "v_add_u32_sdwa v111, s92, v111 dst_sel:DWORD dst_unused:UNUSED_PAD src0_sel:DWORD src1_sel:WORD_0\n\t"
    "ds_read_b64 v[96:97], v111 offset:0\n\t"
    "ds_read_b64 v[98:99], v98 offset:0\n\t"
    "s_add_u32 s89, s89, s95\n\t"
    "buffer_load_dwordx4 v[108:111], v127, %12, s89 offen offset:0 sc1\n\t"
    "v_add_u32_sdwa v126, s92, v112 dst_sel:DWORD dst_unused:UNUSED_PAD src0_sel:DWORD src1_sel:WORD_1\n\t"
    "v_cvt_pk_bf16_f32 v22, v116, v117\n\t"
    "v_cvt_pk_bf16_f32 v23, v118, v119\n\t"
    "v_and_b32 v119, s67, v29\n\t"
    "v_lshlrev_b32 v118, 16, v29\n\t"
    "v_and_b32 v117, s67, v28\n\t"
    "v_lshlrev_b32 v116, 16, v28\n\t"
    "v_add_u32_sdwa v112, s92, v112 dst_sel:DWORD dst_unused:UNUSED_PAD src0_sel:DWORD src1_sel:WORD_0\n\t"
    "s_waitcnt lgkmcnt(4)\n\t"
    "v_smfmac_f32_16x16x64_bf16 v[116:119], v[120:123], v[84:91], v124\n\t"
    "ds_read_b64 v[84:85], v112 offset:0\n\t"
    "ds_read_b64 v[86:87], v126 offset:0\n\t"
    "v_cvt_pk_bf16_f32 v80, v80, v81\n\t"
    "v_cvt_pk_bf16_f32 v81, v82, v83\n\t"
    "ds_write_b64 v125, v[80:81] offset:384\n\t"
    "v_add_u32_sdwa v90, s92, v113 dst_sel:DWORD dst_unused:UNUSED_PAD src0_sel:DWORD src1_sel:WORD_1\n\t"
    "v_add_u32_sdwa v113, s92, v113 dst_sel:DWORD dst_unused:UNUSED_PAD src0_sel:DWORD src1_sel:WORD_0\n\t"
    "ds_read_b64 v[88:89], v113 offset:0\n\t"
    "ds_read_b64 v[90:91], v90 offset:0\n\t"
    "v_add_u32_sdwa v126, s92, v114 dst_sel:DWORD dst_unused:UNUSED_PAD src0_sel:DWORD src1_sel:WORD_1\n\t"
    "v_add_u32_sdwa v114, s92, v114 dst_sel:DWORD dst_unused:UNUSED_PAD src0_sel:DWORD src1_sel:WORD_0\n\t"
    "s_waitcnt vmcnt(2) lgkmcnt(5)\n\t"
    "v_smfmac_f32_16x16x64_bf16 v[116:119], v[120:123], v[92:99], v124\n\t"
    "ds_read_b64 v[92:93], v114 offset:0\n\t"
    "ds_read_b64 v[94:95], v126 offset:0\n\t"
    "v_add_u32_sdwa v98, s92, v115 dst_sel:DWORD dst_unused:UNUSED_PAD src0_sel:DWORD src1_sel:WORD_1\n\t"
    "v_add_u32_sdwa v115, s92, v115 dst_sel:DWORD dst_unused:UNUSED_PAD src0_sel:DWORD src1_sel:WORD_0\n\t"
    "ds_read_b64 v[96:97], v115 offset:0\n\t"
    "ds_read_b64 v[98:99], v98 offset:0\n\t"
    "buffer_load_dwordx4 v[112:115], v127, %12, s89 offen offset:1024 sc1\n\t"
    "v_add_u32_sdwa v126, s92, v100 dst_sel:DWORD dst_unused:UNUSED_PAD src0_sel:DWORD src1_sel:WORD_1\n\t"
    "v_cvt_pk_bf16_f32 v28, v116, v117\n\t"
    "v_cvt_pk_bf16_f32 v29, v118, v119\n\t"
    "v_and_b32 v119, s67, v31\n\t"
    "v_lshlrev_b32 v118, 16, v31\n\t"
    "v_and_b32 v117, s67, v30\n\t"
    "v_lshlrev_b32 v116, 16, v30\n\t"
    "v_add_u32_sdwa v100, s92, v100 dst_sel:DWORD dst_unused:UNUSED_PAD src0_sel:DWORD src1_sel:WORD_0\n\t"
    "s_waitcnt lgkmcnt(4)\n\t"
    "v_smfmac_f32_16x16x64_bf16 v[116:119], v[120:123], v[84:91], v124\n\t"
    "ds_read_b64 v[84:85], v100 offset:0\n\t"
    "v_mfma_f32_16x16x32_bf16 v[80:83], v[64:67], v[196:199], 0\n\t"
    "ds_read_b64 v[86:87], v126 offset:0\n\t"
    "v_mfma_f32_16x16x32_bf16 v[80:83], v[68:71], v[200:203], v[80:83]\n\t"
    "v_add_u32_sdwa v90, s92, v101 dst_sel:DWORD dst_unused:UNUSED_PAD src0_sel:DWORD src1_sel:WORD_1\n\t"
    "v_add_u32_sdwa v101, s92, v101 dst_sel:DWORD dst_unused:UNUSED_PAD src0_sel:DWORD src1_sel:WORD_0\n\t"
    "ds_read_b64 v[88:89], v101 offset:0\n\t"
    "v_mfma_f32_16x16x32_bf16 v[80:83], v[72:75], v[252:255], v[80:83]\n\t"
    "ds_read_b64 v[90:91], v90 offset:0\n\t"
    "v_mfma_f32_16x16x32_bf16 v[80:83], v[76:79], v[128:131], v[80:83]\n\t"
    "v_add_u32_sdwa v126, s92, v102 dst_sel:DWORD dst_unused:UNUSED_PAD src0_sel:DWORD src1_sel:WORD_1\n\t"
    "v_add_u32_sdwa v102, s92, v102 dst_sel:DWORD dst_unused:UNUSED_PAD src0_sel:DWORD src1_sel:WORD_0\n\t"
    "s_xor_b32 s50, s50, 0x2000\n\t"
    "s_add_u32 m0, s50, 128000\n\t"
    "s_add_u32 s63, s63, 0x4000\n\t"
    "buffer_load_dwordx4 v127, %12, s63 offen sc1 lds\n\t"
    "s_waitcnt vmcnt(3) lgkmcnt(4)\n\t"
    "v_smfmac_f32_16x16x64_bf16 v[116:119], v[120:123], v[92:99], v124\n\t"
    "ds_read_b64 v[92:93], v102 offset:0\n\t"
    "ds_read_b64 v[94:95], v126 offset:0\n\t"
    "v_add_u32_sdwa v98, s92, v103 dst_sel:DWORD dst_unused:UNUSED_PAD src0_sel:DWORD src1_sel:WORD_1\n\t"
    "v_add_u32_sdwa v103, s92, v103 dst_sel:DWORD dst_unused:UNUSED_PAD src0_sel:DWORD src1_sel:WORD_0\n\t"
    "ds_read_b64 v[96:97], v103 offset:0\n\t"
    "ds_read_b64 v[98:99], v98 offset:0\n\t"
    "s_add_u32 s90, s90, s95\n\t"
    "buffer_load_dwordx4 v[100:103], v127, %12, s90 offen offset:0 sc1\n\t"
    "v_add_u32_sdwa v126, s92, v104 dst_sel:DWORD dst_unused:UNUSED_PAD src0_sel:DWORD src1_sel:WORD_1\n\t"
    "v_cvt_pk_bf16_f32 v30, v116, v117\n\t"
    "v_cvt_pk_bf16_f32 v31, v118, v119\n\t"
    "v_and_b32 v119, s67, v37\n\t"
    "v_lshlrev_b32 v118, 16, v37\n\t"
    "v_and_b32 v117, s67, v36\n\t"
    "v_lshlrev_b32 v116, 16, v36\n\t"
    "v_add_u32_sdwa v104, s92, v104 dst_sel:DWORD dst_unused:UNUSED_PAD src0_sel:DWORD src1_sel:WORD_0\n\t"
    "s_waitcnt lgkmcnt(4)\n\t"
    "v_smfmac_f32_16x16x64_bf16 v[116:119], v[120:123], v[84:91], v124\n\t"
    "ds_read_b64 v[84:85], v104 offset:0\n\t"
    "ds_read_b64 v[86:87], v126 offset:0\n\t"
    "v_cvt_pk_bf16_f32 v80, v80, v81\n\t"
    "v_cvt_pk_bf16_f32 v81, v82, v83\n\t"
    "ds_write_b64 v125, v[80:81] offset:512\n\t"
    "v_add_u32_sdwa v90, s92, v105 dst_sel:DWORD dst_unused:UNUSED_PAD src0_sel:DWORD src1_sel:WORD_1\n\t"
    "v_add_u32_sdwa v105, s92, v105 dst_sel:DWORD dst_unused:UNUSED_PAD src0_sel:DWORD src1_sel:WORD_0\n\t"
    "ds_read_b64 v[88:89], v105 offset:0\n\t"
    "ds_read_b64 v[90:91], v90 offset:0\n\t"
    "v_add_u32_sdwa v126, s92, v106 dst_sel:DWORD dst_unused:UNUSED_PAD src0_sel:DWORD src1_sel:WORD_1\n\t"
    "v_add_u32_sdwa v106, s92, v106 dst_sel:DWORD dst_unused:UNUSED_PAD src0_sel:DWORD src1_sel:WORD_0\n\t"
    "s_waitcnt vmcnt(3) lgkmcnt(5)\n\t"
    "v_smfmac_f32_16x16x64_bf16 v[116:119], v[120:123], v[92:99], v124\n\t"
    "ds_read_b64 v[92:93], v106 offset:0\n\t"
    "ds_read_b64 v[94:95], v126 offset:0\n\t"
    "v_add_u32_sdwa v98, s92, v107 dst_sel:DWORD dst_unused:UNUSED_PAD src0_sel:DWORD src1_sel:WORD_1\n\t"
    "v_add_u32_sdwa v107, s92, v107 dst_sel:DWORD dst_unused:UNUSED_PAD src0_sel:DWORD src1_sel:WORD_0\n\t"
    "ds_read_b64 v[96:97], v107 offset:0\n\t"
    "ds_read_b64 v[98:99], v98 offset:0\n\t"
    "buffer_load_dwordx4 v[104:107], v127, %12, s90 offen offset:1024 sc1\n\t"
    "v_add_u32_sdwa v126, s92, v108 dst_sel:DWORD dst_unused:UNUSED_PAD src0_sel:DWORD src1_sel:WORD_1\n\t"
    "v_cvt_pk_bf16_f32 v36, v116, v117\n\t"
    "v_cvt_pk_bf16_f32 v37, v118, v119\n\t"
    "v_and_b32 v119, s67, v39\n\t"
    "v_lshlrev_b32 v118, 16, v39\n\t"
    "v_and_b32 v117, s67, v38\n\t"
    "v_lshlrev_b32 v116, 16, v38\n\t"
    "v_add_u32_sdwa v108, s92, v108 dst_sel:DWORD dst_unused:UNUSED_PAD src0_sel:DWORD src1_sel:WORD_0\n\t"
    "s_waitcnt lgkmcnt(4)\n\t"
    "v_smfmac_f32_16x16x64_bf16 v[116:119], v[120:123], v[84:91], v124\n\t"
    "ds_read_b64 v[84:85], v108 offset:0\n\t"
    "v_mfma_f32_16x16x32_bf16 v[80:83], v[64:67], v[204:207], 0\n\t"
    "ds_read_b64 v[86:87], v126 offset:0\n\t"
    "v_mfma_f32_16x16x32_bf16 v[80:83], v[68:71], v[208:211], v[80:83]\n\t"
    "v_add_u32_sdwa v90, s92, v109 dst_sel:DWORD dst_unused:UNUSED_PAD src0_sel:DWORD src1_sel:WORD_1\n\t"
    "v_add_u32_sdwa v109, s92, v109 dst_sel:DWORD dst_unused:UNUSED_PAD src0_sel:DWORD src1_sel:WORD_0\n\t"
    "ds_read_b64 v[88:89], v109 offset:0\n\t"
    "v_mfma_f32_16x16x32_bf16 v[80:83], v[72:75], v[132:135], v[80:83]\n\t"
    "ds_read_b64 v[90:91], v90 offset:0\n\t"
    "v_mfma_f32_16x16x32_bf16 v[80:83], v[76:79], v[136:139], v[80:83]\n\t"
    "v_add_u32_sdwa v126, s92, v110 dst_sel:DWORD dst_unused:UNUSED_PAD src0_sel:DWORD src1_sel:WORD_1\n\t"
    "v_add_u32_sdwa v110, s92, v110 dst_sel:DWORD dst_unused:UNUSED_PAD src0_sel:DWORD src1_sel:WORD_0\n\t"
    "s_cmp_lt_u32 %16, 4\n\t"
    "s_cbranch_scc1 L_tp19_%=\n\t"
    "s_setprio 0\n\t"
    "L_tp19_%=:\n\t"
    "s_waitcnt vmcnt(3) lgkmcnt(4)\n\t"
    "v_smfmac_f32_16x16x64_bf16 v[116:119], v[120:123], v[92:99], v124\n\t"
    "ds_read_b64 v[92:93], v110 offset:0\n\t"
    "ds_read_b64 v[94:95], v126 offset:0\n\t"
    "v_add_u32_sdwa v98, s92, v111 dst_sel:DWORD dst_unused:UNUSED_PAD src0_sel:DWORD src1_sel:WORD_1\n\t"
    "v_add_u32_sdwa v111, s92, v111 dst_sel:DWORD dst_unused:UNUSED_PAD src0_sel:DWORD src1_sel:WORD_0\n\t"
    "ds_read_b64 v[96:97], v111 offset:0\n\t"
    "ds_read_b64 v[98:99], v98 offset:0\n\t"
    "s_add_u32 s91, s91, s95\n\t"
    "buffer_load_dwordx4 v[108:111], v127, %12, s91 offen offset:0 sc1\n\t"
    "v_add_u32_sdwa v126, s92, v112 dst_sel:DWORD dst_unused:UNUSED_PAD src0_sel:DWORD src1_sel:WORD_1\n\t"
    "v_cvt_pk_bf16_f32 v38, v116, v117\n\t"
    "v_cvt_pk_bf16_f32 v39, v118, v119\n\t"
    "v_and_b32 v119, s67, v45\n\t"
    "v_lshlrev_b32 v118, 16, v45\n\t"
    "v_and_b32 v117, s67, v44\n\t"
    "v_lshlrev_b32 v116, 16, v44\n\t"
    "v_add_u32_sdwa v112, s92, v112 dst_sel:DWORD dst_unused:UNUSED_PAD src0_sel:DWORD src1_sel:WORD_0\n\t"
    "s_waitcnt lgkmcnt(4)\n\t"
    "v_smfmac_f32_16x16x64_bf16 v[116:119], v[120:123], v[84:91], v124\n\t"
    "ds_read_b64 v[84:85], v112 offset:0\n\t"
    "ds_read_b64 v[86:87], v126 offset:0\n\t"
    "v_cvt_pk_bf16_f32 v80, v80, v81\n\t"
    "v_cvt_pk_bf16_f32 v81, v82, v83\n\t"
    "ds_write_b64 v125, v[80:81] offset:640\n\t"
    "v_add_u32_sdwa v90, s92, v113 dst_sel:DWORD dst_unused:UNUSED_PAD src0_sel:DWORD src1_sel:WORD_1\n\t"
    "v_add_u32_sdwa v113, s92, v113 dst_sel:DWORD dst_unused:UNUSED_PAD src0_sel:DWORD src1_sel:WORD_0\n\t"
    "ds_read_b64 v[88:89], v113 offset:0\n\t"
    "ds_read_b64 v[90:91], v90 offset:0\n\t"
    "v_add_u32_sdwa v126, s92, v114 dst_sel:DWORD dst_unused:UNUSED_PAD src0_sel:DWORD src1_sel:WORD_1\n\t"
    "v_add_u32_sdwa v114, s92, v114 dst_sel:DWORD dst_unused:UNUSED_PAD src0_sel:DWORD src1_sel:WORD_0\n\t"
    "s_waitcnt vmcnt(2) lgkmcnt(5)\n\t"
    "v_smfmac_f32_16x16x64_bf16 v[116:119], v[120:123], v[92:99], v124\n\t"
    "ds_read_b64 v[92:93], v114 offset:0\n\t"
    "ds_read_b64 v[94:95], v126 offset:0\n\t"
    "v_add_u32_sdwa v98, s92, v115 dst_sel:DWORD dst_unused:UNUSED_PAD src0_sel:DWORD src1_sel:WORD_1\n\t"
    "v_add_u32_sdwa v115, s92, v115 dst_sel:DWORD dst_unused:UNUSED_PAD src0_sel:DWORD src1_sel:WORD_0\n\t"
    "ds_read_b64 v[96:97], v115 offset:0\n\t"
    "ds_read_b64 v[98:99], v98 offset:0\n\t"
    "buffer_load_dwordx4 v[112:115], v127, %12, s91 offen offset:1024 sc1\n\t"
    "v_add_u32_sdwa v126, s92, v100 dst_sel:DWORD dst_unused:UNUSED_PAD src0_sel:DWORD src1_sel:WORD_1\n\t"
    "v_cvt_pk_bf16_f32 v44, v116, v117\n\t"
    "v_cvt_pk_bf16_f32 v45, v118, v119\n\t"
    "v_and_b32 v119, s67, v47\n\t"
    "v_lshlrev_b32 v118, 16, v47\n\t"
    "v_and_b32 v117, s67, v46\n\t"
    "v_lshlrev_b32 v116, 16, v46\n\t"
    "v_add_u32_sdwa v100, s92, v100 dst_sel:DWORD dst_unused:UNUSED_PAD src0_sel:DWORD src1_sel:WORD_0\n\t"
    "s_waitcnt lgkmcnt(4)\n\t"
    "v_smfmac_f32_16x16x64_bf16 v[116:119], v[120:123], v[84:91], v124\n\t"
    "ds_read_b64 v[84:85], v100 offset:0\n\t"
    "v_mfma_f32_16x16x32_bf16 v[80:83], v[64:67], v[156:159], 0\n\t"
    "ds_read_b64 v[86:87], v126 offset:0\n\t"
    "v_mfma_f32_16x16x32_bf16 v[80:83], v[68:71], v[160:163], v[80:83]\n\t"
    "v_add_u32_sdwa v90, s92, v101 dst_sel:DWORD dst_unused:UNUSED_PAD src0_sel:DWORD src1_sel:WORD_1\n\t"
    "v_add_u32_sdwa v101, s92, v101 dst_sel:DWORD dst_unused:UNUSED_PAD src0_sel:DWORD src1_sel:WORD_0\n\t"
    "ds_read_b64 v[88:89], v101 offset:0\n\t"
    "v_mfma_f32_16x16x32_bf16 v[80:83], v[72:75], v[140:143], v[80:83]\n\t"
    "ds_read_b64 v[90:91], v90 offset:0\n\t"
    "v_mfma_f32_16x16x32_bf16 v[80:83], v[76:79], v[144:147], v[80:83]\n\t"
    "v_add_u32_sdwa v126, s92, v102 dst_sel:DWORD dst_unused:UNUSED_PAD src0_sel:DWORD src1_sel:WORD_1\n\t"
    "v_add_u32_sdwa v102, s92, v102 dst_sel:DWORD dst_unused:UNUSED_PAD src0_sel:DWORD src1_sel:WORD_0\n\t"
    "s_waitcnt vmcnt(2) lgkmcnt(4)\n\t"
    "v_smfmac_f32_16x16x64_bf16 v[116:119], v[120:123], v[92:99], v124\n\t"
    "ds_read_b64 v[92:93], v102 offset:0\n\t"
    "ds_read_b64 v[94:95], v126 offset:0\n\t"
    "v_add_u32_sdwa v98, s92, v103 dst_sel:DWORD dst_unused:UNUSED_PAD src0_sel:DWORD src1_sel:WORD_1\n\t"
    "v_add_u32_sdwa v103, s92, v103 dst_sel:DWORD dst_unused:UNUSED_PAD src0_sel:DWORD src1_sel:WORD_0\n\t"
    "ds_read_b64 v[96:97], v103 offset:0\n\t"
    "ds_read_b64 v[98:99], v98 offset:0\n\t"
    "s_add_u32 s84, s84, s94\n\t"
    "buffer_load_dwordx4 v[100:103], v127, %12, s84 offen offset:0 sc1\n\t"
    "v_add_u32_sdwa v126, s92, v104 dst_sel:DWORD dst_unused:UNUSED_PAD src0_sel:DWORD src1_sel:WORD_1\n\t"
    "v_cvt_pk_bf16_f32 v46, v116, v117\n\t"
    "v_cvt_pk_bf16_f32 v47, v118, v119\n\t"
    "v_and_b32 v119, s67, v53\n\t"
    "v_lshlrev_b32 v118, 16, v53\n\t"
    "v_and_b32 v117, s67, v52\n\t"
    "v_lshlrev_b32 v116, 16, v52\n\t"
    "v_add_u32_sdwa v104, s92, v104 dst_sel:DWORD dst_unused:UNUSED_PAD src0_sel:DWORD src1_sel:WORD_0\n\t"
    "s_waitcnt lgkmcnt(4)\n\t"
    "v_smfmac_f32_16x16x64_bf16 v[116:119], v[120:123], v[84:91], v124\n\t"
    "ds_read_b64 v[84:85], v104 offset:0\n\t"
    "ds_read_b64 v[86:87], v126 offset:0\n\t"
    "v_cvt_pk_bf16_f32 v80, v80, v81\n\t"
    "v_cvt_pk_bf16_f32 v81, v82, v83\n\t"
    "ds_write_b64 v125, v[80:81] offset:768\n\t"
    "v_add_u32_sdwa v90, s92, v105 dst_sel:DWORD dst_unused:UNUSED_PAD src0_sel:DWORD src1_sel:WORD_1\n\t"
    "v_add_u32_sdwa v105, s92, v105 dst_sel:DWORD dst_unused:UNUSED_PAD src0_sel:DWORD src1_sel:WORD_0\n\t"
    "ds_read_b64 v[88:89], v105 offset:0\n\t"
    "ds_read_b64 v[90:91], v90 offset:0\n\t"
    "v_add_u32_sdwa v126, s92, v106 dst_sel:DWORD dst_unused:UNUSED_PAD src0_sel:DWORD src1_sel:WORD_1\n\t"
    "v_add_u32_sdwa v106, s92, v106 dst_sel:DWORD dst_unused:UNUSED_PAD src0_sel:DWORD src1_sel:WORD_0\n\t"
    "s_waitcnt vmcnt(2) lgkmcnt(5)\n\t"
    "v_smfmac_f32_16x16x64_bf16 v[116:119], v[120:123], v[92:99], v124\n\t"
    "ds_read_b64 v[92:93], v106 offset:0\n\t"
    "ds_read_b64 v[94:95], v126 offset:0\n\t"
    "v_add_u32_sdwa v98, s92, v107 dst_sel:DWORD dst_unused:UNUSED_PAD src0_sel:DWORD src1_sel:WORD_1\n\t"
    "v_add_u32_sdwa v107, s92, v107 dst_sel:DWORD dst_unused:UNUSED_PAD src0_sel:DWORD src1_sel:WORD_0\n\t"
    "ds_read_b64 v[96:97], v107 offset:0\n\t"
    "ds_read_b64 v[98:99], v98 offset:0\n\t"
    "buffer_load_dwordx4 v[104:107], v127, %12, s84 offen offset:1024 sc1\n\t"
    "v_add_u32_sdwa v126, s92, v108 dst_sel:DWORD dst_unused:UNUSED_PAD src0_sel:DWORD src1_sel:WORD_1\n\t"
    "v_cvt_pk_bf16_f32 v52, v116, v117\n\t"
    "v_cvt_pk_bf16_f32 v53, v118, v119\n\t"
    "v_and_b32 v119, s67, v55\n\t"
    "v_lshlrev_b32 v118, 16, v55\n\t"
    "v_and_b32 v117, s67, v54\n\t"
    "v_lshlrev_b32 v116, 16, v54\n\t"
    "v_add_u32_sdwa v108, s92, v108 dst_sel:DWORD dst_unused:UNUSED_PAD src0_sel:DWORD src1_sel:WORD_0\n\t"
    "s_waitcnt lgkmcnt(4)\n\t"
    "v_smfmac_f32_16x16x64_bf16 v[116:119], v[120:123], v[84:91], v124\n\t"
    "ds_read_b64 v[84:85], v108 offset:0\n\t"
    "v_mfma_f32_16x16x32_bf16 v[80:83], v[64:67], v[212:215], 0\n\t"
    "ds_read_b64 v[86:87], v126 offset:0\n\t"
    "v_mfma_f32_16x16x32_bf16 v[80:83], v[68:71], v[216:219], v[80:83]\n\t"
    "v_add_u32_sdwa v90, s92, v109 dst_sel:DWORD dst_unused:UNUSED_PAD src0_sel:DWORD src1_sel:WORD_1\n\t"
    "v_add_u32_sdwa v109, s92, v109 dst_sel:DWORD dst_unused:UNUSED_PAD src0_sel:DWORD src1_sel:WORD_0\n\t"
    "ds_read_b64 v[88:89], v109 offset:0\n\t"
    "v_mfma_f32_16x16x32_bf16 v[80:83], v[72:75], v[148:151], v[80:83]\n\t"
    "ds_read_b64 v[90:91], v90 offset:0\n\t"
    "v_mfma_f32_16x16x32_bf16 v[80:83], v[76:79], v[152:155], v[80:83]\n\t"
    "v_add_u32 v79, s96, v127\n\t"
    "s_add_u32 s96, s96, 0x1000\n\t"
    "s_and_b32 s96, s96, 0x3000\n\t"
    "v_add_u32 v79, 128000, v79\n\t"
    "ds_read_b128 v[64:67], v79 offset:0\n\t"
    "ds_read_b128 v[68:71], v79 offset:1024\n\t"
    "ds_read_b128 v[72:75], v79 offset:2048\n\t"
    "ds_read_b128 v[76:79], v79 offset:3072\n\t"
    "v_add_u32_sdwa v126, s92, v110 dst_sel:DWORD dst_unused:UNUSED_PAD src0_sel:DWORD src1_sel:WORD_1\n\t"
    "v_add_u32_sdwa v110, s92, v110 dst_sel:DWORD dst_unused:UNUSED_PAD src0_sel:DWORD src1_sel:WORD_0\n\t"
    "s_waitcnt vmcnt(2) lgkmcnt(8)\n\t"
    "v_smfmac_f32_16x16x64_bf16 v[116:119], v[120:123], v[92:99], v124\n\t"
    "ds_read_b64 v[92:93], v110 offset:0\n\t"
    "ds_read_b64 v[94:95], v126 offset:0\n\t"
    "v_add_u32_sdwa v98, s92, v111 dst_sel:DWORD dst_unused:UNUSED_PAD src0_sel:DWORD src1_sel:WORD_1\n\t"
    "v_add_u32_sdwa v111, s92, v111 dst_sel:DWORD dst_unused:UNUSED_PAD src0_sel:DWORD src1_sel:WORD_0\n\t"
    "ds_read_b64 v[96:97], v111 offset:0\n\t"
    "ds_read_b64 v[98:99], v98 offset:0\n\t"
    "s_add_u32 s85, s85, s94\n\t"
    "buffer_load_dwordx4 v[108:111], v127, %12, s85 offen offset:0 sc1\n\t"
    "v_add_u32_sdwa v126, s92, v112 dst_sel:DWORD dst_unused:UNUSED_PAD src0_sel:DWORD src1_sel:WORD_1\n\t"
    "v_cvt_pk_bf16_f32 v54, v116, v117\n\t"
    "v_cvt_pk_bf16_f32 v55, v118, v119\n\t"
    "v_and_b32 v119, s67, v61\n\t"
    "v_lshlrev_b32 v118, 16, v61\n\t"
    "v_and_b32 v117, s67, v60\n\t"
    "v_lshlrev_b32 v116, 16, v60\n\t"
    "v_add_u32_sdwa v112, s92, v112 dst_sel:DWORD dst_unused:UNUSED_PAD src0_sel:DWORD src1_sel:WORD_0\n\t"
    "s_waitcnt lgkmcnt(8)\n\t"
    "v_smfmac_f32_16x16x64_bf16 v[116:119], v[120:123], v[84:91], v124\n\t"
    "ds_read_b64 v[84:85], v112 offset:0\n\t"
    "ds_read_b64 v[86:87], v126 offset:0\n\t"
    "v_cvt_pk_bf16_f32 v80, v80, v81\n\t"
    "v_cvt_pk_bf16_f32 v81, v82, v83\n\t"
    "ds_write_b64 v125, v[80:81] offset:872\n\t"
    "v_add_u32_sdwa v90, s92, v113 dst_sel:DWORD dst_unused:UNUSED_PAD src0_sel:DWORD src1_sel:WORD_1\n\t"
    "v_add_u32_sdwa v113, s92, v113 dst_sel:DWORD dst_unused:UNUSED_PAD src0_sel:DWORD src1_sel:WORD_0\n\t"
    "ds_read_b64 v[88:89], v113 offset:0\n\t"
    "ds_read_b64 v[90:91], v90 offset:0\n\t"
    "v_add_u32_sdwa v126, s92, v114 dst_sel:DWORD dst_unused:UNUSED_PAD src0_sel:DWORD src1_sel:WORD_1\n\t"
    "v_add_u32_sdwa v114, s92, v114 dst_sel:DWORD dst_unused:UNUSED_PAD src0_sel:DWORD src1_sel:WORD_0\n\t"
    "s_waitcnt vmcnt(2) lgkmcnt(5)\n\t"
    "v_smfmac_f32_16x16x64_bf16 v[116:119], v[120:123], v[92:99], v124\n\t"
    "ds_read_b64 v[92:93], v114 offset:0\n\t"
    "ds_read_b64 v[94:95], v126 offset:0\n\t"
    "v_add_u32_sdwa v98, s92, v115 dst_sel:DWORD dst_unused:UNUSED_PAD src0_sel:DWORD src1_sel:WORD_1\n\t"
    "v_add_u32_sdwa v115, s92, v115 dst_sel:DWORD dst_unused:UNUSED_PAD src0_sel:DWORD src1_sel:WORD_0\n\t"
    "ds_read_b64 v[96:97], v115 offset:0\n\t"
    "ds_read_b64 v[98:99], v98 offset:0\n\t"
    "buffer_load_dwordx4 v[112:115], v127, %12, s85 offen offset:1024 sc1\n\t"
    "v_add_u32_sdwa v126, s92, v100 dst_sel:DWORD dst_unused:UNUSED_PAD src0_sel:DWORD src1_sel:WORD_1\n\t"
    "v_cvt_pk_bf16_f32 v60, v116, v117\n\t"
    "v_cvt_pk_bf16_f32 v61, v118, v119\n\t"
    "v_and_b32 v119, s67, v63\n\t"
    "v_lshlrev_b32 v118, 16, v63\n\t"
    "v_and_b32 v117, s67, v62\n\t"
    "v_lshlrev_b32 v116, 16, v62\n\t"
    "v_add_u32_sdwa v100, s92, v100 dst_sel:DWORD dst_unused:UNUSED_PAD src0_sel:DWORD src1_sel:WORD_0\n\t"
    "s_waitcnt lgkmcnt(4)\n\t"
    "v_smfmac_f32_16x16x64_bf16 v[116:119], v[120:123], v[84:91], v124\n\t"
    "ds_read_b64 v[84:85], v100 offset:0\n\t"
    "ds_read_b64 v[86:87], v126 offset:0\n\t"
    "v_add_u32_sdwa v90, s92, v101 dst_sel:DWORD dst_unused:UNUSED_PAD src0_sel:DWORD src1_sel:WORD_1\n\t"
    "v_add_u32_sdwa v101, s92, v101 dst_sel:DWORD dst_unused:UNUSED_PAD src0_sel:DWORD src1_sel:WORD_0\n\t"
    "ds_read_b64 v[88:89], v101 offset:0\n\t"
    "ds_read_b64 v[90:91], v90 offset:0\n\t"
    "v_add_u32_sdwa v126, s92, v102 dst_sel:DWORD dst_unused:UNUSED_PAD src0_sel:DWORD src1_sel:WORD_1\n\t"
    "v_add_u32_sdwa v102, s92, v102 dst_sel:DWORD dst_unused:UNUSED_PAD src0_sel:DWORD src1_sel:WORD_0\n\t"
    "s_cmp_lt_u32 %16, 4\n\t"
    "s_cbranch_scc1 L_tp31_%=\n\t"
    "s_setprio 1\n\t"
    "L_tp31_%=:\n\t"
    "s_waitcnt vmcnt(2) lgkmcnt(4)\n\t"
    "v_smfmac_f32_16x16x64_bf16 v[116:119], v[120:123], v[92:99], v124\n\t"
    "ds_read_b64 v[92:93], v102 offset:0\n\t"
    "ds_read_b64 v[94:95], v126 offset:0\n\t"
    "v_add_u32_sdwa v98, s92, v103 dst_sel:DWORD dst_unused:UNUSED_PAD src0_sel:DWORD src1_sel:WORD_1\n\t"
    "v_add_u32_sdwa v103, s92, v103 dst_sel:DWORD dst_unused:UNUSED_PAD src0_sel:DWORD src1_sel:WORD_0\n\t"
    "ds_read_b64 v[96:97], v103 offset:0\n\t"
    "ds_read_b64 v[98:99], v98 offset:0\n\t"
    "s_add_u32 s86, s86, s94\n\t"
    "buffer_load_dwordx4 v[100:103], v127, %12, s86 offen offset:0 sc1\n\t"
    "v_add_u32_sdwa v126, s92, v104 dst_sel:DWORD dst_unused:UNUSED_PAD src0_sel:DWORD src1_sel:WORD_1\n\t"
    "v_cvt_pk_bf16_f32 v62, v116, v117\n\t"
    "v_cvt_pk_bf16_f32 v63, v118, v119\n\t"
    "v_and_b32 v119, s67, v1\n\t"
    "v_lshlrev_b32 v118, 16, v1\n\t"
    "v_and_b32 v117, s67, v0\n\t"
    "v_lshlrev_b32 v116, 16, v0\n\t"
    "v_add_u32_sdwa v104, s92, v104 dst_sel:DWORD dst_unused:UNUSED_PAD src0_sel:DWORD src1_sel:WORD_0\n\t"
    "s_waitcnt lgkmcnt(4)\n\t"
    "v_smfmac_f32_16x16x64_bf16 v[116:119], v[120:123], v[84:91], v124\n\t"
    "ds_read_b64 v[84:85], v104 offset:0\n\t"
    "v_mfma_f32_16x16x32_bf16 v[80:83], v[64:67], v[164:167], 0\n\t"
    "ds_read_b64 v[86:87], v126 offset:0\n\t"
    "v_mfma_f32_16x16x32_bf16 v[80:83], v[68:71], v[168:171], v[80:83]\n\t"
    "v_add_u32_sdwa v90, s92, v105 dst_sel:DWORD dst_unused:UNUSED_PAD src0_sel:DWORD src1_sel:WORD_1\n\t"
    "v_add_u32_sdwa v105, s92, v105 dst_sel:DWORD dst_unused:UNUSED_PAD src0_sel:DWORD src1_sel:WORD_0\n\t"
    "ds_read_b64 v[88:89], v105 offset:0\n\t"
    "v_mfma_f32_16x16x32_bf16 v[80:83], v[72:75], v[220:223], v[80:83]\n\t"
    "ds_read_b64 v[90:91], v90 offset:0\n\t"
    "v_mfma_f32_16x16x32_bf16 v[80:83], v[76:79], v[224:227], v[80:83]\n\t"
    "v_add_u32_sdwa v126, s92, v106 dst_sel:DWORD dst_unused:UNUSED_PAD src0_sel:DWORD src1_sel:WORD_1\n\t"
    "v_add_u32_sdwa v106, s92, v106 dst_sel:DWORD dst_unused:UNUSED_PAD src0_sel:DWORD src1_sel:WORD_0\n\t"
    "s_waitcnt vmcnt(2) lgkmcnt(4)\n\t"
    "v_smfmac_f32_16x16x64_bf16 v[116:119], v[120:123], v[92:99], v124\n\t"
    "ds_read_b64 v[92:93], v106 offset:0\n\t"
    "ds_read_b64 v[94:95], v126 offset:0\n\t"
    "v_add_u32_sdwa v98, s92, v107 dst_sel:DWORD dst_unused:UNUSED_PAD src0_sel:DWORD src1_sel:WORD_1\n\t"
    "v_add_u32_sdwa v107, s92, v107 dst_sel:DWORD dst_unused:UNUSED_PAD src0_sel:DWORD src1_sel:WORD_0\n\t"
    "ds_read_b64 v[96:97], v107 offset:0\n\t"
    "ds_read_b64 v[98:99], v98 offset:0\n\t"
    "buffer_load_dwordx4 v[104:107], v127, %12, s86 offen offset:1024 sc1\n\t"
    "v_add_u32_sdwa v126, s92, v108 dst_sel:DWORD dst_unused:UNUSED_PAD src0_sel:DWORD src1_sel:WORD_1\n\t"
    "v_cvt_pk_bf16_f32 v0, v116, v117\n\t"
    "v_cvt_pk_bf16_f32 v1, v118, v119\n\t"
    "v_and_b32 v119, s67, v3\n\t"
    "v_lshlrev_b32 v118, 16, v3\n\t"
    "v_and_b32 v117, s67, v2\n\t"
    "v_lshlrev_b32 v116, 16, v2\n\t"
    "v_add_u32_sdwa v108, s92, v108 dst_sel:DWORD dst_unused:UNUSED_PAD src0_sel:DWORD src1_sel:WORD_0\n\t"
    "s_waitcnt lgkmcnt(4)\n\t"
    "v_smfmac_f32_16x16x64_bf16 v[116:119], v[120:123], v[84:91], v124\n\t"
    "ds_read_b64 v[84:85], v108 offset:0\n\t"
    "ds_read_b64 v[86:87], v126 offset:0\n\t"
    "v_cvt_pk_bf16_f32 v80, v80, v81\n\t"
    "v_cvt_pk_bf16_f32 v81, v82, v83\n\t"
    "ds_write_b64 v125, v[80:81] offset:32000\n\t"
    "v_mfma_f32_16x16x32_bf16 v[80:83], v[64:67], v[172:175], 0\n\t"
    "v_add_u32_sdwa v90, s92, v109 dst_sel:DWORD dst_unused:UNUSED_PAD src0_sel:DWORD src1_sel:WORD_1\n\t"
    "v_add_u32_sdwa v109, s92, v109 dst_sel:DWORD dst_unused:UNUSED_PAD src0_sel:DWORD src1_sel:WORD_0\n\t"
    "ds_read_b64 v[88:89], v109 offset:0\n\t"
    "v_mfma_f32_16x16x32_bf16 v[80:83], v[68:71], v[176:179], v[80:83]\n\t"
    "ds_read_b64 v[90:91], v90 offset:0\n\t"
    "v_mfma_f32_16x16x32_bf16 v[80:83], v[72:75], v[228:231], v[80:83]\n\t"
    "v_mfma_f32_16x16x32_bf16 v[80:83], v[76:79], v[232:235], v[80:83]\n\t"
    "v_add_u32_sdwa v126, s92, v110 dst_sel:DWORD dst_unused:UNUSED_PAD src0_sel:DWORD src1_sel:WORD_1\n\t"
    "v_add_u32_sdwa v110, s92, v110 dst_sel:DWORD dst_unused:UNUSED_PAD src0_sel:DWORD src1_sel:WORD_0\n\t"
    "s_waitcnt vmcnt(2) lgkmcnt(5)\n\t"
    "v_smfmac_f32_16x16x64_bf16 v[116:119], v[120:123], v[92:99], v124\n\t"
    "ds_read_b64 v[92:93], v110 offset:0\n\t"
    "ds_read_b64 v[94:95], v126 offset:0\n\t"
    "v_add_u32_sdwa v98, s92, v111 dst_sel:DWORD dst_unused:UNUSED_PAD src0_sel:DWORD src1_sel:WORD_1\n\t"
    "v_add_u32_sdwa v111, s92, v111 dst_sel:DWORD dst_unused:UNUSED_PAD src0_sel:DWORD src1_sel:WORD_0\n\t"
    "ds_read_b64 v[96:97], v111 offset:0\n\t"
    "ds_read_b64 v[98:99], v98 offset:0\n\t"
    "s_add_u32 s87, s87, s94\n\t"
    "buffer_load_dwordx4 v[108:111], v127, %12, s87 offen offset:0 sc1\n\t"
    "v_add_u32_sdwa v126, s92, v112 dst_sel:DWORD dst_unused:UNUSED_PAD src0_sel:DWORD src1_sel:WORD_1\n\t"
    "v_cvt_pk_bf16_f32 v2, v116, v117\n\t"
    "v_cvt_pk_bf16_f32 v3, v118, v119\n\t"
    "v_and_b32 v119, s67, v9\n\t"
    "v_lshlrev_b32 v118, 16, v9\n\t"
    "v_and_b32 v117, s67, v8\n\t"
    "v_lshlrev_b32 v116, 16, v8\n\t"
    "v_add_u32_sdwa v112, s92, v112 dst_sel:DWORD dst_unused:UNUSED_PAD src0_sel:DWORD src1_sel:WORD_0\n\t"
    "s_waitcnt lgkmcnt(4)\n\t"
    "v_smfmac_f32_16x16x64_bf16 v[116:119], v[120:123], v[84:91], v124\n\t"
    "ds_read_b64 v[84:85], v112 offset:0\n\t"
    "ds_read_b64 v[86:87], v126 offset:0\n\t"
    "v_cvt_pk_bf16_f32 v80, v80, v81\n\t"
    "v_cvt_pk_bf16_f32 v81, v82, v83\n\t"
    "ds_write_b64 v125, v[80:81] offset:32128\n\t"
    "v_add_u32_sdwa v90, s92, v113 dst_sel:DWORD dst_unused:UNUSED_PAD src0_sel:DWORD src1_sel:WORD_1\n\t"
    "v_add_u32_sdwa v113, s92, v113 dst_sel:DWORD dst_unused:UNUSED_PAD src0_sel:DWORD src1_sel:WORD_0\n\t"
    "ds_read_b64 v[88:89], v113 offset:0\n\t"
    "ds_read_b64 v[90:91], v90 offset:0\n\t"
    "v_add_u32_sdwa v126, s92, v114 dst_sel:DWORD dst_unused:UNUSED_PAD src0_sel:DWORD src1_sel:WORD_1\n\t"
    "v_add_u32_sdwa v114, s92, v114 dst_sel:DWORD dst_unused:UNUSED_PAD src0_sel:DWORD src1_sel:WORD_0\n\t"
    "s_waitcnt vmcnt(2) lgkmcnt(5)\n\t"
    "v_smfmac_f32_16x16x64_bf16 v[116:119], v[120:123], v[92:99], v124\n\t"
    "ds_read_b64 v[92:93], v114 offset:0\n\t"
    "ds_read_b64 v[94:95], v126 offset:0\n\t"
    "v_add_u32_sdwa v98, s92, v115 dst_sel:DWORD dst_unused:UNUSED_PAD src0_sel:DWORD src1_sel:WORD_1\n\t"
    "v_add_u32_sdwa v115, s92, v115 dst_sel:DWORD dst_unused:UNUSED_PAD src0_sel:DWORD src1_sel:WORD_0\n\t"
    "ds_read_b64 v[96:97], v115 offset:0\n\t"
    "ds_read_b64 v[98:99], v98 offset:0\n\t"
    "buffer_load_dwordx4 v[112:115], v127, %12, s87 offen offset:1024 sc1\n\t"
    "v_add_u32_sdwa v126, s92, v100 dst_sel:DWORD dst_unused:UNUSED_PAD src0_sel:DWORD src1_sel:WORD_1\n\t"
    "v_cvt_pk_bf16_f32 v8, v116, v117\n\t"
    "v_cvt_pk_bf16_f32 v9, v118, v119\n\t"
    "v_and_b32 v119, s67, v11\n\t"
    "v_lshlrev_b32 v118, 16, v11\n\t"
    "v_and_b32 v117, s67, v10\n\t"
    "v_lshlrev_b32 v116, 16, v10\n\t"
    "v_add_u32_sdwa v100, s92, v100 dst_sel:DWORD dst_unused:UNUSED_PAD src0_sel:DWORD src1_sel:WORD_0\n\t"
    "s_waitcnt lgkmcnt(4)\n\t"
    "v_smfmac_f32_16x16x64_bf16 v[116:119], v[120:123], v[84:91], v124\n\t"
    "ds_read_b64 v[84:85], v100 offset:0\n\t"
    "v_mfma_f32_16x16x32_bf16 v[80:83], v[64:67], v[180:183], 0\n\t"
    "ds_read_b64 v[86:87], v126 offset:0\n\t"
    "v_mfma_f32_16x16x32_bf16 v[80:83], v[68:71], v[184:187], v[80:83]\n\t"
    "v_add_u32_sdwa v90, s92, v101 dst_sel:DWORD dst_unused:UNUSED_PAD src0_sel:DWORD src1_sel:WORD_1\n\t"
    "v_add_u32_sdwa v101, s92, v101 dst_sel:DWORD dst_unused:UNUSED_PAD src0_sel:DWORD src1_sel:WORD_0\n\t"
    "ds_read_b64 v[88:89], v101 offset:0\n\t"
    "v_mfma_f32_16x16x32_bf16 v[80:83], v[72:75], v[236:239], v[80:83]\n\t"
    "ds_read_b64 v[90:91], v90 offset:0\n\t"
    "v_mfma_f32_16x16x32_bf16 v[80:83], v[76:79], v[240:243], v[80:83]\n\t"
    "v_add_u32_sdwa v126, s92, v102 dst_sel:DWORD dst_unused:UNUSED_PAD src0_sel:DWORD src1_sel:WORD_1\n\t"
    "v_add_u32_sdwa v102, s92, v102 dst_sel:DWORD dst_unused:UNUSED_PAD src0_sel:DWORD src1_sel:WORD_0\n\t"
    "s_waitcnt vmcnt(2) lgkmcnt(4)\n\t"
    "v_smfmac_f32_16x16x64_bf16 v[116:119], v[120:123], v[92:99], v124\n\t"
    "ds_read_b64 v[92:93], v102 offset:0\n\t"
    "ds_read_b64 v[94:95], v126 offset:0\n\t"
    "v_add_u32_sdwa v98, s92, v103 dst_sel:DWORD dst_unused:UNUSED_PAD src0_sel:DWORD src1_sel:WORD_1\n\t"
    "v_add_u32_sdwa v103, s92, v103 dst_sel:DWORD dst_unused:UNUSED_PAD src0_sel:DWORD src1_sel:WORD_0\n\t"
    "ds_read_b64 v[96:97], v103 offset:0\n\t"
    "ds_read_b64 v[98:99], v98 offset:0\n\t"
    "s_add_u32 s88, s88, s94\n\t"
    "buffer_load_dwordx4 v[100:103], v127, %12, s88 offen offset:0 sc1\n\t"
    "v_add_u32_sdwa v126, s92, v104 dst_sel:DWORD dst_unused:UNUSED_PAD src0_sel:DWORD src1_sel:WORD_1\n\t"
    "v_cvt_pk_bf16_f32 v10, v116, v117\n\t"
    "v_cvt_pk_bf16_f32 v11, v118, v119\n\t"
    "v_and_b32 v119, s67, v17\n\t"
    "v_lshlrev_b32 v118, 16, v17\n\t"
    "v_and_b32 v117, s67, v16\n\t"
    "v_lshlrev_b32 v116, 16, v16\n\t"
    "v_add_u32_sdwa v104, s92, v104 dst_sel:DWORD dst_unused:UNUSED_PAD src0_sel:DWORD src1_sel:WORD_0\n\t"
    "s_waitcnt lgkmcnt(4)\n\t"
    "v_smfmac_f32_16x16x64_bf16 v[116:119], v[120:123], v[84:91], v124\n\t"
    "ds_read_b64 v[84:85], v104 offset:0\n\t"
    "ds_read_b64 v[86:87], v126 offset:0\n\t"
    "v_cvt_pk_bf16_f32 v80, v80, v81\n\t"
    "v_cvt_pk_bf16_f32 v81, v82, v83\n\t"
    "ds_write_b64 v125, v[80:81] offset:32256\n\t"
    "v_add_u32_sdwa v90, s92, v105 dst_sel:DWORD dst_unused:UNUSED_PAD src0_sel:DWORD src1_sel:WORD_1\n\t"
    "v_add_u32_sdwa v105, s92, v105 dst_sel:DWORD dst_unused:UNUSED_PAD src0_sel:DWORD src1_sel:WORD_0\n\t"
    "ds_read_b64 v[88:89], v105 offset:0\n\t"
    "ds_read_b64 v[90:91], v90 offset:0\n\t"
    "v_add_u32_sdwa v126, s92, v106 dst_sel:DWORD dst_unused:UNUSED_PAD src0_sel:DWORD src1_sel:WORD_1\n\t"
    "v_add_u32_sdwa v106, s92, v106 dst_sel:DWORD dst_unused:UNUSED_PAD src0_sel:DWORD src1_sel:WORD_0\n\t"
    "s_waitcnt vmcnt(2) lgkmcnt(5)\n\t"
    "v_smfmac_f32_16x16x64_bf16 v[116:119], v[120:123], v[92:99], v124\n\t"
    "ds_read_b64 v[92:93], v106 offset:0\n\t"
    "ds_read_b64 v[94:95], v126 offset:0\n\t"
    "v_add_u32_sdwa v98, s92, v107 dst_sel:DWORD dst_unused:UNUSED_PAD src0_sel:DWORD src1_sel:WORD_1\n\t"
    "v_add_u32_sdwa v107, s92, v107 dst_sel:DWORD dst_unused:UNUSED_PAD src0_sel:DWORD src1_sel:WORD_0\n\t"
    "ds_read_b64 v[96:97], v107 offset:0\n\t"
    "ds_read_b64 v[98:99], v98 offset:0\n\t"
    "buffer_load_dwordx4 v[104:107], v127, %12, s88 offen offset:1024 sc1\n\t"
    "v_add_u32_sdwa v126, s92, v108 dst_sel:DWORD dst_unused:UNUSED_PAD src0_sel:DWORD src1_sel:WORD_1\n\t"
    "v_cvt_pk_bf16_f32 v16, v116, v117\n\t"
    "v_cvt_pk_bf16_f32 v17, v118, v119\n\t"
    "v_and_b32 v119, s67, v19\n\t"
    "v_lshlrev_b32 v118, 16, v19\n\t"
    "v_and_b32 v117, s67, v18\n\t"
    "v_lshlrev_b32 v116, 16, v18\n\t"
    "v_add_u32_sdwa v108, s92, v108 dst_sel:DWORD dst_unused:UNUSED_PAD src0_sel:DWORD src1_sel:WORD_0\n\t"
    "s_waitcnt lgkmcnt(4)\n\t"
    "v_smfmac_f32_16x16x64_bf16 v[116:119], v[120:123], v[84:91], v124\n\t"
    "ds_read_b64 v[84:85], v108 offset:0\n\t"
    "v_mfma_f32_16x16x32_bf16 v[80:83], v[64:67], v[188:191], 0\n\t"
    "ds_read_b64 v[86:87], v126 offset:0\n\t"
    "v_mfma_f32_16x16x32_bf16 v[80:83], v[68:71], v[192:195], v[80:83]\n\t"
    "v_add_u32_sdwa v90, s92, v109 dst_sel:DWORD dst_unused:UNUSED_PAD src0_sel:DWORD src1_sel:WORD_1\n\t"
    "v_add_u32_sdwa v109, s92, v109 dst_sel:DWORD dst_unused:UNUSED_PAD src0_sel:DWORD src1_sel:WORD_0\n\t"
    "ds_read_b64 v[88:89], v109 offset:0\n\t"
    "v_mfma_f32_16x16x32_bf16 v[80:83], v[72:75], v[244:247], v[80:83]\n\t"
    "ds_read_b64 v[90:91], v90 offset:0\n\t"
    "v_mfma_f32_16x16x32_bf16 v[80:83], v[76:79], v[248:251], v[80:83]\n\t"
    "v_add_u32_sdwa v126, s92, v110 dst_sel:DWORD dst_unused:UNUSED_PAD src0_sel:DWORD src1_sel:WORD_1\n\t"
    "v_add_u32_sdwa v110, s92, v110 dst_sel:DWORD dst_unused:UNUSED_PAD src0_sel:DWORD src1_sel:WORD_0\n\t"
    "s_waitcnt vmcnt(2) lgkmcnt(4)\n\t"
    "v_smfmac_f32_16x16x64_bf16 v[116:119], v[120:123], v[92:99], v124\n\t"
    "ds_read_b64 v[92:93], v110 offset:0\n\t"
    "ds_read_b64 v[94:95], v126 offset:0\n\t"
    "v_add_u32_sdwa v98, s92, v111 dst_sel:DWORD dst_unused:UNUSED_PAD src0_sel:DWORD src1_sel:WORD_1\n\t"
    "v_add_u32_sdwa v111, s92, v111 dst_sel:DWORD dst_unused:UNUSED_PAD src0_sel:DWORD src1_sel:WORD_0\n\t"
    "ds_read_b64 v[96:97], v111 offset:0\n\t"
    "ds_read_b64 v[98:99], v98 offset:0\n\t"
    "s_add_u32 s89, s89, s94\n\t"
    "buffer_load_dwordx4 v[108:111], v127, %12, s89 offen offset:0 sc1\n\t"
    "v_add_u32_sdwa v126, s92, v112 dst_sel:DWORD dst_unused:UNUSED_PAD src0_sel:DWORD src1_sel:WORD_1\n\t"
    "v_cvt_pk_bf16_f32 v18, v116, v117\n\t"
    "v_cvt_pk_bf16_f32 v19, v118, v119\n\t"
    "v_and_b32 v119, s67, v25\n\t"
    "v_lshlrev_b32 v118, 16, v25\n\t"
    "v_and_b32 v117, s67, v24\n\t"
    "v_lshlrev_b32 v116, 16, v24\n\t"
    "v_add_u32_sdwa v112, s92, v112 dst_sel:DWORD dst_unused:UNUSED_PAD src0_sel:DWORD src1_sel:WORD_0\n\t"
    "s_waitcnt lgkmcnt(4)\n\t"
    "v_smfmac_f32_16x16x64_bf16 v[116:119], v[120:123], v[84:91], v124\n\t"
    "ds_read_b64 v[84:85], v112 offset:0\n\t"
    "ds_read_b64 v[86:87], v126 offset:0\n\t"
    "v_cvt_pk_bf16_f32 v80, v80, v81\n\t"
    "v_cvt_pk_bf16_f32 v81, v82, v83\n\t"
    "ds_write_b64 v125, v[80:81] offset:32384\n\t"
    "v_add_u32_sdwa v90, s92, v113 dst_sel:DWORD dst_unused:UNUSED_PAD src0_sel:DWORD src1_sel:WORD_1\n\t"
    "v_add_u32_sdwa v113, s92, v113 dst_sel:DWORD dst_unused:UNUSED_PAD src0_sel:DWORD src1_sel:WORD_0\n\t"
    "ds_read_b64 v[88:89], v113 offset:0\n\t"
    "ds_read_b64 v[90:91], v90 offset:0\n\t"
    "v_add_u32_sdwa v126, s92, v114 dst_sel:DWORD dst_unused:UNUSED_PAD src0_sel:DWORD src1_sel:WORD_1\n\t"
    "v_add_u32_sdwa v114, s92, v114 dst_sel:DWORD dst_unused:UNUSED_PAD src0_sel:DWORD src1_sel:WORD_0\n\t"
    "s_waitcnt vmcnt(2) lgkmcnt(5)\n\t"
    "v_smfmac_f32_16x16x64_bf16 v[116:119], v[120:123], v[92:99], v124\n\t"
    "ds_read_b64 v[92:93], v114 offset:0\n\t"
    "ds_read_b64 v[94:95], v126 offset:0\n\t"
    "v_add_u32_sdwa v98, s92, v115 dst_sel:DWORD dst_unused:UNUSED_PAD src0_sel:DWORD src1_sel:WORD_1\n\t"
    "v_add_u32_sdwa v115, s92, v115 dst_sel:DWORD dst_unused:UNUSED_PAD src0_sel:DWORD src1_sel:WORD_0\n\t"
    "ds_read_b64 v[96:97], v115 offset:0\n\t"
    "ds_read_b64 v[98:99], v98 offset:0\n\t"
    "buffer_load_dwordx4 v[112:115], v127, %12, s89 offen offset:1024 sc1\n\t"
    "v_add_u32_sdwa v126, s92, v100 dst_sel:DWORD dst_unused:UNUSED_PAD src0_sel:DWORD src1_sel:WORD_1\n\t"
    "v_cvt_pk_bf16_f32 v24, v116, v117\n\t"
    "v_cvt_pk_bf16_f32 v25, v118, v119\n\t"
    "v_and_b32 v119, s67, v27\n\t"
    "v_lshlrev_b32 v118, 16, v27\n\t"
    "v_and_b32 v117, s67, v26\n\t"
    "v_lshlrev_b32 v116, 16, v26\n\t"
    "v_add_u32_sdwa v100, s92, v100 dst_sel:DWORD dst_unused:UNUSED_PAD src0_sel:DWORD src1_sel:WORD_0\n\t"
    "s_waitcnt lgkmcnt(4)\n\t"
    "v_smfmac_f32_16x16x64_bf16 v[116:119], v[120:123], v[84:91], v124\n\t"
    "ds_read_b64 v[84:85], v100 offset:0\n\t"
    "v_mfma_f32_16x16x32_bf16 v[80:83], v[64:67], v[196:199], 0\n\t"
    "ds_read_b64 v[86:87], v126 offset:0\n\t"
    "v_mfma_f32_16x16x32_bf16 v[80:83], v[68:71], v[200:203], v[80:83]\n\t"
    "v_add_u32_sdwa v90, s92, v101 dst_sel:DWORD dst_unused:UNUSED_PAD src0_sel:DWORD src1_sel:WORD_1\n\t"
    "v_add_u32_sdwa v101, s92, v101 dst_sel:DWORD dst_unused:UNUSED_PAD src0_sel:DWORD src1_sel:WORD_0\n\t"
    "ds_read_b64 v[88:89], v101 offset:0\n\t"
    "v_mfma_f32_16x16x32_bf16 v[80:83], v[72:75], v[252:255], v[80:83]\n\t"
    "ds_read_b64 v[90:91], v90 offset:0\n\t"
    "v_mfma_f32_16x16x32_bf16 v[80:83], v[76:79], v[128:131], v[80:83]\n\t"
    "v_add_u32_sdwa v126, s92, v102 dst_sel:DWORD dst_unused:UNUSED_PAD src0_sel:DWORD src1_sel:WORD_1\n\t"
    "v_add_u32_sdwa v102, s92, v102 dst_sel:DWORD dst_unused:UNUSED_PAD src0_sel:DWORD src1_sel:WORD_0\n\t"
    "s_waitcnt vmcnt(2) lgkmcnt(4)\n\t"
    "v_smfmac_f32_16x16x64_bf16 v[116:119], v[120:123], v[92:99], v124\n\t"
    "ds_read_b64 v[92:93], v102 offset:0\n\t"
    "ds_read_b64 v[94:95], v126 offset:0\n\t"
    "v_add_u32_sdwa v98, s92, v103 dst_sel:DWORD dst_unused:UNUSED_PAD src0_sel:DWORD src1_sel:WORD_1\n\t"
    "v_add_u32_sdwa v103, s92, v103 dst_sel:DWORD dst_unused:UNUSED_PAD src0_sel:DWORD src1_sel:WORD_0\n\t"
    "ds_read_b64 v[96:97], v103 offset:0\n\t"
    "ds_read_b64 v[98:99], v98 offset:0\n\t"
    "s_add_u32 s90, s90, s94\n\t"
    "buffer_load_dwordx4 v[100:103], v127, %12, s90 offen offset:0 sc1\n\t"
    "v_add_u32_sdwa v126, s92, v104 dst_sel:DWORD dst_unused:UNUSED_PAD src0_sel:DWORD src1_sel:WORD_1\n\t"
    "v_cvt_pk_bf16_f32 v26, v116, v117\n\t"
    "v_cvt_pk_bf16_f32 v27, v118, v119\n\t"
    "v_and_b32 v119, s67, v33\n\t"
    "v_lshlrev_b32 v118, 16, v33\n\t"
    "v_and_b32 v117, s67, v32\n\t"
    "v_lshlrev_b32 v116, 16, v32\n\t"
    "v_add_u32_sdwa v104, s92, v104 dst_sel:DWORD dst_unused:UNUSED_PAD src0_sel:DWORD src1_sel:WORD_0\n\t"
    "s_waitcnt lgkmcnt(4)\n\t"
    "v_smfmac_f32_16x16x64_bf16 v[116:119], v[120:123], v[84:91], v124\n\t"
    "ds_read_b64 v[84:85], v104 offset:0\n\t"
    "ds_read_b64 v[86:87], v126 offset:0\n\t"
    "v_cvt_pk_bf16_f32 v80, v80, v81\n\t"
    "v_cvt_pk_bf16_f32 v81, v82, v83\n\t"
    "ds_write_b64 v125, v[80:81] offset:32512\n\t"
    "v_add_u32_sdwa v90, s92, v105 dst_sel:DWORD dst_unused:UNUSED_PAD src0_sel:DWORD src1_sel:WORD_1\n\t"
    "v_add_u32_sdwa v105, s92, v105 dst_sel:DWORD dst_unused:UNUSED_PAD src0_sel:DWORD src1_sel:WORD_0\n\t"
    "ds_read_b64 v[88:89], v105 offset:0\n\t"
    "ds_read_b64 v[90:91], v90 offset:0\n\t"
    "v_add_u32_sdwa v126, s92, v106 dst_sel:DWORD dst_unused:UNUSED_PAD src0_sel:DWORD src1_sel:WORD_1\n\t"
    "v_add_u32_sdwa v106, s92, v106 dst_sel:DWORD dst_unused:UNUSED_PAD src0_sel:DWORD src1_sel:WORD_0\n\t"
    "s_waitcnt vmcnt(2) lgkmcnt(5)\n\t"
    "v_smfmac_f32_16x16x64_bf16 v[116:119], v[120:123], v[92:99], v124\n\t"
    "ds_read_b64 v[92:93], v106 offset:0\n\t"
    "ds_read_b64 v[94:95], v126 offset:0\n\t"
    "v_add_u32_sdwa v98, s92, v107 dst_sel:DWORD dst_unused:UNUSED_PAD src0_sel:DWORD src1_sel:WORD_1\n\t"
    "v_add_u32_sdwa v107, s92, v107 dst_sel:DWORD dst_unused:UNUSED_PAD src0_sel:DWORD src1_sel:WORD_0\n\t"
    "ds_read_b64 v[96:97], v107 offset:0\n\t"
    "ds_read_b64 v[98:99], v98 offset:0\n\t"
    "buffer_load_dwordx4 v[104:107], v127, %12, s90 offen offset:1024 sc1\n\t"
    "v_add_u32_sdwa v126, s92, v108 dst_sel:DWORD dst_unused:UNUSED_PAD src0_sel:DWORD src1_sel:WORD_1\n\t"
    "v_cvt_pk_bf16_f32 v32, v116, v117\n\t"
    "v_cvt_pk_bf16_f32 v33, v118, v119\n\t"
    "v_and_b32 v119, s67, v35\n\t"
    "v_lshlrev_b32 v118, 16, v35\n\t"
    "v_and_b32 v117, s67, v34\n\t"
    "v_lshlrev_b32 v116, 16, v34\n\t"
    "v_add_u32_sdwa v108, s92, v108 dst_sel:DWORD dst_unused:UNUSED_PAD src0_sel:DWORD src1_sel:WORD_0\n\t"
    "s_waitcnt lgkmcnt(4)\n\t"
    "v_smfmac_f32_16x16x64_bf16 v[116:119], v[120:123], v[84:91], v124\n\t"
    "ds_read_b64 v[84:85], v108 offset:0\n\t"
    "v_mfma_f32_16x16x32_bf16 v[80:83], v[64:67], v[204:207], 0\n\t"
    "ds_read_b64 v[86:87], v126 offset:0\n\t"
    "v_mfma_f32_16x16x32_bf16 v[80:83], v[68:71], v[208:211], v[80:83]\n\t"
    "v_add_u32_sdwa v90, s92, v109 dst_sel:DWORD dst_unused:UNUSED_PAD src0_sel:DWORD src1_sel:WORD_1\n\t"
    "v_add_u32_sdwa v109, s92, v109 dst_sel:DWORD dst_unused:UNUSED_PAD src0_sel:DWORD src1_sel:WORD_0\n\t"
    "ds_read_b64 v[88:89], v109 offset:0\n\t"
    "v_mfma_f32_16x16x32_bf16 v[80:83], v[72:75], v[132:135], v[80:83]\n\t"
    "ds_read_b64 v[90:91], v90 offset:0\n\t"
    "v_mfma_f32_16x16x32_bf16 v[80:83], v[76:79], v[136:139], v[80:83]\n\t"
    "v_add_u32_sdwa v126, s92, v110 dst_sel:DWORD dst_unused:UNUSED_PAD src0_sel:DWORD src1_sel:WORD_1\n\t"
    "v_add_u32_sdwa v110, s92, v110 dst_sel:DWORD dst_unused:UNUSED_PAD src0_sel:DWORD src1_sel:WORD_0\n\t"
    "s_cmp_lt_u32 %16, 4\n\t"
    "s_cbranch_scc1 L_tp51_%=\n\t"
    "s_setprio 0\n\t"
    "L_tp51_%=:\n\t"
    "s_waitcnt vmcnt(2) lgkmcnt(4)\n\t"
    "v_smfmac_f32_16x16x64_bf16 v[116:119], v[120:123], v[92:99], v124\n\t"
    "ds_read_b64 v[92:93], v110 offset:0\n\t"
    "ds_read_b64 v[94:95], v126 offset:0\n\t"
    "v_add_u32_sdwa v98, s92, v111 dst_sel:DWORD dst_unused:UNUSED_PAD src0_sel:DWORD src1_sel:WORD_1\n\t"
    "v_add_u32_sdwa v111, s92, v111 dst_sel:DWORD dst_unused:UNUSED_PAD src0_sel:DWORD src1_sel:WORD_0\n\t"
    "ds_read_b64 v[96:97], v111 offset:0\n\t"
    "ds_read_b64 v[98:99], v98 offset:0\n\t"
    "s_add_u32 s91, s91, s94\n\t"
    "buffer_load_dwordx4 v[108:111], v127, %12, s91 offen offset:0 sc1\n\t"
    "v_add_u32_sdwa v126, s92, v112 dst_sel:DWORD dst_unused:UNUSED_PAD src0_sel:DWORD src1_sel:WORD_1\n\t"
    "v_cvt_pk_bf16_f32 v34, v116, v117\n\t"
    "v_cvt_pk_bf16_f32 v35, v118, v119\n\t"
    "v_and_b32 v119, s67, v41\n\t"
    "v_lshlrev_b32 v118, 16, v41\n\t"
    "v_and_b32 v117, s67, v40\n\t"
    "v_lshlrev_b32 v116, 16, v40\n\t"
    "v_add_u32_sdwa v112, s92, v112 dst_sel:DWORD dst_unused:UNUSED_PAD src0_sel:DWORD src1_sel:WORD_0\n\t"
    "s_waitcnt lgkmcnt(4)\n\t"
    "v_smfmac_f32_16x16x64_bf16 v[116:119], v[120:123], v[84:91], v124\n\t"
    "ds_read_b64 v[84:85], v112 offset:0\n\t"
    "ds_read_b64 v[86:87], v126 offset:0\n\t"
    "v_cvt_pk_bf16_f32 v80, v80, v81\n\t"
    "v_cvt_pk_bf16_f32 v81, v82, v83\n\t"
    "ds_write_b64 v125, v[80:81] offset:32640\n\t"
    "v_add_u32_sdwa v90, s92, v113 dst_sel:DWORD dst_unused:UNUSED_PAD src0_sel:DWORD src1_sel:WORD_1\n\t"
    "v_add_u32_sdwa v113, s92, v113 dst_sel:DWORD dst_unused:UNUSED_PAD src0_sel:DWORD src1_sel:WORD_0\n\t"
    "ds_read_b64 v[88:89], v113 offset:0\n\t"
    "ds_read_b64 v[90:91], v90 offset:0\n\t"
    "v_add_u32_sdwa v126, s92, v114 dst_sel:DWORD dst_unused:UNUSED_PAD src0_sel:DWORD src1_sel:WORD_1\n\t"
    "v_add_u32_sdwa v114, s92, v114 dst_sel:DWORD dst_unused:UNUSED_PAD src0_sel:DWORD src1_sel:WORD_0\n\t"
    "s_waitcnt vmcnt(2) lgkmcnt(5)\n\t"
    "v_smfmac_f32_16x16x64_bf16 v[116:119], v[120:123], v[92:99], v124\n\t"
    "ds_read_b64 v[92:93], v114 offset:0\n\t"
    "ds_read_b64 v[94:95], v126 offset:0\n\t"
    "v_add_u32_sdwa v98, s92, v115 dst_sel:DWORD dst_unused:UNUSED_PAD src0_sel:DWORD src1_sel:WORD_1\n\t"
    "v_add_u32_sdwa v115, s92, v115 dst_sel:DWORD dst_unused:UNUSED_PAD src0_sel:DWORD src1_sel:WORD_0\n\t"
    "ds_read_b64 v[96:97], v115 offset:0\n\t"
    "ds_read_b64 v[98:99], v98 offset:0\n\t"
    "buffer_load_dwordx4 v[112:115], v127, %12, s91 offen offset:1024 sc1\n\t"
    "v_add_u32_sdwa v126, s92, v100 dst_sel:DWORD dst_unused:UNUSED_PAD src0_sel:DWORD src1_sel:WORD_1\n\t"
    "v_cvt_pk_bf16_f32 v40, v116, v117\n\t"
    "v_cvt_pk_bf16_f32 v41, v118, v119\n\t"
    "v_and_b32 v119, s67, v43\n\t"
    "v_lshlrev_b32 v118, 16, v43\n\t"
    "v_and_b32 v117, s67, v42\n\t"
    "v_lshlrev_b32 v116, 16, v42\n\t"
    "v_add_u32_sdwa v100, s92, v100 dst_sel:DWORD dst_unused:UNUSED_PAD src0_sel:DWORD src1_sel:WORD_0\n\t"
    "s_waitcnt lgkmcnt(4)\n\t"
    "v_smfmac_f32_16x16x64_bf16 v[116:119], v[120:123], v[84:91], v124\n\t"
    "ds_read_b64 v[84:85], v100 offset:0\n\t"
    "v_mfma_f32_16x16x32_bf16 v[80:83], v[64:67], v[156:159], 0\n\t"
    "ds_read_b64 v[86:87], v126 offset:0\n\t"
    "v_mfma_f32_16x16x32_bf16 v[80:83], v[68:71], v[160:163], v[80:83]\n\t"
    "v_add_u32_sdwa v90, s92, v101 dst_sel:DWORD dst_unused:UNUSED_PAD src0_sel:DWORD src1_sel:WORD_1\n\t"
    "v_add_u32_sdwa v101, s92, v101 dst_sel:DWORD dst_unused:UNUSED_PAD src0_sel:DWORD src1_sel:WORD_0\n\t"
    "ds_read_b64 v[88:89], v101 offset:0\n\t"
    "v_mfma_f32_16x16x32_bf16 v[80:83], v[72:75], v[140:143], v[80:83]\n\t"
    "ds_read_b64 v[90:91], v90 offset:0\n\t"
    "v_mfma_f32_16x16x32_bf16 v[80:83], v[76:79], v[144:147], v[80:83]\n\t"
    "v_add_u32_sdwa v126, s92, v102 dst_sel:DWORD dst_unused:UNUSED_PAD src0_sel:DWORD src1_sel:WORD_1\n\t"
    "v_add_u32_sdwa v102, s92, v102 dst_sel:DWORD dst_unused:UNUSED_PAD src0_sel:DWORD src1_sel:WORD_0\n\t"
    "s_waitcnt vmcnt(2) lgkmcnt(4)\n\t"
    "v_smfmac_f32_16x16x64_bf16 v[116:119], v[120:123], v[92:99], v124\n\t"
    "ds_read_b64 v[92:93], v102 offset:0\n\t"
    "ds_read_b64 v[94:95], v126 offset:0\n\t"
    "v_add_u32_sdwa v98, s92, v103 dst_sel:DWORD dst_unused:UNUSED_PAD src0_sel:DWORD src1_sel:WORD_1\n\t"
    "v_add_u32_sdwa v103, s92, v103 dst_sel:DWORD dst_unused:UNUSED_PAD src0_sel:DWORD src1_sel:WORD_0\n\t"
    "ds_read_b64 v[96:97], v103 offset:0\n\t"
    "ds_read_b64 v[98:99], v98 offset:0\n\t"
    "s_add_u32 s84, s84, s95\n\t"
    "buffer_load_dwordx4 v[100:103], v127, %12, s84 offen offset:0 sc1\n\t"
    "v_add_u32_sdwa v126, s92, v104 dst_sel:DWORD dst_unused:UNUSED_PAD src0_sel:DWORD src1_sel:WORD_1\n\t"
    "v_cvt_pk_bf16_f32 v42, v116, v117\n\t"
    "v_cvt_pk_bf16_f32 v43, v118, v119\n\t"
    "v_and_b32 v119, s67, v49\n\t"
    "v_lshlrev_b32 v118, 16, v49\n\t"
    "v_and_b32 v117, s67, v48\n\t"
    "v_lshlrev_b32 v116, 16, v48\n\t"
    "v_add_u32_sdwa v104, s92, v104 dst_sel:DWORD dst_unused:UNUSED_PAD src0_sel:DWORD src1_sel:WORD_0\n\t"
    "s_waitcnt lgkmcnt(4)\n\t"
    "v_smfmac_f32_16x16x64_bf16 v[116:119], v[120:123], v[84:91], v124\n\t"
    "ds_read_b64 v[84:85], v104 offset:0\n\t"
    "ds_read_b64 v[86:87], v126 offset:0\n\t"
    "v_cvt_pk_bf16_f32 v80, v80, v81\n\t"
    "v_cvt_pk_bf16_f32 v81, v82, v83\n\t"
    "ds_write_b64 v125, v[80:81] offset:32768\n\t"
    "v_add_u32_sdwa v90, s92, v105 dst_sel:DWORD dst_unused:UNUSED_PAD src0_sel:DWORD src1_sel:WORD_1\n\t"
    "v_add_u32_sdwa v105, s92, v105 dst_sel:DWORD dst_unused:UNUSED_PAD src0_sel:DWORD src1_sel:WORD_0\n\t"
    "ds_read_b64 v[88:89], v105 offset:0\n\t"
    "ds_read_b64 v[90:91], v90 offset:0\n\t"
    "v_add_u32_sdwa v126, s92, v106 dst_sel:DWORD dst_unused:UNUSED_PAD src0_sel:DWORD src1_sel:WORD_1\n\t"
    "v_add_u32_sdwa v106, s92, v106 dst_sel:DWORD dst_unused:UNUSED_PAD src0_sel:DWORD src1_sel:WORD_0\n\t"
    "s_waitcnt vmcnt(2) lgkmcnt(5)\n\t"
    "v_smfmac_f32_16x16x64_bf16 v[116:119], v[120:123], v[92:99], v124\n\t"
    "ds_read_b64 v[92:93], v106 offset:0\n\t"
    "ds_read_b64 v[94:95], v126 offset:0\n\t"
    "v_add_u32_sdwa v98, s92, v107 dst_sel:DWORD dst_unused:UNUSED_PAD src0_sel:DWORD src1_sel:WORD_1\n\t"
    "v_add_u32_sdwa v107, s92, v107 dst_sel:DWORD dst_unused:UNUSED_PAD src0_sel:DWORD src1_sel:WORD_0\n\t"
    "ds_read_b64 v[96:97], v107 offset:0\n\t"
    "ds_read_b64 v[98:99], v98 offset:0\n\t"
    "buffer_load_dwordx4 v[104:107], v127, %12, s84 offen offset:1024 sc1\n\t"
    "v_add_u32_sdwa v126, s92, v108 dst_sel:DWORD dst_unused:UNUSED_PAD src0_sel:DWORD src1_sel:WORD_1\n\t"
    "v_cvt_pk_bf16_f32 v48, v116, v117\n\t"
    "v_cvt_pk_bf16_f32 v49, v118, v119\n\t"
    "v_and_b32 v119, s67, v51\n\t"
    "v_lshlrev_b32 v118, 16, v51\n\t"
    "v_and_b32 v117, s67, v50\n\t"
    "v_lshlrev_b32 v116, 16, v50\n\t"
    "v_add_u32_sdwa v108, s92, v108 dst_sel:DWORD dst_unused:UNUSED_PAD src0_sel:DWORD src1_sel:WORD_0\n\t"
    "s_waitcnt lgkmcnt(4)\n\t"
    "v_smfmac_f32_16x16x64_bf16 v[116:119], v[120:123], v[84:91], v124\n\t"
    "ds_read_b64 v[84:85], v108 offset:0\n\t"
    "v_mfma_f32_16x16x32_bf16 v[80:83], v[64:67], v[212:215], 0\n\t"
    "ds_read_b64 v[86:87], v126 offset:0\n\t"
    "v_mfma_f32_16x16x32_bf16 v[80:83], v[68:71], v[216:219], v[80:83]\n\t"
    "v_add_u32_sdwa v90, s92, v109 dst_sel:DWORD dst_unused:UNUSED_PAD src0_sel:DWORD src1_sel:WORD_1\n\t"
    "v_add_u32_sdwa v109, s92, v109 dst_sel:DWORD dst_unused:UNUSED_PAD src0_sel:DWORD src1_sel:WORD_0\n\t"
    "ds_read_b64 v[88:89], v109 offset:0\n\t"
    "v_mfma_f32_16x16x32_bf16 v[80:83], v[72:75], v[148:151], v[80:83]\n\t"
    "ds_read_b64 v[90:91], v90 offset:0\n\t"
    "v_mfma_f32_16x16x32_bf16 v[80:83], v[76:79], v[152:155], v[80:83]\n\t"
    "v_add_u32_sdwa v126, s92, v110 dst_sel:DWORD dst_unused:UNUSED_PAD src0_sel:DWORD src1_sel:WORD_1\n\t"
    "v_add_u32_sdwa v110, s92, v110 dst_sel:DWORD dst_unused:UNUSED_PAD src0_sel:DWORD src1_sel:WORD_0\n\t"
    "s_waitcnt vmcnt(2) lgkmcnt(4)\n\t"
    "v_smfmac_f32_16x16x64_bf16 v[116:119], v[120:123], v[92:99], v124\n\t"
    "ds_read_b64 v[92:93], v110 offset:0\n\t"
    "ds_read_b64 v[94:95], v126 offset:0\n\t"
    "v_add_u32_sdwa v98, s92, v111 dst_sel:DWORD dst_unused:UNUSED_PAD src0_sel:DWORD src1_sel:WORD_1\n\t"
    "v_add_u32_sdwa v111, s92, v111 dst_sel:DWORD dst_unused:UNUSED_PAD src0_sel:DWORD src1_sel:WORD_0\n\t"
    "ds_read_b64 v[96:97], v111 offset:0\n\t"
    "ds_read_b64 v[98:99], v98 offset:0\n\t"
    "s_add_u32 s85, s85, s95\n\t"
    "buffer_load_dwordx4 v[108:111], v127, %12, s85 offen offset:0 sc1\n\t"
    "v_add_u32_sdwa v126, s92, v112 dst_sel:DWORD dst_unused:UNUSED_PAD src0_sel:DWORD src1_sel:WORD_1\n\t"
    "v_cvt_pk_bf16_f32 v50, v116, v117\n\t"
    "v_cvt_pk_bf16_f32 v51, v118, v119\n\t"
    "v_and_b32 v119, s67, v57\n\t"
    "v_lshlrev_b32 v118, 16, v57\n\t"
    "v_and_b32 v117, s67, v56\n\t"
    "v_lshlrev_b32 v116, 16, v56\n\t"
    "v_add_u32_sdwa v112, s92, v112 dst_sel:DWORD dst_unused:UNUSED_PAD src0_sel:DWORD src1_sel:WORD_0\n\t"
    "s_waitcnt lgkmcnt(4)\n\t"
    "v_smfmac_f32_16x16x64_bf16 v[116:119], v[120:123], v[84:91], v124\n\t"
    "ds_read_b64 v[84:85], v112 offset:0\n\t"
    "ds_read_b64 v[86:87], v126 offset:0\n\t"
    "v_cvt_pk_bf16_f32 v80, v80, v81\n\t"
    "v_cvt_pk_bf16_f32 v81, v82, v83\n\t"
    "ds_write_b64 v125, v[80:81] offset:32872\n\t"
    "v_add_u32_sdwa v90, s92, v113 dst_sel:DWORD dst_unused:UNUSED_PAD src0_sel:DWORD src1_sel:WORD_1\n\t"
    "v_add_u32_sdwa v113, s92, v113 dst_sel:DWORD dst_unused:UNUSED_PAD src0_sel:DWORD src1_sel:WORD_0\n\t"
    "ds_read_b64 v[88:89], v113 offset:0\n\t"
    "ds_read_b64 v[90:91], v90 offset:0\n\t"
    "v_add_u32_sdwa v126, s92, v114 dst_sel:DWORD dst_unused:UNUSED_PAD src0_sel:DWORD src1_sel:WORD_1\n\t"
    "v_add_u32_sdwa v114, s92, v114 dst_sel:DWORD dst_unused:UNUSED_PAD src0_sel:DWORD src1_sel:WORD_0\n\t"
    "s_waitcnt vmcnt(2) lgkmcnt(5)\n\t"
    "v_smfmac_f32_16x16x64_bf16 v[116:119], v[120:123], v[92:99], v124\n\t"
    "ds_read_b64 v[92:93], v114 offset:0\n\t"
    "ds_read_b64 v[94:95], v126 offset:0\n\t"
    "v_add_u32_sdwa v98, s92, v115 dst_sel:DWORD dst_unused:UNUSED_PAD src0_sel:DWORD src1_sel:WORD_1\n\t"
    "v_add_u32_sdwa v115, s92, v115 dst_sel:DWORD dst_unused:UNUSED_PAD src0_sel:DWORD src1_sel:WORD_0\n\t"
    "ds_read_b64 v[96:97], v115 offset:0\n\t"
    "ds_read_b64 v[98:99], v98 offset:0\n\t"
    "buffer_load_dwordx4 v[112:115], v127, %12, s85 offen offset:1024 sc1\n\t"
    "s_sub_u32 s92, 64000, s92\n\t"
    "v_add_u32_sdwa v126, s92, v100 dst_sel:DWORD dst_unused:UNUSED_PAD src0_sel:DWORD src1_sel:WORD_1\n\t"
    "v_cvt_pk_bf16_f32 v56, v116, v117\n\t"
    "v_cvt_pk_bf16_f32 v57, v118, v119\n\t"
    "v_and_b32 v119, s67, v59\n\t"
    "v_lshlrev_b32 v118, 16, v59\n\t"
    "v_and_b32 v117, s67, v58\n\t"
    "v_lshlrev_b32 v116, 16, v58\n\t"
    "v_add_u32_sdwa v100, s92, v100 dst_sel:DWORD dst_unused:UNUSED_PAD src0_sel:DWORD src1_sel:WORD_0\n\t"
    "s_waitcnt lgkmcnt(0)\n\t"
    "s_barrier\n\t"
    "v_add_u32 v125, s93, v125\n\t"
    "s_sub_u32 s93, 0, s93\n\t"
    "v_smfmac_f32_16x16x64_bf16 v[116:119], v[120:123], v[84:91], v124\n\t"
    "ds_read_b64 v[84:85], v100 offset:0\n\t"
    "ds_read_b64 v[86:87], v126 offset:0\n\t"
    "v_add_u32_sdwa v90, s92, v101 dst_sel:DWORD dst_unused:UNUSED_PAD src0_sel:DWORD src1_sel:WORD_1\n\t"
    "v_add_u32_sdwa v101, s92, v101 dst_sel:DWORD dst_unused:UNUSED_PAD src0_sel:DWORD src1_sel:WORD_0\n\t"
    "ds_read_b64 v[88:89], v101 offset:0\n\t"
    "ds_read_b64 v[90:91], v90 offset:0\n\t"
    "v_add_u32_sdwa v126, s92, v102 dst_sel:DWORD dst_unused:UNUSED_PAD src0_sel:DWORD src1_sel:WORD_1\n\t"
    "v_add_u32_sdwa v102, s92, v102 dst_sel:DWORD dst_unused:UNUSED_PAD src0_sel:DWORD src1_sel:WORD_0\n\t"
    "s_cmp_lt_u32 %16, 4\n\t"
    "s_cbranch_scc1 L_tp63_%=\n\t"
    "s_setprio 1\n\t"
    "L_tp63_%=:\n\t"
    "s_waitcnt vmcnt(2)\n\t"
    "v_smfmac_f32_16x16x64_bf16 v[116:119], v[120:123], v[92:99], v124\n\t"
    "ds_read_b64 v[92:93], v102 offset:0\n\t"
    "ds_read_b64 v[94:95], v126 offset:0\n\t"
    "v_add_u32_sdwa v98, s92, v103 dst_sel:DWORD dst_unused:UNUSED_PAD src0_sel:DWORD src1_sel:WORD_1\n\t"
    "v_add_u32_sdwa v103, s92, v103 dst_sel:DWORD dst_unused:UNUSED_PAD src0_sel:DWORD src1_sel:WORD_0\n\t"
    "ds_read_b64 v[96:97], v103 offset:0\n\t"
    "ds_read_b64 v[98:99], v98 offset:0\n\t"
    "s_add_u32 s86, s86, s95\n\t"
    "buffer_load_dwordx4 v[100:103], v127, %12, s86 offen offset:0 sc1\n\t"
    "v_add_u32_sdwa v126, s92, v104 dst_sel:DWORD dst_unused:UNUSED_PAD src0_sel:DWORD src1_sel:WORD_1\n\t"
    "v_cvt_pk_bf16_f32 v58, v116, v117\n\t"
    "v_cvt_pk_bf16_f32 v59, v118, v119\n\t"
    "v_and_b32 v119, s67, v5\n\t"
    "v_lshlrev_b32 v118, 16, v5\n\t"
    "v_and_b32 v117, s67, v4\n\t"
    "v_lshlrev_b32 v116, 16, v4\n\t"
    "v_add_u32_sdwa v104, s92, v104 dst_sel:DWORD dst_unused:UNUSED_PAD src0_sel:DWORD src1_sel:WORD_0\n\t"
    "v_add_u32 v79, s96, v127\n\t"
    "s_add_u32 s96, s96, 0x1000\n\t"
    "s_and_b32 s96, s96, 0x3000\n\t"
    "v_add_u32 v79, 128000, v79\n\t"
    "ds_read_b128 v[64:67], v79 offset:0\n\t"
    "ds_read_b128 v[68:71], v79 offset:1024\n\t"
    "ds_read_b128 v[72:75], v79 offset:2048\n\t"
    "ds_read_b128 v[76:79], v79 offset:3072\n\t"
    "s_sub_u32 s66, s66, 1\n\t"
    "s_cmp_lg_u32 s66, 0\n\t"
    "s_cbranch_scc1 L_loop_%=\n\t"
    "s_cmp_eq_u32 %16, 0\n\t"
    "s_cbranch_scc1 L_pub_%=\n\t"
    "L_pubb_%=:\n\t"
    "s_mov_b32 s92, 0xffff\n\t"
    "s_waitcnt lgkmcnt(8)\n\t"
    "v_smfmac_f32_16x16x64_bf16 v[116:119], v[120:123], v[84:91], v124\n\t"
    "ds_read_b64 v[84:85], v104 offset:0\n\t"
    "ds_read_b64 v[86:87], v126 offset:0\n\t"
    "v_lshrrev_b32 v90, 16, v105\n\t"
    "v_and_b32 v105, s92, v105\n\t"
    "ds_read_b64 v[88:89], v105 offset:0\n\t"
    "ds_read_b64 v[90:91], v90 offset:0\n\t"
    "v_lshrrev_b32 v126, 16, v106\n\t"
    "v_and_b32 v106, s92, v106\n\t"
    "s_waitcnt vmcnt(2) lgkmcnt(8)\n\t"
    "v_smfmac_f32_16x16x64_bf16 v[116:119], v[120:123], v[92:99], v124\n\t"
    "ds_read_b64 v[92:93], v106 offset:0\n\t"
    "s_waitcnt lgkmcnt(5)\n\t"
    "v_mfma_f32_16x16x32_bf16 v[80:83], v[64:67], v[164:167], 0\n\t"
    "ds_read_b64 v[94:95], v126 offset:0\n\t"
    "v_mfma_f32_16x16x32_bf16 v[80:83], v[68:71], v[168:171], v[80:83]\n\t"
    "v_lshrrev_b32 v98, 16, v107\n\t"
    "v_and_b32 v107, s92, v107\n\t"
    "ds_read_b64 v[96:97], v107 offset:0\n\t"
    "v_mfma_f32_16x16x32_bf16 v[80:83], v[72:75], v[220:223], v[80:83]\n\t"
    "ds_read_b64 v[98:99], v98 offset:0\n\t"
    "v_mfma_f32_16x16x32_bf16 v[80:83], v[76:79], v[224:227], v[80:83]\n\t"
    "v_lshrrev_b32 v126, 16, v108\n\t"
    "v_cvt_pk_bf16_f32 v4, v116, v117\n\t"
    "v_cvt_pk_bf16_f32 v5, v118, v119\n\t"
    "v_and_b32 v119, s67, v7\n\t"
    "v_lshlrev_b32 v118, 16, v7\n\t"
    "v_and_b32 v117, s67, v6\n\t"
    "v_lshlrev_b32 v116, 16, v6\n\t"
    "v_and_b32 v108, s92, v108\n\t"
    "s_waitcnt lgkmcnt(4)\n\t"
    "v_smfmac_f32_16x16x64_bf16 v[116:119], v[120:123], v[84:91], v124\n\t"
    "ds_read_b64 v[84:85], v108 offset:0\n\t"
    "v_mfma_f32_16x16x32_bf16 v[104:107], v[64:67], v[172:175], 0\n\t"
    "ds_read_b64 v[86:87], v126 offset:0\n\t"
    "v_mfma_f32_16x16x32_bf16 v[104:107], v[68:71], v[176:179], v[104:107]\n\t"
    "v_lshrrev_b32 v90, 16, v109\n\t"
    "v_and_b32 v109, s92, v109\n\t"
    "ds_read_b64 v[88:89], v109 offset:0\n\t"
    "v_mfma_f32_16x16x32_bf16 v[104:107], v[72:75], v[228:231], v[104:107]\n\t"
    "ds_read_b64 v[90:91], v90 offset:0\n\t"
    "v_mfma_f32_16x16x32_bf16 v[104:107], v[76:79], v[232:235], v[104:107]\n\t"
    "v_lshrrev_b32 v126, 16, v110\n\t"
    "v_cvt_pk_bf16_f32 v80, v80, v81\n\t"
    "v_cvt_pk_bf16_f32 v81, v82, v83\n\t"
    "ds_write_b64 v125, v[80:81] offset:0\n\t"
    "v_mfma_f32_16x16x32_bf16 v[80:83], v[64:67], v[180:183], 0\n\t"
    "v_and_b32 v110, s92, v110\n\t"
    "v_mfma_f32_16x16x32_bf16 v[80:83], v[68:71], v[184:187], v[80:83]\n\t"
    "s_waitcnt vmcnt(1) lgkmcnt(5)\n\t"
    "v_smfmac_f32_16x16x64_bf16 v[116:119], v[120:123], v[92:99], v124\n\t"
    "ds_read_b64 v[92:93], v110 offset:0\n\t"
    "v_mfma_f32_16x16x32_bf16 v[80:83], v[72:75], v[236:239], v[80:83]\n\t"
    "ds_read_b64 v[94:95], v126 offset:0\n\t"
    "v_mfma_f32_16x16x32_bf16 v[80:83], v[76:79], v[240:243], v[80:83]\n\t"
    "v_lshrrev_b32 v98, 16, v111\n\t"
    "v_and_b32 v111, s92, v111\n\t"
    "ds_read_b64 v[96:97], v111 offset:0\n\t"
    "v_cvt_pk_bf16_f32 v104, v104, v105\n\t"
    "v_cvt_pk_bf16_f32 v105, v106, v107\n\t"
    "ds_write_b64 v125, v[104:105] offset:128\n\t"
    "v_mfma_f32_16x16x32_bf16 v[104:107], v[64:67], v[188:191], 0\n\t"
    "ds_read_b64 v[98:99], v98 offset:0\n\t"
    "v_mfma_f32_16x16x32_bf16 v[104:107], v[68:71], v[192:195], v[104:107]\n\t"
    "buffer_load_dwordx4 v[108:111], v127, %12, s86 offen offset:1024 sc1\n\t"
    "v_lshrrev_b32 v126, 16, v112\n\t"
    "v_mfma_f32_16x16x32_bf16 v[104:107], v[72:75], v[244:247], v[104:107]\n\t"
    "v_cvt_pk_bf16_f32 v6, v116, v117\n\t"
    "v_cvt_pk_bf16_f32 v7, v118, v119\n\t"
    "v_and_b32 v119, s67, v13\n\t"
    "v_lshlrev_b32 v118, 16, v13\n\t"
    "v_and_b32 v117, s67, v12\n\t"
    "v_lshlrev_b32 v116, 16, v12\n\t"
    "v_and_b32 v112, s92, v112\n\t"
    "s_waitcnt lgkmcnt(6)\n\t"
    "v_smfmac_f32_16x16x64_bf16 v[116:119], v[120:123], v[84:91], v124\n\t"
    "ds_read_b64 v[84:85], v112 offset:0\n\t"
    "v_mfma_f32_16x16x32_bf16 v[104:107], v[76:79], v[248:251], v[104:107]\n\t"
    "ds_read_b64 v[86:87], v126 offset:0\n\t"
    "v_cvt_pk_bf16_f32 v80, v80, v81\n\t"
    "v_cvt_pk_bf16_f32 v81, v82, v83\n\t"
    "ds_write_b64 v125, v[80:81] offset:256\n\t"
    "v_mfma_f32_16x16x32_bf16 v[80:83], v[64:67], v[196:199], 0\n\t"
    "v_lshrrev_b32 v90, 16, v113\n\t"
    "v_and_b32 v113, s92, v113\n\t"
    "ds_read_b64 v[88:89], v113 offset:0\n\t"
    "v_mfma_f32_16x16x32_bf16 v[80:83], v[68:71], v[200:203], v[80:83]\n\t"
    "ds_read_b64 v[90:91], v90 offset:0\n\t"
    "v_mfma_f32_16x16x32_bf16 v[80:83], v[72:75], v[252:255], v[80:83]\n\t"
    "v_lshrrev_b32 v126, 16, v114\n\t"
    "v_mfma_f32_16x16x32_bf16 v[80:83], v[76:79], v[128:131], v[80:83]\n\t"
    "v_and_b32 v114, s92, v114\n\t"
    "v_cvt_pk_bf16_f32 v104, v104, v105\n\t"
    "v_cvt_pk_bf16_f32 v105, v106, v107\n\t"
    "ds_write_b64 v125, v[104:105] offset:384\n\t"
    "v_mfma_f32_16x16x32_bf16 v[104:107], v[64:67], v[204:207], 0\n\t"
    "s_waitcnt vmcnt(1) lgkmcnt(6)\n\t"
    "v_smfmac_f32_16x16x64_bf16 v[116:119], v[120:123], v[92:99], v124\n\t"
    "ds_read_b64 v[92:93], v114 offset:0\n\t"
    "v_mfma_f32_16x16x32_bf16 v[104:107], v[68:71], v[208:211], v[104:107]\n\t"
    "ds_read_b64 v[94:95], v126 offset:0\n\t"
    "v_mfma_f32_16x16x32_bf16 v[104:107], v[72:75], v[132:135], v[104:107]\n\t"
    "v_lshrrev_b32 v98, 16, v115\n\t"
    "v_and_b32 v115, s92, v115\n\t"
    "ds_read_b64 v[96:97], v115 offset:0\n\t"
    "v_mfma_f32_16x16x32_bf16 v[104:107], v[76:79], v[136:139], v[104:107]\n\t"
    "ds_read_b64 v[98:99], v98 offset:0\n\t"
    "v_cvt_pk_bf16_f32 v80, v80, v81\n\t"
    "v_cvt_pk_bf16_f32 v81, v82, v83\n\t"
    "ds_write_b64 v125, v[80:81] offset:512\n\t"
    "v_mfma_f32_16x16x32_bf16 v[80:83], v[64:67], v[156:159], 0\n\t"
    "s_add_u32 s87, s87, s95\n\t"
    "buffer_load_dwordx4 v[112:115], v127, %12, s87 offen offset:0 sc1\n\t"
    "v_lshrrev_b32 v126, 16, v100\n\t"
    "v_mfma_f32_16x16x32_bf16 v[80:83], v[68:71], v[160:163], v[80:83]\n\t"
    "v_cvt_pk_bf16_f32 v12, v116, v117\n\t"
    "v_cvt_pk_bf16_f32 v13, v118, v119\n\t"
    "v_and_b32 v119, s67, v15\n\t"
    "v_lshlrev_b32 v118, 16, v15\n\t"
    "v_and_b32 v117, s67, v14\n\t"
    "v_lshlrev_b32 v116, 16, v14\n\t"
    "v_and_b32 v100, s92, v100\n\t"
    "s_waitcnt lgkmcnt(6)\n\t"
    "v_smfmac_f32_16x16x64_bf16 v[116:119], v[120:123], v[84:91], v124\n\t"
    "ds_read_b64 v[84:85], v100 offset:0\n\t"
    "v_mfma_f32_16x16x32_bf16 v[80:83], v[72:75], v[140:143], v[80:83]\n\t"
    "ds_read_b64 v[86:87], v126 offset:0\n\t"
    "v_mfma_f32_16x16x32_bf16 v[80:83], v[76:79], v[144:147], v[80:83]\n\t"
    "v_lshrrev_b32 v90, 16, v101\n\t"
    "v_and_b32 v101, s92, v101\n\t"
    "ds_read_b64 v[88:89], v101 offset:0\n\t"
    "v_cvt_pk_bf16_f32 v104, v104, v105\n\t"
    "v_cvt_pk_bf16_f32 v105, v106, v107\n\t"
    "ds_write_b64 v125, v[104:105] offset:640\n\t"
    "v_mfma_f32_16x16x32_bf16 v[104:107], v[64:67], v[212:215], 0\n\t"
    "ds_read_b64 v[90:91], v90 offset:0\n\t"
    "v_mfma_f32_16x16x32_bf16 v[104:107], v[68:71], v[216:219], v[104:107]\n\t"
    "v_lshrrev_b32 v126, 16, v102\n\t"
    "v_mfma_f32_16x16x32_bf16 v[104:107], v[72:75], v[148:151], v[104:107]\n\t"
    "v_and_b32 v102, s92, v102\n\t"
    "v_mfma_f32_16x16x32_bf16 v[104:107], v[76:79], v[152:155], v[104:107]\n\t"
    "v_add_u32 v79, s96, v127\n\t"
    "s_add_u32 s96, s96, 0x1000\n\t"
    "s_and_b32 s96, s96, 0x3000\n\t"
    "v_add_u32 v79, 128000, v79\n\t"
    "ds_read_b128 v[64:67], v79 offset:0\n\t"
    "ds_read_b128 v[68:71], v79 offset:1024\n\t"
    "ds_read_b128 v[72:75], v79 offset:2048\n\t"
    "ds_read_b128 v[76:79], v79 offset:3072\n\t"
    "v_cvt_pk_bf16_f32 v80, v80, v81\n\t"
    "v_cvt_pk_bf16_f32 v81, v82, v83\n\t"
    "ds_write_b64 v125, v[80:81] offset:768\n\t"
    "s_waitcnt vmcnt(1) lgkmcnt(11)\n\t"
    "v_smfmac_f32_16x16x64_bf16 v[116:119], v[120:123], v[92:99], v124\n\t"
    "ds_read_b64 v[92:93], v102 offset:0\n\t"
    "s_waitcnt lgkmcnt(2)\n\t"
    "v_mfma_f32_16x16x32_bf16 v[80:83], v[64:67], v[164:167], 0\n\t"
    "ds_read_b64 v[94:95], v126 offset:0\n\t"
    "v_mfma_f32_16x16x32_bf16 v[80:83], v[68:71], v[168:171], v[80:83]\n\t"
    "v_lshrrev_b32 v98, 16, v103\n\t"
    "v_and_b32 v103, s92, v103\n\t"
    "ds_read_b64 v[96:97], v103 offset:0\n\t"
    "v_mfma_f32_16x16x32_bf16 v[80:83], v[72:75], v[220:223], v[80:83]\n\t"
    "ds_read_b64 v[98:99], v98 offset:0\n\t"
    "buffer_load_dwordx4 v[100:103], v127, %12, s87 offen offset:1024 sc1\n\t"
    "v_lshrrev_b32 v126, 16, v108\n\t"
    "v_cvt_pk_bf16_f32 v14, v116, v117\n\t"
    "v_cvt_pk_bf16_f32 v15, v118, v119\n\t"
    "v_and_b32 v119, s67, v21\n\t"
    "v_lshlrev_b32 v118, 16, v21\n\t"
    "v_and_b32 v117, s67, v20\n\t"
    "v_lshlrev_b32 v116, 16, v20\n\t"
    "v_and_b32 v108, s92, v108\n\t"
    "s_nop 0\n\t"
    "v_smfmac_f32_16x16x64_bf16 v[116:119], v[120:123], v[84:91], v124\n\t"
    "ds_read_b64 v[84:85], v108 offset:0\n\t"
    "v_mfma_f32_16x16x32_bf16 v[80:83], v[76:79], v[224:227], v[80:83]\n\t"
    "ds_read_b64 v[86:87], v126 offset:0\n\t"
    "v_cvt_pk_bf16_f32 v104, v104, v105\n\t"
    "v_cvt_pk_bf16_f32 v105, v106, v107\n\t"
    "ds_write_b64 v125, v[104:105] offset:872\n\t"
    "v_mfma_f32_16x16x32_bf16 v[104:107], v[64:67], v[172:175], 0\n\t"
    "v_lshrrev_b32 v90, 16, v109\n\t"
    "v_and_b32 v109, s92, v109\n\t"
    "ds_read_b64 v[88:89], v109 offset:0\n\t"
    "v_mfma_f32_16x16x32_bf16 v[104:107], v[68:71], v[176:179], v[104:107]\n\t"
    "ds_read_b64 v[90:91], v90 offset:0\n\t"
    "v_mfma_f32_16x16x32_bf16 v[104:107], v[72:75], v[228:231], v[104:107]\n\t"
    "v_lshrrev_b32 v126, 16, v110\n\t"
    "v_mfma_f32_16x16x32_bf16 v[104:107], v[76:79], v[232:235], v[104:107]\n\t"
    "v_and_b32 v110, s92, v110\n\t"
    "v_cvt_pk_bf16_f32 v80, v80, v81\n\t"
    "v_cvt_pk_bf16_f32 v81, v82, v83\n\t"
    "ds_write_b64 v125, v[80:81] offset:32000\n\t"
    "v_mfma_f32_16x16x32_bf16 v[80:83], v[64:67], v[180:183], 0\n\t"
    "s_waitcnt vmcnt(1) lgkmcnt(6)\n\t"
    "v_smfmac_f32_16x16x64_bf16 v[116:119], v[120:123], v[92:99], v124\n\t"
    "ds_read_b64 v[92:93], v110 offset:0\n\t"
    "v_mfma_f32_16x16x32_bf16 v[80:83], v[68:71], v[184:187], v[80:83]\n\t"
    "ds_read_b64 v[94:95], v126 offset:0\n\t"
    "v_mfma_f32_16x16x32_bf16 v[80:83], v[72:75], v[236:239], v[80:83]\n\t"
    "v_lshrrev_b32 v98, 16, v111\n\t"
    "v_and_b32 v111, s92, v111\n\t"
    "ds_read_b64 v[96:97], v111 offset:0\n\t"
    "v_mfma_f32_16x16x32_bf16 v[80:83], v[76:79], v[240:243], v[80:83]\n\t"
    "ds_read_b64 v[98:99], v98 offset:0\n\t"
    "v_cvt_pk_bf16_f32 v104, v104, v105\n\t"
    "v_cvt_pk_bf16_f32 v105, v106, v107\n\t"
    "ds_write_b64 v125, v[104:105] offset:32128\n\t"
    "v_mfma_f32_16x16x32_bf16 v[104:107], v[64:67], v[188:191], 0\n\t"
    "s_add_u32 s84, s84, 0x8000\n\t"
    "buffer_load_dwordx4 v[108:111], v127, %12, s84 offen offset:0 sc1\n\t"
    "v_lshrrev_b32 v126, 16, v112\n\t"
    "v_mfma_f32_16x16x32_bf16 v[104:107], v[68:71], v[192:195], v[104:107]\n\t"
    "v_cvt_pk_bf16_f32 v20, v116, v117\n\t"
    "v_cvt_pk_bf16_f32 v21, v118, v119\n\t"
    "v_and_b32 v119, s67, v23\n\t"
    "v_lshlrev_b32 v118, 16, v23\n\t"
    "v_and_b32 v117, s67, v22\n\t"
    "v_lshlrev_b32 v116, 16, v22\n\t"
    "v_and_b32 v112, s92, v112\n\t"
    "s_waitcnt lgkmcnt(6)\n\t"
    "v_smfmac_f32_16x16x64_bf16 v[116:119], v[120:123], v[84:91], v124\n\t"
    "ds_read_b64 v[84:85], v112 offset:0\n\t"
    "v_mfma_f32_16x16x32_bf16 v[104:107], v[72:75], v[244:247], v[104:107]\n\t"
    "ds_read_b64 v[86:87], v126 offset:0\n\t"
    "v_mfma_f32_16x16x32_bf16 v[104:107], v[76:79], v[248:251], v[104:107]\n\t"
    "v_lshrrev_b32 v90, 16, v113\n\t"
    "v_and_b32 v113, s92, v113\n\t"
    "ds_read_b64 v[88:89], v113 offset:0\n\t"
    "v_cvt_pk_bf16_f32 v80, v80, v81\n\t"
    "v_cvt_pk_bf16_f32 v81, v82, v83\n\t"
    "ds_write_b64 v125, v[80:81] offset:32256\n\t"
    "v_mfma_f32_16x16x32_bf16 v[80:83], v[64:67], v[196:199], 0\n\t"
    "ds_read_b64 v[90:91], v90 offset:0\n\t"
    "v_mfma_f32_16x16x32_bf16 v[80:83], v[68:71], v[200:203], v[80:83]\n\t"
    "v_lshrrev_b32 v126, 16, v114\n\t"
    "v_mfma_f32_16x16x32_bf16 v[80:83], v[72:75], v[252:255], v[80:83]\n\t"
    "v_and_b32 v114, s92, v114\n\t"
    "v_mfma_f32_16x16x32_bf16 v[80:83], v[76:79], v[128:131], v[80:83]\n\t"
    "v_cvt_pk_bf16_f32 v104, v104, v105\n\t"
    "v_cvt_pk_bf16_f32 v105, v106, v107\n\t"
    "ds_write_b64 v125, v[104:105] offset:32384\n\t"
    "s_waitcnt vmcnt(1) lgkmcnt(7)\n\t"
    "v_smfmac_f32_16x16x64_bf16 v[116:119], v[120:123], v[92:99], v124\n\t"
    "ds_read_b64 v[92:93], v114 offset:0\n\t"
    "v_mfma_f32_16x16x32_bf16 v[104:107], v[64:67], v[204:207], 0\n\t"
    "ds_read_b64 v[94:95], v126 offset:0\n\t"
    "v_mfma_f32_16x16x32_bf16 v[104:107], v[68:71], v[208:211], v[104:107]\n\t"
    "v_lshrrev_b32 v98, 16, v115\n\t"
    "v_and_b32 v115, s92, v115\n\t"
    "ds_read_b64 v[96:97], v115 offset:0\n\t"
    "v_mfma_f32_16x16x32_bf16 v[104:107], v[72:75], v[132:135], v[104:107]\n\t"
    "ds_read_b64 v[98:99], v98 offset:0\n\t"
    "v_mfma_f32_16x16x32_bf16 v[104:107], v[76:79], v[136:139], v[104:107]\n\t"
    "buffer_load_dwordx4 v[112:115], v127, %12, s84 offen offset:1024 sc1\n\t"
    "v_lshrrev_b32 v126, 16, v100\n\t"
    "v_cvt_pk_bf16_f32 v80, v80, v81\n\t"
    "v_cvt_pk_bf16_f32 v81, v82, v83\n\t"
    "ds_write_b64 v125, v[80:81] offset:32512\n\t"
    "v_mfma_f32_16x16x32_bf16 v[80:83], v[64:67], v[156:159], 0\n\t"
    "v_cvt_pk_bf16_f32 v22, v116, v117\n\t"
    "v_cvt_pk_bf16_f32 v23, v118, v119\n\t"
    "v_and_b32 v119, s67, v29\n\t"
    "v_lshlrev_b32 v118, 16, v29\n\t"
    "v_and_b32 v117, s67, v28\n\t"
    "v_lshlrev_b32 v116, 16, v28\n\t"
    "v_and_b32 v100, s92, v100\n\t"
    "s_waitcnt lgkmcnt(6)\n\t"
    "v_smfmac_f32_16x16x64_bf16 v[116:119], v[120:123], v[84:91], v124\n\t"
    "ds_read_b64 v[84:85], v100 offset:0\n\t"
    "v_mfma_f32_16x16x32_bf16 v[80:83], v[68:71], v[160:163], v[80:83]\n\t"
    "ds_read_b64 v[86:87], v126 offset:0\n\t"
    "v_mfma_f32_16x16x32_bf16 v[80:83], v[72:75], v[140:143], v[80:83]\n\t"
    "v_lshrrev_b32 v90, 16, v101\n\t"
    "v_and_b32 v101, s92, v101\n\t"
    "ds_read_b64 v[88:89], v101 offset:0\n\t"
    "v_mfma_f32_16x16x32_bf16 v[80:83], v[76:79], v[144:147], v[80:83]\n\t"
    "ds_read_b64 v[90:91], v90 offset:0\n\t"
    "v_cvt_pk_bf16_f32 v104, v104, v105\n\t"
    "v_cvt_pk_bf16_f32 v105, v106, v107\n\t"
    "ds_write_b64 v125, v[104:105] offset:32640\n\t"
    "v_mfma_f32_16x16x32_bf16 v[104:107], v[64:67], v[212:215], 0\n\t"
    "v_lshrrev_b32 v126, 16, v102\n\t"
    "v_mfma_f32_16x16x32_bf16 v[104:107], v[68:71], v[216:219], v[104:107]\n\t"
    "v_and_b32 v102, s92, v102\n\t"
    "v_mfma_f32_16x16x32_bf16 v[104:107], v[72:75], v[148:151], v[104:107]\n\t"
    "s_waitcnt vmcnt(1) lgkmcnt(6)\n\t"
    "v_smfmac_f32_16x16x64_bf16 v[116:119], v[120:123], v[92:99], v124\n\t"
    "ds_read_b64 v[92:93], v102 offset:0\n\t"
    "v_mfma_f32_16x16x32_bf16 v[104:107], v[76:79], v[152:155], v[104:107]\n\t"
    "ds_read_b64 v[94:95], v126 offset:0\n\t"
    "v_cvt_pk_bf16_f32 v80, v80, v81\n\t"
    "v_cvt_pk_bf16_f32 v81, v82, v83\n\t"
    "ds_write_b64 v125, v[80:81] offset:32768\n\t"
    "s_nop 3\n\t"
    "v_cvt_pk_bf16_f32 v104, v104, v105\n\t"
    "v_cvt_pk_bf16_f32 v105, v106, v107\n\t"
    "ds_write_b64 v125, v[104:105] offset:32872\n\t"
    "v_lshrrev_b32 v98, 16, v103\n\t"
    "v_and_b32 v103, s92, v103\n\t"
    "ds_read_b64 v[96:97], v103 offset:0\n\t"
    "ds_read_b64 v[98:99], v98 offset:0\n\t"
    "s_add_u32 s85, s85, 0x8000\n\t"
    "buffer_load_dwordx4 v[100:103], v127, %12, s85 offen offset:0 sc1\n\t"
    "v_lshrrev_b32 v126, 16, v108\n\t"
    "v_cvt_pk_bf16_f32 v28, v116, v117\n\t"
    "v_cvt_pk_bf16_f32 v29, v118, v119\n\t"
    "v_and_b32 v119, s67, v31\n\t"
    "v_lshlrev_b32 v118, 16, v31\n\t"
    "v_and_b32 v117, s67, v30\n\t"
    "v_lshlrev_b32 v116, 16, v30\n\t"
    "v_and_b32 v108, s92, v108\n\t"
    "v_mov_b32 v155, 138896\n\t"
    "v_mov_b32 v144, 0\n\t"
    "v_mov_b32 v145, 0\n\t"
    "ds_write_b64 v155, v[144:145]\n\t"
    "s_waitcnt lgkmcnt(0)\n\t"
    "s_barrier\n\t"
    "v_smfmac_f32_16x16x64_bf16 v[116:119], v[120:123], v[84:91], v124\n\t"
    "ds_read_b64 v[84:85], v108 offset:64000\n\t"
    "ds_read_b64 v[86:87], v126 offset:64000\n\t"
    "v_lshrrev_b32 v90, 16, v109\n\t"
    "v_and_b32 v109, s92, v109\n\t"
    "ds_read_b64 v[88:89], v109 offset:64000\n\t"
    "ds_read_b64 v[90:91], v90 offset:64000\n\t"
    "buffer_load_dwordx4 v[104:107], v127, %12, s85 offen offset:1024 sc1\n\t"
    "v_lshrrev_b32 v126, 16, v110\n\t"
    "v_and_b32 v110, s92, v110\n\t"
    "s_waitcnt vmcnt(2)\n\t"
    "v_smfmac_f32_16x16x64_bf16 v[116:119], v[120:123], v[92:99], v124\n\t"
    "ds_read_b64 v[92:93], v110 offset:64000\n\t"
    "ds_read_b64 v[94:95], v126 offset:64000\n\t"
    "v_lshrrev_b32 v98, 16, v111\n\t"
    "v_and_b32 v111, s92, v111\n\t"
    "ds_read_b64 v[96:97], v111 offset:64000\n\t"
    "ds_read_b64 v[98:99], v98 offset:64000\n\t"
    "s_add_u32 s86, s86, 0x8000\n\t"
    "buffer_load_dwordx4 v[108:111], v127, %12, s86 offen offset:0 sc1\n\t"
    "v_lshrrev_b32 v252, 16, v112\n\t"
    "v_and_b32 v112, s92, v112\n\t"
    "v_lshrrev_b32 v253, 16, v113\n\t"
    "v_cvt_pk_bf16_f32 v30, v116, v117\n\t"
    "v_cvt_pk_bf16_f32 v31, v118, v119\n\t"
    "v_and_b32 v119, s67, v5\n\t"
    "v_lshlrev_b32 v118, 16, v5\n\t"
    "v_and_b32 v117, s67, v4\n\t"
    "v_lshlrev_b32 v116, 16, v4\n\t"
    "v_and_b32 v113, s92, v113\n\t"
    "v_mov_b32 v82, 0xbdd2d3e8\n\t"
    "v_mov_b32 v83, 0xbdd2d3e8\n\t"
    "v_mov_b32 v81, 0xc0135761\n\t"
    "v_lshrrev_b32 v125, 2, v127\n\t"
    "v_mov_b32 v80, 0xc0135761\n\t"
    "s_waitcnt lgkmcnt(4)\n\t"
    "v_smfmac_f32_16x16x64_bf16 v[116:119], v[120:123], v[84:91], v124\n\t"
    "ds_read_b64 v[84:85], v112 offset:64000\n\t"
    "ds_read_b64 v[86:87], v252 offset:64000\n\t"
    "ds_read_b64 v[88:89], v113 offset:64000\n\t"
    "ds_read_b64 v[90:91], v253 offset:64000\n\t"
    "v_lshrrev_b32 v254, 16, v114\n\t"
    "v_and_b32 v114, s92, v114\n\t"
    "v_lshrrev_b32 v255, 16, v115\n\t"
    "v_and_b32 v115, s92, v115\n\t"
    "s_lshl_b32 s65, %16, 10\n\t"
    "s_add_u32 s65, s65, 30408704\n\t"
    "s_xor_b32 s61, %14, 8\n\t"
    "s_mul_i32 s61, s61, 69632\n\t"
    "s_add_u32 s61, s61, s65\n\t"
    "s_mul_i32 s94, %14, 69632\n\t"
    "s_add_u32 s94, s94, s65\n\t"
    "s_lshl_b32 s65, %16, 9\n\t"
    "s_sub_u32 s65, 0x10000, s65\n\t"
    "s_add_u32 s93, s94, s65\n\t"
    "s_add_u32 s96, s61, s65\n\t"
    "v_mov_b32 v165, 1\n\t"
    "s_waitcnt vmcnt(2) lgkmcnt(4)\n\t"
    "v_smfmac_f32_16x16x64_bf16 v[116:119], v[120:123], v[92:99], v124\n\t"
    "ds_read_b64 v[92:93], v114 offset:64000\n\t"
    "ds_read_b64 v[94:95], v254 offset:64000\n\t"
    "ds_read_b64 v[96:97], v115 offset:64000\n\t"
    "ds_read_b64 v[98:99], v255 offset:64000\n\t"
    "buffer_load_dwordx4 v[172:175], v127, %12, s86 offen offset:1024 sc1\n\t"
    "v_lshrrev_b32 v252, 16, v100\n\t"
    "v_and_b32 v100, s92, v100\n\t"
    "v_lshrrev_b32 v253, 16, v101\n\t"
    "v_cvt_pk_bf16_f32 v4, v116, v117\n\t"
    "v_cvt_pk_bf16_f32 v5, v118, v119\n\t"
    "v_and_b32 v119, s67, v7\n\t"
    "v_lshlrev_b32 v118, 16, v7\n\t"
    "v_and_b32 v117, s67, v6\n\t"
    "v_lshlrev_b32 v116, 16, v6\n\t"
    "v_and_b32 v101, s92, v101\n\t"
    "v_mov_b32 v156, v120\n\t"
    "v_mov_b32 v160, 0\n\t"
    "v_mov_b32 v161, v121\n\t"
    "v_mov_b32 v157, 0\n\t"
    "v_mov_b32 v158, v122\n\t"
    "v_mov_b32 v162, 0\n\t"
    "v_mov_b32 v163, v123\n\t"
    "v_mov_b32 v159, 0\n\t"
    "s_waitcnt lgkmcnt(4)\n\t"
    "v_smfmac_f32_16x16x64_bf16 v[116:119], v[120:123], v[84:91], v124\n\t"
    "ds_read_b64 v[84:85], v100 offset:64000\n\t"
    "ds_read_b64 v[86:87], v252 offset:64000\n\t"
    "ds_read_b64 v[88:89], v101 offset:64000\n\t"
    "ds_read_b64 v[90:91], v253 offset:64000\n\t"
    "s_add_u32 s87, s87, 0x8000\n\t"
    "buffer_load_dwordx4 v[176:179], v127, %12, s87 offen offset:0 sc1\n\t"
    "v_lshrrev_b32 v254, 16, v102\n\t"
    "v_and_b32 v102, s92, v102\n\t"
    "v_lshrrev_b32 v255, 16, v103\n\t"
    "v_and_b32 v103, s92, v103\n\t"
    "s_cmp_lt_u32 %16, 4\n\t"
    "s_cbranch_scc1 L_tp163_%=\n\t"
    "s_setprio 0\n\t"
    "L_tp163_%=:\n\t"
    "s_waitcnt vmcnt(3) lgkmcnt(4)\n\t"
    "v_smfmac_f32_16x16x64_bf16 v[116:119], v[120:123], v[92:99], v124\n\t"
    "ds_read_b64 v[92:93], v102 offset:64000\n\t"
    "ds_read_b64 v[94:95], v254 offset:64000\n\t"
    "ds_read_b64 v[96:97], v103 offset:64000\n\t"
    "ds_read_b64 v[98:99], v255 offset:64000\n\t"
    "buffer_load_dwordx4 v[180:183], v127, %12, s87 offen offset:1024 sc1\n\t"
    "v_lshrrev_b32 v252, 16, v104\n\t"
    "v_and_b32 v104, s92, v104\n\t"
    "v_lshrrev_b32 v253, 16, v105\n\t"
    "v_cvt_pk_bf16_f32 v6, v116, v117\n\t"
    "v_cvt_pk_bf16_f32 v7, v118, v119\n\t"
    "v_and_b32 v119, s67, v13\n\t"
    "v_lshlrev_b32 v118, 16, v13\n\t"
    "v_and_b32 v117, s67, v12\n\t"
    "v_lshlrev_b32 v116, 16, v12\n\t"
    "v_and_b32 v105, s92, v105\n\t"
    "buffer_store_dwordx4 v[4:7], v127, %12, s61 offen sc1\n\t"
    "s_waitcnt lgkmcnt(4)\n\t"
    "v_smfmac_f32_16x16x64_bf16 v[116:119], v[120:123], v[84:91], v124\n\t"
    "ds_read_b64 v[84:85], v104 offset:64000\n\t"
    "ds_read_b64 v[86:87], v252 offset:64000\n\t"
    "ds_read_b64 v[88:89], v105 offset:64000\n\t"
    "ds_read_b64 v[90:91], v253 offset:64000\n\t"
    "s_add_u32 s88, s88, s95\n\t"
    "buffer_load_dwordx4 v[184:187], v127, %12, s88 offen offset:0 sc1\n\t"
    "v_lshrrev_b32 v254, 16, v106\n\t"
    "v_and_b32 v106, s92, v106\n\t"
    "v_lshrrev_b32 v255, 16, v107\n\t"
    "v_and_b32 v107, s92, v107\n\t"
    "s_waitcnt vmcnt(5) lgkmcnt(4)\n\t"
    "v_smfmac_f32_16x16x64_bf16 v[116:119], v[120:123], v[92:99], v124\n\t"
    "ds_read_b64 v[92:93], v106 offset:64000\n\t"
    "ds_read_b64 v[94:95], v254 offset:64000\n\t"
    "ds_read_b64 v[96:97], v107 offset:64000\n\t"
    "ds_read_b64 v[98:99], v255 offset:64000\n\t"
    "s_add_u32 s88, s88, 0x8000\n\t"
    "buffer_load_dwordx4 v[188:191], v127, %12, s88 offen offset:0 sc1\n\t"
    "v_lshrrev_b32 v252, 16, v108\n\t"
    "v_and_b32 v108, s92, v108\n\t"
    "v_lshrrev_b32 v253, 16, v109\n\t"
    "v_cvt_pk_bf16_f32 v12, v116, v117\n\t"
    "v_cvt_pk_bf16_f32 v13, v118, v119\n\t"
    "v_and_b32 v119, s67, v15\n\t"
    "v_lshlrev_b32 v118, 16, v15\n\t"
    "v_and_b32 v117, s67, v14\n\t"
    "v_lshlrev_b32 v116, 16, v14\n\t"
    "v_and_b32 v109, s92, v109\n\t"
    "s_waitcnt lgkmcnt(4)\n\t"
    "v_smfmac_f32_16x16x64_bf16 v[116:119], v[120:123], v[84:91], v124\n\t"
    "ds_read_b64 v[84:85], v108 offset:64000\n\t"
    "ds_read_b64 v[86:87], v252 offset:64000\n\t"
    "ds_read_b64 v[88:89], v109 offset:64000\n\t"
    "ds_read_b64 v[90:91], v253 offset:64000\n\t"
    "s_sub_u32 s88, s88, 0x8000\n\t"
    "buffer_load_dwordx4 v[192:195], v127, %12, s88 offen offset:1024 sc1\n\t"
    "v_lshrrev_b32 v254, 16, v110\n\t"
    "v_and_b32 v110, s92, v110\n\t"
    "v_lshrrev_b32 v255, 16, v111\n\t"
    "v_and_b32 v111, s92, v111\n\t"
    "s_waitcnt vmcnt(6) lgkmcnt(4)\n\t"
    "v_smfmac_f32_16x16x64_bf16 v[116:119], v[120:123], v[92:99], v124\n\t"
    "ds_read_b64 v[92:93], v110 offset:64000\n\t"
    "ds_read_b64 v[94:95], v254 offset:64000\n\t"
    "ds_read_b64 v[96:97], v111 offset:64000\n\t"
    "ds_read_b64 v[98:99], v255 offset:64000\n\t"
    "s_add_u32 s88, s88, 0x8000\n\t"
    "buffer_load_dwordx4 v[196:199], v127, %12, s88 offen offset:1024 sc1\n\t"
    "v_lshrrev_b32 v252, 16, v172\n\t"
    "v_and_b32 v172, s92, v172\n\t"
    "v_lshrrev_b32 v253, 16, v173\n\t"
    "v_cvt_pk_bf16_f32 v14, v116, v117\n\t"
    "v_cvt_pk_bf16_f32 v15, v118, v119\n\t"
    "v_and_b32 v119, s67, v21\n\t"
    "v_lshlrev_b32 v118, 16, v21\n\t"
    "v_and_b32 v117, s67, v20\n\t"
    "v_lshlrev_b32 v116, 16, v20\n\t"
    "v_and_b32 v173, s92, v173\n\t"
    "s_waitcnt lgkmcnt(4)\n\t"
    "v_smfmac_f32_16x16x64_bf16 v[116:119], v[120:123], v[84:91], v124\n\t"
    "ds_read_b64 v[84:85], v172 offset:64000\n\t"
    "ds_read_b64 v[86:87], v252 offset:64000\n\t"
    "ds_read_b64 v[88:89], v173 offset:64000\n\t"
    "ds_read_b64 v[90:91], v253 offset:64000\n\t"
    "v_lshrrev_b32 v254, 16, v174\n\t"
    "v_and_b32 v174, s92, v174\n\t"
    "v_lshrrev_b32 v255, 16, v175\n\t"
    "v_and_b32 v175, s92, v175\n\t"
    "s_waitcnt vmcnt(6) lgkmcnt(4)\n\t"
    "v_smfmac_f32_16x16x64_bf16 v[116:119], v[120:123], v[92:99], v124\n\t"
    "ds_read_b64 v[92:93], v174 offset:64000\n\t"
    "ds_read_b64 v[94:95], v254 offset:64000\n\t"
    "ds_read_b64 v[96:97], v175 offset:64000\n\t"
    "ds_read_b64 v[98:99], v255 offset:64000\n\t"
    "s_add_u32 s89, s89, s95\n\t"
    "buffer_load_dwordx4 v[200:203], v127, %12, s89 offen offset:0 sc1\n\t"
    "v_lshrrev_b32 v252, 16, v176\n\t"
    "v_and_b32 v176, s92, v176\n\t"
    "v_lshrrev_b32 v253, 16, v177\n\t"
    "v_cvt_pk_bf16_f32 v20, v116, v117\n\t"
    "v_cvt_pk_bf16_f32 v21, v118, v119\n\t"
    "v_and_b32 v119, s67, v23\n\t"
    "v_lshlrev_b32 v118, 16, v23\n\t"
    "v_and_b32 v117, s67, v22\n\t"
    "v_lshlrev_b32 v116, 16, v22\n\t"
    "v_and_b32 v177, s92, v177\n\t"
    "s_waitcnt lgkmcnt(4)\n\t"
    "v_smfmac_f32_16x16x64_bf16 v[116:119], v[120:123], v[84:91], v124\n\t"
    "ds_read_b64 v[84:85], v176 offset:64000\n\t"
    "ds_read_b64 v[86:87], v252 offset:64000\n\t"
    "ds_read_b64 v[88:89], v177 offset:64000\n\t"
    "ds_read_b64 v[90:91], v253 offset:64000\n\t"
    "v_lshrrev_b32 v254, 16, v178\n\t"
    "v_and_b32 v178, s92, v178\n\t"
    "v_lshrrev_b32 v255, 16, v179\n\t"
    "v_and_b32 v179, s92, v179\n\t"
    "s_waitcnt vmcnt(6) lgkmcnt(4)\n\t"
    "v_smfmac_f32_16x16x64_bf16 v[116:119], v[120:123], v[92:99], v124\n\t"
    "ds_read_b64 v[92:93], v178 offset:64000\n\t"
    "ds_read_b64 v[94:95], v254 offset:64000\n\t"
    "ds_read_b64 v[96:97], v179 offset:64000\n\t"
    "ds_read_b64 v[98:99], v255 offset:64000\n\t"
    "s_add_u32 s89, s89, 0x8000\n\t"
    "buffer_load_dwordx4 v[236:239], v127, %12, s89 offen offset:0 sc1\n\t"
    "v_lshrrev_b32 v252, 16, v180\n\t"
    "v_and_b32 v180, s92, v180\n\t"
    "v_lshrrev_b32 v253, 16, v181\n\t"
    "v_cvt_pk_bf16_f32 v22, v116, v117\n\t"
    "v_cvt_pk_bf16_f32 v23, v118, v119\n\t"
    "v_and_b32 v119, s67, v29\n\t"
    "v_lshlrev_b32 v118, 16, v29\n\t"
    "v_and_b32 v117, s67, v28\n\t"
    "v_lshlrev_b32 v116, 16, v28\n\t"
    "v_and_b32 v181, s92, v181\n\t"
    "s_add_u32 s61, s61, 0x2000\n\t"
    "buffer_store_dwordx4 v[12:15], v127, %12, s61 offen sc1\n\t"
    "s_waitcnt lgkmcnt(4)\n\t"
    "v_smfmac_f32_16x16x64_bf16 v[116:119], v[120:123], v[84:91], v124\n\t"
    "ds_read_b64 v[84:85], v180 offset:64000\n\t"
    "ds_read_b64 v[86:87], v252 offset:64000\n\t"
    "ds_read_b64 v[88:89], v181 offset:64000\n\t"
    "ds_read_b64 v[90:91], v253 offset:64000\n\t"
    "v_lshrrev_b32 v254, 16, v182\n\t"
    "v_and_b32 v182, s92, v182\n\t"
    "v_lshrrev_b32 v255, 16, v183\n\t"
    "v_and_b32 v183, s92, v183\n\t"
    "s_waitcnt vmcnt(6) lgkmcnt(4)\n\t"
    "v_smfmac_f32_16x16x64_bf16 v[116:119], v[120:123], v[92:99], v124\n\t"
    "ds_read_b64 v[92:93], v182 offset:64000\n\t"
    "ds_read_b64 v[94:95], v254 offset:64000\n\t"
    "ds_read_b64 v[96:97], v183 offset:64000\n\t"
    "ds_read_b64 v[98:99], v255 offset:64000\n\t"
    "s_sub_u32 s89, s89, 0x8000\n\t"
    "buffer_load_dwordx4 v[204:207], v127, %12, s89 offen offset:1024 sc1\n\t"
    "v_lshrrev_b32 v252, 16, v184\n\t"
    "v_and_b32 v184, s92, v184\n\t"
    "v_lshrrev_b32 v253, 16, v185\n\t"
    "v_cvt_pk_bf16_f32 v28, v116, v117\n\t"
    "v_cvt_pk_bf16_f32 v29, v118, v119\n\t"
    "v_and_b32 v119, s67, v31\n\t"
    "v_lshlrev_b32 v118, 16, v31\n\t"
    "v_and_b32 v117, s67, v30\n\t"
    "v_lshlrev_b32 v116, 16, v30\n\t"
    "v_and_b32 v185, s92, v185\n\t"
    "s_waitcnt lgkmcnt(4)\n\t"
    "v_smfmac_f32_16x16x64_bf16 v[116:119], v[120:123], v[84:91], v124\n\t"
    "ds_read_b64 v[84:85], v184 offset:0\n\t"
    "ds_read_b64 v[86:87], v252 offset:0\n\t"
    "ds_read_b64 v[88:89], v185 offset:0\n\t"
    "ds_read_b64 v[90:91], v253 offset:0\n\t"
    "v_lshrrev_b32 v254, 16, v186\n\t"
    "v_and_b32 v186, s92, v186\n\t"
    "v_lshrrev_b32 v255, 16, v187\n\t"
    "v_and_b32 v187, s92, v187\n\t"
    "s_cmp_lt_u32 %16, 4\n\t"
    "s_cbranch_scc1 L_tp175_%=\n\t"
    "s_setprio 1\n\t"
    "L_tp175_%=:\n\t"
    "s_waitcnt vmcnt(6) lgkmcnt(4)\n\t"
    "v_smfmac_f32_16x16x64_bf16 v[116:119], v[120:123], v[92:99], v124\n\t"
    "ds_read_b64 v[92:93], v186 offset:0\n\t"
    "ds_read_b64 v[94:95], v254 offset:0\n\t"
    "ds_read_b64 v[96:97], v187 offset:0\n\t"
    "ds_read_b64 v[98:99], v255 offset:0\n\t"
    "s_add_u32 s89, s89, 0x8000\n\t"
    "buffer_load_dwordx4 v[128:131], v127, %12, s89 offen offset:1024 sc1\n\t"
    "v_lshrrev_b32 v252, 16, v188\n\t"
    "v_and_b32 v188, s92, v188\n\t"
    "v_lshrrev_b32 v253, 16, v189\n\t"
    "v_cvt_pk_bf16_f32 v30, v116, v117\n\t"
    "v_cvt_pk_bf16_f32 v31, v118, v119\n\t"
    "v_and_b32 v119, s67, v37\n\t"
    "v_lshlrev_b32 v118, 16, v37\n\t"
    "v_and_b32 v117, s67, v36\n\t"
    "v_lshlrev_b32 v116, 16, v36\n\t"
    "v_and_b32 v189, s92, v189\n\t"
    "s_waitcnt lgkmcnt(4)\n\t"
    "v_smfmac_f32_16x16x64_bf16 v[116:119], v[120:123], v[84:91], v124\n\t"
    "ds_read_b64 v[84:85], v188 offset:64000\n\t"
    "ds_read_b64 v[86:87], v252 offset:64000\n\t"
    "ds_read_b64 v[88:89], v189 offset:64000\n\t"
    "ds_read_b64 v[90:91], v253 offset:64000\n\t"
    "v_lshrrev_b32 v254, 16, v190\n\t"
    "v_and_b32 v190, s92, v190\n\t"
    "v_lshrrev_b32 v255, 16, v191\n\t"
    "v_and_b32 v191, s92, v191\n\t"
    "s_waitcnt vmcnt(6) lgkmcnt(4)\n\t"
    "v_smfmac_f32_16x16x64_bf16 v[116:119], v[120:123], v[92:99], v124\n\t"
    "ds_read_b64 v[92:93], v190 offset:64000\n\t"
    "ds_read_b64 v[94:95], v254 offset:64000\n\t"
    "ds_read_b64 v[96:97], v191 offset:64000\n\t"
    "ds_read_b64 v[98:99], v255 offset:64000\n\t"
    "s_add_u32 s90, s90, s95\n\t"
    "buffer_load_dwordx4 v[132:135], v127, %12, s90 offen offset:0 sc1\n\t"
    "v_lshrrev_b32 v252, 16, v192\n\t"
    "v_and_b32 v192, s92, v192\n\t"
    "v_lshrrev_b32 v253, 16, v193\n\t"
    "v_and_b32 v193, s92, v193\n\t"
    "s_waitcnt lgkmcnt(4)\n\t"
    "v_smfmac_f32_16x16x64_bf16 v[116:119], v[120:123], v[84:91], v124\n\t"
    "ds_read_b64 v[84:85], v192 offset:0\n\t"
    "ds_read_b64 v[86:87], v252 offset:0\n\t"
    "ds_read_b64 v[88:89], v193 offset:0\n\t"
    "ds_read_b64 v[90:91], v253 offset:0\n\t"
    "v_lshrrev_b32 v254, 16, v194\n\t"
    "v_and_b32 v194, s92, v194\n\t"
    "v_lshrrev_b32 v255, 16, v195\n\t"
    "v_and_b32 v195, s92, v195\n\t"
    "s_waitcnt vmcnt(6) lgkmcnt(4)\n\t"
    "v_smfmac_f32_16x16x64_bf16 v[116:119], v[120:123], v[92:99], v124\n\t"
    "ds_read_b64 v[92:93], v194 offset:0\n\t"
    "ds_read_b64 v[94:95], v254 offset:0\n\t"
    "ds_read_b64 v[96:97], v195 offset:0\n\t"
    "ds_read_b64 v[98:99], v255 offset:0\n\t"
    "s_add_u32 s90, s90, 0x8000\n\t"
    "buffer_load_dwordx4 v[136:139], v127, %12, s90 offen offset:0 sc1\n\t"
    "v_lshrrev_b32 v252, 16, v196\n\t"
    "v_and_b32 v196, s92, v196\n\t"
    "v_lshrrev_b32 v253, 16, v197\n\t"
    "v_cvt_pk_bf16_f32 v36, v116, v117\n\t"
    "v_cvt_pk_bf16_f32 v37, v118, v119\n\t"
    "v_and_b32 v119, s67, v39\n\t"
    "v_lshlrev_b32 v118, 16, v39\n\t"
    "v_and_b32 v117, s67, v38\n\t"
    "v_lshlrev_b32 v116, 16, v38\n\t"
    "v_and_b32 v197, s92, v197\n\t"
    "s_waitcnt vmcnt(4)\n\t"
    "s_mov_b64 exec, 0xffffffff\n\t"
    "buffer_store_dword v165, v125, %12, s96 offen offset:0 sc1\n\t"
    "s_mov_b64 exec, -1\n\t"
    "s_add_u32 s61, s61, 0x2000\n\t"
    "buffer_store_dwordx4 v[20:23], v127, %12, s61 offen sc1\n\t"
    "s_waitcnt lgkmcnt(4)\n\t"
    "v_smfmac_f32_16x16x64_bf16 v[116:119], v[120:123], v[84:91], v124\n\t"
    "ds_read_b64 v[84:85], v196 offset:64000\n\t"
    "ds_read_b64 v[86:87], v252 offset:64000\n\t"
    "ds_read_b64 v[88:89], v197 offset:64000\n\t"
    "ds_read_b64 v[90:91], v253 offset:64000\n\t"
    "v_lshrrev_b32 v254, 16, v198\n\t"
    "v_and_b32 v198, s92, v198\n\t"
    "v_lshrrev_b32 v255, 16, v199\n\t"
    "v_and_b32 v199, s92, v199\n\t"
    "s_waitcnt lgkmcnt(4)\n\t"
    "v_smfmac_f32_16x16x64_bf16 v[116:119], v[120:123], v[92:99], v124\n\t"
    "ds_read_b64 v[92:93], v198 offset:64000\n\t"
    "ds_read_b64 v[94:95], v254 offset:64000\n\t"
    "ds_read_b64 v[96:97], v199 offset:64000\n\t"
    "ds_read_b64 v[98:99], v255 offset:64000\n\t"
    "s_sub_u32 s90, s90, 0x8000\n\t"
    "buffer_load_dwordx4 v[140:143], v127, %12, s90 offen offset:1024 sc1\n\t"
    "v_lshrrev_b32 v252, 16, v200\n\t"
    "v_and_b32 v200, s92, v200\n\t"
    "v_lshrrev_b32 v253, 16, v201\n\t"
    "v_and_b32 v201, s92, v201\n\t"
    "s_waitcnt lgkmcnt(4)\n\t"
    "v_smfmac_f32_16x16x64_bf16 v[116:119], v[120:123], v[84:91], v124\n\t"
    "ds_read_b64 v[84:85], v200 offset:0\n\t"
    "ds_read_b64 v[86:87], v252 offset:0\n\t"
    "ds_read_b64 v[88:89], v201 offset:0\n\t"
    "ds_read_b64 v[90:91], v253 offset:0\n\t"
    "v_lshrrev_b32 v254, 16, v202\n\t"
    "v_and_b32 v202, s92, v202\n\t"
    "v_lshrrev_b32 v255, 16, v203\n\t"
    "v_and_b32 v203, s92, v203\n\t"
    "s_waitcnt lgkmcnt(4)\n\t"
    "v_smfmac_f32_16x16x64_bf16 v[116:119], v[120:123], v[92:99], v124\n\t"
    "ds_read_b64 v[92:93], v202 offset:0\n\t"
    "ds_read_b64 v[94:95], v254 offset:0\n\t"
    "ds_read_b64 v[96:97], v203 offset:0\n\t"
    "ds_read_b64 v[98:99], v255 offset:0\n\t"
    "s_add_u32 s90, s90, 0x8000\n\t"
    "buffer_load_dwordx4 v[144:147], v127, %12, s90 offen offset:1024 sc1\n\t"
    "v_lshrrev_b32 v252, 16, v236\n\t"
    "v_and_b32 v236, s92, v236\n\t"
    "v_lshrrev_b32 v253, 16, v237\n\t"
    "v_cvt_pk_bf16_f32 v38, v116, v117\n\t"
    "v_cvt_pk_bf16_f32 v39, v118, v119\n\t"
    "v_and_b32 v119, s67, v45\n\t"
    "v_lshlrev_b32 v118, 16, v45\n\t"
    "v_and_b32 v117, s67, v44\n\t"
    "v_lshlrev_b32 v116, 16, v44\n\t"
    "v_and_b32 v237, s92, v237\n\t"
    "s_waitcnt lgkmcnt(4)\n\t"
    "v_smfmac_f32_16x16x64_bf16 v[116:119], v[120:123], v[84:91], v124\n\t"
    "ds_read_b64 v[84:85], v236 offset:64000\n\t"
    "ds_read_b64 v[86:87], v252 offset:64000\n\t"
    "ds_read_b64 v[88:89], v237 offset:64000\n\t"
    "ds_read_b64 v[90:91], v253 offset:64000\n\t"
    "v_lshrrev_b32 v254, 16, v238\n\t"
    "v_and_b32 v238, s92, v238\n\t"
    "v_lshrrev_b32 v255, 16, v239\n\t"
    "v_and_b32 v239, s92, v239\n\t"
    "s_waitcnt vmcnt(7) lgkmcnt(4)\n\t"
    "v_smfmac_f32_16x16x64_bf16 v[116:119], v[120:123], v[92:99], v124\n\t"
    "ds_read_b64 v[92:93], v238 offset:64000\n\t"
    "ds_read_b64 v[94:95], v254 offset:64000\n\t"
    "ds_read_b64 v[96:97], v239 offset:64000\n\t"
    "ds_read_b64 v[98:99], v255 offset:64000\n\t"
    "s_add_u32 s91, s91, s95\n\t"
    "buffer_load_dwordx4 v[148:151], v127, %12, s91 offen offset:0 sc1\n\t"
    "v_lshrrev_b32 v252, 16, v204\n\t"
    "v_and_b32 v204, s92, v204\n\t"
    "v_lshrrev_b32 v253, 16, v205\n\t"
    "v_and_b32 v205, s92, v205\n\t"
    "s_waitcnt lgkmcnt(4)\n\t"
    "v_smfmac_f32_16x16x64_bf16 v[116:119], v[120:123], v[84:91], v124\n\t"
    "ds_read_b64 v[84:85], v204 offset:0\n\t"
    "ds_read_b64 v[86:87], v252 offset:0\n\t"
    "ds_read_b64 v[88:89], v205 offset:0\n\t"
    "ds_read_b64 v[90:91], v253 offset:0\n\t"
    "v_lshrrev_b32 v254, 16, v206\n\t"
    "v_and_b32 v206, s92, v206\n\t"
    "v_lshrrev_b32 v255, 16, v207\n\t"
    "v_and_b32 v207, s92, v207\n\t"
    "s_waitcnt vmcnt(7) lgkmcnt(4)\n\t"
    "v_smfmac_f32_16x16x64_bf16 v[116:119], v[120:123], v[92:99], v124\n\t"
    "ds_read_b64 v[92:93], v206 offset:0\n\t"
    "ds_read_b64 v[94:95], v254 offset:0\n\t"
    "ds_read_b64 v[96:97], v207 offset:0\n\t"
    "ds_read_b64 v[98:99], v255 offset:0\n\t"
    "s_add_u32 s91, s91, 0x8000\n\t"
    "buffer_load_dwordx4 v[152:155], v127, %12, s91 offen offset:0 sc1\n\t"
    "v_lshrrev_b32 v252, 16, v128\n\t"
    "v_and_b32 v128, s92, v128\n\t"
    "v_lshrrev_b32 v253, 16, v129\n\t"
    "v_cvt_pk_bf16_f32 v44, v116, v117\n\t"
    "v_cvt_pk_bf16_f32 v45, v118, v119\n\t"
    "v_and_b32 v119, s67, v47\n\t"
    "v_lshlrev_b32 v118, 16, v47\n\t"
    "v_and_b32 v117, s67, v46\n\t"
    "v_lshlrev_b32 v116, 16, v46\n\t"
    "v_and_b32 v129, s92, v129\n\t"
    "s_add_u32 s61, s61, 0x2000\n\t"
    "buffer_store_dwordx4 v[28:31], v127, %12, s61 offen sc1\n\t"
    "s_waitcnt lgkmcnt(4)\n\t"
    "v_smfmac_f32_16x16x64_bf16 v[116:119], v[120:123], v[84:91], v124\n\t"
    "ds_read_b64 v[84:85], v128 offset:64000\n\t"
    "ds_read_b64 v[86:87], v252 offset:64000\n\t"
    "ds_read_b64 v[88:89], v129 offset:64000\n\t"
    "ds_read_b64 v[90:91], v253 offset:64000\n\t"
    "v_lshrrev_b32 v254, 16, v130\n\t"
    "v_and_b32 v130, s92, v130\n\t"
    "v_lshrrev_b32 v255, 16, v131\n\t"
    "v_and_b32 v131, s92, v131\n\t"
    "s_waitcnt vmcnt(8) lgkmcnt(4)\n\t"
    "v_smfmac_f32_16x16x64_bf16 v[116:119], v[120:123], v[92:99], v124\n\t"
    "ds_read_b64 v[92:93], v130 offset:64000\n\t"
    "ds_read_b64 v[94:95], v254 offset:64000\n\t"
    "ds_read_b64 v[96:97], v131 offset:64000\n\t"
    "ds_read_b64 v[98:99], v255 offset:64000\n\t"
    "s_sub_u32 s91, s91, 0x8000\n\t"
    "buffer_load_dwordx4 v[208:211], v127, %12, s91 offen offset:1024 sc1\n\t"
    "v_lshrrev_b32 v252, 16, v132\n\t"
    "v_and_b32 v132, s92, v132\n\t"
    "v_lshrrev_b32 v253, 16, v133\n\t"
    "v_and_b32 v133, s92, v133\n\t"
    "s_waitcnt lgkmcnt(4)\n\t"
    "v_smfmac_f32_16x16x64_bf16 v[116:119], v[120:123], v[84:91], v124\n\t"
    "ds_read_b64 v[84:85], v132 offset:0\n\t"
    "ds_read_b64 v[86:87], v252 offset:0\n\t"
    "ds_read_b64 v[88:89], v133 offset:0\n\t"
    "ds_read_b64 v[90:91], v253 offset:0\n\t"
    "v_lshrrev_b32 v254, 16, v134\n\t"
    "v_and_b32 v134, s92, v134\n\t"
    "v_lshrrev_b32 v255, 16, v135\n\t"
    "v_and_b32 v135, s92, v135\n\t"
    "s_waitcnt vmcnt(8) lgkmcnt(4)\n\t"
    "v_smfmac_f32_16x16x64_bf16 v[116:119], v[120:123], v[92:99], v124\n\t"
    "ds_read_b64 v[92:93], v134 offset:0\n\t"
    "ds_read_b64 v[94:95], v254 offset:0\n\t"
    "ds_read_b64 v[96:97], v135 offset:0\n\t"
    "ds_read_b64 v[98:99], v255 offset:0\n\t"
    "s_add_u32 s91, s91, 0x8000\n\t"
    "buffer_load_dwordx4 v[212:215], v127, %12, s91 offen offset:1024 sc1\n\t"
    "v_lshrrev_b32 v252, 16, v136\n\t"
    "v_and_b32 v136, s92, v136\n\t"
    "v_lshrrev_b32 v253, 16, v137\n\t"
    "v_cvt_pk_bf16_f32 v46, v116, v117\n\t"
    "v_cvt_pk_bf16_f32 v47, v118, v119\n\t"
    "v_and_b32 v119, s67, v53\n\t"
    "v_lshlrev_b32 v118, 16, v53\n\t"
    "v_and_b32 v117, s67, v52\n\t"
    "v_lshlrev_b32 v116, 16, v52\n\t"
    "v_and_b32 v137, s92, v137\n\t"
    "s_waitcnt lgkmcnt(4)\n\t"
    "v_smfmac_f32_16x16x64_bf16 v[116:119], v[120:123], v[84:91], v124\n\t"
    "ds_read_b64 v[84:85], v136 offset:64000\n\t"
    "ds_read_b64 v[86:87], v252 offset:64000\n\t"
    "ds_read_b64 v[88:89], v137 offset:64000\n\t"
    "ds_read_b64 v[90:91], v253 offset:64000\n\t"
    "v_lshrrev_b32 v254, 16, v138\n\t"
    "v_and_b32 v138, s92, v138\n\t"
    "v_lshrrev_b32 v255, 16, v139\n\t"
    "v_and_b32 v139, s92, v139\n\t"
    "s_waitcnt vmcnt(6) lgkmcnt(4)\n\t"
    "v_smfmac_f32_16x16x64_bf16 v[116:119], v[120:123], v[92:99], v124\n\t"
    "ds_read_b64 v[92:93], v138 offset:64000\n\t"
    "ds_read_b64 v[94:95], v254 offset:64000\n\t"
    "ds_read_b64 v[96:97], v139 offset:64000\n\t"
    "ds_read_b64 v[98:99], v255 offset:64000\n\t"
    "s_sub_u32 s84, s84, s95\n\t"
    "buffer_load_dwordx4 v[216:219], v127, %12, s84 offen offset:0 sc1\n\t"
    "v_lshrrev_b32 v252, 16, v140\n\t"
    "v_and_b32 v140, s92, v140\n\t"
    "v_lshrrev_b32 v253, 16, v141\n\t"
    "v_and_b32 v141, s92, v141\n\t"
    "s_waitcnt lgkmcnt(4)\n\t"
    "v_smfmac_f32_16x16x64_bf16 v[116:119], v[120:123], v[84:91], v124\n\t"
    "ds_read_b64 v[84:85], v140 offset:0\n\t"
    "ds_read_b64 v[86:87], v252 offset:0\n\t"
    "ds_read_b64 v[88:89], v141 offset:0\n\t"
    "ds_read_b64 v[90:91], v253 offset:0\n\t"
    "v_lshrrev_b32 v254, 16, v142\n\t"
    "v_and_b32 v142, s92, v142\n\t"
    "v_lshrrev_b32 v255, 16, v143\n\t"
    "v_and_b32 v143, s92, v143\n\t"
    "s_cmp_lt_u32 %16, 4\n\t"
    "s_cbranch_scc1 L_tp195_%=\n\t"
    "s_setprio 0\n\t"
    "L_tp195_%=:\n\t"
    "s_waitcnt vmcnt(6) lgkmcnt(4)\n\t"
    "v_smfmac_f32_16x16x64_bf16 v[116:119], v[120:123], v[92:99], v124\n\t"
    "ds_read_b64 v[92:93], v142 offset:0\n\t"
    "ds_read_b64 v[94:95], v254 offset:0\n\t"
    "ds_read_b64 v[96:97], v143 offset:0\n\t"
    "ds_read_b64 v[98:99], v255 offset:0\n\t"
    "buffer_load_dwordx4 v[240:243], v127, %12, s84 offen offset:1024 sc1\n\t"
    "v_lshrrev_b32 v252, 16, v144\n\t"
    "v_and_b32 v144, s92, v144\n\t"
    "v_lshrrev_b32 v253, 16, v145\n\t"
    "v_cvt_pk_bf16_f32 v52, v116, v117\n\t"
    "v_cvt_pk_bf16_f32 v53, v118, v119\n\t"
    "v_and_b32 v119, s67, v55\n\t"
    "v_lshlrev_b32 v118, 16, v55\n\t"
    "v_and_b32 v117, s67, v54\n\t"
    "v_lshlrev_b32 v116, 16, v54\n\t"
    "v_and_b32 v145, s92, v145\n\t"
    "s_waitcnt vmcnt(4)\n\t"
    "s_mov_b64 exec, 0xffffffff\n\t"
    "buffer_store_dword v165, v125, %12, s96 offen offset:128 sc1\n\t"
    "s_mov_b64 exec, -1\n\t"
    "s_add_u32 s61, s61, 0x2000\n\t"
    "buffer_store_dwordx4 v[36:39], v127, %12, s61 offen sc1\n\t"
    "s_waitcnt lgkmcnt(4)\n\t"
    "v_smfmac_f32_16x16x64_bf16 v[116:119], v[120:123], v[84:91], v124\n\t"
    "ds_read_b64 v[84:85], v144 offset:64000\n\t"
    "ds_read_b64 v[86:87], v252 offset:64000\n\t"
    "ds_read_b64 v[88:89], v145 offset:64000\n\t"
    "ds_read_b64 v[90:91], v253 offset:64000\n\t"
    "v_lshrrev_b32 v254, 16, v146\n\t"
    "v_and_b32 v146, s92, v146\n\t"
    "v_lshrrev_b32 v255, 16, v147\n\t"
    "v_and_b32 v147, s92, v147\n\t"
    "s_waitcnt lgkmcnt(4)\n\t"
    "v_smfmac_f32_16x16x64_bf16 v[116:119], v[120:123], v[92:99], v124\n\t"
    "ds_read_b64 v[92:93], v146 offset:64000\n\t"
    "ds_read_b64 v[94:95], v254 offset:64000\n\t"
    "ds_read_b64 v[96:97], v147 offset:64000\n\t"
    "ds_read_b64 v[98:99], v255 offset:64000\n\t"
    "s_sub_u32 s85, s85, s95\n\t"
    "buffer_load_dwordx4 v[220:223], v127, %12, s85 offen offset:0 sc1\n\t"
    "v_lshrrev_b32 v252, 16, v148\n\t"
    "v_and_b32 v148, s92, v148\n\t"
    "v_lshrrev_b32 v253, 16, v149\n\t"
    "v_and_b32 v149, s92, v149\n\t"
    "s_waitcnt lgkmcnt(4)\n\t"
    "v_smfmac_f32_16x16x64_bf16 v[116:119], v[120:123], v[84:91], v124\n\t"
    "ds_read_b64 v[84:85], v148 offset:0\n\t"
    "ds_read_b64 v[86:87], v252 offset:0\n\t"
    "ds_read_b64 v[88:89], v149 offset:0\n\t"
    "ds_read_b64 v[90:91], v253 offset:0\n\t"
    "v_lshrrev_b32 v254, 16, v150\n\t"
    "v_and_b32 v150, s92, v150\n\t"
    "v_lshrrev_b32 v255, 16, v151\n\t"
    "v_and_b32 v151, s92, v151\n\t"
    "s_waitcnt lgkmcnt(4)\n\t"
    "v_smfmac_f32_16x16x64_bf16 v[116:119], v[120:123], v[92:99], v124\n\t"
    "ds_read_b64 v[92:93], v150 offset:0\n\t"
    "ds_read_b64 v[94:95], v254 offset:0\n\t"
    "ds_read_b64 v[96:97], v151 offset:0\n\t"
    "ds_read_b64 v[98:99], v255 offset:0\n\t"
    "buffer_load_dwordx4 v[224:227], v127, %12, s85 offen offset:1024 sc1\n\t"
    "v_lshrrev_b32 v252, 16, v152\n\t"
    "v_and_b32 v152, s92, v152\n\t"
    "v_lshrrev_b32 v253, 16, v153\n\t"
    "v_cvt_pk_bf16_f32 v54, v116, v117\n\t"
    "v_cvt_pk_bf16_f32 v55, v118, v119\n\t"
    "v_and_b32 v119, s67, v61\n\t"
    "v_lshlrev_b32 v118, 16, v61\n\t"
    "v_and_b32 v117, s67, v60\n\t"
    "v_lshlrev_b32 v116, 16, v60\n\t"
    "v_and_b32 v153, s92, v153\n\t"
    "s_waitcnt lgkmcnt(4)\n\t"
    "v_smfmac_f32_16x16x64_bf16 v[116:119], v[120:123], v[84:91], v124\n\t"
    "ds_read_b64 v[84:85], v152 offset:64000\n\t"
    "ds_read_b64 v[86:87], v252 offset:64000\n\t"
    "ds_read_b64 v[88:89], v153 offset:64000\n\t"
    "ds_read_b64 v[90:91], v253 offset:64000\n\t"
    "v_lshrrev_b32 v254, 16, v154\n\t"
    "v_and_b32 v154, s92, v154\n\t"
    "v_lshrrev_b32 v255, 16, v155\n\t"
    "v_and_b32 v155, s92, v155\n\t"
    "s_waitcnt vmcnt(7) lgkmcnt(4)\n\t"
    "v_smfmac_f32_16x16x64_bf16 v[116:119], v[120:123], v[92:99], v124\n\t"
    "ds_read_b64 v[92:93], v154 offset:64000\n\t"
    "ds_read_b64 v[94:95], v254 offset:64000\n\t"
    "ds_read_b64 v[96:97], v155 offset:64000\n\t"
    "ds_read_b64 v[98:99], v255 offset:64000\n\t"
    "s_sub_u32 s86, s86, s95\n\t"
    "buffer_load_dwordx4 v[228:231], v127, %12, s86 offen offset:0 sc1\n\t"
    "v_lshrrev_b32 v252, 16, v208\n\t"
    "v_and_b32 v208, s92, v208\n\t"
    "v_lshrrev_b32 v253, 16, v209\n\t"
    "v_and_b32 v209, s92, v209\n\t"
    "s_waitcnt lgkmcnt(4)\n\t"
    "v_smfmac_f32_16x16x64_bf16 v[116:119], v[120:123], v[84:91], v124\n\t"
    "ds_read_b64 v[84:85], v208 offset:0\n\t"
    "ds_read_b64 v[86:87], v252 offset:0\n\t"
    "ds_read_b64 v[88:89], v209 offset:0\n\t"
    "ds_read_b64 v[90:91], v253 offset:0\n\t"
    "v_lshrrev_b32 v254, 16, v210\n\t"
    "v_and_b32 v210, s92, v210\n\t"
    "v_lshrrev_b32 v255, 16, v211\n\t"
    "v_and_b32 v211, s92, v211\n\t"
    "s_waitcnt vmcnt(7) lgkmcnt(4)\n\t"
    "v_smfmac_f32_16x16x64_bf16 v[116:119], v[120:123], v[92:99], v124\n\t"
    "ds_read_b64 v[92:93], v210 offset:0\n\t"
    "ds_read_b64 v[94:95], v254 offset:0\n\t"
    "ds_read_b64 v[96:97], v211 offset:0\n\t"
    "ds_read_b64 v[98:99], v255 offset:0\n\t"
    "buffer_load_dwordx4 v[232:235], v127, %12, s86 offen offset:1024 sc1\n\t"
    "v_lshrrev_b32 v252, 16, v212\n\t"
    "v_and_b32 v212, s92, v212\n\t"
    "v_lshrrev_b32 v253, 16, v213\n\t"
    "v_cvt_pk_bf16_f32 v60, v116, v117\n\t"
    "v_cvt_pk_bf16_f32 v61, v118, v119\n\t"
    "v_and_b32 v119, s67, v63\n\t"
    "v_lshlrev_b32 v118, 16, v63\n\t"
    "v_and_b32 v117, s67, v62\n\t"
    "v_lshlrev_b32 v116, 16, v62\n\t"
    "v_and_b32 v213, s92, v213\n\t"
    "s_add_u32 s61, s61, 0x2000\n\t"
    "buffer_store_dwordx4 v[44:47], v127, %12, s61 offen sc1\n\t"
    "s_waitcnt lgkmcnt(4)\n\t"
    "v_smfmac_f32_16x16x64_bf16 v[116:119], v[120:123], v[84:91], v124\n\t"
    "ds_read_b64 v[84:85], v212 offset:64000\n\t"
    "ds_read_b64 v[86:87], v252 offset:64000\n\t"
    "ds_read_b64 v[88:89], v213 offset:64000\n\t"
    "ds_read_b64 v[90:91], v253 offset:64000\n\t"
    "v_lshrrev_b32 v254, 16, v214\n\t"
    "v_and_b32 v214, s92, v214\n\t"
    "v_lshrrev_b32 v255, 16, v215\n\t"
    "v_and_b32 v215, s92, v215\n\t"
    "s_waitcnt vmcnt(8) lgkmcnt(4)\n\t"
    "v_smfmac_f32_16x16x64_bf16 v[116:119], v[120:123], v[92:99], v124\n\t"
    "ds_read_b64 v[92:93], v214 offset:64000\n\t"
    "ds_read_b64 v[94:95], v254 offset:64000\n\t"
    "ds_read_b64 v[96:97], v215 offset:64000\n\t"
    "ds_read_b64 v[98:99], v255 offset:64000\n\t"
    "s_sub_u32 s87, s87, s95\n\t"
    "buffer_load_dwordx4 v[244:247], v127, %12, s87 offen offset:0 sc1\n\t"
    "v_lshrrev_b32 v252, 16, v216\n\t"
    "v_and_b32 v216, s92, v216\n\t"
    "v_lshrrev_b32 v253, 16, v217\n\t"
    "v_and_b32 v217, s92, v217\n\t"
    "s_waitcnt lgkmcnt(4)\n\t"
    "v_smfmac_f32_16x16x64_bf16 v[116:119], v[120:123], v[84:91], v124\n\t"
    "ds_read_b64 v[84:85], v216 offset:0\n\t"
    "ds_read_b64 v[86:87], v252 offset:0\n\t"
    "ds_read_b64 v[88:89], v217 offset:0\n\t"
    "ds_read_b64 v[90:91], v253 offset:0\n\t"
    "v_lshrrev_b32 v254, 16, v218\n\t"
    "v_and_b32 v218, s92, v218\n\t"
    "v_lshrrev_b32 v255, 16, v219\n\t"
    "v_and_b32 v219, s92, v219\n\t"
    "s_cmp_lt_u32 %16, 4\n\t"
    "s_cbranch_scc1 L_tp207_%=\n\t"
    "s_setprio 1\n\t"
    "L_tp207_%=:\n\t"
    "s_waitcnt vmcnt(8) lgkmcnt(4)\n\t"
    "v_smfmac_f32_16x16x64_bf16 v[116:119], v[120:123], v[92:99], v124\n\t"
    "ds_read_b64 v[92:93], v218 offset:0\n\t"
    "ds_read_b64 v[94:95], v254 offset:0\n\t"
    "ds_read_b64 v[96:97], v219 offset:0\n\t"
    "ds_read_b64 v[98:99], v255 offset:0\n\t"
    "buffer_load_dwordx4 v[248:251], v127, %12, s87 offen offset:1024 sc1\n\t"
    "v_lshrrev_b32 v252, 16, v240\n\t"
    "v_and_b32 v240, s92, v240\n\t"
    "v_lshrrev_b32 v253, 16, v241\n\t"
    "v_cvt_pk_bf16_f32 v62, v116, v117\n\t"
    "v_cvt_pk_bf16_f32 v63, v118, v119\n\t"
    "v_mov_b64 v[172:173], s[70:71]\n\t"
    "v_mov_b64 v[174:175], s[72:73]\n\t"
    "v_and_b32 v241, s92, v241\n\t"
    "s_waitcnt lgkmcnt(4)\n\t"
    "v_smfmac_f32_16x16x64_bf16 v[172:175], v[120:123], v[84:91], v124\n\t"
    "ds_read_b64 v[84:85], v240 offset:0\n\t"
    "ds_read_b64 v[86:87], v252 offset:0\n\t"
    "ds_read_b64 v[88:89], v241 offset:0\n\t"
    "ds_read_b64 v[90:91], v253 offset:0\n\t"
    "v_lshrrev_b32 v254, 16, v242\n\t"
    "v_and_b32 v242, s92, v242\n\t"
    "v_lshrrev_b32 v255, 16, v243\n\t"
    "v_and_b32 v243, s92, v243\n\t"
    "s_waitcnt vmcnt(6) lgkmcnt(4)\n\t"
    "v_smfmac_f32_16x16x64_bf16 v[172:175], v[120:123], v[92:99], v124\n\t"
    "ds_read_b64 v[92:93], v242 offset:0\n\t"
    "ds_read_b64 v[94:95], v254 offset:0\n\t"
    "ds_read_b64 v[96:97], v243 offset:0\n\t"
    "ds_read_b64 v[98:99], v255 offset:0\n\t"
    "s_add_u32 s84, s84, 0x8000\n\t"
    "buffer_load_dwordx4 v[168:171], v127, %12, s84 offen offset:0 sc1\n\t"
    "v_lshrrev_b32 v252, 16, v220\n\t"
    "v_and_b32 v220, s92, v220\n\t"
    "v_lshrrev_b32 v253, 16, v221\n\t"
    "v_mov_b64 v[176:177], s[70:71]\n\t"
    "v_mov_b64 v[178:179], s[72:73]\n\t"
    "v_and_b32 v221, s92, v221\n\t"
    "s_waitcnt lgkmcnt(4)\n\t"
    "v_smfmac_f32_16x16x64_bf16 v[176:179], v[120:123], v[84:91], v124\n\t"
    "ds_read_b64 v[84:85], v220 offset:0\n\t"
    "ds_read_b64 v[86:87], v252 offset:0\n\t"
    "ds_read_b64 v[88:89], v221 offset:0\n\t"
    "ds_read_b64 v[90:91], v253 offset:0\n\t"
    "v_lshrrev_b32 v254, 16, v222\n\t"
    "v_and_b32 v222, s92, v222\n\t"
    "v_lshrrev_b32 v255, 16, v223\n\t"
    "v_and_b32 v223, s92, v223\n\t"
    "s_mov_b64 exec, 1\n\t"
    "buffer_load_dword v164, off, %12, s93 offset:0 sc1\n\t"
    "s_mov_b64 exec, -1\n\t"
    "s_waitcnt vmcnt(7) lgkmcnt(4)\n\t"
    "v_smfmac_f32_16x16x64_bf16 v[176:179], v[120:123], v[92:99], v124\n\t"
    "ds_read_b64 v[92:93], v222 offset:0\n\t"
    "ds_read_b64 v[94:95], v254 offset:0\n\t"
    "ds_read_b64 v[96:97], v223 offset:0\n\t"
    "ds_read_b64 v[98:99], v255 offset:0\n\t"
    "s_sub_u32 s88, s88, s95\n\t"
    "buffer_load_dwordx4 v[112:115], v127, %12, s88 offen offset:0 sc1\n\t"
    "v_lshrrev_b32 v252, 16, v224\n\t"
    "v_and_b32 v224, s92, v224\n\t"
    "v_lshrrev_b32 v253, 16, v225\n\t"
    "v_mov_b64 v[180:181], s[70:71]\n\t"
    "v_mov_b64 v[182:183], s[72:73]\n\t"
    "v_and_b32 v225, s92, v225\n\t"
    "s_waitcnt vmcnt(5)\n\t"
    "s_mov_b64 exec, 0xffffffff\n\t"
    "buffer_store_dword v165, v125, %12, s96 offen offset:256 sc1\n\t"
    "s_mov_b64 exec, -1\n\t"
    "s_add_u32 s61, s61, 0x2000\n\t"
    "buffer_store_dwordx4 v[52:55], v127, %12, s61 offen sc1\n\t"
    "s_waitcnt lgkmcnt(4)\n\t"
    "v_smfmac_f32_16x16x64_bf16 v[180:183], v[120:123], v[84:91], v124\n\t"
    "ds_read_b64 v[84:85], v224 offset:0\n\t"
    "ds_read_b64 v[86:87], v252 offset:0\n\t"
    "ds_read_b64 v[88:89], v225 offset:0\n\t"
    "ds_read_b64 v[90:91], v253 offset:0\n\t"
    "v_lshrrev_b32 v254, 16, v226\n\t"
    "v_and_b32 v226, s92, v226\n\t"
    "v_lshrrev_b32 v255, 16, v227\n\t"
    "v_and_b32 v227, s92, v227\n\t"
    "s_waitcnt lgkmcnt(4)\n\t"
    "v_smfmac_f32_16x16x64_bf16 v[180:183], v[120:123], v[92:99], v124\n\t"
    "ds_read_b64 v[92:93], v226 offset:0\n\t"
    "ds_read_b64 v[94:95], v254 offset:0\n\t"
    "ds_read_b64 v[96:97], v227 offset:0\n\t"
    "ds_read_b64 v[98:99], v255 offset:0\n\t"
    "buffer_load_dwordx4 v[100:103], v127, %12, s84 offen offset:1024 sc1\n\t"
    "v_lshrrev_b32 v252, 16, v228\n\t"
    "v_and_b32 v228, s92, v228\n\t"
    "v_lshrrev_b32 v253, 16, v229\n\t"
    "v_mov_b64 v[184:185], s[70:71]\n\t"
    "v_mov_b64 v[186:187], s[72:73]\n\t"
    "v_and_b32 v229, s92, v229\n\t"
    "s_waitcnt lgkmcnt(4)\n\t"
    "v_smfmac_f32_16x16x64_bf16 v[184:187], v[120:123], v[84:91], v124\n\t"
    "ds_read_b64 v[84:85], v228 offset:0\n\t"
    "ds_read_b64 v[86:87], v252 offset:0\n\t"
    "ds_read_b64 v[88:89], v229 offset:0\n\t"
    "ds_read_b64 v[90:91], v253 offset:0\n\t"
    "v_lshrrev_b32 v254, 16, v230\n\t"
    "v_and_b32 v230, s92, v230\n\t"
    "v_lshrrev_b32 v255, 16, v231\n\t"
    "v_and_b32 v231, s92, v231\n\t"
    "s_waitcnt lgkmcnt(4)\n\t"
    "v_smfmac_f32_16x16x64_bf16 v[184:187], v[120:123], v[92:99], v124\n\t"
    "ds_read_b64 v[92:93], v230 offset:0\n\t"
    "ds_read_b64 v[94:95], v254 offset:0\n\t"
    "ds_read_b64 v[96:97], v231 offset:0\n\t"
    "ds_read_b64 v[98:99], v255 offset:0\n\t"
    "buffer_load_dwordx4 v[104:107], v127, %12, s88 offen offset:1024 sc1\n\t"
    "v_lshrrev_b32 v252, 16, v232\n\t"
    "v_and_b32 v232, s92, v232\n\t"
    "v_lshrrev_b32 v253, 16, v233\n\t"
    "v_mov_b64 v[188:189], s[70:71]\n\t"
    "v_mov_b64 v[190:191], s[72:73]\n\t"
    "v_and_b32 v233, s92, v233\n\t"
    "s_waitcnt lgkmcnt(4)\n\t"
    "v_smfmac_f32_16x16x64_bf16 v[188:191], v[120:123], v[84:91], v124\n\t"
    "ds_read_b64 v[84:85], v232 offset:0\n\t"
    "ds_read_b64 v[86:87], v252 offset:0\n\t"
    "ds_read_b64 v[88:89], v233 offset:0\n\t"
    "ds_read_b64 v[90:91], v253 offset:0\n\t"
    "v_lshrrev_b32 v254, 16, v234\n\t"
    "v_and_b32 v234, s92, v234\n\t"
    "v_lshrrev_b32 v255, 16, v235\n\t"
    "v_and_b32 v235, s92, v235\n\t"
    "s_waitcnt vmcnt(5)\n\t"
    "v_readfirstlane_b32 s65, v164\n\t"
    "s_nop 0\n\t"
    "s_cmp_eq_u32 s65, 0\n\t"
    "s_cbranch_scc1 L_xw0_%=\n\t"
    "L_xwb0_%=:\n\t"
    "buffer_load_dwordx4 v[4:7], v127, %12, s94 offen sc1\n\t"
    "s_waitcnt lgkmcnt(4)\n\t"
    "v_smfmac_f32_16x16x64_bf16 v[188:191], v[120:123], v[92:99], v124\n\t"
    "ds_read_b64 v[92:93], v234 offset:0\n\t"
    "ds_read_b64 v[94:95], v254 offset:0\n\t"
    "ds_read_b64 v[96:97], v235 offset:0\n\t"
    "ds_read_b64 v[98:99], v255 offset:0\n\t"
    "s_add_u32 s85, s85, 0x8000\n\t"
    "buffer_load_dwordx4 v[108:111], v127, %12, s85 offen offset:0 sc1\n\t"
    "v_lshrrev_b32 v252, 16, v244\n\t"
    "v_and_b32 v244, s92, v244\n\t"
    "v_lshrrev_b32 v253, 16, v245\n\t"
    "v_mov_b64 v[192:193], s[70:71]\n\t"
    "v_mov_b64 v[194:195], s[72:73]\n\t"
    "v_and_b32 v245, s92, v245\n\t"
    "s_waitcnt lgkmcnt(4)\n\t"
    "v_smfmac_f32_16x16x64_bf16 v[192:195], v[120:123], v[84:91], v124\n\t"
    "ds_read_b64 v[84:85], v244 offset:0\n\t"
    "ds_read_b64 v[86:87], v252 offset:0\n\t"
    "ds_read_b64 v[88:89], v245 offset:0\n\t"
    "ds_read_b64 v[90:91], v253 offset:0\n\t"
    "v_lshrrev_b32 v254, 16, v246\n\t"
    "v_and_b32 v246, s92, v246\n\t"
    "v_lshrrev_b32 v255, 16, v247\n\t"
    "v_and_b32 v247, s92, v247\n\t"
    "s_waitcnt lgkmcnt(4)\n\t"
    "v_smfmac_f32_16x16x64_bf16 v[192:195], v[120:123], v[92:99], v124\n\t"
    "ds_read_b64 v[92:93], v246 offset:0\n\t"
    "ds_read_b64 v[94:95], v254 offset:0\n\t"
    "ds_read_b64 v[96:97], v247 offset:0\n\t"
    "ds_read_b64 v[98:99], v255 offset:0\n\t"
    "s_sub_u32 s89, s89, s95\n\t"
    "buffer_load_dwordx4 v[216:219], v127, %12, s89 offen offset:0 sc1\n\t"
    "v_lshrrev_b32 v252, 16, v248\n\t"
    "v_and_b32 v248, s92, v248\n\t"
    "v_lshrrev_b32 v253, 16, v249\n\t"
    "v_mov_b64 v[196:197], s[70:71]\n\t"
    "v_mov_b64 v[198:199], s[72:73]\n\t"
    "v_and_b32 v249, s92, v249\n\t"
    "s_add_u32 s61, s61, 0x2000\n\t"
    "buffer_store_dwordx4 v[60:63], v127, %12, s61 offen sc1\n\t"
    "s_waitcnt lgkmcnt(4)\n\t"
    "v_smfmac_f32_16x16x64_bf16 v[196:199], v[120:123], v[84:91], v124\n\t"
    "ds_read_b64 v[84:85], v248 offset:0\n\t"
    "ds_read_b64 v[86:87], v252 offset:0\n\t"
    "ds_read_b64 v[88:89], v249 offset:0\n\t"
    "ds_read_b64 v[90:91], v253 offset:0\n\t"
    "v_lshrrev_b32 v254, 16, v250\n\t"
    "v_and_b32 v250, s92, v250\n\t"
    "v_lshrrev_b32 v255, 16, v251\n\t"
    "v_and_b32 v251, s92, v251\n\t"
    "s_add_u32 s94, s94, 0x2000\n\t"
    "buffer_load_dwordx4 v[12:15], v127, %12, s94 offen sc1\n\t"
    "s_waitcnt lgkmcnt(4)\n\t"
    "v_smfmac_f32_16x16x64_bf16 v[196:199], v[120:123], v[92:99], v124\n\t"
    "ds_read_b64 v[92:93], v250 offset:0\n\t"
    "ds_read_b64 v[94:95], v254 offset:0\n\t"
    "ds_read_b64 v[96:97], v251 offset:0\n\t"
    "ds_read_b64 v[98:99], v255 offset:0\n\t"
    "buffer_load_dwordx4 v[116:119], v127, %12, s85 offen offset:1024 sc1\n\t"
    "v_lshrrev_b32 v252, 16, v168\n\t"
    "v_and_b32 v168, s92, v168\n\t"
    "v_lshrrev_b32 v253, 16, v169\n\t"
    "v_mov_b64 v[200:201], s[70:71]\n\t"
    "v_mov_b64 v[202:203], s[72:73]\n\t"
    "v_and_b32 v169, s92, v169\n\t"
    "s_waitcnt lgkmcnt(4)\n\t"
    "v_smfmac_f32_16x16x64_bf16 v[200:203], v[120:123], v[84:91], v124\n\t"
    "ds_read_b64 v[84:85], v168 offset:64000\n\t"
    "ds_read_b64 v[86:87], v252 offset:64000\n\t"
    "ds_read_b64 v[88:89], v169 offset:64000\n\t"
    "ds_read_b64 v[90:91], v253 offset:64000\n\t"
    "v_lshrrev_b32 v254, 16, v170\n\t"
    "v_and_b32 v170, s92, v170\n\t"
    "v_lshrrev_b32 v255, 16, v171\n\t"
    "v_and_b32 v171, s92, v171\n\t"
    "s_waitcnt vmcnt(10) lgkmcnt(4)\n\t"
    "v_smfmac_f32_16x16x64_bf16 v[200:203], v[120:123], v[92:99], v124\n\t"
    "ds_read_b64 v[92:93], v170 offset:64000\n\t"
    "ds_read_b64 v[94:95], v254 offset:64000\n\t"
    "ds_read_b64 v[96:97], v171 offset:64000\n\t"
    "ds_read_b64 v[98:99], v255 offset:64000\n\t"
    "buffer_load_dwordx4 v[240:243], v127, %12, s89 offen offset:1024 sc1\n\t"
    "v_lshrrev_b32 v252, 16, v112\n\t"
    "v_and_b32 v112, s92, v112\n\t"
    "v_lshrrev_b32 v253, 16, v113\n\t"
    "v_and_b32 v113, s92, v113\n\t"
    "s_waitcnt lgkmcnt(4)\n\t"
    "v_smfmac_f32_16x16x64_bf16 v[172:175], v[120:123], v[84:91], v124\n\t"
    "ds_read_b64 v[84:85], v112 offset:0\n\t"
    "ds_read_b64 v[86:87], v252 offset:0\n\t"
    "ds_read_b64 v[88:89], v113 offset:0\n\t"
    "ds_read_b64 v[90:91], v253 offset:0\n\t"
    "v_lshrrev_b32 v254, 16, v114\n\t"
    "v_and_b32 v114, s92, v114\n\t"
    "v_lshrrev_b32 v255, 16, v115\n\t"
    "v_and_b32 v115, s92, v115\n\t"
    "s_waitcnt vmcnt(6)\n\t"
    "v_smfmac_f32_16x16x64_bf16 v[172:175], v[156:159], v[0:7], v124\n\t"
    "s_waitcnt lgkmcnt(4)\n\t"
    "v_smfmac_f32_16x16x64_bf16 v[172:175], v[120:123], v[92:99], v124\n\t"
    "ds_read_b64 v[92:93], v114 offset:0\n\t"
    "ds_read_b64 v[94:95], v254 offset:0\n\t"
    "ds_read_b64 v[96:97], v115 offset:0\n\t"
    "ds_read_b64 v[98:99], v255 offset:0\n\t"
    "s_add_u32 s86, s86, 0x8000\n\t"
    "buffer_load_dwordx4 v[220:223], v127, %12, s86 offen offset:0 sc1\n\t"
    "v_lshrrev_b32 v252, 16, v100\n\t"
    "v_and_b32 v100, s92, v100\n\t"
    "v_lshrrev_b32 v253, 16, v101\n\t"
    "v_mov_b64 v[204:205], s[70:71]\n\t"
    "v_mov_b64 v[206:207], s[72:73]\n\t"
    "v_and_b32 v101, s92, v101\n\t"
    "v_smfmac_f32_16x16x64_bf16 v[176:179], v[160:163], v[0:7], v124\n\t"
    "v_pk_mul_f32 v[64:65], v[172:173], v[172:173]\n\t"
    "v_pk_mul_f32 v[66:67], v[174:175], v[174:175]\n\t"
    "v_pk_fma_f32 v[64:65], v[64:65], v[82:83], v[80:81]\n\t"
    "v_pk_fma_f32 v[66:67], v[66:67], v[82:83], v[80:81]\n\t"
    "v_pk_mul_f32 v[64:65], v[64:65], v[172:173]\n\t"
    "v_pk_mul_f32 v[66:67], v[66:67], v[174:175]\n\t"
    "v_exp_f32 v68, v64\n\t"
    "v_exp_f32 v69, v65\n\t"
    "s_waitcnt lgkmcnt(4)\n\t"
    "v_smfmac_f32_16x16x64_bf16 v[204:207], v[120:123], v[84:91], v124\n\t"
    "ds_read_b64 v[84:85], v100 offset:64000\n\t"
    "ds_read_b64 v[86:87], v252 offset:64000\n\t"
    "ds_read_b64 v[88:89], v101 offset:64000\n\t"
    "ds_read_b64 v[90:91], v253 offset:64000\n\t"
    "v_lshrrev_b32 v254, 16, v102\n\t"
    "v_and_b32 v102, s92, v102\n\t"
    "v_lshrrev_b32 v255, 16, v103\n\t"
    "v_and_b32 v103, s92, v103\n\t"
    "s_mov_b64 exec, 1\n\t"
    "buffer_load_dword v164, off, %12, s93 offset:128 sc1\n\t"
    "s_mov_b64 exec, -1\n\t"
    "v_exp_f32 v70, v66\n\t"
    "v_exp_f32 v71, v67\n\t"
    "v_pk_fma_f32 v[68:69], v[68:69], s[40:41], s[40:41]\n\t"
    "v_pk_fma_f32 v[70:71], v[70:71], s[42:43], s[42:43]\n\t"
    "v_rcp_f32 v68, v68\n\t"
    "v_rcp_f32 v69, v69\n\t"
    "v_rcp_f32 v70, v70\n\t"
    "v_rcp_f32 v71, v71\n\t"
    "s_cmp_lt_u32 %16, 4\n\t"
    "s_cbranch_scc1 L_tp227_%=\n\t"
    "s_setprio 0\n\t"
    "L_tp227_%=:\n\t"
    "s_waitcnt lgkmcnt(4)\n\t"
    "v_smfmac_f32_16x16x64_bf16 v[204:207], v[120:123], v[92:99], v124\n\t"
    "ds_read_b64 v[92:93], v102 offset:64000\n\t"
    "ds_read_b64 v[94:95], v254 offset:64000\n\t"
    "ds_read_b64 v[96:97], v103 offset:64000\n\t"
    "ds_read_b64 v[98:99], v255 offset:64000\n\t"
    "s_sub_u32 s90, s90, s95\n\t"
    "buffer_load_dwordx4 v[224:227], v127, %12, s90 offen offset:0 sc1\n\t"
    "v_lshrrev_b32 v252, 16, v104\n\t"
    "v_and_b32 v104, s92, v104\n\t"
    "v_lshrrev_b32 v253, 16, v105\n\t"
    "v_and_b32 v105, s92, v105\n\t"
    "s_waitcnt vmcnt(6)\n\t"
    "s_mov_b64 exec, 0xffffffff\n\t"
    "buffer_store_dword v165, v125, %12, s96 offen offset:384 sc1\n\t"
    "s_mov_b64 exec, -1\n\t"
    "v_pk_mul_f32 v[68:69], v[68:69], v[172:173]\n\t"
    "v_pk_fma_f32 v[68:69], v[70:71], v[174:175], v[68:69]\n\t"
    "v_add_f32 v236, v68, v69\n\t"
    "s_waitcnt lgkmcnt(4)\n\t"
    "v_smfmac_f32_16x16x64_bf16 v[176:179], v[120:123], v[84:91], v124\n\t"
    "ds_read_b64 v[84:85], v104 offset:0\n\t"
    "ds_read_b64 v[86:87], v252 offset:0\n\t"
    "ds_read_b64 v[88:89], v105 offset:0\n\t"
    "ds_read_b64 v[90:91], v253 offset:0\n\t"
    "v_lshrrev_b32 v254, 16, v106\n\t"
    "v_and_b32 v106, s92, v106\n\t"
    "v_lshrrev_b32 v255, 16, v107\n\t"
    "v_and_b32 v107, s92, v107\n\t"
    "s_waitcnt vmcnt(6)\n\t"
    "v_smfmac_f32_16x16x64_bf16 v[180:183], v[156:159], v[8:15], v124\n\t"
    "v_lshrrev_b32 v166, 7, v127\n\t"
    "v_and_b32 v167, 0x70, v127\n\t"
    "v_lshl_add_u32 v126, v166, 8, v167\n\t"
    "v_add_u32 v126, 128, v126\n\t"
    "s_bfe_u32 s46, %14, 0x30003\n\t"
    "s_min_u32 s46, s46, 6\n\t"
    "s_lshl_b32 s69, s46, 3\n\t"
    "s_add_u32 s69, s69, %16\n\t"
    "v_lshrrev_b32 v147, 4, v127\n\t"
    "v_add_u32 v148, 140288, v127\n\t"
    "s_mov_b32 s46, 48244736\n\t"
    "buffer_load_dwordx4 v[128:131], v127, %12, s46 offen sc1\n\t"
    "s_lshl_b32 s65, %14, 12\n\t"
    "s_add_u32 s65, s65, 16777216\n\t"
    "buffer_load_dwordx4 v[0:3], v127, %12, s65 offen offset:0 sc1\n\t"
    "s_waitcnt lgkmcnt(4)\n\t"
    "v_smfmac_f32_16x16x64_bf16 v[176:179], v[120:123], v[92:99], v124\n\t"
    "ds_read_b64 v[92:93], v106 offset:0\n\t"
    "ds_read_b64 v[94:95], v254 offset:0\n\t"
    "ds_read_b64 v[96:97], v107 offset:0\n\t"
    "ds_read_b64 v[98:99], v255 offset:0\n\t"
    "buffer_load_dwordx4 v[228:231], v127, %12, s86 offen offset:1024 sc1\n\t"
    "v_lshrrev_b32 v252, 16, v108\n\t"
    "v_and_b32 v108, s92, v108\n\t"
    "v_lshrrev_b32 v253, 16, v109\n\t"
    "v_mov_b64 v[208:209], s[70:71]\n\t"
    "v_mov_b64 v[210:211], s[72:73]\n\t"
    "v_and_b32 v109, s92, v109\n\t"
    "v_smfmac_f32_16x16x64_bf16 v[184:187], v[160:163], v[8:15], v124\n\t"
    "v_pk_mul_f32 v[72:73], v[176:177], v[176:177]\n\t"
    "v_pk_mul_f32 v[74:75], v[178:179], v[178:179]\n\t"
    "v_pk_fma_f32 v[72:73], v[72:73], v[82:83], v[80:81]\n\t"
    "v_pk_fma_f32 v[74:75], v[74:75], v[82:83], v[80:81]\n\t"
    "v_pk_mul_f32 v[72:73], v[72:73], v[176:177]\n\t"
    "v_pk_mul_f32 v[74:75], v[74:75], v[178:179]\n\t"
    "v_exp_f32 v76, v72\n\t"
    "v_exp_f32 v77, v73\n\t"
    "s_lshl_b32 s65, %14, 12\n\t"
    "s_add_u32 s65, s65, 16777216\n\t"
    "buffer_load_dwordx4 v[4:7], v127, %12, s65 offen offset:1024 sc1\n\t"
    "s_waitcnt lgkmcnt(4)\n\t"
    "v_smfmac_f32_16x16x64_bf16 v[208:211], v[120:123], v[84:91], v124\n\t"
    "ds_read_b64 v[84:85], v108 offset:64000\n\t"
    "ds_read_b64 v[86:87], v252 offset:64000\n\t"
    "ds_read_b64 v[88:89], v109 offset:64000\n\t"
    "ds_read_b64 v[90:91], v253 offset:64000\n\t"
    "v_lshrrev_b32 v254, 16, v110\n\t"
    "v_and_b32 v110, s92, v110\n\t"
    "v_lshrrev_b32 v255, 16, v111\n\t"
    "v_and_b32 v111, s92, v111\n\t"
    "v_exp_f32 v78, v74\n\t"
    "v_exp_f32 v79, v75\n\t"
    "v_pk_fma_f32 v[76:77], v[76:77], s[40:41], s[40:41]\n\t"
    "v_pk_fma_f32 v[78:79], v[78:79], s[42:43], s[42:43]\n\t"
    "v_rcp_f32 v76, v76\n\t"
    "v_rcp_f32 v77, v77\n\t"
    "v_rcp_f32 v78, v78\n\t"
    "v_rcp_f32 v79, v79\n\t"
    "s_lshl_b32 s65, %14, 12\n\t"
    "s_add_u32 s65, s65, 16777216\n\t"
    "buffer_load_dwordx4 v[8:11], v127, %12, s65 offen offset:2048 sc1\n\t"
    "s_waitcnt lgkmcnt(4)\n\t"
    "v_smfmac_f32_16x16x64_bf16 v[208:211], v[120:123], v[92:99], v124\n\t"
    "ds_read_b64 v[92:93], v110 offset:64000\n\t"
    "ds_read_b64 v[94:95], v254 offset:64000\n\t"
    "ds_read_b64 v[96:97], v111 offset:64000\n\t"
    "ds_read_b64 v[98:99], v255 offset:64000\n\t"
    "buffer_load_dwordx4 v[232:235], v127, %12, s90 offen offset:1024 sc1\n\t"
    "v_lshrrev_b32 v252, 16, v216\n\t"
    "v_and_b32 v216, s92, v216\n\t"
    "v_lshrrev_b32 v253, 16, v217\n\t"
    "v_and_b32 v217, s92, v217\n\t"
    "v_pk_mul_f32 v[76:77], v[76:77], v[176:177]\n\t"
    "v_pk_fma_f32 v[76:77], v[78:79], v[178:179], v[76:77]\n\t"
    "v_add_f32 v237, v76, v77\n\t"
    "s_lshl_b32 s65, %14, 12\n\t"
    "s_add_u32 s65, s65, 16777216\n\t"
    "buffer_load_dwordx4 v[12:15], v127, %12, s65 offen offset:3072 sc1\n\t"
    "s_waitcnt lgkmcnt(4)\n\t"
    "v_smfmac_f32_16x16x64_bf16 v[180:183], v[120:123], v[84:91], v124\n\t"
    "ds_read_b64 v[84:85], v216 offset:0\n\t"
    "ds_read_b64 v[86:87], v252 offset:0\n\t"
    "ds_read_b64 v[88:89], v217 offset:0\n\t"
    "ds_read_b64 v[90:91], v253 offset:0\n\t"
    "v_lshrrev_b32 v254, 16, v218\n\t"
    "v_and_b32 v218, s92, v218\n\t"
    "v_lshrrev_b32 v255, 16, v219\n\t"
    "v_and_b32 v219, s92, v219\n\t"
    "s_waitcnt vmcnt(9)\n\t"
    "v_readfirstlane_b32 s65, v164\n\t"
    "s_nop 0\n\t"
    "s_cmp_eq_u32 s65, 0\n\t"
    "s_cbranch_scc1 L_xw1_%=\n\t"
    "L_xwb1_%=:\n\t"
    "s_add_u32 s94, s94, 0x2000\n\t"
    "buffer_load_dwordx4 v[20:23], v127, %12, s94 offen sc1\n\t"
    "s_waitcnt lgkmcnt(4)\n\t"
    "v_smfmac_f32_16x16x64_bf16 v[180:183], v[120:123], v[92:99], v124\n\t"
    "ds_read_b64 v[92:93], v218 offset:0\n\t"
    "ds_read_b64 v[94:95], v254 offset:0\n\t"
    "ds_read_b64 v[96:97], v219 offset:0\n\t"
    "ds_read_b64 v[98:99], v255 offset:0\n\t"
    "s_add_u32 s87, s87, 0x8000\n\t"
    "buffer_load_dwordx4 v[244:247], v127, %12, s87 offen offset:0 sc1\n\t"
    "v_lshrrev_b32 v252, 16, v116\n\t"
    "v_and_b32 v116, s92, v116\n\t"
    "v_lshrrev_b32 v253, 16, v117\n\t"
    "v_mov_b64 v[212:213], s[70:71]\n\t"
    "v_mov_b64 v[214:215], s[72:73]\n\t"
    "v_and_b32 v117, s92, v117\n\t"
    "v_pk_mul_f32 v[64:65], v[180:181], v[180:181]\n\t"
    "v_pk_mul_f32 v[66:67], v[182:183], v[182:183]\n\t"
    "v_pk_fma_f32 v[64:65], v[64:65], v[82:83], v[80:81]\n\t"
    "v_pk_fma_f32 v[66:67], v[66:67], v[82:83], v[80:81]\n\t"
    "v_pk_mul_f32 v[64:65], v[64:65], v[180:181]\n\t"
    "v_pk_mul_f32 v[66:67], v[66:67], v[182:183]\n\t"
    "v_exp_f32 v68, v64\n\t"
    "v_exp_f32 v69, v65\n\t"
    "s_waitcnt lgkmcnt(4)\n\t"
    "v_smfmac_f32_16x16x64_bf16 v[212:215], v[120:123], v[84:91], v124\n\t"
    "ds_read_b64 v[84:85], v116 offset:64000\n\t"
    "ds_read_b64 v[86:87], v252 offset:64000\n\t"
    "ds_read_b64 v[88:89], v117 offset:64000\n\t"
    "ds_read_b64 v[90:91], v253 offset:64000\n\t"
    "v_lshrrev_b32 v254, 16, v118\n\t"
    "v_and_b32 v118, s92, v118\n\t"
    "v_lshrrev_b32 v255, 16, v119\n\t"
    "v_and_b32 v119, s92, v119\n\t"
    "v_exp_f32 v70, v66\n\t"
    "v_exp_f32 v71, v67\n\t"
    "v_pk_fma_f32 v[68:69], v[68:69], s[40:41], s[40:41]\n\t"
    "v_pk_fma_f32 v[70:71], v[70:71], s[42:43], s[42:43]\n\t"
    "v_rcp_f32 v68, v68\n\t"
    "v_rcp_f32 v69, v69\n\t"
    "v_rcp_f32 v70, v70\n\t"
    "v_rcp_f32 v71, v71\n\t"
    "s_waitcnt lgkmcnt(4)\n\t"
    "v_smfmac_f32_16x16x64_bf16 v[212:215], v[120:123], v[92:99], v124\n\t"
    "ds_read_b64 v[92:93], v118 offset:64000\n\t"
    "ds_read_b64 v[94:95], v254 offset:64000\n\t"
    "ds_read_b64 v[96:97], v119 offset:64000\n\t"
    "ds_read_b64 v[98:99], v255 offset:64000\n\t"
    "s_sub_u32 s91, s91, s95\n\t"
    "buffer_load_dwordx4 v[248:251], v127, %12, s91 offen offset:0 sc1\n\t"
    "v_lshrrev_b32 v252, 16, v240\n\t"
    "v_and_b32 v240, s92, v240\n\t"
    "v_lshrrev_b32 v253, 16, v241\n\t"
    "v_and_b32 v241, s92, v241\n\t"
    "v_pk_mul_f32 v[68:69], v[68:69], v[180:181]\n\t"
    "v_pk_fma_f32 v[68:69], v[70:71], v[182:183], v[68:69]\n\t"
    "v_add_f32 v238, v68, v69\n\t"
    "s_waitcnt lgkmcnt(4)\n\t"
    "v_smfmac_f32_16x16x64_bf16 v[184:187], v[120:123], v[84:91], v124\n\t"
    "ds_read_b64 v[84:85], v240 offset:0\n\t"
    "ds_read_b64 v[86:87], v252 offset:0\n\t"
    "ds_read_b64 v[88:89], v241 offset:0\n\t"
    "ds_read_b64 v[90:91], v253 offset:0\n\t"
    "v_lshrrev_b32 v254, 16, v242\n\t"
    "v_and_b32 v242, s92, v242\n\t"
    "v_lshrrev_b32 v255, 16, v243\n\t"
    "v_and_b32 v243, s92, v243\n\t"
    "s_add_u32 s94, s94, 0x2000\n\t"
    "buffer_load_dwordx4 v[28:31], v127, %12, s94 offen sc1\n\t"
    "s_waitcnt vmcnt(10)\n\t"
    "s_cmp_lg_u32 %16, 0\n\t"
    "s_cbranch_scc1 L_bend_%=\n\t"
    "s_mov_b32 s47, 1048576\n\t"
    "L_bchk_%=:\n\t"
    "v_min_u32 v132, v128, v129\n\t"
    "v_min3_u32 v132, v132, v130, v131\n\t"
    "v_cmp_eq_u32 vcc, 0, v132\n\t"
    "s_cbranch_vccz L_bok_%=\n\t"
    "s_sleep 16\n\t"
    "s_mov_b32 s46, 48244736\n\t"
    "buffer_load_dwordx4 v[128:131], v127, %12, s46 offen sc1\n\t"
    "s_sub_u32 s47, s47, 1\n\t"
    "s_waitcnt vmcnt(0)\n\t"
    "s_cmp_lg_u32 s47, 0\n\t"
    "s_cbranch_scc1 L_bchk_%=\n\t"
    "L_bok_%=:\n\t"
    "v_readfirstlane_b32 s46, v128\n\t"
    "v_lshrrev_b32 v135, 2, v127\n\t"
    "v_mov_b32 v138, -4096\n\t"
    "v_mov_b32 v139, 4096\n\t"
    "v_subrev_u32 v128, s46, v128\n\t"
    "v_subrev_u32 v129, s46, v129\n\t"
    "v_subrev_u32 v130, s46, v130\n\t"
    "v_subrev_u32 v131, s46, v131\n\t"
    "v_add3_u32 v132, v128, v129, v130\n\t"
    "v_add_u32 v132, v132, v131\n\t"
    "s_nop 1\n\t"
    "v_add_u32_dpp v132, v132, v132 row_shr:1 row_mask:0xf bank_mask:0xf\n\t"
    "s_nop 1\n\t"
    "v_add_u32_dpp v132, v132, v132 row_shr:2 row_mask:0xf bank_mask:0xf\n\t"
    "s_nop 1\n\t"
    "v_add_u32_dpp v132, v132, v132 row_shr:4 row_mask:0xf bank_mask:0xf\n\t"
    "s_nop 1\n\t"
    "v_add_u32_dpp v132, v132, v132 row_shr:8 row_mask:0xf bank_mask:0xf\n\t"
    "s_nop 1\n\t"
    "v_add_u32_dpp v132, v132, v132 row_bcast:15 row_mask:0xa bank_mask:0xf\n\t"
    "s_nop 1\n\t"
    "v_add_u32_dpp v132, v132, v132 row_bcast:31 row_mask:0xc bank_mask:0xf\n\t"
    "s_nop 0\n\t"
    "v_readlane_b32 s46, v132, 63\n\t"
    "s_movk_i32 s47, 19\n\t"
    "v_mov_b32 v134, 600\n\t"
    "s_ashr_i32 s46, s46, 8\n\t"
    "v_sub_u32 v128, s46, v128\n\t"
    "v_med3_i32 v128, v128, v138, v139\n\t"
    "v_mad_i32_i24 v128, v128, s47, v134\n\t"
    "v_ashrrev_i32 v128, 10, v128\n\t"
    "v_med3_i32 v128, v128, -8, 3\n\t"
    "v_sub_u32 v129, s46, v129\n\t"
    "v_med3_i32 v129, v129, v138, v139\n\t"
    "v_mad_i32_i24 v129, v129, s47, v134\n\t"
    "v_ashrrev_i32 v129, 10, v129\n\t"
    "v_med3_i32 v129, v129, -8, 3\n\t"
    "v_sub_u32 v130, s46, v130\n\t"
    "v_med3_i32 v130, v130, v138, v139\n\t"
    "v_mad_i32_i24 v130, v130, s47, v134\n\t"
    "v_ashrrev_i32 v130, 10, v130\n\t"
    "v_med3_i32 v130, v130, -8, 3\n\t"
    "v_sub_u32 v131, s46, v131\n\t"
    "v_med3_i32 v131, v131, v138, v139\n\t"
    "v_mad_i32_i24 v131, v131, s47, v134\n\t"
    "v_ashrrev_i32 v131, 10, v131\n\t"
    "v_med3_i32 v131, v131, -8, 3\n\t"
    "v_add3_u32 v132, v128, v129, v130\n\t"
    "v_add_u32 v132, v132, v131\n\t"
    "v_mov_b32 v133, 0\n\t"
    "v_mov_b32 v134, 0\n\t"
    "v_cmp_gt_u32 vcc, %14, v135\n\t"
    "v_cmp_ge_u32 s[46:47], %14, v135\n\t"
    "v_add_u32 v135, 1, v135\n\t"
    "v_cndmask_b32 v136, 0, v128, vcc\n\t"
    "v_cndmask_b32 v137, 0, v128, s[46:47]\n\t"
    "v_add_u32 v133, v133, v136\n\t"
    "v_add_u32 v134, v134, v137\n\t"
    "v_cmp_gt_u32 vcc, %14, v135\n\t"
    "v_cmp_ge_u32 s[46:47], %14, v135\n\t"
    "v_add_u32 v135, 1, v135\n\t"
    "v_cndmask_b32 v136, 0, v129, vcc\n\t"
    "v_cndmask_b32 v137, 0, v129, s[46:47]\n\t"
    "v_add_u32 v133, v133, v136\n\t"
    "v_add_u32 v134, v134, v137\n\t"
    "v_cmp_gt_u32 vcc, %14, v135\n\t"
    "v_cmp_ge_u32 s[46:47], %14, v135\n\t"
    "v_add_u32 v135, 1, v135\n\t"
    "v_cndmask_b32 v136, 0, v130, vcc\n\t"
    "v_cndmask_b32 v137, 0, v130, s[46:47]\n\t"
    "v_add_u32 v133, v133, v136\n\t"
    "v_add_u32 v134, v134, v137\n\t"
    "v_cmp_gt_u32 vcc, %14, v135\n\t"
    "v_cmp_ge_u32 s[46:47], %14, v135\n\t"
    "v_add_u32 v135, 1, v135\n\t"
    "v_cndmask_b32 v136, 0, v131, vcc\n\t"
    "v_cndmask_b32 v137, 0, v131, s[46:47]\n\t"
    "v_add_u32 v133, v133, v136\n\t"
    "v_add_u32 v134, v134, v137\n\t"
    "s_nop 0\n\t"
    "v_add_u32_dpp v132, v132, v132 row_shr:1 row_mask:0xf bank_mask:0xf\n\t"
    "v_add_u32_dpp v133, v133, v133 row_shr:1 row_mask:0xf bank_mask:0xf\n\t"
    "v_add_u32_dpp v134, v134, v134 row_shr:1 row_mask:0xf bank_mask:0xf\n\t"
    "v_add_u32_dpp v132, v132, v132 row_shr:2 row_mask:0xf bank_mask:0xf\n\t"
    "v_add_u32_dpp v133, v133, v133 row_shr:2 row_mask:0xf bank_mask:0xf\n\t"
    "v_add_u32_dpp v134, v134, v134 row_shr:2 row_mask:0xf bank_mask:0xf\n\t"
    "v_add_u32_dpp v132, v132, v132 row_shr:4 row_mask:0xf bank_mask:0xf\n\t"
    "v_add_u32_dpp v133, v133, v133 row_shr:4 row_mask:0xf bank_mask:0xf\n\t"
    "v_add_u32_dpp v134, v134, v134 row_shr:4 row_mask:0xf bank_mask:0xf\n\t"
    "v_add_u32_dpp v132, v132, v132 row_shr:8 row_mask:0xf bank_mask:0xf\n\t"
    "v_add_u32_dpp v133, v133, v133 row_shr:8 row_mask:0xf bank_mask:0xf\n\t"
    "v_add_u32_dpp v134, v134, v134 row_shr:8 row_mask:0xf bank_mask:0xf\n\t"
    "v_add_u32_dpp v132, v132, v132 row_bcast:15 row_mask:0xa bank_mask:0xf\n\t"
    "v_add_u32_dpp v133, v133, v133 row_bcast:15 row_mask:0xa bank_mask:0xf\n\t"
    "v_add_u32_dpp v134, v134, v134 row_bcast:15 row_mask:0xa bank_mask:0xf\n\t"
    "v_add_u32_dpp v132, v132, v132 row_bcast:31 row_mask:0xc bank_mask:0xf\n\t"
    "v_add_u32_dpp v133, v133, v133 row_bcast:31 row_mask:0xc bank_mask:0xf\n\t"
    "v_add_u32_dpp v134, v134, v134 row_bcast:31 row_mask:0xc bank_mask:0xf\n\t"
    "v_readlane_b32 s46, v132, 63\n\t"
    "v_readlane_b32 s47, v133, 63\n\t"
    "v_readlane_b32 s48, v134, 63\n\t"
    "s_add_u32 s44, %14, 1\n\t"
    "s_mul_i32 s49, %14, s46\n\t"
    "s_ashr_i32 s49, s49, 8\n\t"
    "s_sub_u32 s47, s47, s49\n\t"
    "s_lshl_b32 s47, s47, 2\n\t"
    "s_mul_i32 s49, s44, s46\n\t"
    "s_ashr_i32 s49, s49, 8\n\t"
    "s_sub_u32 s48, s48, s49\n\t"
    "s_lshl_b32 s48, s48, 2\n\t"
    "s_add_u32 s46, s46, 256\n\t"
    "s_cmp_le_u32 s46, 512\n\t"
    "s_cselect_b32 s47, s47, 0\n\t"
    "s_cselect_b32 s48, s48, 0\n\t"
    "s_lshl_b32 s49, %14, 6\n\t"
    "s_add_u32 s47, s47, s49\n\t"
    "s_add_u32 s48, s48, s49\n\t"
    "s_add_u32 s48, s48, 64\n\t"
    "v_mov_b32 v132, s47\n\t"
    "v_mov_b32 v133, s48\n\t"
    "v_mov_b32 v134, 138896\n\t"
    "ds_write_b64 v134, v[132:133]\n\t"
    "L_bend_%=:\n\t"
    "s_waitcnt lgkmcnt(4)\n\t"
    "v_smfmac_f32_16x16x64_bf16 v[184:187], v[120:123], v[92:99], v124\n\t"
    "ds_read_b64 v[92:93], v242 offset:0\n\t"
    "ds_read_b64 v[94:95], v254 offset:0\n\t"
    "ds_read_b64 v[96:97], v243 offset:0\n\t"
    "ds_read_b64 v[98:99], v255 offset:0\n\t"
    "buffer_load_dwordx4 v[168:171], v127, %12, s87 offen offset:1024 sc1\n\t"
    "v_lshrrev_b32 v252, 16, v220\n\t"
    "v_and_b32 v220, s92, v220\n\t"
    "v_lshrrev_b32 v253, 16, v221\n\t"
    "v_mov_b64 v[216:217], s[70:71]\n\t"
    "v_mov_b64 v[218:219], s[72:73]\n\t"
    "v_and_b32 v221, s92, v221\n\t"
    "v_pk_mul_f32 v[72:73], v[184:185], v[184:185]\n\t"
    "v_pk_mul_f32 v[74:75], v[186:187], v[186:187]\n\t"
    "v_pk_fma_f32 v[72:73], v[72:73], v[82:83], v[80:81]\n\t"
    "v_pk_fma_f32 v[74:75], v[74:75], v[82:83], v[80:81]\n\t"
    "v_pk_mul_f32 v[72:73], v[72:73], v[184:185]\n\t"
    "v_pk_mul_f32 v[74:75], v[74:75], v[186:187]\n\t"
    "v_exp_f32 v76, v72\n\t"
    "v_exp_f32 v77, v73\n\t"
    "s_waitcnt lgkmcnt(4)\n\t"
    "v_smfmac_f32_16x16x64_bf16 v[216:219], v[120:123], v[84:91], v124\n\t"
    "ds_read_b64 v[84:85], v220 offset:64000\n\t"
    "ds_read_b64 v[86:87], v252 offset:64000\n\t"
    "ds_read_b64 v[88:89], v221 offset:64000\n\t"
    "ds_read_b64 v[90:91], v253 offset:64000\n\t"
    "v_lshrrev_b32 v254, 16, v222\n\t"
    "v_and_b32 v222, s92, v222\n\t"
    "v_lshrrev_b32 v255, 16, v223\n\t"
    "v_and_b32 v223, s92, v223\n\t"
    "v_exp_f32 v78, v74\n\t"
    "v_exp_f32 v79, v75\n\t"
    "v_pk_fma_f32 v[76:77], v[76:77], s[40:41], s[40:41]\n\t"
    "v_pk_fma_f32 v[78:79], v[78:79], s[42:43], s[42:43]\n\t"
    "v_rcp_f32 v76, v76\n\t"
    "v_rcp_f32 v77, v77\n\t"
    "v_rcp_f32 v78, v78\n\t"
    "v_rcp_f32 v79, v79\n\t"
    "s_cmp_lt_u32 %16, 4\n\t"
    "s_cbranch_scc1 L_tp239_%=\n\t"
    "s_setprio 1\n\t"
    "L_tp239_%=:\n\t"
    "s_waitcnt lgkmcnt(4)\n\t"
    "v_smfmac_f32_16x16x64_bf16 v[216:219], v[120:123], v[92:99], v124\n\t"
    "ds_read_b64 v[92:93], v222 offset:64000\n\t"
    "ds_read_b64 v[94:95], v254 offset:64000\n\t"
    "ds_read_b64 v[96:97], v223 offset:64000\n\t"
    "ds_read_b64 v[98:99], v255 offset:64000\n\t"
    "buffer_load_dwordx4 v[112:115], v127, %12, s91 offen offset:1024 sc1\n\t"
    "v_lshrrev_b32 v252, 16, v224\n\t"
    "v_and_b32 v224, s92, v224\n\t"
    "v_lshrrev_b32 v253, 16, v225\n\t"
    "v_and_b32 v225, s92, v225\n\t"
    "v_pk_mul_f32 v[76:77], v[76:77], v[184:185]\n\t"
    "v_pk_fma_f32 v[76:77], v[78:79], v[186:187], v[76:77]\n\t"
    "v_add_f32 v239, v76, v77\n\t"
    "s_waitcnt lgkmcnt(4)\n\t"
    "v_smfmac_f32_16x16x64_bf16 v[188:191], v[120:123], v[84:91], v124\n\t"
    "ds_read_b64 v[84:85], v224 offset:0\n\t"
    "ds_read_b64 v[86:87], v252 offset:0\n\t"
    "ds_read_b64 v[88:89], v225 offset:0\n\t"
    "ds_read_b64 v[90:91], v253 offset:0\n\t"
    "v_lshrrev_b32 v254, 16, v226\n\t"
    "v_and_b32 v226, s92, v226\n\t"
    "v_lshrrev_b32 v255, 16, v227\n\t"
    "v_and_b32 v227, s92, v227\n\t"
    "s_waitcnt vmcnt(5)\n\t"
    "v_smfmac_f32_16x16x64_bf16 v[188:191], v[156:159], v[16:23], v124\n\t"
    "s_waitcnt lgkmcnt(4)\n\t"
    "v_smfmac_f32_16x16x64_bf16 v[188:191], v[120:123], v[92:99], v124\n\t"
    "ds_read_b64 v[92:93], v226 offset:0\n\t"
    "ds_read_b64 v[94:95], v254 offset:0\n\t"
    "ds_read_b64 v[96:97], v227 offset:0\n\t"
    "ds_read_b64 v[98:99], v255 offset:0\n\t"
    "s_add_u32 s88, s88, 0x8000\n\t"
    "buffer_load_dwordx4 v[100:103], v127, %12, s88 offen offset:0 sc1\n\t"
    "v_lshrrev_b32 v252, 16, v228\n\t"
    "v_and_b32 v228, s92, v228\n\t"
    "v_lshrrev_b32 v253, 16, v229\n\t"
    "v_mov_b64 v[220:221], s[70:71]\n\t"
    "v_mov_b64 v[222:223], s[72:73]\n\t"
    "v_and_b32 v229, s92, v229\n\t"
    "v_smfmac_f32_16x16x64_bf16 v[192:195], v[160:163], v[16:23], v124\n\t"
    "v_pk_mul_f32 v[64:65], v[188:189], v[188:189]\n\t"
    "v_pk_mul_f32 v[66:67], v[190:191], v[190:191]\n\t"
    "v_pk_fma_f32 v[64:65], v[64:65], v[82:83], v[80:81]\n\t"
    "v_pk_fma_f32 v[66:67], v[66:67], v[82:83], v[80:81]\n\t"
    "v_pk_mul_f32 v[64:65], v[64:65], v[188:189]\n\t"
    "v_pk_mul_f32 v[66:67], v[66:67], v[190:191]\n\t"
    "v_exp_f32 v68, v64\n\t"
    "v_exp_f32 v69, v65\n\t"
    "ds_read_b128 v[16:19], v148 offset:4096\n\t"
    "s_waitcnt lgkmcnt(5)\n\t"
    "v_smfmac_f32_16x16x64_bf16 v[220:223], v[120:123], v[84:91], v124\n\t"
    "ds_read_b64 v[84:85], v228 offset:64000\n\t"
    "ds_read_b64 v[86:87], v252 offset:64000\n\t"
    "ds_read_b64 v[88:89], v229 offset:64000\n\t"
    "ds_read_b64 v[90:91], v253 offset:64000\n\t"
    "v_lshrrev_b32 v254, 16, v230\n\t"
    "v_and_b32 v230, s92, v230\n\t"
    "v_lshrrev_b32 v255, 16, v231\n\t"
    "v_and_b32 v231, s92, v231\n\t"
    "s_mov_b64 exec, 1\n\t"
    "buffer_load_dword v164, off, %12, s93 offset:256 sc1\n\t"
    "s_mov_b64 exec, -1\n\t"
    "v_exp_f32 v70, v66\n\t"
    "v_exp_f32 v71, v67\n\t"
    "v_pk_fma_f32 v[68:69], v[68:69], s[40:41], s[40:41]\n\t"
    "v_pk_fma_f32 v[70:71], v[70:71], s[42:43], s[42:43]\n\t"
    "v_rcp_f32 v68, v68\n\t"
    "v_rcp_f32 v69, v69\n\t"
    "v_rcp_f32 v70, v70\n\t"
    "v_rcp_f32 v71, v71\n\t"
    "ds_read_b128 v[20:23], v148 offset:5120\n\t"
    "s_waitcnt lgkmcnt(6)\n\t"
    "v_smfmac_f32_16x16x64_bf16 v[220:223], v[120:123], v[92:99], v124\n\t"
    "ds_read_b64 v[92:93], v230 offset:64000\n\t"
    "ds_read_b64 v[94:95], v254 offset:64000\n\t"
    "ds_read_b64 v[96:97], v231 offset:64000\n\t"
    "ds_read_b64 v[98:99], v255 offset:64000\n\t"
    "buffer_load_dwordx4 v[104:107], v127, %12, s88 offen offset:1024 sc1\n\t"
    "v_lshrrev_b32 v252, 16, v232\n\t"
    "v_and_b32 v232, s92, v232\n\t"
    "v_lshrrev_b32 v253, 16, v233\n\t"
    "v_and_b32 v233, s92, v233\n\t"
    "v_pk_mul_f32 v[68:69], v[68:69], v[188:189]\n\t"
    "v_pk_fma_f32 v[68:69], v[70:71], v[190:191], v[68:69]\n\t"
    "v_add_f32 v240, v68, v69\n\t"
    "s_waitcnt lgkmcnt(5)\n\t"
    "v_smfmac_f32_16x16x64_bf16 v[192:195], v[120:123], v[84:91], v124\n\t"
    "ds_read_b64 v[84:85], v232 offset:0\n\t"
    "ds_read_b64 v[86:87], v252 offset:0\n\t"
    "ds_read_b64 v[88:89], v233 offset:0\n\t"
    "ds_read_b64 v[90:91], v253 offset:0\n\t"
    "v_lshrrev_b32 v254, 16, v234\n\t"
    "v_and_b32 v234, s92, v234\n\t"
    "v_lshrrev_b32 v255, 16, v235\n\t"
    "v_and_b32 v235, s92, v235\n\t"
    "s_waitcnt vmcnt(5)\n\t"
    "v_smfmac_f32_16x16x64_bf16 v[196:199], v[156:159], v[24:31], v124\n\t"
    "s_waitcnt lgkmcnt(4)\n\t"
    "v_smfmac_f32_16x16x64_bf16 v[192:195], v[120:123], v[92:99], v124\n\t"
    "ds_read_b64 v[92:93], v234 offset:0\n\t"
    "ds_read_b64 v[94:95], v254 offset:0\n\t"
    "ds_read_b64 v[96:97], v235 offset:0\n\t"
    "ds_read_b64 v[98:99], v255 offset:0\n\t"
    "s_add_u32 s89, s89, 0x8000\n\t"
    "buffer_load_dwordx4 v[108:111], v127, %12, s89 offen offset:0 sc1\n\t"
    "v_lshrrev_b32 v252, 16, v244\n\t"
    "v_and_b32 v244, s92, v244\n\t"
    "v_lshrrev_b32 v253, 16, v245\n\t"
    "v_mov_b64 v[224:225], s[70:71]\n\t"
    "v_mov_b64 v[226:227], s[72:73]\n\t"
    "v_and_b32 v245, s92, v245\n\t"
    "v_smfmac_f32_16x16x64_bf16 v[200:203], v[160:163], v[24:31], v124\n\t"
    "v_pk_mul_f32 v[72:73], v[192:193], v[192:193]\n\t"
    "v_pk_mul_f32 v[74:75], v[194:195], v[194:195]\n\t"
    "v_pk_fma_f32 v[72:73], v[72:73], v[82:83], v[80:81]\n\t"
    "v_pk_fma_f32 v[74:75], v[74:75], v[82:83], v[80:81]\n\t"
    "v_pk_mul_f32 v[72:73], v[72:73], v[192:193]\n\t"
    "v_pk_mul_f32 v[74:75], v[74:75], v[194:195]\n\t"
    "v_exp_f32 v76, v72\n\t"
    "v_exp_f32 v77, v73\n\t"
    "ds_read_b128 v[24:27], v148 offset:6144\n\t"
    "s_waitcnt lgkmcnt(5)\n\t"
    "v_smfmac_f32_16x16x64_bf16 v[224:227], v[120:123], v[84:91], v124\n\t"
    "ds_read_b64 v[84:85], v244 offset:64000\n\t"
    "ds_read_b64 v[86:87], v252 offset:64000\n\t"
    "ds_read_b64 v[88:89], v245 offset:64000\n\t"
    "ds_read_b64 v[90:91], v253 offset:64000\n\t"
    "v_lshrrev_b32 v254, 16, v246\n\t"
    "v_and_b32 v246, s92, v246\n\t"
    "v_lshrrev_b32 v255, 16, v247\n\t"
    "v_and_b32 v247, s92, v247\n\t"
    "v_exp_f32 v78, v74\n\t"
    "v_exp_f32 v79, v75\n\t"
    "v_pk_fma_f32 v[76:77], v[76:77], s[40:41], s[40:41]\n\t"
    "v_pk_fma_f32 v[78:79], v[78:79], s[42:43], s[42:43]\n\t"
    "v_rcp_f32 v76, v76\n\t"
    "v_rcp_f32 v77, v77\n\t"
    "v_rcp_f32 v78, v78\n\t"
    "v_rcp_f32 v79, v79\n\t"
    "ds_read_b128 v[28:31], v148 offset:7168\n\t"
    "s_waitcnt lgkmcnt(6)\n\t"
    "v_smfmac_f32_16x16x64_bf16 v[224:227], v[120:123], v[92:99], v124\n\t"
    "ds_read_b64 v[92:93], v246 offset:64000\n\t"
    "ds_read_b64 v[94:95], v254 offset:64000\n\t"
    "ds_read_b64 v[96:97], v247 offset:64000\n\t"
    "ds_read_b64 v[98:99], v255 offset:64000\n\t"
    "buffer_load_dwordx4 v[116:119], v127, %12, s89 offen offset:1024 sc1\n\t"
    "v_lshrrev_b32 v252, 16, v248\n\t"
    "v_and_b32 v248, s92, v248\n\t"
    "v_lshrrev_b32 v253, 16, v249\n\t"
    "v_and_b32 v249, s92, v249\n\t"
    "v_pk_mul_f32 v[76:77], v[76:77], v[192:193]\n\t"
    "v_pk_fma_f32 v[76:77], v[78:79], v[194:195], v[76:77]\n\t"
    "v_add_f32 v241, v76, v77\n\t"
    "s_waitcnt lgkmcnt(5)\n\t"
    "v_smfmac_f32_16x16x64_bf16 v[196:199], v[120:123], v[84:91], v124\n\t"
    "ds_read_b64 v[84:85], v248 offset:0\n\t"
    "ds_read_b64 v[86:87], v252 offset:0\n\t"
    "ds_read_b64 v[88:89], v249 offset:0\n\t"
    "ds_read_b64 v[90:91], v253 offset:0\n\t"
    "v_lshrrev_b32 v254, 16, v250\n\t"
    "v_and_b32 v250, s92, v250\n\t"
    "v_lshrrev_b32 v255, 16, v251\n\t"
    "v_and_b32 v251, s92, v251\n\t"
    "s_waitcnt vmcnt(3)\n\t"
    "v_readfirstlane_b32 s65, v164\n\t"
    "s_nop 0\n\t"
    "s_cmp_eq_u32 s65, 0\n\t"
    "s_cbranch_scc1 L_xw2_%=\n\t"
    "L_xwb2_%=:\n\t"
    "s_add_u32 s94, s94, 0x2000\n\t"
    "buffer_load_dwordx4 v[36:39], v127, %12, s94 offen sc1\n\t"
    "v_mov_b32 v155, 138896\n\t"
    "s_and_b32 s46, %16, 3\n\t"
    "s_mul_i32 s49, s46, 1040\n\t"
    "s_add_u32 s49, s49, 2064\n\t"
    "L_rng_%=:\n\t"
    "ds_read_b64 v[144:145], v155\n\t"
    "s_waitcnt lgkmcnt(0)\n\t"
    "v_readfirstlane_b32 s38, v145\n\t"
    "v_readfirstlane_b32 s37, v144\n\t"
    "s_cmp_eq_u32 s38, 0\n\t"
    "s_cbranch_scc1 L_rng_%=\n\t"
    "s_sub_u32 s38, s38, s37\n\t"
    "s_lshr_b32 s44, s38, 2\n\t"
    "s_mul_i32 s51, s46, s44\n\t"
    "s_add_u32 s48, s51, s44\n\t"
    "s_lshl_b32 s48, s48, 16\n\t"
    "s_add_u32 s48, s48, s51\n\t"
    "s_lshr_b32 s47, %16, 2\n\t"
    "s_add_u32 s48, s48, s47\n\t"
    "s_add_u32 s48, s48, 0x200\n\t"
    "s_mul_i32 s46, s37, 400\n\t"
    "s_add_u32 s96, %18, s46\n\t"
    "s_addc_u32 s97, %19, 0\n\t"
    "s_sub_u32 s98, 6553600, s46\n\t"
    "s_mov_b32 s99, 0x20000\n\t"
    "v_bfe_u32 v154, v147, 2, 2\n\t"
    "v_and_b32 v155, 3, v147\n\t"
    "v_mul_u32_u24 v154, 25, v154\n\t"
    "v_mad_u32_u24 v154, v155, 6, v154\n\t"
    "v_lshlrev_b32 v153, 2, v154\n\t"
    "s_and_b32 s53, s48, 0xff\n\t"
    "s_bfe_u32 s55, s48, 0x80008\n\t"
    "s_add_u32 s54, s53, s55\n\t"
    "s_add_u32 s55, s54, s55\n\t"
    "s_lshr_b32 s64, s48, 16\n\t"
    "s_mul_i32 s66, s53, 400\n\t"
    "buffer_load_dwordx3 v[128:130], v153, s[96:99], s66 offen\n\t"
    "buffer_load_dwordx4 v[132:135], v153, s[96:99], s66 offen offset:12\n\t"
    "s_mul_i32 s66, s54, 400\n\t"
    "buffer_load_dwordx3 v[136:138], v153, s[96:99], s66 offen\n\t"
    "buffer_load_dwordx4 v[140:143], v153, s[96:99], s66 offen offset:12\n\t"
    "v_cmp_eq_u32 s[62:63], 0, v155\n\t"
    "v_and_b32 v151, 48, v147\n\t"
    "v_or_b32 v155, 0x100000, v151\n\t"
    "v_cndmask_b32 v152, v155, v151, s[62:63]\n\t"
    "v_smfmac_f32_16x16x64_bf16 v[196:199], v[120:123], v[92:99], v124\n\t"
    "ds_read_b64 v[92:93], v250 offset:0\n\t"
    "ds_read_b64 v[94:95], v254 offset:0\n\t"
    "ds_read_b64 v[96:97], v251 offset:0\n\t"
    "ds_read_b64 v[98:99], v255 offset:0\n\t"
    "s_add_u32 s90, s90, 0x8000\n\t"
    "buffer_load_dwordx4 v[248:251], v127, %12, s90 offen offset:0 sc1\n\t"
    "v_lshrrev_b32 v252, 16, v168\n\t"
    "v_and_b32 v168, s92, v168\n\t"
    "v_lshrrev_b32 v253, 16, v169\n\t"
    "v_mov_b64 v[228:229], s[70:71]\n\t"
    "v_mov_b64 v[230:231], s[72:73]\n\t"
    "v_and_b32 v169, s92, v169\n\t"
    "v_pk_mul_f32 v[64:65], v[196:197], v[196:197]\n\t"
    "v_pk_mul_f32 v[66:67], v[198:199], v[198:199]\n\t"
    "v_pk_fma_f32 v[64:65], v[64:65], v[82:83], v[80:81]\n\t"
    "v_pk_fma_f32 v[66:67], v[66:67], v[82:83], v[80:81]\n\t"
    "v_pk_mul_f32 v[64:65], v[64:65], v[196:197]\n\t"
    "v_pk_mul_f32 v[66:67], v[66:67], v[198:199]\n\t"
    "v_exp_f32 v68, v64\n\t"
    "v_exp_f32 v69, v65\n\t"
    "v_smfmac_f32_16x16x64_bf16 v[228:231], v[120:123], v[84:91], v124\n\t"
    "ds_read_b64 v[84:85], v168 offset:64000\n\t"
    "ds_read_b64 v[86:87], v252 offset:64000\n\t"
    "ds_read_b64 v[88:89], v169 offset:64000\n\t"
    "ds_read_b64 v[90:91], v253 offset:64000\n\t"
    "v_lshrrev_b32 v254, 16, v170\n\t"
    "v_and_b32 v170, s92, v170\n\t"
    "v_lshrrev_b32 v255, 16, v171\n\t"
    "v_and_b32 v171, s92, v171\n\t"
    "v_exp_f32 v70, v66\n\t"
    "v_exp_f32 v71, v67\n\t"
    "v_pk_fma_f32 v[68:69], v[68:69], s[40:41], s[40:41]\n\t"
    "v_pk_fma_f32 v[70:71], v[70:71], s[42:43], s[42:43]\n\t"
    "v_rcp_f32 v68, v68\n\t"
    "v_rcp_f32 v69, v69\n\t"
    "v_rcp_f32 v70, v70\n\t"
    "v_rcp_f32 v71, v71\n\t"
    "s_waitcnt lgkmcnt(4)\n\t"
    "v_smfmac_f32_16x16x64_bf16 v[228:231], v[120:123], v[92:99], v124\n\t"
    "ds_read_b64 v[92:93], v170 offset:64000\n\t"
    "ds_read_b64 v[94:95], v254 offset:64000\n\t"
    "ds_read_b64 v[96:97], v171 offset:64000\n\t"
    "ds_read_b64 v[98:99], v255 offset:64000\n\t"
    "buffer_load_dwordx4 v[168:171], v127, %12, s90 offen offset:1024 sc1\n\t"
    "v_lshrrev_b32 v252, 16, v112\n\t"
    "v_and_b32 v112, s92, v112\n\t"
    "v_lshrrev_b32 v253, 16, v113\n\t"
    "v_and_b32 v113, s92, v113\n\t"
    "v_pk_mul_f32 v[68:69], v[68:69], v[196:197]\n\t"
    "v_pk_fma_f32 v[68:69], v[70:71], v[198:199], v[68:69]\n\t"
    "v_add_f32 v242, v68, v69\n\t"
    "s_waitcnt lgkmcnt(4)\n\t"
    "v_smfmac_f32_16x16x64_bf16 v[200:203], v[120:123], v[84:91], v124\n\t"
    "ds_read_b64 v[84:85], v112 offset:0\n\t"
    "ds_read_b64 v[86:87], v252 offset:0\n\t"
    "ds_read_b64 v[88:89], v113 offset:0\n\t"
    "ds_read_b64 v[90:91], v253 offset:0\n\t"
    "v_lshrrev_b32 v254, 16, v114\n\t"
    "v_and_b32 v114, s92, v114\n\t"
    "v_lshrrev_b32 v255, 16, v115\n\t"
    "v_and_b32 v115, s92, v115\n\t"
    "s_add_u32 s94, s94, 0x2000\n\t"
    "buffer_load_dwordx4 v[44:47], v127, %12, s94 offen sc1\n\t"
    "s_mov_b64 exec, 1\n\t"
    "buffer_load_dword v164, off, %12, s93 offset:384 sc1\n\t"
    "s_mov_b64 exec, -1\n\t"
    "s_waitcnt lgkmcnt(4)\n\t"
    "v_smfmac_f32_16x16x64_bf16 v[200:203], v[120:123], v[92:99], v124\n\t"
    "ds_read_b64 v[92:93], v114 offset:0\n\t"
    "ds_read_b64 v[94:95], v254 offset:0\n\t"
    "ds_read_b64 v[96:97], v115 offset:0\n\t"
    "ds_read_b64 v[98:99], v255 offset:0\n\t"
    "s_add_u32 s91, s91, 0x8000\n\t"
    "buffer_load_dwordx4 v[112:115], v127, %12, s91 offen offset:0 sc1\n\t"
    "v_lshrrev_b32 v252, 16, v100\n\t"
    "v_and_b32 v100, s92, v100\n\t"
    "v_lshrrev_b32 v253, 16, v101\n\t"
    "v_mov_b64 v[232:233], s[70:71]\n\t"
    "v_mov_b64 v[234:235], s[72:73]\n\t"
    "v_and_b32 v101, s92, v101\n\t"
    "v_pk_mul_f32 v[72:73], v[200:201], v[200:201]\n\t"
    "v_pk_mul_f32 v[74:75], v[202:203], v[202:203]\n\t"
    "v_pk_fma_f32 v[72:73], v[72:73], v[82:83], v[80:81]\n\t"
    "v_pk_fma_f32 v[74:75], v[74:75], v[82:83], v[80:81]\n\t"
    "v_pk_mul_f32 v[72:73], v[72:73], v[200:201]\n\t"
    "v_pk_mul_f32 v[74:75], v[74:75], v[202:203]\n\t"
    "v_exp_f32 v76, v72\n\t"
    "v_exp_f32 v77, v73\n\t"
    "s_waitcnt lgkmcnt(0)\n\t"
    "s_barrier\n\t"
    "v_smfmac_f32_16x16x64_bf16 v[232:235], v[120:123], v[84:91], v124\n\t"
    "ds_read_b64 v[84:85], v100 offset:64000\n\t"
    "ds_read_b64 v[86:87], v252 offset:64000\n\t"
    "ds_read_b64 v[88:89], v101 offset:64000\n\t"
    "ds_read_b64 v[90:91], v253 offset:64000\n\t"
    "v_lshrrev_b32 v254, 16, v102\n\t"
    "v_and_b32 v102, s92, v102\n\t"
    "v_lshrrev_b32 v255, 16, v103\n\t"
    "v_and_b32 v103, s92, v103\n\t"
    "v_exp_f32 v78, v74\n\t"
    "v_exp_f32 v79, v75\n\t"
    "v_pk_fma_f32 v[76:77], v[76:77], s[40:41], s[40:41]\n\t"
    "v_pk_fma_f32 v[78:79], v[78:79], s[42:43], s[42:43]\n\t"
    "v_rcp_f32 v76, v76\n\t"
    "v_rcp_f32 v77, v77\n\t"
    "v_rcp_f32 v78, v78\n\t"
    "v_rcp_f32 v79, v79\n\t"
    "s_mul_i32 s44, s69, 1040\n\t"
    "s_add_u32 m0, s44, 0\n\t"
    "s_lshl_b32 s44, s69, 11\n\t"
    "buffer_load_dwordx4 v126, %15, s44 offen lds\n\t"
    "s_add_u32 s69, s69, 8\n\t"
    "s_sub_u32 s44, s69, 61\n\t"
    "s_cmp_ge_u32 s69, 61\n\t"
    "s_cselect_b32 s69, s44, s69\n\t"
    "s_waitcnt vmcnt(13)\n\t"
    "v_smfmac_f32_16x16x64_bf16 v[232:235], v[120:123], v[92:99], v124\n\t"
    "ds_read_b64 v[92:93], v102 offset:64000\n\t"
    "ds_read_b64 v[94:95], v254 offset:64000\n\t"
    "ds_read_b64 v[96:97], v103 offset:64000\n\t"
    "ds_read_b64 v[98:99], v255 offset:64000\n\t"
    "buffer_load_dwordx4 v[100:103], v127, %12, s91 offen offset:1024 sc1\n\t"
    "v_lshrrev_b32 v252, 16, v104\n\t"
    "v_and_b32 v104, s92, v104\n\t"
    "v_lshrrev_b32 v253, 16, v105\n\t"
    "v_and_b32 v105, s92, v105\n\t"
    "v_pk_mul_f32 v[76:77], v[76:77], v[200:201]\n\t"
    "v_pk_fma_f32 v[76:77], v[78:79], v[202:203], v[76:77]\n\t"
    "v_add_f32 v243, v76, v77\n\t"
    "s_setprio 0\n\t"
    "s_waitcnt lgkmcnt(4)\n\t"
    "v_smfmac_f32_16x16x64_bf16 v[204:207], v[120:123], v[84:91], v124\n\t"
    "ds_read_b64 v[84:85], v104 offset:64000\n\t"
    "ds_read_b64 v[86:87], v252 offset:64000\n\t"
    "ds_read_b64 v[88:89], v105 offset:64000\n\t"
    "ds_read_b64 v[90:91], v253 offset:64000\n\t"
    "v_lshrrev_b32 v254, 16, v106\n\t"
    "v_and_b32 v106, s92, v106\n\t"
    "v_lshrrev_b32 v255, 16, v107\n\t"
    "v_and_b32 v107, s92, v107\n\t"
    "s_waitcnt vmcnt(11)\n\t"
    "v_smfmac_f32_16x16x64_bf16 v[204:207], v[156:159], v[32:39], v124\n\t"
    "s_mul_i32 s44, s69, 1040\n\t"
    "s_add_u32 m0, s44, 0\n\t"
    "s_lshl_b32 s44, s69, 11\n\t"
    "buffer_load_dwordx4 v126, %15, s44 offen lds\n\t"
    "s_add_u32 s69, s69, 8\n\t"
    "s_sub_u32 s44, s69, 61\n\t"
    "s_cmp_ge_u32 s69, 61\n\t"
    "s_cselect_b32 s69, s44, s69\n\t"
    "s_waitcnt lgkmcnt(4)\n\t"
    "v_smfmac_f32_16x16x64_bf16 v[204:207], v[120:123], v[92:99], v124\n\t"
    "ds_read_b64 v[92:93], v106 offset:64000\n\t"
    "ds_read_b64 v[94:95], v254 offset:64000\n\t"
    "ds_read_b64 v[96:97], v107 offset:64000\n\t"
    "ds_read_b64 v[98:99], v255 offset:64000\n\t"
    "v_lshrrev_b32 v252, 16, v108\n\t"
    "v_and_b32 v108, s92, v108\n\t"
    "v_lshrrev_b32 v253, 16, v109\n\t"
    "v_and_b32 v109, s92, v109\n\t"
    "v_smfmac_f32_16x16x64_bf16 v[208:211], v[160:163], v[32:39], v124\n\t"
    "v_pk_mul_f32 v[64:65], v[204:205], v[204:205]\n\t"
    "v_pk_mul_f32 v[66:67], v[206:207], v[206:207]\n\t"
    "v_pk_fma_f32 v[64:65], v[64:65], v[82:83], v[80:81]\n\t"
    "v_pk_fma_f32 v[66:67], v[66:67], v[82:83], v[80:81]\n\t"
    "v_pk_mul_f32 v[64:65], v[64:65], v[204:205]\n\t"
    "v_pk_mul_f32 v[66:67], v[66:67], v[206:207]\n\t"
    "v_exp_f32 v68, v64\n\t"
    "ds_read_b128 v[32:35], v148 offset:8192\n\t"
    "s_waitcnt lgkmcnt(5)\n\t"
    "v_smfmac_f32_16x16x64_bf16 v[208:211], v[120:123], v[84:91], v124\n\t"
    "ds_read_b64 v[84:85], v108 offset:64000\n\t"
    "ds_read_b64 v[86:87], v252 offset:64000\n\t"
    "ds_read_b64 v[88:89], v109 offset:64000\n\t"
    "ds_read_b64 v[90:91], v253 offset:64000\n\t"
    "v_lshrrev_b32 v254, 16, v110\n\t"
    "v_and_b32 v110, s92, v110\n\t"
    "v_lshrrev_b32 v255, 16, v111\n\t"
    "v_and_b32 v111, s92, v111\n\t"
    "s_waitcnt vmcnt(4)\n\t"
    "v_readfirstlane_b32 s65, v164\n\t"
    "s_nop 0\n\t"
    "s_cmp_eq_u32 s65, 0\n\t"
    "s_cbranch_scc1 L_xw3_%=\n\t"
    "L_xwb3_%=:\n\t"
    "s_add_u32 s94, s94, 0x2000\n\t"
    "buffer_load_dwordx4 v[52:55], v127, %12, s94 offen sc1\n\t"
    "v_exp_f32 v69, v65\n\t"
    "v_exp_f32 v70, v66\n\t"
    "v_exp_f32 v71, v67\n\t"
    "v_pk_fma_f32 v[68:69], v[68:69], s[40:41], s[40:41]\n\t"
    "v_pk_fma_f32 v[70:71], v[70:71], s[42:43], s[42:43]\n\t"
    "v_rcp_f32 v68, v68\n\t"
    "v_rcp_f32 v69, v69\n\t"
    "s_mul_i32 s44, s69, 1040\n\t"
    "s_add_u32 m0, s44, 0\n\t"
    "s_lshl_b32 s44, s69, 11\n\t"
    "buffer_load_dwordx4 v126, %15, s44 offen lds\n\t"
    "s_add_u32 s69, s69, 8\n\t"
    "s_sub_u32 s44, s69, 61\n\t"
    "s_cmp_ge_u32 s69, 61\n\t"
    "s_cselect_b32 s69, s44, s69\n\t"
    "ds_read_b128 v[36:39], v148 offset:9216\n\t"
    "s_waitcnt lgkmcnt(6)\n\t"
    "v_smfmac_f32_16x16x64_bf16 v[208:211], v[120:123], v[92:99], v124\n\t"
    "ds_read_b64 v[92:93], v110 offset:64000\n\t"
    "ds_read_b64 v[94:95], v254 offset:64000\n\t"
    "ds_read_b64 v[96:97], v111 offset:64000\n\t"
    "ds_read_b64 v[98:99], v255 offset:64000\n\t"
    "v_lshrrev_b32 v252, 16, v116\n\t"
    "v_and_b32 v116, s92, v116\n\t"
    "v_lshrrev_b32 v253, 16, v117\n\t"
    "v_and_b32 v117, s92, v117\n\t"
    "v_rcp_f32 v70, v70\n\t"
    "v_rcp_f32 v71, v71\n\t"
    "v_pk_mul_f32 v[68:69], v[68:69], v[204:205]\n\t"
    "v_pk_fma_f32 v[68:69], v[70:71], v[206:207], v[68:69]\n\t"
    "v_add_f32 v244, v68, v69\n\t"
    "v_pk_mul_f32 v[72:73], v[208:209], v[208:209]\n\t"
    "v_pk_mul_f32 v[74:75], v[210:211], v[210:211]\n\t"
    "v_lshlrev_b32 v146, 2, v147\n\t"
    "s_lshl_b32 s50, s37, 8\n\t"
    "s_add_u32 s50, s50, 26214400\n\t"
    "s_lshl_b32 s46, s53, 8\n\t"
    "s_add_u32 s46, s46, s50\n\t"
    "buffer_load_dword v144, v146, %12, s46 offen sc1\n\t"
    "buffer_load_dword v145, v146, %12, s46 offen offset:512 sc1\n\t"
    "v_mov_b32 v149, s49\n\t"
    "s_add_u32 s46, s51, 6\n\t"
    "v_mov_b32 v155, s46\n\t"
    "ds_write_b32 v149, v155\n\t"
    "s_mov_b32 s61, 0\n\t"
    "v_mov_b32 v149, 1024\n\t"
    "v_mov_b32 v150, 0\n\t"
    "ds_write_b32 v149, v150\n\t"
    "s_mov_b32 s45, 1\n\t"
    "s_waitcnt lgkmcnt(7)\n\t"
    "v_smfmac_f32_16x16x64_bf16 v[212:215], v[120:123], v[84:91], v124\n\t"
    "ds_read_b64 v[84:85], v116 offset:64000\n\t"
    "ds_read_b64 v[86:87], v252 offset:64000\n\t"
    "ds_read_b64 v[88:89], v117 offset:64000\n\t"
    "ds_read_b64 v[90:91], v253 offset:64000\n\t"
    "v_lshrrev_b32 v254, 16, v118\n\t"
    "v_and_b32 v118, s92, v118\n\t"
    "v_lshrrev_b32 v255, 16, v119\n\t"
    "v_and_b32 v119, s92, v119\n\t"
    "v_smfmac_f32_16x16x64_bf16 v[212:215], v[156:159], v[40:47], v124\n\t"
    "v_pk_fma_f32 v[72:73], v[72:73], v[82:83], v[80:81]\n\t"
    "v_pk_fma_f32 v[74:75], v[74:75], v[82:83], v[80:81]\n\t"
    "v_pk_mul_f32 v[72:73], v[72:73], v[208:209]\n\t"
    "v_pk_mul_f32 v[74:75], v[74:75], v[210:211]\n\t"
    "v_exp_f32 v76, v72\n\t"
    "v_exp_f32 v77, v73\n\t"
    "v_exp_f32 v78, v74\n\t"
    "s_mul_i32 s44, s69, 1040\n\t"
    "s_add_u32 m0, s44, 0\n\t"
    "s_lshl_b32 s44, s69, 11\n\t"
    "buffer_load_dwordx4 v126, %15, s44 offen lds\n\t"
    "s_add_u32 s69, s69, 8\n\t"
    "s_sub_u32 s44, s69, 61\n\t"
    "s_cmp_ge_u32 s69, 61\n\t"
    "s_cselect_b32 s69, s44, s69\n\t"
    "s_waitcnt lgkmcnt(6)\n\t"
    "v_smfmac_f32_16x16x64_bf16 v[212:215], v[120:123], v[92:99], v124\n\t"
    "ds_read_b64 v[92:93], v118 offset:64000\n\t"
    "ds_read_b64 v[94:95], v254 offset:64000\n\t"
    "ds_read_b64 v[96:97], v119 offset:64000\n\t"
    "ds_read_b64 v[98:99], v255 offset:64000\n\t"
    "v_lshrrev_b32 v252, 16, v248\n\t"
    "v_and_b32 v248, s92, v248\n\t"
    "v_lshrrev_b32 v253, 16, v249\n\t"
    "v_and_b32 v249, s92, v249\n\t"
    "v_smfmac_f32_16x16x64_bf16 v[216:219], v[160:163], v[40:47], v124\n\t"
    "v_exp_f32 v79, v75\n\t"
    "v_pk_fma_f32 v[76:77], v[76:77], s[40:41], s[40:41]\n\t"
    "v_pk_fma_f32 v[78:79], v[78:79], s[42:43], s[42:43]\n\t"
    "v_rcp_f32 v76, v76\n\t"
    "v_rcp_f32 v77, v77\n\t"
    "v_rcp_f32 v78, v78\n\t"
    "v_rcp_f32 v79, v79\n\t"
    "ds_read_b128 v[40:43], v148 offset:10240\n\t"
    "s_waitcnt lgkmcnt(5)\n\t"
    "v_smfmac_f32_16x16x64_bf16 v[216:219], v[120:123], v[84:91], v124\n\t"
    "ds_read_b64 v[84:85], v248 offset:64000\n\t"
    "ds_read_b64 v[86:87], v252 offset:64000\n\t"
    "ds_read_b64 v[88:89], v249 offset:64000\n\t"
    "ds_read_b64 v[90:91], v253 offset:64000\n\t"
    "v_lshrrev_b32 v254, 16, v250\n\t"
    "v_and_b32 v250, s92, v250\n\t"
    "v_lshrrev_b32 v255, 16, v251\n\t"
    "v_and_b32 v251, s92, v251\n\t"
    "s_add_u32 s94, s94, 0x2000\n\t"
    "buffer_load_dwordx4 v[60:63], v127, %12, s94 offen sc1\n\t"
    "v_pk_mul_f32 v[76:77], v[76:77], v[208:209]\n\t"
    "v_pk_fma_f32 v[76:77], v[78:79], v[210:211], v[76:77]\n\t"
    "v_add_f32 v245, v76, v77\n\t"
    "v_pk_mul_f32 v[64:65], v[212:213], v[212:213]\n\t"
    "v_pk_mul_f32 v[66:67], v[214:215], v[214:215]\n\t"
    "v_pk_fma_f32 v[64:65], v[64:65], v[82:83], v[80:81]\n\t"
    "v_pk_fma_f32 v[66:67], v[66:67], v[82:83], v[80:81]\n\t"
    "s_mul_i32 s44, s69, 1040\n\t"
    "s_add_u32 m0, s44, 0\n\t"
    "s_lshl_b32 s44, s69, 11\n\t"
    "buffer_load_dwordx4 v126, %15, s44 offen lds\n\t"
    "s_add_u32 s69, s69, 8\n\t"
    "s_sub_u32 s44, s69, 61\n\t"
    "s_cmp_ge_u32 s69, 61\n\t"
    "s_cselect_b32 s69, s44, s69\n\t"
    "ds_read_b128 v[44:47], v148 offset:11264\n\t"
    "s_waitcnt lgkmcnt(6)\n\t"
    "v_smfmac_f32_16x16x64_bf16 v[216:219], v[120:123], v[92:99], v124\n\t"
    "ds_read_b64 v[92:93], v250 offset:64000\n\t"
    "ds_read_b64 v[94:95], v254 offset:64000\n\t"
    "ds_read_b64 v[96:97], v251 offset:64000\n\t"
    "ds_read_b64 v[98:99], v255 offset:64000\n\t"
    "v_lshrrev_b32 v252, 16, v168\n\t"
    "v_and_b32 v168, s92, v168\n\t"
    "v_lshrrev_b32 v253, 16, v169\n\t"
    "v_and_b32 v169, s92, v169\n\t"
    "v_pk_mul_f32 v[64:65], v[64:65], v[212:213]\n\t"
    "v_pk_mul_f32 v[66:67], v[66:67], v[214:215]\n\t"
    "v_exp_f32 v68, v64\n\t"
    "v_exp_f32 v69, v65\n\t"
    "v_exp_f32 v70, v66\n\t"
    "v_exp_f32 v71, v67\n\t"
    "v_pk_fma_f32 v[68:69], v[68:69], s[40:41], s[40:41]\n\t"
    "s_waitcnt lgkmcnt(5)\n\t"
    "v_smfmac_f32_16x16x64_bf16 v[220:223], v[120:123], v[84:91], v124\n\t"
    "ds_read_b64 v[84:85], v168 offset:64000\n\t"
    "ds_read_b64 v[86:87], v252 offset:64000\n\t"
    "ds_read_b64 v[88:89], v169 offset:64000\n\t"
    "ds_read_b64 v[90:91], v253 offset:64000\n\t"
    "v_lshrrev_b32 v254, 16, v170\n\t"
    "v_and_b32 v170, s92, v170\n\t"
    "v_lshrrev_b32 v255, 16, v171\n\t"
    "v_and_b32 v171, s92, v171\n\t"
    "v_pk_fma_f32 v[70:71], v[70:71], s[42:43], s[42:43]\n\t"
    "v_rcp_f32 v68, v68\n\t"
    "v_rcp_f32 v69, v69\n\t"
    "v_rcp_f32 v70, v70\n\t"
    "v_rcp_f32 v71, v71\n\t"
    "v_pk_mul_f32 v[68:69], v[68:69], v[212:213]\n\t"
    "v_pk_fma_f32 v[68:69], v[70:71], v[214:215], v[68:69]\n\t"
    "s_mul_i32 s44, s69, 1040\n\t"
    "s_add_u32 m0, s44, 0\n\t"
    "s_lshl_b32 s44, s69, 11\n\t"
    "buffer_load_dwordx4 v126, %15, s44 offen lds\n\t"
    "s_add_u32 s69, s69, 8\n\t"
    "s_sub_u32 s44, s69, 61\n\t"
    "s_cmp_ge_u32 s69, 61\n\t"
    "s_cselect_b32 s69, s44, s69\n\t"
    "s_waitcnt vmcnt(11) lgkmcnt(4)\n\t"
    "v_smfmac_f32_16x16x64_bf16 v[220:223], v[120:123], v[92:99], v124\n\t"
    "ds_read_b64 v[92:93], v170 offset:64000\n\t"
    "ds_read_b64 v[94:95], v254 offset:64000\n\t"
    "ds_read_b64 v[96:97], v171 offset:64000\n\t"
    "ds_read_b64 v[98:99], v255 offset:64000\n\t"
    "v_lshrrev_b32 v252, 16, v112\n\t"
    "v_and_b32 v112, s92, v112\n\t"
    "v_lshrrev_b32 v253, 16, v113\n\t"
    "v_and_b32 v113, s92, v113\n\t"
    "v_add_f32 v246, v68, v69\n\t"
    "v_pk_mul_f32 v[72:73], v[216:217], v[216:217]\n\t"
    "v_pk_mul_f32 v[74:75], v[218:219], v[218:219]\n\t"
    "v_pk_fma_f32 v[72:73], v[72:73], v[82:83], v[80:81]\n\t"
    "v_pk_fma_f32 v[74:75], v[74:75], v[82:83], v[80:81]\n\t"
    "v_pk_mul_f32 v[72:73], v[72:73], v[216:217]\n\t"
    "v_pk_mul_f32 v[74:75], v[74:75], v[218:219]\n\t"
    "s_waitcnt lgkmcnt(4)\n\t"
    "v_smfmac_f32_16x16x64_bf16 v[224:227], v[120:123], v[84:91], v124\n\t"
    "ds_read_b64 v[84:85], v112 offset:64000\n\t"
    "ds_read_b64 v[86:87], v252 offset:64000\n\t"
    "ds_read_b64 v[88:89], v113 offset:64000\n\t"
    "ds_read_b64 v[90:91], v253 offset:64000\n\t"
    "v_lshrrev_b32 v254, 16, v114\n\t"
    "v_and_b32 v114, s92, v114\n\t"
    "v_lshrrev_b32 v255, 16, v115\n\t"
    "v_and_b32 v115, s92, v115\n\t"
    "s_waitcnt vmcnt(7)\n\t"
    "v_smfmac_f32_16x16x64_bf16 v[220:223], v[156:159], v[48:55], v124\n\t"
    "v_exp_f32 v76, v72\n\t"
    "v_exp_f32 v77, v73\n\t"
    "v_exp_f32 v78, v74\n\t"
    "v_exp_f32 v79, v75\n\t"
    "v_pk_fma_f32 v[76:77], v[76:77], s[40:41], s[40:41]\n\t"
    "v_pk_fma_f32 v[78:79], v[78:79], s[42:43], s[42:43]\n\t"
    "v_rcp_f32 v76, v76\n\t"
    "s_mul_i32 s44, s69, 1040\n\t"
    "s_add_u32 m0, s44, 0\n\t"
    "s_lshl_b32 s44, s69, 11\n\t"
    "buffer_load_dwordx4 v126, %15, s44 offen lds\n\t"
    "s_add_u32 s69, s69, 8\n\t"
    "s_sub_u32 s44, s69, 61\n\t"
    "s_cmp_ge_u32 s69, 61\n\t"
    "s_cselect_b32 s69, s44, s69\n\t"
    "s_waitcnt lgkmcnt(4)\n\t"
    "v_smfmac_f32_16x16x64_bf16 v[224:227], v[120:123], v[92:99], v124\n\t"
    "ds_read_b64 v[92:93], v114 offset:64000\n\t"
    "ds_read_b64 v[94:95], v254 offset:64000\n\t"
    "ds_read_b64 v[96:97], v115 offset:64000\n\t"
    "ds_read_b64 v[98:99], v255 offset:64000\n\t"
    "v_lshrrev_b32 v252, 16, v100\n\t"
    "v_and_b32 v100, s92, v100\n\t"
    "v_lshrrev_b32 v253, 16, v101\n\t"
    "v_and_b32 v101, s92, v101\n\t"
    "v_smfmac_f32_16x16x64_bf16 v[224:227], v[160:163], v[48:55], v124\n\t"
    "v_rcp_f32 v77, v77\n\t"
    "v_rcp_f32 v78, v78\n\t"
    "v_rcp_f32 v79, v79\n\t"
    "v_pk_mul_f32 v[76:77], v[76:77], v[216:217]\n\t"
    "v_pk_fma_f32 v[76:77], v[78:79], v[218:219], v[76:77]\n\t"
    "v_add_f32 v247, v76, v77\n\t"
    "v_pk_mul_f32 v[64:65], v[220:221], v[220:221]\n\t"
    "ds_read_b128 v[48:51], v148 offset:12288\n\t"
    "s_waitcnt lgkmcnt(5)\n\t"
    "v_smfmac_f32_16x16x64_bf16 v[228:231], v[120:123], v[84:91], v124\n\t"
    "ds_read_b64 v[84:85], v100 offset:64000\n\t"
    "ds_read_b64 v[86:87], v252 offset:64000\n\t"
    "ds_read_b64 v[88:89], v101 offset:64000\n\t"
    "ds_read_b64 v[90:91], v253 offset:64000\n\t"
    "v_lshrrev_b32 v254, 16, v102\n\t"
    "v_and_b32 v102, s92, v102\n\t"
    "v_lshrrev_b32 v255, 16, v103\n\t"
    "v_and_b32 v103, s92, v103\n\t"
    "v_pk_mul_f32 v[66:67], v[222:223], v[222:223]\n\t"
    "v_pk_fma_f32 v[64:65], v[64:65], v[82:83], v[80:81]\n\t"
    "v_pk_fma_f32 v[66:67], v[66:67], v[82:83], v[80:81]\n\t"
    "v_pk_mul_f32 v[64:65], v[64:65], v[220:221]\n\t"
    "v_pk_mul_f32 v[66:67], v[66:67], v[222:223]\n\t"
    "v_exp_f32 v68, v64\n\t"
    "v_exp_f32 v69, v65\n\t"
    "s_mul_i32 s44, s69, 1040\n\t"
    "s_add_u32 m0, s44, 0\n\t"
    "s_lshl_b32 s44, s69, 11\n\t"
    "buffer_load_dwordx4 v126, %15, s44 offen lds\n\t"
    "s_add_u32 s69, s69, 8\n\t"
    "s_sub_u32 s44, s69, 61\n\t"
    "s_cmp_ge_u32 s69, 61\n\t"
    "s_cselect_b32 s69, s44, s69\n\t"
    "ds_read_b128 v[52:55], v148 offset:13312\n\t"
    "s_waitcnt lgkmcnt(6)\n\t"
    "v_smfmac_f32_16x16x64_bf16 v[228:231], v[120:123], v[92:99], v124\n\t"
    "ds_read_b64 v[92:93], v102 offset:64000\n\t"
    "ds_read_b64 v[94:95], v254 offset:64000\n\t"
    "ds_read_b64 v[96:97], v103 offset:64000\n\t"
    "ds_read_b64 v[98:99], v255 offset:64000\n\t"
    "v_exp_f32 v70, v66\n\t"
    "v_exp_f32 v71, v67\n\t"
    "v_pk_fma_f32 v[68:69], v[68:69], s[40:41], s[40:41]\n\t"
    "v_pk_fma_f32 v[70:71], v[70:71], s[42:43], s[42:43]\n\t"
    "v_rcp_f32 v68, v68\n\t"
    "v_rcp_f32 v69, v69\n\t"
    "v_rcp_f32 v70, v70\n\t"
    "s_waitcnt lgkmcnt(5)\n\t"
    "v_smfmac_f32_16x16x64_bf16 v[232:235], v[120:123], v[84:91], v124\n\t"
    "s_waitcnt vmcnt(4)\n\t"
    "v_smfmac_f32_16x16x64_bf16 v[228:231], v[156:159], v[56:63], v124\n\t"
    "v_rcp_f32 v71, v71\n\t"
    "v_pk_mul_f32 v[68:69], v[68:69], v[220:221]\n\t"
    "v_pk_fma_f32 v[68:69], v[70:71], v[222:223], v[68:69]\n\t"
    "v_add_f32 v248, v68, v69\n\t"
    "v_pk_mul_f32 v[72:73], v[224:225], v[224:225]\n\t"
    "v_pk_mul_f32 v[74:75], v[226:227], v[226:227]\n\t"
    "v_pk_fma_f32 v[72:73], v[72:73], v[82:83], v[80:81]\n\t"
    "s_waitcnt lgkmcnt(0)\n\t"
    "v_smfmac_f32_16x16x64_bf16 v[232:235], v[120:123], v[92:99], v124\n\t"
    "v_smfmac_f32_16x16x64_bf16 v[232:235], v[160:163], v[56:63], v124\n\t"
    "v_pk_fma_f32 v[74:75], v[74:75], v[82:83], v[80:81]\n\t"
    "v_pk_mul_f32 v[72:73], v[72:73], v[224:225]\n\t"
    "v_pk_mul_f32 v[74:75], v[74:75], v[226:227]\n\t"
    "v_exp_f32 v76, v72\n\t"
    "v_exp_f32 v77, v73\n\t"
    "v_exp_f32 v78, v74\n\t"
    "v_exp_f32 v79, v75\n\t"
    "ds_read_b128 v[56:59], v148 offset:14336\n\t"
    "s_barrier\n\t"
    "v_add_u32 v126, 124928, v126\n\t"
    "s_bfe_u32 s46, %14, 0x30003\n\t"
    "s_min_u32 s46, s46, 6\n\t"
    "s_lshl_b32 s69, s46, 3\n\t"
    "s_add_u32 s69, s69, %16\n\t"
    "s_mul_i32 s44, s69, 1040\n\t"
    "s_add_u32 m0, s44, 63440\n\t"
    "s_lshl_b32 s44, s69, 11\n\t"
    "buffer_load_dwordx4 v126, %15, s44 offen lds\n\t"
    "s_add_u32 s69, s69, 8\n\t"
    "s_sub_u32 s44, s69, 64\n\t"
    "s_cmp_ge_u32 s69, 64\n\t"
    "s_cselect_b32 s69, s44, s69\n\t"
    "s_mul_i32 s44, s69, 1040\n\t"
    "s_add_u32 m0, s44, 63440\n\t"
    "s_lshl_b32 s44, s69, 11\n\t"
    "buffer_load_dwordx4 v126, %15, s44 offen lds\n\t"
    "s_add_u32 s69, s69, 8\n\t"
    "s_sub_u32 s44, s69, 64\n\t"
    "s_cmp_ge_u32 s69, 64\n\t"
    "s_cselect_b32 s69, s44, s69\n\t"
    "s_mul_i32 s44, s69, 1040\n\t"
    "s_add_u32 m0, s44, 63440\n\t"
    "s_lshl_b32 s44, s69, 11\n\t"
    "buffer_load_dwordx4 v126, %15, s44 offen lds\n\t"
    "s_add_u32 s69, s69, 8\n\t"
    "s_sub_u32 s44, s69, 64\n\t"
    "s_cmp_ge_u32 s69, 64\n\t"
    "s_cselect_b32 s69, s44, s69\n\t"
    "s_mul_i32 s44, s69, 1040\n\t"
    "s_add_u32 m0, s44, 63440\n\t"
    "s_lshl_b32 s44, s69, 11\n\t"
    "buffer_load_dwordx4 v126, %15, s44 offen lds\n\t"
    "s_add_u32 s69, s69, 8\n\t"
    "s_sub_u32 s44, s69, 64\n\t"
    "s_cmp_ge_u32 s69, 64\n\t"
    "s_cselect_b32 s69, s44, s69\n\t"
    "ds_read_b128 v[60:63], v148 offset:15360\n\t"
    "ds_read_b128 v[84:87], v148 offset:21504\n\t"
    "ds_read_b128 v[88:91], v148 offset:22528\n\t"
    "v_pk_fma_f32 v[76:77], v[76:77], s[40:41], s[40:41]\n\t"
    "v_pk_fma_f32 v[78:79], v[78:79], s[42:43], s[42:43]\n\t"
    "v_rcp_f32 v76, v76\n\t"
    "v_rcp_f32 v77, v77\n\t"
    "v_rcp_f32 v78, v78\n\t"
    "v_rcp_f32 v79, v79\n\t"
    "v_pk_mul_f32 v[76:77], v[76:77], v[224:225]\n\t"
    "v_pk_fma_f32 v[76:77], v[78:79], v[226:227], v[76:77]\n\t"
    "v_add_f32 v249, v76, v77\n\t"
    "s_mul_i32 s44, s69, 1040\n\t"
    "s_add_u32 m0, s44, 63440\n\t"
    "s_lshl_b32 s44, s69, 11\n\t"
    "buffer_load_dwordx4 v126, %15, s44 offen lds\n\t"
    "s_add_u32 s69, s69, 8\n\t"
    "s_sub_u32 s44, s69, 64\n\t"
    "s_cmp_ge_u32 s69, 64\n\t"
    "s_cselect_b32 s69, s44, s69\n\t"
    "s_mul_i32 s44, s69, 1040\n\t"
    "s_add_u32 m0, s44, 63440\n\t"
    "s_lshl_b32 s44, s69, 11\n\t"
    "buffer_load_dwordx4 v126, %15, s44 offen lds\n\t"
    "s_add_u32 s69, s69, 8\n\t"
    "s_sub_u32 s44, s69, 64\n\t"
    "s_cmp_ge_u32 s69, 64\n\t"
    "s_cselect_b32 s69, s44, s69\n\t"
    "v_pk_mul_f32 v[64:65], v[228:229], v[228:229]\n\t"
    "v_pk_mul_f32 v[66:67], v[230:231], v[230:231]\n\t"
    "v_pk_fma_f32 v[64:65], v[64:65], v[82:83], v[80:81]\n\t"
    "v_pk_fma_f32 v[66:67], v[66:67], v[82:83], v[80:81]\n\t"
    "v_pk_mul_f32 v[64:65], v[64:65], v[228:229]\n\t"
    "v_pk_mul_f32 v[66:67], v[66:67], v[230:231]\n\t"
    "v_exp_f32 v68, v64\n\t"
    "v_exp_f32 v69, v65\n\t"
    "v_exp_f32 v70, v66\n\t"
    "v_exp_f32 v71, v67\n\t"
    "v_pk_fma_f32 v[68:69], v[68:69], s[40:41], s[40:41]\n\t"
    "v_pk_fma_f32 v[70:71], v[70:71], s[42:43], s[42:43]\n\t"
    "v_rcp_f32 v68, v68\n\t"
    "v_rcp_f32 v69, v69\n\t"
    "v_rcp_f32 v70, v70\n\t"
    "v_rcp_f32 v71, v71\n\t"
    "v_pk_mul_f32 v[68:69], v[68:69], v[228:229]\n\t"
    "v_pk_fma_f32 v[68:69], v[70:71], v[230:231], v[68:69]\n\t"
    "v_add_f32 v250, v68, v69\n\t"
    "s_mul_i32 s44, s69, 1040\n\t"
    "s_add_u32 m0, s44, 63440\n\t"
    "s_lshl_b32 s44, s69, 11\n\t"
    "buffer_load_dwordx4 v126, %15, s44 offen lds\n\t"
    "s_add_u32 s69, s69, 8\n\t"
    "s_sub_u32 s44, s69, 64\n\t"
    "s_cmp_ge_u32 s69, 64\n\t"
    "s_cselect_b32 s69, s44, s69\n\t"
    "s_mul_i32 s44, s69, 1040\n\t"
    "s_add_u32 m0, s44, 63440\n\t"
    "s_lshl_b32 s44, s69, 11\n\t"
    "buffer_load_dwordx4 v126, %15, s44 offen lds\n\t"
    "s_add_u32 s69, s69, 8\n\t"
    "s_sub_u32 s44, s69, 64\n\t"
    "s_cmp_ge_u32 s69, 64\n\t"
    "s_cselect_b32 s69, s44, s69\n\t"
    "v_pk_mul_f32 v[72:73], v[232:233], v[232:233]\n\t"
    "v_pk_mul_f32 v[74:75], v[234:235], v[234:235]\n\t"
    "v_pk_fma_f32 v[72:73], v[72:73], v[82:83], v[80:81]\n\t"
    "v_pk_fma_f32 v[74:75], v[74:75], v[82:83], v[80:81]\n\t"
    "v_pk_mul_f32 v[72:73], v[72:73], v[232:233]\n\t"
    "v_pk_mul_f32 v[74:75], v[74:75], v[234:235]\n\t"
    "v_exp_f32 v76, v72\n\t"
    "v_exp_f32 v77, v73\n\t"
    "v_exp_f32 v78, v74\n\t"
    "v_exp_f32 v79, v75\n\t"
    "v_pk_fma_f32 v[76:77], v[76:77], s[40:41], s[40:41]\n\t"
    "v_pk_fma_f32 v[78:79], v[78:79], s[42:43], s[42:43]\n\t"
    "v_rcp_f32 v76, v76\n\t"
    "v_rcp_f32 v77, v77\n\t"
    "v_rcp_f32 v78, v78\n\t"
    "v_rcp_f32 v79, v79\n\t"
    "v_pk_mul_f32 v[76:77], v[76:77], v[232:233]\n\t"
    "v_pk_fma_f32 v[76:77], v[78:79], v[234:235], v[76:77]\n\t"
    "v_add_f32 v251, v76, v77\n\t"
    "ds_read_b128 v[64:67], v148 offset:16384\n\t"
    "ds_read_b128 v[68:71], v148 offset:17408\n\t"
    "ds_read_b128 v[72:75], v148 offset:18432\n\t"
    "ds_read_b128 v[76:79], v148 offset:19456\n\t"
    "ds_read_b128 v[80:83], v148 offset:20480\n\t"
    "s_waitcnt vmcnt(0) lgkmcnt(0)\n\t"
    "s_mov_b32 s68, 130\n\t"
    "v_mad_u32_u24 v128, v128, s68, v152\n\t"
    "v_and_b32 v128, -16, v128\n\t"
    "v_mad_u32_u24 v129, v129, s68, v151\n\t"
    "v_and_b32 v129, -16, v129\n\t"
    "v_mad_u32_u24 v130, v130, s68, v151\n\t"
    "v_and_b32 v130, -16, v130\n\t"
    "v_mad_u32_u24 v132, v132, s68, v151\n\t"
    "v_and_b32 v132, -16, v132\n\t"
    "v_mad_u32_u24 v133, v133, s68, v151\n\t"
    "v_and_b32 v133, -16, v133\n\t"
    "v_mad_u32_u24 v134, v134, s68, v151\n\t"
    "v_and_b32 v134, -16, v134\n\t"
    "v_mad_u32_u24 v135, v135, s68, v151\n\t"
    "v_and_b32 v135, -16, v135\n\t"
    "v_lshrrev_b32 v122, 4, v147\n\t"
    "v_and_b32 v155, 15, v147\n\t"
    "v_lshlrev_b32 v155, 4, v155\n\t"
    "v_lshl_add_u32 v122, v122, 16, v155\n\t"
    "v_mov_b32 v150, 1\n\t"
    "v_mov_b32 v123, 0\n\t"
    "v_lshlrev_b32 v149, 20, v147\n\t"
    "v_add_u32 v149, s49, v149\n\t"
    "s_mov_b32 s52, s53\n\t"
    "s_cmp_ge_u32 %16, 4\n\t"
    "s_cbranch_scc1 L_st4_%=\n\t"
    "s_bfe_u32 s65, s84, 0xc0002\n\t"
    "s_add_u32 s65, s65, s36\n\t"
    "buffer_store_dword v236, v125, %12, s65 offen offset:0 sc1\n\t"
    "buffer_store_dword v237, v125, %12, s65 offen offset:256 sc1\n\t"
    "s_bfe_u32 s65, s85, 0xc0002\n\t"
    "s_add_u32 s65, s65, s36\n\t"
    "buffer_store_dword v238, v125, %12, s65 offen offset:0 sc1\n\t"
    "buffer_store_dword v239, v125, %12, s65 offen offset:256 sc1\n\t"
    "s_bfe_u32 s65, s86, 0xc0002\n\t"
    "s_add_u32 s65, s65, s36\n\t"
    "buffer_store_dword v240, v125, %12, s65 offen offset:0 sc1\n\t"
    "buffer_store_dword v241, v125, %12, s65 offen offset:256 sc1\n\t"
    "s_bfe_u32 s65, s87, 0xc0002\n\t"
    "s_add_u32 s65, s65, s36\n\t"
    "buffer_store_dword v242, v125, %12, s65 offen offset:0 sc1\n\t"
    "buffer_store_dword v243, v125, %12, s65 offen offset:256 sc1\n\t"
    "s_bfe_u32 s65, s88, 0xc0002\n\t"
    "s_add_u32 s65, s65, s36\n\t"
    "buffer_store_dword v244, v125, %12, s65 offen offset:0 sc1\n\t"
    "buffer_store_dword v245, v125, %12, s65 offen offset:256 sc1\n\t"
    "s_bfe_u32 s65, s89, 0xc0002\n\t"
    "s_add_u32 s65, s65, s36\n\t"
    "buffer_store_dword v246, v125, %12, s65 offen offset:0 sc1\n\t"
    "buffer_store_dword v247, v125, %12, s65 offen offset:256 sc1\n\t"
    "s_bfe_u32 s65, s90, 0xc0002\n\t"
    "s_add_u32 s65, s65, s36\n\t"
    "buffer_store_dword v248, v125, %12, s65 offen offset:0 sc1\n\t"
    "buffer_store_dword v249, v125, %12, s65 offen offset:256 sc1\n\t"
    "s_bfe_u32 s65, s91, 0xc0002\n\t"
    "s_add_u32 s65, s65, s36\n\t"
    "buffer_store_dword v250, v125, %12, s65 offen offset:0 sc1\n\t"
    "buffer_store_dword v251, v125, %12, s65 offen offset:256 sc1\n\t"
    "s_sub_u32 s61, s64, 3\n\t"
    "L_st4_%=:\n\t"
    "s_barrier\n\t"
    "s_mov_b32 s68, 130\n\t"
    "ds_read_b128 v[156:159], v128 offset:0\n\t"
    "ds_read_b128 v[160:163], v128 offset:64\n\t"
    "ds_read_b128 v[164:167], v129 offset:0\n\t"
    "ds_read_b128 v[168:171], v129 offset:64\n\t"
    "ds_read_b128 v[172:175], v130 offset:0\n\t"
    "ds_read_b128 v[176:179], v130 offset:64\n\t"
    "ds_read_b128 v[180:183], v132 offset:0\n\t"
    "ds_read_b128 v[184:187], v132 offset:64\n\t"
    "ds_read_b128 v[188:191], v133 offset:0\n\t"
    "ds_read_b128 v[192:195], v133 offset:64\n\t"
    "ds_read_b128 v[196:199], v134 offset:0\n\t"
    "ds_read_b128 v[200:203], v134 offset:64\n\t"
    "ds_read_b128 v[204:207], v135 offset:0\n\t"
    "ds_read_b128 v[208:211], v135 offset:64\n\t"
    "s_setprio 1\n\t"
    "L_pnn_%=:\n\t"
    "s_mul_i32 s69, s52, 272\n\t"
    "s_add_u32 s69, s69, 130048\n\t"
    "s_mul_i32 s66, s55, 400\n\t"
    "s_waitcnt lgkmcnt(8)\n\t"
    "v_mfma_f32_16x16x32_bf16 v[92:95], v[156:159], v[156:159], 0\n\t"
    "v_add_u32 v147, s69, v146\n\t"
    "v_fmac_f32 v121, v108, v72\n\t"
    "ds_add_rtn_u32 v148, v149, v150\n\t"
    "v_mfma_f32_16x16x32_bf16 v[92:95], v[160:163], v[160:163], v[92:95]\n\t"
    "v_fmac_f32 v120, v109, v73\n\t"
    "v_fmac_f32 v121, v110, v74\n\t"
    "buffer_load_dwordx3 v[128:130], v153, s[96:99], s66 offen\n\t"
    "v_mfma_f32_16x16x32_bf16 v[96:99], v[164:167], v[164:167], 0\n\t"
    "v_fmac_f32 v120, v111, v75\n\t"
    "v_fmac_f32 v121, v112, v76\n\t"
    "v_mfma_f32_16x16x32_bf16 v[96:99], v[168:171], v[168:171], v[96:99]\n\t"
    "v_fmac_f32 v120, v113, v77\n\t"
    "v_fmac_f32 v121, v114, v78\n\t"
    "buffer_load_dwordx4 v[132:135], v153, s[96:99], s66 offen offset:12\n\t"
    "v_mfma_f32_16x16x32_bf16 v[100:103], v[164:167], v[172:175], 0\n\t"
    "v_fmac_f32 v120, v115, v79\n\t"
    "v_fmac_f32 v121, v116, v80\n\t"
    "v_mfma_f32_16x16x32_bf16 v[100:103], v[168:171], v[176:179], v[100:103]\n\t"
    "v_fmac_f32 v120, v117, v81\n\t"
    "v_fmac_f32 v121, v118, v82\n\t"
    "s_waitcnt vmcnt(3)\n\t"
    "s_cmp_ge_u32 s55, s61\n\t"
    "s_cbranch_scc1 L_req0_%=\n\t"
    "L_back0_%=:\n\t"
    "v_mfma_f32_16x16x32_bf16 v[104:107], v[172:175], v[172:175], 0\n\t"
    "v_mad_u32_u24 v136, v136, s68, v152\n\t"
    "v_and_b32 v136, -16, v136\n\t"
    "v_mfma_f32_16x16x32_bf16 v[104:107], v[176:179], v[176:179], v[104:107]\n\t"
    "v_fmac_f32 v120, v119, v83\n\t"
    "v_add_f32 v154, v120, v121\n\t"
    "ds_write_b32 v147, v154\n\t"
    "v_mfma_f32_16x16x32_bf16 v[108:111], v[156:159], v[164:167], 0\n\t"
    "v_fma_f32 v120, v92, v84, v144\n\t"
    "v_mul_f32 v121, v96, v0\n\t"
    "v_mfma_f32_16x16x32_bf16 v[108:111], v[160:163], v[168:171], v[108:111]\n\t"
    "v_fmac_f32 v120, v97, v1\n\t"
    "v_fmac_f32 v121, v98, v2\n\t"
    "s_waitcnt lgkmcnt(8)\n\t"
    "v_mfma_f32_16x16x32_bf16 v[112:115], v[164:167], v[180:183], 0\n\t"
    "v_fmac_f32 v120, v99, v3\n\t"
    "v_fmac_f32 v121, v100, v4\n\t"
    "v_mfma_f32_16x16x32_bf16 v[112:115], v[168:171], v[184:187], v[112:115]\n\t"
    "v_fmac_f32 v120, v101, v5\n\t"
    "v_fmac_f32 v121, v102, v6\n\t"
    "v_mfma_f32_16x16x32_bf16 v[116:119], v[172:175], v[180:183], 0\n\t"
    "v_fmac_f32 v120, v103, v7\n\t"
    "v_fmac_f32 v121, v104, v8\n\t"
    "v_mfma_f32_16x16x32_bf16 v[116:119], v[176:179], v[184:187], v[116:119]\n\t"
    "v_fmac_f32 v120, v105, v9\n\t"
    "v_fmac_f32 v121, v106, v10\n\t"
    "ds_read_b128 v[212:215], v136 offset:0\n\t"
    "ds_read_b128 v[216:219], v136 offset:64\n\t"
    "v_mfma_f32_16x16x32_bf16 v[92:95], v[180:183], v[180:183], 0\n\t"
    "v_fmac_f32 v120, v107, v11\n\t"
    "v_mad_u32_u24 v137, v137, s68, v151\n\t"
    "v_mfma_f32_16x16x32_bf16 v[92:95], v[184:187], v[184:187], v[92:95]\n\t"
    "v_and_b32 v137, -16, v137\n\t"
    "v_fmac_f32 v121, v108, v85\n\t"
    "v_mfma_f32_16x16x32_bf16 v[96:99], v[156:159], v[172:175], 0\n\t"
    "v_fmac_f32 v120, v112, v12\n\t"
    "v_fmac_f32 v121, v113, v13\n\t"
    "v_mfma_f32_16x16x32_bf16 v[96:99], v[160:163], v[176:179], v[96:99]\n\t"
    "v_fmac_f32 v120, v114, v14\n\t"
    "v_fmac_f32 v121, v115, v15\n\t"
    "s_lshl_b32 s51, s55, 8\n\t"
    "s_add_u32 s51, s51, s50\n\t"
    "s_waitcnt lgkmcnt(8)\n\t"
    "v_mfma_f32_16x16x32_bf16 v[100:103], v[164:167], v[188:191], 0\n\t"
    "v_mad_u32_u24 v138, v138, s68, v151\n\t"
    "v_and_b32 v138, -16, v138\n\t"
    "v_mfma_f32_16x16x32_bf16 v[100:103], v[168:171], v[192:195], v[100:103]\n\t"
    "v_fmac_f32 v120, v116, v16\n\t"
    "v_fmac_f32 v121, v117, v17\n\t"
    "buffer_load_dword v144, v146, %12, s51 offen sc1\n\t"
    "v_mfma_f32_16x16x32_bf16 v[104:107], v[172:175], v[188:191], 0\n\t"
    "v_fmac_f32 v120, v118, v18\n\t"
    "v_fmac_f32 v121, v119, v19\n\t"
    "v_mfma_f32_16x16x32_bf16 v[104:107], v[176:179], v[192:195], v[104:107]\n\t"
    "v_fmac_f32 v120, v92, v20\n\t"
    "v_fmac_f32 v121, v93, v21\n\t"
    "s_waitcnt lgkmcnt(6)\n\t"
    "v_mfma_f32_16x16x32_bf16 v[108:111], v[164:167], v[196:199], 0\n\t"
    "v_fmac_f32 v120, v94, v22\n\t"
    "v_fmac_f32 v121, v95, v23\n\t"
    "v_mfma_f32_16x16x32_bf16 v[108:111], v[168:171], v[200:203], v[108:111]\n\t"
    "v_fmac_f32 v120, v96, v86\n\t"
    "v_mad_u32_u24 v140, v140, s68, v151\n\t"
    "v_mfma_f32_16x16x32_bf16 v[112:115], v[156:159], v[180:183], 0\n\t"
    "v_fmac_f32 v121, v100, v24\n\t"
    "v_fmac_f32 v120, v101, v25\n\t"
    "v_mfma_f32_16x16x32_bf16 v[112:115], v[160:163], v[184:187], v[112:115]\n\t"
    "v_fmac_f32 v121, v102, v26\n\t"
    "v_fmac_f32 v120, v103, v27\n\t"
    "s_waitcnt lgkmcnt(3)\n\t"
    "v_mfma_f32_16x16x32_bf16 v[116:119], v[164:167], v[204:207], 0\n\t"
    "v_readfirstlane_b32 s60, v148\n\t"
    "v_and_b32 v140, -16, v140\n\t"
    "v_mfma_f32_16x16x32_bf16 v[116:119], v[168:171], v[208:211], v[116:119]\n\t"
    "v_fmac_f32 v121, v104, v28\n\t"
    "v_fmac_f32 v120, v105, v29\n\t"
    "ds_read_b128 v[164:167], v137 offset:0\n\t"
    "ds_read_b128 v[168:171], v137 offset:64\n\t"
    "v_mfma_f32_16x16x32_bf16 v[92:95], v[172:175], v[196:199], 0\n\t"
    "v_fmac_f32 v121, v106, v30\n\t"
    "v_fmac_f32 v120, v107, v31\n\t"
    "v_mfma_f32_16x16x32_bf16 v[92:95], v[176:179], v[200:203], v[92:95]\n\t"
    "v_fmac_f32 v121, v108, v32\n\t"
    "v_fmac_f32 v120, v109, v33\n\t"
    "v_mfma_f32_16x16x32_bf16 v[96:99], v[172:175], v[204:207], 0\n\t"
    "v_fmac_f32 v121, v110, v34\n\t"
    "v_fmac_f32 v120, v111, v35\n\t"
    "v_mfma_f32_16x16x32_bf16 v[96:99], v[176:179], v[208:211], v[96:99]\n\t"
    "v_fmac_f32 v121, v112, v87\n\t"
    "v_mad_u32_u24 v141, v141, s68, v151\n\t"
    "ds_read_b128 v[172:175], v138 offset:0\n\t"
    "ds_read_b128 v[176:179], v138 offset:64\n\t"
    "v_mfma_f32_16x16x32_bf16 v[100:103], v[156:159], v[188:191], 0\n\t"
    "v_fmac_f32 v120, v116, v36\n\t"
    "v_fmac_f32 v121, v117, v37\n\t"
    "v_mfma_f32_16x16x32_bf16 v[100:103], v[160:163], v[192:195], v[100:103]\n\t"
    "v_fmac_f32 v120, v118, v38\n\t"
    "v_fmac_f32 v121, v119, v39\n\t"
    "v_mfma_f32_16x16x32_bf16 v[104:107], v[180:183], v[188:191], 0\n\t"
    "v_and_b32 v141, -16, v141\n\t"
    "v_fmac_f32 v120, v92, v40\n\t"
    "v_mfma_f32_16x16x32_bf16 v[104:107], v[184:187], v[192:195], v[104:107]\n\t"
    "v_fmac_f32 v121, v93, v41\n\t"
    "v_fmac_f32 v120, v94, v42\n\t"
    "v_mfma_f32_16x16x32_bf16 v[108:111], v[180:183], v[196:199], 0\n\t"
    "v_fmac_f32 v121, v95, v43\n\t"
    "v_fmac_f32 v120, v96, v44\n\t"
    "v_mfma_f32_16x16x32_bf16 v[108:111], v[184:187], v[200:203], v[108:111]\n\t"
    "v_fmac_f32 v121, v97, v45\n\t"
    "v_fmac_f32 v120, v98, v46\n\t"
    "v_mfma_f32_16x16x32_bf16 v[112:115], v[180:183], v[204:207], 0\n\t"
    "v_fmac_f32 v121, v99, v47\n\t"
    "v_fmac_f32 v120, v100, v88\n\t"
    "v_mfma_f32_16x16x32_bf16 v[112:115], v[184:187], v[208:211], v[112:115]\n\t"
    "v_mad_u32_u24 v142, v142, s68, v151\n\t"
    "v_and_b32 v142, -16, v142\n\t"
    "ds_read_b128 v[180:183], v140 offset:0\n\t"
    "ds_read_b128 v[184:187], v140 offset:64\n\t"
    "s_cmp_eq_u32 s45, 1\n\t"
    "s_cbranch_scc1 L_sig_%=\n\t"
    "L_sigb_%=:\n\t"
    "v_mfma_f32_16x16x32_bf16 v[116:119], v[156:159], v[196:199], 0\n\t"
    "v_fmac_f32 v121, v104, v48\n\t"
    "v_fmac_f32 v120, v105, v49\n\t"
    "v_mfma_f32_16x16x32_bf16 v[116:119], v[160:163], v[200:203], v[116:119]\n\t"
    "v_fmac_f32 v121, v106, v50\n\t"
    "v_fmac_f32 v120, v107, v51\n\t"
    "v_mfma_f32_16x16x32_bf16 v[92:95], v[188:191], v[188:191], 0\n\t"
    "v_mad_u32_u24 v143, v143, s68, v151\n\t"
    "v_and_b32 v143, -16, v143\n\t"
    "v_mfma_f32_16x16x32_bf16 v[92:95], v[192:195], v[192:195], v[92:95]\n\t"
    "v_fmac_f32 v121, v108, v52\n\t"
    "v_fmac_f32 v120, v109, v53\n\t"
    "v_mfma_f32_16x16x32_bf16 v[96:99], v[188:191], v[196:199], 0\n\t"
    "v_fmac_f32 v121, v110, v54\n\t"
    "v_fmac_f32 v120, v111, v55\n\t"
    "v_mfma_f32_16x16x32_bf16 v[96:99], v[192:195], v[200:203], v[96:99]\n\t"
    "v_fmac_f32 v121, v112, v56\n\t"
    "v_fmac_f32 v120, v113, v57\n\t"
    "v_mfma_f32_16x16x32_bf16 v[100:103], v[188:191], v[204:207], 0\n\t"
    "v_fmac_f32 v121, v114, v58\n\t"
    "v_fmac_f32 v120, v115, v59\n\t"
    "v_mfma_f32_16x16x32_bf16 v[100:103], v[192:195], v[208:211], v[100:103]\n\t"
    "v_fmac_f32 v121, v116, v89\n\t"
    "ds_read_b128 v[188:191], v141 offset:0\n\t"
    "ds_read_b128 v[192:195], v141 offset:64\n\t"
    "v_mfma_f32_16x16x32_bf16 v[104:107], v[156:159], v[204:207], 0\n\t"
    "v_fmac_f32 v120, v92, v60\n\t"
    "v_fmac_f32 v121, v93, v61\n\t"
    "v_mfma_f32_16x16x32_bf16 v[104:107], v[160:163], v[208:211], v[104:107]\n\t"
    "v_fmac_f32 v120, v94, v62\n\t"
    "v_fmac_f32 v121, v95, v63\n\t"
    "v_mfma_f32_16x16x32_bf16 v[108:111], v[196:199], v[196:199], 0\n\t"
    "v_fmac_f32 v120, v96, v64\n\t"
    "v_fmac_f32 v121, v97, v65\n\t"
    "v_mfma_f32_16x16x32_bf16 v[108:111], v[200:203], v[200:203], v[108:111]\n\t"
    "v_fmac_f32 v120, v98, v66\n\t"
    "v_fmac_f32 v121, v99, v67\n\t"
    "v_mfma_f32_16x16x32_bf16 v[112:115], v[196:199], v[204:207], 0\n\t"
    "v_fmac_f32 v120, v100, v68\n\t"
    "v_fmac_f32 v121, v101, v69\n\t"
    "v_mfma_f32_16x16x32_bf16 v[112:115], v[200:203], v[208:211], v[112:115]\n\t"
    "v_fmac_f32 v120, v102, v70\n\t"
    "v_fmac_f32 v121, v103, v71\n\t"
    "ds_read_b128 v[196:199], v142 offset:0\n\t"
    "ds_read_b128 v[200:203], v142 offset:64\n\t"
    "v_mfma_f32_16x16x32_bf16 v[116:119], v[204:207], v[204:207], 0\n\t"
    "v_fmac_f32 v120, v104, v90\n\t"
    "v_mfma_f32_16x16x32_bf16 v[116:119], v[208:211], v[208:211], v[116:119]\n\t"
    "ds_read_b128 v[204:207], v143 offset:0\n\t"
    "ds_read_b128 v[208:211], v143 offset:64\n\t"
    "s_mov_b32 s52, s53\n\t"
    "s_mov_b32 s53, s54\n\t"
    "s_mov_b32 s54, s55\n\t"
    "s_mov_b32 s55, s60\n\t"
    "s_cmp_lt_u32 s54, s64\n\t"
    "s_cbranch_scc1 L_pr0_%=\n\t"
    "s_setprio 0\n\t"
    "L_pr0_%=:\n\t"
    "s_cmp_lt_u32 s53, s64\n\t"
    "s_cbranch_scc0 L_done_%=\n\t"
    "s_mul_i32 s69, s52, 272\n\t"
    "s_add_u32 s69, s69, 130048\n\t"
    "s_mul_i32 s66, s55, 400\n\t"
    "s_waitcnt lgkmcnt(8)\n\t"
    "v_mfma_f32_16x16x32_bf16 v[92:95], v[212:215], v[212:215], 0\n\t"
    "v_add_u32 v147, s69, v146\n\t"
    "v_fmac_f32 v121, v108, v72\n\t"
    "ds_add_rtn_u32 v148, v149, v150\n\t"
    "v_mfma_f32_16x16x32_bf16 v[92:95], v[216:219], v[216:219], v[92:95]\n\t"
    "v_fmac_f32 v120, v109, v73\n\t"
    "v_fmac_f32 v121, v110, v74\n\t"
    "buffer_load_dwordx3 v[136:138], v153, s[96:99], s66 offen\n\t"
    "v_mfma_f32_16x16x32_bf16 v[96:99], v[164:167], v[164:167], 0\n\t"
    "v_fmac_f32 v120, v111, v75\n\t"
    "v_fmac_f32 v121, v112, v76\n\t"
    "v_mfma_f32_16x16x32_bf16 v[96:99], v[168:171], v[168:171], v[96:99]\n\t"
    "v_fmac_f32 v120, v113, v77\n\t"
    "v_fmac_f32 v121, v114, v78\n\t"
    "buffer_load_dwordx4 v[140:143], v153, s[96:99], s66 offen offset:12\n\t"
    "v_mfma_f32_16x16x32_bf16 v[100:103], v[164:167], v[172:175], 0\n\t"
    "v_fmac_f32 v120, v115, v79\n\t"
    "v_fmac_f32 v121, v116, v80\n\t"
    "v_mfma_f32_16x16x32_bf16 v[100:103], v[168:171], v[176:179], v[100:103]\n\t"
    "v_fmac_f32 v120, v117, v81\n\t"
    "v_fmac_f32 v121, v118, v82\n\t"
    "s_waitcnt vmcnt(3)\n\t"
    "s_cmp_ge_u32 s55, s61\n\t"
    "s_cbranch_scc1 L_req1_%=\n\t"
    "L_back1_%=:\n\t"
    "v_mfma_f32_16x16x32_bf16 v[104:107], v[172:175], v[172:175], 0\n\t"
    "v_mad_u32_u24 v128, v128, s68, v152\n\t"
    "v_and_b32 v128, -16, v128\n\t"
    "v_mfma_f32_16x16x32_bf16 v[104:107], v[176:179], v[176:179], v[104:107]\n\t"
    "v_fmac_f32 v120, v119, v83\n\t"
    "v_add_f32 v154, v120, v121\n\t"
    "ds_write_b32 v147, v154\n\t"
    "v_mfma_f32_16x16x32_bf16 v[108:111], v[212:215], v[164:167], 0\n\t"
    "v_fma_f32 v120, v92, v84, v145\n\t"
    "v_mul_f32 v121, v96, v0\n\t"
    "v_mfma_f32_16x16x32_bf16 v[108:111], v[216:219], v[168:171], v[108:111]\n\t"
    "v_fmac_f32 v120, v97, v1\n\t"
    "v_fmac_f32 v121, v98, v2\n\t"
    "s_waitcnt lgkmcnt(8)\n\t"
    "v_mfma_f32_16x16x32_bf16 v[112:115], v[164:167], v[180:183], 0\n\t"
    "v_fmac_f32 v120, v99, v3\n\t"
    "v_fmac_f32 v121, v100, v4\n\t"
    "v_mfma_f32_16x16x32_bf16 v[112:115], v[168:171], v[184:187], v[112:115]\n\t"
    "v_fmac_f32 v120, v101, v5\n\t"
    "v_fmac_f32 v121, v102, v6\n\t"
    "v_mfma_f32_16x16x32_bf16 v[116:119], v[172:175], v[180:183], 0\n\t"
    "v_fmac_f32 v120, v103, v7\n\t"
    "v_fmac_f32 v121, v104, v8\n\t"
    "v_mfma_f32_16x16x32_bf16 v[116:119], v[176:179], v[184:187], v[116:119]\n\t"
    "v_fmac_f32 v120, v105, v9\n\t"
    "v_fmac_f32 v121, v106, v10\n\t"
    "ds_read_b128 v[156:159], v128 offset:0\n\t"
    "ds_read_b128 v[160:163], v128 offset:64\n\t"
    "v_mfma_f32_16x16x32_bf16 v[92:95], v[180:183], v[180:183], 0\n\t"
    "v_fmac_f32 v120, v107, v11\n\t"
    "v_mad_u32_u24 v129, v129, s68, v151\n\t"
    "v_mfma_f32_16x16x32_bf16 v[92:95], v[184:187], v[184:187], v[92:95]\n\t"
    "v_and_b32 v129, -16, v129\n\t"
    "v_fmac_f32 v121, v108, v85\n\t"
    "v_mfma_f32_16x16x32_bf16 v[96:99], v[212:215], v[172:175], 0\n\t"
    "v_fmac_f32 v120, v112, v12\n\t"
    "v_fmac_f32 v121, v113, v13\n\t"
    "v_mfma_f32_16x16x32_bf16 v[96:99], v[216:219], v[176:179], v[96:99]\n\t"
    "v_fmac_f32 v120, v114, v14\n\t"
    "v_fmac_f32 v121, v115, v15\n\t"
    "s_lshl_b32 s51, s55, 8\n\t"
    "s_add_u32 s51, s51, s50\n\t"
    "s_waitcnt lgkmcnt(8)\n\t"
    "v_mfma_f32_16x16x32_bf16 v[100:103], v[164:167], v[188:191], 0\n\t"
    "v_mad_u32_u24 v130, v130, s68, v151\n\t"
    "v_and_b32 v130, -16, v130\n\t"
    "v_mfma_f32_16x16x32_bf16 v[100:103], v[168:171], v[192:195], v[100:103]\n\t"
    "v_fmac_f32 v120, v116, v16\n\t"
    "v_fmac_f32 v121, v117, v17\n\t"
    "buffer_load_dword v145, v146, %12, s51 offen sc1\n\t"
    "v_mfma_f32_16x16x32_bf16 v[104:107], v[172:175], v[188:191], 0\n\t"
    "v_fmac_f32 v120, v118, v18\n\t"
    "v_fmac_f32 v121, v119, v19\n\t"
    "v_mfma_f32_16x16x32_bf16 v[104:107], v[176:179], v[192:195], v[104:107]\n\t"
    "v_fmac_f32 v120, v92, v20\n\t"
    "v_fmac_f32 v121, v93, v21\n\t"
    "s_waitcnt lgkmcnt(6)\n\t"
    "v_mfma_f32_16x16x32_bf16 v[108:111], v[164:167], v[196:199], 0\n\t"
    "v_fmac_f32 v120, v94, v22\n\t"
    "v_fmac_f32 v121, v95, v23\n\t"
    "v_mfma_f32_16x16x32_bf16 v[108:111], v[168:171], v[200:203], v[108:111]\n\t"
    "v_fmac_f32 v120, v96, v86\n\t"
    "v_mad_u32_u24 v132, v132, s68, v151\n\t"
    "v_mfma_f32_16x16x32_bf16 v[112:115], v[212:215], v[180:183], 0\n\t"
    "v_fmac_f32 v121, v100, v24\n\t"
    "v_fmac_f32 v120, v101, v25\n\t"
    "v_mfma_f32_16x16x32_bf16 v[112:115], v[216:219], v[184:187], v[112:115]\n\t"
    "v_fmac_f32 v121, v102, v26\n\t"
    "v_fmac_f32 v120, v103, v27\n\t"
    "s_waitcnt lgkmcnt(3)\n\t"
    "v_mfma_f32_16x16x32_bf16 v[116:119], v[164:167], v[204:207], 0\n\t"
    "v_readfirstlane_b32 s60, v148\n\t"
    "v_and_b32 v132, -16, v132\n\t"
    "v_mfma_f32_16x16x32_bf16 v[116:119], v[168:171], v[208:211], v[116:119]\n\t"
    "v_fmac_f32 v121, v104, v28\n\t"
    "v_fmac_f32 v120, v105, v29\n\t"
    "ds_read_b128 v[164:167], v129 offset:0\n\t"
    "ds_read_b128 v[168:171], v129 offset:64\n\t"
    "v_mfma_f32_16x16x32_bf16 v[92:95], v[172:175], v[196:199], 0\n\t"
    "v_fmac_f32 v121, v106, v30\n\t"
    "v_fmac_f32 v120, v107, v31\n\t"
    "v_mfma_f32_16x16x32_bf16 v[92:95], v[176:179], v[200:203], v[92:95]\n\t"
    "v_fmac_f32 v121, v108, v32\n\t"
    "v_fmac_f32 v120, v109, v33\n\t"
    "v_mfma_f32_16x16x32_bf16 v[96:99], v[172:175], v[204:207], 0\n\t"
    "v_fmac_f32 v121, v110, v34\n\t"
    "v_fmac_f32 v120, v111, v35\n\t"
    "v_mfma_f32_16x16x32_bf16 v[96:99], v[176:179], v[208:211], v[96:99]\n\t"
    "v_fmac_f32 v121, v112, v87\n\t"
    "v_mad_u32_u24 v133, v133, s68, v151\n\t"
    "ds_read_b128 v[172:175], v130 offset:0\n\t"
    "ds_read_b128 v[176:179], v130 offset:64\n\t"
    "v_mfma_f32_16x16x32_bf16 v[100:103], v[212:215], v[188:191], 0\n\t"
    "v_fmac_f32 v120, v116, v36\n\t"
    "v_fmac_f32 v121, v117, v37\n\t"
    "v_mfma_f32_16x16x32_bf16 v[100:103], v[216:219], v[192:195], v[100:103]\n\t"
    "v_fmac_f32 v120, v118, v38\n\t"
    "v_fmac_f32 v121, v119, v39\n\t"
    "v_mfma_f32_16x16x32_bf16 v[104:107], v[180:183], v[188:191], 0\n\t"
    "v_and_b32 v133, -16, v133\n\t"
    "v_fmac_f32 v120, v92, v40\n\t"
    "v_mfma_f32_16x16x32_bf16 v[104:107], v[184:187], v[192:195], v[104:107]\n\t"
    "v_fmac_f32 v121, v93, v41\n\t"
    "v_fmac_f32 v120, v94, v42\n\t"
    "v_mfma_f32_16x16x32_bf16 v[108:111], v[180:183], v[196:199], 0\n\t"
    "v_fmac_f32 v121, v95, v43\n\t"
    "v_fmac_f32 v120, v96, v44\n\t"
    "v_mfma_f32_16x16x32_bf16 v[108:111], v[184:187], v[200:203], v[108:111]\n\t"
    "v_fmac_f32 v121, v97, v45\n\t"
    "v_fmac_f32 v120, v98, v46\n\t"
    "v_mfma_f32_16x16x32_bf16 v[112:115], v[180:183], v[204:207], 0\n\t"
    "v_fmac_f32 v121, v99, v47\n\t"
    "v_fmac_f32 v120, v100, v88\n\t"
    "v_mfma_f32_16x16x32_bf16 v[112:115], v[184:187], v[208:211], v[112:115]\n\t"
    "v_mad_u32_u24 v134, v134, s68, v151\n\t"
    "v_and_b32 v134, -16, v134\n\t"
    "ds_read_b128 v[180:183], v132 offset:0\n\t"
    "ds_read_b128 v[184:187], v132 offset:64\n\t"
    "v_mfma_f32_16x16x32_bf16 v[116:119], v[212:215], v[196:199], 0\n\t"
    "v_fmac_f32 v121, v104, v48\n\t"
    "v_fmac_f32 v120, v105, v49\n\t"
    "v_mfma_f32_16x16x32_bf16 v[116:119], v[216:219], v[200:203], v[116:119]\n\t"
    "v_fmac_f32 v121, v106, v50\n\t"
    "v_fmac_f32 v120, v107, v51\n\t"
    "v_mfma_f32_16x16x32_bf16 v[92:95], v[188:191], v[188:191], 0\n\t"
    "v_mad_u32_u24 v135, v135, s68, v151\n\t"
    "v_and_b32 v135, -16, v135\n\t"
    "v_mfma_f32_16x16x32_bf16 v[92:95], v[192:195], v[192:195], v[92:95]\n\t"
    "v_fmac_f32 v121, v108, v52\n\t"
    "v_fmac_f32 v120, v109, v53\n\t"
    "v_mfma_f32_16x16x32_bf16 v[96:99], v[188:191], v[196:199], 0\n\t"
    "v_fmac_f32 v121, v110, v54\n\t"
    "v_fmac_f32 v120, v111, v55\n\t"
    "v_mfma_f32_16x16x32_bf16 v[96:99], v[192:195], v[200:203], v[96:99]\n\t"
    "v_fmac_f32 v121, v112, v56\n\t"
    "v_fmac_f32 v120, v113, v57\n\t"
    "v_mfma_f32_16x16x32_bf16 v[100:103], v[188:191], v[204:207], 0\n\t"
    "v_fmac_f32 v121, v114, v58\n\t"
    "v_fmac_f32 v120, v115, v59\n\t"
    "v_mfma_f32_16x16x32_bf16 v[100:103], v[192:195], v[208:211], v[100:103]\n\t"
    "v_fmac_f32 v121, v116, v89\n\t"
    "ds_read_b128 v[188:191], v133 offset:0\n\t"
    "ds_read_b128 v[192:195], v133 offset:64\n\t"
    "v_mfma_f32_16x16x32_bf16 v[104:107], v[212:215], v[204:207], 0\n\t"
    "v_fmac_f32 v120, v92, v60\n\t"
    "v_fmac_f32 v121, v93, v61\n\t"
    "v_mfma_f32_16x16x32_bf16 v[104:107], v[216:219], v[208:211], v[104:107]\n\t"
    "v_fmac_f32 v120, v94, v62\n\t"
    "v_fmac_f32 v121, v95, v63\n\t"
    "v_mfma_f32_16x16x32_bf16 v[108:111], v[196:199], v[196:199], 0\n\t"
    "v_fmac_f32 v120, v96, v64\n\t"
    "v_fmac_f32 v121, v97, v65\n\t"
    "v_mfma_f32_16x16x32_bf16 v[108:111], v[200:203], v[200:203], v[108:111]\n\t"
    "v_fmac_f32 v120, v98, v66\n\t"
    "v_fmac_f32 v121, v99, v67\n\t"
    "v_mfma_f32_16x16x32_bf16 v[112:115], v[196:199], v[204:207], 0\n\t"
    "v_fmac_f32 v120, v100, v68\n\t"
    "v_fmac_f32 v121, v101, v69\n\t"
    "v_mfma_f32_16x16x32_bf16 v[112:115], v[200:203], v[208:211], v[112:115]\n\t"
    "v_fmac_f32 v120, v102, v70\n\t"
    "v_fmac_f32 v121, v103, v71\n\t"
    "ds_read_b128 v[196:199], v134 offset:0\n\t"
    "ds_read_b128 v[200:203], v134 offset:64\n\t"
    "v_mfma_f32_16x16x32_bf16 v[116:119], v[204:207], v[204:207], 0\n\t"
    "v_fmac_f32 v120, v104, v90\n\t"
    "v_mfma_f32_16x16x32_bf16 v[116:119], v[208:211], v[208:211], v[116:119]\n\t"
    "ds_read_b128 v[204:207], v135 offset:0\n\t"
    "ds_read_b128 v[208:211], v135 offset:64\n\t"
    "s_mov_b32 s52, s53\n\t"
    "s_mov_b32 s53, s54\n\t"
    "s_mov_b32 s54, s55\n\t"
    "s_mov_b32 s55, s60\n\t"
    "s_cmp_lt_u32 s54, s64\n\t"
    "s_cbranch_scc1 L_pr1_%=\n\t"
    "s_setprio 0\n\t"
    "L_pr1_%=:\n\t"
    "s_cmp_lt_u32 s53, s64\n\t"
    "s_cbranch_scc1 L_pnn_%=\n\t"
    "L_done_%=:\n\t"
    "s_mul_i32 s69, s52, 272\n\t"
    "s_add_u32 s69, s69, 130048\n\t"
    "v_add_u32 v147, s69, v146\n\t"
    "v_fmac_f32 v121, v108, v72\n\t"
    "v_fmac_f32 v120, v109, v73\n\t"
    "v_fmac_f32 v121, v110, v74\n\t"
    "v_fmac_f32 v120, v111, v75\n\t"
    "v_fmac_f32 v121, v112, v76\n\t"
    "v_fmac_f32 v120, v113, v77\n\t"
    "v_fmac_f32 v121, v114, v78\n\t"
    "v_fmac_f32 v120, v115, v79\n\t"
    "v_fmac_f32 v121, v116, v80\n\t"
    "v_fmac_f32 v120, v117, v81\n\t"
    "v_fmac_f32 v121, v118, v82\n\t"
    "v_fmac_f32 v120, v119, v83\n\t"
    "v_add_f32 v154, v120, v121\n\t"
    "ds_write_b32 v147, v154\n\t"
    "v_lshrrev_b32 v147, 2, v146\n\t"
    "s_mov_b64 s[92:93], %17\n\t"
    "s_and_b32 s93, s93, 0xffff\n\t"
    "s_mov_b32 s94, 32768\n\t"
    "s_mov_b32 s95, 0x20000\n\t"
    "s_lshl_b32 s44, s37, 1\n\t"
    "v_and_b32 v155, 15, v147\n\t"
    "v_lshl_add_u32 v155, %16, 4, v155\n\t"
    "v_lshrrev_b32 v154, 4, v147\n\t"
    "v_mul_u32_u24 v122, 272, v155\n\t"
    "v_lshl_add_u32 v122, v154, 6, v122\n\t"
    "v_add_u32 v122, 130048, v122\n\t"
    "v_mul_u32_u24 v154, 2560, v154\n\t"
    "v_lshl_add_u32 v154, v155, 2, v154\n\t"
    "v_add_u32 v154, 151808, v154\n\t"
    "v_cmp_le_u32 vcc, 48, v147\n\t"
    "v_cmp_gt_u32 s[46:47], s38, v155\n\t"
    "v_lshlrev_b32 v155, 1, v155\n\t"
    "s_and_b64 vcc, vcc, s[46:47]\n\t"
    "s_waitcnt lgkmcnt(0)\n\t"
    "s_barrier\n\t"
    "s_cbranch_vccz L_fin_%=\n\t"
    "ds_read_b128 v[92:95], v122 offset:0\n\t"
    "ds_read_b128 v[96:99], v122 offset:16\n\t"
    "ds_read_b128 v[100:103], v122 offset:32\n\t"
    "ds_read_b128 v[104:107], v122 offset:48\n\t"
    "ds_read2_b32 v[108:109], v154 offset0:0 offset1:80\n\t"
    "ds_read2_b32 v[110:111], v154 offset0:160 offset1:240\n\t"
    "v_add_u32 v154, 1280, v154\n\t"
    "ds_read2_b32 v[112:113], v154 offset0:0 offset1:80\n\t"
    "ds_read2_b32 v[114:115], v154 offset0:160 offset1:240\n\t"
    "s_waitcnt lgkmcnt(6)\n\t"
    "v_add_f32 v92, v92, v96\n\t"
    "v_add_f32 v93, v93, v97\n\t"
    "v_add_f32 v94, v94, v98\n\t"
    "v_add_f32 v95, v95, v99\n\t"
    "s_waitcnt lgkmcnt(5)\n\t"
    "v_add_f32 v92, v92, v100\n\t"
    "v_add_f32 v93, v93, v101\n\t"
    "v_add_f32 v94, v94, v102\n\t"
    "v_add_f32 v95, v95, v103\n\t"
    "s_waitcnt lgkmcnt(4)\n\t"
    "v_add_f32 v92, v92, v104\n\t"
    "v_add_f32 v93, v93, v105\n\t"
    "v_add_f32 v94, v94, v106\n\t"
    "v_add_f32 v95, v95, v107\n\t"
    "s_waitcnt lgkmcnt(2)\n\t"
    "v_add_f32 v92, v92, v108\n\t"
    "v_add_f32 v93, v93, v109\n\t"
    "v_add_f32 v94, v94, v110\n\t"
    "v_add_f32 v95, v95, v111\n\t"
    "s_waitcnt lgkmcnt(0)\n\t"
    "v_add_f32 v92, v92, v112\n\t"
    "v_add_f32 v93, v93, v113\n\t"
    "v_add_f32 v94, v94, v114\n\t"
    "v_add_f32 v95, v95, v115\n\t"
    "v_add_f32 v92, v92, v93\n\t"
    "v_add_f32 v94, v94, v95\n\t"
    "v_add_f32 v92, v92, v94\n\t"
    "v_mov_b32 v93, v92\n\t"
    "s_nop 1\n\t"
    "v_permlane16_swap_b32 v93, v92\n\t"
    "s_nop 1\n\t"
    "v_add_f32 v92, v92, v93\n\t"
    "v_mov_b32 v93, v92\n\t"
    "s_nop 1\n\t"
    "v_permlane32_swap_b32 v93, v92\n\t"
    "s_nop 1\n\t"
    "v_add_f32 v92, v92, v93\n\t"
    "v_add_f32 v92, s39, v92\n\t"
    "v_cvt_pk_bf16_f32 v92, v92, v92\n\t"
    "s_mov_b64 exec, vcc\n\t"
    "buffer_store_short v92, v155, s[92:95], s44 offen\n\t"
    "s_mov_b64 exec, -1\n\t"
    "s_branch L_fin_%=\n\t"
    "L_req0_%=:\n\t"
    "s_cmp_lg_u32 s61, 0\n\t"
    "s_cbranch_scc1 L_req0b_%=\n\t"
    "s_bfe_u32 s65, s84, 0xc0002\n\t"
    "s_add_u32 s65, s65, s36\n\t"
    "buffer_store_dword v236, v125, %12, s65 offen offset:0 sc1\n\t"
    "buffer_store_dword v237, v125, %12, s65 offen offset:256 sc1\n\t"
    "s_bfe_u32 s65, s85, 0xc0002\n\t"
    "s_add_u32 s65, s65, s36\n\t"
    "buffer_store_dword v238, v125, %12, s65 offen offset:0 sc1\n\t"
    "buffer_store_dword v239, v125, %12, s65 offen offset:256 sc1\n\t"
    "s_bfe_u32 s65, s86, 0xc0002\n\t"
    "s_add_u32 s65, s65, s36\n\t"
    "buffer_store_dword v240, v125, %12, s65 offen offset:0 sc1\n\t"
    "buffer_store_dword v241, v125, %12, s65 offen offset:256 sc1\n\t"
    "s_bfe_u32 s65, s87, 0xc0002\n\t"
    "s_add_u32 s65, s65, s36\n\t"
    "buffer_store_dword v242, v125, %12, s65 offen offset:0 sc1\n\t"
    "buffer_store_dword v243, v125, %12, s65 offen offset:256 sc1\n\t"
    "s_bfe_u32 s65, s88, 0xc0002\n\t"
    "s_add_u32 s65, s65, s36\n\t"
    "buffer_store_dword v244, v125, %12, s65 offen offset:0 sc1\n\t"
    "buffer_store_dword v245, v125, %12, s65 offen offset:256 sc1\n\t"
    "s_bfe_u32 s65, s89, 0xc0002\n\t"
    "s_add_u32 s65, s65, s36\n\t"
    "buffer_store_dword v246, v125, %12, s65 offen offset:0 sc1\n\t"
    "buffer_store_dword v247, v125, %12, s65 offen offset:256 sc1\n\t"
    "s_bfe_u32 s65, s90, 0xc0002\n\t"
    "s_add_u32 s65, s65, s36\n\t"
    "buffer_store_dword v248, v125, %12, s65 offen offset:0 sc1\n\t"
    "buffer_store_dword v249, v125, %12, s65 offen offset:256 sc1\n\t"
    "s_bfe_u32 s65, s91, 0xc0002\n\t"
    "s_add_u32 s65, s65, s36\n\t"
    "buffer_store_dword v250, v125, %12, s65 offen offset:0 sc1\n\t"
    "buffer_store_dword v251, v125, %12, s65 offen offset:256 sc1\n\t"
    "s_sub_u32 s61, s64, 3\n\t"
    "s_branch L_back0_%=\n\t"
    "L_req0b_%=:\n\t"
    "s_cmp_lg_u32 s61, 1\n\t"
    "s_cbranch_scc1 L_req0c_%=\n\t"
    "v_lshrrev_b32 v154, 16, v122\n\t"
    "v_lshl_add_u32 v154, %16, 2, v154\n\t"
    "v_mul_u32_u24 v154, 320, v154\n\t"
    "v_and_b32 v122, 0xf0, v122\n\t"
    "v_cmp_gt_u32 vcc, 64, v122\n\t"
    "v_add3_u32 v122, v122, v154, s45\n\t"
    "s_mov_b32 s61, -1\n\t"
    "v_add_f32 v124, v220, v224\n\t"
    "v_add_f32 v154, v228, v232\n\t"
    "v_add_f32 v124, v124, v154\n\t"
    "v_add_f32 v125, v221, v225\n\t"
    "v_add_f32 v154, v229, v233\n\t"
    "v_add_f32 v125, v125, v154\n\t"
    "v_add_f32 v126, v222, v226\n\t"
    "v_add_f32 v154, v230, v234\n\t"
    "v_add_f32 v126, v126, v154\n\t"
    "v_add_f32 v127, v223, v227\n\t"
    "v_add_f32 v154, v231, v235\n\t"
    "v_add_f32 v127, v127, v154\n\t"
    "ds_write_b128 v122, v[124:127] offset:0\n\t"
    "s_cmp_le_u32 s38, 64\n\t"
    "s_cbranch_scc1 L_back0_%=\n\t"
    "v_add_f32 v124, v236, v240\n\t"
    "v_add_f32 v154, v244, v248\n\t"
    "v_add_f32 v124, v124, v154\n\t"
    "v_add_f32 v125, v237, v241\n\t"
    "v_add_f32 v154, v245, v249\n\t"
    "v_add_f32 v125, v125, v154\n\t"
    "v_add_f32 v126, v238, v242\n\t"
    "v_add_f32 v154, v246, v250\n\t"
    "v_add_f32 v126, v126, v154\n\t"
    "v_add_f32 v127, v239, v243\n\t"
    "v_add_f32 v154, v247, v251\n\t"
    "v_add_f32 v127, v127, v154\n\t"
    "s_and_saveexec_b64 s[46:47], vcc\n\t"
    "ds_write_b128 v122, v[124:127] offset:256\n\t"
    "s_mov_b64 exec, s[46:47]\n\t"
    "s_branch L_back0_%=\n\t"
    "L_req0c_%=:\n\t"
    "s_lshl_b32 s46, %14, 7\n\t"
    "s_add_u32 s46, s46, 48245760\n\t"
    "s_mov_b32 s47, 1048576\n\t"
    "s_mov_b64 exec, 1\n\t"
    "s_cmp_ge_u32 s55, s64\n\t"
    "s_cbranch_scc1 L_pq0_%=\n\t"
    "buffer_load_dword v123, off, %12, s46 sc1\n\t"
    "s_mov_b64 exec, -1\n\t"
    "s_branch L_back0_%=\n\t"
    "L_pq0_%=:\n\t"
    "v_cmp_eq_u32 vcc, 0, v123\n\t"
    "s_cbranch_vccz L_in0_%=\n\t"
    "L_poll0_%=:\n\t"
    "buffer_load_dword v155, off, %12, s46 sc1\n\t"
    "s_sub_u32 s47, s47, 1\n\t"
    "s_cmp_eq_u32 s47, 0\n\t"
    "s_cbranch_scc1 L_in0_%=\n\t"
    "s_waitcnt vmcnt(0)\n\t"
    "v_cmp_eq_u32 vcc, 0, v155\n\t"
    "s_cbranch_vccz L_in0_%=\n\t"
    "s_sleep 8\n\t"
    "s_branch L_poll0_%=\n\t"
    "L_in0_%=:\n\t"
    "s_mov_b64 exec, -1\n\t"
    "s_lshl_b32 s46, s37, 2\n\t"
    "s_lshl_b32 s47, %16, 20\n\t"
    "s_add_u32 s46, s46, s47\n\t"
    "s_add_u32 s46, s46, 17825792\n\t"
    "v_and_b32 v155, 0xc0, v122\n\t"
    "v_lshl_add_u32 v155, v155, 24, v122\n\t"
    "buffer_load_dwordx4 v[220:223], v122, %12, s46 offen sc1\n\t"
    "s_add_u32 s46, s46, 0x40000\n\t"
    "buffer_load_dwordx4 v[224:227], v122, %12, s46 offen sc1\n\t"
    "s_add_u32 s46, s46, 0x40000\n\t"
    "buffer_load_dwordx4 v[228:231], v122, %12, s46 offen sc1\n\t"
    "s_add_u32 s46, s46, 0x40000\n\t"
    "buffer_load_dwordx4 v[232:235], v122, %12, s46 offen sc1\n\t"
    "s_add_u32 s46, s46, 0x40000\n\t"
    "s_mov_b32 s61, 1\n\t"
    "s_cmp_le_u32 s38, 64\n\t"
    "s_cbranch_scc1 L_back0_%=\n\t"
    "s_sub_u32 s46, s46, 0x40000\n\t"
    "buffer_load_dwordx4 v[248:251], v155, %12, s46 offen offset:256 sc1\n\t"
    "s_sub_u32 s46, s46, 0x40000\n\t"
    "buffer_load_dwordx4 v[244:247], v155, %12, s46 offen offset:256 sc1\n\t"
    "s_sub_u32 s46, s46, 0x40000\n\t"
    "buffer_load_dwordx4 v[240:243], v155, %12, s46 offen offset:256 sc1\n\t"
    "s_sub_u32 s46, s46, 0x40000\n\t"
    "buffer_load_dwordx4 v[236:239], v155, %12, s46 offen offset:256 sc1\n\t"
    "s_branch L_back0_%=\n\t"
    "L_req1_%=:\n\t"
    "s_cmp_lg_u32 s61, 1\n\t"
    "s_cbranch_scc1 L_req1c_%=\n\t"
    "v_lshrrev_b32 v154, 16, v122\n\t"
    "v_lshl_add_u32 v154, %16, 2, v154\n\t"
    "v_mul_u32_u24 v154, 320, v154\n\t"
    "v_and_b32 v122, 0xf0, v122\n\t"
    "v_cmp_gt_u32 vcc, 64, v122\n\t"
    "v_add3_u32 v122, v122, v154, s45\n\t"
    "s_mov_b32 s61, -1\n\t"
    "v_add_f32 v124, v220, v224\n\t"
    "v_add_f32 v154, v228, v232\n\t"
    "v_add_f32 v124, v124, v154\n\t"
    "v_add_f32 v125, v221, v225\n\t"
    "v_add_f32 v154, v229, v233\n\t"
    "v_add_f32 v125, v125, v154\n\t"
    "v_add_f32 v126, v222, v226\n\t"
    "v_add_f32 v154, v230, v234\n\t"
    "v_add_f32 v126, v126, v154\n\t"
    "v_add_f32 v127, v223, v227\n\t"
    "v_add_f32 v154, v231, v235\n\t"
    "v_add_f32 v127, v127, v154\n\t"
    "ds_write_b128 v122, v[124:127] offset:0\n\t"
    "s_cmp_le_u32 s38, 64\n\t"
    "s_cbranch_scc1 L_back1_%=\n\t"
    "v_add_f32 v124, v236, v240\n\t"
    "v_add_f32 v154, v244, v248\n\t"
    "v_add_f32 v124, v124, v154\n\t"
    "v_add_f32 v125, v237, v241\n\t"
    "v_add_f32 v154, v245, v249\n\t"
    "v_add_f32 v125, v125, v154\n\t"
    "v_add_f32 v126, v238, v242\n\t"
    "v_add_f32 v154, v246, v250\n\t"
    "v_add_f32 v126, v126, v154\n\t"
    "v_add_f32 v127, v239, v243\n\t"
    "v_add_f32 v154, v247, v251\n\t"
    "v_add_f32 v127, v127, v154\n\t"
    "s_and_saveexec_b64 s[46:47], vcc\n\t"
    "ds_write_b128 v122, v[124:127] offset:256\n\t"
    "s_mov_b64 exec, s[46:47]\n\t"
    "s_branch L_back1_%=\n\t"
    "L_req1c_%=:\n\t"
    "s_lshl_b32 s46, %14, 7\n\t"
    "s_add_u32 s46, s46, 48245760\n\t"
    "s_mov_b32 s47, 1048576\n\t"
    "s_mov_b64 exec, 1\n\t"
    "s_cmp_ge_u32 s55, s64\n\t"
    "s_cbranch_scc1 L_pq1_%=\n\t"
    "buffer_load_dword v123, off, %12, s46 sc1\n\t"
    "s_mov_b64 exec, -1\n\t"
    "s_branch L_back1_%=\n\t"
    "L_pq1_%=:\n\t"
    "v_cmp_eq_u32 vcc, 0, v123\n\t"
    "s_cbranch_vccz L_in1_%=\n\t"
    "L_poll1_%=:\n\t"
    "buffer_load_dword v155, off, %12, s46 sc1\n\t"
    "s_sub_u32 s47, s47, 1\n\t"
    "s_cmp_eq_u32 s47, 0\n\t"
    "s_cbranch_scc1 L_in1_%=\n\t"
    "s_waitcnt vmcnt(0)\n\t"
    "v_cmp_eq_u32 vcc, 0, v155\n\t"
    "s_cbranch_vccz L_in1_%=\n\t"
    "s_sleep 8\n\t"
    "s_branch L_poll1_%=\n\t"
    "L_in1_%=:\n\t"
    "s_mov_b64 exec, -1\n\t"
    "s_lshl_b32 s46, s37, 2\n\t"
    "s_lshl_b32 s47, %16, 20\n\t"
    "s_add_u32 s46, s46, s47\n\t"
    "s_add_u32 s46, s46, 17825792\n\t"
    "v_and_b32 v155, 0xc0, v122\n\t"
    "v_lshl_add_u32 v155, v155, 24, v122\n\t"
    "buffer_load_dwordx4 v[220:223], v122, %12, s46 offen sc1\n\t"
    "s_add_u32 s46, s46, 0x40000\n\t"
    "buffer_load_dwordx4 v[224:227], v122, %12, s46 offen sc1\n\t"
    "s_add_u32 s46, s46, 0x40000\n\t"
    "buffer_load_dwordx4 v[228:231], v122, %12, s46 offen sc1\n\t"
    "s_add_u32 s46, s46, 0x40000\n\t"
    "buffer_load_dwordx4 v[232:235], v122, %12, s46 offen sc1\n\t"
    "s_add_u32 s46, s46, 0x40000\n\t"
    "s_mov_b32 s61, 1\n\t"
    "s_cmp_le_u32 s38, 64\n\t"
    "s_cbranch_scc1 L_back1_%=\n\t"
    "s_sub_u32 s46, s46, 0x40000\n\t"
    "buffer_load_dwordx4 v[248:251], v155, %12, s46 offen offset:256 sc1\n\t"
    "s_sub_u32 s46, s46, 0x40000\n\t"
    "buffer_load_dwordx4 v[244:247], v155, %12, s46 offen offset:256 sc1\n\t"
    "s_sub_u32 s46, s46, 0x40000\n\t"
    "buffer_load_dwordx4 v[240:243], v155, %12, s46 offen offset:256 sc1\n\t"
    "s_sub_u32 s46, s46, 0x40000\n\t"
    "buffer_load_dwordx4 v[236:239], v155, %12, s46 offen offset:256 sc1\n\t"
    "s_branch L_back1_%=\n\t"
    "L_sig_%=:\n\t"
    "v_subrev_u32 v155, s49, v149\n\t"
    "v_add_u32 v155, 1024, v155\n\t"
    "s_waitcnt vmcnt(0)\n\t"
    "ds_add_rtn_u32 v148, v155, v150\n\t"
    "s_mov_b32 s45, 151808\n\t"
    "s_mov_b32 s46, 48234496\n\t"
    "v_mov_b32 v155, 1\n\t"
    "s_waitcnt lgkmcnt(0)\n\t"
    "v_readfirstlane_b32 s47, v148\n\t"
    "s_nop 0\n\t"
    "s_cmp_lg_u32 s47, 7\n\t"
    "s_cbranch_scc1 L_sigb_%=\n\t"
    "s_mov_b64 exec, 1\n\t"
    "buffer_atomic_add v155, off, %12, s46 sc0 sc1\n\t"
    "s_mov_b64 exec, -1\n\t"
    "s_waitcnt vmcnt(0)\n\t"
    "v_readfirstlane_b32 s47, v155\n\t"
    "v_lshlrev_b32 v155, 5, v146\n\t"
    "s_cmp_lg_u32 s47, 255\n\t"
    "s_cbranch_scc1 L_sigb_%=\n\t"
    "s_mov_b32 s46, 48245760\n\t"
    "buffer_store_dword v150, v155, %12, s46 offen sc1\n\t"
    "s_add_u32 s46, s46, 0x2000\n\t"
    "buffer_store_dword v150, v155, %12, s46 offen sc1\n\t"
    "s_add_u32 s46, s46, 0x2000\n\t"
    "buffer_store_dword v150, v155, %12, s46 offen sc1\n\t"
    "s_add_u32 s46, s46, 0x2000\n\t"
    "buffer_store_dword v150, v155, %12, s46 offen sc1\n\t"
    "s_add_u32 s46, s46, 0x2000\n\t"
    "s_branch L_sigb_%=\n\t"
    "L_fin_%=:\n\t"
    "s_branch L_pend_%=\n\t"
    "L_pub_%=:\n\t"
    "s_memrealtime s[46:47]\n\t"
    "s_lshl_b32 s49, %14, 2\n\t"
    "s_add_u32 s49, s49, 48244736\n\t"
    "s_waitcnt lgkmcnt(0)\n\t"
    "s_sub_u32 s47, s46, s44\n\t"
    "s_mul_i32 s47, s47, 236\n\t"
    "s_lshr_b32 s47, s47, 8\n\t"
    "s_add_u32 s46, s46, s47\n\t"
    "s_or_b32 s46, s46, 1\n\t"
    "v_mov_b32 v83, s46\n\t"
    "s_mov_b64 exec, 1\n\t"
    "buffer_store_dword v83, off, %12, s49 sc1\n\t"
    "s_mov_b64 exec, -1\n\t"
    "s_branch L_pubb_%=\n\t"
    "L_xw0_%=:\n\t"
    "s_mov_b32 s68, 1048576\n\t"
    "L_xv0_%=:\n\t"
    "s_sleep 4\n\t"
    "s_mov_b64 exec, 1\n\t"
    "buffer_load_dword v164, off, %12, s93 offset:0 sc1\n\t"
    "s_mov_b64 exec, -1\n\t"
    "s_sub_u32 s68, s68, 1\n\t"
    "s_cmp_eq_u32 s68, 0\n\t"
    "s_cbranch_scc1 L_xwb0_%=\n\t"
    "s_waitcnt vmcnt(0)\n\t"
    "v_readfirstlane_b32 s65, v164\n\t"
    "s_nop 0\n\t"
    "s_cmp_eq_u32 s65, 0\n\t"
    "s_cbranch_scc1 L_xv0_%=\n\t"
    "s_branch L_xwb0_%=\n\t"
    "L_xw1_%=:\n\t"
    "s_mov_b32 s68, 1048576\n\t"
    "L_xv1_%=:\n\t"
    "s_sleep 4\n\t"
    "s_mov_b64 exec, 1\n\t"
    "buffer_load_dword v164, off, %12, s93 offset:128 sc1\n\t"
    "s_mov_b64 exec, -1\n\t"
    "s_sub_u32 s68, s68, 1\n\t"
    "s_cmp_eq_u32 s68, 0\n\t"
    "s_cbranch_scc1 L_xwb1_%=\n\t"
    "s_waitcnt vmcnt(0)\n\t"
    "v_readfirstlane_b32 s65, v164\n\t"
    "s_nop 0\n\t"
    "s_cmp_eq_u32 s65, 0\n\t"
    "s_cbranch_scc1 L_xv1_%=\n\t"
    "s_branch L_xwb1_%=\n\t"
    "L_xw2_%=:\n\t"
    "s_mov_b32 s68, 1048576\n\t"
    "L_xv2_%=:\n\t"
    "s_sleep 4\n\t"
    "s_mov_b64 exec, 1\n\t"
    "buffer_load_dword v164, off, %12, s93 offset:256 sc1\n\t"
    "s_mov_b64 exec, -1\n\t"
    "s_sub_u32 s68, s68, 1\n\t"
    "s_cmp_eq_u32 s68, 0\n\t"
    "s_cbranch_scc1 L_xwb2_%=\n\t"
    "s_waitcnt vmcnt(0)\n\t"
    "v_readfirstlane_b32 s65, v164\n\t"
    "s_nop 0\n\t"
    "s_cmp_eq_u32 s65, 0\n\t"
    "s_cbranch_scc1 L_xv2_%=\n\t"
    "s_branch L_xwb2_%=\n\t"
    "L_xw3_%=:\n\t"
    "s_mov_b32 s68, 1048576\n\t"
    "L_xv3_%=:\n\t"
    "s_sleep 4\n\t"
    "s_mov_b64 exec, 1\n\t"
    "buffer_load_dword v164, off, %12, s93 offset:384 sc1\n\t"
    "s_mov_b64 exec, -1\n\t"
    "s_sub_u32 s68, s68, 1\n\t"
    "s_cmp_eq_u32 s68, 0\n\t"
    "s_cbranch_scc1 L_xwb3_%=\n\t"
    "s_waitcnt vmcnt(0)\n\t"
    "v_readfirstlane_b32 s65, v164\n\t"
    "s_nop 0\n\t"
    "s_cmp_eq_u32 s65, 0\n\t"
    "s_cbranch_scc1 L_xv3_%=\n\t"
    "s_branch L_xwb3_%=\n\t"
    "L_mp2_%=:\n\t"
    "v_lshrrev_b32 v57, 4, v127\n\t"
    "v_add_u32 v57, 32, v57\n\t"
    "s_and_b32 s65, %14, 7\n\t"
    "s_add_u32 s65, s65, 24\n\t"
    "v_writelane_b32 v57, s65, 16\n\t"
    "v_mov_b32 v58, 16\n\t"
    "v_lshlrev_b32 v57, 7, v57\n\t"
    "v_writelane_b32 v58, 32, 16\n\t"
    "s_mov_b32 s65, 48234496\n\t"
    "v_mov_b32 v56, 32\n\t"
    "s_mov_b32 s66, 1048576\n\t"
    "s_mov_b32 s65, 48234496\n\t"
    "s_mov_b64 exec, 0x1ffff\n\t"
    "buffer_load_dword v56, v57, %12, s65 offen sc1\n\t"
    "s_mov_b64 exec, -1\n\t"
    "s_branch L_mp2b_%=\n\t"
    "L_mp3_%=:\n\t"
    "v_cmp_gt_u32 vcc, v58, v56\n\t"
    "s_cbranch_vccz L_mp3b_%=\n\t"
    "s_sub_u32 s66, s66, 1\n\t"
    "s_cmp_eq_u32 s66, 0\n\t"
    "s_cbranch_scc1 L_mp3b_%=\n\t"
    "s_sleep 8\n\t"
    "s_mov_b32 s65, 48234496\n\t"
    "s_mov_b64 exec, 0x1ffff\n\t"
    "buffer_load_dword v56, v57, %12, s65 offen sc1\n\t"
    "s_mov_b64 exec, -1\n\t"
    "s_waitcnt vmcnt(0)\n\t"
    "s_branch L_mp3_%=\n\t"
    "L_pend_%=:\n\t"
    ".Ldcp_end:\n\t"
    :: "s"(s_idx), "s"(p_wp), "s"(p_w1), "n"(0), "n"(0), "n"(0), "s"(pb1), "s"(pw2), "s"(pb2), "s"(b2s),
       "n"(0),
       "n"(0), "s"(s_ws), "n"(0), "s"(bid), "s"(s_tab), "s"(w), "s"(p_out),
       "s"((unsigned)(unsigned long long)idx), "s"((unsigned)((unsigned long long)idx >> 32) & 0xFFFFu)
    : "memory", "vcc", "scc", "m0", "s36", "s37", "s38", "s39", "s40", "s41", "s42", "s43", "s44", "s45", "s46", "s47", "s48", "s49", "s50", "s51", "s52", "s53", "s54", "s55", "s56", "s57", "s58", "s59", "s60", "s61", "s62", "s63", "s64", "s65", "s66", "s67", "s68", "s69", "s70", "s71", "s72", "s73", "s74", "s75", "s76", "s77", "s78", "s79", "s80", "s81", "s82", "s83", "s84", "s85", "s86", "s87", "s88", "s89", "s90", "s91", "s92", "s93", "s94", "s95", "s96", "s97", "s98", "s99", "v0", "v1", "v2", "v3", "v4", "v5", "v6", "v7", "v8", "v9", "v10", "v11", "v12", "v13", "v14", "v15", "v16", "v17", "v18", "v19", "v20", "v21", "v22", "v23", "v24", "v25", "v26", "v27", "v28", "v29", "v30", "v31", "v32", "v33", "v34", "v35", "v36", "v37", "v38", "v39", "v40", "v41", "v42", "v43", "v44", "v45", "v46", "v47", "v48", "v49", "v50", "v51", "v52", "v53", "v54", "v55", "v56", "v57", "v58", "v59", "v60", "v61", "v62", "v63", "v64", "v65", "v66", "v67", "v68", "v69", "v70", "v71", "v72", "v73", "v74", "v75", "v76", "v77", "v78", "v79", "v80", "v81", "v82", "v83", "v84", "v85", "v86", "v87", "v88", "v89", "v90", "v91", "v92", "v93", "v94", "v95", "v96", "v97", "v98", "v99", "v100", "v101", "v102", "v103", "v104", "v105", "v106", "v107", "v108", "v109", "v110", "v111", "v112", "v113", "v114", "v115", "v116", "v117", "v118", "v119", "v120", "v121", "v122", "v123", "v124", "v125", "v126", "v127", "v128", "v129", "v130", "v131", "v132", "v133", "v134", "v135", "v136", "v137", "v138", "v139", "v140", "v141", "v142", "v143", "v144", "v145", "v146", "v147", "v148", "v149", "v150", "v151", "v152", "v153", "v154", "v155", "v156", "v157", "v158", "v159", "v160", "v161", "v162", "v163", "v164", "v165", "v166", "v167", "v168", "v169", "v170", "v171", "v172", "v173", "v174", "v175", "v176", "v177", "v178", "v179", "v180", "v181", "v182", "v183", "v184", "v185", "v186", "v187", "v188", "v189", "v190", "v191", "v192", "v193", "v194", "v195", "v196", "v197", "v198", "v199", "v200", "v201", "v202", "v203", "v204", "v205", "v206", "v207", "v208", "v209", "v210", "v211", "v212", "v213", "v214", "v215", "v216", "v217", "v218", "v219", "v220", "v221", "v222", "v223", "v224", "v225", "v226", "v227", "v228", "v229", "v230", "v231", "v232", "v233", "v234", "v235", "v236", "v237", "v238", "v239", "v240", "v241", "v242", "v243", "v244", "v245", "v246", "v247", "v248", "v249", "v250", "v251", "v252", "v253", "v254", "v255");
}

extern "C" void kernel_launch(void* const* d_in, const int* in_sizes, int n_in, void* d_out, int out_size, void* d_ws,
                              size_t ws_size, hipStream_t stream) {
  const int* idx = (const int*)d_in[0];
  const unsigned short* table = (const unsigned short*)d_in[1];
  const unsigned short* w_p = (const unsigned short*)d_in[2];
  const unsigned short* w1 = (const unsigned short*)d_in[3];
  const unsigned short* b1 = (const unsigned short*)d_in[4];
  const unsigned short* w2 = (const unsigned short*)d_in[5];
  const unsigned short* b2 = (const unsigned short*)d_in[6];
  char* ws = (char*)d_ws;
  dcp_all<<<dim3(256), dim3(512), 0, stream>>>(idx, table, w_p, w1, b1, w2, b2, ws, (__hip_bfloat16*)d_out);
}
```

```cpp
#include <hip/hip_runtime.h>
#include <hip/hip_bf16.h>
#include <stdint.h>

typedef __attribute__((ext_vector_type(2))) unsigned u32x2;

constexpr int BATCH = 16384, NF = 100, DIM = 128, VOC = 1000, HID = 512;
constexpr size_t NWIN = 13;
constexpr size_t WS_IDX = 0;
constexpr size_t IDX_STREAM = 212992;
constexpr size_t WS_W1F = 3407872;
constexpr size_t W1F_SLAB = 102400;
constexpr size_t WS_TF = 16515072;
constexpr size_t WS_WT = 16777216;
constexpr size_t WS_PART = 17825792;
constexpr size_t WS_P0 = 26214400;
constexpr size_t WS_XCH = 30408704;
constexpr size_t XCH_PITCH = 69632;
constexpr size_t WS_CNT = 48234496;
constexpr size_t WS_CH = 48242688;
constexpr size_t WS_RP = 48243712;
constexpr size_t WS_TS = 48244736;
constexpr size_t WS_RDY = 48245760;
constexpr size_t WS_DBG = 48278528;
constexpr size_t WS_END = 48671744;

__device__ __forceinline__ __amdgpu_buffer_rsrc_t mk_srd(const void* p, unsigned bytes) {
  return __builtin_amdgcn_make_buffer_rsrc((void*)p, (short)0, (int)bytes, 0x00020000);
}

__global__ __launch_bounds__(512, 2) void dcp_all(const int* __restrict__ idx, const unsigned short* __restrict__ table,
                                                  const unsigned short* __restrict__ w_p, const unsigned short* __restrict__ w1,
                                                  const unsigned short* __restrict__ b1, const unsigned short* __restrict__ w2,
                                                  const unsigned short* __restrict__ b2, char* __restrict__ ws,
                                                  __hip_bfloat16* __restrict__ out) {
  __shared__ __attribute__((aligned(1024))) char smem[163840];
  const int w = __builtin_amdgcn_readfirstlane(threadIdx.x >> 6) + (int)(uintptr_t)smem;
  const int bid = blockIdx.x;
  asm volatile(
    "s_getpc_b64 s[36:37]\n\t"
    ".Ldcp_pc:\n\t"
    "s_bfe_u32 s38, %0, 0x40003\n\t"
    "s_lshl_b32 s38, s38, 13\n\t"
    "v_and_b32 v122, 63, %1\n\t"
    "v_lshlrev_b32 v122, 7, v122\n\t"
    "v_add_u32 v122, s38, v122\n\t"
    "v_min_u32 v122, .Ldcp_end-.Ldcp_pc-4, v122\n\t"
    "s_cmp_lg_u32 %2, 3\n\t"
    "s_cbranch_scc1 L_nopf_%=\n\t"
    "global_load_dword v123, v122, s[36:37]\n\t"
    "L_nopf_%=:\n\t"
    :: "s"(bid), "v"(threadIdx.x), "s"(w) : "memory", "scc", "s36", "s37", "s38", "s74", "s75", "v122", "v123");
  const int xcd = bid & 7, loc = bid >> 3;
  const int cg = xcd * 16 + (loc >> 1);
  const __amdgpu_buffer_rsrc_t s_idx = mk_srd(idx + (size_t)bid * 64 * NF, (unsigned)((BATCH - bid * 64) * NF * 4));
  const __amdgpu_buffer_rsrc_t s_tab = mk_srd(table, VOC * DIM * 2);
  const __amdgpu_buffer_rsrc_t s_ws = mk_srd(ws, (unsigned)WS_END);
  const unsigned short* pb1 = b1 + 4 * cg;
  const unsigned short* pw2 = w2 + 4 * cg;
  const unsigned long long pb2 = (unsigned long long)b2 & ~3ull;
  const unsigned b2s = ((unsigned)(unsigned long long)b2 & 2u) * 8u;
  const unsigned long long p_wp = (unsigned long long)w_p & 0xFFFFFFFFFFFFull, p_w1 = (unsigned long long)w1 & 0xFFFFFFFFFFFFull;
  const unsigned long long p_out = (unsigned long long)out & 0xFFFFFFFFFFFFull;
  asm volatile(
    "v_mbcnt_lo_u32_b32 v147, -1, 0\n\t"
    "v_mbcnt_hi_u32_b32 v147, -1, v147\n\t"
    "s_mov_b64 s[92:93], %1\n\t"
    "s_mov_b32 s94, 20000\n\t"
    "s_mov_b32 s95, 0x20000\n\t"
    "s_lshr_b32 s46, %14, 3\n\t"
    "s_lshl_b32 s51, s46, 2\n\t"
    "v_lshlrev_b32 v122, 4, v147\n\t"
    "s_cmp_ge_u32 %16, 2\n\t"
    "s_cbranch_scc1 L_aimg_%=\n\t"
    "s_lshl_b32 s44, %16, 10\n\t"
    "s_add_u32 m0, s44, 140288\n\t"
    "s_nop 0\n\t"
    "buffer_load_dwordx4 v122, s[92:95], s44 offen lds\n\t"
    "s_add_u32 s44, s44, 0x800\n\t"
    "s_add_u32 m0, s44, 140288\n\t"
    "s_nop 0\n\t"
    "buffer_load_dwordx4 v122, s[92:95], s44 offen lds\n\t"
    "s_add_u32 s44, s44, 0x800\n\t"
    "s_add_u32 m0, s44, 140288\n\t"
    "s_nop 0\n\t"
    "buffer_load_dwordx4 v122, s[92:95], s44 offen lds\n\t"
    "s_add_u32 s44, s44, 0x800\n\t"
    "s_add_u32 m0, s44, 140288\n\t"
    "s_nop 0\n\t"
    "buffer_load_dwordx4 v122, s[92:95], s44 offen lds\n\t"
    "s_add_u32 s44, s44, 0x800\n\t"
    "s_add_u32 m0, s44, 140288\n\t"
    "s_nop 0\n\t"
    "buffer_load_dwordx4 v122, s[92:95], s44 offen lds\n\t"
    "s_add_u32 s44, s44, 0x800\n\t"
    "s_add_u32 m0, s44, 140288\n\t"
    "s_nop 0\n\t"
    "buffer_load_dwordx4 v122, s[92:95], s44 offen lds\n\t"
    "s_add_u32 s44, s44, 0x800\n\t"
    "s_add_u32 m0, s44, 140288\n\t"
    "s_nop 0\n\t"
    "buffer_load_dwordx4 v122, s[92:95], s44 offen lds\n\t"
    "s_add_u32 s44, s44, 0x800\n\t"
    "s_add_u32 m0, s44, 140288\n\t"
    "s_nop 0\n\t"
    "buffer_load_dwordx4 v122, s[92:95], s44 offen lds\n\t"
    "s_add_u32 s44, s44, 0x800\n\t"
    "s_add_u32 m0, s44, 140288\n\t"
    "s_nop 0\n\t"
    "buffer_load_dwordx4 v122, s[92:95], s44 offen lds\n\t"
    "s_add_u32 s44, s44, 0x800\n\t"
    "s_add_u32 m0, s44, 140288\n\t"
    "s_nop 0\n\t"
    "buffer_load_dwordx4 v122, s[92:95], s44 offen lds\n\t"
    "s_add_u32 s44, s44, 0x800\n\t"
    "s_mov_b32 s98, 13107200\n\t"
    "s_mov_b32 s99, 0x20000\n\t"
    "s_cmp_eq_u32 %16, 0\n\t"
    "s_cbranch_scc1 L_aside_%=\n\t"
    "s_branch L_ahelp_%=\n\t"
    "L_aimg_%=:\n\t"
    "s_cmp_lg_u32 %16, 2\n\t"
    "s_cbranch_scc1 L_anoinit_%=\n\t"
    "v_mov_b32 v0, 0\n\t"
    "v_mov_b32 v1, 0\n\t"
    "v_mov_b32 v2, 0\n\t"
    "v_mov_b32 v3, 0\n\t"
    "v_lshlrev_b32 v8, 4, v147\n\t"
    "v_add_u32 v8, 130048, v8\n\t"
    "v_mov_b32 v9, 1024\n\t"
    "ds_write_b128 v9, v[0:3]\n\t"
    "ds_write_b128 v8, v[0:3] offset:0\n\t"
    "ds_write_b128 v8, v[0:3] offset:1024\n\t"
    "ds_write_b128 v8, v[0:3] offset:2048\n\t"
    "ds_write_b32 v9, v0 offset:2084\n\t"
    "ds_write_b32 v9, v0 offset:2088\n\t"
    "ds_write_b32 v9, v0 offset:1044\n\t"
    "ds_write_b32 v9, v0 offset:1052\n\t"
    "v_mov_b32 v4, 3\n\t"
    "ds_write_b32 v9, v4 offset:1040\n\t"
    "v_mov_b32 v5, 16\n\t"
    "ds_write_b32 v9, v5 offset:2080\n\t"
    "v_mov_b32 v6, 33\n\t"
    "ds_write_b32 v9, v6 offset:3120\n\t"
    "v_mov_b32 v7, 52\n\t"
    "ds_write_b32 v9, v7 offset:4160\n\t"
    "L_anoinit_%=:\n\t"
    "s_and_b32 s46, %16, 3\n\t"
    "s_mul_i32 s49, s46, 1040\n\t"
    "s_add_u32 s49, s49, 2064\n\t"
    "s_lshl_b32 s46, s46, 3\n\t"
    "s_lshr_b32 s47, 1076763402, s46\n\t"
    "s_and_b32 s47, s47, 0xff\n\t"
    "s_lshl_b32 s48, s47, 16\n\t"
    "s_lshr_b32 s47, 773523968, s46\n\t"
    "s_and_b32 s47, s47, 0xff\n\t"
    "s_add_u32 s48, s48, s47\n\t"
    "s_lshr_b32 s47, %16, 2\n\t"
    "s_add_u32 s47, s47, 0x200\n\t"
    "s_cmp_eq_u32 s46, 0\n\t"
    "s_cselect_b32 s47, 0x100, s47\n\t"
    "s_add_u32 s48, s48, s47\n\t"
    "v_lshrrev_b32 v126, 3, v147\n\t"
    "v_and_b32 v154, 7, v147\n\t"
    "v_lshlrev_b32 v126, 8, v126\n\t"
    "v_lshl_add_u32 v126, v154, 4, v126\n\t"
    "v_lshlrev_b32 v117, 20, v147\n\t"
    "v_add_u32 v117, 1024, v117\n\t"
    "v_mov_b32 v118, 1\n\t"
    "s_and_b32 s46, %16, 6\n\t"
    "s_cmp_eq_u32 s46, 4\n\t"
    "s_cbranch_scc1 L_adma_%=\n\t"
    "s_lshr_b32 s61, %16, 2\n\t"
    "s_lshl_b32 s61, s61, 1\n\t"
    "s_and_b32 s46, %16, 1\n\t"
    "s_add_u32 s61, s61, s46\n\t"
    "s_add_u32 s46, s61, 84\n\t"
    "s_add_u32 s69, s51, s46\n\t"
    "s_sub_u32 s44, s69, 125\n\t"
    "s_cmp_ge_u32 s69, 125\n\t"
    "s_cselect_b32 s69, s44, s69\n\t"
    "s_mul_i32 m0, s69, 1040\n\t"
    "s_lshl_b32 s44, s69, 11\n\t"
    "buffer_load_dwordx4 v126, %15, s44 offen lds\n\t"
    "s_add_u32 s69, s69, 4\n\t"
    "s_sub_u32 s44, s69, 125\n\t"
    "s_cmp_ge_u32 s69, 125\n\t"
    "s_cselect_b32 s69, s44, s69\n\t"
    "s_mul_i32 m0, s69, 1040\n\t"
    "s_lshl_b32 s44, s69, 11\n\t"
    "buffer_load_dwordx4 v126, %15, s44 offen lds\n\t"
    "s_add_u32 s69, s69, 4\n\t"
    "s_sub_u32 s44, s69, 125\n\t"
    "s_cmp_ge_u32 s69, 125\n\t"
    "s_cselect_b32 s69, s44, s69\n\t"
    "s_waitcnt lgkmcnt(0)\n\t"
    "s_barrier\n\t"
    "s_load_dwordx2 s[72:73], %6, 0\n\t"
    "s_load_dwordx2 s[88:89], %7, 0\n\t"
    "s_load_dword s91, %8, 0\n\t"
    "s_mul_i32 m0, s69, 1040\n\t"
    "s_lshl_b32 s44, s69, 11\n\t"
    "buffer_load_dwordx4 v126, %15, s44 offen lds\n\t"
    "s_add_u32 s69, s69, 4\n\t"
    "s_sub_u32 s44, s69, 125\n\t"
    "s_cmp_ge_u32 s69, 125\n\t"
    "s_cselect_b32 s69, s44, s69\n\t"
    "s_mul_i32 m0, s69, 1040\n\t"
    "s_lshl_b32 s44, s69, 11\n\t"
    "buffer_load_dwordx4 v126, %15, s44 offen lds\n\t"
    "s_add_u32 s69, s69, 4\n\t"
    "s_sub_u32 s44, s69, 125\n\t"
    "s_cmp_ge_u32 s69, 125\n\t"
    "s_cselect_b32 s69, s44, s69\n\t"
    "s_mul_i32 m0, s69, 1040\n\t"
    "s_lshl_b32 s44, s69, 11\n\t"
    "buffer_load_dwordx4 v126, %15, s44 offen lds\n\t"
    "s_add_u32 s69, s69, 4\n\t"
    "s_sub_u32 s44, s69, 125\n\t"
    "s_cmp_ge_u32 s69, 125\n\t"
    "s_cselect_b32 s69, s44, s69\n\t"
    "s_mul_i32 m0, s69, 1040\n\t"
    "s_lshl_b32 s44, s69, 11\n\t"
    "buffer_load_dwordx4 v126, %15, s44 offen lds\n\t"
    "s_add_u32 s69, s69, 4\n\t"
    "s_sub_u32 s44, s69, 125\n\t"
    "s_cmp_ge_u32 s69, 125\n\t"
    "s_cselect_b32 s69, s44, s69\n\t"
    "v_bfe_u32 v154, v147, 2, 2\n\t"
    "v_and_b32 v155, 3, v147\n\t"
    "v_mul_u32_u24 v154, 25, v154\n\t"
    "v_mad_u32_u24 v154, v155, 6, v154\n\t"
    "v_lshlrev_b32 v153, 2, v154\n\t"
    "s_and_b32 s53, s48, 0xff\n\t"
    "s_bfe_u32 s55, s48, 0x80008\n\t"
    "s_add_u32 s54, s53, s55\n\t"
    "s_add_u32 s55, s54, s55\n\t"
    "s_lshr_b32 s64, s48, 16\n\t"
    "v_cmp_eq_u32 s[62:63], 0, v155\n\t"
    "v_and_b32 v151, 48, v147\n\t"
    "v_or_b32 v155, 0x100000, v151\n\t"
    "v_cndmask_b32 v152, v155, v151, s[62:63]\n\t"
    "v_lshrrev_b32 v94, 4, v147\n\t"
    "v_mul_u32_u24 v92, 0x1388, v94\n\t"
    "v_lshl_add_u32 v92, v154, 1, v92\n\t"
    "v_add_u32 v92, 140288, v92\n\t"
    "v_mul_u32_u24 v93, 200, v154\n\t"
    "v_mad_u32_u24 v93, v94, 50, v93\n\t"
    "v_add_u32 v93, 140288, v93\n\t"
    "v_add_u32 v95, 140288, v122\n\t"
    "v_mov_b32 v0, 0\n\t"
    "v_mov_b32 v28, 0\n\t"
    "v_mov_b32 v1, 0\n\t"
    "v_mov_b32 v29, 0\n\t"
    "v_mov_b32 v2, 0\n\t"
    "v_mov_b32 v30, 0\n\t"
    "v_mov_b32 v3, 0\n\t"
    "v_mov_b32 v31, 0\n\t"
    "v_mov_b32 v4, 0\n\t"
    "v_mov_b32 v32, 0\n\t"
    "v_mov_b32 v5, 0\n\t"
    "v_mov_b32 v33, 0\n\t"
    "v_mov_b32 v6, 0\n\t"
    "v_mov_b32 v34, 0\n\t"
    "v_mov_b32 v7, 0\n\t"
    "v_mov_b32 v35, 0\n\t"
    "v_mov_b32 v8, 0\n\t"
    "v_mov_b32 v36, 0\n\t"
    "v_mov_b32 v9, 0\n\t"
    "v_mov_b32 v37, 0\n\t"
    "v_mov_b32 v10, 0\n\t"
    "v_mov_b32 v38, 0\n\t"
    "v_mov_b32 v11, 0\n\t"
    "v_mov_b32 v39, 0\n\t"
    "v_mov_b32 v12, 0\n\t"
    "v_mov_b32 v40, 0\n\t"
    "v_mov_b32 v13, 0\n\t"
    "v_mov_b32 v41, 0\n\t"
    "v_mov_b32 v14, 0\n\t"
    "v_mov_b32 v42, 0\n\t"
    "v_mov_b32 v15, 0\n\t"
    "v_mov_b32 v43, 0\n\t"
    "v_mov_b32 v16, 0\n\t"
    "v_mov_b32 v44, 0\n\t"
    "v_mov_b32 v17, 0\n\t"
    "v_mov_b32 v45, 0\n\t"
    "v_mov_b32 v18, 0\n\t"
    "v_mov_b32 v46, 0\n\t"
    "v_mov_b32 v19, 0\n\t"
    "v_mov_b32 v47, 0\n\t"
    "v_mov_b32 v20, 0\n\t"
    "v_mov_b32 v48, 0\n\t"
    "v_mov_b32 v21, 0\n\t"
    "v_mov_b32 v49, 0\n\t"
    "v_mov_b32 v22, 0\n\t"
    "v_mov_b32 v50, 0\n\t"
    "v_mov_b32 v23, 0\n\t"
    "v_mov_b32 v51, 0\n\t"
    "s_cmp_eq_u32 s61, 1\n\t"
    "s_cbranch_scc1 L_afold1_%=\n\t"
    "s_cmp_eq_u32 s61, 2\n\t"
    "s_cbranch_scc1 L_afold2_%=\n\t"
    "s_cmp_eq_u32 s61, 3\n\t"
    "s_cbranch_scc1 L_afold3_%=\n\t"
    "L_afold0_%=:\n\t"
    "L_asp0_%=:\n\t"
    "ds_read_b32 v119, v117 offset:1044\n\t"
    "s_waitcnt lgkmcnt(0)\n\t"
    "v_readfirstlane_b32 s46, v119\n\t"
    "s_nop 0\n\t"
    "s_cmp_ge_u32 s46, 2\n\t"
    "s_cbranch_scc1 L_ago0_%=\n\t"
    "s_sleep 1\n\t"
    "s_branch L_asp0_%=\n\t"
    "L_ago0_%=:\n\t"
    "ds_read_u16_d16_hi v0, v92 offset:208\n\t"
    "ds_read_u16_d16_hi v28, v93 offset:802\n\t"
    "ds_read_u16_d16_hi v1, v92 offset:1408\n\t"
    "ds_read_u16_d16_hi v29, v93 offset:814\n\t"
    "ds_read_u16_d16_hi v2, v92 offset:2608\n\t"
    "ds_read_u16_d16_hi v30, v93 offset:826\n\t"
    "ds_read_u16_d16_hi v3, v92 offset:3808\n\t"
    "ds_read_u16_d16_hi v31, v93 offset:838\n\t"
    "ds_read_u16_d16_hi v4, v92 offset:410\n\t"
    "ds_read_u16_d16_hi v32, v93 offset:1004\n\t"
    "ds_read_u16_d16_hi v5, v92 offset:1610\n\t"
    "ds_read_u16_d16_hi v33, v93 offset:1016\n\t"
    "ds_read_u16_d16_hi v6, v92 offset:2810\n\t"
    "ds_read_u16_d16_hi v34, v93 offset:1028\n\t"
    "ds_read_u16_d16_hi v7, v92 offset:4010\n\t"
    "ds_read_u16_d16_hi v35, v93 offset:1040\n\t"
    "ds_read_u16_d16_hi v8, v92 offset:612\n\t"
    "ds_read_u16_d16_hi v36, v93 offset:1206\n\t"
    "ds_read_u16_d16_hi v9, v92 offset:1812\n\t"
    "ds_read_u16_d16_hi v37, v93 offset:1218\n\t"
    "ds_read_u16_d16_hi v10, v92 offset:3012\n\t"
    "ds_read_u16_d16_hi v38, v93 offset:1230\n\t"
    "ds_read_u16_d16_hi v11, v92 offset:4212\n\t"
    "ds_read_u16_d16_hi v39, v93 offset:1242\n\t"
    "ds_read_u16_d16_hi v12, v92 offset:0\n\t"
    "ds_read_u16_d16_hi v13, v92 offset:2\n\t"
    "ds_read_u16_d16_hi v41, v93 offset:200\n\t"
    "ds_read_u16_d16_hi v14, v92 offset:4\n\t"
    "ds_read_u16_d16_hi v42, v93 offset:400\n\t"
    "ds_read_u16_d16_hi v15, v92 offset:6\n\t"
    "ds_read_u16_d16_hi v43, v93 offset:600\n\t"
    "ds_read_u16_d16_hi v16, v92 offset:8\n\t"
    "ds_read_u16_d16_hi v44, v93 offset:800\n\t"
    "ds_read_u16_d16_hi v17, v92 offset:10\n\t"
    "ds_read_u16_d16_hi v45, v93 offset:1000\n\t"
    "ds_read_u16_d16_hi v18, v92 offset:12\n\t"
    "ds_read_u16_d16_hi v46, v93 offset:1200\n\t"
    "ds_add_u32 v117, v118 offset:0\n\t"
    "s_waitcnt lgkmcnt(15)\n\t"
    "v_add_f32 v0, v0, v28\n\t"
    "v_add_f32 v1, v1, v29\n\t"
    "v_add_f32 v2, v2, v30\n\t"
    "v_add_f32 v3, v3, v31\n\t"
    "v_add_f32 v4, v4, v32\n\t"
    "v_add_f32 v5, v5, v33\n\t"
    "v_add_f32 v6, v6, v34\n\t"
    "v_add_f32 v7, v7, v35\n\t"
    "s_waitcnt lgkmcnt(14)\n\t"
    "v_add_f32 v8, v8, v36\n\t"
    "v_add_f32 v9, v9, v37\n\t"
    "v_add_f32 v10, v10, v38\n\t"
    "v_add_f32 v11, v11, v39\n\t"
    "s_waitcnt lgkmcnt(7)\n\t"
    "v_cndmask_b32 v12, 0, v12, s[62:63]\n\t"
    "v_add_f32 v13, v13, v41\n\t"
    "v_add_f32 v14, v14, v42\n\t"
    "v_add_f32 v15, v15, v43\n\t"
    "s_waitcnt lgkmcnt(1)\n\t"
    "v_add_f32 v16, v16, v44\n\t"
    "v_add_f32 v17, v17, v45\n\t"
    "v_add_f32 v18, v18, v46\n\t"
    "s_call_b64 s[98:99], L_setup_%=\n\t"
    "L_asp1_%=:\n\t"
    "ds_read_b32 v119, v117 offset:0\n\t"
    "s_waitcnt lgkmcnt(0)\n\t"
    "v_readfirstlane_b32 s46, v119\n\t"
    "s_nop 0\n\t"
    "s_cmp_ge_u32 s46, 4\n\t"
    "s_cbranch_scc1 L_ago1_%=\n\t"
    "s_sleep 1\n\t"
    "s_branch L_asp1_%=\n\t"
    "L_ago1_%=:\n\t"
    "ds_write_b128 v95, v[0:3] offset:6144\n\t"
    "ds_write_b128 v95, v[4:7] offset:10240\n\t"
    "ds_write_b128 v95, v[8:11] offset:14336\n\t"
    "ds_write_b128 v95, v[12:15] offset:21504\n\t"
    "ds_write_b128 v95, v[16:19] offset:22528\n\t"
    "ds_add_u32 v117, v118 offset:4\n\t"
    "s_branch L_ab0_%=\n\t"
    "L_afold1_%=:\n\t"
    "L_asp2_%=:\n\t"
    "ds_read_b32 v119, v117 offset:1044\n\t"
    "s_waitcnt lgkmcnt(0)\n\t"
    "v_readfirstlane_b32 s46, v119\n\t"
    "s_nop 0\n\t"
    "s_cmp_ge_u32 s46, 2\n\t"
    "s_cbranch_scc1 L_ago2_%=\n\t"
    "s_sleep 1\n\t"
    "s_branch L_asp2_%=\n\t"
    "L_ago2_%=:\n\t"
    "ds_read_u16_d16_hi v0, v92 offset:204\n\t"
    "ds_read_u16_d16_hi v28, v93 offset:402\n\t"
    "ds_read_u16_d16_hi v1, v92 offset:1404\n\t"
    "ds_read_u16_d16_hi v29, v93 offset:414\n\t"
    "ds_read_u16_d16_hi v2, v92 offset:2604\n\t"
    "ds_read_u16_d16_hi v30, v93 offset:426\n\t"
    "ds_read_u16_d16_hi v3, v92 offset:3804\n\t"
    "ds_read_u16_d16_hi v31, v93 offset:438\n\t"
    "ds_read_u16_d16_hi v4, v92 offset:408\n\t"
    "ds_read_u16_d16_hi v32, v93 offset:804\n\t"
    "ds_read_u16_d16_hi v5, v92 offset:1608\n\t"
    "ds_read_u16_d16_hi v33, v93 offset:816\n\t"
    "ds_read_u16_d16_hi v6, v92 offset:2808\n\t"
    "ds_read_u16_d16_hi v34, v93 offset:828\n\t"
    "ds_read_u16_d16_hi v7, v92 offset:4008\n\t"
    "ds_read_u16_d16_hi v35, v93 offset:840\n\t"
    "ds_read_u16_d16_hi v8, v92 offset:412\n\t"
    "ds_read_u16_d16_hi v36, v93 offset:1204\n\t"
    "ds_read_u16_d16_hi v9, v92 offset:1612\n\t"
    "ds_read_u16_d16_hi v37, v93 offset:1216\n\t"
    "ds_read_u16_d16_hi v10, v92 offset:2812\n\t"
    "ds_read_u16_d16_hi v38, v93 offset:1228\n\t"
    "ds_read_u16_d16_hi v11, v92 offset:4012\n\t"
    "ds_read_u16_d16_hi v39, v93 offset:1240\n\t"
    "ds_read_u16_d16_hi v12, v92 offset:810\n\t"
    "ds_read_u16_d16_hi v40, v93 offset:1008\n\t"
    "ds_read_u16_d16_hi v13, v92 offset:2010\n\t"
    "ds_read_u16_d16_hi v41, v93 offset:1020\n\t"
    "ds_read_u16_d16_hi v14, v92 offset:3210\n\t"
    "ds_read_u16_d16_hi v42, v93 offset:1032\n\t"
    "ds_read_u16_d16_hi v15, v92 offset:4410\n\t"
    "ds_read_u16_d16_hi v43, v93 offset:1044\n\t"
    "ds_read_u16_d16_hi v16, v92 offset:202\n\t"
    "ds_read_u16_d16_hi v17, v92 offset:1402\n\t"
    "ds_read_u16_d16_hi v18, v92 offset:2602\n\t"
    "ds_read_u16_d16_hi v19, v92 offset:3802\n\t"
    "ds_read_u16_d16_hi v20, v92 offset:808\n\t"
    "ds_read_u16_d16_hi v21, v92 offset:2008\n\t"
    "ds_read_u16_d16_hi v22, v92 offset:3208\n\t"
    "ds_read_u16_d16_hi v23, v92 offset:4408\n\t"
    "ds_add_u32 v117, v118 offset:0\n\t"
    "s_waitcnt lgkmcnt(15)\n\t"
    "v_add_f32 v0, v0, v28\n\t"
    "v_add_f32 v1, v1, v29\n\t"
    "v_add_f32 v2, v2, v30\n\t"
    "v_add_f32 v3, v3, v31\n\t"
    "v_add_f32 v4, v4, v32\n\t"
    "v_add_f32 v5, v5, v33\n\t"
    "v_add_f32 v6, v6, v34\n\t"
    "v_add_f32 v7, v7, v35\n\t"
    "v_add_f32 v8, v8, v36\n\t"
    "v_add_f32 v9, v9, v37\n\t"
    "v_add_f32 v10, v10, v38\n\t"
    "v_add_f32 v11, v11, v39\n\t"
    "s_waitcnt lgkmcnt(9)\n\t"
    "v_add_f32 v12, v12, v40\n\t"
    "v_add_f32 v13, v13, v41\n\t"
    "v_add_f32 v14, v14, v42\n\t"
    "v_add_f32 v15, v15, v43\n\t"
    "s_waitcnt lgkmcnt(5)\n\t"
    "s_waitcnt lgkmcnt(1)\n\t"
    "s_call_b64 s[98:99], L_setup_%=\n\t"
    "L_asp3_%=:\n\t"
    "ds_read_b32 v119, v117 offset:0\n\t"
    "s_waitcnt lgkmcnt(0)\n\t"
    "v_readfirstlane_b32 s46, v119\n\t"
    "s_nop 0\n\t"
    "s_cmp_ge_u32 s46, 4\n\t"
    "s_cbranch_scc1 L_ago3_%=\n\t"
    "s_sleep 1\n\t"
    "s_branch L_asp3_%=\n\t"
    "L_ago3_%=:\n\t"
    "ds_write_b128 v95, v[0:3] offset:1024\n\t"
    "ds_write_b128 v95, v[4:7] offset:7168\n\t"
    "ds_write_b128 v95, v[8:11] offset:11264\n\t"
    "ds_write_b128 v95, v[12:15] offset:16384\n\t"
    "ds_write_b128 v95, v[16:19] offset:0\n\t"
    "ds_write_b128 v95, v[20:23] offset:15360\n\t"
    "ds_add_u32 v117, v118 offset:4\n\t"
    "s_branch L_ab0_%=\n\t"
    "L_afold2_%=:\n\t"
    "s_call_b64 s[98:99], L_setup_%=\n\t"
    "L_asp4_%=:\n\t"
    "ds_read_b32 v119, v117 offset:1044\n\t"
    "s_waitcnt lgkmcnt(0)\n\t"
    "v_readfirstlane_b32 s46, v119\n\t"
    "s_nop 0\n\t"
    "s_cmp_ge_u32 s46, 2\n\t"
    "s_cbranch_scc1 L_ago4_%=\n\t"
    "s_sleep 1\n\t"
    "s_branch L_asp4_%=\n\t"
    "L_ago4_%=:\n\t"
    "ds_read_u16_d16_hi v0, v92 offset:206\n\t"
    "ds_read_u16_d16_hi v28, v93 offset:602\n\t"
    "ds_read_u16_d16_hi v1, v92 offset:1406\n\t"
    "ds_read_u16_d16_hi v29, v93 offset:614\n\t"
    "ds_read_u16_d16_hi v2, v92 offset:2606\n\t"
    "ds_read_u16_d16_hi v30, v93 offset:626\n\t"
    "ds_read_u16_d16_hi v3, v92 offset:3806\n\t"
    "ds_read_u16_d16_hi v31, v93 offset:638\n\t"
    "ds_read_u16_d16_hi v4, v92 offset:210\n\t"
    "ds_read_u16_d16_hi v32, v93 offset:1002\n\t"
    "ds_read_u16_d16_hi v5, v92 offset:1410\n\t"
    "ds_read_u16_d16_hi v33, v93 offset:1014\n\t"
    "ds_read_u16_d16_hi v6, v92 offset:2610\n\t"
    "ds_read_u16_d16_hi v34, v93 offset:1026\n\t"
    "ds_read_u16_d16_hi v7, v92 offset:3810\n\t"
    "ds_read_u16_d16_hi v35, v93 offset:1038\n\t"
    "ds_read_u16_d16_hi v8, v92 offset:608\n\t"
    "ds_read_u16_d16_hi v36, v93 offset:806\n\t"
    "ds_read_u16_d16_hi v9, v92 offset:1808\n\t"
    "ds_read_u16_d16_hi v37, v93 offset:818\n\t"
    "ds_read_u16_d16_hi v10, v92 offset:3008\n\t"
    "ds_read_u16_d16_hi v38, v93 offset:830\n\t"
    "ds_read_u16_d16_hi v11, v92 offset:4208\n\t"
    "ds_read_u16_d16_hi v39, v93 offset:842\n\t"
    "ds_read_u16_d16_hi v12, v92 offset:812\n\t"
    "ds_read_u16_d16_hi v40, v93 offset:1208\n\t"
    "ds_read_u16_d16_hi v13, v92 offset:2012\n\t"
    "ds_read_u16_d16_hi v41, v93 offset:1220\n\t"
    "ds_read_u16_d16_hi v14, v92 offset:3212\n\t"
    "ds_read_u16_d16_hi v42, v93 offset:1232\n\t"
    "ds_read_u16_d16_hi v15, v92 offset:4412\n\t"
    "ds_read_u16_d16_hi v43, v93 offset:1244\n\t"
    "ds_read_u16_d16_hi v16, v92 offset:404\n\t"
    "ds_read_u16_d16_hi v17, v92 offset:1604\n\t"
    "ds_read_u16_d16_hi v18, v92 offset:2804\n\t"
    "ds_read_u16_d16_hi v19, v92 offset:4004\n\t"
    "ds_read_u16_d16_hi v20, v92 offset:1010\n\t"
    "ds_read_u16_d16_hi v21, v92 offset:2210\n\t"
    "ds_read_u16_d16_hi v22, v92 offset:3410\n\t"
    "ds_read_u16_d16_hi v23, v92 offset:4610\n\t"
    "ds_add_u32 v117, v118 offset:0\n\t"
    "s_waitcnt lgkmcnt(15)\n\t"
    "v_add_f32 v0, v0, v28\n\t"
    "v_add_f32 v1, v1, v29\n\t"
    "v_add_f32 v2, v2, v30\n\t"
    "v_add_f32 v3, v3, v31\n\t"
    "v_add_f32 v4, v4, v32\n\t"
    "v_add_f32 v5, v5, v33\n\t"
    "v_add_f32 v6, v6, v34\n\t"
    "v_add_f32 v7, v7, v35\n\t"
    "v_add_f32 v8, v8, v36\n\t"
    "v_add_f32 v9, v9, v37\n\t"
    "v_add_f32 v10, v10, v38\n\t"
    "v_add_f32 v11, v11, v39\n\t"
    "s_waitcnt lgkmcnt(9)\n\t"
    "v_add_f32 v12, v12, v40\n\t"
    "v_add_f32 v13, v13, v41\n\t"
    "v_add_f32 v14, v14, v42\n\t"
    "v_add_f32 v15, v15, v43\n\t"
    "s_waitcnt lgkmcnt(5)\n\t"
    "s_waitcnt lgkmcnt(1)\n\t"
    "L_asp5_%=:\n\t"
    "ds_read_b32 v119, v117 offset:0\n\t"
    "s_waitcnt lgkmcnt(0)\n\t"
    "v_readfirstlane_b32 s46, v119\n\t"
    "s_nop 0\n\t"
    "s_cmp_ge_u32 s46, 4\n\t"
    "s_cbranch_scc1 L_ago5_%=\n\t"
    "s_sleep 1\n\t"
    "s_branch L_asp5_%=\n\t"
    "L_ago5_%=:\n\t"
    "ds_write_b128 v95, v[0:3] offset:3072\n\t"
    "ds_write_b128 v95, v[4:7] offset:8192\n\t"
    "ds_write_b128 v95, v[8:11] offset:12288\n\t"
    "ds_write_b128 v95, v[12:15] offset:17408\n\t"
    "ds_write_b128 v95, v[16:19] offset:2048\n\t"
    "ds_write_b128 v95, v[20:23] offset:18432\n\t"
    "ds_add_u32 v117, v118 offset:4\n\t"
    "s_branch L_ab0_%=\n\t"
    "L_afold3_%=:\n\t"
    "s_call_b64 s[98:99], L_setup_%=\n\t"
    "L_asp6_%=:\n\t"
    "ds_read_b32 v119, v117 offset:1044\n\t"
    "s_waitcnt lgkmcnt(0)\n\t"
    "v_readfirstlane_b32 s46, v119\n\t"
    "s_nop 0\n\t"
    "s_cmp_ge_u32 s46, 2\n\t"
    "s_cbranch_scc1 L_ago6_%=\n\t"
    "s_sleep 1\n\t"
    "s_branch L_asp6_%=\n\t"
    "L_ago6_%=:\n\t"
    "ds_read_u16_d16_hi v0, v92 offset:406\n\t"
    "ds_read_u16_d16_hi v28, v93 offset:604\n\t"
    "ds_read_u16_d16_hi v1, v92 offset:1606\n\t"
    "ds_read_u16_d16_hi v29, v93 offset:616\n\t"
    "ds_read_u16_d16_hi v2, v92 offset:2806\n\t"
    "ds_read_u16_d16_hi v30, v93 offset:628\n\t"
    "ds_read_u16_d16_hi v3, v92 offset:4006\n\t"
    "ds_read_u16_d16_hi v31, v93 offset:640\n\t"
    "ds_read_u16_d16_hi v4, v92 offset:212\n\t"
    "ds_read_u16_d16_hi v32, v93 offset:1202\n\t"
    "ds_read_u16_d16_hi v5, v92 offset:1412\n\t"
    "ds_read_u16_d16_hi v33, v93 offset:1214\n\t"
    "ds_read_u16_d16_hi v6, v92 offset:2612\n\t"
    "ds_read_u16_d16_hi v34, v93 offset:1226\n\t"
    "ds_read_u16_d16_hi v7, v92 offset:3812\n\t"
    "ds_read_u16_d16_hi v35, v93 offset:1238\n\t"
    "ds_read_u16_d16_hi v8, v92 offset:610\n\t"
    "ds_read_u16_d16_hi v36, v93 offset:1006\n\t"
    "ds_read_u16_d16_hi v9, v92 offset:1810\n\t"
    "ds_read_u16_d16_hi v37, v93 offset:1018\n\t"
    "ds_read_u16_d16_hi v10, v92 offset:3010\n\t"
    "ds_read_u16_d16_hi v38, v93 offset:1030\n\t"
    "ds_read_u16_d16_hi v11, v92 offset:4210\n\t"
    "ds_read_u16_d16_hi v39, v93 offset:1042\n\t"
    "ds_read_u16_d16_hi v12, v92 offset:1012\n\t"
    "ds_read_u16_d16_hi v40, v93 offset:1210\n\t"
    "ds_read_u16_d16_hi v13, v92 offset:2212\n\t"
    "ds_read_u16_d16_hi v41, v93 offset:1222\n\t"
    "ds_read_u16_d16_hi v14, v92 offset:3412\n\t"
    "ds_read_u16_d16_hi v42, v93 offset:1234\n\t"
    "ds_read_u16_d16_hi v15, v92 offset:4612\n\t"
    "ds_read_u16_d16_hi v43, v93 offset:1246\n\t"
    "ds_read_u16_d16_hi v16, v92 offset:606\n\t"
    "ds_read_u16_d16_hi v17, v92 offset:1806\n\t"
    "ds_read_u16_d16_hi v18, v92 offset:3006\n\t"
    "ds_read_u16_d16_hi v19, v92 offset:4206\n\t"
    "ds_read_u16_d16_hi v20, v92 offset:1212\n\t"
    "ds_read_u16_d16_hi v21, v92 offset:2412\n\t"
    "ds_read_u16_d16_hi v22, v92 offset:3612\n\t"
    "ds_read_u16_d16_hi v23, v92 offset:4812\n\t"
    "ds_add_u32 v117, v118 offset:0\n\t"
    "s_waitcnt lgkmcnt(15)\n\t"
    "v_add_f32 v0, v0, v28\n\t"
    "v_add_f32 v1, v1, v29\n\t"
    "v_add_f32 v2, v2, v30\n\t"
    "v_add_f32 v3, v3, v31\n\t"
    "v_add_f32 v4, v4, v32\n\t"
    "v_add_f32 v5, v5, v33\n\t"
    "v_add_f32 v6, v6, v34\n\t"
    "v_add_f32 v7, v7, v35\n\t"
    "v_add_f32 v8, v8, v36\n\t"
    "v_add_f32 v9, v9, v37\n\t"
    "v_add_f32 v10, v10, v38\n\t"
    "v_add_f32 v11, v11, v39\n\t"
    "s_waitcnt lgkmcnt(9)\n\t"
    "v_add_f32 v12, v12, v40\n\t"
    "v_add_f32 v13, v13, v41\n\t"
    "v_add_f32 v14, v14, v42\n\t"
    "v_add_f32 v15, v15, v43\n\t"
    "s_waitcnt lgkmcnt(5)\n\t"
    "s_waitcnt lgkmcnt(1)\n\t"
    "L_asp7_%=:\n\t"
    "ds_read_b32 v119, v117 offset:0\n\t"
    "s_waitcnt lgkmcnt(0)\n\t"
    "v_readfirstlane_b32 s46, v119\n\t"
    "s_nop 0\n\t"
    "s_cmp_ge_u32 s46, 4\n\t"
    "s_cbranch_scc1 L_ago7_%=\n\t"
    "s_sleep 1\n\t"
    "s_branch L_asp7_%=\n\t"
    "L_ago7_%=:\n\t"
    "ds_write_b128 v95, v[0:3] offset:4096\n\t"
    "ds_write_b128 v95, v[4:7] offset:9216\n\t"
    "ds_write_b128 v95, v[8:11] offset:13312\n\t"
    "ds_write_b128 v95, v[12:15] offset:19456\n\t"
    "ds_write_b128 v95, v[16:19] offset:5120\n\t"
    "ds_write_b128 v95, v[20:23] offset:20480\n\t"
    "ds_add_u32 v117, v118 offset:4\n\t"
    "L_ab0_%=:\n\t"
    "s_mul_i32 m0, s69, 1040\n\t"
    "s_lshl_b32 s44, s69, 11\n\t"
    "buffer_load_dwordx4 v126, %15, s44 offen lds\n\t"
    "s_add_u32 s69, s69, 4\n\t"
    "s_sub_u32 s44, s69, 125\n\t"
    "s_cmp_ge_u32 s69, 125\n\t"
    "s_cselect_b32 s69, s44, s69\n\t"
    "s_mul_i32 m0, s69, 1040\n\t"
    "s_lshl_b32 s44, s69, 11\n\t"
    "buffer_load_dwordx4 v126, %15, s44 offen lds\n\t"
    "s_add_u32 s69, s69, 4\n\t"
    "s_sub_u32 s44, s69, 125\n\t"
    "s_cmp_ge_u32 s69, 125\n\t"
    "s_cselect_b32 s69, s44, s69\n\t"
    "s_mul_i32 m0, s69, 1040\n\t"
    "s_lshl_b32 s44, s69, 11\n\t"
    "buffer_load_dwordx4 v126, %15, s44 offen lds\n\t"
    "s_add_u32 s69, s69, 4\n\t"
    "s_sub_u32 s44, s69, 125\n\t"
    "s_cmp_ge_u32 s69, 125\n\t"
    "s_cselect_b32 s69, s44, s69\n\t"
    "s_mul_i32 m0, s69, 1040\n\t"
    "s_lshl_b32 s44, s69, 11\n\t"
    "buffer_load_dwordx4 v126, %15, s44 offen lds\n\t"
    "s_add_u32 s69, s69, 4\n\t"
    "s_sub_u32 s44, s69, 125\n\t"
    "s_cmp_ge_u32 s69, 125\n\t"
    "s_cselect_b32 s69, s44, s69\n\t"
    "s_mul_i32 m0, s69, 1040\n\t"
    "s_lshl_b32 s44, s69, 11\n\t"
    "buffer_load_dwordx4 v126, %15, s44 offen lds\n\t"
    "s_add_u32 s69, s69, 4\n\t"
    "s_sub_u32 s44, s69, 125\n\t"
    "s_cmp_ge_u32 s69, 125\n\t"
    "s_cselect_b32 s69, s44, s69\n\t"
    "s_branch L_ab_%=\n\t"
    "L_adma_%=:\n\t"
    "s_sub_u32 s46, %16, 4\n\t"
    "s_add_u32 s69, s51, s46\n\t"
    "s_sub_u32 s44, s69, 125\n\t"
    "s_cmp_ge_u32 s69, 125\n\t"
    "s_cselect_b32 s69, s44, s69\n\t"
    "s_mul_i32 m0, s69, 1040\n\t"
    "s_lshl_b32 s44, s69, 11\n\t"
    "buffer_load_dwordx4 v126, %15, s44 offen lds\n\t"
    "s_add_u32 s69, s69, 2\n\t"
    "s_sub_u32 s44, s69, 125\n\t"
    "s_cmp_ge_u32 s69, 125\n\t"
    "s_cselect_b32 s69, s44, s69\n\t"
    "s_mul_i32 m0, s69, 1040\n\t"
    "s_lshl_b32 s44, s69, 11\n\t"
    "buffer_load_dwordx4 v126, %15, s44 offen lds\n\t"
    "s_add_u32 s69, s69, 2\n\t"
    "s_sub_u32 s44, s69, 125\n\t"
    "s_cmp_ge_u32 s69, 125\n\t"
    "s_cselect_b32 s69, s44, s69\n\t"
    "s_barrier\n\t"
    "s_load_dwordx2 s[72:73], %6, 0\n\t"
    "s_load_dwordx2 s[88:89], %7, 0\n\t"
    "s_load_dword s91, %8, 0\n\t"
    "s_mul_i32 m0, s69, 1040\n\t"
    "s_lshl_b32 s44, s69, 11\n\t"
    "buffer_load_dwordx4 v126, %15, s44 offen lds\n\t"
    "s_add_u32 s69, s69, 2\n\t"
    "s_sub_u32 s44, s69, 125\n\t"
    "s_cmp_ge_u32 s69, 125\n\t"
    "s_cselect_b32 s69, s44, s69\n\t"
    "s_mul_i32 m0, s69, 1040\n\t"
    "s_lshl_b32 s44, s69, 11\n\t"
    "buffer_load_dwordx4 v126, %15, s44 offen lds\n\t"
    "s_add_u32 s69, s69, 2\n\t"
    "s_sub_u32 s44, s69, 125\n\t"
    "s_cmp_ge_u32 s69, 125\n\t"
    "s_cselect_b32 s69, s44, s69\n\t"
    "s_mul_i32 m0, s69, 1040\n\t"
    "s_lshl_b32 s44, s69, 11\n\t"
    "buffer_load_dwordx4 v126, %15, s44 offen lds\n\t"
    "s_add_u32 s69, s69, 2\n\t"
    "s_sub_u32 s44, s69, 125\n\t"
    "s_cmp_ge_u32 s69, 125\n\t"
    "s_cselect_b32 s69, s44, s69\n\t"
    "s_mul_i32 m0, s69, 1040\n\t"
    "s_lshl_b32 s44, s69, 11\n\t"
    "buffer_load_dwordx4 v126, %15, s44 offen lds\n\t"
    "s_add_u32 s69, s69, 2\n\t"
    "s_sub_u32 s44, s69, 125\n\t"
    "s_cmp_ge_u32 s69, 125\n\t"
    "s_cselect_b32 s69, s44, s69\n\t"
    "s_mul_i32 m0, s69, 1040\n\t"
    "s_lshl_b32 s44, s69, 11\n\t"
    "buffer_load_dwordx4 v126, %15, s44 offen lds\n\t"
    "s_add_u32 s69, s69, 2\n\t"
    "s_sub_u32 s44, s69, 125\n\t"
    "s_cmp_ge_u32 s69, 125\n\t"
    "s_cselect_b32 s69, s44, s69\n\t"
    "s_mul_i32 m0, s69, 1040\n\t"
    "s_lshl_b32 s44, s69, 11\n\t"
    "buffer_load_dwordx4 v126, %15, s44 offen lds\n\t"
    "s_add_u32 s69, s69, 2\n\t"
    "s_sub_u32 s44, s69, 125\n\t"
    "s_cmp_ge_u32 s69, 125\n\t"
    "s_cselect_b32 s69, s44, s69\n\t"
    "s_mul_i32 m0, s69, 1040\n\t"
    "s_lshl_b32 s44, s69, 11\n\t"
    "buffer_load_dwordx4 v126, %15, s44 offen lds\n\t"
    "s_add_u32 s69, s69, 2\n\t"
    "s_sub_u32 s44, s69, 125\n\t"
    "s_cmp_ge_u32 s69, 125\n\t"
    "s_cselect_b32 s69, s44, s69\n\t"
    "s_mul_i32 m0, s69, 1040\n\t"
    "s_lshl_b32 s44, s69, 11\n\t"
    "buffer_load_dwordx4 v126, %15, s44 offen lds\n\t"
    "s_add_u32 s69, s69, 2\n\t"
    "s_sub_u32 s44, s69, 125\n\t"
    "s_cmp_ge_u32 s69, 125\n\t"
    "s_cselect_b32 s69, s44, s69\n\t"
    "s_mul_i32 m0, s69, 1040\n\t"
    "s_lshl_b32 s44, s69, 11\n\t"
    "buffer_load_dwordx4 v126, %15, s44 offen lds\n\t"
    "s_add_u32 s69, s69, 2\n\t"
    "s_sub_u32 s44, s69, 125\n\t"
    "s_cmp_ge_u32 s69, 125\n\t"
    "s_cselect_b32 s69, s44, s69\n\t"
    "s_mul_i32 m0, s69, 1040\n\t"
    "s_lshl_b32 s44, s69, 11\n\t"
    "buffer_load_dwordx4 v126, %15, s44 offen lds\n\t"
    "s_add_u32 s69, s69, 2\n\t"
    "s_sub_u32 s44, s69, 125\n\t"
    "s_cmp_ge_u32 s69, 125\n\t"
    "s_cselect_b32 s69, s44, s69\n\t"
    "v_bfe_u32 v154, v147, 2, 2\n\t"
    "v_and_b32 v155, 3, v147\n\t"
    "v_mul_u32_u24 v154, 25, v154\n\t"
    "v_mad_u32_u24 v154, v155, 6, v154\n\t"
    "v_lshlrev_b32 v153, 2, v154\n\t"
    "s_and_b32 s53, s48, 0xff\n\t"
    "s_bfe_u32 s55, s48, 0x80008\n\t"
    "s_add_u32 s54, s53, s55\n\t"
    "s_add_u32 s55, s54, s55\n\t"
    "s_lshr_b32 s64, s48, 16\n\t"
    "v_cmp_eq_u32 s[62:63], 0, v155\n\t"
    "v_and_b32 v151, 48, v147\n\t"
    "v_or_b32 v155, 0x100000, v151\n\t"
    "v_cndmask_b32 v152, v155, v151, s[62:63]\n\t"
    "v_add_u32 v95, 140288, v122\n\t"
    "s_mul_i32 m0, s69, 1040\n\t"
    "s_lshl_b32 s44, s69, 11\n\t"
    "buffer_load_dwordx4 v126, %15, s44 offen lds\n\t"
    "s_add_u32 s69, s69, 2\n\t"
    "s_sub_u32 s44, s69, 125\n\t"
    "s_cmp_ge_u32 s69, 125\n\t"
    "s_cselect_b32 s69, s44, s69\n\t"
    "s_mul_i32 m0, s69, 1040\n\t"
    "s_lshl_b32 s44, s69, 11\n\t"
    "buffer_load_dwordx4 v126, %15, s44 offen lds\n\t"
    "s_add_u32 s69, s69, 2\n\t"
    "s_sub_u32 s44, s69, 125\n\t"
    "s_cmp_ge_u32 s69, 125\n\t"
    "s_cselect_b32 s69, s44, s69\n\t"
    "s_mul_i32 m0, s69, 1040\n\t"
    "s_lshl_b32 s44, s69, 11\n\t"
    "buffer_load_dwordx4 v126, %15, s44 offen lds\n\t"
    "s_add_u32 s69, s69, 2\n\t"
    "s_sub_u32 s44, s69, 125\n\t"
    "s_cmp_ge_u32 s69, 125\n\t"
    "s_cselect_b32 s69, s44, s69\n\t"
    "s_mul_i32 m0, s69, 1040\n\t"
    "s_lshl_b32 s44, s69, 11\n\t"
    "buffer_load_dwordx4 v126, %15, s44 offen lds\n\t"
    "s_add_u32 s69, s69, 2\n\t"
    "s_sub_u32 s44, s69, 125\n\t"
    "s_cmp_ge_u32 s69, 125\n\t"
    "s_cselect_b32 s69, s44, s69\n\t"
    "s_mul_i32 m0, s69, 1040\n\t"
    "s_lshl_b32 s44, s69, 11\n\t"
    "buffer_load_dwordx4 v126, %15, s44 offen lds\n\t"
    "s_add_u32 s69, s69, 2\n\t"
    "s_sub_u32 s44, s69, 125\n\t"
    "s_cmp_ge_u32 s69, 125\n\t"
    "s_cselect_b32 s69, s44, s69\n\t"
    "s_mul_i32 m0, s69, 1040\n\t"
    "s_lshl_b32 s44, s69, 11\n\t"
    "buffer_load_dwordx4 v126, %15, s44 offen lds\n\t"
    "s_add_u32 s69, s69, 2\n\t"
    "s_sub_u32 s44, s69, 125\n\t"
    "s_cmp_ge_u32 s69, 125\n\t"
    "s_cselect_b32 s69, s44, s69\n\t"
    "s_mul_i32 m0, s69, 1040\n\t"
    "s_lshl_b32 s44, s69, 11\n\t"
    "buffer_load_dwordx4 v126, %15, s44 offen lds\n\t"
    "s_add_u32 s69, s69, 2\n\t"
    "s_sub_u32 s44, s69, 125\n\t"
    "s_cmp_ge_u32 s69, 125\n\t"
    "s_cselect_b32 s69, s44, s69\n\t"
    "s_mul_i32 m0, s69, 1040\n\t"
    "s_lshl_b32 s44, s69, 11\n\t"
    "buffer_load_dwordx4 v126, %15, s44 offen lds\n\t"
    "s_add_u32 s69, s69, 2\n\t"
    "s_sub_u32 s44, s69, 125\n\t"
    "s_cmp_ge_u32 s69, 125\n\t"
    "s_cselect_b32 s69, s44, s69\n\t"
    "s_mul_i32 m0, s69, 1040\n\t"
    "s_lshl_b32 s44, s69, 11\n\t"
    "buffer_load_dwordx4 v126, %15, s44 offen lds\n\t"
    "s_add_u32 s69, s69, 2\n\t"
    "s_sub_u32 s44, s69, 125\n\t"
    "s_cmp_ge_u32 s69, 125\n\t"
    "s_cselect_b32 s69, s44, s69\n\t"
    "s_mul_i32 m0, s69, 1040\n\t"
    "s_lshl_b32 s44, s69, 11\n\t"
    "buffer_load_dwordx4 v126, %15, s44 offen lds\n\t"
    "s_add_u32 s69, s69, 2\n\t"
    "s_sub_u32 s44, s69, 125\n\t"
    "s_cmp_ge_u32 s69, 125\n\t"
    "s_cselect_b32 s69, s44, s69\n\t"
    "s_mul_i32 m0, s69, 1040\n\t"
    "s_lshl_b32 s44, s69, 11\n\t"
    "buffer_load_dwordx4 v126, %15, s44 offen lds\n\t"
    "s_add_u32 s69, s69, 2\n\t"
    "s_sub_u32 s44, s69, 125\n\t"
    "s_cmp_ge_u32 s69, 125\n\t"
    "s_cselect_b32 s69, s44, s69\n\t"
    "s_mul_i32 m0, s69, 1040\n\t"
    "s_lshl_b32 s44, s69, 11\n\t"
    "buffer_load_dwordx4 v126, %15, s44 offen lds\n\t"
    "s_add_u32 s69, s69, 2\n\t"
    "s_sub_u32 s44, s69, 125\n\t"
    "s_cmp_ge_u32 s69, 125\n\t"
    "s_cselect_b32 s69, s44, s69\n\t"
    "s_mul_i32 m0, s69, 1040\n\t"
    "s_lshl_b32 s44, s69, 11\n\t"
    "buffer_load_dwordx4 v126, %15, s44 offen lds\n\t"
    "s_add_u32 s69, s69, 2\n\t"
    "s_sub_u32 s44, s69, 125\n\t"
    "s_cmp_ge_u32 s69, 125\n\t"
    "s_cselect_b32 s69, s44, s69\n\t"
    "s_mul_i32 m0, s69, 1040\n\t"
    "s_lshl_b32 s44, s69, 11\n\t"
    "buffer_load_dwordx4 v126, %15, s44 offen lds\n\t"
    "s_add_u32 s69, s69, 2\n\t"
    "s_sub_u32 s44, s69, 125\n\t"
    "s_cmp_ge_u32 s69, 125\n\t"
    "s_cselect_b32 s69, s44, s69\n\t"
    "s_mul_i32 m0, s69, 1040\n\t"
    "s_lshl_b32 s44, s69, 11\n\t"
    "buffer_load_dwordx4 v126, %15, s44 offen lds\n\t"
    "s_add_u32 s69, s69, 2\n\t"
    "s_sub_u32 s44, s69, 125\n\t"
    "s_cmp_ge_u32 s69, 125\n\t"
    "s_cselect_b32 s69, s44, s69\n\t"
    "s_mul_i32 m0, s69, 1040\n\t"
    "s_lshl_b32 s44, s69, 11\n\t"
    "buffer_load_dwordx4 v126, %15, s44 offen lds\n\t"
    "s_add_u32 s69, s69, 2\n\t"
    "s_sub_u32 s44, s69, 125\n\t"
    "s_cmp_ge_u32 s69, 125\n\t"
    "s_cselect_b32 s69, s44, s69\n\t"
    "s_mul_i32 m0, s69, 1040\n\t"
    "s_lshl_b32 s44, s69, 11\n\t"
    "buffer_load_dwordx4 v126, %15, s44 offen lds\n\t"
    "s_add_u32 s69, s69, 2\n\t"
    "s_sub_u32 s44, s69, 125\n\t"
    "s_cmp_ge_u32 s69, 125\n\t"
    "s_cselect_b32 s69, s44, s69\n\t"
    "s_mul_i32 m0, s69, 1040\n\t"
    "s_lshl_b32 s44, s69, 11\n\t"
    "buffer_load_dwordx4 v126, %15, s44 offen lds\n\t"
    "s_add_u32 s69, s69, 2\n\t"
    "s_sub_u32 s44, s69, 125\n\t"
    "s_cmp_ge_u32 s69, 125\n\t"
    "s_cselect_b32 s69, s44, s69\n\t"
    "s_mul_i32 m0, s69, 1040\n\t"
    "s_lshl_b32 s44, s69, 11\n\t"
    "buffer_load_dwordx4 v126, %15, s44 offen lds\n\t"
    "s_add_u32 s69, s69, 2\n\t"
    "s_sub_u32 s44, s69, 125\n\t"
    "s_cmp_ge_u32 s69, 125\n\t"
    "s_cselect_b32 s69, s44, s69\n\t"
    "s_mul_i32 m0, s69, 1040\n\t"
    "s_lshl_b32 s44, s69, 11\n\t"
    "buffer_load_dwordx4 v126, %15, s44 offen lds\n\t"
    "s_add_u32 s69, s69, 2\n\t"
    "s_sub_u32 s44, s69, 125\n\t"
    "s_cmp_ge_u32 s69, 125\n\t"
    "s_cselect_b32 s69, s44, s69\n\t"
    "s_mul_i32 m0, s69, 1040\n\t"
    "s_lshl_b32 s44, s69, 11\n\t"
    "buffer_load_dwordx4 v126, %15, s44 offen lds\n\t"
    "s_add_u32 s69, s69, 2\n\t"
    "s_sub_u32 s44, s69, 125\n\t"
    "s_cmp_ge_u32 s69, 125\n\t"
    "s_cselect_b32 s69, s44, s69\n\t"
    "s_mul_i32 m0, s69, 1040\n\t"
    "s_lshl_b32 s44, s69, 11\n\t"
    "buffer_load_dwordx4 v126, %15, s44 offen lds\n\t"
    "s_add_u32 s69, s69, 2\n\t"
    "s_sub_u32 s44, s69, 125\n\t"
    "s_cmp_ge_u32 s69, 125\n\t"
    "s_cselect_b32 s69, s44, s69\n\t"
    "s_mul_i32 m0, s69, 1040\n\t"
    "s_lshl_b32 s44, s69, 11\n\t"
    "buffer_load_dwordx4 v126, %15, s44 offen lds\n\t"
    "s_add_u32 s69, s69, 2\n\t"
    "s_sub_u32 s44, s69, 125\n\t"
    "s_cmp_ge_u32 s69, 125\n\t"
    "s_cselect_b32 s69, s44, s69\n\t"
    "s_mul_i32 m0, s69, 1040\n\t"
    "s_lshl_b32 s44, s69, 11\n\t"
    "buffer_load_dwordx4 v126, %15, s44 offen lds\n\t"
    "s_add_u32 s69, s69, 2\n\t"
    "s_sub_u32 s44, s69, 125\n\t"
    "s_cmp_ge_u32 s69, 125\n\t"
    "s_cselect_b32 s69, s44, s69\n\t"
    "s_mul_i32 m0, s69, 1040\n\t"
    "s_lshl_b32 s44, s69, 11\n\t"
    "buffer_load_dwordx4 v126, %15, s44 offen lds\n\t"
    "s_add_u32 s69, s69, 2\n\t"
    "s_sub_u32 s44, s69, 125\n\t"
    "s_cmp_ge_u32 s69, 125\n\t"
    "s_cselect_b32 s69, s44, s69\n\t"
    "s_mul_i32 m0, s69, 1040\n\t"
    "s_lshl_b32 s44, s69, 11\n\t"
    "buffer_load_dwordx4 v126, %15, s44 offen lds\n\t"
    "s_add_u32 s69, s69, 2\n\t"
    "s_sub_u32 s44, s69, 125\n\t"
    "s_cmp_ge_u32 s69, 125\n\t"
    "s_cselect_b32 s69, s44, s69\n\t"
    "s_mul_i32 m0, s69, 1040\n\t"
    "s_lshl_b32 s44, s69, 11\n\t"
    "buffer_load_dwordx4 v126, %15, s44 offen lds\n\t"
    "s_add_u32 s69, s69, 2\n\t"
    "s_sub_u32 s44, s69, 125\n\t"
    "s_cmp_ge_u32 s69, 125\n\t"
    "s_cselect_b32 s69, s44, s69\n\t"
    "s_mul_i32 m0, s69, 1040\n\t"
    "s_lshl_b32 s44, s69, 11\n\t"
    "buffer_load_dwordx4 v126, %15, s44 offen lds\n\t"
    "s_add_u32 s69, s69, 2\n\t"
    "s_sub_u32 s44, s69, 125\n\t"
    "s_cmp_ge_u32 s69, 125\n\t"
    "s_cselect_b32 s69, s44, s69\n\t"
    "s_mul_i32 m0, s69, 1040\n\t"
    "s_lshl_b32 s44, s69, 11\n\t"
    "buffer_load_dwordx4 v126, %15, s44 offen lds\n\t"
    "s_add_u32 s69, s69, 2\n\t"
    "s_sub_u32 s44, s69, 125\n\t"
    "s_cmp_ge_u32 s69, 125\n\t"
    "s_cselect_b32 s69, s44, s69\n\t"
    "s_mul_i32 m0, s69, 1040\n\t"
    "s_lshl_b32 s44, s69, 11\n\t"
    "buffer_load_dwordx4 v126, %15, s44 offen lds\n\t"
    "s_add_u32 s69, s69, 2\n\t"
    "s_sub_u32 s44, s69, 125\n\t"
    "s_cmp_ge_u32 s69, 125\n\t"
    "s_cselect_b32 s69, s44, s69\n\t"
    "s_call_b64 s[98:99], L_setup_%=\n\t"
    "L_ab_%=:\n\t"
    "s_mul_i32 s66, s53, 400\n\t"
    "buffer_load_dwordx3 v[128:130], v153, %0, s66 offen\n\t"
    "buffer_load_dwordx4 v[132:135], v153, %0, s66 offen offset:12\n\t"
    "s_mul_i32 s66, s54, 400\n\t"
    "buffer_load_dwordx3 v[136:138], v153, %0, s66 offen\n\t"
    "buffer_load_dwordx4 v[140:143], v153, %0, s66 offen offset:12\n\t"
    "L_asp8_%=:\n\t"
    "ds_read_b32 v119, v117 offset:4\n\t"
    "s_waitcnt lgkmcnt(0)\n\t"
    "v_readfirstlane_b32 s46, v119\n\t"
    "s_nop 0\n\t"
    "s_cmp_ge_u32 s46, 4\n\t"
    "s_cbranch_scc1 L_ago8_%=\n\t"
    "s_sleep 1\n\t"
    "s_branch L_asp8_%=\n\t"
    "L_ago8_%=:\n\t"
    "ds_read_b128 v[0:3], v95 offset:0\n\t"
    "ds_read_b128 v[4:7], v95 offset:1024\n\t"
    "ds_read_b128 v[8:11], v95 offset:2048\n\t"
    "ds_read_b128 v[12:15], v95 offset:3072\n\t"
    "ds_read_b128 v[16:19], v95 offset:4096\n\t"
    "ds_read_b128 v[20:23], v95 offset:5120\n\t"
    "ds_read_b128 v[24:27], v95 offset:6144\n\t"
    "ds_read_b128 v[28:31], v95 offset:7168\n\t"
    "ds_read_b128 v[32:35], v95 offset:8192\n\t"
    "ds_read_b128 v[36:39], v95 offset:9216\n\t"
    "ds_read_b128 v[40:43], v95 offset:10240\n\t"
    "ds_read_b128 v[44:47], v95 offset:11264\n\t"
    "ds_read_b128 v[48:51], v95 offset:12288\n\t"
    "ds_read_b128 v[52:55], v95 offset:13312\n\t"
    "ds_read_b128 v[56:59], v95 offset:14336\n\t"
    "ds_read_b128 v[60:63], v95 offset:15360\n\t"
    "ds_read_b128 v[64:67], v95 offset:16384\n\t"
    "ds_read_b128 v[68:71], v95 offset:17408\n\t"
    "ds_read_b128 v[72:75], v95 offset:18432\n\t"
    "ds_read_b128 v[76:79], v95 offset:19456\n\t"
    "ds_read_b128 v[80:83], v95 offset:20480\n\t"
    "ds_read_b128 v[84:87], v95 offset:21504\n\t"
    "ds_read_b128 v[88:91], v95 offset:22528\n\t"
    "s_waitcnt vmcnt(4)\n\t"
    "ds_add_u32 v117, v118 offset:8\n\t"
    "s_waitcnt vmcnt(0)\n\t"
    "s_branch L_awd_%=\n\t"
    "s_waitcnt vmcnt(4)\n\t"
    "ds_add_u32 v117, v118 offset:8\n\t"
    "s_branch L_await_%=\n\t"
    "L_setup_%=:\n\t"
    "v_mbcnt_lo_u32_b32 v100, -1, 0\n\t"
    "v_mbcnt_hi_u32_b32 v100, -1, v100\n\t"
    "s_lshl_b32 s61, %16, 15\n\t"
    "s_add_u32 s61, s61, 16515072\n\t"
    "v_lshlrev_b32 v127, 4, v100\n\t"
    "s_lshr_b32 s37, %14, 4\n\t"
    "s_and_b32 s39, %14, 7\n\t"
    "v_and_b32 v101, 15, v100\n\t"
    "s_lshl_b32 s39, s39, 4\n\t"
    "s_add_u32 s39, s39, s37\n\t"
    "s_mul_i32 s65, %16, 125\n\t"
    "v_add_u32 v155, s65, v101\n\t"
    "s_mul_i32 s63, s39, 102400\n\t"
    "s_add_u32 s63, s63, 3407872\n\t"
    "v_lshrrev_b32 v102, 2, v101\n\t"
    "s_and_b32 s37, s37, 7\n\t"
    "s_lshl_b32 s37, s37, 11\n\t"
    "v_lshrrev_b32 v103, 3, v100\n\t"
    "s_lshl_b32 s36, s39, 16\n\t"
    "s_bfe_u32 s65, %14, 0x10003\n\t"
    "v_and_b32 v103, 2, v103\n\t"
    "s_lshl_b32 s38, s65, 15\n\t"
    "s_add_u32 s36, s36, s38\n\t"
    "v_and_b32 v104, 1, v100\n\t"
    "s_lshl_b32 s38, %16, 12\n\t"
    "s_add_u32 s36, s36, s38\n\t"
    "v_lshlrev_b32 v104, 4, v104\n\t"
    "s_add_u32 s36, s36, 17825792\n\t"
    "v_mov_b32 v101, 0x3f80\n\t"
    "v_lshlrev_b32 v104, v104, v101\n\t"
    "v_cmp_eq_u32 vcc, v102, v103\n\t"
    "s_waitcnt lgkmcnt(0)\n\t"
    "s_lshr_b32 s39, s91, %9\n\t"
    "s_lshl_b32 s39, s39, 16\n\t"
    "v_cndmask_b32 v120, 0, v104, vcc\n\t"
    "v_cndmask_b32 v121, 0, v104, vcc\n\t"
    "v_add_u32 v103, 1, v103\n\t"
    "v_cmp_eq_u32 vcc, v102, v103\n\t"
    "s_lshl_b32 s40, s88, 16\n\t"
    "s_and_b32 s41, s88, 0xffff0000\n\t"
    "v_cndmask_b32 v122, 0, v104, vcc\n\t"
    "v_cndmask_b32 v123, 0, v104, vcc\n\t"
    "v_and_b32 v101, 2, v100\n\t"
    "v_mov_b32 v102, 0x44444444\n\t"
    "v_mov_b32 v103, 0xeeeeeeee\n\t"
    "v_cmp_eq_u32 vcc, 0, v101\n\t"
    "s_lshl_b32 s42, s89, 16\n\t"
    "s_and_b32 s43, s89, 0xffff0000\n\t"
    "v_cndmask_b32 v124, v103, v102, vcc\n\t"
    "v_lshrrev_b32 v101, 4, v100\n\t"
    "v_mul_u32_u24 v101, 8000, v101\n\t"
    "v_lshl_add_u32 v125, v155, 3, v101\n\t"
    "v_add_u32 v125, 64000, v125\n\t"
    "s_lshl_b32 s70, s72, 16\n\t"
    "s_and_b32 s71, s72, 0xffff0000\n\t"
    "s_lshl_b32 s72, s73, 16\n\t"
    "s_and_b32 s73, s73, 0xffff0000\n\t"
    "s_mov_b32 s67, 0xffff0000\n\t"
    "s_and_b32 s65, %14, 8\n\t"
    "s_add_u32 s38, s65, %16\n\t"
    "s_mul_i32 s38, s38, 212992\n\t"
    "s_add_u32 s38, s38, 196608\n\t"
    "s_add_u32 s84, s37, s38\n\t"
    "s_add_u32 s37, s37, 0x800\n\t"
    "s_and_b32 s37, s37, 0x3800\n\t"
    "s_add_u32 s85, s37, s38\n\t"
    "s_add_u32 s37, s37, 0x800\n\t"
    "s_and_b32 s37, s37, 0x3800\n\t"
    "s_add_u32 s86, s37, s38\n\t"
    "s_add_u32 s37, s37, 0x800\n\t"
    "s_and_b32 s37, s37, 0x3800\n\t"
    "s_add_u32 s87, s37, s38\n\t"
    "s_add_u32 s37, s37, 0x800\n\t"
    "s_and_b32 s37, s37, 0x3800\n\t"
    "s_add_u32 s88, s37, s38\n\t"
    "s_add_u32 s37, s37, 0x800\n\t"
    "s_and_b32 s37, s37, 0x3800\n\t"
    "s_add_u32 s89, s37, s38\n\t"
    "s_add_u32 s37, s37, 0x800\n\t"
    "s_and_b32 s37, s37, 0x3800\n\t"
    "s_add_u32 s90, s37, s38\n\t"
    "s_add_u32 s37, s37, 0x800\n\t"
    "s_and_b32 s37, s37, 0x3800\n\t"
    "s_add_u32 s91, s37, s38\n\t"
    "s_mul_i32 s94, s65, 425984\n\t"
    "s_sub_u32 s94, s94, 1703936\n\t"
    "s_sub_u32 s95, 0x8000, s94\n\t"
    "s_lshl_b32 s97, s65, 11\n\t"
    "s_sub_u32 s97, s97, 229376\n\t"
    "s_mov_b32 s92, 64000\n\t"
    "s_mov_b32 s93, -64000\n\t"
    "s_mov_b32 s96, 0x2000\n\t"
    "s_mov_b32 s62, 2\n\t"
    "v_rcp_f32 v101, s40\n\t"
    "v_rcp_f32 v102, s41\n\t"
    "v_rcp_f32 v103, s42\n\t"
    "v_rcp_f32 v104, s43\n\t"
    "v_mov_b32 v100, 0x7f7fffff\n\t"
    "v_med3_f32 v101, v101, -v100, v100\n\t"
    "v_med3_f32 v102, v102, -v100, v100\n\t"
    "v_med3_f32 v103, v103, -v100, v100\n\t"
    "v_med3_f32 v104, v104, -v100, v100\n\t"
    "v_readfirstlane_b32 s40, v101\n\t"
    "v_readfirstlane_b32 s41, v102\n\t"
    "v_readfirstlane_b32 s42, v103\n\t"
    "v_readfirstlane_b32 s43, v104\n\t"
    "s_setpc_b64 s[98:99]\n\t"
    "L_aside_%=:\n\t"
    "s_setprio 3\n\t"
    "v_cmp_gt_u32 vcc, 48, v147\n\t"
    "v_mov_b32 v133, 0x40000000\n\t"
    "v_lshrrev_b32 v134, 3, v147\n\t"
    "v_bfe_u32 v135, v147, 2, 1\n\t"
    "v_and_b32 v130, 3, v147\n\t"
    "v_lshl_add_u32 v132, v135, 5, v130\n\t"
    "v_mul_u32_u24 v64, 400, v132\n\t"
    "v_lshl_add_u32 v64, v134, 5, v64\n\t"
    "s_movk_i32 s50, 0\n\t"
    "buffer_load_dwordx4 v[0:3], v64, %0, s50 offen offset:16\n\t"
    "buffer_load_dwordx4 v[4:7], v64, %0, s50 offen offset:32\n\t"
    "v_lshlrev_b32 v66, 14, v134\n\t"
    "v_lshl_add_u32 v66, v132, 4, v66\n\t"
    "v_lshlrev_b32 v68, 8, v134\n\t"
    "v_lshl_add_u32 v68, v135, 2, v68\n\t"
    "v_add_u32 v68, 130048, v68\n\t"
    "v_cndmask_b32 v68, v133, v68, vcc\n\t"
    "v_cndmask_b32 v66, v133, v66, vcc\n\t"
    "v_lshrrev_b32 v134, 2, v147\n\t"
    "v_bfe_u32 v135, v147, 1, 1\n\t"
    "v_and_b32 v130, 1, v147\n\t"
    "v_lshl_add_u32 v132, v135, 5, v130\n\t"
    "v_mul_u32_u24 v65, 400, v132\n\t"
    "v_lshl_add_u32 v65, v134, 5, v65\n\t"
    "v_lshlrev_b32 v67, 14, v134\n\t"
    "v_lshl_add_u32 v67, v132, 4, v67\n\t"
    "v_lshlrev_b32 v69, 8, v134\n\t"
    "v_lshl_add_u32 v69, v135, 2, v69\n\t"
    "v_add_u32 v69, 130048, v69\n\t"
    "v_cndmask_b32 v69, v133, v69, vcc\n\t"
    "v_cndmask_b32 v67, v133, v67, vcc\n\t"
    "s_barrier\n\t"
    "s_load_dwordx2 s[72:73], %6, 0\n\t"
    "s_load_dwordx2 s[88:89], %7, 0\n\t"
    "s_load_dword s91, %8, 0\n\t"
    "v_lshlrev_b32 v128, 4, v147\n\t"
    "v_add_u32 v128, 133120, v128\n\t"
    "v_and_b32 v130, 1, v147\n\t"
    "v_cndmask_b32 v128, v133, v128, vcc\n\t"
    "v_mul_u32_u24 v127, 7, v130\n\t"
    "v_cmp_eq_u32 vcc, 1, v130\n\t"
    "v_mov_b32 v126, 0x07060100\n\t"
    "v_mov_b32 v133, 0x05040100\n\t"
    "v_mov_b32 v136, 1\n\t"
    "v_cndmask_b32 v126, v126, v133, vcc\n\t"
    "s_mov_b32 s36, 0\n\t"
    "s_mov_b32 s37, 8000\n\t"
    "s_mov_b32 s38, 16000\n\t"
    "s_mov_b32 s39, 24000\n\t"
    "s_mov_b32 s40, 32000\n\t"
    "s_mov_b32 s41, 40000\n\t"
    "s_mov_b32 s42, 48000\n\t"
    "s_mov_b32 s43, 56000\n\t"
    "s_movk_i32 s44, 0xf8\n\t"
    "s_mov_b32 s45, 0x01010000\n\t"
    "s_mov_b32 s46, 0x020c000c\n\t"
    "s_lshr_b32 s49, %14, 4\n\t"
    "s_mul_i32 s49, s49, 212992\n\t"
    "s_and_b32 s50, %14, 15\n\t"
    "s_lshl_b32 s50, s50, 10\n\t"
    "s_add_u32 s49, s49, s50\n\t"
    "v_lshrrev_b32 v134, 4, v147\n\t"
    "v_bfe_u32 v135, v147, 2, 2\n\t"
    "v_and_b32 v130, 3, v147\n\t"
    "v_lshlrev_b32 v137, 17, v134\n\t"
    "v_lshl_add_u32 v137, v135, 10, v137\n\t"
    "v_lshl_add_u32 v137, v130, 4, v137\n\t"
    "v_add_u32 v138, 0x1000, v137\n\t"
    "v_mul_u32_u24 v141, 0x210, v134\n\t"
    "v_lshl_add_u32 v141, v135, 6, v141\n\t"
    "v_lshl_add_u32 v141, v130, 4, v141\n\t"
    "v_add_u32 v141, 134656, v141\n\t"
    "v_mul_u32_u24 v142, 0x210, v130\n\t"
    "v_lshl_add_u32 v142, v135, 6, v142\n\t"
    "v_lshl_add_u32 v142, v134, 3, v142\n\t"
    "v_add_u32 v142, 134656, v142\n\t"
    "s_mov_b32 s50, 102400\n\t"
    "v_mul_u32_u24 v139, s50, v134\n\t"
    "v_and_b32 v132, 15, v147\n\t"
    "v_lshl_add_u32 v139, v132, 4, v139\n\t"
    "v_add_u32 v140, 409600, v139\n\t"
    "s_lshr_b32 s50, %14, 4\n\t"
    "s_lshl_b32 s47, s50, 13\n\t"
    "s_lshl_b32 s48, s50, 8\n\t"
    "s_and_b32 s50, %14, 7\n\t"
    "s_lshl_b32 s51, s50, 7\n\t"
    "s_add_u32 s47, s47, s51\n\t"
    "s_mul_i32 s51, s50, 1638400\n\t"
    "s_add_u32 s48, s48, s51\n\t"
    "s_bfe_u32 s50, %14, 0x10003\n\t"
    "s_lshl_b32 s51, s50, 19\n\t"
    "s_add_u32 s47, s47, s51\n\t"
    "s_lshl_b32 s51, s50, 12\n\t"
    "s_add_u32 s48, s48, s51\n\t"
    "s_add_u32 s48, s48, 3407872\n\t"
    "s_waitcnt vmcnt(2)\n\t"
    "v_lshlrev_b32 v134, 20, v147\n\t"
    "v_add_u32 v134, 2068, v134\n\t"
    "ds_add_u32 v134, v136\n\t"
    "v_mov_b32 v110, 65664\n\t"
    "v_mov_b32 v111, 131136\n\t"
    "v_mov_b32 v112, 262176\n\t"
    "v_mov_b32 v113, 524304\n\t"
    "v_mov_b32 v114, 1048584\n\t"
    "v_mov_b32 v115, 2097156\n\t"
    "v_mov_b32 v116, 4194306\n\t"
    "v_mov_b32 v117, 8388609\n\t"
    "s_waitcnt vmcnt(0)\n\t"
    "v_lshl_add_u32 v76, v0, 3, s36\n\t"
    "v_lshl_add_u32 v77, v1, 3, s37\n\t"
    "v_lshl_add_u32 v78, v2, 3, s38\n\t"
    "v_lshl_add_u32 v79, v3, 3, s39\n\t"
    "v_lshl_add_u32 v80, v4, 3, s40\n\t"
    "v_lshl_add_u32 v81, v5, 3, s41\n\t"
    "v_lshl_add_u32 v82, v6, 3, s42\n\t"
    "v_lshl_add_u32 v83, v7, 3, s43\n\t"
    "v_and_or_b32 v92, v76, s44, v68\n\t"
    "ds_or_rtn_b32 v118, v92, v110\n\t"
    "v_and_or_b32 v93, v77, s44, v68\n\t"
    "ds_or_rtn_b32 v119, v93, v111\n\t"
    "v_and_or_b32 v94, v78, s44, v68\n\t"
    "ds_or_rtn_b32 v120, v94, v112\n\t"
    "v_and_or_b32 v95, v79, s44, v68\n\t"
    "ds_or_rtn_b32 v121, v95, v113\n\t"
    "v_and_or_b32 v96, v80, s44, v68\n\t"
    "ds_or_rtn_b32 v122, v96, v114\n\t"
    "v_and_or_b32 v97, v81, s44, v68\n\t"
    "ds_or_rtn_b32 v123, v97, v115\n\t"
    "v_and_or_b32 v98, v82, s44, v68\n\t"
    "ds_or_rtn_b32 v124, v98, v116\n\t"
    "v_and_or_b32 v99, v83, s44, v68\n\t"
    "ds_or_rtn_b32 v125, v99, v117\n\t"
    "v_lshl_or_b32 v164, v77, 16, v76\n\t"
    "v_lshl_or_b32 v165, v79, 16, v78\n\t"
    "v_lshl_or_b32 v166, v81, 16, v80\n\t"
    "v_lshl_or_b32 v167, v83, 16, v82\n\t"
    "s_movk_i32 s50, 0\n\t"
    "buffer_load_dwordx4 v[8:11], v64, %0, s50 offen offset:1616\n\t"
    "buffer_load_dwordx4 v[12:15], v64, %0, s50 offen offset:1632\n\t"
    "s_movk_i32 s50, 3200\n\t"
    "buffer_load_dwordx4 v[16:19], v64, %0, s50 offen offset:16\n\t"
    "buffer_load_dwordx4 v[20:23], v64, %0, s50 offen offset:32\n\t"
    "s_waitcnt vmcnt(2)\n\t"
    "v_lshl_add_u32 v84, v8, 3, s36\n\t"
    "v_lshl_add_u32 v85, v9, 3, s37\n\t"
    "v_lshl_add_u32 v86, v10, 3, s38\n\t"
    "v_lshl_add_u32 v87, v11, 3, s39\n\t"
    "v_lshl_add_u32 v88, v12, 3, s40\n\t"
    "v_lshl_add_u32 v89, v13, 3, s41\n\t"
    "v_lshl_add_u32 v90, v14, 3, s42\n\t"
    "v_lshl_add_u32 v91, v15, 3, s43\n\t"
    "s_waitcnt lgkmcnt(4)\n\t"
    "v_and_b32 v118, v118, v110\n\t"
    "v_perm_b32 v118, v118, v118, s46\n\t"
    "ds_or_b32 v92, v118\n\t"
    "v_and_b32 v119, v119, v111\n\t"
    "v_perm_b32 v119, v119, v119, s46\n\t"
    "ds_or_b32 v93, v119\n\t"
    "v_and_b32 v120, v120, v112\n\t"
    "v_perm_b32 v120, v120, v120, s46\n\t"
    "ds_or_b32 v94, v120\n\t"
    "v_and_b32 v121, v121, v113\n\t"
    "v_perm_b32 v121, v121, v121, s46\n\t"
    "ds_or_b32 v95, v121\n\t"
    "s_waitcnt lgkmcnt(4)\n\t"
    "v_and_b32 v122, v122, v114\n\t"
    "v_perm_b32 v122, v122, v122, s46\n\t"
    "ds_or_b32 v96, v122\n\t"
    "v_and_b32 v123, v123, v115\n\t"
    "v_perm_b32 v123, v123, v123, s46\n\t"
    "ds_or_b32 v97, v123\n\t"
    "v_and_b32 v124, v124, v116\n\t"
    "v_perm_b32 v124, v124, v124, s46\n\t"
    "ds_or_b32 v98, v124\n\t"
    "v_and_b32 v125, v125, v117\n\t"
    "v_perm_b32 v125, v125, v125, s46\n\t"
    "ds_or_b32 v99, v125\n\t"
    "buffer_store_dwordx4 v[164:167], v66, %12, s49 offen offset:0 sc1\n\t"
    "v_and_or_b32 v100, v84, s44, v68\n\t"
    "ds_read_b32 v100, v100\n\t"
    "v_and_or_b32 v101, v85, s44, v68\n\t"
    "ds_read_b32 v101, v101\n\t"
    "v_and_or_b32 v102, v86, s44, v68\n\t"
    "ds_read_b32 v102, v102\n\t"
    "v_and_or_b32 v103, v87, s44, v68\n\t"
    "ds_read_b32 v103, v103\n\t"
    "v_and_or_b32 v104, v88, s44, v68\n\t"
    "ds_read_b32 v104, v104\n\t"
    "v_and_or_b32 v105, v89, s44, v68\n\t"
    "ds_read_b32 v105, v105\n\t"
    "v_and_or_b32 v106, v90, s44, v68\n\t"
    "ds_read_b32 v106, v106\n\t"
    "v_and_or_b32 v107, v91, s44, v68\n\t"
    "ds_read_b32 v107, v107\n\t"
    "s_waitcnt vmcnt(1)\n\t"
    "v_lshl_add_u32 v76, v16, 3, s36\n\t"
    "v_lshl_add_u32 v77, v17, 3, s37\n\t"
    "v_lshl_add_u32 v78, v18, 3, s38\n\t"
    "v_lshl_add_u32 v79, v19, 3, s39\n\t"
    "v_lshl_add_u32 v80, v20, 3, s40\n\t"
    "v_lshl_add_u32 v81, v21, 3, s41\n\t"
    "v_lshl_add_u32 v82, v22, 3, s42\n\t"
    "v_lshl_add_u32 v83, v23, 3, s43\n\t"
    "s_movk_i32 s50, 3200\n\t"
    "buffer_load_dwordx4 v[24:27], v64, %0, s50 offen offset:1616\n\t"
    "buffer_load_dwordx4 v[28:31], v64, %0, s50 offen offset:1632\n\t"
    "s_movk_i32 s50, 6400\n\t"
    "buffer_load_dwordx4 v[32:35], v64, %0, s50 offen offset:16\n\t"
    "buffer_load_dwordx4 v[36:39], v64, %0, s50 offen offset:32\n\t"
    "v_mov_b32 v129, 0xffff0000\n\t"
    "s_waitcnt lgkmcnt(4)\n\t"
    "v_perm_b32 v100, v100, v84, v126\n\t"
    "v_perm_b32 v101, v101, v85, v126\n\t"
    "v_perm_b32 v102, v102, v86, v126\n\t"
    "v_perm_b32 v103, v103, v87, v126\n\t"
    "s_waitcnt lgkmcnt(0)\n\t"
    "v_perm_b32 v104, v104, v88, v126\n\t"
    "v_perm_b32 v105, v105, v89, v126\n\t"
    "v_perm_b32 v106, v106, v90, v126\n\t"
    "v_perm_b32 v107, v107, v91, v126\n\t"
    "v_max_u32 v108, v100, v101\n\t"
    "v_min_u32 v101, v100, v101\n\t"
    "v_max_u32 v100, v102, v103\n\t"
    "v_min_u32 v103, v102, v103\n\t"
    "v_max_u32 v102, v104, v105\n\t"
    "v_min_u32 v105, v104, v105\n\t"
    "v_max_u32 v104, v106, v107\n\t"
    "v_min_u32 v107, v106, v107\n\t"
    "v_max_u32 v106, v108, v100\n\t"
    "v_min_u32 v100, v108, v100\n\t"
    "v_max_u32 v108, v101, v103\n\t"
    "v_min_u32 v103, v101, v103\n\t"
    "v_max_u32 v101, v102, v104\n\t"
    "v_min_u32 v104, v102, v104\n\t"
    "v_max_u32 v102, v105, v107\n\t"
    "v_min_u32 v107, v105, v107\n\t"
    "v_bfi_b32 v130, v106, 0, v129\n\t"
    "v_cmp_eq_u32 vcc, 0, v130\n\t"
    "v_and_or_b32 v92, v106, s44, v68\n\t"
    "s_nop 0\n\t"
    "v_cndmask_b32 v130, v130, v129, vcc\n\t"
    "v_ffbl_b32 v131, v130\n\t"
    "v_and_b32 v134, 7, v131\n\t"
    "v_lshlrev_b32 v134, v134, s45\n\t"
    "v_xor_b32 v129, v129, v134\n\t"
    "v_xor_b32 v132, v131, v127\n\t"
    "v_bfi_b32 v130, v101, 0, v129\n\t"
    "v_cmp_eq_u32 vcc, 0, v130\n\t"
    "v_and_or_b32 v93, v101, s44, v68\n\t"
    "v_xor_b32 v133, 23, v132\n\t"
    "v_lshlrev_b32 v110, v132, v136\n\t"
    "v_and_b32 v132, 7, v132\n\t"
    "v_lshl_or_b32 v110, v136, v133, v110\n\t"
    "v_lshl_add_u32 v132, v132, 1, v128\n\t"
    "ds_or_rtn_b32 v118, v92, v110\n\t"
    "ds_write_b16 v132, v106\n\t"
    "v_cndmask_b32 v130, v130, v129, vcc\n\t"
    "v_ffbl_b32 v131, v130\n\t"
    "v_and_b32 v134, 7, v131\n\t"
    "v_lshlrev_b32 v134, v134, s45\n\t"
    "v_xor_b32 v129, v129, v134\n\t"
    "v_xor_b32 v132, v131, v127\n\t"
    "v_bfi_b32 v130, v108, 0, v129\n\t"
    "v_cmp_eq_u32 vcc, 0, v130\n\t"
    "v_and_or_b32 v94, v108, s44, v68\n\t"
    "v_xor_b32 v133, 23, v132\n\t"
    "v_lshlrev_b32 v111, v132, v136\n\t"
    "v_and_b32 v132, 7, v132\n\t"
    "v_lshl_or_b32 v111, v136, v133, v111\n\t"
    "v_lshl_add_u32 v132, v132, 1, v128\n\t"
    "ds_or_rtn_b32 v119, v93, v111\n\t"
    "ds_write_b16 v132, v101\n\t"
    "v_cndmask_b32 v130, v130, v129, vcc\n\t"
    "v_ffbl_b32 v131, v130\n\t"
    "v_and_b32 v134, 7, v131\n\t"
    "v_lshlrev_b32 v134, v134, s45\n\t"
    "v_xor_b32 v129, v129, v134\n\t"
    "v_xor_b32 v132, v131, v127\n\t"
    "v_bfi_b32 v130, v100, 0, v129\n\t"
    "v_cmp_eq_u32 vcc, 0, v130\n\t"
    "v_and_or_b32 v95, v100, s44, v68\n\t"
    "v_xor_b32 v133, 23, v132\n\t"
    "v_lshlrev_b32 v112, v132, v136\n\t"
    "v_and_b32 v132, 7, v132\n\t"
    "v_lshl_or_b32 v112, v136, v133, v112\n\t"
    "v_lshl_add_u32 v132, v132, 1, v128\n\t"
    "ds_or_rtn_b32 v120, v94, v112\n\t"
    "ds_write_b16 v132, v108\n\t"
    "v_cndmask_b32 v130, v130, v129, vcc\n\t"
    "v_ffbl_b32 v131, v130\n\t"
    "v_and_b32 v134, 7, v131\n\t"
    "v_lshlrev_b32 v134, v134, s45\n\t"
    "v_xor_b32 v129, v129, v134\n\t"
    "v_xor_b32 v132, v131, v127\n\t"
    "v_bfi_b32 v130, v102, 0, v129\n\t"
    "v_cmp_eq_u32 vcc, 0, v130\n\t"
    "v_and_or_b32 v96, v102, s44, v68\n\t"
    "v_xor_b32 v133, 23, v132\n\t"
    "v_lshlrev_b32 v113, v132, v136\n\t"
    "v_and_b32 v132, 7, v132\n\t"
    "v_lshl_or_b32 v113, v136, v133, v113\n\t"
    "v_lshl_add_u32 v132, v132, 1, v128\n\t"
    "ds_or_rtn_b32 v121, v95, v113\n\t"
    "ds_write_b16 v132, v100\n\t"
    "v_cndmask_b32 v130, v130, v129, vcc\n\t"
    "v_ffbl_b32 v131, v130\n\t"
    "v_and_b32 v134, 7, v131\n\t"
    "v_lshlrev_b32 v134, v134, s45\n\t"
    "v_xor_b32 v129, v129, v134\n\t"
    "v_xor_b32 v132, v131, v127\n\t"
    "s_waitcnt lgkmcnt(7)\n\t"
    "v_and_b32 v118, v118, v110\n\t"
    "v_perm_b32 v118, v118, v118, s46\n\t"
    "ds_or_b32 v92, v118\n\t"
    "v_bfi_b32 v130, v104, 0, v129\n\t"
    "v_cmp_eq_u32 vcc, 0, v130\n\t"
    "v_and_or_b32 v97, v104, s44, v68\n\t"
    "v_xor_b32 v133, 23, v132\n\t"
    "v_lshlrev_b32 v114, v132, v136\n\t"
    "v_and_b32 v132, 7, v132\n\t"
    "v_lshl_or_b32 v114, v136, v133, v114\n\t"
    "v_lshl_add_u32 v132, v132, 1, v128\n\t"
    "ds_or_rtn_b32 v122, v96, v114\n\t"
    "ds_write_b16 v132, v102\n\t"
    "v_cndmask_b32 v130, v130, v129, vcc\n\t"
    "v_ffbl_b32 v131, v130\n\t"
    "v_and_b32 v134, 7, v131\n\t"
    "v_lshlrev_b32 v134, v134, s45\n\t"
    "v_xor_b32 v129, v129, v134\n\t"
    "v_xor_b32 v132, v131, v127\n\t"
    "s_waitcnt lgkmcnt(8)\n\t"
    "v_and_b32 v119, v119, v111\n\t"
    "v_perm_b32 v119, v119, v119, s46\n\t"
    "ds_or_b32 v93, v119\n\t"
    "v_bfi_b32 v130, v103, 0, v129\n\t"
    "v_cmp_eq_u32 vcc, 0, v130\n\t"
    "v_and_or_b32 v98, v103, s44, v68\n\t"
    "v_xor_b32 v133, 23, v132\n\t"
    "v_lshlrev_b32 v115, v132, v136\n\t"
    "v_and_b32 v132, 7, v132\n\t"
    "v_lshl_or_b32 v115, v136, v133, v115\n\t"
    "v_lshl_add_u32 v132, v132, 1, v128\n\t"
    "ds_or_rtn_b32 v123, v97, v115\n\t"
    "ds_write_b16 v132, v104\n\t"
    "v_cndmask_b32 v130, v130, v129, vcc\n\t"
    "v_ffbl_b32 v131, v130\n\t"
    "v_and_b32 v134, 7, v131\n\t"
    "v_lshlrev_b32 v134, v134, s45\n\t"
    "v_xor_b32 v129, v129, v134\n\t"
    "v_xor_b32 v132, v131, v127\n\t"
    "s_waitcnt lgkmcnt(9)\n\t"
    "v_and_b32 v120, v120, v112\n\t"
    "v_perm_b32 v120, v120, v120, s46\n\t"
    "ds_or_b32 v94, v120\n\t"
    "v_bfi_b32 v130, v107, 0, v129\n\t"
    "v_cmp_eq_u32 vcc, 0, v130\n\t"
    "v_and_or_b32 v99, v107, s44, v68\n\t"
    "v_xor_b32 v133, 23, v132\n\t"
    "v_lshlrev_b32 v116, v132, v136\n\t"
    "v_and_b32 v132, 7, v132\n\t"
    "v_lshl_or_b32 v116, v136, v133, v116\n\t"
    "v_lshl_add_u32 v132, v132, 1, v128\n\t"
    "ds_or_rtn_b32 v124, v98, v116\n\t"
    "ds_write_b16 v132, v103\n\t"
    "v_cndmask_b32 v130, v130, v129, vcc\n\t"
    "v_ffbl_b32 v131, v130\n\t"
    "v_xor_b32 v132, v131, v127\n\t"
    "s_waitcnt lgkmcnt(10)\n\t"
    "v_and_b32 v121, v121, v113\n\t"
    "v_perm_b32 v121, v121, v121, s46\n\t"
    "ds_or_b32 v95, v121\n\t"
    "v_xor_b32 v133, 23, v132\n\t"
    "v_lshlrev_b32 v117, v132, v136\n\t"
    "v_and_b32 v132, 7, v132\n\t"
    "v_lshl_or_b32 v117, v136, v133, v117\n\t"
    "v_lshl_add_u32 v132, v132, 1, v128\n\t"
    "ds_or_rtn_b32 v125, v99, v117\n\t"
    "ds_write_b16 v132, v107\n\t"
    "s_waitcnt lgkmcnt(10)\n\t"
    "v_and_b32 v122, v122, v114\n\t"
    "v_perm_b32 v122, v122, v122, s46\n\t"
    "ds_or_b32 v96, v122\n\t"
    "ds_read_b128 v[72:75], v128\n\t"
    "v_and_or_b32 v100, v76, s44, v68\n\t"
    "ds_read_b32 v100, v100\n\t"
    "v_and_or_b32 v101, v77, s44, v68\n\t"
    "ds_read_b32 v101, v101\n\t"
    "v_and_or_b32 v102, v78, s44, v68\n\t"
    "ds_read_b32 v102, v102\n\t"
    "v_and_or_b32 v103, v79, s44, v68\n\t"
    "ds_read_b32 v103, v103\n\t"
    "v_and_or_b32 v104, v80, s44, v68\n\t"
    "ds_read_b32 v104, v104\n\t"
    "v_and_or_b32 v105, v81, s44, v68\n\t"
    "ds_read_b32 v105, v105\n\t"
    "v_and_or_b32 v106, v82, s44, v68\n\t"
    "ds_read_b32 v106, v106\n\t"
    "v_and_or_b32 v107, v83, s44, v68\n\t"
    "ds_read_b32 v107, v107\n\t"
    "s_waitcnt vmcnt(2)\n\t"
    "v_lshl_add_u32 v84, v24, 3, s36\n\t"
    "v_lshl_add_u32 v85, v25, 3, s37\n\t"
    "v_lshl_add_u32 v86, v26, 3, s38\n\t"
    "v_lshl_add_u32 v87, v27, 3, s39\n\t"
    "v_lshl_add_u32 v88, v28, 3, s40\n\t"
    "v_lshl_add_u32 v89, v29, 3, s41\n\t"
    "v_lshl_add_u32 v90, v30, 3, s42\n\t"
    "v_lshl_add_u32 v91, v31, 3, s43\n\t"
    "s_movk_i32 s50, 6400\n\t"
    "buffer_load_dwordx4 v[40:43], v65, %0, s50 offen offset:1616\n\t"
    "buffer_load_dwordx4 v[44:47], v65, %0, s50 offen offset:1632\n\t"
    "s_movk_i32 s50, 6400\n\t"
    "buffer_load_dwordx4 v[48:51], v65, %0, s50 offen offset:2416\n\t"
    "buffer_load_dwordx4 v[52:55], v65, %0, s50 offen offset:2432\n\t"
    "s_movk_i32 s50, 9600\n\t"
    "buffer_load_dwordx4 v[56:59], v65, %0, s50 offen offset:16\n\t"
    "buffer_load_dwordx4 v[60:63], v65, %0, s50 offen offset:32\n\t"
    "s_movk_i32 s50, 9600\n\t"
    "buffer_load_dwordx4 v[0:3], v65, %0, s50 offen offset:816\n\t"
    "buffer_load_dwordx4 v[4:7], v65, %0, s50 offen offset:832\n\t"
    "s_movk_i32 s50, 9600\n\t"
    "buffer_load_dwordx4 v[8:11], v65, %0, s50 offen offset:1616\n\t"
    "buffer_load_dwordx4 v[12:15], v65, %0, s50 offen offset:1632\n\t"
    "s_waitcnt lgkmcnt(11)\n\t"
    "v_and_b32 v123, v123, v115\n\t"
    "v_perm_b32 v123, v123, v123, s46\n\t"
    "ds_or_b32 v97, v123\n\t"
    "v_and_b32 v124, v124, v116\n\t"
    "v_perm_b32 v124, v124, v124, s46\n\t"
    "ds_or_b32 v98, v124\n\t"
    "v_and_b32 v125, v125, v117\n\t"
    "v_perm_b32 v125, v125, v125, s46\n\t"
    "ds_or_b32 v99, v125\n\t"
    "s_waitcnt lgkmcnt(11)\n\t"
    "buffer_store_dwordx4 v[72:75], v66, %12, s49 offen offset:64 sc1\n\t"
    "v_mov_b32 v129, 0xffff0000\n\t"
    "s_waitcnt lgkmcnt(7)\n\t"
    "v_perm_b32 v100, v100, v76, v126\n\t"
    "v_perm_b32 v101, v101, v77, v126\n\t"
    "v_perm_b32 v102, v102, v78, v126\n\t"
    "v_perm_b32 v103, v103, v79, v126\n\t"
    "s_waitcnt lgkmcnt(3)\n\t"
    "v_perm_b32 v104, v104, v80, v126\n\t"
    "v_perm_b32 v105, v105, v81, v126\n\t"
    "v_perm_b32 v106, v106, v82, v126\n\t"
    "v_perm_b32 v107, v107, v83, v126\n\t"
    "v_max_u32 v108, v100, v101\n\t"
    "v_min_u32 v101, v100, v101\n\t"
    "v_max_u32 v100, v102, v103\n\t"
    "v_min_u32 v103, v102, v103\n\t"
    "v_max_u32 v102, v104, v105\n\t"
    "v_min_u32 v105, v104, v105\n\t"
    "v_max_u32 v104, v106, v107\n\t"
    "v_min_u32 v107, v106, v107\n\t"
    "v_max_u32 v106, v108, v100\n\t"
    "v_min_u32 v100, v108, v100\n\t"
    "v_max_u32 v108, v101, v103\n\t"
    "v_min_u32 v103, v101, v103\n\t"
    "v_max_u32 v101, v102, v104\n\t"
    "v_min_u32 v104, v102, v104\n\t"
    "v_max_u32 v102, v105, v107\n\t"
    "v_min_u32 v107, v105, v107\n\t"
    "v_bfi_b32 v130, v106, 0, v129\n\t"
    "v_cmp_eq_u32 vcc, 0, v130\n\t"
    "v_and_or_b32 v92, v106, s44, v68\n\t"
    "s_nop 0\n\t"
    "v_cndmask_b32 v130, v130, v129, vcc\n\t"
    "v_ffbl_b32 v131, v130\n\t"
    "v_and_b32 v134, 7, v131\n\t"
    "v_lshlrev_b32 v134, v134, s45\n\t"
    "v_xor_b32 v129, v129, v134\n\t"
    "v_xor_b32 v132, v131, v127\n\t"
    "v_bfi_b32 v130, v101, 0, v129\n\t"
    "v_cmp_eq_u32 vcc, 0, v130\n\t"
    "v_and_or_b32 v93, v101, s44, v68\n\t"
    "v_xor_b32 v133, 23, v132\n\t"
    "v_lshlrev_b32 v110, v132, v136\n\t"
    "v_and_b32 v132, 7, v132\n\t"
    "v_lshl_or_b32 v110, v136, v133, v110\n\t"
    "v_lshl_add_u32 v132, v132, 1, v128\n\t"
    "ds_or_rtn_b32 v118, v92, v110\n\t"
    "ds_write_b16 v132, v106\n\t"
    "v_cndmask_b32 v130, v130, v129, vcc\n\t"
    "v_ffbl_b32 v131, v130\n\t"
    "v_and_b32 v134, 7, v131\n\t"
    "v_lshlrev_b32 v134, v134, s45\n\t"
    "v_xor_b32 v129, v129, v134\n\t"
    "v_xor_b32 v132, v131, v127\n\t"
    "v_bfi_b32 v130, v108, 0, v129\n\t"
    "v_cmp_eq_u32 vcc, 0, v130\n\t"
    "v_and_or_b32 v94, v108, s44, v68\n\t"
    "v_xor_b32 v133, 23, v132\n\t"
    "v_lshlrev_b32 v111, v132, v136\n\t"
    "v_and_b32 v132, 7, v132\n\t"
    "v_lshl_or_b32 v111, v136, v133, v111\n\t"
    "v_lshl_add_u32 v132, v132, 1, v128\n\t"
    "ds_or_rtn_b32 v119, v93, v111\n\t"
    "ds_write_b16 v132, v101\n\t"
    "v_cndmask_b32 v130, v130, v129, vcc\n\t"
    "v_ffbl_b32 v131, v130\n\t"
    "v_and_b32 v134, 7, v131\n\t"
    "v_lshlrev_b32 v134, v134, s45\n\t"
    "v_xor_b32 v129, v129, v134\n\t"
    "v_xor_b32 v132, v131, v127\n\t"
    "v_bfi_b32 v130, v100, 0, v129\n\t"
    "v_cmp_eq_u32 vcc, 0, v130\n\t"
    "v_and_or_b32 v95, v100, s44, v68\n\t"
    "v_xor_b32 v133, 23, v132\n\t"
    "v_lshlrev_b32 v112, v132, v136\n\t"
    "v_and_b32 v132, 7, v132\n\t"
    "v_lshl_or_b32 v112, v136, v133, v112\n\t"
    "v_lshl_add_u32 v132, v132, 1, v128\n\t"
    "ds_or_rtn_b32 v120, v94, v112\n\t"
    "ds_write_b16 v132, v108\n\t"
    "v_cndmask_b32 v130, v130, v129, vcc\n\t"
    "v_ffbl_b32 v131, v130\n\t"
    "v_and_b32 v134, 7, v131\n\t"
    "v_lshlrev_b32 v134, v134, s45\n\t"
    "v_xor_b32 v129, v129, v134\n\t"
    "v_xor_b32 v132, v131, v127\n\t"
    "v_bfi_b32 v130, v102, 0, v129\n\t"
    "v_cmp_eq_u32 vcc, 0, v130\n\t"
    "v_and_or_b32 v96, v102, s44, v68\n\t"
    "v_xor_b32 v133, 23, v132\n\t"
    "v_lshlrev_b32 v113, v132, v136\n\t"
    "v_and_b32 v132, 7, v132\n\t"
    "v_lshl_or_b32 v113, v136, v133, v113\n\t"
    "v_lshl_add_u32 v132, v132, 1, v128\n\t"
    "ds_or_rtn_b32 v121, v95, v113\n\t"
    "ds_write_b16 v132, v100\n\t"
    "v_cndmask_b32 v130, v130, v129, vcc\n\t"
    "v_ffbl_b32 v131, v130\n\t"
    "v_and_b32 v134, 7, v131\n\t"
    "v_lshlrev_b32 v134, v134, s45\n\t"
    "v_xor_b32 v129, v129, v134\n\t"
    "v_xor_b32 v132, v131, v127\n\t"
    "s_waitcnt lgkmcnt(7)\n\t"
    "v_and_b32 v118, v118, v110\n\t"
    "v_perm_b32 v118, v118, v118, s46\n\t"
    "ds_or_b32 v92, v118\n\t"
    "v_bfi_b32 v130, v104, 0, v129\n\t"
    "v_cmp_eq_u32 vcc, 0, v130\n\t"
    "v_and_or_b32 v97, v104, s44, v68\n\t"
    "v_xor_b32 v133, 23, v132\n\t"
    "v_lshlrev_b32 v114, v132, v136\n\t"
    "v_and_b32 v132, 7, v132\n\t"
    "v_lshl_or_b32 v114, v136, v133, v114\n\t"
    "v_lshl_add_u32 v132, v132, 1, v128\n\t"
    "ds_or_rtn_b32 v122, v96, v114\n\t"
    "ds_write_b16 v132, v102\n\t"
    "v_cndmask_b32 v130, v130, v129, vcc\n\t"
    "v_ffbl_b32 v131, v130\n\t"
    "v_and_b32 v134, 7, v131\n\t"
    "v_lshlrev_b32 v134, v134, s45\n\t"
    "v_xor_b32 v129, v129, v134\n\t"
    "v_xor_b32 v132, v131, v127\n\t"
    "s_waitcnt lgkmcnt(8)\n\t"
    "v_and_b32 v119, v119, v111\n\t"
    "v_perm_b32 v119, v119, v119, s46\n\t"
    "ds_or_b32 v93, v119\n\t"
    "v_bfi_b32 v130, v103, 0, v129\n\t"
    "v_cmp_eq_u32 vcc, 0, v130\n\t"
    "v_and_or_b32 v98, v103, s44, v68\n\t"
    "v_xor_b32 v133, 23, v132\n\t"
    "v_lshlrev_b32 v115, v132, v136\n\t"
    "v_and_b32 v132, 7, v132\n\t"
    "v_lshl_or_b32 v115, v136, v133, v115\n\t"
    "v_lshl_add_u32 v132, v132, 1, v128\n\t"
    "ds_or_rtn_b32 v123, v97, v115\n\t"
    "ds_write_b16 v132, v104\n\t"
    "v_cndmask_b32 v130, v130, v129, vcc\n\t"
    "v_ffbl_b32 v131, v130\n\t"
    "v_and_b32 v134, 7, v131\n\t"
    "v_lshlrev_b32 v134, v134, s45\n\t"
    "v_xor_b32 v129, v129, v134\n\t"
    "v_xor_b32 v132, v131, v127\n\t"
    "s_waitcnt lgkmcnt(9)\n\t"
    "v_and_b32 v120, v120, v112\n\t"
    "v_perm_b32 v120, v120, v120, s46\n\t"
    "ds_or_b32 v94, v120\n\t"
    "v_bfi_b32 v130, v107, 0, v129\n\t"
    "v_cmp_eq_u32 vcc, 0, v130\n\t"
    "v_and_or_b32 v99, v107, s44, v68\n\t"
    "v_xor_b32 v133, 23, v132\n\t"
    "v_lshlrev_b32 v116, v132, v136\n\t"
    "v_and_b32 v132, 7, v132\n\t"
    "v_lshl_or_b32 v116, v136, v133, v116\n\t"
    "v_lshl_add_u32 v132, v132, 1, v128\n\t"
    "ds_or_rtn_b32 v124, v98, v116\n\t"
    "ds_write_b16 v132, v103\n\t"
    "v_cndmask_b32 v130, v130, v129, vcc\n\t"
    "v_ffbl_b32 v131, v130\n\t"
    "v_xor_b32 v132, v131, v127\n\t"
    "s_waitcnt lgkmcnt(10)\n\t"
    "v_and_b32 v121, v121, v113\n\t"
    "v_perm_b32 v121, v121, v121, s46\n\t"
    "ds_or_b32 v95, v121\n\t"
    "v_xor_b32 v133, 23, v132\n\t"
    "v_lshlrev_b32 v117, v132, v136\n\t"
    "v_and_b32 v132, 7, v132\n\t"
    "v_lshl_or_b32 v117, v136, v133, v117\n\t"
    "v_lshl_add_u32 v132, v132, 1, v128\n\t"
    "ds_or_rtn_b32 v125, v99, v117\n\t"
    "ds_write_b16 v132, v107\n\t"
    "s_waitcnt lgkmcnt(10)\n\t"
    "v_and_b32 v122, v122, v114\n\t"
    "v_perm_b32 v122, v122, v122, s46\n\t"
    "ds_or_b32 v96, v122\n\t"
    "ds_read_b128 v[72:75], v128\n\t"
    "v_and_or_b32 v100, v84, s44, v68\n\t"
    "ds_read_b32 v100, v100\n\t"
    "v_and_or_b32 v101, v85, s44, v68\n\t"
    "ds_read_b32 v101, v101\n\t"
    "v_and_or_b32 v102, v86, s44, v68\n\t"
    "ds_read_b32 v102, v102\n\t"
    "v_and_or_b32 v103, v87, s44, v68\n\t"
    "ds_read_b32 v103, v103\n\t"
    "v_and_or_b32 v104, v88, s44, v68\n\t"
    "ds_read_b32 v104, v104\n\t"
    "v_and_or_b32 v105, v89, s44, v68\n\t"
    "ds_read_b32 v105, v105\n\t"
    "v_and_or_b32 v106, v90, s44, v68\n\t"
    "ds_read_b32 v106, v106\n\t"
    "v_and_or_b32 v107, v91, s44, v68\n\t"
    "ds_read_b32 v107, v107\n\t"
    "s_waitcnt vmcnt(11)\n\t"
    "v_lshl_add_u32 v76, v32, 3, s36\n\t"
    "v_lshl_add_u32 v77, v33, 3, s37\n\t"
    "v_lshl_add_u32 v78, v34, 3, s38\n\t"
    "v_lshl_add_u32 v79, v35, 3, s39\n\t"
    "v_lshl_add_u32 v80, v36, 3, s40\n\t"
    "v_lshl_add_u32 v81, v37, 3, s41\n\t"
    "v_lshl_add_u32 v82, v38, 3, s42\n\t"
    "v_lshl_add_u32 v83, v39, 3, s43\n\t"
    "s_movk_i32 s50, 9600\n\t"
    "buffer_load_dwordx4 v[16:19], v65, %0, s50 offen offset:2416\n\t"
    "buffer_load_dwordx4 v[20:23], v65, %0, s50 offen offset:2432\n\t"
    "v_mov_b32 v134, 1032\n\t"
    "L_asvsp8_%=:\n\t"
    "ds_read_b32 v130, v134\n\t"
    "s_waitcnt lgkmcnt(0)\n\t"
    "v_readfirstlane_b32 s50, v130\n\t"
    "s_nop 0\n\t"
    "s_cmp_lt_u32 s50, 6\n\t"
    "s_cbranch_scc1 L_asvsp8_%=\n\t"
    "s_mov_b64 s[96:97], %2\n\t"
    "s_and_b32 s97, s97, 0xffff\n\t"
    "buffer_load_dwordx4 v[156:159], v137, s[96:99], s47 offen offset:0\n\t"
    "buffer_load_dwordx4 v[160:163], v138, s[96:99], s47 offen offset:0\n\t"
    "buffer_load_dwordx4 v[164:167], v137, s[96:99], s47 offen offset:64\n\t"
    "buffer_load_dwordx4 v[168:171], v138, s[96:99], s47 offen offset:64\n\t"
    "s_add_u32 s47, s47, 0x100000\n\t"
    "buffer_load_dwordx4 v[172:175], v137, s[96:99], s47 offen offset:0\n\t"
    "buffer_load_dwordx4 v[176:179], v138, s[96:99], s47 offen offset:0\n\t"
    "buffer_load_dwordx4 v[180:183], v137, s[96:99], s47 offen offset:64\n\t"
    "buffer_load_dwordx4 v[184:187], v138, s[96:99], s47 offen offset:64\n\t"
    "s_add_u32 s47, s47, 0x100000\n\t"
    "buffer_load_dwordx4 v[188:191], v137, s[96:99], s47 offen offset:0\n\t"
    "buffer_load_dwordx4 v[192:195], v138, s[96:99], s47 offen offset:0\n\t"
    "buffer_load_dwordx4 v[196:199], v137, s[96:99], s47 offen offset:64\n\t"
    "buffer_load_dwordx4 v[200:203], v138, s[96:99], s47 offen offset:64\n\t"
    "s_add_u32 s47, s47, 0x100000\n\t"
    "v_and_b32 v123, v123, v115\n\t"
    "v_perm_b32 v123, v123, v123, s46\n\t"
    "ds_or_b32 v97, v123\n\t"
    "v_and_b32 v124, v124, v116\n\t"
    "v_perm_b32 v124, v124, v124, s46\n\t"
    "ds_or_b32 v98, v124\n\t"
    "v_and_b32 v125, v125, v117\n\t"
    "v_perm_b32 v125, v125, v125, s46\n\t"
    "ds_or_b32 v99, v125\n\t"
    "buffer_store_dwordx4 v[72:75], v66, %12, s49 offen offset:128 sc1\n\t"
    "v_mov_b32 v129, 0xffff0000\n\t"
    "v_perm_b32 v100, v100, v84, v126\n\t"
    "v_perm_b32 v101, v101, v85, v126\n\t"
    "v_perm_b32 v102, v102, v86, v126\n\t"
    "v_perm_b32 v103, v103, v87, v126\n\t"
    "v_perm_b32 v104, v104, v88, v126\n\t"
    "v_perm_b32 v105, v105, v89, v126\n\t"
    "v_perm_b32 v106, v106, v90, v126\n\t"
    "v_perm_b32 v107, v107, v91, v126\n\t"
    "v_max_u32 v108, v100, v101\n\t"
    "v_min_u32 v101, v100, v101\n\t"
    "v_max_u32 v100, v102, v103\n\t"
    "v_min_u32 v103, v102, v103\n\t"
    "v_max_u32 v102, v104, v105\n\t"
    "v_min_u32 v105, v104, v105\n\t"
    "v_max_u32 v104, v106, v107\n\t"
    "v_min_u32 v107, v106, v107\n\t"
    "v_max_u32 v106, v108, v100\n\t"
    "v_min_u32 v100, v108, v100\n\t"
    "v_max_u32 v108, v101, v103\n\t"
    "v_min_u32 v103, v101, v103\n\t"
    "v_max_u32 v101, v102, v104\n\t"
    "v_min_u32 v104, v102, v104\n\t"
    "v_max_u32 v102, v105, v107\n\t"
    "v_min_u32 v107, v105, v107\n\t"
    "v_bfi_b32 v130, v106, 0, v129\n\t"
    "v_cmp_eq_u32 vcc, 0, v130\n\t"
    "v_and_or_b32 v92, v106, s44, v68\n\t"
    "s_nop 0\n\t"
    "v_cndmask_b32 v130, v130, v129, vcc\n\t"
    "v_ffbl_b32 v131, v130\n\t"
    "v_and_b32 v134, 7, v131\n\t"
    "v_lshlrev_b32 v134, v134, s45\n\t"
    "v_xor_b32 v129, v129, v134\n\t"
    "v_xor_b32 v132, v131, v127\n\t"
    "v_bfi_b32 v130, v101, 0, v129\n\t"
    "v_cmp_eq_u32 vcc, 0, v130\n\t"
    "v_and_or_b32 v93, v101, s44, v68\n\t"
    "v_xor_b32 v133, 23, v132\n\t"
    "v_lshlrev_b32 v110, v132, v136\n\t"
    "v_and_b32 v132, 7, v132\n\t"
    "v_lshl_or_b32 v110, v136, v133, v110\n\t"
    "v_lshl_add_u32 v132, v132, 1, v128\n\t"
    "ds_or_rtn_b32 v118, v92, v110\n\t"
    "ds_write_b16 v132, v106\n\t"
    "v_cndmask_b32 v130, v130, v129, vcc\n\t"
    "v_ffbl_b32 v131, v130\n\t"
    "v_and_b32 v134, 7, v131\n\t"
    "v_lshlrev_b32 v134, v134, s45\n\t"
    "v_xor_b32 v129, v129, v134\n\t"
    "v_xor_b32 v132, v131, v127\n\t"
    "v_bfi_b32 v130, v108, 0, v129\n\t"
    "v_cmp_eq_u32 vcc, 0, v130\n\t"
    "v_and_or_b32 v94, v108, s44, v68\n\t"
    "v_xor_b32 v133, 23, v132\n\t"
    "v_lshlrev_b32 v111, v132, v136\n\t"
    "v_and_b32 v132, 7, v132\n\t"
    "v_lshl_or_b32 v111, v136, v133, v111\n\t"
    "v_lshl_add_u32 v132, v132, 1, v128\n\t"
    "ds_or_rtn_b32 v119, v93, v111\n\t"
    "ds_write_b16 v132, v101\n\t"
    "v_cndmask_b32 v130, v130, v129, vcc\n\t"
    "v_ffbl_b32 v131, v130\n\t"
    "v_and_b32 v134, 7, v131\n\t"
    "v_lshlrev_b32 v134, v134, s45\n\t"
    "v_xor_b32 v129, v129, v134\n\t"
    "v_xor_b32 v132, v131, v127\n\t"
    "v_bfi_b32 v130, v100, 0, v129\n\t"
    "v_cmp_eq_u32 vcc, 0, v130\n\t"
    "v_and_or_b32 v95, v100, s44, v68\n\t"
    "v_xor_b32 v133, 23, v132\n\t"
    "v_lshlrev_b32 v112, v132, v136\n\t"
    "v_and_b32 v132, 7, v132\n\t"
    "v_lshl_or_b32 v112, v136, v133, v112\n\t"
    "v_lshl_add_u32 v132, v132, 1, v128\n\t"
    "ds_or_rtn_b32 v120, v94, v112\n\t"
    "ds_write_b16 v132, v108\n\t"
    "v_cndmask_b32 v130, v130, v129, vcc\n\t"
    "v_ffbl_b32 v131, v130\n\t"
    "v_and_b32 v134, 7, v131\n\t"
    "v_lshlrev_b32 v134, v134, s45\n\t"
    "v_xor_b32 v129, v129, v134\n\t"
    "v_xor_b32 v132, v131, v127\n\t"
    "v_bfi_b32 v130, v102, 0, v129\n\t"
    "v_cmp_eq_u32 vcc, 0, v130\n\t"
    "v_and_or_b32 v96, v102, s44, v68\n\t"
    "v_xor_b32 v133, 23, v132\n\t"
    "v_lshlrev_b32 v113, v132, v136\n\t"
    "v_and_b32 v132, 7, v132\n\t"
    "v_lshl_or_b32 v113, v136, v133, v113\n\t"
    "v_lshl_add_u32 v132, v132, 1, v128\n\t"
    "ds_or_rtn_b32 v121, v95, v113\n\t"
    "ds_write_b16 v132, v100\n\t"
    "v_cndmask_b32 v130, v130, v129, vcc\n\t"
    "v_ffbl_b32 v131, v130\n\t"
    "v_and_b32 v134, 7, v131\n\t"
    "v_lshlrev_b32 v134, v134, s45\n\t"
    "v_xor_b32 v129, v129, v134\n\t"
    "v_xor_b32 v132, v131, v127\n\t"
    "s_waitcnt lgkmcnt(7)\n\t"
    "v_and_b32 v118, v118, v110\n\t"
    "v_perm_b32 v118, v118, v118, s46\n\t"
    "ds_or_b32 v92, v118\n\t"
    "v_bfi_b32 v130, v104, 0, v129\n\t"
    "v_cmp_eq_u32 vcc, 0, v130\n\t"
    "v_and_or_b32 v97, v104, s44, v68\n\t"
    "v_xor_b32 v133, 23, v132\n\t"
    "v_lshlrev_b32 v114, v132, v136\n\t"
    "v_and_b32 v132, 7, v132\n\t"
    "v_lshl_or_b32 v114, v136, v133, v114\n\t"
    "v_lshl_add_u32 v132, v132, 1, v128\n\t"
    "ds_or_rtn_b32 v122, v96, v114\n\t"
    "ds_write_b16 v132, v102\n\t"
    "v_cndmask_b32 v130, v130, v129, vcc\n\t"
    "v_ffbl_b32 v131, v130\n\t"
    "v_and_b32 v134, 7, v131\n\t"
    "v_lshlrev_b32 v134, v134, s45\n\t"
    "v_xor_b32 v129, v129, v134\n\t"
    "v_xor_b32 v132, v131, v127\n\t"
    "s_waitcnt lgkmcnt(8)\n\t"
    "v_and_b32 v119, v119, v111\n\t"
    "v_perm_b32 v119, v119, v119, s46\n\t"
    "ds_or_b32 v93, v119\n\t"
    "v_bfi_b32 v130, v103, 0, v129\n\t"
    "v_cmp_eq_u32 vcc, 0, v130\n\t"
    "v_and_or_b32 v98, v103, s44, v68\n\t"
    "v_xor_b32 v133, 23, v132\n\t"
    "v_lshlrev_b32 v115, v132, v136\n\t"
    "v_and_b32 v132, 7, v132\n\t"
    "v_lshl_or_b32 v115, v136, v133, v115\n\t"
    "v_lshl_add_u32 v132, v132, 1, v128\n\t"
    "ds_or_rtn_b32 v123, v97, v115\n\t"
    "ds_write_b16 v132, v104\n\t"
    "v_cndmask_b32 v130, v130, v129, vcc\n\t"
    "v_ffbl_b32 v131, v130\n\t"
    "v_and_b32 v134, 7, v131\n\t"
    "v_lshlrev_b32 v134, v134, s45\n\t"
    "v_xor_b32 v129, v129, v134\n\t"
    "v_xor_b32 v132, v131, v127\n\t"
    "s_waitcnt lgkmcnt(9)\n\t"
    "v_and_b32 v120, v120, v112\n\t"
    "v_perm_b32 v120, v120, v120, s46\n\t"
    "ds_or_b32 v94, v120\n\t"
    "v_bfi_b32 v130, v107, 0, v129\n\t"
    "v_cmp_eq_u32 vcc, 0, v130\n\t"
    "v_and_or_b32 v99, v107, s44, v68\n\t"
    "v_xor_b32 v133, 23, v132\n\t"
    "v_lshlrev_b32 v116, v132, v136\n\t"
    "v_and_b32 v132, 7, v132\n\t"
    "v_lshl_or_b32 v116, v136, v133, v116\n\t"
    "v_lshl_add_u32 v132, v132, 1, v128\n\t"
    "ds_or_rtn_b32 v124, v98, v116\n\t"
    "ds_write_b16 v132, v103\n\t"
    "v_cndmask_b32 v130, v130, v129, vcc\n\t"
    "v_ffbl_b32 v131, v130\n\t"
    "v_xor_b32 v132, v131, v127\n\t"
    "s_waitcnt lgkmcnt(10)\n\t"
    "v_and_b32 v121, v121, v113\n\t"
    "v_perm_b32 v121, v121, v121, s46\n\t"
    "ds_or_b32 v95, v121\n\t"
    "v_xor_b32 v133, 23, v132\n\t"
    "v_lshlrev_b32 v117, v132, v136\n\t"
    "v_and_b32 v132, 7, v132\n\t"
    "v_lshl_or_b32 v117, v136, v133, v117\n\t"
    "v_lshl_add_u32 v132, v132, 1, v128\n\t"
    "ds_or_rtn_b32 v125, v99, v117\n\t"
    "ds_write_b16 v132, v107\n\t"
    "s_waitcnt lgkmcnt(10)\n\t"
    "v_and_b32 v122, v122, v114\n\t"
    "v_perm_b32 v122, v122, v122, s46\n\t"
    "ds_or_b32 v96, v122\n\t"
    "ds_read_b128 v[72:75], v128\n\t"
    "v_and_or_b32 v100, v76, s44, v68\n\t"
    "ds_read_b32 v100, v100\n\t"
    "v_and_or_b32 v101, v77, s44, v68\n\t"
    "ds_read_b32 v101, v101\n\t"
    "v_and_or_b32 v102, v78, s44, v68\n\t"
    "ds_read_b32 v102, v102\n\t"
    "v_and_or_b32 v103, v79, s44, v68\n\t"
    "ds_read_b32 v103, v103\n\t"
    "v_and_or_b32 v104, v80, s44, v68\n\t"
    "ds_read_b32 v104, v104\n\t"
    "v_and_or_b32 v105, v81, s44, v68\n\t"
    "ds_read_b32 v105, v105\n\t"
    "v_and_or_b32 v106, v82, s44, v68\n\t"
    "ds_read_b32 v106, v106\n\t"
    "v_and_or_b32 v107, v83, s44, v68\n\t"
    "ds_read_b32 v107, v107\n\t"
    "s_waitcnt vmcnt(24)\n\t"
    "v_lshl_add_u32 v84, v40, 3, s36\n\t"
    "v_lshl_add_u32 v85, v41, 3, s37\n\t"
    "v_lshl_add_u32 v86, v42, 3, s38\n\t"
    "v_lshl_add_u32 v87, v43, 3, s39\n\t"
    "v_lshl_add_u32 v88, v44, 3, s40\n\t"
    "v_lshl_add_u32 v89, v45, 3, s41\n\t"
    "v_lshl_add_u32 v90, v46, 3, s42\n\t"
    "v_lshl_add_u32 v91, v47, 3, s43\n\t"
    "buffer_load_dwordx4 v[204:207], v137, s[96:99], s47 offen offset:0\n\t"
    "buffer_load_dwordx4 v[208:211], v138, s[96:99], s47 offen offset:0\n\t"
    "buffer_load_dwordx4 v[212:215], v137, s[96:99], s47 offen offset:64\n\t"
    "buffer_load_dwordx4 v[216:219], v138, s[96:99], s47 offen offset:64\n\t"
    "s_add_u32 s47, s47, 0x100000\n\t"
    "buffer_load_dwordx4 v[220:223], v137, s[96:99], s47 offen offset:0\n\t"
    "buffer_load_dwordx4 v[224:227], v138, s[96:99], s47 offen offset:0\n\t"
    "buffer_load_dwordx4 v[228:231], v137, s[96:99], s47 offen offset:64\n\t"
    "buffer_load_dwordx4 v[232:235], v138, s[96:99], s47 offen offset:64\n\t"
    "s_add_u32 s47, s47, 0x100000\n\t"
    "buffer_load_dwordx4 v[236:239], v137, s[96:99], s47 offen offset:0\n\t"
    "buffer_load_dwordx4 v[240:243], v138, s[96:99], s47 offen offset:0\n\t"
    "buffer_load_dwordx4 v[244:247], v137, s[96:99], s47 offen offset:64\n\t"
    "buffer_load_dwordx4 v[248:251], v138, s[96:99], s47 offen offset:64\n\t"
    "s_add_u32 s47, s47, 0x100000\n\t"
    "s_waitcnt lgkmcnt(11)\n\t"
    "v_and_b32 v123, v123, v115\n\t"
    "v_perm_b32 v123, v123, v123, s46\n\t"
    "ds_or_b32 v97, v123\n\t"
    "v_and_b32 v124, v124, v116\n\t"
    "v_perm_b32 v124, v124, v124, s46\n\t"
    "ds_or_b32 v98, v124\n\t"
    "v_and_b32 v125, v125, v117\n\t"
    "v_perm_b32 v125, v125, v125, s46\n\t"
    "ds_or_b32 v99, v125\n\t"
    "s_waitcnt lgkmcnt(11)\n\t"
    "buffer_store_dwordx4 v[72:75], v66, %12, s49 offen offset:192 sc1\n\t"
    "v_mov_b32 v129, 0xffff0000\n\t"
    "s_waitcnt lgkmcnt(7)\n\t"
    "v_perm_b32 v100, v100, v76, v126\n\t"
    "v_perm_b32 v101, v101, v77, v126\n\t"
    "v_perm_b32 v102, v102, v78, v126\n\t"
    "v_perm_b32 v103, v103, v79, v126\n\t"
    "s_waitcnt lgkmcnt(3)\n\t"
    "v_perm_b32 v104, v104, v80, v126\n\t"
    "v_perm_b32 v105, v105, v81, v126\n\t"
    "v_perm_b32 v106, v106, v82, v126\n\t"
    "v_perm_b32 v107, v107, v83, v126\n\t"
    "v_max_u32 v108, v100, v101\n\t"
    "v_min_u32 v101, v100, v101\n\t"
    "v_max_u32 v100, v102, v103\n\t"
    "v_min_u32 v103, v102, v103\n\t"
    "v_max_u32 v102, v104, v105\n\t"
    "v_min_u32 v105, v104, v105\n\t"
    "v_max_u32 v104, v106, v107\n\t"
    "v_min_u32 v107, v106, v107\n\t"
    "v_max_u32 v106, v108, v100\n\t"
    "v_min_u32 v100, v108, v100\n\t"
    "v_max_u32 v108, v101, v103\n\t"
    "v_min_u32 v103, v101, v103\n\t"
    "v_max_u32 v101, v102, v104\n\t"
    "v_min_u32 v104, v102, v104\n\t"
    "v_max_u32 v102, v105, v107\n\t"
    "v_min_u32 v107, v105, v107\n\t"
    "v_bfi_b32 v130, v106, 0, v129\n\t"
    "v_cmp_eq_u32 vcc, 0, v130\n\t"
    "v_and_or_b32 v92, v106, s44, v68\n\t"
    "s_nop 0\n\t"
    "v_cndmask_b32 v130, v130, v129, vcc\n\t"
    "v_ffbl_b32 v131, v130\n\t"
    "v_and_b32 v134, 7, v131\n\t"
    "v_lshlrev_b32 v134, v134, s45\n\t"
    "v_xor_b32 v129, v129, v134\n\t"
    "v_xor_b32 v132, v131, v127\n\t"
    "v_bfi_b32 v130, v101, 0, v129\n\t"
    "v_cmp_eq_u32 vcc, 0, v130\n\t"
    "v_and_or_b32 v93, v101, s44, v68\n\t"
    "v_xor_b32 v133, 23, v132\n\t"
    "v_lshlrev_b32 v110, v132, v136\n\t"
    "v_and_b32 v132, 7, v132\n\t"
    "v_lshl_or_b32 v110, v136, v133, v110\n\t"
    "v_lshl_add_u32 v132, v132, 1, v128\n\t"
    "ds_or_rtn_b32 v118, v92, v110\n\t"
    "ds_write_b16 v132, v106\n\t"
    "v_cndmask_b32 v130, v130, v129, vcc\n\t"
    "v_ffbl_b32 v131, v130\n\t"
    "v_and_b32 v134, 7, v131\n\t"
    "v_lshlrev_b32 v134, v134, s45\n\t"
    "v_xor_b32 v129, v129, v134\n\t"
    "v_xor_b32 v132, v131, v127\n\t"
    "v_bfi_b32 v130, v108, 0, v129\n\t"
    "v_cmp_eq_u32 vcc, 0, v130\n\t"
    "v_and_or_b32 v94, v108, s44, v68\n\t"
    "v_xor_b32 v133, 23, v132\n\t"
    "v_lshlrev_b32 v111, v132, v136\n\t"
    "v_and_b32 v132, 7, v132\n\t"
    "v_lshl_or_b32 v111, v136, v133, v111\n\t"
    "v_lshl_add_u32 v132, v132, 1, v128\n\t"
    "ds_or_rtn_b32 v119, v93, v111\n\t"
    "ds_write_b16 v132, v101\n\t"
    "v_cndmask_b32 v130, v130, v129, vcc\n\t"
    "v_ffbl_b32 v131, v130\n\t"
    "v_and_b32 v134, 7, v131\n\t"
    "v_lshlrev_b32 v134, v134, s45\n\t"
    "v_xor_b32 v129, v129, v134\n\t"
    "v_xor_b32 v132, v131, v127\n\t"
    "v_bfi_b32 v130, v100, 0, v129\n\t"
    "v_cmp_eq_u32 vcc, 0, v130\n\t"
    "v_and_or_b32 v95, v100, s44, v68\n\t"
    "v_xor_b32 v133, 23, v132\n\t"
    "v_lshlrev_b32 v112, v132, v136\n\t"
    "v_and_b32 v132, 7, v132\n\t"
    "v_lshl_or_b32 v112, v136, v133, v112\n\t"
    "v_lshl_add_u32 v132, v132, 1, v128\n\t"
    "ds_or_rtn_b32 v120, v94, v112\n\t"
    "ds_write_b16 v132, v108\n\t"
    "v_cndmask_b32 v130, v130, v129, vcc\n\t"
    "v_ffbl_b32 v131, v130\n\t"
    "v_and_b32 v134, 7, v131\n\t"
    "v_lshlrev_b32 v134, v134, s45\n\t"
    "v_xor_b32 v129, v129, v134\n\t"
    "v_xor_b32 v132, v131, v127\n\t"
    "v_bfi_b32 v130, v102, 0, v129\n\t"
    "v_cmp_eq_u32 vcc, 0, v130\n\t"
    "v_and_or_b32 v96, v102, s44, v68\n\t"
    "v_xor_b32 v133, 23, v132\n\t"
    "v_lshlrev_b32 v113, v132, v136\n\t"
    "v_and_b32 v132, 7, v132\n\t"
    "v_lshl_or_b32 v113, v136, v133, v113\n\t"
    "v_lshl_add_u32 v132, v132, 1, v128\n\t"
    "ds_or_rtn_b32 v121, v95, v113\n\t"
    "ds_write_b16 v132, v100\n\t"
    "v_cndmask_b32 v130, v130, v129, vcc\n\t"
    "v_ffbl_b32 v131, v130\n\t"
    "v_and_b32 v134, 7, v131\n\t"
    "v_lshlrev_b32 v134, v134, s45\n\t"
    "v_xor_b32 v129, v129, v134\n\t"
    "v_xor_b32 v132, v131, v127\n\t"
    "s_waitcnt lgkmcnt(7)\n\t"
    "v_and_b32 v118, v118, v110\n\t"
    "v_perm_b32 v118, v118, v118, s46\n\t"
    "ds_or_b32 v92, v118\n\t"
    "v_bfi_b32 v130, v104, 0, v129\n\t"
    "v_cmp_eq_u32 vcc, 0, v130\n\t"
    "v_and_or_b32 v97, v104, s44, v68\n\t"
    "v_xor_b32 v133, 23, v132\n\t"
    "v_lshlrev_b32 v114, v132, v136\n\t"
    "v_and_b32 v132, 7, v132\n\t"
    "v_lshl_or_b32 v114, v136, v133, v114\n\t"
    "v_lshl_add_u32 v132, v132, 1, v128\n\t"
    "ds_or_rtn_b32 v122, v96, v114\n\t"
    "ds_write_b16 v132, v102\n\t"
    "v_cndmask_b32 v130, v130, v129, vcc\n\t"
    "v_ffbl_b32 v131, v130\n\t"
    "v_and_b32 v134, 7, v131\n\t"
    "v_lshlrev_b32 v134, v134, s45\n\t"
    "v_xor_b32 v129, v129, v134\n\t"
    "v_xor_b32 v132, v131, v127\n\t"
    "s_waitcnt lgkmcnt(8)\n\t"
    "v_and_b32 v119, v119, v111\n\t"
    "v_perm_b32 v119, v119, v119, s46\n\t"
    "ds_or_b32 v93, v119\n\t"
    "v_bfi_b32 v130, v103, 0, v129\n\t"
    "v_cmp_eq_u32 vcc, 0, v130\n\t"
    "v_and_or_b32 v98, v103, s44, v68\n\t"
    "v_xor_b32 v133, 23, v132\n\t"
    "v_lshlrev_b32 v115, v132, v136\n\t"
    "v_and_b32 v132, 7, v132\n\t"
    "v_lshl_or_b32 v115, v136, v133, v115\n\t"
    "v_lshl_add_u32 v132, v132, 1, v128\n\t"
    "ds_or_rtn_b32 v123, v97, v115\n\t"
    "ds_write_b16 v132, v104\n\t"
    "v_cndmask_b32 v130, v130, v129, vcc\n\t"
    "v_ffbl_b32 v131, v130\n\t"
    "v_and_b32 v134, 7, v131\n\t"
    "v_lshlrev_b32 v134, v134, s45\n\t"
    "v_xor_b32 v129, v129, v134\n\t"
    "v_xor_b32 v132, v131, v127\n\t"
    "s_waitcnt lgkmcnt(9)\n\t"
    "v_and_b32 v120, v120, v112\n\t"
    "v_perm_b32 v120, v120, v120, s46\n\t"
    "ds_or_b32 v94, v120\n\t"
    "v_bfi_b32 v130, v107, 0, v129\n\t"
    "v_cmp_eq_u32 vcc, 0, v130\n\t"
    "v_and_or_b32 v99, v107, s44, v68\n\t"
    "v_xor_b32 v133, 23, v132\n\t"
    "v_lshlrev_b32 v116, v132, v136\n\t"
    "v_and_b32 v132, 7, v132\n\t"
    "v_lshl_or_b32 v116, v136, v133, v116\n\t"
    "v_lshl_add_u32 v132, v132, 1, v128\n\t"
    "ds_or_rtn_b32 v124, v98, v116\n\t"
    "ds_write_b16 v132, v103\n\t"
    "v_cndmask_b32 v130, v130, v129, vcc\n\t"
    "v_ffbl_b32 v131, v130\n\t"
    "v_xor_b32 v132, v131, v127\n\t"
    "s_waitcnt lgkmcnt(10)\n\t"
    "v_and_b32 v121, v121, v113\n\t"
    "v_perm_b32 v121, v121, v121, s46\n\t"
    "ds_or_b32 v95, v121\n\t"
    "v_xor_b32 v133, 23, v132\n\t"
    "v_lshlrev_b32 v117, v132, v136\n\t"
    "v_and_b32 v132, 7, v132\n\t"
    "v_lshl_or_b32 v117, v136, v133, v117\n\t"
    "v_lshl_add_u32 v132, v132, 1, v128\n\t"
    "ds_or_rtn_b32 v125, v99, v117\n\t"
    "ds_write_b16 v132, v107\n\t"
    "s_waitcnt lgkmcnt(10)\n\t"
    "v_and_b32 v122, v122, v114\n\t"
    "v_perm_b32 v122, v122, v122, s46\n\t"
    "ds_or_b32 v96, v122\n\t"
    "ds_read_b128 v[72:75], v128\n\t"
    "v_mov_b32 v134, 1036\n\t"
    "L_asvsp12_%=:\n\t"
    "ds_read_b32 v130, v134\n\t"
    "s_waitcnt lgkmcnt(0)\n\t"
    "v_readfirstlane_b32 s50, v130\n\t"
    "s_nop 0\n\t"
    "s_cmp_lt_u32 s50, 1\n\t"
    "s_cbranch_scc1 L_asvsp12_%=\n\t"
    "v_and_or_b32 v100, v84, s44, v69\n\t"
    "ds_read_b32 v100, v100\n\t"
    "v_and_or_b32 v101, v85, s44, v69\n\t"
    "ds_read_b32 v101, v101\n\t"
    "v_and_or_b32 v102, v86, s44, v69\n\t"
    "ds_read_b32 v102, v102\n\t"
    "v_and_or_b32 v103, v87, s44, v69\n\t"
    "ds_read_b32 v103, v103\n\t"
    "v_and_or_b32 v104, v88, s44, v69\n\t"
    "ds_read_b32 v104, v104\n\t"
    "v_and_or_b32 v105, v89, s44, v69\n\t"
    "ds_read_b32 v105, v105\n\t"
    "v_and_or_b32 v106, v90, s44, v69\n\t"
    "ds_read_b32 v106, v106\n\t"
    "v_and_or_b32 v107, v91, s44, v69\n\t"
    "ds_read_b32 v107, v107\n\t"
    "s_waitcnt vmcnt(35)\n\t"
    "v_lshl_add_u32 v76, v48, 3, s36\n\t"
    "v_lshl_add_u32 v77, v49, 3, s37\n\t"
    "v_lshl_add_u32 v78, v50, 3, s38\n\t"
    "v_lshl_add_u32 v79, v51, 3, s39\n\t"
    "v_lshl_add_u32 v80, v52, 3, s40\n\t"
    "v_lshl_add_u32 v81, v53, 3, s41\n\t"
    "v_lshl_add_u32 v82, v54, 3, s42\n\t"
    "v_lshl_add_u32 v83, v55, 3, s43\n\t"
    "s_lshl_b32 s52, %14, 2\n\t"
    "s_add_u32 s52, s52, 48242688\n\t"
    "buffer_load_dword v71, off, %12, s52 offset:1024 sc1\n\t"
    "buffer_load_dwordx4 v[24:27], v137, s[96:99], s47 offen offset:0\n\t"
    "buffer_load_dwordx4 v[28:31], v138, s[96:99], s47 offen offset:0\n\t"
    "buffer_load_dwordx4 v[32:35], v137, s[96:99], s47 offen offset:64\n\t"
    "buffer_load_dwordx4 v[36:39], v138, s[96:99], s47 offen offset:64\n\t"
    "s_add_u32 s47, s47, 0x100000\n\t"
    "buffer_load_dwordx4 v[40:43], v137, s[96:99], s47 offen offset:0\n\t"
    "buffer_load_dwordx4 v[44:47], v138, s[96:99], s47 offen offset:0\n\t"
    "buffer_load_dwordx4 v[48:51], v137, s[96:99], s47 offen offset:64\n\t"
    "buffer_load_dwordx4 v[52:55], v138, s[96:99], s47 offen offset:64\n\t"
    "s_add_u32 s47, s47, 0x100000\n\t"
    "v_and_b32 v123, v123, v115\n\t"
    "v_perm_b32 v123, v123, v123, s46\n\t"
    "ds_or_b32 v97, v123\n\t"
    "v_and_b32 v124, v124, v116\n\t"
    "v_perm_b32 v124, v124, v124, s46\n\t"
    "ds_or_b32 v98, v124\n\t"
    "v_and_b32 v125, v125, v117\n\t"
    "v_perm_b32 v125, v125, v125, s46\n\t"
    "ds_or_b32 v99, v125\n\t"
    "buffer_store_dwordx4 v[72:75], v66, %12, s49 offen offset:256 sc1\n\t"
    "v_mov_b32 v129, 0xffff0000\n\t"
    "s_waitcnt lgkmcnt(7)\n\t"
    "v_perm_b32 v100, v100, v84, v126\n\t"
    "v_perm_b32 v101, v101, v85, v126\n\t"
    "v_perm_b32 v102, v102, v86, v126\n\t"
    "v_perm_b32 v103, v103, v87, v126\n\t"
    "s_waitcnt lgkmcnt(3)\n\t"
    "v_perm_b32 v104, v104, v88, v126\n\t"
    "v_perm_b32 v105, v105, v89, v126\n\t"
    "v_perm_b32 v106, v106, v90, v126\n\t"
    "v_perm_b32 v107, v107, v91, v126\n\t"
    "s_waitcnt vmcnt(34)\n\t"
    "ds_write_b128 v141, v[156:159] offset:0\n\t"
    "ds_write_b128 v141, v[160:163] offset:256\n\t"
    "ds_read_b64_tr_b16 v[156:157], v142 offset:0\n\t"
    "ds_read_b64_tr_b16 v[158:159], v142 offset:256\n\t"
    "ds_read_b64_tr_b16 v[160:161], v142 offset:32\n\t"
    "ds_read_b64_tr_b16 v[162:163], v142 offset:288\n\t"
    "s_waitcnt vmcnt(32)\n\t"
    "ds_write_b128 v141, v[164:167] offset:2112\n\t"
    "ds_write_b128 v141, v[168:171] offset:2368\n\t"
    "ds_read_b64_tr_b16 v[164:165], v142 offset:2112\n\t"
    "ds_read_b64_tr_b16 v[166:167], v142 offset:2368\n\t"
    "ds_read_b64_tr_b16 v[168:169], v142 offset:2144\n\t"
    "ds_read_b64_tr_b16 v[170:171], v142 offset:2400\n\t"
    "v_max_u32 v108, v100, v101\n\t"
    "v_min_u32 v101, v100, v101\n\t"
    "v_max_u32 v100, v102, v103\n\t"
    "v_min_u32 v103, v102, v103\n\t"
    "v_max_u32 v102, v104, v105\n\t"
    "v_min_u32 v105, v104, v105\n\t"
    "v_max_u32 v104, v106, v107\n\t"
    "v_min_u32 v107, v106, v107\n\t"
    "v_max_u32 v106, v108, v100\n\t"
    "v_min_u32 v100, v108, v100\n\t"
    "v_max_u32 v108, v101, v103\n\t"
    "v_min_u32 v103, v101, v103\n\t"
    "v_max_u32 v101, v102, v104\n\t"
    "v_min_u32 v104, v102, v104\n\t"
    "v_max_u32 v102, v105, v107\n\t"
    "v_min_u32 v107, v105, v107\n\t"
    "v_bfi_b32 v130, v106, 0, v129\n\t"
    "v_cmp_eq_u32 vcc, 0, v130\n\t"
    "v_and_or_b32 v92, v106, s44, v69\n\t"
    "s_nop 0\n\t"
    "v_cndmask_b32 v130, v130, v129, vcc\n\t"
    "v_ffbl_b32 v131, v130\n\t"
    "v_and_b32 v134, 7, v131\n\t"
    "v_lshlrev_b32 v134, v134, s45\n\t"
    "v_xor_b32 v129, v129, v134\n\t"
    "v_xor_b32 v132, v131, v127\n\t"
    "v_bfi_b32 v130, v101, 0, v129\n\t"
    "v_cmp_eq_u32 vcc, 0, v130\n\t"
    "v_and_or_b32 v93, v101, s44, v69\n\t"
    "v_xor_b32 v133, 23, v132\n\t"
    "v_lshlrev_b32 v110, v132, v136\n\t"
    "v_and_b32 v132, 7, v132\n\t"
    "v_lshl_or_b32 v110, v136, v133, v110\n\t"
    "v_lshl_add_u32 v132, v132, 1, v128\n\t"
    "ds_or_rtn_b32 v118, v92, v110\n\t"
    "ds_write_b16 v132, v106\n\t"
    "v_cndmask_b32 v130, v130, v129, vcc\n\t"
    "v_ffbl_b32 v131, v130\n\t"
    "v_and_b32 v134, 7, v131\n\t"
    "v_lshlrev_b32 v134, v134, s45\n\t"
    "v_xor_b32 v129, v129, v134\n\t"
    "v_xor_b32 v132, v131, v127\n\t"
    "v_bfi_b32 v130, v108, 0, v129\n\t"
    "v_cmp_eq_u32 vcc, 0, v130\n\t"
    "v_and_or_b32 v94, v108, s44, v69\n\t"
    "v_xor_b32 v133, 23, v132\n\t"
    "v_lshlrev_b32 v111, v132, v136\n\t"
    "v_and_b32 v132, 7, v132\n\t"
    "v_lshl_or_b32 v111, v136, v133, v111\n\t"
    "v_lshl_add_u32 v132, v132, 1, v128\n\t"
    "ds_or_rtn_b32 v119, v93, v111\n\t"
    "ds_write_b16 v132, v101\n\t"
    "v_cndmask_b32 v130, v130, v129, vcc\n\t"
    "v_ffbl_b32 v131, v130\n\t"
    "v_and_b32 v134, 7, v131\n\t"
    "v_lshlrev_b32 v134, v134, s45\n\t"
    "v_xor_b32 v129, v129, v134\n\t"
    "v_xor_b32 v132, v131, v127\n\t"
    "s_waitcnt lgkmcnt(10)\n\t"
    "buffer_store_dwordx4 v[156:159], v139, %12, s48 offen sc1\n\t"
    "buffer_store_dwordx4 v[160:163], v140, %12, s48 offen sc1\n\t"
    "s_add_u32 s48, s48, 819200\n\t"
    "s_waitcnt lgkmcnt(4)\n\t"
    "buffer_store_dwordx4 v[164:167], v139, %12, s48 offen sc1\n\t"
    "buffer_store_dwordx4 v[168:171], v140, %12, s48 offen sc1\n\t"
    "s_add_u32 s48, s48, -811008\n\t"
    "s_waitcnt vmcnt(34)\n\t"
    "ds_write_b128 v141, v[172:175] offset:0\n\t"
    "ds_write_b128 v141, v[176:179] offset:256\n\t"
    "ds_read_b64_tr_b16 v[172:173], v142 offset:0\n\t"
    "ds_read_b64_tr_b16 v[174:175], v142 offset:256\n\t"
    "ds_read_b64_tr_b16 v[176:177], v142 offset:32\n\t"
    "ds_read_b64_tr_b16 v[178:179], v142 offset:288\n\t"
    "s_waitcnt vmcnt(32)\n\t"
    "ds_write_b128 v141, v[180:183] offset:2112\n\t"
    "ds_write_b128 v141, v[184:187] offset:2368\n\t"
    "ds_read_b64_tr_b16 v[180:181], v142 offset:2112\n\t"
    "ds_read_b64_tr_b16 v[182:183], v142 offset:2368\n\t"
    "ds_read_b64_tr_b16 v[184:185], v142 offset:2144\n\t"
    "ds_read_b64_tr_b16 v[186:187], v142 offset:2400\n\t"
    "v_bfi_b32 v130, v100, 0, v129\n\t"
    "v_cmp_eq_u32 vcc, 0, v130\n\t"
    "v_and_or_b32 v95, v100, s44, v69\n\t"
    "v_xor_b32 v133, 23, v132\n\t"
    "v_lshlrev_b32 v112, v132, v136\n\t"
    "v_and_b32 v132, 7, v132\n\t"
    "v_lshl_or_b32 v112, v136, v133, v112\n\t"
    "v_lshl_add_u32 v132, v132, 1, v128\n\t"
    "ds_or_rtn_b32 v120, v94, v112\n\t"
    "ds_write_b16 v132, v108\n\t"
    "v_cndmask_b32 v130, v130, v129, vcc\n\t"
    "v_ffbl_b32 v131, v130\n\t"
    "v_and_b32 v134, 7, v131\n\t"
    "v_lshlrev_b32 v134, v134, s45\n\t"
    "v_xor_b32 v129, v129, v134\n\t"
    "v_xor_b32 v132, v131, v127\n\t"
    "v_bfi_b32 v130, v102, 0, v129\n\t"
    "v_cmp_eq_u32 vcc, 0, v130\n\t"
    "v_and_or_b32 v96, v102, s44, v69\n\t"
    "v_xor_b32 v133, 23, v132\n\t"
    "v_lshlrev_b32 v113, v132, v136\n\t"
    "v_and_b32 v132, 7, v132\n\t"
    "v_lshl_or_b32 v113, v136, v133, v113\n\t"
    "v_lshl_add_u32 v132, v132, 1, v128\n\t"
    "ds_or_rtn_b32 v121, v95, v113\n\t"
    "ds_write_b16 v132, v100\n\t"
    "v_cndmask_b32 v130, v130, v129, vcc\n\t"
    "v_ffbl_b32 v131, v130\n\t"
    "v_and_b32 v134, 7, v131\n\t"
    "v_lshlrev_b32 v134, v134, s45\n\t"
    "v_xor_b32 v129, v129, v134\n\t"
    "v_xor_b32 v132, v131, v127\n\t"
    "s_waitcnt lgkmcnt(15)\n\t"
    "v_and_b32 v118, v118, v110\n\t"
    "v_perm_b32 v118, v118, v118, s46\n\t"
    "ds_or_b32 v92, v118\n\t"
    "v_bfi_b32 v130, v104, 0, v129\n\t"
    "v_cmp_eq_u32 vcc, 0, v130\n\t"
    "v_and_or_b32 v97, v104, s44, v69\n\t"
    "v_xor_b32 v133, 23, v132\n\t"
    "v_lshlrev_b32 v114, v132, v136\n\t"
    "v_and_b32 v132, 7, v132\n\t"
    "v_lshl_or_b32 v114, v136, v133, v114\n\t"
    "v_lshl_add_u32 v132, v132, 1, v128\n\t"
    "ds_or_rtn_b32 v122, v96, v114\n\t"
    "ds_write_b16 v132, v102\n\t"
    "v_cndmask_b32 v130, v130, v129, vcc\n\t"
    "v_ffbl_b32 v131, v130\n\t"
    "v_and_b32 v134, 7, v131\n\t"
    "v_lshlrev_b32 v134, v134, s45\n\t"
    "v_xor_b32 v129, v129, v134\n\t"
    "v_xor_b32 v132, v131, v127\n\t"
    "v_and_b32 v119, v119, v111\n\t"
    "v_perm_b32 v119, v119, v119, s46\n\t"
    "ds_or_b32 v93, v119\n\t"
    "s_waitcnt lgkmcnt(14)\n\t"
    "buffer_store_dwordx4 v[172:175], v139, %12, s48 offen sc1\n\t"
    "buffer_store_dwordx4 v[176:179], v140, %12, s48 offen sc1\n\t"
    "s_add_u32 s48, s48, 819200\n\t"
    "s_waitcnt lgkmcnt(8)\n\t"
    "buffer_store_dwordx4 v[180:183], v139, %12, s48 offen sc1\n\t"
    "buffer_store_dwordx4 v[184:187], v140, %12, s48 offen sc1\n\t"
    "s_add_u32 s48, s48, -811008\n\t"
    "s_waitcnt vmcnt(34)\n\t"
    "ds_write_b128 v141, v[188:191] offset:0\n\t"
    "ds_write_b128 v141, v[192:195] offset:256\n\t"
    "ds_read_b64_tr_b16 v[188:189], v142 offset:0\n\t"
    "ds_read_b64_tr_b16 v[190:191], v142 offset:256\n\t"
    "ds_read_b64_tr_b16 v[192:193], v142 offset:32\n\t"
    "ds_read_b64_tr_b16 v[194:195], v142 offset:288\n\t"
    "s_waitcnt vmcnt(32)\n\t"
    "ds_write_b128 v141, v[196:199] offset:2112\n\t"
    "ds_write_b128 v141, v[200:203] offset:2368\n\t"
    "ds_read_b64_tr_b16 v[196:197], v142 offset:2112\n\t"
    "ds_read_b64_tr_b16 v[198:199], v142 offset:2368\n\t"
    "ds_read_b64_tr_b16 v[200:201], v142 offset:2144\n\t"
    "ds_read_b64_tr_b16 v[202:203], v142 offset:2400\n\t"
    "v_bfi_b32 v130, v103, 0, v129\n\t"
    "v_cmp_eq_u32 vcc, 0, v130\n\t"
    "v_and_or_b32 v98, v103, s44, v69\n\t"
    "v_xor_b32 v133, 23, v132\n\t"
    "v_lshlrev_b32 v115, v132, v136\n\t"
    "v_and_b32 v132, 7, v132\n\t"
    "v_lshl_or_b32 v115, v136, v133, v115\n\t"
    "v_lshl_add_u32 v132, v132, 1, v128\n\t"
    "ds_or_rtn_b32 v123, v97, v115\n\t"
    "ds_write_b16 v132, v104\n\t"
    "v_cndmask_b32 v130, v130, v129, vcc\n\t"
    "v_ffbl_b32 v131, v130\n\t"
    "v_and_b32 v134, 7, v131\n\t"
    "v_lshlrev_b32 v134, v134, s45\n\t"
    "v_xor_b32 v129, v129, v134\n\t"
    "v_xor_b32 v132, v131, v127\n\t"
    "s_waitcnt lgkmcnt(15)\n\t"
    "v_and_b32 v120, v120, v112\n\t"
    "v_perm_b32 v120, v120, v120, s46\n\t"
    "ds_or_b32 v94, v120\n\t"
    "v_bfi_b32 v130, v107, 0, v129\n\t"
    "v_cmp_eq_u32 vcc, 0, v130\n\t"
    "v_and_or_b32 v99, v107, s44, v69\n\t"
    "v_xor_b32 v133, 23, v132\n\t"
    "v_lshlrev_b32 v116, v132, v136\n\t"
    "v_and_b32 v132, 7, v132\n\t"
    "v_lshl_or_b32 v116, v136, v133, v116\n\t"
    "v_lshl_add_u32 v132, v132, 1, v128\n\t"
    "ds_or_rtn_b32 v124, v98, v116\n\t"
    "ds_write_b16 v132, v103\n\t"
    "v_cndmask_b32 v130, v130, v129, vcc\n\t"
    "v_ffbl_b32 v131, v130\n\t"
    "v_xor_b32 v132, v131, v127\n\t"
    "v_and_b32 v121, v121, v113\n\t"
    "v_perm_b32 v121, v121, v121, s46\n\t"
    "ds_or_b32 v95, v121\n\t"
    "v_xor_b32 v133, 23, v132\n\t"
    "v_lshlrev_b32 v117, v132, v136\n\t"
    "v_and_b32 v132, 7, v132\n\t"
    "v_lshl_or_b32 v117, v136, v133, v117\n\t"
    "v_lshl_add_u32 v132, v132, 1, v128\n\t"
    "ds_or_rtn_b32 v125, v99, v117\n\t"
    "ds_write_b16 v132, v107\n\t"
    "v_and_b32 v122, v122, v114\n\t"
    "v_perm_b32 v122, v122, v122, s46\n\t"
    "ds_or_b32 v96, v122\n\t"
    "ds_read_b128 v[72:75], v128\n\t"
    "s_waitcnt lgkmcnt(15)\n\t"
    "buffer_store_dwordx4 v[188:191], v139, %12, s48 offen sc1\n\t"
    "buffer_store_dwordx4 v[192:195], v140, %12, s48 offen sc1\n\t"
    "s_add_u32 s48, s48, 819200\n\t"
    "s_waitcnt lgkmcnt(10)\n\t"
    "buffer_store_dwordx4 v[196:199], v139, %12, s48 offen sc1\n\t"
    "buffer_store_dwordx4 v[200:203], v140, %12, s48 offen sc1\n\t"
    "s_add_u32 s48, s48, -811008\n\t"
    "v_and_or_b32 v100, v76, s44, v69\n\t"
    "ds_read_b32 v100, v100\n\t"
    "v_and_or_b32 v101, v77, s44, v69\n\t"
    "ds_read_b32 v101, v101\n\t"
    "v_and_or_b32 v102, v78, s44, v69\n\t"
    "ds_read_b32 v102, v102\n\t"
    "v_and_or_b32 v103, v79, s44, v69\n\t"
    "ds_read_b32 v103, v103\n\t"
    "v_and_or_b32 v104, v80, s44, v69\n\t"
    "ds_read_b32 v104, v104\n\t"
    "v_and_or_b32 v105, v81, s44, v69\n\t"
    "ds_read_b32 v105, v105\n\t"
    "v_and_or_b32 v106, v82, s44, v69\n\t"
    "ds_read_b32 v106, v106\n\t"
    "v_and_or_b32 v107, v83, s44, v69\n\t"
    "ds_read_b32 v107, v107\n\t"
    "v_lshl_add_u32 v84, v56, 3, s36\n\t"
    "v_lshl_add_u32 v85, v57, 3, s37\n\t"
    "v_lshl_add_u32 v86, v58, 3, s38\n\t"
    "v_lshl_add_u32 v87, v59, 3, s39\n\t"
    "v_lshl_add_u32 v88, v60, 3, s40\n\t"
    "v_lshl_add_u32 v89, v61, 3, s41\n\t"
    "v_lshl_add_u32 v90, v62, 3, s42\n\t"
    "v_lshl_add_u32 v91, v63, 3, s43\n\t"
    "v_mov_b32 v134, 2076\n\t"
    "L_asvsp1052_%=:\n\t"
    "ds_read_b32 v130, v134\n\t"
    "s_waitcnt lgkmcnt(0)\n\t"
    "v_readfirstlane_b32 s50, v130\n\t"
    "s_nop 0\n\t"
    "s_cmp_lt_u32 s50, 1\n\t"
    "s_cbranch_scc1 L_asvsp1052_%=\n\t"
    "v_mov_b32 v70, 2072\n\t"
    "ds_read_b32 v70, v70\n\t"
    "s_and_b32 s50, %14, 7\n\t"
    "s_lshr_b32 s51, %14, 5\n\t"
    "s_add_u32 s50, s50, 8\n\t"
    "s_lshl_b32 s50, s50, 7\n\t"
    "s_lshl_b32 s51, s51, 7\n\t"
    "v_mov_b32 v132, s51\n\t"
    "v_writelane_b32 v132, s50, 0\n\t"
    "s_waitcnt vmcnt(21)\n\t"
    "s_waitcnt vmcnt(0)\n\t"
    "s_waitcnt lgkmcnt(0)\n\t"
    "v_cmp_ne_u32 vcc, v71, v70\n\t"
    "s_cbranch_vccz L_ahsok_%=\n\t"
    "s_mov_b32 s51, 1048576\n\t"
    "L_ahs_%=:\n\t"
    "buffer_load_dword v71, off, %12, s52 offset:1024 sc1\n\t"
    "s_sub_u32 s51, s51, 1\n\t"
    "s_waitcnt vmcnt(0)\n\t"
    "s_cmp_eq_u32 s51, 0\n\t"
    "s_cbranch_scc1 L_ahsok_%=\n\t"
    "v_cmp_ne_u32 vcc, v71, v70\n\t"
    "s_cbranch_vccnz L_ahs_%=\n\t"
    "L_ahsok_%=:\n\t"
    "s_mov_b32 s50, 48235520\n\t"
    "s_mov_b64 exec, 3\n\t"
    "buffer_atomic_add v136, v132, %12, s50 offen sc1\n\t"
    "s_mov_b64 exec, -1\n\t"
    "buffer_load_dwordx4 v[156:159], v137, s[96:99], s47 offen offset:0\n\t"
    "buffer_load_dwordx4 v[160:163], v138, s[96:99], s47 offen offset:0\n\t"
    "buffer_load_dwordx4 v[164:167], v137, s[96:99], s47 offen offset:64\n\t"
    "buffer_load_dwordx4 v[168:171], v138, s[96:99], s47 offen offset:64\n\t"
    "s_add_u32 s47, s47, 0x100000\n\t"
    "buffer_load_dwordx4 v[172:175], v137, s[96:99], s47 offen offset:0\n\t"
    "buffer_load_dwordx4 v[176:179], v138, s[96:99], s47 offen offset:0\n\t"
    "buffer_load_dwordx4 v[180:183], v137, s[96:99], s47 offen offset:64\n\t"
    "buffer_load_dwordx4 v[184:187], v138, s[96:99], s47 offen offset:64\n\t"
    "s_add_u32 s47, s47, 0x100000\n\t"
    "buffer_load_dwordx4 v[188:191], v137, s[96:99], s47 offen offset:0\n\t"
    "buffer_load_dwordx4 v[192:195], v138, s[96:99], s47 offen offset:0\n\t"
    "buffer_load_dwordx4 v[196:199], v137, s[96:99], s47 offen offset:64\n\t"
    "buffer_load_dwordx4 v[200:203], v138, s[96:99], s47 offen offset:64\n\t"
    "s_add_u32 s47, s47, 0x100000\n\t"
    "v_and_b32 v123, v123, v115\n\t"
    "v_perm_b32 v123, v123, v123, s46\n\t"
    "ds_or_b32 v97, v123\n\t"
    "v_and_b32 v124, v124, v116\n\t"
    "v_perm_b32 v124, v124, v124, s46\n\t"
    "ds_or_b32 v98, v124\n\t"
    "v_and_b32 v125, v125, v117\n\t"
    "v_perm_b32 v125, v125, v125, s46\n\t"
    "ds_or_b32 v99, v125\n\t"
    "buffer_store_dwordx4 v[72:75], v67, %12, s49 offen offset:320 sc1\n\t"
    "v_mov_b32 v129, 0xffff0000\n\t"
    "v_perm_b32 v100, v100, v76, v126\n\t"
    "v_perm_b32 v101, v101, v77, v126\n\t"
    "v_perm_b32 v102, v102, v78, v126\n\t"
    "v_perm_b32 v103, v103, v79, v126\n\t"
    "v_perm_b32 v104, v104, v80, v126\n\t"
    "v_perm_b32 v105, v105, v81, v126\n\t"
    "v_perm_b32 v106, v106, v82, v126\n\t"
    "v_perm_b32 v107, v107, v83, v126\n\t"
    "ds_write_b128 v141, v[204:207] offset:0\n\t"
    "ds_write_b128 v141, v[208:211] offset:256\n\t"
    "ds_read_b64_tr_b16 v[204:205], v142 offset:0\n\t"
    "ds_read_b64_tr_b16 v[206:207], v142 offset:256\n\t"
    "ds_read_b64_tr_b16 v[208:209], v142 offset:32\n\t"
    "ds_read_b64_tr_b16 v[210:211], v142 offset:288\n\t"
    "ds_write_b128 v141, v[212:215] offset:2112\n\t"
    "ds_write_b128 v141, v[216:219] offset:2368\n\t"
    "ds_read_b64_tr_b16 v[212:213], v142 offset:2112\n\t"
    "ds_read_b64_tr_b16 v[214:215], v142 offset:2368\n\t"
    "ds_read_b64_tr_b16 v[216:217], v142 offset:2144\n\t"
    "ds_read_b64_tr_b16 v[218:219], v142 offset:2400\n\t"
    "v_max_u32 v108, v100, v101\n\t"
    "v_min_u32 v101, v100, v101\n\t"
    "v_max_u32 v100, v102, v103\n\t"
    "v_min_u32 v103, v102, v103\n\t"
    "v_max_u32 v102, v104, v105\n\t"
    "v_min_u32 v105, v104, v105\n\t"
    "v_max_u32 v104, v106, v107\n\t"
    "v_min_u32 v107, v106, v107\n\t"
    "v_max_u32 v106, v108, v100\n\t"
    "v_min_u32 v100, v108, v100\n\t"
    "v_max_u32 v108, v101, v103\n\t"
    "v_min_u32 v103, v101, v103\n\t"
    "v_max_u32 v101, v102, v104\n\t"
    "v_min_u32 v104, v102, v104\n\t"
    "v_max_u32 v102, v105, v107\n\t"
    "v_min_u32 v107, v105, v107\n\t"
    "v_bfi_b32 v130, v106, 0, v129\n\t"
    "v_cmp_eq_u32 vcc, 0, v130\n\t"
    "v_and_or_b32 v92, v106, s44, v69\n\t"
    "s_nop 0\n\t"
    "v_cndmask_b32 v130, v130, v129, vcc\n\t"
    "v_ffbl_b32 v131, v130\n\t"
    "v_and_b32 v134, 7, v131\n\t"
    "v_lshlrev_b32 v134, v134, s45\n\t"
    "v_xor_b32 v129, v129, v134\n\t"
    "v_xor_b32 v132, v131, v127\n\t"
    "v_bfi_b32 v130, v101, 0, v129\n\t"
    "v_cmp_eq_u32 vcc, 0, v130\n\t"
    "v_and_or_b32 v93, v101, s44, v69\n\t"
    "v_xor_b32 v133, 23, v132\n\t"
    "v_lshlrev_b32 v110, v132, v136\n\t"
    "v_and_b32 v132, 7, v132\n\t"
    "v_lshl_or_b32 v110, v136, v133, v110\n\t"
    "v_lshl_add_u32 v132, v132, 1, v128\n\t"
    "ds_or_rtn_b32 v118, v92, v110\n\t"
    "ds_write_b16 v132, v106\n\t"
    "v_cndmask_b32 v130, v130, v129, vcc\n\t"
    "v_ffbl_b32 v131, v130\n\t"
    "v_and_b32 v134, 7, v131\n\t"
    "v_lshlrev_b32 v134, v134, s45\n\t"
    "v_xor_b32 v129, v129, v134\n\t"
    "v_xor_b32 v132, v131, v127\n\t"
    "v_bfi_b32 v130, v108, 0, v129\n\t"
    "v_cmp_eq_u32 vcc, 0, v130\n\t"
    "v_and_or_b32 v94, v108, s44, v69\n\t"
    "v_xor_b32 v133, 23, v132\n\t"
    "v_lshlrev_b32 v111, v132, v136\n\t"
    "v_and_b32 v132, 7, v132\n\t"
    "v_lshl_or_b32 v111, v136, v133, v111\n\t"
    "v_lshl_add_u32 v132, v132, 1, v128\n\t"
    "ds_or_rtn_b32 v119, v93, v111\n\t"
    "ds_write_b16 v132, v101\n\t"
    "v_cndmask_b32 v130, v130, v129, vcc\n\t"
    "v_ffbl_b32 v131, v130\n\t"
    "v_and_b32 v134, 7, v131\n\t"
    "v_lshlrev_b32 v134, v134, s45\n\t"
    "v_xor_b32 v129, v129, v134\n\t"
    "v_xor_b32 v132, v131, v127\n\t"
    "s_waitcnt lgkmcnt(10)\n\t"
    "buffer_store_dwordx4 v[204:207], v139, %12, s48 offen sc1\n\t"
    "buffer_store_dwordx4 v[208:211], v140, %12, s48 offen sc1\n\t"
    "s_add_u32 s48, s48, 819200\n\t"
    "s_waitcnt lgkmcnt(4)\n\t"
    "buffer_store_dwordx4 v[212:215], v139, %12, s48 offen sc1\n\t"
    "buffer_store_dwordx4 v[216:219], v140, %12, s48 offen sc1\n\t"
    "s_add_u32 s48, s48, -811008\n\t"
    "ds_write_b128 v141, v[220:223] offset:0\n\t"
    "ds_write_b128 v141, v[224:227] offset:256\n\t"
    "ds_read_b64_tr_b16 v[220:221], v142 offset:0\n\t"
    "ds_read_b64_tr_b16 v[222:223], v142 offset:256\n\t"
    "ds_read_b64_tr_b16 v[224:225], v142 offset:32\n\t"
    "ds_read_b64_tr_b16 v[226:227], v142 offset:288\n\t"
    "ds_write_b128 v141, v[228:231] offset:2112\n\t"
    "ds_write_b128 v141, v[232:235] offset:2368\n\t"
    "ds_read_b64_tr_b16 v[228:229], v142 offset:2112\n\t"
    "ds_read_b64_tr_b16 v[230:231], v142 offset:2368\n\t"
    "ds_read_b64_tr_b16 v[232:233], v142 offset:2144\n\t"
    "ds_read_b64_tr_b16 v[234:235], v142 offset:2400\n\t"
    "v_bfi_b32 v130, v100, 0, v129\n\t"
    "v_cmp_eq_u32 vcc, 0, v130\n\t"
    "v_and_or_b32 v95, v100, s44, v69\n\t"
    "v_xor_b32 v133, 23, v132\n\t"
    "v_lshlrev_b32 v112, v132, v136\n\t"
    "v_and_b32 v132, 7, v132\n\t"
    "v_lshl_or_b32 v112, v136, v133, v112\n\t"
    "v_lshl_add_u32 v132, v132, 1, v128\n\t"
    "ds_or_rtn_b32 v120, v94, v112\n\t"
    "ds_write_b16 v132, v108\n\t"
    "v_cndmask_b32 v130, v130, v129, vcc\n\t"
    "v_ffbl_b32 v131, v130\n\t"
    "v_and_b32 v134, 7, v131\n\t"
    "v_lshlrev_b32 v134, v134, s45\n\t"
    "v_xor_b32 v129, v129, v134\n\t"
    "v_xor_b32 v132, v131, v127\n\t"
    "v_bfi_b32 v130, v102, 0, v129\n\t"
    "v_cmp_eq_u32 vcc, 0, v130\n\t"
    "v_and_or_b32 v96, v102, s44, v69\n\t"
    "v_xor_b32 v133, 23, v132\n\t"
    "v_lshlrev_b32 v113, v132, v136\n\t"
    "v_and_b32 v132, 7, v132\n\t"
    "v_lshl_or_b32 v113, v136, v133, v113\n\t"
    "v_lshl_add_u32 v132, v132, 1, v128\n\t"
    "ds_or_rtn_b32 v121, v95, v113\n\t"
    "ds_write_b16 v132, v100\n\t"
    "v_cndmask_b32 v130, v130, v129, vcc\n\t"
    "v_ffbl_b32 v131, v130\n\t"
    "v_and_b32 v134, 7, v131\n\t"
    "v_lshlrev_b32 v134, v134, s45\n\t"
    "v_xor_b32 v129, v129, v134\n\t"
    "v_xor_b32 v132, v131, v127\n\t"
    "s_waitcnt lgkmcnt(15)\n\t"
    "v_and_b32 v118, v118, v110\n\t"
    "v_perm_b32 v118, v118, v118, s46\n\t"
    "ds_or_b32 v92, v118\n\t"
    "v_bfi_b32 v130, v104, 0, v129\n\t"
    "v_cmp_eq_u32 vcc, 0, v130\n\t"
    "v_and_or_b32 v97, v104, s44, v69\n\t"
    "v_xor_b32 v133, 23, v132\n\t"
    "v_lshlrev_b32 v114, v132, v136\n\t"
    "v_and_b32 v132, 7, v132\n\t"
    "v_lshl_or_b32 v114, v136, v133, v114\n\t"
    "v_lshl_add_u32 v132, v132, 1, v128\n\t"
    "ds_or_rtn_b32 v122, v96, v114\n\t"
    "ds_write_b16 v132, v102\n\t"
    "v_cndmask_b32 v130, v130, v129, vcc\n\t"
    "v_ffbl_b32 v131, v130\n\t"
    "v_and_b32 v134, 7, v131\n\t"
    "v_lshlrev_b32 v134, v134, s45\n\t"
    "v_xor_b32 v129, v129, v134\n\t"
    "v_xor_b32 v132, v131, v127\n\t"
    "v_and_b32 v119, v119, v111\n\t"
    "v_perm_b32 v119, v119, v119, s46\n\t"
    "ds_or_b32 v93, v119\n\t"
    "s_waitcnt lgkmcnt(14)\n\t"
    "buffer_store_dwordx4 v[220:223], v139, %12, s48 offen sc1\n\t"
    "buffer_store_dwordx4 v[224:227], v140, %12, s48 offen sc1\n\t"
    "s_add_u32 s48, s48, 819200\n\t"
    "s_waitcnt lgkmcnt(8)\n\t"
    "buffer_store_dwordx4 v[228:231], v139, %12, s48 offen sc1\n\t"
    "buffer_store_dwordx4 v[232:235], v140, %12, s48 offen sc1\n\t"
    "s_add_u32 s48, s48, -811008\n\t"
    "ds_write_b128 v141, v[236:239] offset:0\n\t"
    "ds_write_b128 v141, v[240:243] offset:256\n\t"
    "ds_read_b64_tr_b16 v[236:237], v142 offset:0\n\t"
    "ds_read_b64_tr_b16 v[238:239], v142 offset:256\n\t"
    "ds_read_b64_tr_b16 v[240:241], v142 offset:32\n\t"
    "ds_read_b64_tr_b16 v[242:243], v142 offset:288\n\t"
    "ds_write_b128 v141, v[244:247] offset:2112\n\t"
    "ds_write_b128 v141, v[248:251] offset:2368\n\t"
    "ds_read_b64_tr_b16 v[244:245], v142 offset:2112\n\t"
    "ds_read_b64_tr_b16 v[246:247], v142 offset:2368\n\t"
    "ds_read_b64_tr_b16 v[248:249], v142 offset:2144\n\t"
    "ds_read_b64_tr_b16 v[250:251], v142 offset:2400\n\t"
    "v_bfi_b32 v130, v103, 0, v129\n\t"
    "v_cmp_eq_u32 vcc, 0, v130\n\t"
    "v_and_or_b32 v98, v103, s44, v69\n\t"
    "v_xor_b32 v133, 23, v132\n\t"
    "v_lshlrev_b32 v115, v132, v136\n\t"
    "v_and_b32 v132, 7, v132\n\t"
    "v_lshl_or_b32 v115, v136, v133, v115\n\t"
    "v_lshl_add_u32 v132, v132, 1, v128\n\t"
    "ds_or_rtn_b32 v123, v97, v115\n\t"
    "ds_write_b16 v132, v104\n\t"
    "v_cndmask_b32 v130, v130, v129, vcc\n\t"
    "v_ffbl_b32 v131, v130\n\t"
    "v_and_b32 v134, 7, v131\n\t"
    "v_lshlrev_b32 v134, v134, s45\n\t"
    "v_xor_b32 v129, v129, v134\n\t"
    "v_xor_b32 v132, v131, v127\n\t"
    "s_waitcnt lgkmcnt(15)\n\t"
    "v_and_b32 v120, v120, v112\n\t"
    "v_perm_b32 v120, v120, v120, s46\n\t"
    "ds_or_b32 v94, v120\n\t"
    "v_bfi_b32 v130, v107, 0, v129\n\t"
    "v_cmp_eq_u32 vcc, 0, v130\n\t"
    "v_and_or_b32 v99, v107, s44, v69\n\t"
    "v_xor_b32 v133, 23, v132\n\t"
    "v_lshlrev_b32 v116, v132, v136\n\t"
    "v_and_b32 v132, 7, v132\n\t"
    "v_lshl_or_b32 v116, v136, v133, v116\n\t"
    "v_lshl_add_u32 v132, v132, 1, v128\n\t"
    "ds_or_rtn_b32 v124, v98, v116\n\t"
    "ds_write_b16 v132, v103\n\t"
    "v_cndmask_b32 v130, v130, v129, vcc\n\t"
    "v_ffbl_b32 v131, v130\n\t"
    "v_xor_b32 v132, v131, v127\n\t"
    "v_and_b32 v121, v121, v113\n\t"
    "v_perm_b32 v121, v121, v121, s46\n\t"
    "ds_or_b32 v95, v121\n\t"
    "v_xor_b32 v133, 23, v132\n\t"
    "v_lshlrev_b32 v117, v132, v136\n\t"
    "v_and_b32 v132, 7, v132\n\t"
    "v_lshl_or_b32 v117, v136, v133, v117\n\t"
    "v_lshl_add_u32 v132, v132, 1, v128\n\t"
    "ds_or_rtn_b32 v125, v99, v117\n\t"
    "ds_write_b16 v132, v107\n\t"
    "v_and_b32 v122, v122, v114\n\t"
    "v_perm_b32 v122, v122, v122, s46\n\t"
    "ds_or_b32 v96, v122\n\t"
    "ds_read_b128 v[72:75], v128\n\t"
    "v_and_or_b32 v100, v84, s44, v69\n\t"
    "ds_read_b32 v100, v100\n\t"
    "v_and_or_b32 v101, v85, s44, v69\n\t"
    "ds_read_b32 v101, v101\n\t"
    "v_and_or_b32 v102, v86, s44, v69\n\t"
    "ds_read_b32 v102, v102\n\t"
    "v_and_or_b32 v103, v87, s44, v69\n\t"
    "ds_read_b32 v103, v103\n\t"
    "v_and_or_b32 v104, v88, s44, v69\n\t"
    "ds_read_b32 v104, v104\n\t"
    "v_and_or_b32 v105, v89, s44, v69\n\t"
    "ds_read_b32 v105, v105\n\t"
    "v_and_or_b32 v106, v90, s44, v69\n\t"
    "ds_read_b32 v106, v106\n\t"
    "v_and_or_b32 v107, v91, s44, v69\n\t"
    "ds_read_b32 v107, v107\n\t"
    "v_lshl_add_u32 v76, v0, 3, s36\n\t"
    "v_lshl_add_u32 v77, v1, 3, s37\n\t"
    "v_lshl_add_u32 v78, v2, 3, s38\n\t"
    "v_lshl_add_u32 v79, v3, 3, s39\n\t"
    "v_lshl_add_u32 v80, v4, 3, s40\n\t"
    "v_lshl_add_u32 v81, v5, 3, s41\n\t"
    "v_lshl_add_u32 v82, v6, 3, s42\n\t"
    "v_lshl_add_u32 v83, v7, 3, s43\n\t"
    "s_waitcnt lgkmcnt(15)\n\t"
    "buffer_store_dwordx4 v[236:239], v139, %12, s48 offen sc1\n\t"
    "buffer_store_dwordx4 v[240:243], v140, %12, s48 offen sc1\n\t"
    "s_add_u32 s48, s48, 819200\n\t"
    "buffer_store_dwordx4 v[244:247], v139, %12, s48 offen sc1\n\t"
    "buffer_store_dwordx4 v[248:251], v140, %12, s48 offen sc1\n\t"
    "s_add_u32 s48, s48, -811008\n\t"
    "buffer_load_dwordx4 v[56:59], v137, s[96:99], s47 offen offset:0\n\t"
    "buffer_load_dwordx4 v[60:63], v138, s[96:99], s47 offen offset:0\n\t"
    "buffer_load_dwordx4 v[204:207], v137, s[96:99], s47 offen offset:64\n\t"
    "buffer_load_dwordx4 v[208:211], v138, s[96:99], s47 offen offset:64\n\t"
    "s_add_u32 s47, s47, 0x100000\n\t"
    "buffer_load_dwordx4 v[212:215], v137, s[96:99], s47 offen offset:0\n\t"
    "buffer_load_dwordx4 v[216:219], v138, s[96:99], s47 offen offset:0\n\t"
    "buffer_load_dwordx4 v[220:223], v137, s[96:99], s47 offen offset:64\n\t"
    "buffer_load_dwordx4 v[224:227], v138, s[96:99], s47 offen offset:64\n\t"
    "s_add_u32 s47, s47, 0x100000\n\t"
    "s_waitcnt lgkmcnt(11)\n\t"
    "v_and_b32 v123, v123, v115\n\t"
    "v_perm_b32 v123, v123, v123, s46\n\t"
    "ds_or_b32 v97, v123\n\t"
    "v_and_b32 v124, v124, v116\n\t"
    "v_perm_b32 v124, v124, v124, s46\n\t"
    "ds_or_b32 v98, v124\n\t"
    "v_and_b32 v125, v125, v117\n\t"
    "v_perm_b32 v125, v125, v125, s46\n\t"
    "ds_or_b32 v99, v125\n\t"
    "s_waitcnt lgkmcnt(11)\n\t"
    "buffer_store_dwordx4 v[72:75], v67, %12, s49 offen offset:352 sc1\n\t"
    "v_mov_b32 v129, 0xffff0000\n\t"
    "s_waitcnt lgkmcnt(7)\n\t"
    "v_perm_b32 v100, v100, v84, v126\n\t"
    "v_perm_b32 v101, v101, v85, v126\n\t"
    "v_perm_b32 v102, v102, v86, v126\n\t"
    "v_perm_b32 v103, v103, v87, v126\n\t"
    "s_waitcnt lgkmcnt(3)\n\t"
    "v_perm_b32 v104, v104, v88, v126\n\t"
    "v_perm_b32 v105, v105, v89, v126\n\t"
    "v_perm_b32 v106, v106, v90, v126\n\t"
    "v_perm_b32 v107, v107, v91, v126\n\t"
    "ds_write_b128 v141, v[24:27] offset:0\n\t"
    "ds_write_b128 v141, v[28:31] offset:256\n\t"
    "ds_read_b64_tr_b16 v[24:25], v142 offset:0\n\t"
    "ds_read_b64_tr_b16 v[26:27], v142 offset:256\n\t"
    "ds_read_b64_tr_b16 v[28:29], v142 offset:32\n\t"
    "ds_read_b64_tr_b16 v[30:31], v142 offset:288\n\t"
    "ds_write_b128 v141, v[32:35] offset:2112\n\t"
    "ds_write_b128 v141, v[36:39] offset:2368\n\t"
    "ds_read_b64_tr_b16 v[32:33], v142 offset:2112\n\t"
    "ds_read_b64_tr_b16 v[34:35], v142 offset:2368\n\t"
    "ds_read_b64_tr_b16 v[36:37], v142 offset:2144\n\t"
    "ds_read_b64_tr_b16 v[38:39], v142 offset:2400\n\t"
    "v_max_u32 v108, v100, v101\n\t"
    "v_min_u32 v101, v100, v101\n\t"
    "v_max_u32 v100, v102, v103\n\t"
    "v_min_u32 v103, v102, v103\n\t"
    "v_max_u32 v102, v104, v105\n\t"
    "v_min_u32 v105, v104, v105\n\t"
    "v_max_u32 v104, v106, v107\n\t"
    "v_min_u32 v107, v106, v107\n\t"
    "v_max_u32 v106, v108, v100\n\t"
    "v_min_u32 v100, v108, v100\n\t"
    "v_max_u32 v108, v101, v103\n\t"
    "v_min_u32 v103, v101, v103\n\t"
    "v_max_u32 v101, v102, v104\n\t"
    "v_min_u32 v104, v102, v104\n\t"
    "v_max_u32 v102, v105, v107\n\t"
    "v_min_u32 v107, v105, v107\n\t"
    "v_bfi_b32 v130, v106, 0, v129\n\t"
    "v_cmp_eq_u32 vcc, 0, v130\n\t"
    "v_and_or_b32 v92, v106, s44, v69\n\t"
    "s_nop 0\n\t"
    "v_cndmask_b32 v130, v130, v129, vcc\n\t"
    "v_ffbl_b32 v131, v130\n\t"
    "v_and_b32 v134, 7, v131\n\t"
    "v_lshlrev_b32 v134, v134, s45\n\t"
    "v_xor_b32 v129, v129, v134\n\t"
    "v_xor_b32 v132, v131, v127\n\t"
    "v_bfi_b32 v130, v101, 0, v129\n\t"
    "v_cmp_eq_u32 vcc, 0, v130\n\t"
    "v_and_or_b32 v93, v101, s44, v69\n\t"
    "v_xor_b32 v133, 23, v132\n\t"
    "v_lshlrev_b32 v110, v132, v136\n\t"
    "v_and_b32 v132, 7, v132\n\t"
    "v_lshl_or_b32 v110, v136, v133, v110\n\t"
    "v_lshl_add_u32 v132, v132, 1, v128\n\t"
    "ds_or_b32 v92, v110\n\t"
    "ds_write_b16 v132, v106\n\t"
    "v_cndmask_b32 v130, v130, v129, vcc\n\t"
    "v_ffbl_b32 v131, v130\n\t"
    "v_and_b32 v134, 7, v131\n\t"
    "v_lshlrev_b32 v134, v134, s45\n\t"
    "v_xor_b32 v129, v129, v134\n\t"
    "v_xor_b32 v132, v131, v127\n\t"
    "v_bfi_b32 v130, v108, 0, v129\n\t"
    "v_cmp_eq_u32 vcc, 0, v130\n\t"
    "v_and_or_b32 v94, v108, s44, v69\n\t"
    "v_xor_b32 v133, 23, v132\n\t"
    "v_lshlrev_b32 v111, v132, v136\n\t"
    "v_and_b32 v132, 7, v132\n\t"
    "v_lshl_or_b32 v111, v136, v133, v111\n\t"
    "v_lshl_add_u32 v132, v132, 1, v128\n\t"
    "ds_or_b32 v93, v111\n\t"
    "ds_write_b16 v132, v101\n\t"
    "v_cndmask_b32 v130, v130, v129, vcc\n\t"
    "v_ffbl_b32 v131, v130\n\t"
    "v_and_b32 v134, 7, v131\n\t"
    "v_lshlrev_b32 v134, v134, s45\n\t"
    "v_xor_b32 v129, v129, v134\n\t"
    "v_xor_b32 v132, v131, v127\n\t"
    "s_waitcnt lgkmcnt(10)\n\t"
    "buffer_store_dwordx4 v[24:27], v139, %12, s48 offen sc1\n\t"
    "buffer_store_dwordx4 v[28:31], v140, %12, s48 offen sc1\n\t"
    "s_add_u32 s48, s48, 819200\n\t"
    "s_waitcnt lgkmcnt(4)\n\t"
    "buffer_store_dwordx4 v[32:35], v139, %12, s48 offen sc1\n\t"
    "buffer_store_dwordx4 v[36:39], v140, %12, s48 offen sc1\n\t"
    "s_add_u32 s48, s48, -811008\n\t"
    "ds_write_b128 v141, v[40:43] offset:0\n\t"
    "ds_write_b128 v141, v[44:47] offset:256\n\t"
    "ds_read_b64_tr_b16 v[40:41], v142 offset:0\n\t"
    "ds_read_b64_tr_b16 v[42:43], v142 offset:256\n\t"
    "ds_read_b64_tr_b16 v[44:45], v142 offset:32\n\t"
    "ds_read_b64_tr_b16 v[46:47], v142 offset:288\n\t"
    "ds_write_b128 v141, v[48:51] offset:2112\n\t"
    "ds_write_b128 v141, v[52:55] offset:2368\n\t"
    "ds_read_b64_tr_b16 v[48:49], v142 offset:2112\n\t"
    "ds_read_b64_tr_b16 v[50:51], v142 offset:2368\n\t"
    "ds_read_b64_tr_b16 v[52:53], v142 offset:2144\n\t"
    "ds_read_b64_tr_b16 v[54:55], v142 offset:2400\n\t"
    "v_bfi_b32 v130, v100, 0, v129\n\t"
    "v_cmp_eq_u32 vcc, 0, v130\n\t"
    "v_and_or_b32 v95, v100, s44, v69\n\t"
    "v_xor_b32 v133, 23, v132\n\t"
    "v_lshlrev_b32 v112, v132, v136\n\t"
    "v_and_b32 v132, 7, v132\n\t"
    "v_lshl_or_b32 v112, v136, v133, v112\n\t"
    "v_lshl_add_u32 v132, v132, 1, v128\n\t"
    "ds_or_b32 v94, v112\n\t"
    "ds_write_b16 v132, v108\n\t"
    "v_cndmask_b32 v130, v130, v129, vcc\n\t"
    "v_ffbl_b32 v131, v130\n\t"
    "v_and_b32 v134, 7, v131\n\t"
    "v_lshlrev_b32 v134, v134, s45\n\t"
    "v_xor_b32 v129, v129, v134\n\t"
    "v_xor_b32 v132, v131, v127\n\t"
    "v_bfi_b32 v130, v102, 0, v129\n\t"
    "v_cmp_eq_u32 vcc, 0, v130\n\t"
    "v_and_or_b32 v96, v102, s44, v69\n\t"
    "v_xor_b32 v133, 23, v132\n\t"
    "v_lshlrev_b32 v113, v132, v136\n\t"
    "v_and_b32 v132, 7, v132\n\t"
    "v_lshl_or_b32 v113, v136, v133, v113\n\t"
    "v_lshl_add_u32 v132, v132, 1, v128\n\t"
    "ds_or_b32 v95, v113\n\t"
    "ds_write_b16 v132, v100\n\t"
    "v_cndmask_b32 v130, v130, v129, vcc\n\t"
    "v_ffbl_b32 v131, v130\n\t"
    "v_and_b32 v134, 7, v131\n\t"
    "v_lshlrev_b32 v134, v134, s45\n\t"
    "v_xor_b32 v129, v129, v134\n\t"
    "v_xor_b32 v132, v131, v127\n\t"
    "v_mbcnt_lo_u32_b32 v70, -1, 0\n\t"
    "v_mbcnt_hi_u32_b32 v70, -1, v70\n\t"
    "v_add_u32 v70, 8, v70\n\t"
    "s_and_b32 s50, %14, 7\n\t"
    "s_add_u32 s50, s50, 16\n\t"
    "v_writelane_b32 v70, s50, 8\n\t"
    "v_mov_b32 v148, 32\n\t"
    "v_lshlrev_b32 v70, 7, v70\n\t"
    "s_mov_b32 s52, 48234496\n\t"
    "s_mov_b64 exec, 0x1ff\n\t"
    "buffer_load_dword v71, v70, %12, s52 offen sc1\n\t"
    "s_mov_b64 exec, -1\n\t"
    "s_mov_b32 s54, 0x40000000\n\t"
    "s_mov_b32 s55, 0\n\t"
    "v_bfi_b32 v130, v104, 0, v129\n\t"
    "v_cmp_eq_u32 vcc, 0, v130\n\t"
    "v_and_or_b32 v97, v104, s44, v69\n\t"
    "v_xor_b32 v133, 23, v132\n\t"
    "v_lshlrev_b32 v114, v132, v136\n\t"
    "v_and_b32 v132, 7, v132\n\t"
    "v_lshl_or_b32 v114, v136, v133, v114\n\t"
    "v_lshl_add_u32 v132, v132, 1, v128\n\t"
    "ds_or_b32 v96, v114\n\t"
    "ds_write_b16 v132, v102\n\t"
    "v_cndmask_b32 v130, v130, v129, vcc\n\t"
    "v_ffbl_b32 v131, v130\n\t"
    "v_and_b32 v134, 7, v131\n\t"
    "v_lshlrev_b32 v134, v134, s45\n\t"
    "v_xor_b32 v129, v129, v134\n\t"
    "v_xor_b32 v132, v131, v127\n\t"
    "s_waitcnt lgkmcnt(12)\n\t"
    "buffer_store_dwordx4 v[40:43], v139, %12, s48 offen sc1\n\t"
    "buffer_store_dwordx4 v[44:47], v140, %12, s48 offen sc1\n\t"
    "s_add_u32 s48, s48, 819200\n\t"
    "s_waitcnt lgkmcnt(6)\n\t"
    "buffer_store_dwordx4 v[48:51], v139, %12, s48 offen sc1\n\t"
    "buffer_store_dwordx4 v[52:55], v140, %12, s48 offen sc1\n\t"
    "s_add_u32 s48, s48, -811008\n\t"
    "v_bfi_b32 v130, v103, 0, v129\n\t"
    "v_cmp_eq_u32 vcc, 0, v130\n\t"
    "v_and_or_b32 v98, v103, s44, v69\n\t"
    "v_xor_b32 v133, 23, v132\n\t"
    "v_lshlrev_b32 v115, v132, v136\n\t"
    "v_and_b32 v132, 7, v132\n\t"
    "v_lshl_or_b32 v115, v136, v133, v115\n\t"
    "v_lshl_add_u32 v132, v132, 1, v128\n\t"
    "ds_or_b32 v97, v115\n\t"
    "ds_write_b16 v132, v104\n\t"
    "v_cndmask_b32 v130, v130, v129, vcc\n\t"
    "v_ffbl_b32 v131, v130\n\t"
    "v_and_b32 v134, 7, v131\n\t"
    "v_lshlrev_b32 v134, v134, s45\n\t"
    "v_xor_b32 v129, v129, v134\n\t"
    "v_xor_b32 v132, v131, v127\n\t"
    "v_bfi_b32 v130, v107, 0, v129\n\t"
    "v_cmp_eq_u32 vcc, 0, v130\n\t"
    "v_and_or_b32 v99, v107, s44, v69\n\t"
    "v_xor_b32 v133, 23, v132\n\t"
    "v_lshlrev_b32 v116, v132, v136\n\t"
    "v_and_b32 v132, 7, v132\n\t"
    "v_lshl_or_b32 v116, v136, v133, v116\n\t"
    "v_lshl_add_u32 v132, v132, 1, v128\n\t"
    "ds_or_b32 v98, v116\n\t"
    "ds_write_b16 v132, v103\n\t"
    "v_cndmask_b32 v130, v130, v129, vcc\n\t"
    "v_ffbl_b32 v131, v130\n\t"
    "v_xor_b32 v132, v131, v127\n\t"
    "v_xor_b32 v133, 23, v132\n\t"
    "v_lshlrev_b32 v117, v132, v136\n\t"
    "v_and_b32 v132, 7, v132\n\t"
    "v_lshl_or_b32 v117, v136, v133, v117\n\t"
    "v_lshl_add_u32 v132, v132, 1, v128\n\t"
    "ds_or_b32 v99, v117\n\t"
    "ds_write_b16 v132, v107\n\t"
    "ds_read_b128 v[72:75], v128\n\t"
    "v_and_or_b32 v100, v76, s44, v69\n\t"
    "ds_read_b32 v100, v100\n\t"
    "v_and_or_b32 v101, v77, s44, v69\n\t"
    "ds_read_b32 v101, v101\n\t"
    "v_and_or_b32 v102, v78, s44, v69\n\t"
    "ds_read_b32 v102, v102\n\t"
    "v_and_or_b32 v103, v79, s44, v69\n\t"
    "ds_read_b32 v103, v103\n\t"
    "v_and_or_b32 v104, v80, s44, v69\n\t"
    "ds_read_b32 v104, v104\n\t"
    "v_and_or_b32 v105, v81, s44, v69\n\t"
    "ds_read_b32 v105, v105\n\t"
    "v_and_or_b32 v106, v82, s44, v69\n\t"
    "ds_read_b32 v106, v106\n\t"
    "v_and_or_b32 v107, v83, s44, v69\n\t"
    "ds_read_b32 v107, v107\n\t"
    "v_lshl_add_u32 v84, v8, 3, s36\n\t"
    "v_lshl_add_u32 v85, v9, 3, s37\n\t"
    "v_lshl_add_u32 v86, v10, 3, s38\n\t"
    "v_lshl_add_u32 v87, v11, 3, s39\n\t"
    "v_lshl_add_u32 v88, v12, 3, s40\n\t"
    "v_lshl_add_u32 v89, v13, 3, s41\n\t"
    "v_lshl_add_u32 v90, v14, 3, s42\n\t"
    "v_lshl_add_u32 v91, v15, 3, s43\n\t"
    "s_waitcnt vmcnt(4)\n\t"
    "s_call_b64 s[54:55], L_af1w_%=\n\t"
    "s_mov_b64 s[54:55], 0\n\t"
    "L_af1n16_%=:\n\t"
    "s_waitcnt lgkmcnt(8)\n\t"
    "buffer_store_dwordx4 v[72:75], v67, %12, s49 offen offset:384 sc1\n\t"
    "v_mov_b32 v129, 0xffff0000\n\t"
    "s_waitcnt lgkmcnt(4)\n\t"
    "v_perm_b32 v100, v100, v76, v126\n\t"
    "v_perm_b32 v101, v101, v77, v126\n\t"
    "v_perm_b32 v102, v102, v78, v126\n\t"
    "v_perm_b32 v103, v103, v79, v126\n\t"
    "s_waitcnt lgkmcnt(0)\n\t"
    "v_perm_b32 v104, v104, v80, v126\n\t"
    "v_perm_b32 v105, v105, v81, v126\n\t"
    "v_perm_b32 v106, v106, v82, v126\n\t"
    "v_perm_b32 v107, v107, v83, v126\n\t"
    "ds_write_b128 v141, v[156:159] offset:0\n\t"
    "ds_write_b128 v141, v[160:163] offset:256\n\t"
    "ds_read_b64_tr_b16 v[156:157], v142 offset:0\n\t"
    "ds_read_b64_tr_b16 v[158:159], v142 offset:256\n\t"
    "ds_read_b64_tr_b16 v[160:161], v142 offset:32\n\t"
    "ds_read_b64_tr_b16 v[162:163], v142 offset:288\n\t"
    "ds_write_b128 v141, v[164:167] offset:2112\n\t"
    "ds_write_b128 v141, v[168:171] offset:2368\n\t"
    "ds_read_b64_tr_b16 v[164:165], v142 offset:2112\n\t"
    "ds_read_b64_tr_b16 v[166:167], v142 offset:2368\n\t"
    "ds_read_b64_tr_b16 v[168:169], v142 offset:2144\n\t"
    "ds_read_b64_tr_b16 v[170:171], v142 offset:2400\n\t"
    "v_max_u32 v108, v100, v101\n\t"
    "v_min_u32 v101, v100, v101\n\t"
    "v_max_u32 v100, v102, v103\n\t"
    "v_min_u32 v103, v102, v103\n\t"
    "v_max_u32 v102, v104, v105\n\t"
    "v_min_u32 v105, v104, v105\n\t"
    "v_max_u32 v104, v106, v107\n\t"
    "v_min_u32 v107, v106, v107\n\t"
    "v_max_u32 v106, v108, v100\n\t"
    "v_min_u32 v100, v108, v100\n\t"
    "v_max_u32 v108, v101, v103\n\t"
    "v_min_u32 v103, v101, v103\n\t"
    "v_max_u32 v101, v102, v104\n\t"
    "v_min_u32 v104, v102, v104\n\t"
    "v_max_u32 v102, v105, v107\n\t"
    "v_min_u32 v107, v105, v107\n\t"
    "v_bfi_b32 v130, v106, 0, v129\n\t"
    "v_cmp_eq_u32 vcc, 0, v130\n\t"
    "v_and_or_b32 v92, v106, s44, v69\n\t"
    "s_nop 0\n\t"
    "v_cndmask_b32 v130, v130, v129, vcc\n\t"
    "v_ffbl_b32 v131, v130\n\t"
    "v_and_b32 v134, 7, v131\n\t"
    "v_lshlrev_b32 v134, v134, s45\n\t"
    "v_xor_b32 v129, v129, v134\n\t"
    "v_xor_b32 v132, v131, v127\n\t"
    "v_bfi_b32 v130, v101, 0, v129\n\t"
    "v_cmp_eq_u32 vcc, 0, v130\n\t"
    "v_and_or_b32 v93, v101, s44, v69\n\t"
    "v_xor_b32 v133, 23, v132\n\t"
    "v_lshlrev_b32 v110, v132, v136\n\t"
    "v_and_b32 v132, 7, v132\n\t"
    "v_lshl_or_b32 v110, v136, v133, v110\n\t"
    "v_lshl_add_u32 v132, v132, 1, v128\n\t"
    "ds_or_b32 v92, v110\n\t"
    "ds_write_b16 v132, v106\n\t"
    "v_cndmask_b32 v130, v130, v129, vcc\n\t"
    "v_ffbl_b32 v131, v130\n\t"
    "v_and_b32 v134, 7, v131\n\t"
    "v_lshlrev_b32 v134, v134, s45\n\t"
    "v_xor_b32 v129, v129, v134\n\t"
    "v_xor_b32 v132, v131, v127\n\t"
    "v_bfi_b32 v130, v108, 0, v129\n\t"
    "v_cmp_eq_u32 vcc, 0, v130\n\t"
    "v_and_or_b32 v94, v108, s44, v69\n\t"
    "v_xor_b32 v133, 23, v132\n\t"
    "v_lshlrev_b32 v111, v132, v136\n\t"
    "v_and_b32 v132, 7, v132\n\t"
    "v_lshl_or_b32 v111, v136, v133, v111\n\t"
    "v_lshl_add_u32 v132, v132, 1, v128\n\t"
    "ds_or_b32 v93, v111\n\t"
    "ds_write_b16 v132, v101\n\t"
    "v_cndmask_b32 v130, v130, v129, vcc\n\t"
    "v_ffbl_b32 v131, v130\n\t"
    "v_and_b32 v134, 7, v131\n\t"
    "v_lshlrev_b32 v134, v134, s45\n\t"
    "v_xor_b32 v129, v129, v134\n\t"
    "v_xor_b32 v132, v131, v127\n\t"
    "s_waitcnt lgkmcnt(10)\n\t"
    "buffer_store_dwordx4 v[156:159], v139, %12, s48 offen sc1\n\t"
    "buffer_store_dwordx4 v[160:163], v140, %12, s48 offen sc1\n\t"
    "s_add_u32 s48, s48, 819200\n\t"
    "s_waitcnt lgkmcnt(4)\n\t"
    "buffer_store_dwordx4 v[164:167], v139, %12, s48 offen sc1\n\t"
    "buffer_store_dwordx4 v[168:171], v140, %12, s48 offen sc1\n\t"
    "s_add_u32 s48, s48, -811008\n\t"
    "ds_write_b128 v141, v[172:175] offset:0\n\t"
    "ds_write_b128 v141, v[176:179] offset:256\n\t"
    "ds_read_b64_tr_b16 v[172:173], v142 offset:0\n\t"
    "ds_read_b64_tr_b16 v[174:175], v142 offset:256\n\t"
    "ds_read_b64_tr_b16 v[176:177], v142 offset:32\n\t"
    "ds_read_b64_tr_b16 v[178:179], v142 offset:288\n\t"
    "ds_write_b128 v141, v[180:183] offset:2112\n\t"
    "ds_write_b128 v141, v[184:187] offset:2368\n\t"
    "ds_read_b64_tr_b16 v[180:181], v142 offset:2112\n\t"
    "ds_read_b64_tr_b16 v[182:183], v142 offset:2368\n\t"
    "ds_read_b64_tr_b16 v[184:185], v142 offset:2144\n\t"
    "ds_read_b64_tr_b16 v[186:187], v142 offset:2400\n\t"
    "v_bfi_b32 v130, v100, 0, v129\n\t"
    "v_cmp_eq_u32 vcc, 0, v130\n\t"
    "v_and_or_b32 v95, v100, s44, v69\n\t"
    "v_xor_b32 v133, 23, v132\n\t"
    "v_lshlrev_b32 v112, v132, v136\n\t"
    "v_and_b32 v132, 7, v132\n\t"
    "v_lshl_or_b32 v112, v136, v133, v112\n\t"
    "v_lshl_add_u32 v132, v132, 1, v128\n\t"
    "ds_or_b32 v94, v112\n\t"
    "ds_write_b16 v132, v108\n\t"
    "v_cndmask_b32 v130, v130, v129, vcc\n\t"
    "v_ffbl_b32 v131, v130\n\t"
    "v_and_b32 v134, 7, v131\n\t"
    "v_lshlrev_b32 v134, v134, s45\n\t"
    "v_xor_b32 v129, v129, v134\n\t"
    "v_xor_b32 v132, v131, v127\n\t"
    "v_bfi_b32 v130, v102, 0, v129\n\t"
    "v_cmp_eq_u32 vcc, 0, v130\n\t"
    "v_and_or_b32 v96, v102, s44, v69\n\t"
    "v_xor_b32 v133, 23, v132\n\t"
    "v_lshlrev_b32 v113, v132, v136\n\t"
    "v_and_b32 v132, 7, v132\n\t"
    "v_lshl_or_b32 v113, v136, v133, v113\n\t"
    "v_lshl_add_u32 v132, v132, 1, v128\n\t"
    "ds_or_b32 v95, v113\n\t"
    "ds_write_b16 v132, v100\n\t"
    "v_cndmask_b32 v130, v130, v129, vcc\n\t"
    "v_ffbl_b32 v131, v130\n\t"
    "v_and_b32 v134, 7, v131\n\t"
    "v_lshlrev_b32 v134, v134, s45\n\t"
    "v_xor_b32 v129, v129, v134\n\t"
    "v_xor_b32 v132, v131, v127\n\t"
    "v_bfi_b32 v130, v104, 0, v129\n\t"
    "v_cmp_eq_u32 vcc, 0, v130\n\t"
    "v_and_or_b32 v97, v104, s44, v69\n\t"
    "v_xor_b32 v133, 23, v132\n\t"
    "v_lshlrev_b32 v114, v132, v136\n\t"
    "v_and_b32 v132, 7, v132\n\t"
    "v_lshl_or_b32 v114, v136, v133, v114\n\t"
    "v_lshl_add_u32 v132, v132, 1, v128\n\t"
    "ds_or_b32 v96, v114\n\t"
    "ds_write_b16 v132, v102\n\t"
    "v_cndmask_b32 v130, v130, v129, vcc\n\t"
    "v_ffbl_b32 v131, v130\n\t"
    "v_and_b32 v134, 7, v131\n\t"
    "v_lshlrev_b32 v134, v134, s45\n\t"
    "v_xor_b32 v129, v129, v134\n\t"
    "v_xor_b32 v132, v131, v127\n\t"
    "s_waitcnt lgkmcnt(12)\n\t"
    "buffer_store_dwordx4 v[172:175], v139, %12, s48 offen sc1\n\t"
    "buffer_store_dwordx4 v[176:179], v140, %12, s48 offen sc1\n\t"
    "s_add_u32 s48, s48, 819200\n\t"
    "s_waitcnt lgkmcnt(6)\n\t"
    "buffer_store_dwordx4 v[180:183], v139, %12, s48 offen sc1\n\t"
    "buffer_store_dwordx4 v[184:187], v140, %12, s48 offen sc1\n\t"
    "s_add_u32 s48, s48, -811008\n\t"
    "ds_write_b128 v141, v[188:191] offset:0\n\t"
    "ds_write_b128 v141, v[192:195] offset:256\n\t"
    "ds_read_b64_tr_b16 v[188:189], v142 offset:0\n\t"
    "ds_read_b64_tr_b16 v[190:191], v142 offset:256\n\t"
    "ds_read_b64_tr_b16 v[192:193], v142 offset:32\n\t"
    "ds_read_b64_tr_b16 v[194:195], v142 offset:288\n\t"
    "ds_write_b128 v141, v[196:199] offset:2112\n\t"
    "ds_write_b128 v141, v[200:203] offset:2368\n\t"
    "ds_read_b64_tr_b16 v[196:197], v142 offset:2112\n\t"
    "ds_read_b64_tr_b16 v[198:199], v142 offset:2368\n\t"
    "ds_read_b64_tr_b16 v[200:201], v142 offset:2144\n\t"
    "ds_read_b64_tr_b16 v[202:203], v142 offset:2400\n\t"
    "v_bfi_b32 v130, v103, 0, v129\n\t"
    "v_cmp_eq_u32 vcc, 0, v130\n\t"
    "v_and_or_b32 v98, v103, s44, v69\n\t"
    "v_xor_b32 v133, 23, v132\n\t"
    "v_lshlrev_b32 v115, v132, v136\n\t"
    "v_and_b32 v132, 7, v132\n\t"
    "v_lshl_or_b32 v115, v136, v133, v115\n\t"
    "v_lshl_add_u32 v132, v132, 1, v128\n\t"
    "ds_or_b32 v97, v115\n\t"
    "ds_write_b16 v132, v104\n\t"
    "v_cndmask_b32 v130, v130, v129, vcc\n\t"
    "v_ffbl_b32 v131, v130\n\t"
    "v_and_b32 v134, 7, v131\n\t"
    "v_lshlrev_b32 v134, v134, s45\n\t"
    "v_xor_b32 v129, v129, v134\n\t"
    "v_xor_b32 v132, v131, v127\n\t"
    "v_bfi_b32 v130, v107, 0, v129\n\t"
    "v_cmp_eq_u32 vcc, 0, v130\n\t"
    "v_and_or_b32 v99, v107, s44, v69\n\t"
    "v_xor_b32 v133, 23, v132\n\t"
    "v_lshlrev_b32 v116, v132, v136\n\t"
    "v_and_b32 v132, 7, v132\n\t"
    "v_lshl_or_b32 v116, v136, v133, v116\n\t"
    "v_lshl_add_u32 v132, v132, 1, v128\n\t"
    "ds_or_b32 v98, v116\n\t"
    "ds_write_b16 v132, v103\n\t"
    "v_cndmask_b32 v130, v130, v129, vcc\n\t"
    "v_ffbl_b32 v131, v130\n\t"
    "v_xor_b32 v132, v131, v127\n\t"
    "v_xor_b32 v133, 23, v132\n\t"
    "v_lshlrev_b32 v117, v132, v136\n\t"
    "v_and_b32 v132, 7, v132\n\t"
    "v_lshl_or_b32 v117, v136, v133, v117\n\t"
    "v_lshl_add_u32 v132, v132, 1, v128\n\t"
    "ds_or_b32 v99, v117\n\t"
    "ds_write_b16 v132, v107\n\t"
    "ds_read_b128 v[72:75], v128\n\t"
    "v_and_or_b32 v100, v84, s44, v69\n\t"
    "ds_read_b32 v100, v100\n\t"
    "v_and_or_b32 v101, v85, s44, v69\n\t"
    "ds_read_b32 v101, v101\n\t"
    "v_and_or_b32 v102, v86, s44, v69\n\t"
    "ds_read_b32 v102, v102\n\t"
    "v_and_or_b32 v103, v87, s44, v69\n\t"
    "ds_read_b32 v103, v103\n\t"
    "v_and_or_b32 v104, v88, s44, v69\n\t"
    "ds_read_b32 v104, v104\n\t"
    "v_and_or_b32 v105, v89, s44, v69\n\t"
    "ds_read_b32 v105, v105\n\t"
    "v_and_or_b32 v106, v90, s44, v69\n\t"
    "ds_read_b32 v106, v106\n\t"
    "v_and_or_b32 v107, v91, s44, v69\n\t"
    "ds_read_b32 v107, v107\n\t"
    "v_lshl_add_u32 v76, v16, 3, s36\n\t"
    "v_lshl_add_u32 v77, v17, 3, s37\n\t"
    "v_lshl_add_u32 v78, v18, 3, s38\n\t"
    "v_lshl_add_u32 v79, v19, 3, s39\n\t"
    "v_lshl_add_u32 v80, v20, 3, s40\n\t"
    "v_lshl_add_u32 v81, v21, 3, s41\n\t"
    "v_lshl_add_u32 v82, v22, 3, s42\n\t"
    "v_lshl_add_u32 v83, v23, 3, s43\n\t"
    "s_waitcnt lgkmcnt(15)\n\t"
    "buffer_store_dwordx4 v[188:191], v139, %12, s48 offen sc1\n\t"
    "buffer_store_dwordx4 v[192:195], v140, %12, s48 offen sc1\n\t"
    "s_add_u32 s48, s48, 819200\n\t"
    "buffer_store_dwordx4 v[196:199], v139, %12, s48 offen sc1\n\t"
    "buffer_store_dwordx4 v[200:203], v140, %12, s48 offen sc1\n\t"
    "s_add_u32 s48, s48, -811008\n\t"
    "s_waitcnt lgkmcnt(8)\n\t"
    "buffer_store_dwordx4 v[72:75], v67, %12, s49 offen offset:416 sc1\n\t"
    "v_mov_b32 v129, 0xffff0000\n\t"
    "s_waitcnt lgkmcnt(4)\n\t"
    "v_perm_b32 v100, v100, v84, v126\n\t"
    "v_perm_b32 v101, v101, v85, v126\n\t"
    "v_perm_b32 v102, v102, v86, v126\n\t"
    "v_perm_b32 v103, v103, v87, v126\n\t"
    "s_waitcnt lgkmcnt(0)\n\t"
    "v_perm_b32 v104, v104, v88, v126\n\t"
    "v_perm_b32 v105, v105, v89, v126\n\t"
    "v_perm_b32 v106, v106, v90, v126\n\t"
    "v_perm_b32 v107, v107, v91, v126\n\t"
    "ds_write_b128 v141, v[56:59] offset:0\n\t"
    "ds_write_b128 v141, v[60:63] offset:256\n\t"
    "ds_read_b64_tr_b16 v[56:57], v142 offset:0\n\t"
    "ds_read_b64_tr_b16 v[58:59], v142 offset:256\n\t"
    "ds_read_b64_tr_b16 v[60:61], v142 offset:32\n\t"
    "ds_read_b64_tr_b16 v[62:63], v142 offset:288\n\t"
    "ds_write_b128 v141, v[204:207] offset:2112\n\t"
    "ds_write_b128 v141, v[208:211] offset:2368\n\t"
    "ds_read_b64_tr_b16 v[204:205], v142 offset:2112\n\t"
    "ds_read_b64_tr_b16 v[206:207], v142 offset:2368\n\t"
    "ds_read_b64_tr_b16 v[208:209], v142 offset:2144\n\t"
    "ds_read_b64_tr_b16 v[210:211], v142 offset:2400\n\t"
    "v_max_u32 v108, v100, v101\n\t"
    "v_min_u32 v101, v100, v101\n\t"
    "v_max_u32 v100, v102, v103\n\t"
    "v_min_u32 v103, v102, v103\n\t"
    "v_max_u32 v102, v104, v105\n\t"
    "v_min_u32 v105, v104, v105\n\t"
    "v_max_u32 v104, v106, v107\n\t"
    "v_min_u32 v107, v106, v107\n\t"
    "v_max_u32 v106, v108, v100\n\t"
    "v_min_u32 v100, v108, v100\n\t"
    "v_max_u32 v108, v101, v103\n\t"
    "v_min_u32 v103, v101, v103\n\t"
    "v_max_u32 v101, v102, v104\n\t"
    "v_min_u32 v104, v102, v104\n\t"
    "v_max_u32 v102, v105, v107\n\t"
    "v_min_u32 v107, v105, v107\n\t"
    "v_bfi_b32 v130, v106, 0, v129\n\t"
    "v_cmp_eq_u32 vcc, 0, v130\n\t"
    "v_and_or_b32 v92, v106, s44, v69\n\t"
    "s_nop 0\n\t"
    "v_cndmask_b32 v130, v130, v129, vcc\n\t"
    "v_ffbl_b32 v131, v130\n\t"
    "v_and_b32 v134, 7, v131\n\t"
    "v_lshlrev_b32 v134, v134, s45\n\t"
    "v_xor_b32 v129, v129, v134\n\t"
    "v_xor_b32 v132, v131, v127\n\t"
    "v_bfi_b32 v130, v101, 0, v129\n\t"
    "v_cmp_eq_u32 vcc, 0, v130\n\t"
    "v_and_or_b32 v93, v101, s44, v69\n\t"
    "v_xor_b32 v133, 23, v132\n\t"
    "v_lshlrev_b32 v110, v132, v136\n\t"
    "v_and_b32 v132, 7, v132\n\t"
    "v_lshl_or_b32 v110, v136, v133, v110\n\t"
    "v_lshl_add_u32 v132, v132, 1, v128\n\t"
    "ds_or_b32 v92, v110\n\t"
    "ds_write_b16 v132, v106\n\t"
    "v_cndmask_b32 v130, v130, v129, vcc\n\t"
    "v_ffbl_b32 v131, v130\n\t"
    "v_and_b32 v134, 7, v131\n\t"
    "v_lshlrev_b32 v134, v134, s45\n\t"
    "v_xor_b32 v129, v129, v134\n\t"
    "v_xor_b32 v132, v131, v127\n\t"
    "v_bfi_b32 v130, v108, 0, v129\n\t"
    "v_cmp_eq_u32 vcc, 0, v130\n\t"
    "v_and_or_b32 v94, v108, s44, v69\n\t"
    "v_xor_b32 v133, 23, v132\n\t"
    "v_lshlrev_b32 v111, v132, v136\n\t"
    "v_and_b32 v132, 7, v132\n\t"
    "v_lshl_or_b32 v111, v136, v133, v111\n\t"
    "v_lshl_add_u32 v132, v132, 1, v128\n\t"
    "ds_or_b32 v93, v111\n\t"
    "ds_write_b16 v132, v101\n\t"
    "v_cndmask_b32 v130, v130, v129, vcc\n\t"
    "v_ffbl_b32 v131, v130\n\t"
    "v_and_b32 v134, 7, v131\n\t"
    "v_lshlrev_b32 v134, v134, s45\n\t"
    "v_xor_b32 v129, v129, v134\n\t"
    "v_xor_b32 v132, v131, v127\n\t"
    "s_waitcnt lgkmcnt(10)\n\t"
    "buffer_store_dwordx4 v[56:59], v139, %12, s48 offen sc1\n\t"
    "buffer_store_dwordx4 v[60:63], v140, %12, s48 offen sc1\n\t"
    "s_add_u32 s48, s48, 819200\n\t"
    "s_waitcnt lgkmcnt(4)\n\t"
    "buffer_store_dwordx4 v[204:207], v139, %12, s48 offen sc1\n\t"
    "buffer_store_dwordx4 v[208:211], v140, %12, s48 offen sc1\n\t"
    "s_add_u32 s48, s48, -811008\n\t"
    "ds_write_b128 v141, v[212:215] offset:0\n\t"
    "ds_write_b128 v141, v[216:219] offset:256\n\t"
    "ds_read_b64_tr_b16 v[212:213], v142 offset:0\n\t"
    "ds_read_b64_tr_b16 v[214:215], v142 offset:256\n\t"
    "ds_read_b64_tr_b16 v[216:217], v142 offset:32\n\t"
    "ds_read_b64_tr_b16 v[218:219], v142 offset:288\n\t"
    "ds_write_b128 v141, v[220:223] offset:2112\n\t"
    "ds_write_b128 v141, v[224:227] offset:2368\n\t"
    "ds_read_b64_tr_b16 v[220:221], v142 offset:2112\n\t"
    "ds_read_b64_tr_b16 v[222:223], v142 offset:2368\n\t"
    "ds_read_b64_tr_b16 v[224:225], v142 offset:2144\n\t"
    "ds_read_b64_tr_b16 v[226:227], v142 offset:2400\n\t"
    "v_bfi_b32 v130, v100, 0, v129\n\t"
    "v_cmp_eq_u32 vcc, 0, v130\n\t"
    "v_and_or_b32 v95, v100, s44, v69\n\t"
    "v_xor_b32 v133, 23, v132\n\t"
    "v_lshlrev_b32 v112, v132, v136\n\t"
    "v_and_b32 v132, 7, v132\n\t"
    "v_lshl_or_b32 v112, v136, v133, v112\n\t"
    "v_lshl_add_u32 v132, v132, 1, v128\n\t"
    "ds_or_b32 v94, v112\n\t"
    "ds_write_b16 v132, v108\n\t"
    "v_cndmask_b32 v130, v130, v129, vcc\n\t"
    "v_ffbl_b32 v131, v130\n\t"
    "v_and_b32 v134, 7, v131\n\t"
    "v_lshlrev_b32 v134, v134, s45\n\t"
    "v_xor_b32 v129, v129, v134\n\t"
    "v_xor_b32 v132, v131, v127\n\t"
    "v_bfi_b32 v130, v102, 0, v129\n\t"
    "v_cmp_eq_u32 vcc, 0, v130\n\t"
    "v_and_or_b32 v96, v102, s44, v69\n\t"
    "v_xor_b32 v133, 23, v132\n\t"
    "v_lshlrev_b32 v113, v132, v136\n\t"
    "v_and_b32 v132, 7, v132\n\t"
    "v_lshl_or_b32 v113, v136, v133, v113\n\t"
    "v_lshl_add_u32 v132, v132, 1, v128\n\t"
    "ds_or_b32 v95, v113\n\t"
    "ds_write_b16 v132, v100\n\t"
    "v_cndmask_b32 v130, v130, v129, vcc\n\t"
    "v_ffbl_b32 v131, v130\n\t"
    "v_and_b32 v134, 7, v131\n\t"
    "v_lshlrev_b32 v134, v134, s45\n\t"
    "v_xor_b32 v129, v129, v134\n\t"
    "v_xor_b32 v132, v131, v127\n\t"
    "v_bfi_b32 v130, v104, 0, v129\n\t"
    "v_cmp_eq_u32 vcc, 0, v130\n\t"
    "v_and_or_b32 v97, v104, s44, v69\n\t"
    "v_xor_b32 v133, 23, v132\n\t"
    "v_lshlrev_b32 v114, v132, v136\n\t"
    "v_and_b32 v132, 7, v132\n\t"
    "v_lshl_or_b32 v114, v136, v133, v114\n\t"
    "v_lshl_add_u32 v132, v132, 1, v128\n\t"
    "ds_or_b32 v96, v114\n\t"
    "ds_write_b16 v132, v102\n\t"
    "v_cndmask_b32 v130, v130, v129, vcc\n\t"
    "v_ffbl_b32 v131, v130\n\t"
    "v_and_b32 v134, 7, v131\n\t"
    "v_lshlrev_b32 v134, v134, s45\n\t"
    "v_xor_b32 v129, v129, v134\n\t"
    "v_xor_b32 v132, v131, v127\n\t"
    "s_waitcnt lgkmcnt(12)\n\t"
    "s_bitcmp1_b32 %14, 3\n\t"
    "s_cselect_b32 s48, 0x40000000, s48\n\t"
    "buffer_store_dwordx4 v[212:215], v139, %12, s48 offen sc1\n\t"
    "buffer_store_dwordx4 v[216:219], v140, %12, s48 offen sc1\n\t"
    "s_add_u32 s48, s48, 819200\n\t"
    "s_waitcnt lgkmcnt(6)\n\t"
    "buffer_store_dwordx4 v[220:223], v139, %12, s48 offen sc1\n\t"
    "buffer_store_dwordx4 v[224:227], v140, %12, s48 offen sc1\n\t"
    "s_add_u32 s48, s48, -811008\n\t"
    "v_bfi_b32 v130, v103, 0, v129\n\t"
    "v_cmp_eq_u32 vcc, 0, v130\n\t"
    "v_and_or_b32 v98, v103, s44, v69\n\t"
    "v_xor_b32 v133, 23, v132\n\t"
    "v_lshlrev_b32 v115, v132, v136\n\t"
    "v_and_b32 v132, 7, v132\n\t"
    "v_lshl_or_b32 v115, v136, v133, v115\n\t"
    "v_lshl_add_u32 v132, v132, 1, v128\n\t"
    "ds_or_b32 v97, v115\n\t"
    "ds_write_b16 v132, v104\n\t"
    "v_cndmask_b32 v130, v130, v129, vcc\n\t"
    "v_ffbl_b32 v131, v130\n\t"
    "v_and_b32 v134, 7, v131\n\t"
    "v_lshlrev_b32 v134, v134, s45\n\t"
    "v_xor_b32 v129, v129, v134\n\t"
    "v_xor_b32 v132, v131, v127\n\t"
    "v_bfi_b32 v130, v107, 0, v129\n\t"
    "v_cmp_eq_u32 vcc, 0, v130\n\t"
    "v_and_or_b32 v99, v107, s44, v69\n\t"
    "v_xor_b32 v133, 23, v132\n\t"
    "v_lshlrev_b32 v116, v132, v136\n\t"
    "v_and_b32 v132, 7, v132\n\t"
    "v_lshl_or_b32 v116, v136, v133, v116\n\t"
    "v_lshl_add_u32 v132, v132, 1, v128\n\t"
    "ds_or_b32 v98, v116\n\t"
    "ds_write_b16 v132, v103\n\t"
    "v_cndmask_b32 v130, v130, v129, vcc\n\t"
    "v_ffbl_b32 v131, v130\n\t"
    "v_xor_b32 v132, v131, v127\n\t"
    "v_xor_b32 v133, 23, v132\n\t"
    "v_lshlrev_b32 v117, v132, v136\n\t"
    "v_and_b32 v132, 7, v132\n\t"
    "v_lshl_or_b32 v117, v136, v133, v117\n\t"
    "v_lshl_add_u32 v132, v132, 1, v128\n\t"
    "ds_or_b32 v99, v117\n\t"
    "ds_write_b16 v132, v107\n\t"
    "ds_read_b128 v[72:75], v128\n\t"
    "v_and_or_b32 v100, v76, s44, v69\n\t"
    "ds_read_b32 v100, v100\n\t"
    "v_and_or_b32 v101, v77, s44, v69\n\t"
    "ds_read_b32 v101, v101\n\t"
    "v_and_or_b32 v102, v78, s44, v69\n\t"
    "ds_read_b32 v102, v102\n\t"
    "v_and_or_b32 v103, v79, s44, v69\n\t"
    "ds_read_b32 v103, v103\n\t"
    "v_and_or_b32 v104, v80, s44, v69\n\t"
    "ds_read_b32 v104, v104\n\t"
    "v_and_or_b32 v105, v81, s44, v69\n\t"
    "ds_read_b32 v105, v105\n\t"
    "v_and_or_b32 v106, v82, s44, v69\n\t"
    "ds_read_b32 v106, v106\n\t"
    "v_and_or_b32 v107, v83, s44, v69\n\t"
    "ds_read_b32 v107, v107\n\t"
    "s_waitcnt lgkmcnt(8)\n\t"
    "buffer_store_dwordx4 v[72:75], v67, %12, s49 offen offset:448 sc1\n\t"
    "v_mov_b32 v129, 0xffff0000\n\t"
    "s_waitcnt lgkmcnt(4)\n\t"
    "v_perm_b32 v100, v100, v76, v126\n\t"
    "v_perm_b32 v101, v101, v77, v126\n\t"
    "v_perm_b32 v102, v102, v78, v126\n\t"
    "v_perm_b32 v103, v103, v79, v126\n\t"
    "s_waitcnt lgkmcnt(0)\n\t"
    "v_perm_b32 v104, v104, v80, v126\n\t"
    "v_perm_b32 v105, v105, v81, v126\n\t"
    "v_perm_b32 v106, v106, v82, v126\n\t"
    "v_perm_b32 v107, v107, v83, v126\n\t"
    "v_max_u32 v108, v100, v101\n\t"
    "v_min_u32 v101, v100, v101\n\t"
    "v_max_u32 v100, v102, v103\n\t"
    "v_min_u32 v103, v102, v103\n\t"
    "v_max_u32 v102, v104, v105\n\t"
    "v_min_u32 v105, v104, v105\n\t"
    "v_max_u32 v104, v106, v107\n\t"
    "v_min_u32 v107, v106, v107\n\t"
    "v_max_u32 v106, v108, v100\n\t"
    "v_min_u32 v100, v108, v100\n\t"
    "v_max_u32 v108, v101, v103\n\t"
    "v_min_u32 v103, v101, v103\n\t"
    "v_max_u32 v101, v102, v104\n\t"
    "v_min_u32 v104, v102, v104\n\t"
    "v_max_u32 v102, v105, v107\n\t"
    "v_min_u32 v107, v105, v107\n\t"
    "v_bfi_b32 v130, v106, 0, v129\n\t"
    "v_cmp_eq_u32 vcc, 0, v130\n\t"
    "v_and_or_b32 v92, v106, s44, v69\n\t"
    "s_nop 0\n\t"
    "v_cndmask_b32 v130, v130, v129, vcc\n\t"
    "v_ffbl_b32 v131, v130\n\t"
    "v_and_b32 v134, 7, v131\n\t"
    "v_lshlrev_b32 v134, v134, s45\n\t"
    "v_xor_b32 v129, v129, v134\n\t"
    "v_xor_b32 v132, v131, v127\n\t"
    "v_bfi_b32 v130, v101, 0, v129\n\t"
    "v_cmp_eq_u32 vcc, 0, v130\n\t"
    "v_and_b32 v132, 7, v132\n\t"
    "v_lshl_add_u32 v132, v132, 1, v128\n\t"
    "ds_write_b16 v132, v106\n\t"
    "v_cndmask_b32 v130, v130, v129, vcc\n\t"
    "v_ffbl_b32 v131, v130\n\t"
    "v_and_b32 v134, 7, v131\n\t"
    "v_lshlrev_b32 v134, v134, s45\n\t"
    "v_xor_b32 v129, v129, v134\n\t"
    "v_xor_b32 v132, v131, v127\n\t"
    "v_bfi_b32 v130, v108, 0, v129\n\t"
    "v_cmp_eq_u32 vcc, 0, v130\n\t"
    "v_and_b32 v132, 7, v132\n\t"
    "v_lshl_add_u32 v132, v132, 1, v128\n\t"
    "ds_write_b16 v132, v101\n\t"
    "v_cndmask_b32 v130, v130, v129, vcc\n\t"
    "v_ffbl_b32 v131, v130\n\t"
    "v_and_b32 v134, 7, v131\n\t"
    "v_lshlrev_b32 v134, v134, s45\n\t"
    "v_xor_b32 v129, v129, v134\n\t"
    "v_xor_b32 v132, v131, v127\n\t"
    "v_bfi_b32 v130, v100, 0, v129\n\t"
    "v_cmp_eq_u32 vcc, 0, v130\n\t"
    "v_and_b32 v132, 7, v132\n\t"
    "v_lshl_add_u32 v132, v132, 1, v128\n\t"
    "ds_write_b16 v132, v108\n\t"
    "v_cndmask_b32 v130, v130, v129, vcc\n\t"
    "v_ffbl_b32 v131, v130\n\t"
    "v_and_b32 v134, 7, v131\n\t"
    "v_lshlrev_b32 v134, v134, s45\n\t"
    "v_xor_b32 v129, v129, v134\n\t"
    "v_xor_b32 v132, v131, v127\n\t"
    "v_bfi_b32 v130, v102, 0, v129\n\t"
    "v_cmp_eq_u32 vcc, 0, v130\n\t"
    "v_and_b32 v132, 7, v132\n\t"
    "v_lshl_add_u32 v132, v132, 1, v128\n\t"
    "ds_write_b16 v132, v100\n\t"
    "v_cndmask_b32 v130, v130, v129, vcc\n\t"
    "v_ffbl_b32 v131, v130\n\t"
    "v_and_b32 v134, 7, v131\n\t"
    "v_lshlrev_b32 v134, v134, s45\n\t"
    "v_xor_b32 v129, v129, v134\n\t"
    "v_xor_b32 v132, v131, v127\n\t"
    "v_bfi_b32 v130, v104, 0, v129\n\t"
    "v_cmp_eq_u32 vcc, 0, v130\n\t"
    "v_and_b32 v132, 7, v132\n\t"
    "v_lshl_add_u32 v132, v132, 1, v128\n\t"
    "ds_write_b16 v132, v102\n\t"
    "v_cndmask_b32 v130, v130, v129, vcc\n\t"
    "v_ffbl_b32 v131, v130\n\t"
    "v_and_b32 v134, 7, v131\n\t"
    "v_lshlrev_b32 v134, v134, s45\n\t"
    "v_xor_b32 v129, v129, v134\n\t"
    "v_xor_b32 v132, v131, v127\n\t"
    "v_bfi_b32 v130, v103, 0, v129\n\t"
    "v_cmp_eq_u32 vcc, 0, v130\n\t"
    "v_and_b32 v132, 7, v132\n\t"
    "v_lshl_add_u32 v132, v132, 1, v128\n\t"
    "ds_write_b16 v132, v104\n\t"
    "v_cndmask_b32 v130, v130, v129, vcc\n\t"
    "v_ffbl_b32 v131, v130\n\t"
    "v_and_b32 v134, 7, v131\n\t"
    "v_lshlrev_b32 v134, v134, s45\n\t"
    "v_xor_b32 v129, v129, v134\n\t"
    "v_xor_b32 v132, v131, v127\n\t"
    "v_bfi_b32 v130, v107, 0, v129\n\t"
    "v_cmp_eq_u32 vcc, 0, v130\n\t"
    "v_and_b32 v132, 7, v132\n\t"
    "v_lshl_add_u32 v132, v132, 1, v128\n\t"
    "ds_write_b16 v132, v103\n\t"
    "v_cndmask_b32 v130, v130, v129, vcc\n\t"
    "v_ffbl_b32 v131, v130\n\t"
    "v_xor_b32 v132, v131, v127\n\t"
    "v_and_b32 v132, 7, v132\n\t"
    "v_lshl_add_u32 v132, v132, 1, v128\n\t"
    "ds_write_b16 v132, v107\n\t"
    "ds_read_b128 v[72:75], v128\n\t"
    "s_waitcnt lgkmcnt(0)\n\t"
    "buffer_store_dwordx4 v[72:75], v67, %12, s49 offen offset:480 sc1\n\t"
    "s_setprio 0\n\t"
    "v_mov_b32 v134, 1036\n\t"
    "L_asvsp12d_%=:\n\t"
    "ds_read_b32 v130, v134\n\t"
    "s_waitcnt lgkmcnt(0)\n\t"
    "v_readfirstlane_b32 s50, v130\n\t"
    "s_nop 0\n\t"
    "s_cmp_lt_u32 s50, 4\n\t"
    "s_cbranch_scc1 L_asvsp12d_%=\n\t"
    "s_call_b64 s[98:99], L_setup_%=\n\t"
    "v_lshrrev_b32 v105, 4, v127\n\t"
    "v_and_b32 v105, 48, v105\n\t"
    "s_movk_i32 s65, 130\n\t"
    "v_mad_u32_u24 v104, v155, s65, v105\n\t"
    "v_add_u32 v105, 14170, v104\n\t"
    "v_and_b32 v104, -16, v104\n\t"
    "v_and_b32 v105, -16, v105\n\t"
    "ds_read_b128 v[164:167], v104 offset:0\n\t"
    "ds_read_b128 v[168:171], v104 offset:64\n\t"
    "ds_read_b128 v[172:175], v104 offset:2080\n\t"
    "ds_read_b128 v[176:179], v104 offset:2144\n\t"
    "ds_read_b128 v[180:183], v104 offset:4160\n\t"
    "ds_read_b128 v[184:187], v104 offset:4224\n\t"
    "ds_read_b128 v[188:191], v104 offset:6240\n\t"
    "ds_read_b128 v[192:195], v104 offset:6304\n\t"
    "ds_read_b128 v[196:199], v104 offset:8320\n\t"
    "ds_read_b128 v[200:203], v104 offset:8384\n\t"
    "ds_read_b128 v[204:207], v104 offset:10400\n\t"
    "ds_read_b128 v[208:211], v104 offset:10464\n\t"
    "s_cmp_lt_u32 s62, 2\n\t"
    "s_cbranch_scc1 L_mel0_%=\n\t"
    "buffer_load_dwordx4 v[220:223], v127, %12, s61 offen offset:2048 sc1\n\t"
    "buffer_load_dwordx4 v[224:227], v127, %12, s61 offen offset:3072 sc1\n\t"
    "s_add_u32 s61, s61, 0x1000\n\t"
    "buffer_load_dwordx4 v[228:231], v127, %12, s61 offen offset:2048 sc1\n\t"
    "buffer_load_dwordx4 v[232:235], v127, %12, s61 offen offset:3072 sc1\n\t"
    "s_add_u32 s61, s61, 0x1000\n\t"
    "buffer_load_dwordx4 v[236:239], v127, %12, s61 offen offset:2048 sc1\n\t"
    "buffer_load_dwordx4 v[240:243], v127, %12, s61 offen offset:3072 sc1\n\t"
    "s_add_u32 s61, s61, 0x1000\n\t"
    "buffer_load_dwordx4 v[244:247], v127, %12, s61 offen offset:2048 sc1\n\t"
    "buffer_load_dwordx4 v[248:251], v127, %12, s61 offen offset:3072 sc1\n\t"
    "s_add_u32 s61, s61, 0x1000\n\t"
    "buffer_load_dwordx4 v[252:255], v127, %12, s61 offen offset:2048 sc1\n\t"
    "s_mov_b32 s62, 1\n\t"
    "L_mel0_%=:\n\t"
    "buffer_load_dwordx4 v[0:3], v127, %12, s63 offen offset:0 sc1\n\t"
    "buffer_load_dwordx4 v[4:7], v127, %12, s63 offen offset:1024 sc1\n\t"
    "buffer_load_dwordx4 v[8:11], v127, %12, s63 offen offset:2048 sc1\n\t"
    "buffer_load_dwordx4 v[12:15], v127, %12, s63 offen offset:3072 sc1\n\t"
    "s_add_u32 s63, s63, 0x1000\n\t"
    "s_mov_b32 s62, 0\n\t"
    "s_and_b32 s65, %14, 8\n\t"
    "s_lshl_b32 s65, s65, 10\n\t"
    "s_add_u32 s65, s65, s63\n\t"
    "s_mov_b32 m0, 136192\n\t"
    "s_add_u32 s65, s65, 0\n\t"
    "buffer_load_dwordx4 v127, %12, s65 offen offset:0 sc1 lds\n\t"
    "buffer_load_dwordx4 v127, %12, s65 offen offset:1024 sc1 lds\n\t"
    "buffer_load_dwordx4 v127, %12, s65 offen offset:2048 sc1 lds\n\t"
    "buffer_load_dwordx4 v127, %12, s65 offen offset:3072 sc1 lds\n\t"
    "s_mov_b32 m0, 140288\n\t"
    "s_add_u32 s65, s65, 4096\n\t"
    "buffer_load_dwordx4 v127, %12, s65 offen offset:0 sc1 lds\n\t"
    "buffer_load_dwordx4 v127, %12, s65 offen offset:1024 sc1 lds\n\t"
    "buffer_load_dwordx4 v127, %12, s65 offen offset:2048 sc1 lds\n\t"
    "buffer_load_dwordx4 v127, %12, s65 offen offset:3072 sc1 lds\n\t"
    "s_waitcnt vmcnt(21)\n\t"
    "s_and_b32 s50, %14, 7\n\t"
    "s_lshr_b32 s51, %14, 4\n\t"
    "s_add_u32 s51, s51, 8\n\t"
    "s_lshl_b32 s50, s50, 7\n\t"
    "s_lshl_b32 s51, s51, 7\n\t"
    "v_mov_b32 v132, s51\n\t"
    "v_writelane_b32 v132, s50, 0\n\t"
    "s_mov_b32 s50, 48237568\n\t"
    "s_mov_b64 exec, 3\n\t"
    "buffer_atomic_add v136, v132, %12, s50 offen sc1\n\t"
    "s_mov_b64 exec, -1\n\t"
    "s_branch L_af1x_%=\n\t"
    "L_af1w_%=:\n\t"
    "s_mov_b32 s51, 1048576\n\t"
    "s_mov_b64 exec, 0x1ff\n\t"
    "L_af1_%=:\n\t"
    "v_cmp_gt_u32 vcc, v148, v71\n\t"
    "s_cbranch_vccz L_af1ok_%=\n\t"
    "s_sleep 8\n\t"
    "buffer_load_dword v71, v70, %12, s52 offen sc1\n\t"
    "s_sub_u32 s51, s51, 1\n\t"
    "s_waitcnt vmcnt(0)\n\t"
    "s_cmp_lg_u32 s51, 0\n\t"
    "s_cbranch_scc1 L_af1_%=\n\t"
    "L_af1ok_%=:\n\t"
    "s_mov_b64 exec, -1\n\t"
    "v_mov_b32 v134, 3112\n\t"
    "ds_write_b32 v134, v136\n\t"
    "v_mbcnt_lo_u32_b32 v70, -1, 0\n\t"
    "v_mbcnt_hi_u32_b32 v70, -1, v70\n\t"
    "s_and_b32 s50, %14, 7\n\t"
    "s_lshl_b32 s50, s50, 4\n\t"
    "s_lshr_b32 s51, %14, 4\n\t"
    "s_add_u32 s50, s50, s51\n\t"
    "s_mul_i32 s50, s50, 102400\n\t"
    "s_add_u32 s50, s50, 3407872\n\t"
    "v_lshlrev_b32 v148, 7, v70\n\t"
    "buffer_load_dword v71, v148, %12, s50 offen sc1\n\t"
    "s_and_b32 s51, %14, 8\n\t"
    "s_lshl_b32 s51, s51, 10\n\t"
    "s_add_u32 s50, s50, s51\n\t"
    "s_add_u32 s50, s50, 0x1000\n\t"
    "v_lshrrev_b32 v149, 4, v70\n\t"
    "v_lshl_add_u32 v149, v149, 11, v148\n\t"
    "buffer_load_dword v71, v148, %12, s50 offen sc1\n\t"
    "s_bfe_u32 s50, %14, 0x40003\n\t"
    "s_lshl_b32 s50, s50, 14\n\t"
    "s_add_u32 s50, s50, 16517120\n\t"
    "v_lshrrev_b32 v150, 3, v70\n\t"
    "v_lshl_add_u32 v150, v150, 10, v148\n\t"
    "buffer_load_dword v71, v149, %12, s50 offen sc1\n\t"
    "s_and_b32 s50, %14, 8\n\t"
    "s_bfe_u32 s51, %14, 0x30004\n\t"
    "s_add_u32 s50, s50, s51\n\t"
    "s_mul_i32 s50, s50, 212992\n\t"
    "s_add_u32 s50, s50, 196608\n\t"
    "buffer_load_dword v71, v150, %12, s50 offen sc1\n\t"
    "s_setpc_b64 s[54:55]\n\t"
    "L_af1x_%=:\n\t"
    "s_mov_b32 s69, 0x40000000\n\t"
    "s_branch L_aend2_%=\n\t"
    "L_ahelp_%=:\n\t"
    "s_setprio 3\n\t"
    "v_cmp_gt_u32 vcc, 48, v147\n\t"
    "v_mov_b32 v133, 0x40000000\n\t"
    "v_lshrrev_b32 v134, 3, v147\n\t"
    "v_add_u32 v134, 6, v134\n\t"
    "v_bfe_u32 v135, v147, 2, 1\n\t"
    "v_and_b32 v130, 3, v147\n\t"
    "v_lshl_add_u32 v132, v135, 5, v130\n\t"
    "v_mul_u32_u24 v64, 400, v132\n\t"
    "v_lshl_add_u32 v64, v134, 5, v64\n\t"
    "s_movk_i32 s50, 0\n\t"
    "buffer_load_dwordx4 v[0:3], v64, %0, s50 offen offset:16\n\t"
    "buffer_load_dwordx4 v[4:7], v64, %0, s50 offen offset:32\n\t"
    "v_lshlrev_b32 v66, 14, v134\n\t"
    "v_lshl_add_u32 v66, v132, 4, v66\n\t"
    "v_lshlrev_b32 v68, 8, v134\n\t"
    "v_lshl_add_u32 v68, v135, 2, v68\n\t"
    "v_add_u32 v68, 130048, v68\n\t"
    "v_cndmask_b32 v68, v133, v68, vcc\n\t"
    "v_cndmask_b32 v66, v133, v66, vcc\n\t"
    "s_barrier\n\t"
    "s_load_dwordx2 s[72:73], %6, 0\n\t"
    "s_load_dwordx2 s[88:89], %7, 0\n\t"
    "s_load_dword s91, %8, 0\n\t"
    "v_lshlrev_b32 v128, 4, v147\n\t"
    "v_add_u32 v128, 133888, v128\n\t"
    "v_and_b32 v130, 1, v147\n\t"
    "v_cndmask_b32 v128, v133, v128, vcc\n\t"
    "v_mul_u32_u24 v127, 7, v130\n\t"
    "v_cmp_eq_u32 vcc, 1, v130\n\t"
    "v_mov_b32 v126, 0x07060100\n\t"
    "v_mov_b32 v133, 0x05040100\n\t"
    "v_mov_b32 v136, 1\n\t"
    "v_cndmask_b32 v126, v126, v133, vcc\n\t"
    "s_mov_b32 s36, 0\n\t"
    "s_mov_b32 s37, 8000\n\t"
    "s_mov_b32 s38, 16000\n\t"
    "s_mov_b32 s39, 24000\n\t"
    "s_mov_b32 s40, 32000\n\t"
    "s_mov_b32 s41, 40000\n\t"
    "s_mov_b32 s42, 48000\n\t"
    "s_mov_b32 s43, 56000\n\t"
    "s_movk_i32 s44, 0xf8\n\t"
    "s_mov_b32 s45, 0x01010000\n\t"
    "s_mov_b32 s46, 0x020c000c\n\t"
    "s_lshr_b32 s49, %14, 4\n\t"
    "s_mul_i32 s49, s49, 212992\n\t"
    "s_and_b32 s50, %14, 15\n\t"
    "s_lshl_b32 s50, s50, 10\n\t"
    "s_add_u32 s49, s49, s50\n\t"
    "s_lshl_b32 s52, %14, 2\n\t"
    "s_add_u32 s52, s52, 48242688\n\t"
    "buffer_load_dword v70, off, %12, s52 offset:1024 sc1\n\t"
    "s_cmp_eq_u32 %14, 0\n\t"
    "s_cselect_b32 s47, 48242688, 0x40000000\n\t"
    "s_cselect_b32 s48, 48234496, 0x40000000\n\t"
    "v_lshlrev_b32 v152, 4, v147\n\t"
    "v_lshlrev_b32 v132, 7, v147\n\t"
    "v_mov_b32 v71, 0\n\t"
    "buffer_store_dword v71, v132, %12, s48 offen sc1\n\t"
    "v_mov_b32 v148, 0\n\t"
    "v_mov_b32 v149, 0\n\t"
    "v_mov_b32 v150, 0\n\t"
    "v_mov_b32 v151, 0\n\t"
    "buffer_store_dwordx4 v[148:151], v152, %12, s47 offen offset:2048 sc1\n\t"
    "s_add_u32 s50, s48, 0x1000\n\t"
    "s_waitcnt vmcnt(5)\n\t"
    "v_lshlrev_b32 v134, 20, v147\n\t"
    "v_add_u32 v134, 2068, v134\n\t"
    "ds_add_u32 v134, v136\n\t"
    "v_mov_b32 v110, 65664\n\t"
    "v_mov_b32 v111, 131136\n\t"
    "v_mov_b32 v112, 262176\n\t"
    "v_mov_b32 v113, 524304\n\t"
    "v_mov_b32 v114, 1048584\n\t"
    "v_mov_b32 v115, 2097156\n\t"
    "v_mov_b32 v116, 4194306\n\t"
    "v_mov_b32 v117, 8388609\n\t"
    "s_waitcnt vmcnt(3)\n\t"
    "v_lshl_add_u32 v76, v0, 3, s36\n\t"
    "v_lshl_add_u32 v77, v1, 3, s37\n\t"
    "v_lshl_add_u32 v78, v2, 3, s38\n\t"
    "v_lshl_add_u32 v79, v3, 3, s39\n\t"
    "v_lshl_add_u32 v80, v4, 3, s40\n\t"
    "v_lshl_add_u32 v81, v5, 3, s41\n\t"
    "v_lshl_add_u32 v82, v6, 3, s42\n\t"
    "v_lshl_add_u32 v83, v7, 3, s43\n\t"
    "v_and_or_b32 v92, v76, s44, v68\n\t"
    "ds_or_rtn_b32 v118, v92, v110\n\t"
    "v_and_or_b32 v93, v77, s44, v68\n\t"
    "ds_or_rtn_b32 v119, v93, v111\n\t"
    "v_and_or_b32 v94, v78, s44, v68\n\t"
    "ds_or_rtn_b32 v120, v94, v112\n\t"
    "v_and_or_b32 v95, v79, s44, v68\n\t"
    "ds_or_rtn_b32 v121, v95, v113\n\t"
    "v_and_or_b32 v96, v80, s44, v68\n\t"
    "ds_or_rtn_b32 v122, v96, v114\n\t"
    "v_and_or_b32 v97, v81, s44, v68\n\t"
    "ds_or_rtn_b32 v123, v97, v115\n\t"
    "v_and_or_b32 v98, v82, s44, v68\n\t"
    "ds_or_rtn_b32 v124, v98, v116\n\t"
    "v_and_or_b32 v99, v83, s44, v68\n\t"
    "ds_or_rtn_b32 v125, v99, v117\n\t"
    "v_lshl_or_b32 v164, v77, 16, v76\n\t"
    "v_lshl_or_b32 v165, v79, 16, v78\n\t"
    "v_lshl_or_b32 v166, v81, 16, v80\n\t"
    "v_lshl_or_b32 v167, v83, 16, v82\n\t"
    "s_movk_i32 s50, 0\n\t"
    "buffer_load_dwordx4 v[8:11], v64, %0, s50 offen offset:1616\n\t"
    "buffer_load_dwordx4 v[12:15], v64, %0, s50 offen offset:1632\n\t"
    "s_movk_i32 s50, 3200\n\t"
    "buffer_load_dwordx4 v[16:19], v64, %0, s50 offen offset:16\n\t"
    "buffer_load_dwordx4 v[20:23], v64, %0, s50 offen offset:32\n\t"
    "s_waitcnt vmcnt(6)\n\t"
    "v_add_u32 v70, 1, v70\n\t"
    "buffer_store_dword v70, off, %12, s52 sc1\n\t"
    "v_mov_b32 v71, 2072\n\t"
    "ds_write_b32 v71, v70\n\t"
    "s_waitcnt vmcnt(3)\n\t"
    "v_lshl_add_u32 v84, v8, 3, s36\n\t"
    "v_lshl_add_u32 v85, v9, 3, s37\n\t"
    "v_lshl_add_u32 v86, v10, 3, s38\n\t"
    "v_lshl_add_u32 v87, v11, 3, s39\n\t"
    "v_lshl_add_u32 v88, v12, 3, s40\n\t"
    "v_lshl_add_u32 v89, v13, 3, s41\n\t"
    "v_lshl_add_u32 v90, v14, 3, s42\n\t"
    "v_lshl_add_u32 v91, v15, 3, s43\n\t"
    "s_waitcnt lgkmcnt(5)\n\t"
    "v_and_b32 v118, v118, v110\n\t"
    "v_perm_b32 v118, v118, v118, s46\n\t"
    "ds_or_b32 v92, v118\n\t"
    "v_and_b32 v119, v119, v111\n\t"
    "v_perm_b32 v119, v119, v119, s46\n\t"
    "ds_or_b32 v93, v119\n\t"
    "v_and_b32 v120, v120, v112\n\t"
    "v_perm_b32 v120, v120, v120, s46\n\t"
    "ds_or_b32 v94, v120\n\t"
    "v_and_b32 v121, v121, v113\n\t"
    "v_perm_b32 v121, v121, v121, s46\n\t"
    "ds_or_b32 v95, v121\n\t"
    "s_waitcnt lgkmcnt(5)\n\t"
    "v_and_b32 v122, v122, v114\n\t"
    "v_perm_b32 v122, v122, v122, s46\n\t"
    "ds_or_b32 v96, v122\n\t"
    "v_and_b32 v123, v123, v115\n\t"
    "v_perm_b32 v123, v123, v123, s46\n\t"
    "ds_or_b32 v97, v123\n\t"
    "v_and_b32 v124, v124, v116\n\t"
    "v_perm_b32 v124, v124, v124, s46\n\t"
    "ds_or_b32 v98, v124\n\t"
    "v_and_b32 v125, v125, v117\n\t"
    "v_perm_b32 v125, v125, v125, s46\n\t"
    "ds_or_b32 v99, v125\n\t"
    "buffer_store_dwordx4 v[164:167], v66, %12, s49 offen offset:0 sc1\n\t"
    "v_and_or_b32 v100, v84, s44, v68\n\t"
    "ds_read_b32 v100, v100\n\t"
    "v_and_or_b32 v101, v85, s44, v68\n\t"
    "ds_read_b32 v101, v101\n\t"
    "v_and_or_b32 v102, v86, s44, v68\n\t"
    "ds_read_b32 v102, v102\n\t"
    "v_and_or_b32 v103, v87, s44, v68\n\t"
    "ds_read_b32 v103, v103\n\t"
    "v_and_or_b32 v104, v88, s44, v68\n\t"
    "ds_read_b32 v104, v104\n\t"
    "v_and_or_b32 v105, v89, s44, v68\n\t"
    "ds_read_b32 v105, v105\n\t"
    "v_and_or_b32 v106, v90, s44, v68\n\t"
    "ds_read_b32 v106, v106\n\t"
    "v_and_or_b32 v107, v91, s44, v68\n\t"
    "ds_read_b32 v107, v107\n\t"
    "s_waitcnt vmcnt(2)\n\t"
    "v_lshl_add_u32 v76, v16, 3, s36\n\t"
    "v_lshl_add_u32 v77, v17, 3, s37\n\t"
    "v_lshl_add_u32 v78, v18, 3, s38\n\t"
    "v_lshl_add_u32 v79, v19, 3, s39\n\t"
    "v_lshl_add_u32 v80, v20, 3, s40\n\t"
    "v_lshl_add_u32 v81, v21, 3, s41\n\t"
    "v_lshl_add_u32 v82, v22, 3, s42\n\t"
    "v_lshl_add_u32 v83, v23, 3, s43\n\t"
    "s_movk_i32 s50, 3200\n\t"
    "buffer_load_dwordx4 v[24:27], v64, %0, s50 offen offset:1616\n\t"
    "buffer_load_dwordx4 v[28:31], v64, %0, s50 offen offset:1632\n\t"
    "s_movk_i32 s50, 6400\n\t"
    "buffer_load_dwordx4 v[32:35], v64, %0, s50 offen offset:16\n\t"
    "buffer_load_dwordx4 v[36:39], v64, %0, s50 offen offset:32\n\t"
    "s_cmp_lg_u32 %14, 0\n\t"
    "s_cbranch_scc1 L_ahe183_%=\n\t"
    "buffer_load_dwordx4 v[148:151], v152, %12, s47 offen sc1\n\t"
    "L_ahe183_%=:\n\t"
    "v_mov_b32 v129, 0xffff0000\n\t"
    "s_waitcnt lgkmcnt(4)\n\t"
    "v_perm_b32 v100, v100, v84, v126\n\t"
    "v_perm_b32 v101, v101, v85, v126\n\t"
    "v_perm_b32 v102, v102, v86, v126\n\t"
    "v_perm_b32 v103, v103, v87, v126\n\t"
    "s_waitcnt lgkmcnt(0)\n\t"
    "v_perm_b32 v104, v104, v88, v126\n\t"
    "v_perm_b32 v105, v105, v89, v126\n\t"
    "v_perm_b32 v106, v106, v90, v126\n\t"
    "v_perm_b32 v107, v107, v91, v126\n\t"
    "v_max_u32 v108, v100, v101\n\t"
    "v_min_u32 v101, v100, v101\n\t"
    "v_max_u32 v100, v102, v103\n\t"
    "v_min_u32 v103, v102, v103\n\t"
    "v_max_u32 v102, v104, v105\n\t"
    "v_min_u32 v105, v104, v105\n\t"
    "v_max_u32 v104, v106, v107\n\t"
    "v_min_u32 v107, v106, v107\n\t"
    "v_max_u32 v106, v108, v100\n\t"
    "v_min_u32 v100, v108, v100\n\t"
    "v_max_u32 v108, v101, v103\n\t"
    "v_min_u32 v103, v101, v103\n\t"
    "v_max_u32 v101, v102, v104\n\t"
    "v_min_u32 v104, v102, v104\n\t"
    "v_max_u32 v102, v105, v107\n\t"
    "v_min_u32 v107, v105, v107\n\t"
    "v_bfi_b32 v130, v106, 0, v129\n\t"
    "v_cmp_eq_u32 vcc, 0, v130\n\t"
    "v_and_or_b32 v92, v106, s44, v68\n\t"
    "s_nop 0\n\t"
    "v_cndmask_b32 v130, v130, v129, vcc\n\t"
    "v_ffbl_b32 v131, v130\n\t"
    "v_and_b32 v134, 7, v131\n\t"
    "v_lshlrev_b32 v134, v134, s45\n\t"
    "v_xor_b32 v129, v129, v134\n\t"
    "v_xor_b32 v132, v131, v127\n\t"
    "v_bfi_b32 v130, v101, 0, v129\n\t"
    "v_cmp_eq_u32 vcc, 0, v130\n\t"
    "v_and_or_b32 v93, v101, s44, v68\n\t"
    "v_xor_b32 v133, 23, v132\n\t"
    "v_lshlrev_b32 v110, v132, v136\n\t"
    "v_and_b32 v132, 7, v132\n\t"
    "v_lshl_or_b32 v110, v136, v133, v110\n\t"
    "v_lshl_add_u32 v132, v132, 1, v128\n\t"
    "ds_or_rtn_b32 v118, v92, v110\n\t"
    "ds_write_b16 v132, v106\n\t"
    "v_cndmask_b32 v130, v130, v129, vcc\n\t"
    "v_ffbl_b32 v131, v130\n\t"
    "v_and_b32 v134, 7, v131\n\t"
    "v_lshlrev_b32 v134, v134, s45\n\t"
    "v_xor_b32 v129, v129, v134\n\t"
    "v_xor_b32 v132, v131, v127\n\t"
    "v_bfi_b32 v130, v108, 0, v129\n\t"
    "v_cmp_eq_u32 vcc, 0, v130\n\t"
    "v_and_or_b32 v94, v108, s44, v68\n\t"
    "v_xor_b32 v133, 23, v132\n\t"
    "v_lshlrev_b32 v111, v132, v136\n\t"
    "v_and_b32 v132, 7, v132\n\t"
    "v_lshl_or_b32 v111, v136, v133, v111\n\t"
    "v_lshl_add_u32 v132, v132, 1, v128\n\t"
    "ds_or_rtn_b32 v119, v93, v111\n\t"
    "ds_write_b16 v132, v101\n\t"
    "v_cndmask_b32 v130, v130, v129, vcc\n\t"
    "v_ffbl_b32 v131, v130\n\t"
    "v_and_b32 v134, 7, v131\n\t"
    "v_lshlrev_b32 v134, v134, s45\n\t"
    "v_xor_b32 v129, v129, v134\n\t"
    "v_xor_b32 v132, v131, v127\n\t"
    "v_bfi_b32 v130, v100, 0, v129\n\t"
    "v_cmp_eq_u32 vcc, 0, v130\n\t"
    "v_and_or_b32 v95, v100, s44, v68\n\t"
    "v_xor_b32 v133, 23, v132\n\t"
    "v_lshlrev_b32 v112, v132, v136\n\t"
    "v_and_b32 v132, 7, v132\n\t"
    "v_lshl_or_b32 v112, v136, v133, v112\n\t"
    "v_lshl_add_u32 v132, v132, 1, v128\n\t"
    "ds_or_rtn_b32 v120, v94, v112\n\t"
    "ds_write_b16 v132, v108\n\t"
    "v_cndmask_b32 v130, v130, v129, vcc\n\t"
    "v_ffbl_b32 v131, v130\n\t"
    "v_and_b32 v134, 7, v131\n\t"
    "v_lshlrev_b32 v134, v134, s45\n\t"
    "v_xor_b32 v129, v129, v134\n\t"
    "v_xor_b32 v132, v131, v127\n\t"
    "v_bfi_b32 v130, v102, 0, v129\n\t"
    "v_cmp_eq_u32 vcc, 0, v130\n\t"
    "v_and_or_b32 v96, v102, s44, v68\n\t"
    "v_xor_b32 v133, 23, v132\n\t"
    "v_lshlrev_b32 v113, v132, v136\n\t"
    "v_and_b32 v132, 7, v132\n\t"
    "v_lshl_or_b32 v113, v136, v133, v113\n\t"
    "v_lshl_add_u32 v132, v132, 1, v128\n\t"
    "ds_or_rtn_b32 v121, v95, v113\n\t"
    "ds_write_b16 v132, v100\n\t"
    "v_cndmask_b32 v130, v130, v129, vcc\n\t"
    "v_ffbl_b32 v131, v130\n\t"
    "v_and_b32 v134, 7, v131\n\t"
    "v_lshlrev_b32 v134, v134, s45\n\t"
    "v_xor_b32 v129, v129, v134\n\t"
    "v_xor_b32 v132, v131, v127\n\t"
    "s_waitcnt lgkmcnt(7)\n\t"
    "v_and_b32 v118, v118, v110\n\t"
    "v_perm_b32 v118, v118, v118, s46\n\t"
    "ds_or_b32 v92, v118\n\t"
    "v_bfi_b32 v130, v104, 0, v129\n\t"
    "v_cmp_eq_u32 vcc, 0, v130\n\t"
    "v_and_or_b32 v97, v104, s44, v68\n\t"
    "v_xor_b32 v133, 23, v132\n\t"
    "v_lshlrev_b32 v114, v132, v136\n\t"
    "v_and_b32 v132, 7, v132\n\t"
    "v_lshl_or_b32 v114, v136, v133, v114\n\t"
    "v_lshl_add_u32 v132, v132, 1, v128\n\t"
    "ds_or_rtn_b32 v122, v96, v114\n\t"
    "ds_write_b16 v132, v102\n\t"
    "v_cndmask_b32 v130, v130, v129, vcc\n\t"
    "v_ffbl_b32 v131, v130\n\t"
    "v_and_b32 v134, 7, v131\n\t"
    "v_lshlrev_b32 v134, v134, s45\n\t"
    "v_xor_b32 v129, v129, v134\n\t"
    "v_xor_b32 v132, v131, v127\n\t"
    "s_waitcnt lgkmcnt(8)\n\t"
    "v_and_b32 v119, v119, v111\n\t"
    "v_perm_b32 v119, v119, v119, s46\n\t"
    "ds_or_b32 v93, v119\n\t"
    "v_bfi_b32 v130, v103, 0, v129\n\t"
    "v_cmp_eq_u32 vcc, 0, v130\n\t"
    "v_and_or_b32 v98, v103, s44, v68\n\t"
    "v_xor_b32 v133, 23, v132\n\t"
    "v_lshlrev_b32 v115, v132, v136\n\t"
    "v_and_b32 v132, 7, v132\n\t"
    "v_lshl_or_b32 v115, v136, v133, v115\n\t"
    "v_lshl_add_u32 v132, v132, 1, v128\n\t"
    "ds_or_rtn_b32 v123, v97, v115\n\t"
    "ds_write_b16 v132, v104\n\t"
    "v_cndmask_b32 v130, v130, v129, vcc\n\t"
    "v_ffbl_b32 v131, v130\n\t"
    "v_and_b32 v134, 7, v131\n\t"
    "v_lshlrev_b32 v134, v134, s45\n\t"
    "v_xor_b32 v129, v129, v134\n\t"
    "v_xor_b32 v132, v131, v127\n\t"
    "s_waitcnt lgkmcnt(9)\n\t"
    "v_and_b32 v120, v120, v112\n\t"
    "v_perm_b32 v120, v120, v120, s46\n\t"
    "ds_or_b32 v94, v120\n\t"
    "v_bfi_b32 v130, v107, 0, v129\n\t"
    "v_cmp_eq_u32 vcc, 0, v130\n\t"
    "v_and_or_b32 v99, v107, s44, v68\n\t"
    "v_xor_b32 v133, 23, v132\n\t"
    "v_lshlrev_b32 v116, v132, v136\n\t"
    "v_and_b32 v132, 7, v132\n\t"
    "v_lshl_or_b32 v116, v136, v133, v116\n\t"
    "v_lshl_add_u32 v132, v132, 1, v128\n\t"
    "ds_or_rtn_b32 v124, v98, v116\n\t"
    "ds_write_b16 v132, v103\n\t"
    "v_cndmask_b32 v130, v130, v129, vcc\n\t"
    "v_ffbl_b32 v131, v130\n\t"
    "v_xor_b32 v132, v131, v127\n\t"
    "s_waitcnt lgkmcnt(10)\n\t"
    "v_and_b32 v121, v121, v113\n\t"
    "v_perm_b32 v121, v121, v121, s46\n\t"
    "ds_or_b32 v95, v121\n\t"
    "v_xor_b32 v133, 23, v132\n\t"
    "v_lshlrev_b32 v117, v132, v136\n\t"
    "v_and_b32 v132, 7, v132\n\t"
    "v_lshl_or_b32 v117, v136, v133, v117\n\t"
    "v_lshl_add_u32 v132, v132, 1, v128\n\t"
    "ds_or_rtn_b32 v125, v99, v117\n\t"
    "ds_write_b16 v132, v107\n\t"
    "s_waitcnt lgkmcnt(10)\n\t"
    "v_and_b32 v122, v122, v114\n\t"
    "v_perm_b32 v122, v122, v122, s46\n\t"
    "ds_or_b32 v96, v122\n\t"
    "ds_read_b128 v[72:75], v128\n\t"
    "v_and_or_b32 v100, v76, s44, v68\n\t"
    "ds_read_b32 v100, v100\n\t"
    "v_and_or_b32 v101, v77, s44, v68\n\t"
    "ds_read_b32 v101, v101\n\t"
    "v_and_or_b32 v102, v78, s44, v68\n\t"
    "ds_read_b32 v102, v102\n\t"
    "v_and_or_b32 v103, v79, s44, v68\n\t"
    "ds_read_b32 v103, v103\n\t"
    "v_and_or_b32 v104, v80, s44, v68\n\t"
    "ds_read_b32 v104, v104\n\t"
    "v_and_or_b32 v105, v81, s44, v68\n\t"
    "ds_read_b32 v105, v105\n\t"
    "v_and_or_b32 v106, v82, s44, v68\n\t"
    "ds_read_b32 v106, v106\n\t"
    "v_and_or_b32 v107, v83, s44, v68\n\t"
    "ds_read_b32 v107, v107\n\t"
    "s_cmp_lg_u32 %14, 0\n\t"
    "s_cbranch_scc1 L_ahe377_%=\n\t"
    "s_waitcnt vmcnt(0)\n\t"
    "buffer_store_dwordx4 v[148:151], v152, %12, s47 offen offset:1024 sc1\n\t"
    "L_ahe377_%=:\n\t"
    "s_waitcnt vmcnt(2)\n\t"
    "v_lshl_add_u32 v84, v24, 3, s36\n\t"
    "v_lshl_add_u32 v85, v25, 3, s37\n\t"
    "v_lshl_add_u32 v86, v26, 3, s38\n\t"
    "v_lshl_add_u32 v87, v27, 3, s39\n\t"
    "v_lshl_add_u32 v88, v28, 3, s40\n\t"
    "v_lshl_add_u32 v89, v29, 3, s41\n\t"
    "v_lshl_add_u32 v90, v30, 3, s42\n\t"
    "v_lshl_add_u32 v91, v31, 3, s43\n\t"
    "v_mul_u32_u24 v58, 400, v147\n\t"
    "buffer_load_dwordx4 v[48:51], v58, %0, 0 offen\n\t"
    "s_lshr_b32 s50, %14, 5\n\t"
    "s_mul_i32 s50, s50, 125\n\t"
    "s_bfe_u32 s51, %14, 0x30002\n\t"
    "s_lshl_b32 s51, s51, 4\n\t"
    "s_min_u32 s51, s51, 109\n\t"
    "s_add_u32 s50, s50, s51\n\t"
    "s_lshl_b32 s50, s50, 8\n\t"
    "s_and_b32 s51, %14, 3\n\t"
    "s_lshl_b32 s51, s51, 6\n\t"
    "s_add_u32 s50, s50, s51\n\t"
    "s_bitcmp1_b32 %14, 1\n\t"
    "s_cselect_b32 s50, s50, 0x40000000\n\t"
    "v_and_b32 v59, 48, v147\n\t"
    "v_and_b32 v60, 15, v147\n\t"
    "v_lshl_add_u32 v59, v60, 8, v59\n\t"
    "buffer_load_dwordx4 v[52:55], v59, %15, s50 offen\n\t"
    "s_cmp_lg_u32 %14, 0\n\t"
    "s_cbranch_scc1 L_ahe409_%=\n\t"
    "buffer_load_dwordx4 v[148:151], v152, %12, s47 offen sc1\n\t"
    "L_ahe409_%=:\n\t"
    "s_waitcnt lgkmcnt(11)\n\t"
    "v_and_b32 v123, v123, v115\n\t"
    "v_perm_b32 v123, v123, v123, s46\n\t"
    "ds_or_b32 v97, v123\n\t"
    "v_and_b32 v124, v124, v116\n\t"
    "v_perm_b32 v124, v124, v124, s46\n\t"
    "ds_or_b32 v98, v124\n\t"
    "v_and_b32 v125, v125, v117\n\t"
    "v_perm_b32 v125, v125, v125, s46\n\t"
    "ds_or_b32 v99, v125\n\t"
    "s_waitcnt lgkmcnt(11)\n\t"
    "buffer_store_dwordx4 v[72:75], v66, %12, s49 offen offset:64 sc1\n\t"
    "v_mov_b32 v129, 0xffff0000\n\t"
    "s_waitcnt lgkmcnt(7)\n\t"
    "v_perm_b32 v100, v100, v76, v126\n\t"
    "v_perm_b32 v101, v101, v77, v126\n\t"
    "v_perm_b32 v102, v102, v78, v126\n\t"
    "v_perm_b32 v103, v103, v79, v126\n\t"
    "s_waitcnt lgkmcnt(3)\n\t"
    "v_perm_b32 v104, v104, v80, v126\n\t"
    "v_perm_b32 v105, v105, v81, v126\n\t"
    "v_perm_b32 v106, v106, v82, v126\n\t"
    "v_perm_b32 v107, v107, v83, v126\n\t"
    "v_max_u32 v108, v100, v101\n\t"
    "v_min_u32 v101, v100, v101\n\t"
    "v_max_u32 v100, v102, v103\n\t"
    "v_min_u32 v103, v102, v103\n\t"
    "v_max_u32 v102, v104, v105\n\t"
    "v_min_u32 v105, v104, v105\n\t"
    "v_max_u32 v104, v106, v107\n\t"
    "v_min_u32 v107, v106, v107\n\t"
    "v_max_u32 v106, v108, v100\n\t"
    "v_min_u32 v100, v108, v100\n\t"
    "v_max_u32 v108, v101, v103\n\t"
    "v_min_u32 v103, v101, v103\n\t"
    "v_max_u32 v101, v102, v104\n\t"
    "v_min_u32 v104, v102, v104\n\t"
    "v_max_u32 v102, v105, v107\n\t"
    "v_min_u32 v107, v105, v107\n\t"
    "v_bfi_b32 v130, v106, 0, v129\n\t"
    "v_cmp_eq_u32 vcc, 0, v130\n\t"
    "v_and_or_b32 v92, v106, s44, v68\n\t"
    "s_nop 0\n\t"
    "v_cndmask_b32 v130, v130, v129, vcc\n\t"
    "v_ffbl_b32 v131, v130\n\t"
    "v_and_b32 v134, 7, v131\n\t"
    "v_lshlrev_b32 v134, v134, s45\n\t"
    "v_xor_b32 v129, v129, v134\n\t"
    "v_xor_b32 v132, v131, v127\n\t"
    "v_bfi_b32 v130, v101, 0, v129\n\t"
    "v_cmp_eq_u32 vcc, 0, v130\n\t"
    "v_and_or_b32 v93, v101, s44, v68\n\t"
    "v_xor_b32 v133, 23, v132\n\t"
    "v_lshlrev_b32 v110, v132, v136\n\t"
    "v_and_b32 v132, 7, v132\n\t"
    "v_lshl_or_b32 v110, v136, v133, v110\n\t"
    "v_lshl_add_u32 v132, v132, 1, v128\n\t"
    "ds_or_rtn_b32 v118, v92, v110\n\t"
    "ds_write_b16 v132, v106\n\t"
    "v_cndmask_b32 v130, v130, v129, vcc\n\t"
    "v_ffbl_b32 v131, v130\n\t"
    "v_and_b32 v134, 7, v131\n\t"
    "v_lshlrev_b32 v134, v134, s45\n\t"
    "v_xor_b32 v129, v129, v134\n\t"
    "v_xor_b32 v132, v131, v127\n\t"
    "v_bfi_b32 v130, v108, 0, v129\n\t"
    "v_cmp_eq_u32 vcc, 0, v130\n\t"
    "v_and_or_b32 v94, v108, s44, v68\n\t"
    "v_xor_b32 v133, 23, v132\n\t"
    "v_lshlrev_b32 v111, v132, v136\n\t"
    "v_and_b32 v132, 7, v132\n\t"
    "v_lshl_or_b32 v111, v136, v133, v111\n\t"
    "v_lshl_add_u32 v132, v132, 1, v128\n\t"
    "ds_or_rtn_b32 v119, v93, v111\n\t"
    "ds_write_b16 v132, v101\n\t"
    "v_cndmask_b32 v130, v130, v129, vcc\n\t"
    "v_ffbl_b32 v131, v130\n\t"
    "v_and_b32 v134, 7, v131\n\t"
    "v_lshlrev_b32 v134, v134, s45\n\t"
    "v_xor_b32 v129, v129, v134\n\t"
    "v_xor_b32 v132, v131, v127\n\t"
    "v_bfi_b32 v130, v100, 0, v129\n\t"
    "v_cmp_eq_u32 vcc, 0, v130\n\t"
    "v_and_or_b32 v95, v100, s44, v68\n\t"
    "v_xor_b32 v133, 23, v132\n\t"
    "v_lshlrev_b32 v112, v132, v136\n\t"
    "v_and_b32 v132, 7, v132\n\t"
    "v_lshl_or_b32 v112, v136, v133, v112\n\t"
    "v_lshl_add_u32 v132, v132, 1, v128\n\t"
    "ds_or_rtn_b32 v120, v94, v112\n\t"
    "ds_write_b16 v132, v108\n\t"
    "v_cndmask_b32 v130, v130, v129, vcc\n\t"
    "v_ffbl_b32 v131, v130\n\t"
    "v_and_b32 v134, 7, v131\n\t"
    "v_lshlrev_b32 v134, v134, s45\n\t"
    "v_xor_b32 v129, v129, v134\n\t"
    "v_xor_b32 v132, v131, v127\n\t"
    "v_bfi_b32 v130, v102, 0, v129\n\t"
    "v_cmp_eq_u32 vcc, 0, v130\n\t"
    "v_and_or_b32 v96, v102, s44, v68\n\t"
    "v_xor_b32 v133, 23, v132\n\t"
    "v_lshlrev_b32 v113, v132, v136\n\t"
    "v_and_b32 v132, 7, v132\n\t"
    "v_lshl_or_b32 v113, v136, v133, v113\n\t"
    "v_lshl_add_u32 v132, v132, 1, v128\n\t"
    "ds_or_rtn_b32 v121, v95, v113\n\t"
    "ds_write_b16 v132, v100\n\t"
    "v_cndmask_b32 v130, v130, v129, vcc\n\t"
    "v_ffbl_b32 v131, v130\n\t"
    "v_and_b32 v134, 7, v131\n\t"
    "v_lshlrev_b32 v134, v134, s45\n\t"
    "v_xor_b32 v129, v129, v134\n\t"
    "v_xor_b32 v132, v131, v127\n\t"
    "s_waitcnt lgkmcnt(7)\n\t"
    "v_and_b32 v118, v118, v110\n\t"
    "v_perm_b32 v118, v118, v118, s46\n\t"
    "ds_or_b32 v92, v118\n\t"
    "v_bfi_b32 v130, v104, 0, v129\n\t"
    "v_cmp_eq_u32 vcc, 0, v130\n\t"
    "v_and_or_b32 v97, v104, s44, v68\n\t"
    "v_xor_b32 v133, 23, v132\n\t"
    "v_lshlrev_b32 v114, v132, v136\n\t"
    "v_and_b32 v132, 7, v132\n\t"
    "v_lshl_or_b32 v114, v136, v133, v114\n\t"
    "v_lshl_add_u32 v132, v132, 1, v128\n\t"
    "ds_or_rtn_b32 v122, v96, v114\n\t"
    "ds_write_b16 v132, v102\n\t"
    "v_cndmask_b32 v130, v130, v129, vcc\n\t"
    "v_ffbl_b32 v131, v130\n\t"
    "v_and_b32 v134, 7, v131\n\t"
    "v_lshlrev_b32 v134, v134, s45\n\t"
    "v_xor_b32 v129, v129, v134\n\t"
    "v_xor_b32 v132, v131, v127\n\t"
    "s_waitcnt lgkmcnt(8)\n\t"
    "v_and_b32 v119, v119, v111\n\t"
    "v_perm_b32 v119, v119, v119, s46\n\t"
    "ds_or_b32 v93, v119\n\t"
    "v_bfi_b32 v130, v103, 0, v129\n\t"
    "v_cmp_eq_u32 vcc, 0, v130\n\t"
    "v_and_or_b32 v98, v103, s44, v68\n\t"
    "v_xor_b32 v133, 23, v132\n\t"
    "v_lshlrev_b32 v115, v132, v136\n\t"
    "v_and_b32 v132, 7, v132\n\t"
    "v_lshl_or_b32 v115, v136, v133, v115\n\t"
    "v_lshl_add_u32 v132, v132, 1, v128\n\t"
    "ds_or_rtn_b32 v123, v97, v115\n\t"
    "ds_write_b16 v132, v104\n\t"
    "v_cndmask_b32 v130, v130, v129, vcc\n\t"
    "v_ffbl_b32 v131, v130\n\t"
    "v_and_b32 v134, 7, v131\n\t"
    "v_lshlrev_b32 v134, v134, s45\n\t"
    "v_xor_b32 v129, v129, v134\n\t"
    "v_xor_b32 v132, v131, v127\n\t"
    "s_waitcnt lgkmcnt(9)\n\t"
    "v_and_b32 v120, v120, v112\n\t"
    "v_perm_b32 v120, v120, v120, s46\n\t"
    "ds_or_b32 v94, v120\n\t"
    "v_bfi_b32 v130, v107, 0, v129\n\t"
    "v_cmp_eq_u32 vcc, 0, v130\n\t"
    "v_and_or_b32 v99, v107, s44, v68\n\t"
    "v_xor_b32 v133, 23, v132\n\t"
    "v_lshlrev_b32 v116, v132, v136\n\t"
    "v_and_b32 v132, 7, v132\n\t"
    "v_lshl_or_b32 v116, v136, v133, v116\n\t"
    "v_lshl_add_u32 v132, v132, 1, v128\n\t"
    "ds_or_rtn_b32 v124, v98, v116\n\t"
    "ds_write_b16 v132, v103\n\t"
    "v_cndmask_b32 v130, v130, v129, vcc\n\t"
    "v_ffbl_b32 v131, v130\n\t"
    "v_xor_b32 v132, v131, v127\n\t"
    "s_waitcnt lgkmcnt(10)\n\t"
    "v_and_b32 v121, v121, v113\n\t"
    "v_perm_b32 v121, v121, v121, s46\n\t"
    "ds_or_b32 v95, v121\n\t"
    "v_xor_b32 v133, 23, v132\n\t"
    "v_lshlrev_b32 v117, v132, v136\n\t"
    "v_and_b32 v132, 7, v132\n\t"
    "v_lshl_or_b32 v117, v136, v133, v117\n\t"
    "v_lshl_add_u32 v132, v132, 1, v128\n\t"
    "ds_or_rtn_b32 v125, v99, v117\n\t"
    "ds_write_b16 v132, v107\n\t"
    "s_waitcnt lgkmcnt(10)\n\t"
    "v_and_b32 v122, v122, v114\n\t"
    "v_perm_b32 v122, v122, v122, s46\n\t"
    "ds_or_b32 v96, v122\n\t"
    "ds_read_b128 v[72:75], v128\n\t"
    "v_and_or_b32 v100, v84, s44, v68\n\t"
    "ds_read_b32 v100, v100\n\t"
    "v_and_or_b32 v101, v85, s44, v68\n\t"
    "ds_read_b32 v101, v101\n\t"
    "v_and_or_b32 v102, v86, s44, v68\n\t"
    "ds_read_b32 v102, v102\n\t"
    "v_and_or_b32 v103, v87, s44, v68\n\t"
    "ds_read_b32 v103, v103\n\t"
    "v_and_or_b32 v104, v88, s44, v68\n\t"
    "ds_read_b32 v104, v104\n\t"
    "v_and_or_b32 v105, v89, s44, v68\n\t"
    "ds_read_b32 v105, v105\n\t"
    "v_and_or_b32 v106, v90, s44, v68\n\t"
    "ds_read_b32 v106, v106\n\t"
    "v_and_or_b32 v107, v91, s44, v68\n\t"
    "ds_read_b32 v107, v107\n\t"
    "s_cmp_lg_u32 %14, 0\n\t"
    "s_cbranch_scc1 L_ahe615_%=\n\t"
    "s_waitcnt vmcnt(0)\n\t"
    "buffer_store_dwordx4 v[148:151], v152, %12, s47 offen offset:1024 sc1\n\t"
    "L_ahe615_%=:\n\t"
    "s_waitcnt vmcnt(3)\n\t"
    "v_lshl_add_u32 v76, v32, 3, s36\n\t"
    "v_lshl_add_u32 v77, v33, 3, s37\n\t"
    "v_lshl_add_u32 v78, v34, 3, s38\n\t"
    "v_lshl_add_u32 v79, v35, 3, s39\n\t"
    "v_lshl_add_u32 v80, v36, 3, s40\n\t"
    "v_lshl_add_u32 v81, v37, 3, s41\n\t"
    "v_lshl_add_u32 v82, v38, 3, s42\n\t"
    "v_lshl_add_u32 v83, v39, 3, s43\n\t"
    "s_waitcnt vmcnt(1)\n\t"
    "v_lshl_add_u32 v48, v48, 3, s36\n\t"
    "v_lshl_add_u32 v49, v49, 3, s37\n\t"
    "v_lshl_add_u32 v50, v50, 3, s38\n\t"
    "v_lshl_add_u32 v51, v51, 3, s39\n\t"
    "v_lshl_or_b32 v56, v49, 16, v48\n\t"
    "v_lshl_or_b32 v57, v51, 16, v50\n\t"
    "s_and_b32 s51, %14, 1\n\t"
    "s_mul_i32 s51, s51, 1016\n\t"
    "s_sub_u32 s50, s49, s51\n\t"
    "s_add_u32 s50, s50, 196608\n\t"
    "buffer_store_dwordx2 v[56:57], v152, %12, s50 offen sc1\n\t"
    "s_lshl_b32 s50, %14, 10\n\t"
    "s_add_u32 s50, s50, 16515072\n\t"
    "s_bitcmp1_b32 %14, 1\n\t"
    "s_cselect_b32 s50, s50, 0x40000000\n\t"
    "buffer_store_dwordx4 v[52:55], v152, %12, s50 offen sc1\n\t"
    "s_mul_i32 s50, %14, 69632\n\t"
    "s_add_u32 s50, s50, 30474240\n\t"
    "v_mov_b32 v60, 0\n\t"
    "v_lshlrev_b32 v61, 3, v152\n\t"
    "s_mov_b64 exec, 0xffffffff\n\t"
    "buffer_store_dword v60, v61, %12, s50 offen sc1\n\t"
    "s_lshl_b32 s50, %14, 7\n\t"
    "s_add_u32 s50, s50, 48245760\n\t"
    "v_lshrrev_b32 v62, 2, v152\n\t"
    "buffer_store_dword v60, v62, %12, s50 offen sc1\n\t"
    "s_mov_b64 exec, -1\n\t"
    "s_cmp_lg_u32 %14, 0\n\t"
    "s_cbranch_scc1 L_ahe657_%=\n\t"
    "buffer_load_dwordx4 v[148:151], v152, %12, s47 offen sc1\n\t"
    "L_ahe657_%=:\n\t"
    "s_waitcnt lgkmcnt(11)\n\t"
    "v_and_b32 v123, v123, v115\n\t"
    "v_perm_b32 v123, v123, v123, s46\n\t"
    "ds_or_b32 v97, v123\n\t"
    "v_and_b32 v124, v124, v116\n\t"
    "v_perm_b32 v124, v124, v124, s46\n\t"
    "ds_or_b32 v98, v124\n\t"
    "v_and_b32 v125, v125, v117\n\t"
    "v_perm_b32 v125, v125, v125, s46\n\t"
    "ds_or_b32 v99, v125\n\t"
    "s_waitcnt lgkmcnt(11)\n\t"
    "buffer_store_dwordx4 v[72:75], v66, %12, s49 offen offset:128 sc1\n\t"
    "v_mov_b32 v129, 0xffff0000\n\t"
    "s_waitcnt lgkmcnt(7)\n\t"
    "v_perm_b32 v100, v100, v84, v126\n\t"
    "v_perm_b32 v101, v101, v85, v126\n\t"
    "v_perm_b32 v102, v102, v86, v126\n\t"
    "v_perm_b32 v103, v103, v87, v126\n\t"
    "s_waitcnt lgkmcnt(3)\n\t"
    "v_perm_b32 v104, v104, v88, v126\n\t"
    "v_perm_b32 v105, v105, v89, v126\n\t"
    "v_perm_b32 v106, v106, v90, v126\n\t"
    "v_perm_b32 v107, v107, v91, v126\n\t"
    "v_max_u32 v108, v100, v101\n\t"
    "v_min_u32 v101, v100, v101\n\t"
    "v_max_u32 v100, v102, v103\n\t"
    "v_min_u32 v103, v102, v103\n\t"
    "v_max_u32 v102, v104, v105\n\t"
    "v_min_u32 v105, v104, v105\n\t"
    "v_max_u32 v104, v106, v107\n\t"
    "v_min_u32 v107, v106, v107\n\t"
    "v_max_u32 v106, v108, v100\n\t"
    "v_min_u32 v100, v108, v100\n\t"
    "v_max_u32 v108, v101, v103\n\t"
    "v_min_u32 v103, v101, v103\n\t"
    "v_max_u32 v101, v102, v104\n\t"
    "v_min_u32 v104, v102, v104\n\t"
    "v_max_u32 v102, v105, v107\n\t"
    "v_min_u32 v107, v105, v107\n\t"
    "v_bfi_b32 v130, v106, 0, v129\n\t"
    "v_cmp_eq_u32 vcc, 0, v130\n\t"
    "v_and_or_b32 v92, v106, s44, v68\n\t"
    "s_nop 0\n\t"
    "v_cndmask_b32 v130, v130, v129, vcc\n\t"
    "v_ffbl_b32 v131, v130\n\t"
    "v_and_b32 v134, 7, v131\n\t"
    "v_lshlrev_b32 v134, v134, s45\n\t"
    "v_xor_b32 v129, v129, v134\n\t"
    "v_xor_b32 v132, v131, v127\n\t"
    "v_bfi_b32 v130, v101, 0, v129\n\t"
    "v_cmp_eq_u32 vcc, 0, v130\n\t"
    "v_and_or_b32 v93, v101, s44, v68\n\t"
    "v_xor_b32 v133, 23, v132\n\t"
    "v_lshlrev_b32 v110, v132, v136\n\t"
    "v_and_b32 v132, 7, v132\n\t"
    "v_lshl_or_b32 v110, v136, v133, v110\n\t"
    "v_lshl_add_u32 v132, v132, 1, v128\n\t"
    "ds_or_rtn_b32 v118, v92, v110\n\t"
    "ds_write_b16 v132, v106\n\t"
    "v_cndmask_b32 v130, v130, v129, vcc\n\t"
    "v_ffbl_b32 v131, v130\n\t"
    "v_and_b32 v134, 7, v131\n\t"
    "v_lshlrev_b32 v134, v134, s45\n\t"
    "v_xor_b32 v129, v129, v134\n\t"
    "v_xor_b32 v132, v131, v127\n\t"
    "v_bfi_b32 v130, v108, 0, v129\n\t"
    "v_cmp_eq_u32 vcc, 0, v130\n\t"
    "v_and_or_b32 v94, v108, s44, v68\n\t"
    "v_xor_b32 v133, 23, v132\n\t"
    "v_lshlrev_b32 v111, v132, v136\n\t"
    "v_and_b32 v132, 7, v132\n\t"
    "v_lshl_or_b32 v111, v136, v133, v111\n\t"
    "v_lshl_add_u32 v132, v132, 1, v128\n\t"
    "ds_or_rtn_b32 v119, v93, v111\n\t"
    "ds_write_b16 v132, v101\n\t"
    "v_cndmask_b32 v130, v130, v129, vcc\n\t"
    "v_ffbl_b32 v131, v130\n\t"
    "v_and_b32 v134, 7, v131\n\t"
    "v_lshlrev_b32 v134, v134, s45\n\t"
    "v_xor_b32 v129, v129, v134\n\t"
    "v_xor_b32 v132, v131, v127\n\t"
    "v_bfi_b32 v130, v100, 0, v129\n\t"
    "v_cmp_eq_u32 vcc, 0, v130\n\t"
    "v_and_or_b32 v95, v100, s44, v68\n\t"
    "v_xor_b32 v133, 23, v132\n\t"
    "v_lshlrev_b32 v112, v132, v136\n\t"
    "v_and_b32 v132, 7, v132\n\t"
    "v_lshl_or_b32 v112, v136, v133, v112\n\t"
    "v_lshl_add_u32 v132, v132, 1, v128\n\t"
    "ds_or_rtn_b32 v120, v94, v112\n\t"
    "ds_write_b16 v132, v108\n\t"
    "v_cndmask_b32 v130, v130, v129, vcc\n\t"
    "v_ffbl_b32 v131, v130\n\t"
    "v_and_b32 v134, 7, v131\n\t"
    "v_lshlrev_b32 v134, v134, s45\n\t"
    "v_xor_b32 v129, v129, v134\n\t"
    "v_xor_b32 v132, v131, v127\n\t"
    "v_bfi_b32 v130, v102, 0, v129\n\t"
    "v_cmp_eq_u32 vcc, 0, v130\n\t"
    "v_and_or_b32 v96, v102, s44, v68\n\t"
    "v_xor_b32 v133, 23, v132\n\t"
    "v_lshlrev_b32 v113, v132, v136\n\t"
    "v_and_b32 v132, 7, v132\n\t"
    "v_lshl_or_b32 v113, v136, v133, v113\n\t"
    "v_lshl_add_u32 v132, v132, 1, v128\n\t"
    "ds_or_rtn_b32 v121, v95, v113\n\t"
    "ds_write_b16 v132, v100\n\t"
    "v_cndmask_b32 v130, v130, v129, vcc\n\t"
    "v_ffbl_b32 v131, v130\n\t"
    "v_and_b32 v134, 7, v131\n\t"
    "v_lshlrev_b32 v134, v134, s45\n\t"
    "v_xor_b32 v129, v129, v134\n\t"
    "v_xor_b32 v132, v131, v127\n\t"
    "s_waitcnt lgkmcnt(7)\n\t"
    "v_and_b32 v118, v118, v110\n\t"
    "v_perm_b32 v118, v118, v118, s46\n\t"
    "ds_or_b32 v92, v118\n\t"
    "v_bfi_b32 v130, v104, 0, v129\n\t"
    "v_cmp_eq_u32 vcc, 0, v130\n\t"
    "v_and_or_b32 v97, v104, s44, v68\n\t"
    "v_xor_b32 v133, 23, v132\n\t"
    "v_lshlrev_b32 v114, v132, v136\n\t"
    "v_and_b32 v132, 7, v132\n\t"
    "v_lshl_or_b32 v114, v136, v133, v114\n\t"
    "v_lshl_add_u32 v132, v132, 1, v128\n\t"
    "ds_or_rtn_b32 v122, v96, v114\n\t"
    "ds_write_b16 v132, v102\n\t"
    "v_cndmask_b32 v130, v130, v129, vcc\n\t"
    "v_ffbl_b32 v131, v130\n\t"
    "v_and_b32 v134, 7, v131\n\t"
    "v_lshlrev_b32 v134, v134, s45\n\t"
    "v_xor_b32 v129, v129, v134\n\t"
    "v_xor_b32 v132, v131, v127\n\t"
    "s_waitcnt lgkmcnt(8)\n\t"
    "v_and_b32 v119, v119, v111\n\t"
    "v_perm_b32 v119, v119, v119, s46\n\t"
    "ds_or_b32 v93, v119\n\t"
    "v_bfi_b32 v130, v103, 0, v129\n\t"
    "v_cmp_eq_u32 vcc, 0, v130\n\t"
    "v_and_or_b32 v98, v103, s44, v68\n\t"
    "v_xor_b32 v133, 23, v132\n\t"
    "v_lshlrev_b32 v115, v132, v136\n\t"
    "v_and_b32 v132, 7, v132\n\t"
    "v_lshl_or_b32 v115, v136, v133, v115\n\t"
    "v_lshl_add_u32 v132, v132, 1, v128\n\t"
    "ds_or_rtn_b32 v123, v97, v115\n\t"
    "ds_write_b16 v132, v104\n\t"
    "v_cndmask_b32 v130, v130, v129, vcc\n\t"
    "v_ffbl_b32 v131, v130\n\t"
    "v_and_b32 v134, 7, v131\n\t"
    "v_lshlrev_b32 v134, v134, s45\n\t"
    "v_xor_b32 v129, v129, v134\n\t"
    "v_xor_b32 v132, v131, v127\n\t"
    "s_waitcnt lgkmcnt(9)\n\t"
    "v_and_b32 v120, v120, v112\n\t"
    "v_perm_b32 v120, v120, v120, s46\n\t"
    "ds_or_b32 v94, v120\n\t"
    "v_bfi_b32 v130, v107, 0, v129\n\t"
    "v_cmp_eq_u32 vcc, 0, v130\n\t"
    "v_and_or_b32 v99, v107, s44, v68\n\t"
    "v_xor_b32 v133, 23, v132\n\t"
    "v_lshlrev_b32 v116, v132, v136\n\t"
    "v_and_b32 v132, 7, v132\n\t"
    "v_lshl_or_b32 v116, v136, v133, v116\n\t"
    "v_lshl_add_u32 v132, v132, 1, v128\n\t"
    "ds_or_rtn_b32 v124, v98, v116\n\t"
    "ds_write_b16 v132, v103\n\t"
    "v_cndmask_b32 v130, v130, v129, vcc\n\t"
    "v_ffbl_b32 v131, v130\n\t"
    "v_xor_b32 v132, v131, v127\n\t"
    "s_waitcnt lgkmcnt(10)\n\t"
    "v_and_b32 v121, v121, v113\n\t"
    "v_perm_b32 v121, v121, v121, s46\n\t"
    "ds_or_b32 v95, v121\n\t"
    "v_xor_b32 v133, 23, v132\n\t"
    "v_lshlrev_b32 v117, v132, v136\n\t"
    "v_and_b32 v132, 7, v132\n\t"
    "v_lshl_or_b32 v117, v136, v133, v117\n\t"
    "v_lshl_add_u32 v132, v132, 1, v128\n\t"
    "ds_or_rtn_b32 v125, v99, v117\n\t"
    "ds_write_b16 v132, v107\n\t"
    "s_waitcnt lgkmcnt(10)\n\t"
    "v_and_b32 v122, v122, v114\n\t"
    "v_perm_b32 v122, v122, v122, s46\n\t"
    "ds_or_b32 v96, v122\n\t"
    "ds_read_b128 v[72:75], v128\n\t"
    "v_and_or_b32 v100, v76, s44, v68\n\t"
    "ds_read_b32 v100, v100\n\t"
    "v_and_or_b32 v101, v77, s44, v68\n\t"
    "ds_read_b32 v101, v101\n\t"
    "v_and_or_b32 v102, v78, s44, v68\n\t"
    "ds_read_b32 v102, v102\n\t"
    "v_and_or_b32 v103, v79, s44, v68\n\t"
    "ds_read_b32 v103, v103\n\t"
    "v_and_or_b32 v104, v80, s44, v68\n\t"
    "ds_read_b32 v104, v104\n\t"
    "v_and_or_b32 v105, v81, s44, v68\n\t"
    "ds_read_b32 v105, v105\n\t"
    "v_and_or_b32 v106, v82, s44, v68\n\t"
    "ds_read_b32 v106, v106\n\t"
    "v_and_or_b32 v107, v83, s44, v68\n\t"
    "ds_read_b32 v107, v107\n\t"
    "s_cmp_lg_u32 %14, 0\n\t"
    "s_cbranch_scc1 L_ahe863_%=\n\t"
    "s_waitcnt vmcnt(0)\n\t"
    "buffer_store_dwordx4 v[148:151], v152, %12, s47 offen offset:1024 sc1\n\t"
    "L_ahe863_%=:\n\t"
    "s_cmp_lg_u32 %14, 0\n\t"
    "s_cbranch_scc1 L_ahe868_%=\n\t"
    "buffer_load_dwordx4 v[148:151], v152, %12, s47 offen sc1\n\t"
    "L_ahe868_%=:\n\t"
    "s_waitcnt lgkmcnt(11)\n\t"
    "v_and_b32 v123, v123, v115\n\t"
    "v_perm_b32 v123, v123, v123, s46\n\t"
    "ds_or_b32 v97, v123\n\t"
    "v_and_b32 v124, v124, v116\n\t"
    "v_perm_b32 v124, v124, v124, s46\n\t"
    "ds_or_b32 v98, v124\n\t"
    "v_and_b32 v125, v125, v117\n\t"
    "v_perm_b32 v125, v125, v125, s46\n\t"
    "ds_or_b32 v99, v125\n\t"
    "s_waitcnt lgkmcnt(11)\n\t"
    "buffer_store_dwordx4 v[72:75], v66, %12, s49 offen offset:192 sc1\n\t"
    "v_mov_b32 v129, 0xffff0000\n\t"
    "s_waitcnt lgkmcnt(7)\n\t"
    "v_perm_b32 v100, v100, v76, v126\n\t"
    "v_perm_b32 v101, v101, v77, v126\n\t"
    "v_perm_b32 v102, v102, v78, v126\n\t"
    "v_perm_b32 v103, v103, v79, v126\n\t"
    "s_waitcnt lgkmcnt(3)\n\t"
    "v_perm_b32 v104, v104, v80, v126\n\t"
    "v_perm_b32 v105, v105, v81, v126\n\t"
    "v_perm_b32 v106, v106, v82, v126\n\t"
    "v_perm_b32 v107, v107, v83, v126\n\t"
    "v_max_u32 v108, v100, v101\n\t"
    "v_min_u32 v101, v100, v101\n\t"
    "v_max_u32 v100, v102, v103\n\t"
    "v_min_u32 v103, v102, v103\n\t"
    "v_max_u32 v102, v104, v105\n\t"
    "v_min_u32 v105, v104, v105\n\t"
    "v_max_u32 v104, v106, v107\n\t"
    "v_min_u32 v107, v106, v107\n\t"
    "v_max_u32 v106, v108, v100\n\t"
    "v_min_u32 v100, v108, v100\n\t"
    "v_max_u32 v108, v101, v103\n\t"
    "v_min_u32 v103, v101, v103\n\t"
    "v_max_u32 v101, v102, v104\n\t"
    "v_min_u32 v104, v102, v104\n\t"
    "v_max_u32 v102, v105, v107\n\t"
    "v_min_u32 v107, v105, v107\n\t"
    "v_bfi_b32 v130, v106, 0, v129\n\t"
    "v_cmp_eq_u32 vcc, 0, v130\n\t"
    "v_and_or_b32 v92, v106, s44, v68\n\t"
    "s_nop 0\n\t"
    "v_cndmask_b32 v130, v130, v129, vcc\n\t"
    "v_ffbl_b32 v131, v130\n\t"
    "v_and_b32 v134, 7, v131\n\t"
    "v_lshlrev_b32 v134, v134, s45\n\t"
    "v_xor_b32 v129, v129, v134\n\t"
    "v_xor_b32 v132, v131, v127\n\t"
    "v_bfi_b32 v130, v101, 0, v129\n\t"
    "v_cmp_eq_u32 vcc, 0, v130\n\t"
    "v_and_or_b32 v93, v101, s44, v68\n\t"
    "v_xor_b32 v133, 23, v132\n\t"
    "v_lshlrev_b32 v110, v132, v136\n\t"
    "v_and_b32 v132, 7, v132\n\t"
    "v_lshl_or_b32 v110, v136, v133, v110\n\t"
    "v_lshl_add_u32 v132, v132, 1, v128\n\t"
    "ds_or_rtn_b32 v118, v92, v110\n\t"
    "ds_write_b16 v132, v106\n\t"
    "v_cndmask_b32 v130, v130, v129, vcc\n\t"
    "v_ffbl_b32 v131, v130\n\t"
    "v_and_b32 v134, 7, v131\n\t"
    "v_lshlrev_b32 v134, v134, s45\n\t"
    "v_xor_b32 v129, v129, v134\n\t"
    "v_xor_b32 v132, v131, v127\n\t"
    "v_bfi_b32 v130, v108, 0, v129\n\t"
    "v_cmp_eq_u32 vcc, 0, v130\n\t"
    "v_and_or_b32 v94, v108, s44, v68\n\t"
    "v_xor_b32 v133, 23, v132\n\t"
    "v_lshlrev_b32 v111, v132, v136\n\t"
    "v_and_b32 v132, 7, v132\n\t"
    "v_lshl_or_b32 v111, v136, v133, v111\n\t"
    "v_lshl_add_u32 v132, v132, 1, v128\n\t"
    "ds_or_rtn_b32 v119, v93, v111\n\t"
    "ds_write_b16 v132, v101\n\t"
    "v_cndmask_b32 v130, v130, v129, vcc\n\t"
    "v_ffbl_b32 v131, v130\n\t"
    "v_and_b32 v134, 7, v131\n\t"
    "v_lshlrev_b32 v134, v134, s45\n\t"
    "v_xor_b32 v129, v129, v134\n\t"
    "v_xor_b32 v132, v131, v127\n\t"
    "v_bfi_b32 v130, v100, 0, v129\n\t"
    "v_cmp_eq_u32 vcc, 0, v130\n\t"
    "v_and_or_b32 v95, v100, s44, v68\n\t"
    "v_xor_b32 v133, 23, v132\n\t"
    "v_lshlrev_b32 v112, v132, v136\n\t"
    "v_and_b32 v132, 7, v132\n\t"
    "v_lshl_or_b32 v112, v136, v133, v112\n\t"
    "v_lshl_add_u32 v132, v132, 1, v128\n\t"
    "ds_or_rtn_b32 v120, v94, v112\n\t"
    "ds_write_b16 v132, v108\n\t"
    "v_cndmask_b32 v130, v130, v129, vcc\n\t"
    "v_ffbl_b32 v131, v130\n\t"
    "v_and_b32 v134, 7, v131\n\t"
    "v_lshlrev_b32 v134, v134, s45\n\t"
    "v_xor_b32 v129, v129, v134\n\t"
    "v_xor_b32 v132, v131, v127\n\t"
    "v_bfi_b32 v130, v102, 0, v129\n\t"
    "v_cmp_eq_u32 vcc, 0, v130\n\t"
    "v_and_or_b32 v96, v102, s44, v68\n\t"
    "v_xor_b32 v133, 23, v132\n\t"
    "v_lshlrev_b32 v113, v132, v136\n\t"
    "v_and_b32 v132, 7, v132\n\t"
    "v_lshl_or_b32 v113, v136, v133, v113\n\t"
    "v_lshl_add_u32 v132, v132, 1, v128\n\t"
    "ds_or_rtn_b32 v121, v95, v113\n\t"
    "ds_write_b16 v132, v100\n\t"
    "v_cndmask_b32 v130, v130, v129, vcc\n\t"
    "v_ffbl_b32 v131, v130\n\t"
    "v_and_b32 v134, 7, v131\n\t"
    "v_lshlrev_b32 v134, v134, s45\n\t"
    "v_xor_b32 v129, v129, v134\n\t"
    "v_xor_b32 v132, v131, v127\n\t"
    "s_waitcnt lgkmcnt(7)\n\t"
    "v_and_b32 v118, v118, v110\n\t"
    "v_perm_b32 v118, v118, v118, s46\n\t"
    "ds_or_b32 v92, v118\n\t"
    "s_waitcnt vmcnt(2)\n\t"
    "ds_write_b32 v71, v136 offset:4\n\t"
    "v_bfi_b32 v130, v104, 0, v129\n\t"
    "v_cmp_eq_u32 vcc, 0, v130\n\t"
    "v_and_or_b32 v97, v104, s44, v68\n\t"
    "v_xor_b32 v133, 23, v132\n\t"
    "v_lshlrev_b32 v114, v132, v136\n\t"
    "v_and_b32 v132, 7, v132\n\t"
    "v_lshl_or_b32 v114, v136, v133, v114\n\t"
    "v_lshl_add_u32 v132, v132, 1, v128\n\t"
    "ds_or_rtn_b32 v122, v96, v114\n\t"
    "ds_write_b16 v132, v102\n\t"
    "v_cndmask_b32 v130, v130, v129, vcc\n\t"
    "v_ffbl_b32 v131, v130\n\t"
    "v_and_b32 v134, 7, v131\n\t"
    "v_lshlrev_b32 v134, v134, s45\n\t"
    "v_xor_b32 v129, v129, v134\n\t"
    "v_xor_b32 v132, v131, v127\n\t"
    "s_waitcnt lgkmcnt(9)\n\t"
    "v_and_b32 v119, v119, v111\n\t"
    "v_perm_b32 v119, v119, v119, s46\n\t"
    "ds_or_b32 v93, v119\n\t"
    "v_bfi_b32 v130, v103, 0, v129\n\t"
    "v_cmp_eq_u32 vcc, 0, v130\n\t"
    "v_and_or_b32 v98, v103, s44, v68\n\t"
    "v_xor_b32 v133, 23, v132\n\t"
    "v_lshlrev_b32 v115, v132, v136\n\t"
    "v_and_b32 v132, 7, v132\n\t"
    "v_lshl_or_b32 v115, v136, v133, v115\n\t"
    "v_lshl_add_u32 v132, v132, 1, v128\n\t"
    "ds_or_rtn_b32 v123, v97, v115\n\t"
    "ds_write_b16 v132, v104\n\t"
    "v_cndmask_b32 v130, v130, v129, vcc\n\t"
    "v_ffbl_b32 v131, v130\n\t"
    "v_and_b32 v134, 7, v131\n\t"
    "v_lshlrev_b32 v134, v134, s45\n\t"
    "v_xor_b32 v129, v129, v134\n\t"
    "v_xor_b32 v132, v131, v127\n\t"
    "s_waitcnt lgkmcnt(10)\n\t"
    "v_and_b32 v120, v120, v112\n\t"
    "v_perm_b32 v120, v120, v120, s46\n\t"
    "ds_or_b32 v94, v120\n\t"
    "v_bfi_b32 v130, v107, 0, v129\n\t"
    "v_cmp_eq_u32 vcc, 0, v130\n\t"
    "v_and_or_b32 v99, v107, s44, v68\n\t"
    "v_xor_b32 v133, 23, v132\n\t"
    "v_lshlrev_b32 v116, v132, v136\n\t"
    "v_and_b32 v132, 7, v132\n\t"
    "v_lshl_or_b32 v116, v136, v133, v116\n\t"
    "v_lshl_add_u32 v132, v132, 1, v128\n\t"
    "ds_or_rtn_b32 v124, v98, v116\n\t"
    "ds_write_b16 v132, v103\n\t"
    "v_cndmask_b32 v130, v130, v129, vcc\n\t"
    "v_ffbl_b32 v131, v130\n\t"
    "v_xor_b32 v132, v131, v127\n\t"
    "s_waitcnt lgkmcnt(11)\n\t"
    "v_and_b32 v121, v121, v113\n\t"
    "v_perm_b32 v121, v121, v121, s46\n\t"
    "ds_or_b32 v95, v121\n\t"
    "v_xor_b32 v133, 23, v132\n\t"
    "v_lshlrev_b32 v117, v132, v136\n\t"
    "v_and_b32 v132, 7, v132\n\t"
    "v_lshl_or_b32 v117, v136, v133, v117\n\t"
    "v_lshl_add_u32 v132, v132, 1, v128\n\t"
    "ds_or_rtn_b32 v125, v99, v117\n\t"
    "ds_write_b16 v132, v107\n\t"
    "s_waitcnt lgkmcnt(10)\n\t"
    "v_and_b32 v122, v122, v114\n\t"
    "v_perm_b32 v122, v122, v122, s46\n\t"
    "ds_or_b32 v96, v122\n\t"
    "ds_read_b128 v[72:75], v128\n\t"
    "s_waitcnt lgkmcnt(3)\n\t"
    "v_and_b32 v123, v123, v115\n\t"
    "v_perm_b32 v123, v123, v123, s46\n\t"
    "ds_or_b32 v97, v123\n\t"
    "v_and_b32 v124, v124, v116\n\t"
    "v_perm_b32 v124, v124, v124, s46\n\t"
    "ds_or_b32 v98, v124\n\t"
    "v_and_b32 v125, v125, v117\n\t"
    "v_perm_b32 v125, v125, v125, s46\n\t"
    "ds_or_b32 v99, v125\n\t"
    "s_waitcnt lgkmcnt(3)\n\t"
    "buffer_store_dwordx4 v[72:75], v66, %12, s49 offen offset:256 sc1\n\t"
    "v_mov_b32 v134, 1036\n\t"
    "ds_write_b32 v134, v136\n\t"
    "s_cmp_lg_u32 %14, 0\n\t"
    "s_cbranch_scc1 L_ahsend_%=\n\t"
    "s_waitcnt vmcnt(0)\n\t"
    "buffer_store_dwordx4 v[148:151], v152, %12, s47 offen offset:1024 sc1\n\t"
    "s_setprio 0\n\t"
    "v_and_b32 v132, 0x70, v152\n\t"
    "v_lshlrev_b32 v132, 3, v132\n\t"
    "s_mov_b32 s51, 1048576\n\t"
    "L_ahs2_%=:\n\t"
    "s_sleep 8\n\t"
    "buffer_load_dword v71, v132, %12, s48 offen offset:1024 sc1\n\t"
    "buffer_load_dwordx4 v[148:151], v152, %12, s47 offen sc1\n\t"
    "s_sub_u32 s51, s51, 1\n\t"
    "s_cmp_eq_u32 s51, 0\n\t"
    "s_cbranch_scc1 L_ahsend_%=\n\t"
    "s_waitcnt vmcnt(0)\n\t"
    "buffer_store_dwordx4 v[148:151], v152, %12, s47 offen offset:1024 sc1\n\t"
    "v_cmp_gt_u32 vcc, 32, v71\n\t"
    "s_cbranch_vccnz L_ahs2_%=\n\t"
    "L_ahsend_%=:\n\t"
    "s_setprio 0\n\t"
    "s_and_b32 s46, %16, 3\n\t"
    "s_mul_i32 s49, s46, 1040\n\t"
    "s_add_u32 s49, s49, 2064\n\t"
    "s_lshl_b32 s46, s46, 3\n\t"
    "s_lshr_b32 s47, 1076763402, s46\n\t"
    "s_and_b32 s47, s47, 0xff\n\t"
    "s_lshl_b32 s48, s47, 16\n\t"
    "s_lshr_b32 s47, 773523968, s46\n\t"
    "s_and_b32 s47, s47, 0xff\n\t"
    "s_add_u32 s48, s48, s47\n\t"
    "s_lshr_b32 s47, %16, 2\n\t"
    "s_add_u32 s47, s47, 0x200\n\t"
    "s_cmp_eq_u32 s46, 0\n\t"
    "s_cselect_b32 s47, 0x100, s47\n\t"
    "s_add_u32 s48, s48, s47\n\t"
    "v_bfe_u32 v154, v147, 2, 2\n\t"
    "v_and_b32 v155, 3, v147\n\t"
    "v_mul_u32_u24 v154, 25, v154\n\t"
    "v_mad_u32_u24 v154, v155, 6, v154\n\t"
    "v_lshlrev_b32 v153, 2, v154\n\t"
    "s_and_b32 s53, s48, 0xff\n\t"
    "s_bfe_u32 s55, s48, 0x80008\n\t"
    "s_add_u32 s54, s53, s55\n\t"
    "s_add_u32 s55, s54, s55\n\t"
    "s_lshr_b32 s64, s48, 16\n\t"
    "s_mul_i32 s66, s53, 400\n\t"
    "buffer_load_dwordx3 v[128:130], v153, %0, s66 offen\n\t"
    "buffer_load_dwordx4 v[132:135], v153, %0, s66 offen offset:12\n\t"
    "s_mul_i32 s66, s54, 400\n\t"
    "buffer_load_dwordx3 v[136:138], v153, %0, s66 offen\n\t"
    "buffer_load_dwordx4 v[140:143], v153, %0, s66 offen offset:12\n\t"
    "v_cmp_eq_u32 s[62:63], 0, v155\n\t"
    "v_and_b32 v151, 48, v147\n\t"
    "v_or_b32 v155, 0x100000, v151\n\t"
    "v_cndmask_b32 v152, v155, v151, s[62:63]\n\t"
    "v_lshlrev_b32 v117, 20, v147\n\t"
    "v_add_u32 v117, 1024, v117\n\t"
    "v_mov_b32 v118, 1\n\t"
    "v_lshlrev_b32 v95, 4, v147\n\t"
    "v_add_u32 v95, 140288, v95\n\t"
    "s_call_b64 s[98:99], L_setup_%=\n\t"
    "L_await_%=:\n\t"
    "L_asp9_%=:\n\t"
    "ds_read_b32 v118, v117 offset:8\n\t"
    "ds_read_b32 v119, v117 offset:4\n\t"
    "s_waitcnt lgkmcnt(0)\n\t"
    "v_readfirstlane_b32 s46, v119\n\t"
    "s_nop 0\n\t"
    "s_cmp_ge_u32 s46, 4\n\t"
    "s_cbranch_scc1 L_ago9_%=\n\t"
    "s_sleep 1\n\t"
    "s_branch L_asp9_%=\n\t"
    "L_ago9_%=:\n\t"
    "v_readfirstlane_b32 s47, v118\n\t"
    "ds_read_b128 v[0:3], v95 offset:0\n\t"
    "ds_read_b128 v[4:7], v95 offset:1024\n\t"
    "ds_read_b128 v[8:11], v95 offset:2048\n\t"
    "ds_read_b128 v[12:15], v95 offset:3072\n\t"
    "ds_read_b128 v[16:19], v95 offset:4096\n\t"
    "ds_read_b128 v[20:23], v95 offset:5120\n\t"
    "ds_read_b128 v[24:27], v95 offset:6144\n\t"
    "ds_read_b128 v[28:31], v95 offset:7168\n\t"
    "ds_read_b128 v[32:35], v95 offset:8192\n\t"
    "ds_read_b128 v[36:39], v95 offset:9216\n\t"
    "ds_read_b128 v[40:43], v95 offset:10240\n\t"
    "ds_read_b128 v[44:47], v95 offset:11264\n\t"
    "ds_read_b128 v[48:51], v95 offset:12288\n\t"
    "ds_read_b128 v[52:55], v95 offset:13312\n\t"
    "ds_read_b128 v[56:59], v95 offset:14336\n\t"
    "ds_read_b128 v[60:63], v95 offset:15360\n\t"
    "ds_read_b128 v[64:67], v95 offset:16384\n\t"
    "ds_read_b128 v[68:71], v95 offset:17408\n\t"
    "ds_read_b128 v[72:75], v95 offset:18432\n\t"
    "ds_read_b128 v[76:79], v95 offset:19456\n\t"
    "ds_read_b128 v[80:83], v95 offset:20480\n\t"
    "ds_read_b128 v[84:87], v95 offset:21504\n\t"
    "ds_read_b128 v[88:91], v95 offset:22528\n\t"
    "s_waitcnt vmcnt(0)\n\t"
    "s_cmp_lg_u32 %16, 1\n\t"
    "s_cbranch_scc1 L_anh3_%=\n\t"
    "v_mov_b32 v119, 4\n\t"
    "ds_write_b32 v117, v119 offset:12\n\t"
    "s_lshl_b32 s65, %14, 12\n\t"
    "s_add_u32 s65, s65, 16777216\n\t"
    "s_waitcnt lgkmcnt(15)\n\t"
    "buffer_store_dwordx4 v[0:3], v127, %12, s65 offen offset:0 sc1\n\t"
    "buffer_store_dwordx4 v[4:7], v127, %12, s65 offen offset:1024 sc1\n\t"
    "buffer_store_dwordx4 v[8:11], v127, %12, s65 offen offset:2048 sc1\n\t"
    "buffer_store_dwordx4 v[12:15], v127, %12, s65 offen offset:3072 sc1\n\t"
    "L_anh3_%=:\n\t"
    "s_cmp_ge_u32 s47, 6\n\t"
    "s_cbranch_scc1 L_adok_%=\n\t"
    "L_awd_%=:\n\t"
    "L_asp10_%=:\n\t"
    "ds_read_b32 v119, v117 offset:8\n\t"
    "s_waitcnt lgkmcnt(0)\n\t"
    "v_readfirstlane_b32 s46, v119\n\t"
    "s_nop 0\n\t"
    "s_cmp_ge_u32 s46, 6\n\t"
    "s_cbranch_scc1 L_ago10_%=\n\t"
    "s_sleep 1\n\t"
    "s_branch L_asp10_%=\n\t"
    "L_ago10_%=:\n\t"
    "L_adok_%=:\n\t"
    "L_amain_%=:\n\t"
    "s_mov_b32 s68, 130\n\t"
    "v_mad_u32_u24 v128, v128, s68, v152\n\t"
    "v_and_b32 v128, -16, v128\n\t"
    "ds_read_b128 v[156:159], v128 offset:0\n\t"
    "ds_read_b128 v[160:163], v128 offset:64\n\t"
    "v_mad_u32_u24 v129, v129, s68, v151\n\t"
    "v_and_b32 v129, -16, v129\n\t"
    "ds_read_b128 v[164:167], v129 offset:0\n\t"
    "ds_read_b128 v[168:171], v129 offset:64\n\t"
    "v_mad_u32_u24 v130, v130, s68, v151\n\t"
    "v_and_b32 v130, -16, v130\n\t"
    "ds_read_b128 v[172:175], v130 offset:0\n\t"
    "ds_read_b128 v[176:179], v130 offset:64\n\t"
    "s_mul_i32 s66, s55, 400\n\t"
    "buffer_load_dwordx3 v[128:130], v153, %0, s66 offen\n\t"
    "v_mad_u32_u24 v132, v132, s68, v151\n\t"
    "v_and_b32 v132, -16, v132\n\t"
    "ds_read_b128 v[180:183], v132 offset:0\n\t"
    "ds_read_b128 v[184:187], v132 offset:64\n\t"
    "v_mad_u32_u24 v133, v133, s68, v151\n\t"
    "v_and_b32 v133, -16, v133\n\t"
    "ds_read_b128 v[188:191], v133 offset:0\n\t"
    "ds_read_b128 v[192:195], v133 offset:64\n\t"
    "v_mad_u32_u24 v134, v134, s68, v151\n\t"
    "v_and_b32 v134, -16, v134\n\t"
    "ds_read_b128 v[196:199], v134 offset:0\n\t"
    "ds_read_b128 v[200:203], v134 offset:64\n\t"
    "v_mad_u32_u24 v135, v135, s68, v151\n\t"
    "v_and_b32 v135, -16, v135\n\t"
    "ds_read_b128 v[204:207], v135 offset:0\n\t"
    "ds_read_b128 v[208:211], v135 offset:64\n\t"
    "v_mov_b32 v150, 1\n\t"
    "v_lshlrev_b32 v149, 20, v147\n\t"
    "v_add_u32 v149, s49, v149\n\t"
    "v_lshlrev_b32 v146, 2, v147\n\t"
    "s_mov_b32 s52, s53\n\t"
    "s_lshl_b32 s50, %14, 14\n\t"
    "s_add_u32 s50, s50, 26214400\n\t"
    "L_apnn_%=:\n\t"
    "s_mul_i32 s69, s52, 256\n\t"
    "s_add_u32 s69, s69, s50\n\t"
    "s_waitcnt lgkmcnt(8)\n\t"
    "v_mfma_f32_16x16x32_bf16 v[92:95], v[156:159], v[156:159], 0\n\t"
    "v_fmac_f32 v145, v108, v72\n\t"
    "v_fmac_f32 v144, v109, v73\n\t"
    "ds_add_rtn_u32 v148, v149, v150\n\t"
    "v_mfma_f32_16x16x32_bf16 v[92:95], v[160:163], v[160:163], v[92:95]\n\t"
    "v_fmac_f32 v145, v110, v74\n\t"
    "v_fmac_f32 v144, v111, v75\n\t"
    "buffer_load_dwordx4 v[132:135], v153, %0, s66 offen offset:12\n\t"
    "v_mfma_f32_16x16x32_bf16 v[96:99], v[164:167], v[164:167], 0\n\t"
    "v_fmac_f32 v145, v112, v76\n\t"
    "v_fmac_f32 v144, v113, v77\n\t"
    "v_mfma_f32_16x16x32_bf16 v[96:99], v[168:171], v[168:171], v[96:99]\n\t"
    "v_fmac_f32 v145, v114, v78\n\t"
    "v_fmac_f32 v144, v115, v79\n\t"
    "v_mfma_f32_16x16x32_bf16 v[100:103], v[164:167], v[172:175], 0\n\t"
    "v_fmac_f32 v145, v116, v80\n\t"
    "v_fmac_f32 v144, v117, v81\n\t"
    "v_mfma_f32_16x16x32_bf16 v[100:103], v[168:171], v[176:179], v[100:103]\n\t"
    "v_fmac_f32 v145, v118, v82\n\t"
    "v_fmac_f32 v144, v119, v83\n\t"
    "s_waitcnt vmcnt(4)\n\t"
    "s_cmp_ge_u32 s55, s64\n\t"
    "s_cbranch_scc1 L_ahk00_%=\n\t"
    "L_ahb00_%=:\n\t"
    "v_mfma_f32_16x16x32_bf16 v[104:107], v[172:175], v[172:175], 0\n\t"
    "v_mad_u32_u24 v136, v136, s68, v152\n\t"
    "v_and_b32 v136, -16, v136\n\t"
    "v_mfma_f32_16x16x32_bf16 v[104:107], v[176:179], v[176:179], v[104:107]\n\t"
    "v_add_f32 v154, v144, v145\n\t"
    "buffer_store_dword v154, v146, %12, s69 offen sc1\n\t"
    "v_mul_f32 v144, v92, v84\n\t"
    "v_mfma_f32_16x16x32_bf16 v[108:111], v[156:159], v[164:167], 0\n\t"
    "v_mul_f32 v145, v96, v0\n\t"
    "v_fmac_f32 v144, v97, v1\n\t"
    "v_mfma_f32_16x16x32_bf16 v[108:111], v[160:163], v[168:171], v[108:111]\n\t"
    "v_fmac_f32 v145, v98, v2\n\t"
    "v_fmac_f32 v144, v99, v3\n\t"
    "s_waitcnt lgkmcnt(7)\n\t"
    "v_mfma_f32_16x16x32_bf16 v[112:115], v[164:167], v[180:183], 0\n\t"
    "v_fmac_f32 v145, v100, v4\n\t"
    "v_fmac_f32 v144, v101, v5\n\t"
    "v_mfma_f32_16x16x32_bf16 v[112:115], v[168:171], v[184:187], v[112:115]\n\t"
    "v_fmac_f32 v145, v102, v6\n\t"
    "v_fmac_f32 v144, v103, v7\n\t"
    "v_mfma_f32_16x16x32_bf16 v[116:119], v[172:175], v[180:183], 0\n\t"
    "v_fmac_f32 v145, v104, v8\n\t"
    "v_fmac_f32 v144, v105, v9\n\t"
    "v_mfma_f32_16x16x32_bf16 v[116:119], v[176:179], v[184:187], v[116:119]\n\t"
    "v_fmac_f32 v145, v106, v10\n\t"
    "v_fmac_f32 v144, v107, v11\n\t"
    "ds_read_b128 v[212:215], v136 offset:0\n\t"
    "ds_read_b128 v[216:219], v136 offset:64\n\t"
    "v_mfma_f32_16x16x32_bf16 v[92:95], v[180:183], v[180:183], 0\n\t"
    "v_mad_u32_u24 v137, v137, s68, v151\n\t"
    "v_and_b32 v137, -16, v137\n\t"
    "v_mfma_f32_16x16x32_bf16 v[92:95], v[184:187], v[184:187], v[92:95]\n\t"
    "v_fmac_f32 v145, v108, v85\n\t"
    "v_mad_u32_u24 v138, v138, s68, v151\n\t"
    "v_mfma_f32_16x16x32_bf16 v[96:99], v[156:159], v[172:175], 0\n\t"
    "v_fmac_f32 v144, v112, v12\n\t"
    "v_fmac_f32 v145, v113, v13\n\t"
    "v_mfma_f32_16x16x32_bf16 v[96:99], v[160:163], v[176:179], v[96:99]\n\t"
    "v_fmac_f32 v144, v114, v14\n\t"
    "v_fmac_f32 v145, v115, v15\n\t"
    "s_waitcnt lgkmcnt(7)\n\t"
    "v_mfma_f32_16x16x32_bf16 v[100:103], v[164:167], v[188:191], 0\n\t"
    "v_and_b32 v138, -16, v138\n\t"
    "v_fmac_f32 v144, v116, v16\n\t"
    "v_mfma_f32_16x16x32_bf16 v[100:103], v[168:171], v[192:195], v[100:103]\n\t"
    "v_fmac_f32 v145, v117, v17\n\t"
    "v_fmac_f32 v144, v118, v18\n\t"
    "v_mfma_f32_16x16x32_bf16 v[104:107], v[172:175], v[188:191], 0\n\t"
    "v_fmac_f32 v145, v119, v19\n\t"
    "v_fmac_f32 v144, v92, v20\n\t"
    "v_mfma_f32_16x16x32_bf16 v[104:107], v[176:179], v[192:195], v[104:107]\n\t"
    "v_fmac_f32 v145, v93, v21\n\t"
    "v_fmac_f32 v144, v94, v22\n\t"
    "s_waitcnt lgkmcnt(5)\n\t"
    "v_mfma_f32_16x16x32_bf16 v[108:111], v[164:167], v[196:199], 0\n\t"
    "v_fmac_f32 v145, v95, v23\n\t"
    "v_fmac_f32 v144, v96, v86\n\t"
    "v_mfma_f32_16x16x32_bf16 v[108:111], v[168:171], v[200:203], v[108:111]\n\t"
    "v_mfma_f32_16x16x32_bf16 v[112:115], v[156:159], v[180:183], 0\n\t"
    "v_fmac_f32 v145, v100, v24\n\t"
    "v_fmac_f32 v144, v101, v25\n\t"
    "v_mfma_f32_16x16x32_bf16 v[112:115], v[160:163], v[184:187], v[112:115]\n\t"
    "v_fmac_f32 v145, v102, v26\n\t"
    "v_fmac_f32 v144, v103, v27\n\t"
    "s_waitcnt lgkmcnt(2)\n\t"
    "v_mfma_f32_16x16x32_bf16 v[116:119], v[164:167], v[204:207], 0\n\t"
    "v_readfirstlane_b32 s60, v148\n\t"
    "v_fmac_f32 v145, v104, v28\n\t"
    "v_mfma_f32_16x16x32_bf16 v[116:119], v[168:171], v[208:211], v[116:119]\n\t"
    "v_fmac_f32 v144, v105, v29\n\t"
    "v_fmac_f32 v145, v106, v30\n\t"
    "ds_read_b128 v[164:167], v137 offset:0\n\t"
    "ds_read_b128 v[168:171], v137 offset:64\n\t"
    "v_mfma_f32_16x16x32_bf16 v[92:95], v[172:175], v[196:199], 0\n\t"
    "v_fmac_f32 v144, v107, v31\n\t"
    "v_fmac_f32 v145, v108, v32\n\t"
    "v_mfma_f32_16x16x32_bf16 v[92:95], v[176:179], v[200:203], v[92:95]\n\t"
    "v_fmac_f32 v144, v109, v33\n\t"
    "v_fmac_f32 v145, v110, v34\n\t"
    "s_waitcnt vmcnt(4)\n\t"
    "s_cmp_ge_u32 s55, s64\n\t"
    "s_cbranch_scc1 L_ahk10_%=\n\t"
    "L_ahb10_%=:\n\t"
    "v_mfma_f32_16x16x32_bf16 v[96:99], v[172:175], v[204:207], 0\n\t"
    "v_mad_u32_u24 v140, v140, s68, v151\n\t"
    "v_and_b32 v140, -16, v140\n\t"
    "v_mfma_f32_16x16x32_bf16 v[96:99], v[176:179], v[208:211], v[96:99]\n\t"
    "v_fmac_f32 v144, v111, v35\n\t"
    "v_fmac_f32 v145, v112, v87\n\t"
    "ds_read_b128 v[172:175], v138 offset:0\n\t"
    "ds_read_b128 v[176:179], v138 offset:64\n\t"
    "s_mul_i32 s66, s60, 400\n\t"
    "v_mfma_f32_16x16x32_bf16 v[100:103], v[156:159], v[188:191], 0\n\t"
    "v_fmac_f32 v144, v116, v36\n\t"
    "v_fmac_f32 v145, v117, v37\n\t"
    "v_mfma_f32_16x16x32_bf16 v[100:103], v[160:163], v[192:195], v[100:103]\n\t"
    "v_fmac_f32 v144, v118, v38\n\t"
    "v_fmac_f32 v145, v119, v39\n\t"
    "buffer_load_dwordx3 v[136:138], v153, %0, s66 offen\n\t"
    "v_mfma_f32_16x16x32_bf16 v[104:107], v[180:183], v[188:191], 0\n\t"
    "v_mad_u32_u24 v141, v141, s68, v151\n\t"
    "v_and_b32 v141, -16, v141\n\t"
    "v_mfma_f32_16x16x32_bf16 v[104:107], v[184:187], v[192:195], v[104:107]\n\t"
    "v_fmac_f32 v144, v92, v40\n\t"
    "v_fmac_f32 v145, v93, v41\n\t"
    "v_mfma_f32_16x16x32_bf16 v[108:111], v[180:183], v[196:199], 0\n\t"
    "v_fmac_f32 v144, v94, v42\n\t"
    "v_fmac_f32 v145, v95, v43\n\t"
    "v_mfma_f32_16x16x32_bf16 v[108:111], v[184:187], v[200:203], v[108:111]\n\t"
    "v_fmac_f32 v144, v96, v44\n\t"
    "v_fmac_f32 v145, v97, v45\n\t"
    "v_mfma_f32_16x16x32_bf16 v[112:115], v[180:183], v[204:207], 0\n\t"
    "v_fmac_f32 v144, v98, v46\n\t"
    "v_fmac_f32 v145, v99, v47\n\t"
    "v_mfma_f32_16x16x32_bf16 v[112:115], v[184:187], v[208:211], v[112:115]\n\t"
    "v_fmac_f32 v144, v100, v88\n\t"
    "v_mad_u32_u24 v142, v142, s68, v151\n\t"
    "ds_read_b128 v[180:183], v140 offset:0\n\t"
    "ds_read_b128 v[184:187], v140 offset:64\n\t"
    "v_mfma_f32_16x16x32_bf16 v[116:119], v[156:159], v[196:199], 0\n\t"
    "v_and_b32 v142, -16, v142\n\t"
    "v_fmac_f32 v145, v104, v48\n\t"
    "v_mfma_f32_16x16x32_bf16 v[116:119], v[160:163], v[200:203], v[116:119]\n\t"
    "v_fmac_f32 v144, v105, v49\n\t"
    "v_fmac_f32 v145, v106, v50\n\t"
    "v_mfma_f32_16x16x32_bf16 v[92:95], v[188:191], v[188:191], 0\n\t"
    "v_fmac_f32 v144, v107, v51\n\t"
    "v_mad_u32_u24 v143, v143, s68, v151\n\t"
    "v_mfma_f32_16x16x32_bf16 v[92:95], v[192:195], v[192:195], v[92:95]\n\t"
    "v_and_b32 v143, -16, v143\n\t"
    "v_fmac_f32 v145, v108, v52\n\t"
    "v_mfma_f32_16x16x32_bf16 v[96:99], v[188:191], v[196:199], 0\n\t"
    "v_fmac_f32 v144, v109, v53\n\t"
    "v_fmac_f32 v145, v110, v54\n\t"
    "v_mfma_f32_16x16x32_bf16 v[96:99], v[192:195], v[200:203], v[96:99]\n\t"
    "v_fmac_f32 v144, v111, v55\n\t"
    "v_fmac_f32 v145, v112, v56\n\t"
    "v_mfma_f32_16x16x32_bf16 v[100:103], v[188:191], v[204:207], 0\n\t"
    "v_fmac_f32 v144, v113, v57\n\t"
    "v_fmac_f32 v145, v114, v58\n\t"
    "v_mfma_f32_16x16x32_bf16 v[100:103], v[192:195], v[208:211], v[100:103]\n\t"
    "v_fmac_f32 v144, v115, v59\n\t"
    "v_fmac_f32 v145, v116, v89\n\t"
    "ds_read_b128 v[188:191], v141 offset:0\n\t"
    "ds_read_b128 v[192:195], v141 offset:64\n\t"
    "v_mfma_f32_16x16x32_bf16 v[104:107], v[156:159], v[204:207], 0\n\t"
    "v_fmac_f32 v144, v92, v60\n\t"
    "v_fmac_f32 v145, v93, v61\n\t"
    "v_mfma_f32_16x16x32_bf16 v[104:107], v[160:163], v[208:211], v[104:107]\n\t"
    "v_fmac_f32 v144, v94, v62\n\t"
    "v_fmac_f32 v145, v95, v63\n\t"
    "v_mfma_f32_16x16x32_bf16 v[108:111], v[196:199], v[196:199], 0\n\t"
    "v_fmac_f32 v144, v96, v64\n\t"
    "v_fmac_f32 v145, v97, v65\n\t"
    "v_mfma_f32_16x16x32_bf16 v[108:111], v[200:203], v[200:203], v[108:111]\n\t"
    "v_fmac_f32 v144, v98, v66\n\t"
    "v_fmac_f32 v145, v99, v67\n\t"
    "v_mfma_f32_16x16x32_bf16 v[112:115], v[196:199], v[204:207], 0\n\t"
    "v_fmac_f32 v144, v100, v68\n\t"
    "v_fmac_f32 v145, v101, v69\n\t"
    "v_mfma_f32_16x16x32_bf16 v[112:115], v[200:203], v[208:211], v[112:115]\n\t"
    "v_fmac_f32 v144, v102, v70\n\t"
    "v_fmac_f32 v145, v103, v71\n\t"
    "ds_read_b128 v[196:199], v142 offset:0\n\t"
    "ds_read_b128 v[200:203], v142 offset:64\n\t"
    "v_mfma_f32_16x16x32_bf16 v[116:119], v[204:207], v[204:207], 0\n\t"
    "v_fmac_f32 v144, v104, v90\n\t"
    "v_mfma_f32_16x16x32_bf16 v[116:119], v[208:211], v[208:211], v[116:119]\n\t"
    "ds_read_b128 v[204:207], v143 offset:0\n\t"
    "ds_read_b128 v[208:211], v143 offset:64\n\t"
    "s_mov_b32 s52, s53\n\t"
    "s_mov_b32 s53, s54\n\t"
    "s_mov_b32 s54, s55\n\t"
    "s_mov_b32 s55, s60\n\t"
    "s_cmp_lt_u32 s53, s64\n\t"
    "s_cbranch_scc0 L_adone_%=\n\t"
    "s_mul_i32 s69, s52, 256\n\t"
    "s_add_u32 s69, s69, s50\n\t"
    "s_waitcnt lgkmcnt(8)\n\t"
    "v_mfma_f32_16x16x32_bf16 v[92:95], v[212:215], v[212:215], 0\n\t"
    "v_fmac_f32 v145, v108, v72\n\t"
    "v_fmac_f32 v144, v109, v73\n\t"
    "ds_add_rtn_u32 v148, v149, v150\n\t"
    "v_mfma_f32_16x16x32_bf16 v[92:95], v[216:219], v[216:219], v[92:95]\n\t"
    "v_fmac_f32 v145, v110, v74\n\t"
    "v_fmac_f32 v144, v111, v75\n\t"
    "buffer_load_dwordx4 v[140:143], v153, %0, s66 offen offset:12\n\t"
    "v_mfma_f32_16x16x32_bf16 v[96:99], v[164:167], v[164:167], 0\n\t"
    "v_fmac_f32 v145, v112, v76\n\t"
    "v_fmac_f32 v144, v113, v77\n\t"
    "v_mfma_f32_16x16x32_bf16 v[96:99], v[168:171], v[168:171], v[96:99]\n\t"
    "v_fmac_f32 v145, v114, v78\n\t"
    "v_fmac_f32 v144, v115, v79\n\t"
    "v_mfma_f32_16x16x32_bf16 v[100:103], v[164:167], v[172:175], 0\n\t"
    "v_fmac_f32 v145, v116, v80\n\t"
    "v_fmac_f32 v144, v117, v81\n\t"
    "v_mfma_f32_16x16x32_bf16 v[100:103], v[168:171], v[176:179], v[100:103]\n\t"
    "v_fmac_f32 v145, v118, v82\n\t"
    "v_fmac_f32 v144, v119, v83\n\t"
    "s_waitcnt vmcnt(4)\n\t"
    "s_cmp_ge_u32 s55, s64\n\t"
    "s_cbranch_scc1 L_ahk01_%=\n\t"
    "L_ahb01_%=:\n\t"
    "v_mfma_f32_16x16x32_bf16 v[104:107], v[172:175], v[172:175], 0\n\t"
    "v_mad_u32_u24 v128, v128, s68, v152\n\t"
    "v_and_b32 v128, -16, v128\n\t"
    "v_mfma_f32_16x16x32_bf16 v[104:107], v[176:179], v[176:179], v[104:107]\n\t"
    "v_add_f32 v154, v144, v145\n\t"
    "buffer_store_dword v154, v146, %12, s69 offen sc1\n\t"
    "v_mul_f32 v144, v92, v84\n\t"
    "v_mfma_f32_16x16x32_bf16 v[108:111], v[212:215], v[164:167], 0\n\t"
    "v_mul_f32 v145, v96, v0\n\t"
    "v_fmac_f32 v144, v97, v1\n\t"
    "v_mfma_f32_16x16x32_bf16 v[108:111], v[216:219], v[168:171], v[108:111]\n\t"
    "v_fmac_f32 v145, v98, v2\n\t"
    "v_fmac_f32 v144, v99, v3\n\t"
    "s_waitcnt lgkmcnt(7)\n\t"
    "v_mfma_f32_16x16x32_bf16 v[112:115], v[164:167], v[180:183], 0\n\t"
    "v_fmac_f32 v145, v100, v4\n\t"
    "v_fmac_f32 v144, v101, v5\n\t"
    "v_mfma_f32_16x16x32_bf16 v[112:115], v[168:171], v[184:187], v[112:115]\n\t"
    "v_fmac_f32 v145, v102, v6\n\t"
    "v_fmac_f32 v144, v103, v7\n\t"
    "v_mfma_f32_16x16x32_bf16 v[116:119], v[172:175], v[180:183], 0\n\t"
    "v_fmac_f32 v145, v104, v8\n\t"
    "v_fmac_f32 v144, v105, v9\n\t"
    "v_mfma_f32_16x16x32_bf16 v[116:119], v[176:179], v[184:187], v[116:119]\n\t"
    "v_fmac_f32 v145, v106, v10\n\t"
    "v_fmac_f32 v144, v107, v11\n\t"
    "ds_read_b128 v[156:159], v128 offset:0\n\t"
    "ds_read_b128 v[160:163], v128 offset:64\n\t"
    "v_mfma_f32_16x16x32_bf16 v[92:95], v[180:183], v[180:183], 0\n\t"
    "v_mad_u32_u24 v129, v129, s68, v151\n\t"
    "v_and_b32 v129, -16, v129\n\t"
    "v_mfma_f32_16x16x32_bf16 v[92:95], v[184:187], v[184:187], v[92:95]\n\t"
    "v_fmac_f32 v145, v108, v85\n\t"
    "v_mad_u32_u24 v130, v130, s68, v151\n\t"
    "v_mfma_f32_16x16x32_bf16 v[96:99], v[212:215], v[172:175], 0\n\t"
    "v_fmac_f32 v144, v112, v12\n\t"
    "v_fmac_f32 v145, v113, v13\n\t"
    "v_mfma_f32_16x16x32_bf16 v[96:99], v[216:219], v[176:179], v[96:99]\n\t"
    "v_fmac_f32 v144, v114, v14\n\t"
    "v_fmac_f32 v145, v115, v15\n\t"
    "s_waitcnt lgkmcnt(7)\n\t"
    "v_mfma_f32_16x16x32_bf16 v[100:103], v[164:167], v[188:191], 0\n\t"
    "v_and_b32 v130, -16, v130\n\t"
    "v_fmac_f32 v144, v116, v16\n\t"
    "v_mfma_f32_16x16x32_bf16 v[100:103], v[168:171], v[192:195], v[100:103]\n\t"
    "v_fmac_f32 v145, v117, v17\n\t"
    "v_fmac_f32 v144, v118, v18\n\t"
    "v_mfma_f32_16x16x32_bf16 v[104:107], v[172:175], v[188:191], 0\n\t"
    "v_fmac_f32 v145, v119, v19\n\t"
    "v_fmac_f32 v144, v92, v20\n\t"
    "v_mfma_f32_16x16x32_bf16 v[104:107], v[176:179], v[192:195], v[104:107]\n\t"
    "v_fmac_f32 v145, v93, v21\n\t"
    "v_fmac_f32 v144, v94, v22\n\t"
    "s_waitcnt lgkmcnt(5)\n\t"
    "v_mfma_f32_16x16x32_bf16 v[108:111], v[164:167], v[196:199], 0\n\t"
    "v_fmac_f32 v145, v95, v23\n\t"
    "v_fmac_f32 v144, v96, v86\n\t"
    "v_mfma_f32_16x16x32_bf16 v[108:111], v[168:171], v[200:203], v[108:111]\n\t"
    "v_mfma_f32_16x16x32_bf16 v[112:115], v[212:215], v[180:183], 0\n\t"
    "v_fmac_f32 v145, v100, v24\n\t"
    "v_fmac_f32 v144, v101, v25\n\t"
    "v_mfma_f32_16x16x32_bf16 v[112:115], v[216:219], v[184:187], v[112:115]\n\t"
    "v_fmac_f32 v145, v102, v26\n\t"
    "v_fmac_f32 v144, v103, v27\n\t"
    "s_waitcnt lgkmcnt(2)\n\t"
    "v_mfma_f32_16x16x32_bf16 v[116:119], v[164:167], v[204:207], 0\n\t"
    "v_readfirstlane_b32 s60, v148\n\t"
    "v_fmac_f32 v145, v104, v28\n\t"
    "v_mfma_f32_16x16x32_bf16 v[116:119], v[168:171], v[208:211], v[116:119]\n\t"
    "v_fmac_f32 v144, v105, v29\n\t"
    "v_fmac_f32 v145, v106, v30\n\t"
    "ds_read_b128 v[164:167], v129 offset:0\n\t"
    "ds_read_b128 v[168:171], v129 offset:64\n\t"
    "v_mfma_f32_16x16x32_bf16 v[92:95], v[172:175], v[196:199], 0\n\t"
    "v_fmac_f32 v144, v107, v31\n\t"
    "v_fmac_f32 v145, v108, v32\n\t"
    "v_mfma_f32_16x16x32_bf16 v[92:95], v[176:179], v[200:203], v[92:95]\n\t"
    "v_fmac_f32 v144, v109, v33\n\t"
    "v_fmac_f32 v145, v110, v34\n\t"
    "s_waitcnt vmcnt(4)\n\t"
    "s_cmp_ge_u32 s55, s64\n\t"
    "s_cbranch_scc1 L_ahk11_%=\n\t"
    "L_ahb11_%=:\n\t"
    "v_mfma_f32_16x16x32_bf16 v[96:99], v[172:175], v[204:207], 0\n\t"
    "v_mad_u32_u24 v132, v132, s68, v151\n\t"
    "v_and_b32 v132, -16, v132\n\t"
    "v_mfma_f32_16x16x32_bf16 v[96:99], v[176:179], v[208:211], v[96:99]\n\t"
    "v_fmac_f32 v144, v111, v35\n\t"
    "v_fmac_f32 v145, v112, v87\n\t"
    "ds_read_b128 v[172:175], v130 offset:0\n\t"
    "ds_read_b128 v[176:179], v130 offset:64\n\t"
    "s_mul_i32 s66, s60, 400\n\t"
    "v_mfma_f32_16x16x32_bf16 v[100:103], v[212:215], v[188:191], 0\n\t"
    "v_fmac_f32 v144, v116, v36\n\t"
    "v_fmac_f32 v145, v117, v37\n\t"
    "v_mfma_f32_16x16x32_bf16 v[100:103], v[216:219], v[192:195], v[100:103]\n\t"
    "v_fmac_f32 v144, v118, v38\n\t"
    "v_fmac_f32 v145, v119, v39\n\t"
    "buffer_load_dwordx3 v[128:130], v153, %0, s66 offen\n\t"
    "v_mfma_f32_16x16x32_bf16 v[104:107], v[180:183], v[188:191], 0\n\t"
    "v_mad_u32_u24 v133, v133, s68, v151\n\t"
    "v_and_b32 v133, -16, v133\n\t"
    "v_mfma_f32_16x16x32_bf16 v[104:107], v[184:187], v[192:195], v[104:107]\n\t"
    "v_fmac_f32 v144, v92, v40\n\t"
    "v_fmac_f32 v145, v93, v41\n\t"
    "v_mfma_f32_16x16x32_bf16 v[108:111], v[180:183], v[196:199], 0\n\t"
    "v_fmac_f32 v144, v94, v42\n\t"
    "v_fmac_f32 v145, v95, v43\n\t"
    "v_mfma_f32_16x16x32_bf16 v[108:111], v[184:187], v[200:203], v[108:111]\n\t"
    "v_fmac_f32 v144, v96, v44\n\t"
    "v_fmac_f32 v145, v97, v45\n\t"
    "v_mfma_f32_16x16x32_bf16 v[112:115], v[180:183], v[204:207], 0\n\t"
    "v_fmac_f32 v144, v98, v46\n\t"
    "v_fmac_f32 v145, v99, v47\n\t"
    "v_mfma_f32_16x16x32_bf16 v[112:115], v[184:187], v[208:211], v[112:115]\n\t"
    "v_fmac_f32 v144, v100, v88\n\t"
    "v_mad_u32_u24 v134, v134, s68, v151\n\t"
    "ds_read_b128 v[180:183], v132 offset:0\n\t"
    "ds_read_b128 v[184:187], v132 offset:64\n\t"
    "v_mfma_f32_16x16x32_bf16 v[116:119], v[212:215], v[196:199], 0\n\t"
    "v_and_b32 v134, -16, v134\n\t"
    "v_fmac_f32 v145, v104, v48\n\t"
    "v_mfma_f32_16x16x32_bf16 v[116:119], v[216:219], v[200:203], v[116:119]\n\t"
    "v_fmac_f32 v144, v105, v49\n\t"
    "v_fmac_f32 v145, v106, v50\n\t"
    "v_mfma_f32_16x16x32_bf16 v[92:95], v[188:191], v[188:191], 0\n\t"
    "v_fmac_f32 v144, v107, v51\n\t"
    "v_mad_u32_u24 v135, v135, s68, v151\n\t"
    "v_mfma_f32_16x16x32_bf16 v[92:95], v[192:195], v[192:195], v[92:95]\n\t"
    "v_and_b32 v135, -16, v135\n\t"
    "v_fmac_f32 v145, v108, v52\n\t"
    "v_mfma_f32_16x16x32_bf16 v[96:99], v[188:191], v[196:199], 0\n\t"
    "v_fmac_f32 v144, v109, v53\n\t"
    "v_fmac_f32 v145, v110, v54\n\t"
    "v_mfma_f32_16x16x32_bf16 v[96:99], v[192:195], v[200:203], v[96:99]\n\t"
    "v_fmac_f32 v144, v111, v55\n\t"
    "v_fmac_f32 v145, v112, v56\n\t"
    "v_mfma_f32_16x16x32_bf16 v[100:103], v[188:191], v[204:207], 0\n\t"
    "v_fmac_f32 v144, v113, v57\n\t"
    "v_fmac_f32 v145, v114, v58\n\t"
    "v_mfma_f32_16x16x32_bf16 v[100:103], v[192:195], v[208:211], v[100:103]\n\t"
    "v_fmac_f32 v144, v115, v59\n\t"
    "v_fmac_f32 v145, v116, v89\n\t"
    "ds_read_b128 v[188:191], v133 offset:0\n\t"
    "ds_read_b128 v[192:195], v133 offset:64\n\t"
    "v_mfma_f32_16x16x32_bf16 v[104:107], v[212:215], v[204:207], 0\n\t"
    "v_fmac_f32 v144, v92, v60\n\t"
    "v_fmac_f32 v145, v93, v61\n\t"
    "v_mfma_f32_16x16x32_bf16 v[104:107], v[216:219], v[208:211], v[104:107]\n\t"
    "v_fmac_f32 v144, v94, v62\n\t"
    "v_fmac_f32 v145, v95, v63\n\t"
    "v_mfma_f32_16x16x32_bf16 v[108:111], v[196:199], v[196:199], 0\n\t"
    "v_fmac_f32 v144, v96, v64\n\t"
    "v_fmac_f32 v145, v97, v65\n\t"
    "v_mfma_f32_16x16x32_bf16 v[108:111], v[200:203], v[200:203], v[108:111]\n\t"
    "v_fmac_f32 v144, v98, v66\n\t"
    "v_fmac_f32 v145, v99, v67\n\t"
    "v_mfma_f32_16x16x32_bf16 v[112:115], v[196:199], v[204:207], 0\n\t"
    "v_fmac_f32 v144, v100, v68\n\t"
    "v_fmac_f32 v145, v101, v69\n\t"
    "v_mfma_f32_16x16x32_bf16 v[112:115], v[200:203], v[208:211], v[112:115]\n\t"
    "v_fmac_f32 v144, v102, v70\n\t"
    "v_fmac_f32 v145, v103, v71\n\t"
    "ds_read_b128 v[196:199], v134 offset:0\n\t"
    "ds_read_b128 v[200:203], v134 offset:64\n\t"
    "v_mfma_f32_16x16x32_bf16 v[116:119], v[204:207], v[204:207], 0\n\t"
    "v_fmac_f32 v144, v104, v90\n\t"
    "v_mfma_f32_16x16x32_bf16 v[116:119], v[208:211], v[208:211], v[116:119]\n\t"
    "ds_read_b128 v[204:207], v135 offset:0\n\t"
    "ds_read_b128 v[208:211], v135 offset:64\n\t"
    "s_mov_b32 s52, s53\n\t"
    "s_mov_b32 s53, s54\n\t"
    "s_mov_b32 s54, s55\n\t"
    "s_mov_b32 s55, s60\n\t"
    "s_cmp_lt_u32 s53, s64\n\t"
    "s_cbranch_scc1 L_apnn_%=\n\t"
    "L_adone_%=:\n\t"
    "s_mul_i32 s69, s52, 256\n\t"
    "s_add_u32 s69, s69, s50\n\t"
    "v_fmac_f32 v145, v108, v72\n\t"
    "v_fmac_f32 v144, v109, v73\n\t"
    "v_fmac_f32 v145, v110, v74\n\t"
    "v_fmac_f32 v144, v111, v75\n\t"
    "v_fmac_f32 v145, v112, v76\n\t"
    "v_fmac_f32 v144, v113, v77\n\t"
    "v_fmac_f32 v145, v114, v78\n\t"
    "v_fmac_f32 v144, v115, v79\n\t"
    "v_fmac_f32 v145, v116, v80\n\t"
    "v_fmac_f32 v144, v117, v81\n\t"
    "v_fmac_f32 v145, v118, v82\n\t"
    "v_fmac_f32 v144, v119, v83\n\t"
    "v_add_f32 v40, v144, v145\n\t"
    "L_aend_%=:\n\t"
    "s_branch L_aend2_%=\n\t"
    "L_ahk00_%=:\n\t"
    "v_mov_b32 v147, 3112\n\t"
    "s_cmp_lt_u32 s54, s64\n\t"
    "ds_read_b32 v147, v147\n\t"
    "s_cbranch_scc1 L_ahb00_%=\n\t"
    "s_waitcnt vmcnt(3)\n\t"
    "v_add_u32 v137, 0, v155\n\t"
    "v_add_u32 v138, 16, v155\n\t"
    "v_add_u32 v140, 32, v155\n\t"
    "v_add_u32 v141, 48, v155\n\t"
    "v_add_u32 v142, 64, v155\n\t"
    "v_add_u32 v143, 80, v155\n\t"
    "s_branch L_ahb00_%=\n\t"
    "L_ahk10_%=:\n\t"
    "s_waitcnt lgkmcnt(4)\n\t"
    "v_readfirstlane_b32 s65, v147\n\t"
    "s_nop 0\n\t"
    "s_cmp_eq_u32 s65, 0\n\t"
    "s_cbranch_scc1 L_ahb10_%=\n\t"
    "s_cmp_lt_u32 s62, 2\n\t"
    "s_cbranch_scc1 L_mel1_%=\n\t"
    "buffer_load_dwordx4 v[220:223], v127, %12, s61 offen offset:2048 sc1\n\t"
    "buffer_load_dwordx4 v[224:227], v127, %12, s61 offen offset:3072 sc1\n\t"
    "s_add_u32 s61, s61, 0x1000\n\t"
    "buffer_load_dwordx4 v[228:231], v127, %12, s61 offen offset:2048 sc1\n\t"
    "buffer_load_dwordx4 v[232:235], v127, %12, s61 offen offset:3072 sc1\n\t"
    "s_add_u32 s61, s61, 0x1000\n\t"
    "buffer_load_dwordx4 v[236:239], v127, %12, s61 offen offset:2048 sc1\n\t"
    "buffer_load_dwordx4 v[240:243], v127, %12, s61 offen offset:3072 sc1\n\t"
    "s_add_u32 s61, s61, 0x1000\n\t"
    "buffer_load_dwordx4 v[244:247], v127, %12, s61 offen offset:2048 sc1\n\t"
    "buffer_load_dwordx4 v[248:251], v127, %12, s61 offen offset:3072 sc1\n\t"
    "s_add_u32 s61, s61, 0x1000\n\t"
    "buffer_load_dwordx4 v[252:255], v127, %12, s61 offen offset:2048 sc1\n\t"
    "s_mov_b32 s62, 1\n\t"
    "L_mel1_%=:\n\t"
    "s_cmp_lt_u32 s54, s64\n\t"
    "s_cbranch_scc1 L_ahb10_%=\n\t"
    "buffer_load_dwordx4 v[0:3], v127, %12, s63 offen offset:0 sc1\n\t"
    "buffer_load_dwordx4 v[4:7], v127, %12, s63 offen offset:1024 sc1\n\t"
    "buffer_load_dwordx4 v[8:11], v127, %12, s63 offen offset:2048 sc1\n\t"
    "buffer_load_dwordx4 v[12:15], v127, %12, s63 offen offset:3072 sc1\n\t"
    "s_add_u32 s63, s63, 0x1000\n\t"
    "s_mov_b32 s62, 0\n\t"
    "s_branch L_ahb10_%=\n\t"
    "L_ahk01_%=:\n\t"
    "v_mov_b32 v147, 3112\n\t"
    "s_cmp_lt_u32 s54, s64\n\t"
    "ds_read_b32 v147, v147\n\t"
    "s_cbranch_scc1 L_ahb01_%=\n\t"
    "s_waitcnt vmcnt(3)\n\t"
    "v_add_u32 v129, 0, v155\n\t"
    "v_add_u32 v130, 16, v155\n\t"
    "v_add_u32 v132, 32, v155\n\t"
    "v_add_u32 v133, 48, v155\n\t"
    "v_add_u32 v134, 64, v155\n\t"
    "v_add_u32 v135, 80, v155\n\t"
    "s_branch L_ahb01_%=\n\t"
    "L_ahk11_%=:\n\t"
    "s_waitcnt lgkmcnt(4)\n\t"
    "v_readfirstlane_b32 s65, v147\n\t"
    "s_nop 0\n\t"
    "s_cmp_eq_u32 s65, 0\n\t"
    "s_cbranch_scc1 L_ahb11_%=\n\t"
    "s_cmp_lt_u32 s62, 2\n\t"
    "s_cbranch_scc1 L_mel3_%=\n\t"
    "buffer_load_dwordx4 v[220:223], v127, %12, s61 offen offset:2048 sc1\n\t"
    "buffer_load_dwordx4 v[224:227], v127, %12, s61 offen offset:3072 sc1\n\t"
    "s_add_u32 s61, s61, 0x1000\n\t"
    "buffer_load_dwordx4 v[228:231], v127, %12, s61 offen offset:2048 sc1\n\t"
    "buffer_load_dwordx4 v[232:235], v127, %12, s61 offen offset:3072 sc1\n\t"
    "s_add_u32 s61, s61, 0x1000\n\t"
    "buffer_load_dwordx4 v[236:239], v127, %12, s61 offen offset:2048 sc1\n\t"
    "buffer_load_dwordx4 v[240:243], v127, %12, s61 offen offset:3072 sc1\n\t"
    "s_add_u32 s61, s61, 0x1000\n\t"
    "buffer_load_dwordx4 v[244:247], v127, %12, s61 offen offset:2048 sc1\n\t"
    "buffer_load_dwordx4 v[248:251], v127, %12, s61 offen offset:3072 sc1\n\t"
    "s_add_u32 s61, s61, 0x1000\n\t"
    "buffer_load_dwordx4 v[252:255], v127, %12, s61 offen offset:2048 sc1\n\t"
    "s_mov_b32 s62, 1\n\t"
    "L_mel3_%=:\n\t"
    "s_cmp_lt_u32 s54, s64\n\t"
    "s_cbranch_scc1 L_ahb11_%=\n\t"
    "buffer_load_dwordx4 v[0:3], v127, %12, s63 offen offset:0 sc1\n\t"
    "buffer_load_dwordx4 v[4:7], v127, %12, s63 offen offset:1024 sc1\n\t"
    "buffer_load_dwordx4 v[8:11], v127, %12, s63 offen offset:2048 sc1\n\t"
    "buffer_load_dwordx4 v[12:15], v127, %12, s63 offen offset:3072 sc1\n\t"
    "s_add_u32 s63, s63, 0x1000\n\t"
    "s_mov_b32 s62, 0\n\t"
    "s_branch L_ahb11_%=\n\t"
    "L_aend2_%=:\n\t"
    "s_setprio 0\n\t"
    "v_lshlrev_b32 v113, 16, v127\n\t"
    "v_add_u32 v113, 3108, v113\n\t"
    "s_cmp_eq_u32 s62, 0\n\t"
    "s_cbranch_scc1 L_mpf_%=\n\t"
    "L_ml0_%=:\n\t"
    "ds_read_b32 v114, v113 offset:4\n\t"
    "s_waitcnt lgkmcnt(0)\n\t"
    "v_readfirstlane_b32 s65, v114\n\t"
    "s_nop 0\n\t"
    "s_cmp_lg_u32 s65, 0\n\t"
    "s_cbranch_scc1 L_mm0_%=\n\t"
    "s_sleep 2\n\t"
    "s_branch L_ml0_%=\n\t"
    "L_mm0_%=:\n\t"
    "s_cmp_lt_u32 s62, 2\n\t"
    "s_cbranch_scc1 L_mel5_%=\n\t"
    "buffer_load_dwordx4 v[220:223], v127, %12, s61 offen offset:2048 sc1\n\t"
    "buffer_load_dwordx4 v[224:227], v127, %12, s61 offen offset:3072 sc1\n\t"
    "s_add_u32 s61, s61, 0x1000\n\t"
    "buffer_load_dwordx4 v[228:231], v127, %12, s61 offen offset:2048 sc1\n\t"
    "buffer_load_dwordx4 v[232:235], v127, %12, s61 offen offset:3072 sc1\n\t"
    "s_add_u32 s61, s61, 0x1000\n\t"
    "buffer_load_dwordx4 v[236:239], v127, %12, s61 offen offset:2048 sc1\n\t"
    "buffer_load_dwordx4 v[240:243], v127, %12, s61 offen offset:3072 sc1\n\t"
    "s_add_u32 s61, s61, 0x1000\n\t"
    "buffer_load_dwordx4 v[244:247], v127, %12, s61 offen offset:2048 sc1\n\t"
    "buffer_load_dwordx4 v[248:251], v127, %12, s61 offen offset:3072 sc1\n\t"
    "s_add_u32 s61, s61, 0x1000\n\t"
    "buffer_load_dwordx4 v[252:255], v127, %12, s61 offen offset:2048 sc1\n\t"
    "s_mov_b32 s62, 1\n\t"
    "L_mel5_%=:\n\t"
    "buffer_load_dwordx4 v[0:3], v127, %12, s63 offen offset:0 sc1\n\t"
    "buffer_load_dwordx4 v[4:7], v127, %12, s63 offen offset:1024 sc1\n\t"
    "buffer_load_dwordx4 v[8:11], v127, %12, s63 offen offset:2048 sc1\n\t"
    "buffer_load_dwordx4 v[12:15], v127, %12, s63 offen offset:3072 sc1\n\t"
    "s_add_u32 s63, s63, 0x1000\n\t"
    "s_mov_b32 s62, 0\n\t"
    "L_mpf_%=:\n\t"
    "v_lshrrev_b32 v105, 4, v127\n\t"
    "v_and_b32 v105, 48, v105\n\t"
    "s_movk_i32 s65, 130\n\t"
    "v_mad_u32_u24 v104, v155, s65, v105\n\t"
    "v_add_u32 v105, 14170, v104\n\t"
    "v_and_b32 v104, -16, v104\n\t"
    "v_and_b32 v105, -16, v105\n\t"
    "buffer_load_dwordx4 v[128:131], v127, %12, s61 offen offset:3072 sc1\n\t"
    "s_add_u32 s61, s61, 0x1000\n\t"
    "buffer_load_dwordx4 v[132:135], v127, %12, s61 offen offset:2048 sc1\n\t"
    "ds_read_b128 v[156:159], v104 offset:12480\n\t"
    "ds_read_b128 v[160:163], v104 offset:12544\n\t"
    "buffer_load_dwordx4 v[136:139], v127, %12, s61 offen offset:3072 sc1\n\t"
    "s_add_u32 s61, s61, 0x1000\n\t"
    "buffer_load_dwordx4 v[140:143], v127, %12, s61 offen offset:2048 sc1\n\t"
    "ds_read_b128 v[212:215], v105 offset:0\n\t"
    "ds_read_b128 v[216:219], v105 offset:64\n\t"
    "buffer_load_dwordx4 v[144:147], v127, %12, s61 offen offset:3072 sc1\n\t"
    "s_add_u32 s61, s61, 0x1000\n\t"
    "buffer_load_dwordx4 v[148:151], v127, %12, s61 offen offset:2048 sc1\n\t"
    "buffer_load_dwordx4 v[152:155], v127, %12, s61 offen offset:3072 sc1\n\t"
    "s_add_u32 s61, s61, 0x1000\n\t"
    "v_mov_b32 v114, 1\n\t"
    "ds_add_u32 v113, v114\n\t"
    "s_waitcnt lgkmcnt(5)\n\t"
    "s_waitcnt vmcnt(7)\n\t"
    "v_mfma_f32_16x16x32_bf16 v[16:19], v[0:3], v[164:167], 0\n\t"
    "v_mfma_f32_16x16x32_bf16 v[16:19], v[4:7], v[168:171], v[16:19]\n\t"
    "v_mfma_f32_16x16x32_bf16 v[16:19], v[8:11], v[220:223], v[16:19]\n\t"
    "v_mfma_f32_16x16x32_bf16 v[16:19], v[12:15], v[224:227], v[16:19]\n\t"
    "v_mfma_f32_16x16x32_bf16 v[20:23], v[0:3], v[172:175], 0\n\t"
    "v_mfma_f32_16x16x32_bf16 v[20:23], v[4:7], v[176:179], v[20:23]\n\t"
    "v_mfma_f32_16x16x32_bf16 v[20:23], v[8:11], v[228:231], v[20:23]\n\t"
    "v_mfma_f32_16x16x32_bf16 v[20:23], v[12:15], v[232:235], v[20:23]\n\t"
    "v_mfma_f32_16x16x32_bf16 v[80:83], v[0:3], v[180:183], 0\n\t"
    "v_mfma_f32_16x16x32_bf16 v[80:83], v[4:7], v[184:187], v[80:83]\n\t"
    "v_mfma_f32_16x16x32_bf16 v[80:83], v[8:11], v[236:239], v[80:83]\n\t"
    "v_mfma_f32_16x16x32_bf16 v[80:83], v[12:15], v[240:243], v[80:83]\n\t"
    "v_cvt_pk_bf16_f32 v24, v16, v17\n\t"
    "v_cvt_pk_bf16_f32 v25, v18, v19\n\t"
    "v_mfma_f32_16x16x32_bf16 v[16:19], v[0:3], v[188:191], 0\n\t"
    "v_mfma_f32_16x16x32_bf16 v[16:19], v[4:7], v[192:195], v[16:19]\n\t"
    "v_mfma_f32_16x16x32_bf16 v[16:19], v[8:11], v[244:247], v[16:19]\n\t"
    "v_mfma_f32_16x16x32_bf16 v[16:19], v[12:15], v[248:251], v[16:19]\n\t"
    "v_cvt_pk_bf16_f32 v26, v20, v21\n\t"
    "v_cvt_pk_bf16_f32 v27, v22, v23\n\t"
    "s_waitcnt vmcnt(6)\n\t"
    "v_mfma_f32_16x16x32_bf16 v[20:23], v[0:3], v[196:199], 0\n\t"
    "v_mfma_f32_16x16x32_bf16 v[20:23], v[4:7], v[200:203], v[20:23]\n\t"
    "v_mfma_f32_16x16x32_bf16 v[20:23], v[8:11], v[252:255], v[20:23]\n\t"
    "v_mfma_f32_16x16x32_bf16 v[20:23], v[12:15], v[128:131], v[20:23]\n\t"
    "v_cvt_pk_bf16_f32 v28, v80, v81\n\t"
    "v_cvt_pk_bf16_f32 v29, v82, v83\n\t"
    "s_waitcnt lgkmcnt(0)\n\t"
    "s_waitcnt vmcnt(4)\n\t"
    "v_mfma_f32_16x16x32_bf16 v[80:83], v[0:3], v[204:207], 0\n\t"
    "v_mfma_f32_16x16x32_bf16 v[80:83], v[4:7], v[208:211], v[80:83]\n\t"
    "v_mfma_f32_16x16x32_bf16 v[80:83], v[8:11], v[132:135], v[80:83]\n\t"
    "v_mfma_f32_16x16x32_bf16 v[80:83], v[12:15], v[136:139], v[80:83]\n\t"
    "v_cvt_pk_bf16_f32 v30, v16, v17\n\t"
    "v_cvt_pk_bf16_f32 v31, v18, v19\n\t"
    "s_waitcnt vmcnt(2)\n\t"
    "v_mfma_f32_16x16x32_bf16 v[16:19], v[0:3], v[156:159], 0\n\t"
    "buffer_load_dwordx4 v[100:103], v127, %12, s84 offen offset:0 sc1\n\t"
    "s_add_u32 s84, s84, s97\n\t"
    "v_mfma_f32_16x16x32_bf16 v[16:19], v[4:7], v[160:163], v[16:19]\n\t"
    "buffer_load_dwordx4 v[104:107], v127, %12, s85 offen offset:0 sc1\n\t"
    "s_add_u32 s85, s85, s97\n\t"
    "v_mfma_f32_16x16x32_bf16 v[16:19], v[8:11], v[140:143], v[16:19]\n\t"
    "v_mfma_f32_16x16x32_bf16 v[16:19], v[12:15], v[144:147], v[16:19]\n\t"
    "v_cvt_pk_bf16_f32 v32, v20, v21\n\t"
    "v_cvt_pk_bf16_f32 v33, v22, v23\n\t"
    "s_waitcnt vmcnt(2)\n\t"
    "v_mfma_f32_16x16x32_bf16 v[20:23], v[0:3], v[212:215], 0\n\t"
    "buffer_load_dwordx4 v[108:111], v127, %12, s86 offen offset:0 sc1\n\t"
    "s_add_u32 s86, s86, s97\n\t"
    "v_mfma_f32_16x16x32_bf16 v[20:23], v[4:7], v[216:219], v[20:23]\n\t"
    "buffer_load_dwordx4 v[112:115], v127, %12, s87 offen offset:0 sc1\n\t"
    "s_add_u32 s87, s87, s97\n\t"
    "v_mfma_f32_16x16x32_bf16 v[20:23], v[8:11], v[148:151], v[20:23]\n\t"
    "v_mfma_f32_16x16x32_bf16 v[20:23], v[12:15], v[152:155], v[20:23]\n\t"
    "v_cvt_pk_bf16_f32 v34, v80, v81\n\t"
    "v_cvt_pk_bf16_f32 v35, v82, v83\n\t"
    "v_cvt_pk_bf16_f32 v36, v16, v17\n\t"
    "v_cvt_pk_bf16_f32 v37, v18, v19\n\t"
    "s_nop 3\n\t"
    "v_cvt_pk_bf16_f32 v38, v20, v21\n\t"
    "v_cvt_pk_bf16_f32 v39, v22, v23\n\t"
    "v_lshrrev_b32 v41, 2, v127\n\t"
    "s_and_b32 s65, %14, 8\n\t"
    "s_lshl_b32 s65, s65, 10\n\t"
    "s_add_u32 s63, s63, s65\n\t"
    "buffer_store_dword v40, v41, %12, s69 offen sc1\n\t"
    "v_mov_b32 v82, 3108\n\t"
    "s_cmp_eq_u32 %16, 0\n\t"
    "s_cselect_b32 s47, 0x1ff, 0\n\t"
    "L_my_%=:\n\t"
    "ds_read_b32 v81, v82\n\t"
    "s_waitcnt lgkmcnt(0)\n\t"
    "v_cmp_gt_u32 vcc, 8, v81\n\t"
    "s_cbranch_vccz L_mz_%=\n\t"
    "s_sleep 2\n\t"
    "s_branch L_my_%=\n\t"
    "L_mz_%=:\n\t"
    "ds_write_b64 v125, v[24:25] offset:0\n\t"
    "v_mov_b64 v[0:1], 0\n\t"
    "ds_write_b64 v125, v[26:27] offset:128\n\t"
    "v_mov_b64 v[2:3], 0\n\t"
    "ds_write_b64 v125, v[28:29] offset:256\n\t"
    "v_mov_b64 v[4:5], 0\n\t"
    "ds_write_b64 v125, v[30:31] offset:384\n\t"
    "v_mov_b64 v[6:7], 0\n\t"
    "ds_write_b64 v125, v[32:33] offset:512\n\t"
    "ds_write_b64 v125, v[34:35] offset:640\n\t"
    "ds_write_b64 v125, v[36:37] offset:768\n\t"
    "ds_write_b64 v125, v[38:39] offset:872\n\t"
    "v_add_u32 v125, s93, v125\n\t"
    "s_sub_u32 s93, 0, s93\n\t"
    "s_waitcnt lgkmcnt(0)\n\t"
    "s_barrier\n\t"
    "s_memrealtime s[44:45]\n\t"
    "s_waitcnt lgkmcnt(0)\n\t"
    "s_waitcnt vmcnt(4)\n\t"
    "v_add_u32_sdwa v126, s92, v100 dst_sel:DWORD dst_unused:UNUSED_PAD src0_sel:DWORD src1_sel:WORD_1\n\t"
    "v_add_u32_sdwa v100, s92, v100 dst_sel:DWORD dst_unused:UNUSED_PAD src0_sel:DWORD src1_sel:WORD_0\n\t"
    "ds_read_b64 v[84:85], v100 offset:0\n\t"
    "ds_read_b64 v[86:87], v126 offset:0\n\t"
    "v_add_u32_sdwa v90, s92, v101 dst_sel:DWORD dst_unused:UNUSED_PAD src0_sel:DWORD src1_sel:WORD_1\n\t"
    "v_add_u32_sdwa v101, s92, v101 dst_sel:DWORD dst_unused:UNUSED_PAD src0_sel:DWORD src1_sel:WORD_0\n\t"
    "ds_read_b64 v[88:89], v101 offset:0\n\t"
    "ds_read_b64 v[90:91], v90 offset:0\n\t"
    "v_add_u32 v63, s96, v127\n\t"
    "s_add_u32 s96, s96, 0x1000\n\t"
    "s_and_b32 s96, s96, 0x3000\n\t"
    "v_add_u32 v63, 128000, v63\n\t"
    "ds_read_b128 v[48:51], v63 offset:0\n\t"
    "ds_read_b128 v[52:55], v63 offset:1024\n\t"
    "ds_read_b128 v[56:59], v63 offset:2048\n\t"
    "ds_read_b128 v[60:63], v63 offset:3072\n\t"
    "v_add_u32_sdwa v126, s92, v102 dst_sel:DWORD dst_unused:UNUSED_PAD src0_sel:DWORD src1_sel:WORD_1\n\t"
    "v_add_u32_sdwa v102, s92, v102 dst_sel:DWORD dst_unused:UNUSED_PAD src0_sel:DWORD src1_sel:WORD_0\n\t"
    "ds_read_b64 v[92:93], v102 offset:0\n\t"
    "ds_read_b64 v[94:95], v126 offset:0\n\t"
    "v_add_u32_sdwa v98, s92, v103 dst_sel:DWORD dst_unused:UNUSED_PAD src0_sel:DWORD src1_sel:WORD_1\n\t"
    "v_add_u32_sdwa v103, s92, v103 dst_sel:DWORD dst_unused:UNUSED_PAD src0_sel:DWORD src1_sel:WORD_0\n\t"
    "ds_read_b64 v[96:97], v103 offset:0\n\t"
    "ds_read_b64 v[98:99], v98 offset:0\n\t"
    "buffer_load_dwordx4 v[100:103], v127, %12, s88 offen offset:0 sc1\n\t"
    "s_add_u32 s88, s88, s97\n\t"
    "s_waitcnt vmcnt(4)\n\t"
    "v_add_u32_sdwa v126, s92, v104 dst_sel:DWORD dst_unused:UNUSED_PAD src0_sel:DWORD src1_sel:WORD_1\n\t"
    "v_add_u32_sdwa v104, s92, v104 dst_sel:DWORD dst_unused:UNUSED_PAD src0_sel:DWORD src1_sel:WORD_0\n\t"
    "v_mov_b64 v[8:9], 0\n\t"
    "v_mov_b64 v[10:11], 0\n\t"
    "v_mov_b64 v[12:13], 0\n\t"
    "v_mov_b64 v[14:15], 0\n\t"
    "v_mov_b64 v[16:17], 0\n\t"
    "v_mov_b64 v[18:19], 0\n\t"
    "s_waitcnt lgkmcnt(8)\n\t"
    "v_smfmac_f32_16x16x64_bf16 v[0:3], v[120:123], v[84:91], v124\n\t"
    "ds_read_b64 v[84:85], v104 offset:0\n\t"
    "s_waitcnt lgkmcnt(5)\n\t"
    "v_mfma_f32_16x16x32_bf16 v[116:119], v[48:51], v[164:167], 0\n\t"
    "ds_read_b64 v[86:87], v126 offset:0\n\t"
    "v_mfma_f32_16x16x32_bf16 v[116:119], v[52:55], v[168:171], v[116:119]\n\t"
    "v_add_u32_sdwa v90, s92, v105 dst_sel:DWORD dst_unused:UNUSED_PAD src0_sel:DWORD src1_sel:WORD_1\n\t"
    "v_add_u32_sdwa v105, s92, v105 dst_sel:DWORD dst_unused:UNUSED_PAD src0_sel:DWORD src1_sel:WORD_0\n\t"
    "ds_read_b64 v[88:89], v105 offset:0\n\t"
    "v_mfma_f32_16x16x32_bf16 v[116:119], v[56:59], v[220:223], v[116:119]\n\t"
    "ds_read_b64 v[90:91], v90 offset:0\n\t"
    "v_mfma_f32_16x16x32_bf16 v[116:119], v[60:63], v[224:227], v[116:119]\n\t"
    "v_add_u32_sdwa v126, s92, v106 dst_sel:DWORD dst_unused:UNUSED_PAD src0_sel:DWORD src1_sel:WORD_1\n\t"
    "v_add_u32_sdwa v106, s92, v106 dst_sel:DWORD dst_unused:UNUSED_PAD src0_sel:DWORD src1_sel:WORD_0\n\t"
    "v_mfma_f32_16x16x32_bf16 v[80:83], v[48:51], v[172:175], 0\n\t"
    "v_mov_b64 v[20:21], 0\n\t"
    "v_mov_b64 v[22:23], 0\n\t"
    "v_mov_b64 v[24:25], 0\n\t"
    "v_mov_b64 v[26:27], 0\n\t"
    "v_mov_b64 v[28:29], 0\n\t"
    "v_mov_b64 v[30:31], 0\n\t"
    "s_waitcnt vmcnt(3) lgkmcnt(4)\n\t"
    "v_smfmac_f32_16x16x64_bf16 v[4:7], v[120:123], v[92:99], v124\n\t"
    "ds_read_b64 v[92:93], v106 offset:0\n\t"
    "v_mfma_f32_16x16x32_bf16 v[80:83], v[52:55], v[176:179], v[80:83]\n\t"
    "ds_read_b64 v[94:95], v126 offset:0\n\t"
    "v_mfma_f32_16x16x32_bf16 v[80:83], v[56:59], v[228:231], v[80:83]\n\t"
    "v_add_u32_sdwa v98, s92, v107 dst_sel:DWORD dst_unused:UNUSED_PAD src0_sel:DWORD src1_sel:WORD_1\n\t"
    "v_add_u32_sdwa v107, s92, v107 dst_sel:DWORD dst_unused:UNUSED_PAD src0_sel:DWORD src1_sel:WORD_0\n\t"
    "ds_read_b64 v[96:97], v107 offset:0\n\t"
    "v_mfma_f32_16x16x32_bf16 v[80:83], v[60:63], v[232:235], v[80:83]\n\t"
    "ds_read_b64 v[98:99], v98 offset:0\n\t"
    "v_cvt_pk_bf16_f32 v116, v116, v117\n\t"
    "v_cvt_pk_bf16_f32 v117, v118, v119\n\t"
    "ds_write_b64 v125, v[116:117] offset:0\n\t"
    "v_mfma_f32_16x16x32_bf16 v[116:119], v[48:51], v[180:183], 0\n\t"
    "buffer_load_dwordx4 v[104:107], v127, %12, s89 offen offset:0 sc1\n\t"
    "s_add_u32 s89, s89, s97\n\t"
    "v_add_u32_sdwa v126, s92, v108 dst_sel:DWORD dst_unused:UNUSED_PAD src0_sel:DWORD src1_sel:WORD_1\n\t"
    "v_add_u32_sdwa v108, s92, v108 dst_sel:DWORD dst_unused:UNUSED_PAD src0_sel:DWORD src1_sel:WORD_0\n\t"
    "v_mfma_f32_16x16x32_bf16 v[116:119], v[52:55], v[184:187], v[116:119]\n\t"
    "v_mov_b64 v[32:33], 0\n\t"
    "v_mov_b64 v[34:35], 0\n\t"
    "v_mov_b64 v[36:37], 0\n\t"
    "v_mov_b64 v[38:39], 0\n\t"
    "v_mov_b64 v[40:41], 0\n\t"
    "v_mov_b64 v[42:43], 0\n\t"
    "v_mov_b64 v[44:45], 0\n\t"
    "v_mov_b64 v[46:47], 0\n\t"
    "s_waitcnt lgkmcnt(5)\n\t"
    "v_smfmac_f32_16x16x64_bf16 v[8:11], v[120:123], v[84:91], v124\n\t"
    "ds_read_b64 v[84:85], v108 offset:0\n\t"
    "v_mfma_f32_16x16x32_bf16 v[116:119], v[56:59], v[236:239], v[116:119]\n\t"
    "ds_read_b64 v[86:87], v126 offset:0\n\t"
    "v_mfma_f32_16x16x32_bf16 v[116:119], v[60:63], v[240:243], v[116:119]\n\t"
    "v_add_u32_sdwa v90, s92, v109 dst_sel:DWORD dst_unused:UNUSED_PAD src0_sel:DWORD src1_sel:WORD_1\n\t"
    "v_add_u32_sdwa v109, s92, v109 dst_sel:DWORD dst_unused:UNUSED_PAD src0_sel:DWORD src1_sel:WORD_0\n\t"
    "ds_read_b64 v[88:89], v109 offset:0\n\t"
    "v_cvt_pk_bf16_f32 v80, v80, v81\n\t"
    "v_cvt_pk_bf16_f32 v81, v82, v83\n\t"
    "ds_write_b64 v125, v[80:81] offset:128\n\t"
    "v_mfma_f32_16x16x32_bf16 v[80:83], v[48:51], v[188:191], 0\n\t"
    "ds_read_b64 v[90:91], v90 offset:0\n\t"
    "v_mfma_f32_16x16x32_bf16 v[80:83], v[52:55], v[192:195], v[80:83]\n\t"
    "v_add_u32_sdwa v126, s92, v110 dst_sel:DWORD dst_unused:UNUSED_PAD src0_sel:DWORD src1_sel:WORD_1\n\t"
    "v_add_u32_sdwa v110, s92, v110 dst_sel:DWORD dst_unused:UNUSED_PAD src0_sel:DWORD src1_sel:WORD_0\n\t"
    "v_mfma_f32_16x16x32_bf16 v[80:83], v[56:59], v[244:247], v[80:83]\n\t"
    "s_waitcnt vmcnt(3) lgkmcnt(6)\n\t"
    "v_smfmac_f32_16x16x64_bf16 v[12:15], v[120:123], v[92:99], v124\n\t"
    "ds_read_b64 v[92:93], v110 offset:0\n\t"
    "v_mfma_f32_16x16x32_bf16 v[80:83], v[60:63], v[248:251], v[80:83]\n\t"
    "ds_read_b64 v[94:95], v126 offset:0\n\t"
    "v_cvt_pk_bf16_f32 v116, v116, v117\n\t"
    "v_cvt_pk_bf16_f32 v117, v118, v119\n\t"
    "ds_write_b64 v125, v[116:117] offset:256\n\t"
    "v_mfma_f32_16x16x32_bf16 v[116:119], v[48:51], v[196:199], 0\n\t"
    "v_add_u32_sdwa v98, s92, v111 dst_sel:DWORD dst_unused:UNUSED_PAD src0_sel:DWORD src1_sel:WORD_1\n\t"
    "v_add_u32_sdwa v111, s92, v111 dst_sel:DWORD dst_unused:UNUSED_PAD src0_sel:DWORD src1_sel:WORD_0\n\t"
    "ds_read_b64 v[96:97], v111 offset:0\n\t"
    "v_mfma_f32_16x16x32_bf16 v[116:119], v[52:55], v[200:203], v[116:119]\n\t"
    "ds_read_b64 v[98:99], v98 offset:0\n\t"
    "v_mfma_f32_16x16x32_bf16 v[116:119], v[56:59], v[252:255], v[116:119]\n\t"
    "buffer_load_dwordx4 v[108:111], v127, %12, s90 offen offset:0 sc1\n\t"
    "s_add_u32 s90, s90, s97\n\t"
    "v_add_u32_sdwa v126, s92, v112 dst_sel:DWORD dst_unused:UNUSED_PAD src0_sel:DWORD src1_sel:WORD_1\n\t"
    "v_add_u32_sdwa v112, s92, v112 dst_sel:DWORD dst_unused:UNUSED_PAD src0_sel:DWORD src1_sel:WORD_0\n\t"
    "v_mfma_f32_16x16x32_bf16 v[116:119], v[60:63], v[128:131], v[116:119]\n\t"
    "v_cvt_pk_bf16_f32 v0, v0, v1\n\t"
    "v_cvt_pk_bf16_f32 v1, v2, v3\n\t"
    "v_cvt_pk_bf16_f32 v80, v80, v81\n\t"
    "v_cvt_pk_bf16_f32 v81, v82, v83\n\t"
    "ds_write_b64 v125, v[80:81] offset:384\n\t"
    "v_add_u32 v79, s96, v127\n\t"
    "s_add_u32 s96, s96, 0x1000\n\t"
    "s_and_b32 s96, s96, 0x3000\n\t"
    "v_add_u32 v79, 128000, v79\n\t"
    "ds_read_b128 v[64:67], v79 offset:0\n\t"
    "ds_read_b128 v[68:71], v79 offset:1024\n\t"
    "ds_read_b128 v[72:75], v79 offset:2048\n\t"
    "ds_read_b128 v[76:79], v79 offset:3072\n\t"
    "s_waitcnt lgkmcnt(10)\n\t"
    "v_smfmac_f32_16x16x64_bf16 v[16:19], v[120:123], v[84:91], v124\n\t"
    "ds_read_b64 v[84:85], v112 offset:0\n\t"
    "v_mfma_f32_16x16x32_bf16 v[80:83], v[48:51], v[204:207], 0\n\t"
    "ds_read_b64 v[86:87], v126 offset:0\n\t"
    "v_mfma_f32_16x16x32_bf16 v[80:83], v[52:55], v[208:211], v[80:83]\n\t"
    "v_add_u32_sdwa v90, s92, v113 dst_sel:DWORD dst_unused:UNUSED_PAD src0_sel:DWORD src1_sel:WORD_1\n\t"
    "v_add_u32_sdwa v113, s92, v113 dst_sel:DWORD dst_unused:UNUSED_PAD src0_sel:DWORD src1_sel:WORD_0\n\t"
    "ds_read_b64 v[88:89], v113 offset:0\n\t"
    "v_mfma_f32_16x16x32_bf16 v[80:83], v[56:59], v[132:135], v[80:83]\n\t"
    "ds_read_b64 v[90:91], v90 offset:0\n\t"
    "v_mfma_f32_16x16x32_bf16 v[80:83], v[60:63], v[136:139], v[80:83]\n\t"
    "v_add_u32_sdwa v126, s92, v114 dst_sel:DWORD dst_unused:UNUSED_PAD src0_sel:DWORD src1_sel:WORD_1\n\t"
    "v_add_u32_sdwa v114, s92, v114 dst_sel:DWORD dst_unused:UNUSED_PAD src0_sel:DWORD src1_sel:WORD_0\n\t"
    "v_cvt_pk_bf16_f32 v116, v116, v117\n\t"
    "v_cvt_pk_bf16_f32 v117, v118, v119\n\t"
    "ds_write_b64 v125, v[116:117] offset:512\n\t"
    "v_mfma_f32_16x16x32_bf16 v[116:119], v[48:51], v[156:159], 0\n\t"
    "v_cvt_pk_bf16_f32 v2, v4, v5\n\t"
    "v_cvt_pk_bf16_f32 v3, v6, v7\n\t"
    "v_mov_b64 v[4:5], 0\n\t"
    "v_mov_b64 v[6:7], 0\n\t"
    "s_waitcnt vmcnt(2) lgkmcnt(10)\n\t"
    "v_smfmac_f32_16x16x64_bf16 v[20:23], v[120:123], v[92:99], v124\n\t"
    "ds_read_b64 v[92:93], v114 offset:0\n\t"
    "v_mfma_f32_16x16x32_bf16 v[116:119], v[52:55], v[160:163], v[116:119]\n\t"
    "ds_read_b64 v[94:95], v126 offset:0\n\t"
    "v_mfma_f32_16x16x32_bf16 v[116:119], v[56:59], v[140:143], v[116:119]\n\t"
    "v_add_u32_sdwa v98, s92, v115 dst_sel:DWORD dst_unused:UNUSED_PAD src0_sel:DWORD src1_sel:WORD_1\n\t"
    "v_add_u32_sdwa v115, s92, v115 dst_sel:DWORD dst_unused:UNUSED_PAD src0_sel:DWORD src1_sel:WORD_0\n\t"
    "ds_read_b64 v[96:97], v115 offset:0\n\t"
    "v_mfma_f32_16x16x32_bf16 v[116:119], v[60:63], v[144:147], v[116:119]\n\t"
    "ds_read_b64 v[98:99], v98 offset:0\n\t"
    "v_cvt_pk_bf16_f32 v80, v80, v81\n\t"
    "v_cvt_pk_bf16_f32 v81, v82, v83\n\t"
    "ds_write_b64 v125, v[80:81] offset:640\n\t"
    "v_mfma_f32_16x16x32_bf16 v[80:83], v[48:51], v[212:215], 0\n\t"
    "buffer_load_dwordx4 v[112:115], v127, %12, s91 offen offset:0 sc1\n\t"
    "s_add_u32 s91, s91, s97\n\t"
    "v_add_u32_sdwa v126, s92, v100 dst_sel:DWORD dst_unused:UNUSED_PAD src0_sel:DWORD src1_sel:WORD_1\n\t"
    "v_add_u32_sdwa v100, s92, v100 dst_sel:DWORD dst_unused:UNUSED_PAD src0_sel:DWORD src1_sel:WORD_0\n\t"
    "v_mfma_f32_16x16x32_bf16 v[80:83], v[52:55], v[216:219], v[80:83]\n\t"
    "v_cvt_pk_bf16_f32 v8, v8, v9\n\t"
    "v_cvt_pk_bf16_f32 v9, v10, v11\n\t"
    "s_waitcnt lgkmcnt(6)\n\t"
    "v_smfmac_f32_16x16x64_bf16 v[24:27], v[120:123], v[84:91], v124\n\t"
    "ds_read_b64 v[84:85], v100 offset:0\n\t"
    "v_mfma_f32_16x16x32_bf16 v[80:83], v[56:59], v[148:151], v[80:83]\n\t"
    "ds_read_b64 v[86:87], v126 offset:0\n\t"
    "v_mfma_f32_16x16x32_bf16 v[80:83], v[60:63], v[152:155], v[80:83]\n\t"
    "v_add_u32_sdwa v90, s92, v101 dst_sel:DWORD dst_unused:UNUSED_PAD src0_sel:DWORD src1_sel:WORD_1\n\t"
    "v_add_u32_sdwa v101, s92, v101 dst_sel:DWORD dst_unused:UNUSED_PAD src0_sel:DWORD src1_sel:WORD_0\n\t"
    "ds_read_b64 v[88:89], v101 offset:0\n\t"
    "v_cvt_pk_bf16_f32 v116, v116, v117\n\t"
    "v_cvt_pk_bf16_f32 v117, v118, v119\n\t"
    "ds_write_b64 v125, v[116:117] offset:768\n\t"
    "v_mfma_f32_16x16x32_bf16 v[116:119], v[64:67], v[164:167], 0\n\t"
    "ds_read_b64 v[90:91], v90 offset:0\n\t"
    "v_mfma_f32_16x16x32_bf16 v[116:119], v[68:71], v[168:171], v[116:119]\n\t"
    "v_add_u32_sdwa v126, s92, v102 dst_sel:DWORD dst_unused:UNUSED_PAD src0_sel:DWORD src1_sel:WORD_1\n\t"
    "v_add_u32_sdwa v102, s92, v102 dst_sel:DWORD dst_unused:UNUSED_PAD src0_sel:DWORD src1_sel:WORD_0\n\t"
    "v_mfma_f32_16x16x32_bf16 v[116:119], v[72:75], v[220:223], v[116:119]\n\t"
    "v_cvt_pk_bf16_f32 v10, v12, v13\n\t"
    "v_cvt_pk_bf16_f32 v11, v14, v15\n\t"
    "v_mov_b64 v[12:13], 0\n\t"
    "v_mov_b64 v[14:15], 0\n\t"
    "s_cmp_lg_u32 s47, 0\n\t"
    "s_cbranch_scc1 L_mp2_%=\n\t"
    "L_mp2b_%=:\n\t"
    "s_waitcnt vmcnt(2) lgkmcnt(6)\n\t"
    "v_smfmac_f32_16x16x64_bf16 v[28:31], v[120:123], v[92:99], v124\n\t"
    "ds_read_b64 v[92:93], v102 offset:0\n\t"
    "v_mfma_f32_16x16x32_bf16 v[116:119], v[76:79], v[224:227], v[116:119]\n\t"
    "ds_read_b64 v[94:95], v126 offset:0\n\t"
    "v_cvt_pk_bf16_f32 v80, v80, v81\n\t"
    "v_cvt_pk_bf16_f32 v81, v82, v83\n\t"
    "ds_write_b64 v125, v[80:81] offset:872\n\t"
    "v_mfma_f32_16x16x32_bf16 v[80:83], v[64:67], v[172:175], 0\n\t"
    "v_add_u32_sdwa v98, s92, v103 dst_sel:DWORD dst_unused:UNUSED_PAD src0_sel:DWORD src1_sel:WORD_1\n\t"
    "v_add_u32_sdwa v103, s92, v103 dst_sel:DWORD dst_unused:UNUSED_PAD src0_sel:DWORD src1_sel:WORD_0\n\t"
    "ds_read_b64 v[96:97], v103 offset:0\n\t"
    "v_mfma_f32_16x16x32_bf16 v[80:83], v[68:71], v[176:179], v[80:83]\n\t"
    "ds_read_b64 v[98:99], v98 offset:0\n\t"
    "v_mfma_f32_16x16x32_bf16 v[80:83], v[72:75], v[228:231], v[80:83]\n\t"
    "v_add_u32_sdwa v126, s92, v104 dst_sel:DWORD dst_unused:UNUSED_PAD src0_sel:DWORD src1_sel:WORD_1\n\t"
    "v_add_u32_sdwa v104, s92, v104 dst_sel:DWORD dst_unused:UNUSED_PAD src0_sel:DWORD src1_sel:WORD_0\n\t"
    "v_mfma_f32_16x16x32_bf16 v[80:83], v[76:79], v[232:235], v[80:83]\n\t"
    "v_cvt_pk_bf16_f32 v16, v16, v17\n\t"
    "v_cvt_pk_bf16_f32 v17, v18, v19\n\t"
    "v_cvt_pk_bf16_f32 v116, v116, v117\n\t"
    "v_cvt_pk_bf16_f32 v117, v118, v119\n\t"
    "ds_write_b64 v125, v[116:117] offset:32000\n\t"
    "s_waitcnt lgkmcnt(6)\n\t"
    "v_smfmac_f32_16x16x64_bf16 v[32:35], v[120:123], v[84:91], v124\n\t"
    "ds_read_b64 v[84:85], v104 offset:0\n\t"
    "v_mfma_f32_16x16x32_bf16 v[116:119], v[64:67], v[180:183], 0\n\t"
    "ds_read_b64 v[86:87], v126 offset:0\n\t"
    "v_mfma_f32_16x16x32_bf16 v[116:119], v[68:71], v[184:187], v[116:119]\n\t"
    "v_add_u32_sdwa v90, s92, v105 dst_sel:DWORD dst_unused:UNUSED_PAD src0_sel:DWORD src1_sel:WORD_1\n\t"
    "v_add_u32_sdwa v105, s92, v105 dst_sel:DWORD dst_unused:UNUSED_PAD src0_sel:DWORD src1_sel:WORD_0\n\t"
    "ds_read_b64 v[88:89], v105 offset:0\n\t"
    "v_mfma_f32_16x16x32_bf16 v[116:119], v[72:75], v[236:239], v[116:119]\n\t"
    "ds_read_b64 v[90:91], v90 offset:0\n\t"
    "v_mfma_f32_16x16x32_bf16 v[116:119], v[76:79], v[240:243], v[116:119]\n\t"
    "v_add_u32_sdwa v126, s92, v106 dst_sel:DWORD dst_unused:UNUSED_PAD src0_sel:DWORD src1_sel:WORD_1\n\t"
    "v_add_u32_sdwa v106, s92, v106 dst_sel:DWORD dst_unused:UNUSED_PAD src0_sel:DWORD src1_sel:WORD_0\n\t"
    "v_cvt_pk_bf16_f32 v80, v80, v81\n\t"
    "v_cvt_pk_bf16_f32 v81, v82, v83\n\t"
    "ds_write_b64 v125, v[80:81] offset:32128\n\t"
    "v_mfma_f32_16x16x32_bf16 v[80:83], v[64:67], v[188:191], 0\n\t"
    "v_cvt_pk_bf16_f32 v18, v20, v21\n\t"
    "v_cvt_pk_bf16_f32 v19, v22, v23\n\t"
    "v_mov_b64 v[20:21], 0\n\t"
    "v_mov_b64 v[22:23], 0\n\t"
    "s_waitcnt vmcnt(0)\n\t"
    "s_cmp_lg_u32 s47, 0\n\t"
    "s_cbranch_scc1 L_mp3_%=\n\t"
    "L_mp3b_%=:\n\t"
    "s_barrier\n\t"
    "s_add_u32 s84, s84, s95\n\t"
    "buffer_load_dwordx4 v[100:103], v127, %12, s84 offen offset:0 sc1\n\t"
    "s_lshl_b32 s50, %16, 10\n\t"
    "s_add_u32 s63, s63, s50\n\t"
    "s_add_u32 s50, s50, 0x2000\n\t"
    "s_xor_b32 s50, s50, 0x2000\n\t"
    "s_add_u32 m0, s50, 128000\n\t"
    "s_add_u32 s63, s63, 0x4000\n\t"
    "buffer_load_dwordx4 v127, %12, s63 offen sc1 lds\n\t"
    "s_cmp_lt_u32 %16, 4\n\t"
    "s_cbranch_scc1 L_sp1_%=\n\t"
    "s_setprio 1\n\t"
    "L_sp1_%=:\n\t"
    "s_waitcnt lgkmcnt(6)\n\t"
    "v_smfmac_f32_16x16x64_bf16 v[36:39], v[120:123], v[92:99], v124\n\t"
    "ds_read_b64 v[92:93], v106 offset:0\n\t"
    "v_mfma_f32_16x16x32_bf16 v[80:83], v[68:71], v[192:195], v[80:83]\n\t"
    "ds_read_b64 v[94:95], v126 offset:0\n\t"
    "v_mfma_f32_16x16x32_bf16 v[80:83], v[72:75], v[244:247], v[80:83]\n\t"
    "v_add_u32_sdwa v98, s92, v107 dst_sel:DWORD dst_unused:UNUSED_PAD src0_sel:DWORD src1_sel:WORD_1\n\t"
    "v_add_u32_sdwa v107, s92, v107 dst_sel:DWORD dst_unused:UNUSED_PAD src0_sel:DWORD src1_sel:WORD_0\n\t"
    "ds_read_b64 v[96:97], v107 offset:0\n\t"
    "v_mfma_f32_16x16x32_bf16 v[80:83], v[76:79], v[248:251], v[80:83]\n\t"
    "ds_read_b64 v[98:99], v98 offset:0\n\t"
    "v_cvt_pk_bf16_f32 v116, v116, v117\n\t"
    "v_cvt_pk_bf16_f32 v117, v118, v119\n\t"
    "ds_write_b64 v125, v[116:117] offset:32256\n\t"
    "v_mfma_f32_16x16x32_bf16 v[116:119], v[64:67], v[196:199], 0\n\t"
    "buffer_load_dwordx4 v[104:107], v127, %12, s84 offen offset:1024 sc1\n\t"
    "v_add_u32_sdwa v126, s92, v108 dst_sel:DWORD dst_unused:UNUSED_PAD src0_sel:DWORD src1_sel:WORD_1\n\t"
    "v_add_u32_sdwa v108, s92, v108 dst_sel:DWORD dst_unused:UNUSED_PAD src0_sel:DWORD src1_sel:WORD_0\n\t"
    "v_mfma_f32_16x16x32_bf16 v[116:119], v[68:71], v[200:203], v[116:119]\n\t"
    "v_cvt_pk_bf16_f32 v24, v24, v25\n\t"
    "v_cvt_pk_bf16_f32 v25, v26, v27\n\t"
    "v_mov_b64 v[48:49], 0\n\t"
    "v_mov_b64 v[50:51], 0\n\t"
    "v_mov_b64 v[52:53], 0\n\t"
    "v_mov_b64 v[54:55], 0\n\t"
    "s_waitcnt lgkmcnt(6)\n\t"
    "v_smfmac_f32_16x16x64_bf16 v[40:43], v[120:123], v[84:91], v124\n\t"
    "ds_read_b64 v[84:85], v108 offset:0\n\t"
    "v_mfma_f32_16x16x32_bf16 v[116:119], v[72:75], v[252:255], v[116:119]\n\t"
    "ds_read_b64 v[86:87], v126 offset:0\n\t"
    "v_mfma_f32_16x16x32_bf16 v[116:119], v[76:79], v[128:131], v[116:119]\n\t"
    "v_add_u32_sdwa v90, s92, v109 dst_sel:DWORD dst_unused:UNUSED_PAD src0_sel:DWORD src1_sel:WORD_1\n\t"
    "v_add_u32_sdwa v109, s92, v109 dst_sel:DWORD dst_unused:UNUSED_PAD src0_sel:DWORD src1_sel:WORD_0\n\t"
    "ds_read_b64 v[88:89], v109 offset:0\n\t"
    "v_cvt_pk_bf16_f32 v80, v80, v81\n\t"
    "v_cvt_pk_bf16_f32 v81, v82, v83\n\t"
    "ds_write_b64 v125, v[80:81] offset:32384\n\t"
    "v_mfma_f32_16x16x32_bf16 v[80:83], v[64:67], v[204:207], 0\n\t"
    "ds_read_b64 v[90:91], v90 offset:0\n\t"
    "v_mfma_f32_16x16x32_bf16 v[80:83], v[68:71], v[208:211], v[80:83]\n\t"
    "v_add_u32_sdwa v126, s92, v110 dst_sel:DWORD dst_unused:UNUSED_PAD src0_sel:DWORD src1_sel:WORD_1\n\t"
    "v_add_u32_sdwa v110, s92, v110 dst_sel:DWORD dst_unused:UNUSED_PAD src0_sel:DWORD src1_sel:WORD_0\n\t"
    "v_mfma_f32_16x16x32_bf16 v[80:83], v[72:75], v[132:135], v[80:83]\n\t"
    "v_cvt_pk_bf16_f32 v26, v28, v29\n\t"
    "v_cvt_pk_bf16_f32 v27, v30, v31\n\t"
    "v_mov_b64 v[28:29], 0\n\t"
    "v_mov_b64 v[30:31], 0\n\t"
    "s_waitcnt lgkmcnt(6)\n\t"
    "v_smfmac_f32_16x16x64_bf16 v[44:47], v[120:123], v[92:99], v124\n\t"
    "ds_read_b64 v[92:93], v110 offset:0\n\t"
    "v_mfma_f32_16x16x32_bf16 v[80:83], v[76:79], v[136:139], v[80:83]\n\t"
    "ds_read_b64 v[94:95], v126 offset:0\n\t"
    "v_cvt_pk_bf16_f32 v116, v116, v117\n\t"
    "v_cvt_pk_bf16_f32 v117, v118, v119\n\t"
    "ds_write_b64 v125, v[116:117] offset:32512\n\t"
    "v_mfma_f32_16x16x32_bf16 v[116:119], v[64:67], v[156:159], 0\n\t"
    "v_add_u32_sdwa v98, s92, v111 dst_sel:DWORD dst_unused:UNUSED_PAD src0_sel:DWORD src1_sel:WORD_1\n\t"
    "v_add_u32_sdwa v111, s92, v111 dst_sel:DWORD dst_unused:UNUSED_PAD src0_sel:DWORD src1_sel:WORD_0\n\t"
    "ds_read_b64 v[96:97], v111 offset:0\n\t"
    "v_mfma_f32_16x16x32_bf16 v[116:119], v[68:71], v[160:163], v[116:119]\n\t"
    "ds_read_b64 v[98:99], v98 offset:0\n\t"
    "v_mfma_f32_16x16x32_bf16 v[116:119], v[72:75], v[140:143], v[116:119]\n\t"
    "s_add_u32 s85, s85, s95\n\t"
    "buffer_load_dwordx4 v[108:111], v127, %12, s85 offen offset:0 sc1\n\t"
    "v_add_u32_sdwa v126, s92, v112 dst_sel:DWORD dst_unused:UNUSED_PAD src0_sel:DWORD src1_sel:WORD_1\n\t"
    "v_add_u32_sdwa v112, s92, v112 dst_sel:DWORD dst_unused:UNUSED_PAD src0_sel:DWORD src1_sel:WORD_0\n\t"
    "v_mfma_f32_16x16x32_bf16 v[116:119], v[76:79], v[144:147], v[116:119]\n\t"
    "v_cvt_pk_bf16_f32 v32, v32, v33\n\t"
    "v_cvt_pk_bf16_f32 v33, v34, v35\n\t"
    "v_cvt_pk_bf16_f32 v80, v80, v81\n\t"
    "v_cvt_pk_bf16_f32 v81, v82, v83\n\t"
    "ds_write_b64 v125, v[80:81] offset:32640\n\t"
    "v_mov_b64 v[56:57], 0\n\t"
    "v_mov_b64 v[58:59], 0\n\t"
    "v_mov_b64 v[60:61], 0\n\t"
    "v_mov_b64 v[62:63], 0\n\t"
    "s_waitcnt lgkmcnt(6)\n\t"
    "v_smfmac_f32_16x16x64_bf16 v[48:51], v[120:123], v[84:91], v124\n\t"
    "ds_read_b64 v[84:85], v112 offset:0\n\t"
    "v_mfma_f32_16x16x32_bf16 v[80:83], v[64:67], v[212:215], 0\n\t"
    "ds_read_b64 v[86:87], v126 offset:0\n\t"
    "v_mfma_f32_16x16x32_bf16 v[80:83], v[68:71], v[216:219], v[80:83]\n\t"
    "v_add_u32_sdwa v90, s92, v113 dst_sel:DWORD dst_unused:UNUSED_PAD src0_sel:DWORD src1_sel:WORD_1\n\t"
    "v_add_u32_sdwa v113, s92, v113 dst_sel:DWORD dst_unused:UNUSED_PAD src0_sel:DWORD src1_sel:WORD_0\n\t"
    "ds_read_b64 v[88:89], v113 offset:0\n\t"
    "v_mfma_f32_16x16x32_bf16 v[80:83], v[72:75], v[148:151], v[80:83]\n\t"
    "ds_read_b64 v[90:91], v90 offset:0\n\t"
    "v_mfma_f32_16x16x32_bf16 v[80:83], v[76:79], v[152:155], v[80:83]\n\t"
    "v_add_u32_sdwa v126, s92, v114 dst_sel:DWORD dst_unused:UNUSED_PAD src0_sel:DWORD src1_sel:WORD_1\n\t"
    "v_add_u32_sdwa v114, s92, v114 dst_sel:DWORD dst_unused:UNUSED_PAD src0_sel:DWORD src1_sel:WORD_0\n\t"
    "v_cvt_pk_bf16_f32 v116, v116, v117\n\t"
    "v_cvt_pk_bf16_f32 v117, v118, v119\n\t"
    "ds_write_b64 v125, v[116:117] offset:32768\n\t"
    "s_nop 2\n\t"
    "v_cvt_pk_bf16_f32 v80, v80, v81\n\t"
    "v_cvt_pk_bf16_f32 v81, v82, v83\n\t"
    "ds_write_b64 v125, v[80:81] offset:32872\n\t"
    "v_cvt_pk_bf16_f32 v34, v36, v37\n\t"
    "v_cvt_pk_bf16_f32 v35, v38, v39\n\t"
    "v_mov_b64 v[36:37], 0\n\t"
    "v_mov_b64 v[38:39], 0\n\t"
    "v_cvt_pk_bf16_f32 v40, v40, v41\n\t"
    "v_cvt_pk_bf16_f32 v41, v42, v43\n\t"
    "v_cvt_pk_bf16_f32 v42, v44, v45\n\t"
    "v_cvt_pk_bf16_f32 v43, v46, v47\n\t"
    "v_mov_b64 v[44:45], 0\n\t"
    "v_mov_b64 v[46:47], 0\n\t"
    "s_waitcnt vmcnt(3) lgkmcnt(7)\n\t"
    "v_smfmac_f32_16x16x64_bf16 v[52:55], v[120:123], v[92:99], v124\n\t"
    "ds_read_b64 v[92:93], v114 offset:0\n\t"
    "ds_read_b64 v[94:95], v126 offset:0\n\t"
    "v_add_u32_sdwa v98, s92, v115 dst_sel:DWORD dst_unused:UNUSED_PAD src0_sel:DWORD src1_sel:WORD_1\n\t"
    "v_add_u32_sdwa v115, s92, v115 dst_sel:DWORD dst_unused:UNUSED_PAD src0_sel:DWORD src1_sel:WORD_0\n\t"
    "ds_read_b64 v[96:97], v115 offset:0\n\t"
    "ds_read_b64 v[98:99], v98 offset:0\n\t"
    "buffer_load_dwordx4 v[112:115], v127, %12, s85 offen offset:1024 sc1\n\t"
    "s_sub_u32 s92, 64000, s92\n\t"
    "v_add_u32_sdwa v126, s92, v100 dst_sel:DWORD dst_unused:UNUSED_PAD src0_sel:DWORD src1_sel:WORD_1\n\t"
    "v_add_u32_sdwa v100, s92, v100 dst_sel:DWORD dst_unused:UNUSED_PAD src0_sel:DWORD src1_sel:WORD_0\n\t"
    "v_cvt_pk_bf16_f32 v48, v48, v49\n\t"
    "v_cvt_pk_bf16_f32 v49, v50, v51\n\t"
    "s_waitcnt lgkmcnt(4)\n\t"
    "s_waitcnt lgkmcnt(0)\n\t"
    "s_waitcnt vmcnt(3)\n\t"
    "s_barrier\n\t"
    "v_add_u32 v125, s93, v125\n\t"
    "s_sub_u32 s93, 0, s93\n\t"
    "v_smfmac_f32_16x16x64_bf16 v[56:59], v[120:123], v[84:91], v124\n\t"
    "ds_read_b64 v[84:85], v100 offset:0\n\t"
    "ds_read_b64 v[86:87], v126 offset:0\n\t"
    "v_add_u32_sdwa v90, s92, v101 dst_sel:DWORD dst_unused:UNUSED_PAD src0_sel:DWORD src1_sel:WORD_1\n\t"
    "v_add_u32_sdwa v101, s92, v101 dst_sel:DWORD dst_unused:UNUSED_PAD src0_sel:DWORD src1_sel:WORD_0\n\t"
    "ds_read_b64 v[88:89], v101 offset:0\n\t"
    "ds_read_b64 v[90:91], v90 offset:0\n\t"
    "v_add_u32_sdwa v126, s92, v102 dst_sel:DWORD dst_unused:UNUSED_PAD src0_sel:DWORD src1_sel:WORD_1\n\t"
    "v_add_u32_sdwa v102, s92, v102 dst_sel:DWORD dst_unused:UNUSED_PAD src0_sel:DWORD src1_sel:WORD_0\n\t"
    "v_cvt_pk_bf16_f32 v50, v52, v53\n\t"
    "v_cvt_pk_bf16_f32 v51, v54, v55\n\t"
    "v_mov_b64 v[52:53], 0\n\t"
    "v_mov_b64 v[54:55], 0\n\t"
    "s_waitcnt vmcnt(2)\n\t"
    "v_smfmac_f32_16x16x64_bf16 v[60:63], v[120:123], v[92:99], v124\n\t"
    "ds_read_b64 v[92:93], v102 offset:0\n\t"
    "ds_read_b64 v[94:95], v126 offset:0\n\t"
    "v_add_u32_sdwa v98, s92, v103 dst_sel:DWORD dst_unused:UNUSED_PAD src0_sel:DWORD src1_sel:WORD_1\n\t"
    "v_add_u32_sdwa v103, s92, v103 dst_sel:DWORD dst_unused:UNUSED_PAD src0_sel:DWORD src1_sel:WORD_0\n\t"
    "ds_read_b64 v[96:97], v103 offset:0\n\t"
    "ds_read_b64 v[98:99], v98 offset:0\n\t"
    "s_add_u32 s86, s86, s95\n\t"
    "buffer_load_dwordx4 v[100:103], v127, %12, s86 offen offset:0 sc1\n\t"
    "v_add_u32_sdwa v126, s92, v104 dst_sel:DWORD dst_unused:UNUSED_PAD src0_sel:DWORD src1_sel:WORD_1\n\t"
    "v_add_u32_sdwa v104, s92, v104 dst_sel:DWORD dst_unused:UNUSED_PAD src0_sel:DWORD src1_sel:WORD_0\n\t"
    "v_cvt_pk_bf16_f32 v56, v56, v57\n\t"
    "v_cvt_pk_bf16_f32 v57, v58, v59\n\t"
    "v_add_u32 v79, s96, v127\n\t"
    "s_add_u32 s96, s96, 0x1000\n\t"
    "s_and_b32 s96, s96, 0x3000\n\t"
    "v_add_u32 v79, 128000, v79\n\t"
    "ds_read_b128 v[64:67], v79 offset:0\n\t"
    "ds_read_b128 v[68:71], v79 offset:1024\n\t"
    "ds_read_b128 v[72:75], v79 offset:2048\n\t"
    "ds_read_b128 v[76:79], v79 offset:3072\n\t"
    "v_mov_b64 v[116:117], 0\n\t"
    "v_mov_b64 v[118:119], 0\n\t"
    "v_cvt_pk_bf16_f32 v58, v60, v61\n\t"
    "v_cvt_pk_bf16_f32 v59, v62, v63\n\t"
    "v_mov_b64 v[60:61], 0\n\t"
    "v_mov_b64 v[62:63], 0\n\t"
    "s_mov_b32 s66, 4\n\t"
    "s_branch L_loop_%=\n\t"
    ".p2align 9\n\t"
    "s_nop 0\n\t"
    "s_nop 0\n\t"
    "s_nop 0\n\t"
    "s_nop 0\n\t"
    "s_nop 0\n\t"
    "s_nop 0\n\t"
    "s_nop 0\n\t"
    "s_nop 0\n\t"
    "s_nop 0\n\t"
    "s_nop 0\n\t"
    "s_nop 0\n\t"
    "s_nop 0\n\t"
    "s_nop 0\n\t"
    "s_nop 0\n\t"
    "s_nop 0\n\t"
    "s_nop 0\n\t"
    "s_nop 0\n\t"
    "s_nop 0\n\t"
    "s_nop 0\n\t"
    "s_nop 0\n\t"
    "s_nop 0\n\t"
    "s_nop 0\n\t"
    "s_nop 0\n\t"
    "s_nop 0\n\t"
    "s_nop 0\n\t"
    "s_nop 0\n\t"
    "s_nop 0\n\t"
    "s_nop 0\n\t"
    "s_nop 0\n\t"
    "s_nop 0\n\t"
    "s_nop 0\n\t"
    "s_nop 0\n\t"
    "s_nop 0\n\t"
    "L_loop_%=:\n\t"
    "s_waitcnt lgkmcnt(8)\n\t"
    "v_smfmac_f32_16x16x64_bf16 v[116:119], v[120:123], v[84:91], v124\n\t"
    "ds_read_b64 v[84:85], v104 offset:0\n\t"
    "ds_read_b64 v[86:87], v126 offset:0\n\t"
    "v_add_u32_sdwa v90, s92, v105 dst_sel:DWORD dst_unused:UNUSED_PAD src0_sel:DWORD src1_sel:WORD_1\n\t"
    "v_add_u32_sdwa v105, s92, v105 dst_sel:DWORD dst_unused:UNUSED_PAD src0_sel:DWORD src1_sel:WORD_0\n\t"
    "ds_read_b64 v[88:89], v105 offset:0\n\t"
    "ds_read_b64 v[90:91], v90 offset:0\n\t"
    "v_add_u32_sdwa v126, s92, v106 dst_sel:DWORD dst_unused:UNUSED_PAD src0_sel:DWORD src1_sel:WORD_1\n\t"
    "v_add_u32_sdwa v106, s92, v106 dst_sel:DWORD dst_unused:UNUSED_PAD src0_sel:DWORD src1_sel:WORD_0\n\t"
    "s_waitcnt vmcnt(2) lgkmcnt(8)\n\t"
    "v_smfmac_f32_16x16x64_bf16 v[116:119], v[120:123], v[92:99], v124\n\t"
    "ds_read_b64 v[92:93], v106 offset:0\n\t"
    "ds_read_b64 v[94:95], v126 offset:0\n\t"
    "v_add_u32_sdwa v98, s92, v107 dst_sel:DWORD dst_unused:UNUSED_PAD src0_sel:DWORD src1_sel:WORD_1\n\t"
    "v_add_u32_sdwa v107, s92, v107 dst_sel:DWORD dst_unused:UNUSED_PAD src0_sel:DWORD src1_sel:WORD_0\n\t"
    "ds_read_b64 v[96:97], v107 offset:0\n\t"
    "ds_read_b64 v[98:99], v98 offset:0\n\t"
    "buffer_load_dwordx4 v[104:107], v127, %12, s86 offen offset:1024 sc1\n\t"
    "v_add_u32_sdwa v126, s92, v108 dst_sel:DWORD dst_unused:UNUSED_PAD src0_sel:DWORD src1_sel:WORD_1\n\t"
    "v_cvt_pk_bf16_f32 v4, v116, v117\n\t"
    "v_cvt_pk_bf16_f32 v5, v118, v119\n\t"
    "v_and_b32 v119, s67, v7\n\t"
    "v_lshlrev_b32 v118, 16, v7\n\t"
    "v_and_b32 v117, s67, v6\n\t"
    "v_lshlrev_b32 v116, 16, v6\n\t"
    "v_add_u32_sdwa v108, s92, v108 dst_sel:DWORD dst_unused:UNUSED_PAD src0_sel:DWORD src1_sel:WORD_0\n\t"
    "s_waitcnt lgkmcnt(4)\n\t"
    "v_smfmac_f32_16x16x64_bf16 v[116:119], v[120:123], v[84:91], v124\n\t"
    "ds_read_b64 v[84:85], v108 offset:0\n\t"
    "v_mfma_f32_16x16x32_bf16 v[80:83], v[64:67], v[164:167], 0\n\t"
    "ds_read_b64 v[86:87], v126 offset:0\n\t"
    "v_mfma_f32_16x16x32_bf16 v[80:83], v[68:71], v[168:171], v[80:83]\n\t"
    "v_add_u32_sdwa v90, s92, v109 dst_sel:DWORD dst_unused:UNUSED_PAD src0_sel:DWORD src1_sel:WORD_1\n\t"
    "v_add_u32_sdwa v109, s92, v109 dst_sel:DWORD dst_unused:UNUSED_PAD src0_sel:DWORD src1_sel:WORD_0\n\t"
    "ds_read_b64 v[88:89], v109 offset:0\n\t"
    "v_mfma_f32_16x16x32_bf16 v[80:83], v[72:75], v[220:223], v[80:83]\n\t"
    "ds_read_b64 v[90:91], v90 offset:0\n\t"
    "v_mfma_f32_16x16x32_bf16 v[80:83], v[76:79], v[224:227], v[80:83]\n\t"
    "v_add_u32_sdwa v126, s92, v110 dst_sel:DWORD dst_unused:UNUSED_PAD src0_sel:DWORD src1_sel:WORD_1\n\t"
    "v_add_u32_sdwa v110, s92, v110 dst_sel:DWORD dst_unused:UNUSED_PAD src0_sel:DWORD src1_sel:WORD_0\n\t"
    "s_waitcnt vmcnt(2) lgkmcnt(4)\n\t"
    "v_smfmac_f32_16x16x64_bf16 v[116:119], v[120:123], v[92:99], v124\n\t"
    "ds_read_b64 v[92:93], v110 offset:0\n\t"
    "ds_read_b64 v[94:95], v126 offset:0\n\t"
    "v_add_u32_sdwa v98, s92, v111 dst_sel:DWORD dst_unused:UNUSED_PAD src0_sel:DWORD src1_sel:WORD_1\n\t"
    "v_add_u32_sdwa v111, s92, v111 dst_sel:DWORD dst_unused:UNUSED_PAD src0_sel:DWORD src1_sel:WORD_0\n\t"
    "ds_read_b64 v[96:97], v111 offset:0\n\t"
    "ds_read_b64 v[98:99], v98 offset:0\n\t"
    "s_add_u32 s87, s87, s95\n\t"
    "buffer_load_dwordx4 v[108:111], v127, %12, s87 offen offset:0 sc1\n\t"
    "v_add_u32_sdwa v126, s92, v112 dst_sel:DWORD dst_unused:UNUSED_PAD src0_sel:DWORD src1_sel:WORD_1\n\t"
    "v_cvt_pk_bf16_f32 v6, v116, v117\n\t"
    "v_cvt_pk_bf16_f32 v7, v118, v119\n\t"
    "v_and_b32 v119, s67, v13\n\t"
    "v_lshlrev_b32 v118, 16, v13\n\t"
    "v_and_b32 v117, s67, v12\n\t"
    "v_lshlrev_b32 v116, 16, v12\n\t"
    "v_add_u32_sdwa v112, s92, v112 dst_sel:DWORD dst_unused:UNUSED_PAD src0_sel:DWORD src1_sel:WORD_0\n\t"
    "s_waitcnt lgkmcnt(4)\n\t"
    "v_smfmac_f32_16x16x64_bf16 v[116:119], v[120:123], v[84:91], v124\n\t"
    "ds_read_b64 v[84:85], v112 offset:0\n\t"
    "ds_read_b64 v[86:87], v126 offset:0\n\t"
    "v_cvt_pk_bf16_f32 v80, v80, v81\n\t"
    "v_cvt_pk_bf16_f32 v81, v82, v83\n\t"
    "ds_write_b64 v125, v[80:81] offset:0\n\t"
    "v_mfma_f32_16x16x32_bf16 v[80:83], v[64:67], v[172:175], 0\n\t"
    "v_add_u32_sdwa v90, s92, v113 dst_sel:DWORD dst_unused:UNUSED_PAD src0_sel:DWORD src1_sel:WORD_1\n\t"
    "v_add_u32_sdwa v113, s92, v113 dst_sel:DWORD dst_unused:UNUSED_PAD src0_sel:DWORD src1_sel:WORD_0\n\t"
    "ds_read_b64 v[88:89], v113 offset:0\n\t"
    "v_mfma_f32_16x16x32_bf16 v[80:83], v[68:71], v[176:179], v[80:83]\n\t"
    "ds_read_b64 v[90:91], v90 offset:0\n\t"
    "v_mfma_f32_16x16x32_bf16 v[80:83], v[72:75], v[228:231], v[80:83]\n\t"
    "v_mfma_f32_16x16x32_bf16 v[80:83], v[76:79], v[232:235], v[80:83]\n\t"
    "v_add_u32_sdwa v126, s92, v114 dst_sel:DWORD dst_unused:UNUSED_PAD src0_sel:DWORD src1_sel:WORD_1\n\t"
    "v_add_u32_sdwa v114, s92, v114 dst_sel:DWORD dst_unused:UNUSED_PAD src0_sel:DWORD src1_sel:WORD_0\n\t"
    "s_waitcnt vmcnt(2) lgkmcnt(5)\n\t"
    "v_smfmac_f32_16x16x64_bf16 v[116:119], v[120:123], v[92:99], v124\n\t"
    "ds_read_b64 v[92:93], v114 offset:0\n\t"
    "ds_read_b64 v[94:95], v126 offset:0\n\t"
    "v_add_u32_sdwa v98, s92, v115 dst_sel:DWORD dst_unused:UNUSED_PAD src0_sel:DWORD src1_sel:WORD_1\n\t"
    "v_add_u32_sdwa v115, s92, v115 dst_sel:DWORD dst_unused:UNUSED_PAD src0_sel:DWORD src1_sel:WORD_0\n\t"
    "ds_read_b64 v[96:97], v115 offset:0\n\t"
    "ds_read_b64 v[98:99], v98 offset:0\n\t"
    "buffer_load_dwordx4 v[112:115], v127, %12, s87 offen offset:1024 sc1\n\t"
    "v_add_u32_sdwa v126, s92, v100 dst_sel:DWORD dst_unused:UNUSED_PAD src0_sel:DWORD src1_sel:WORD_1\n\t"
    "v_cvt_pk_bf16_f32 v12, v116, v117\n\t"
    "v_cvt_pk_bf16_f32 v13, v118, v119\n\t"
    "v_and_b32 v119, s67, v15\n\t"
    "v_lshlrev_b32 v118, 16, v15\n\t"
    "v_and_b32 v117, s67, v14\n\t"
    "v_lshlrev_b32 v116, 16, v14\n\t"
    "v_add_u32_sdwa v100, s92, v100 dst_sel:DWORD dst_unused:UNUSED_PAD src0_sel:DWORD src1_sel:WORD_0\n\t"
    "s_waitcnt lgkmcnt(4)\n\t"
    "v_smfmac_f32_16x16x64_bf16 v[116:119], v[120:123], v[84:91], v124\n\t"
    "ds_read_b64 v[84:85], v100 offset:0\n\t"
    "ds_read_b64 v[86:87], v126 offset:0\n\t"
    "v_cvt_pk_bf16_f32 v80, v80, v81\n\t"
    "v_cvt_pk_bf16_f32 v81, v82, v83\n\t"
    "ds_write_b64 v125, v[80:81] offset:128\n\t"
    "v_mfma_f32_16x16x32_bf16 v[80:83], v[64:67], v[180:183], 0\n\t"
    "v_add_u32_sdwa v90, s92, v101 dst_sel:DWORD dst_unused:UNUSED_PAD src0_sel:DWORD src1_sel:WORD_1\n\t"
    "v_add_u32_sdwa v101, s92, v101 dst_sel:DWORD dst_unused:UNUSED_PAD src0_sel:DWORD src1_sel:WORD_0\n\t"
    "ds_read_b64 v[88:89], v101 offset:0\n\t"
    "v_mfma_f32_16x16x32_bf16 v[80:83], v[68:71], v[184:187], v[80:83]\n\t"
    "ds_read_b64 v[90:91], v90 offset:0\n\t"
    "v_mfma_f32_16x16x32_bf16 v[80:83], v[72:75], v[236:239], v[80:83]\n\t"
    "v_mfma_f32_16x16x32_bf16 v[80:83], v[76:79], v[240:243], v[80:83]\n\t"
    "v_add_u32_sdwa v126, s92, v102 dst_sel:DWORD dst_unused:UNUSED_PAD src0_sel:DWORD src1_sel:WORD_1\n\t"
    "v_add_u32_sdwa v102, s92, v102 dst_sel:DWORD dst_unused:UNUSED_PAD src0_sel:DWORD src1_sel:WORD_0\n\t"
    "s_waitcnt vmcnt(2) lgkmcnt(5)\n\t"
    "v_smfmac_f32_16x16x64_bf16 v[116:119], v[120:123], v[92:99], v124\n\t"
    "ds_read_b64 v[92:93], v102 offset:0\n\t"
    "ds_read_b64 v[94:95], v126 offset:0\n\t"
    "v_add_u32_sdwa v98, s92, v103 dst_sel:DWORD dst_unused:UNUSED_PAD src0_sel:DWORD src1_sel:WORD_1\n\t"
    "v_add_u32_sdwa v103, s92, v103 dst_sel:DWORD dst_unused:UNUSED_PAD src0_sel:DWORD src1_sel:WORD_0\n\t"
    "ds_read_b64 v[96:97], v103 offset:0\n\t"
    "ds_read_b64 v[98:99], v98 offset:0\n\t"
    "s_add_u32 s88, s88, s95\n\t"
    "buffer_load_dwordx4 v[100:103], v127, %12, s88 offen offset:0 sc1\n\t"
    "v_add_u32_sdwa v126, s92, v104 dst_sel:DWORD dst_unused:UNUSED_PAD src0_sel:DWORD src1_sel:WORD_1\n\t"
    "v_cvt_pk_bf16_f32 v14, v116, v117\n\t"
    "v_cvt_pk_bf16_f32 v15, v118, v119\n\t"
    "v_and_b32 v119, s67, v21\n\t"
    "v_lshlrev_b32 v118, 16, v21\n\t"
    "v_and_b32 v117, s67, v20\n\t"
    "v_lshlrev_b32 v116, 16, v20\n\t"
    "v_add_u32_sdwa v104, s92, v104 dst_sel:DWORD dst_unused:UNUSED_PAD src0_sel:DWORD src1_sel:WORD_0\n\t"
    "s_waitcnt lgkmcnt(4)\n\t"
    "v_smfmac_f32_16x16x64_bf16 v[116:119], v[120:123], v[84:91], v124\n\t"
    "ds_read_b64 v[84:85], v104 offset:0\n\t"
    "ds_read_b64 v[86:87], v126 offset:0\n\t"
    "v_cvt_pk_bf16_f32 v80, v80, v81\n\t"
    "v_cvt_pk_bf16_f32 v81, v82, v83\n\t"
    "ds_write_b64 v125, v[80:81] offset:256\n\t"
    "v_add_u32_sdwa v90, s92, v105 dst_sel:DWORD dst_unused:UNUSED_PAD src0_sel:DWORD src1_sel:WORD_1\n\t"
    "v_add_u32_sdwa v105, s92, v105 dst_sel:DWORD dst_unused:UNUSED_PAD src0_sel:DWORD src1_sel:WORD_0\n\t"
    "ds_read_b64 v[88:89], v105 offset:0\n\t"
    "ds_read_b64 v[90:91], v90 offset:0\n\t"
    "v_add_u32_sdwa v126, s92, v106 dst_sel:DWORD dst_unused:UNUSED_PAD src0_sel:DWORD src1_sel:WORD_1\n\t"
    "v_add_u32_sdwa v106, s92, v106 dst_sel:DWORD dst_unused:UNUSED_PAD src0_sel:DWORD src1_sel:WORD_0\n\t"
    "s_waitcnt vmcnt(2) lgkmcnt(5)\n\t"
    "v_smfmac_f32_16x16x64_bf16 v[116:119], v[120:123], v[92:99], v124\n\t"
    "ds_read_b64 v[92:93], v106 offset:0\n\t"
    "ds_read_b64 v[94:95], v126 offset:0\n\t"
    "v_add_u32_sdwa v98, s92, v107 dst_sel:DWORD dst_unused:UNUSED_PAD src0_sel:DWORD src1_sel:WORD_1\n\t"
    "v_add_u32_sdwa v107, s92, v107 dst_sel:DWORD dst_unused:UNUSED_PAD src0_sel:DWORD src1_sel:WORD_0\n\t"
    "ds_read_b64 v[96:97], v107 offset:0\n\t"
    "ds_read_b64 v[98:99], v98 offset:0\n\t"
    "buffer_load_dwordx4 v[104:107], v127, %12, s88 offen offset:1024 sc1\n\t"
    "v_add_u32_sdwa v126, s92, v108 dst_sel:DWORD dst_unused:UNUSED_PAD src0_sel:DWORD src1_sel:WORD_1\n\t"
    "v_cvt_pk_bf16_f32 v20, v116, v117\n\t"
    "v_cvt_pk_bf16_f32 v21, v118, v119\n\t"
    "v_and_b32 v119, s67, v23\n\t"
    "v_lshlrev_b32 v118, 16, v23\n\t"
    "v_and_b32 v117, s67, v22\n\t"
    "v_lshlrev_b32 v116, 16, v22\n\t"
    "v_add_u32_sdwa v108, s92, v108 dst_sel:DWORD dst_unused:UNUSED_PAD src0_sel:DWORD src1_sel:WORD_0\n\t"
    "s_waitcnt lgkmcnt(4)\n\t"
    "v_smfmac_f32_16x16x64_bf16 v[116:119], v[120:123], v[84:91], v124\n\t"
    "ds_read_b64 v[84:85], v108 offset:0\n\t"
    "v_mfma_f32_16x16x32_bf16 v[80:83], v[64:67], v[188:191], 0\n\t"
    "ds_read_b64 v[86:87], v126 offset:0\n\t"
    "v_mfma_f32_16x16x32_bf16 v[80:83], v[68:71], v[192:195], v[80:83]\n\t"
    "v_add_u32_sdwa v90, s92, v109 dst_sel:DWORD dst_unused:UNUSED_PAD src0_sel:DWORD src1_sel:WORD_1\n\t"
    "v_add_u32_sdwa v109, s92, v109 dst_sel:DWORD dst_unused:UNUSED_PAD src0_sel:DWORD src1_sel:WORD_0\n\t"
    "ds_read_b64 v[88:89], v109 offset:0\n\t"
    "v_mfma_f32_16x16x32_bf16 v[80:83], v[72:75], v[244:247], v[80:83]\n\t"
    "ds_read_b64 v[90:91], v90 offset:0\n\t"
    "v_mfma_f32_16x16x32_bf16 v[80:83], v[76:79], v[248:251], v[80:83]\n\t"
    "v_add_u32_sdwa v126, s92, v110 dst_sel:DWORD dst_unused:UNUSED_PAD src0_sel:DWORD src1_sel:WORD_1\n\t"
    "v_add_u32_sdwa v110, s92, v110 dst_sel:DWORD dst_unused:UNUSED_PAD src0_sel:DWORD src1_sel:WORD_0\n\t"
    "s_waitcnt vmcnt(2) lgkmcnt(4)\n\t"
    "v_smfmac_f32_16x16x64_bf16 v[116:119], v[120:123], v[92:99], v124\n\t"
    "ds_read_b64 v[92:93], v110 offset:0\n\t"
    "ds_read_b64 v[94:95], v126 offset:0\n\t"
    "v_add_u32_sdwa v98, s92, v111 dst_sel:DWORD dst_unused:UNUSED_PAD src0_sel:DWORD src1_sel:WORD_1\n\t"
    "v_add_u32_sdwa v111, s92, v111 dst_sel:DWORD dst_unused:UNUSED_PAD src0_sel:DWORD src1_sel:WORD_0\n\t"
    "ds_read_b64 v[96:97], v111 offset:0\n\t"
    "ds_read_b64 v[98:99], v98 offset:0\n\t"
    "s_add_u32 s89, s89, s95\n\t"
    "buffer_load_dwordx4 v[108:111], v127, %12, s89 offen offset:0 sc1\n\t"
    "v_add_u32_sdwa v126, s92, v112 dst_sel:DWORD dst_unused:UNUSED_PAD src0_sel:DWORD src1_sel:WORD_1\n\t"
    "v_cvt_pk_bf16_f32 v22, v116, v117\n\t"
    "v_cvt_pk_bf16_f32 v23, v118, v119\n\t"
    "v_and_b32 v119, s67, v29\n\t"
    "v_lshlrev_b32 v118, 16, v29\n\t"
    "v_and_b32 v117, s67, v28\n\t"
    "v_lshlrev_b32 v116, 16, v28\n\t"
    "v_add_u32_sdwa v112, s92, v112 dst_sel:DWORD dst_unused:UNUSED_PAD src0_sel:DWORD src1_sel:WORD_0\n\t"
    "s_waitcnt lgkmcnt(4)\n\t"
    "v_smfmac_f32_16x16x64_bf16 v[116:119], v[120:123], v[84:91], v124\n\t"
    "ds_read_b64 v[84:85], v112 offset:0\n\t"
    "ds_read_b64 v[86:87], v126 offset:0\n\t"
    "v_cvt_pk_bf16_f32 v80, v80, v81\n\t"
    "v_cvt_pk_bf16_f32 v81, v82, v83\n\t"
    "ds_write_b64 v125, v[80:81] offset:384\n\t"
    "v_add_u32_sdwa v90, s92, v113 dst_sel:DWORD dst_unused:UNUSED_PAD src0_sel:DWORD src1_sel:WORD_1\n\t"
    "v_add_u32_sdwa v113, s92, v113 dst_sel:DWORD dst_unused:UNUSED_PAD src0_sel:DWORD src1_sel:WORD_0\n\t"
    "ds_read_b64 v[88:89], v113 offset:0\n\t"
    "ds_read_b64 v[90:91], v90 offset:0\n\t"
    "v_add_u32_sdwa v126, s92, v114 dst_sel:DWORD dst_unused:UNUSED_PAD src0_sel:DWORD src1_sel:WORD_1\n\t"
    "v_add_u32_sdwa v114, s92, v114 dst_sel:DWORD dst_unused:UNUSED_PAD src0_sel:DWORD src1_sel:WORD_0\n\t"
    "s_waitcnt vmcnt(2) lgkmcnt(5)\n\t"
    "v_smfmac_f32_16x16x64_bf16 v[116:119], v[120:123], v[92:99], v124\n\t"
    "ds_read_b64 v[92:93], v114 offset:0\n\t"
    "ds_read_b64 v[94:95], v126 offset:0\n\t"
    "v_add_u32_sdwa v98, s92, v115 dst_sel:DWORD dst_unused:UNUSED_PAD src0_sel:DWORD src1_sel:WORD_1\n\t"
    "v_add_u32_sdwa v115, s92, v115 dst_sel:DWORD dst_unused:UNUSED_PAD src0_sel:DWORD src1_sel:WORD_0\n\t"
    "ds_read_b64 v[96:97], v115 offset:0\n\t"
    "ds_read_b64 v[98:99], v98 offset:0\n\t"
    "buffer_load_dwordx4 v[112:115], v127, %12, s89 offen offset:1024 sc1\n\t"
    "v_add_u32_sdwa v126, s92, v100 dst_sel:DWORD dst_unused:UNUSED_PAD src0_sel:DWORD src1_sel:WORD_1\n\t"
    "v_cvt_pk_bf16_f32 v28, v116, v117\n\t"
    "v_cvt_pk_bf16_f32 v29, v118, v119\n\t"
    "v_and_b32 v119, s67, v31\n\t"
    "v_lshlrev_b32 v118, 16, v31\n\t"
    "v_and_b32 v117, s67, v30\n\t"
    "v_lshlrev_b32 v116, 16, v30\n\t"
    "v_add_u32_sdwa v100, s92, v100 dst_sel:DWORD dst_unused:UNUSED_PAD src0_sel:DWORD src1_sel:WORD_0\n\t"
    "s_waitcnt lgkmcnt(4)\n\t"
    "v_smfmac_f32_16x16x64_bf16 v[116:119], v[120:123], v[84:91], v124\n\t"
    "ds_read_b64 v[84:85], v100 offset:0\n\t"
    "v_mfma_f32_16x16x32_bf16 v[80:83], v[64:67], v[196:199], 0\n\t"
    "ds_read_b64 v[86:87], v126 offset:0\n\t"
    "v_mfma_f32_16x16x32_bf16 v[80:83], v[68:71], v[200:203], v[80:83]\n\t"
    "v_add_u32_sdwa v90, s92, v101 dst_sel:DWORD dst_unused:UNUSED_PAD src0_sel:DWORD src1_sel:WORD_1\n\t"
    "v_add_u32_sdwa v101, s92, v101 dst_sel:DWORD dst_unused:UNUSED_PAD src0_sel:DWORD src1_sel:WORD_0\n\t"
    "ds_read_b64 v[88:89], v101 offset:0\n\t"
    "v_mfma_f32_16x16x32_bf16 v[80:83], v[72:75], v[252:255], v[80:83]\n\t"
    "ds_read_b64 v[90:91], v90 offset:0\n\t"
    "v_mfma_f32_16x16x32_bf16 v[80:83], v[76:79], v[128:131], v[80:83]\n\t"
    "v_add_u32_sdwa v126, s92, v102 dst_sel:DWORD dst_unused:UNUSED_PAD src0_sel:DWORD src1_sel:WORD_1\n\t"
    "v_add_u32_sdwa v102, s92, v102 dst_sel:DWORD dst_unused:UNUSED_PAD src0_sel:DWORD src1_sel:WORD_0\n\t"
    "s_xor_b32 s50, s50, 0x2000\n\t"
    "s_add_u32 m0, s50, 128000\n\t"
    "s_add_u32 s63, s63, 0x4000\n\t"
    "buffer_load_dwordx4 v127, %12, s63 offen sc1 lds\n\t"
    "s_waitcnt vmcnt(3) lgkmcnt(4)\n\t"
    "v_smfmac_f32_16x16x64_bf16 v[116:119], v[120:123], v[92:99], v124\n\t"
    "ds_read_b64 v[92:93], v102 offset:0\n\t"
    "ds_read_b64 v[94:95], v126 offset:0\n\t"
    "v_add_u32_sdwa v98, s92, v103 dst_sel:DWORD dst_unused:UNUSED_PAD src0_sel:DWORD src1_sel:WORD_1\n\t"
    "v_add_u32_sdwa v103, s92, v103 dst_sel:DWORD dst_unused:UNUSED_PAD src0_sel:DWORD src1_sel:WORD_0\n\t"
    "ds_read_b64 v[96:97], v103 offset:0\n\t"
    "ds_read_b64 v[98:99], v98 offset:0\n\t"
    "s_add_u32 s90, s90, s95\n\t"
    "buffer_load_dwordx4 v[100:103], v127, %12, s90 offen offset:0 sc1\n\t"
    "v_add_u32_sdwa v126, s92, v104 dst_sel:DWORD dst_unused:UNUSED_PAD src0_sel:DWORD src1_sel:WORD_1\n\t"
    "v_cvt_pk_bf16_f32 v30, v116, v117\n\t"
    "v_cvt_pk_bf16_f32 v31, v118, v119\n\t"
    "v_and_b32 v119, s67, v37\n\t"
    "v_lshlrev_b32 v118, 16, v37\n\t"
    "v_and_b32 v117, s67, v36\n\t"
    "v_lshlrev_b32 v116, 16, v36\n\t"
    "v_add_u32_sdwa v104, s92, v104 dst_sel:DWORD dst_unused:UNUSED_PAD src0_sel:DWORD src1_sel:WORD_0\n\t"
    "s_waitcnt lgkmcnt(4)\n\t"
    "v_smfmac_f32_16x16x64_bf16 v[116:119], v[120:123], v[84:91], v124\n\t"
    "ds_read_b64 v[84:85], v104 offset:0\n\t"
    "ds_read_b64 v[86:87], v126 offset:0\n\t"
    "v_cvt_pk_bf16_f32 v80, v80, v81\n\t"
    "v_cvt_pk_bf16_f32 v81, v82, v83\n\t"
    "ds_write_b64 v125, v[80:81] offset:512\n\t"
    "v_add_u32_sdwa v90, s92, v105 dst_sel:DWORD dst_unused:UNUSED_PAD src0_sel:DWORD src1_sel:WORD_1\n\t"
    "v_add_u32_sdwa v105, s92, v105 dst_sel:DWORD dst_unused:UNUSED_PAD src0_sel:DWORD src1_sel:WORD_0\n\t"
    "ds_read_b64 v[88:89], v105 offset:0\n\t"
    "ds_read_b64 v[90:91], v90 offset:0\n\t"
    "v_add_u32_sdwa v126, s92, v106 dst_sel:DWORD dst_unused:UNUSED_PAD src0_sel:DWORD src1_sel:WORD_1\n\t"
    "v_add_u32_sdwa v106, s92, v106 dst_sel:DWORD dst_unused:UNUSED_PAD src0_sel:DWORD src1_sel:WORD_0\n\t"
    "s_waitcnt vmcnt(3) lgkmcnt(5)\n\t"
    "v_smfmac_f32_16x16x64_bf16 v[116:119], v[120:123], v[92:99], v124\n\t"
    "ds_read_b64 v[92:93], v106 offset:0\n\t"
    "ds_read_b64 v[94:95], v126 offset:0\n\t"
    "v_add_u32_sdwa v98, s92, v107 dst_sel:DWORD dst_unused:UNUSED_PAD src0_sel:DWORD src1_sel:WORD_1\n\t"
    "v_add_u32_sdwa v107, s92, v107 dst_sel:DWORD dst_unused:UNUSED_PAD src0_sel:DWORD src1_sel:WORD_0\n\t"
    "ds_read_b64 v[96:97], v107 offset:0\n\t"
    "ds_read_b64 v[98:99], v98 offset:0\n\t"
    "buffer_load_dwordx4 v[104:107], v127, %12, s90 offen offset:1024 sc1\n\t"
    "v_add_u32_sdwa v126, s92, v108 dst_sel:DWORD dst_unused:UNUSED_PAD src0_sel:DWORD src1_sel:WORD_1\n\t"
    "v_cvt_pk_bf16_f32 v36, v116, v117\n\t"
    "v_cvt_pk_bf16_f32 v37, v118, v119\n\t"
    "v_and_b32 v119, s67, v39\n\t"
    "v_lshlrev_b32 v118, 16, v39\n\t"
    "v_and_b32 v117, s67, v38\n\t"
    "v_lshlrev_b32 v116, 16, v38\n\t"
    "v_add_u32_sdwa v108, s92, v108 dst_sel:DWORD dst_unused:UNUSED_PAD src0_sel:DWORD src1_sel:WORD_0\n\t"
    "s_waitcnt lgkmcnt(4)\n\t"
    "v_smfmac_f32_16x16x64_bf16 v[116:119], v[120:123], v[84:91], v124\n\t"
    "ds_read_b64 v[84:85], v108 offset:0\n\t"
    "v_mfma_f32_16x16x32_bf16 v[80:83], v[64:67], v[204:207], 0\n\t"
    "ds_read_b64 v[86:87], v126 offset:0\n\t"
    "v_mfma_f32_16x16x32_bf16 v[80:83], v[68:71], v[208:211], v[80:83]\n\t"
    "v_add_u32_sdwa v90, s92, v109 dst_sel:DWORD dst_unused:UNUSED_PAD src0_sel:DWORD src1_sel:WORD_1\n\t"
    "v_add_u32_sdwa v109, s92, v109 dst_sel:DWORD dst_unused:UNUSED_PAD src0_sel:DWORD src1_sel:WORD_0\n\t"
    "ds_read_b64 v[88:89], v109 offset:0\n\t"
    "v_mfma_f32_16x16x32_bf16 v[80:83], v[72:75], v[132:135], v[80:83]\n\t"
    "ds_read_b64 v[90:91], v90 offset:0\n\t"
    "v_mfma_f32_16x16x32_bf16 v[80:83], v[76:79], v[136:139], v[80:83]\n\t"
    "v_add_u32_sdwa v126, s92, v110 dst_sel:DWORD dst_unused:UNUSED_PAD src0_sel:DWORD src1_sel:WORD_1\n\t"
    "v_add_u32_sdwa v110, s92, v110 dst_sel:DWORD dst_unused:UNUSED_PAD src0_sel:DWORD src1_sel:WORD_0\n\t"
    "s_cmp_lt_u32 %16, 4\n\t"
    "s_cbranch_scc1 L_tp19_%=\n\t"
    "s_setprio 0\n\t"
    "L_tp19_%=:\n\t"
    "s_waitcnt vmcnt(3) lgkmcnt(4)\n\t"
    "v_smfmac_f32_16x16x64_bf16 v[116:119], v[120:123], v[92:99], v124\n\t"
    "ds_read_b64 v[92:93], v110 offset:0\n\t"
    "ds_read_b64 v[94:95], v126 offset:0\n\t"
    "v_add_u32_sdwa v98, s92, v111 dst_sel:DWORD dst_unused:UNUSED_PAD src0_sel:DWORD src1_sel:WORD_1\n\t"
    "v_add_u32_sdwa v111, s92, v111 dst_sel:DWORD dst_unused:UNUSED_PAD src0_sel:DWORD src1_sel:WORD_0\n\t"
    "ds_read_b64 v[96:97], v111 offset:0\n\t"
    "ds_read_b64 v[98:99], v98 offset:0\n\t"
    "s_add_u32 s91, s91, s95\n\t"
    "buffer_load_dwordx4 v[108:111], v127, %12, s91 offen offset:0 sc1\n\t"
    "v_add_u32_sdwa v126, s92, v112 dst_sel:DWORD dst_unused:UNUSED_PAD src0_sel:DWORD src1_sel:WORD_1\n\t"
    "v_cvt_pk_bf16_f32 v38, v116, v117\n\t"
    "v_cvt_pk_bf16_f32 v39, v118, v119\n\t"
    "v_and_b32 v119, s67, v45\n\t"
    "v_lshlrev_b32 v118, 16, v45\n\t"
    "v_and_b32 v117, s67, v44\n\t"
    "v_lshlrev_b32 v116, 16, v44\n\t"
    "v_add_u32_sdwa v112, s92, v112 dst_sel:DWORD dst_unused:UNUSED_PAD src0_sel:DWORD src1_sel:WORD_0\n\t"
    "s_waitcnt lgkmcnt(4)\n\t"
    "v_smfmac_f32_16x16x64_bf16 v[116:119], v[120:123], v[84:91], v124\n\t"
    "ds_read_b64 v[84:85], v112 offset:0\n\t"
    "ds_read_b64 v[86:87], v126 offset:0\n\t"
    "v_cvt_pk_bf16_f32 v80, v80, v81\n\t"
    "v_cvt_pk_bf16_f32 v81, v82, v83\n\t"
    "ds_write_b64 v125, v[80:81] offset:640\n\t"
    "v_add_u32_sdwa v90, s92, v113 dst_sel:DWORD dst_unused:UNUSED_PAD src0_sel:DWORD src1_sel:WORD_1\n\t"
    "v_add_u32_sdwa v113, s92, v113 dst_sel:DWORD dst_unused:UNUSED_PAD src0_sel:DWORD src1_sel:WORD_0\n\t"
    "ds_read_b64 v[88:89], v113 offset:0\n\t"
    "ds_read_b64 v[90:91], v90 offset:0\n\t"
    "v_add_u32_sdwa v126, s92, v114 dst_sel:DWORD dst_unused:UNUSED_PAD src0_sel:DWORD src1_sel:WORD_1\n\t"
    "v_add_u32_sdwa v114, s92, v114 dst_sel:DWORD dst_unused:UNUSED_PAD src0_sel:DWORD src1_sel:WORD_0\n\t"
    "s_waitcnt vmcnt(2) lgkmcnt(5)\n\t"
    "v_smfmac_f32_16x16x64_bf16 v[116:119], v[120:123], v[92:99], v124\n\t"
    "ds_read_b64 v[92:93], v114 offset:0\n\t"
    "ds_read_b64 v[94:95], v126 offset:0\n\t"
    "v_add_u32_sdwa v98, s92, v115 dst_sel:DWORD dst_unused:UNUSED_PAD src0_sel:DWORD src1_sel:WORD_1\n\t"
    "v_add_u32_sdwa v115, s92, v115 dst_sel:DWORD dst_unused:UNUSED_PAD src0_sel:DWORD src1_sel:WORD_0\n\t"
    "ds_read_b64 v[96:97], v115 offset:0\n\t"
    "ds_read_b64 v[98:99], v98 offset:0\n\t"
    "buffer_load_dwordx4 v[112:115], v127, %12, s91 offen offset:1024 sc1\n\t"
    "v_add_u32_sdwa v126, s92, v100 dst_sel:DWORD dst_unused:UNUSED_PAD src0_sel:DWORD src1_sel:WORD_1\n\t"
    "v_cvt_pk_bf16_f32 v44, v116, v117\n\t"
    "v_cvt_pk_bf16_f32 v45, v118, v119\n\t"
    "v_and_b32 v119, s67, v47\n\t"
    "v_lshlrev_b32 v118, 16, v47\n\t"
    "v_and_b32 v117, s67, v46\n\t"
    "v_lshlrev_b32 v116, 16, v46\n\t"
    "v_add_u32_sdwa v100, s92, v100 dst_sel:DWORD dst_unused:UNUSED_PAD src0_sel:DWORD src1_sel:WORD_0\n\t"
    "s_waitcnt lgkmcnt(4)\n\t"
    "v_smfmac_f32_16x16x64_bf16 v[116:119], v[120:123], v[84:91], v124\n\t"
    "ds_read_b64 v[84:85], v100 offset:0\n\t"
    "v_mfma_f32_16x16x32_bf16 v[80:83], v[64:67], v[156:159], 0\n\t"
    "ds_read_b64 v[86:87], v126 offset:0\n\t"
    "v_mfma_f32_16x16x32_bf16 v[80:83], v[68:71], v[160:163], v[80:83]\n\t"
    "v_add_u32_sdwa v90, s92, v101 dst_sel:DWORD dst_unused:UNUSED_PAD src0_sel:DWORD src1_sel:WORD_1\n\t"
    "v_add_u32_sdwa v101, s92, v101 dst_sel:DWORD dst_unused:UNUSED_PAD src0_sel:DWORD src1_sel:WORD_0\n\t"
    "ds_read_b64 v[88:89], v101 offset:0\n\t"
    "v_mfma_f32_16x16x32_bf16 v[80:83], v[72:75], v[140:143], v[80:83]\n\t"
    "ds_read_b64 v[90:91], v90 offset:0\n\t"
    "v_mfma_f32_16x16x32_bf16 v[80:83], v[76:79], v[144:147], v[80:83]\n\t"
    "v_add_u32_sdwa v126, s92, v102 dst_sel:DWORD dst_unused:UNUSED_PAD src0_sel:DWORD src1_sel:WORD_1\n\t"
    "v_add_u32_sdwa v102, s92, v102 dst_sel:DWORD dst_unused:UNUSED_PAD src0_sel:DWORD src1_sel:WORD_0\n\t"
    "s_waitcnt vmcnt(2) lgkmcnt(4)\n\t"
    "v_smfmac_f32_16x16x64_bf16 v[116:119], v[120:123], v[92:99], v124\n\t"
    "ds_read_b64 v[92:93], v102 offset:0\n\t"
    "ds_read_b64 v[94:95], v126 offset:0\n\t"
    "v_add_u32_sdwa v98, s92, v103 dst_sel:DWORD dst_unused:UNUSED_PAD src0_sel:DWORD src1_sel:WORD_1\n\t"
    "v_add_u32_sdwa v103, s92, v103 dst_sel:DWORD dst_unused:UNUSED_PAD src0_sel:DWORD src1_sel:WORD_0\n\t"
    "ds_read_b64 v[96:97], v103 offset:0\n\t"
    "ds_read_b64 v[98:99], v98 offset:0\n\t"
    "s_add_u32 s84, s84, s94\n\t"
    "buffer_load_dwordx4 v[100:103], v127, %12, s84 offen offset:0 sc1\n\t"
    "v_add_u32_sdwa v126, s92, v104 dst_sel:DWORD dst_unused:UNUSED_PAD src0_sel:DWORD src1_sel:WORD_1\n\t"
    "v_cvt_pk_bf16_f32 v46, v116, v117\n\t"
    "v_cvt_pk_bf16_f32 v47, v118, v119\n\t"
    "v_and_b32 v119, s67, v53\n\t"
    "v_lshlrev_b32 v118, 16, v53\n\t"
    "v_and_b32 v117, s67, v52\n\t"
    "v_lshlrev_b32 v116, 16, v52\n\t"
    "v_add_u32_sdwa v104, s92, v104 dst_sel:DWORD dst_unused:UNUSED_PAD src0_sel:DWORD src1_sel:WORD_0\n\t"
    "s_waitcnt lgkmcnt(4)\n\t"
    "v_smfmac_f32_16x16x64_bf16 v[116:119], v[120:123], v[84:91], v124\n\t"
    "ds_read_b64 v[84:85], v104 offset:0\n\t"
    "ds_read_b64 v[86:87], v126 offset:0\n\t"
    "v_cvt_pk_bf16_f32 v80, v80, v81\n\t"
    "v_cvt_pk_bf16_f32 v81, v82, v83\n\t"
    "ds_write_b64 v125, v[80:81] offset:768\n\t"
    "v_add_u32_sdwa v90, s92, v105 dst_sel:DWORD dst_unused:UNUSED_PAD src0_sel:DWORD src1_sel:WORD_1\n\t"
    "v_add_u32_sdwa v105, s92, v105 dst_sel:DWORD dst_unused:UNUSED_PAD src0_sel:DWORD src1_sel:WORD_0\n\t"
    "ds_read_b64 v[88:89], v105 offset:0\n\t"
    "ds_read_b64 v[90:91], v90 offset:0\n\t"
    "v_add_u32_sdwa v126, s92, v106 dst_sel:DWORD dst_unused:UNUSED_PAD src0_sel:DWORD src1_sel:WORD_1\n\t"
    "v_add_u32_sdwa v106, s92, v106 dst_sel:DWORD dst_unused:UNUSED_PAD src0_sel:DWORD src1_sel:WORD_0\n\t"
    "s_waitcnt vmcnt(2) lgkmcnt(5)\n\t"
    "v_smfmac_f32_16x16x64_bf16 v[116:119], v[120:123], v[92:99], v124\n\t"
    "ds_read_b64 v[92:93], v106 offset:0\n\t"
    "ds_read_b64 v[94:95], v126 offset:0\n\t"
    "v_add_u32_sdwa v98, s92, v107 dst_sel:DWORD dst_unused:UNUSED_PAD src0_sel:DWORD src1_sel:WORD_1\n\t"
    "v_add_u32_sdwa v107, s92, v107 dst_sel:DWORD dst_unused:UNUSED_PAD src0_sel:DWORD src1_sel:WORD_0\n\t"
    "ds_read_b64 v[96:97], v107 offset:0\n\t"
    "ds_read_b64 v[98:99], v98 offset:0\n\t"
    "buffer_load_dwordx4 v[104:107], v127, %12, s84 offen offset:1024 sc1\n\t"
    "v_add_u32_sdwa v126, s92, v108 dst_sel:DWORD dst_unused:UNUSED_PAD src0_sel:DWORD src1_sel:WORD_1\n\t"
    "v_cvt_pk_bf16_f32 v52, v116, v117\n\t"
    "v_cvt_pk_bf16_f32 v53, v118, v119\n\t"
    "v_and_b32 v119, s67, v55\n\t"
    "v_lshlrev_b32 v118, 16, v55\n\t"
    "v_and_b32 v117, s67, v54\n\t"
    "v_lshlrev_b32 v116, 16, v54\n\t"
    "v_add_u32_sdwa v108, s92, v108 dst_sel:DWORD dst_unused:UNUSED_PAD src0_sel:DWORD src1_sel:WORD_0\n\t"
    "s_waitcnt lgkmcnt(4)\n\t"
    "v_smfmac_f32_16x16x64_bf16 v[116:119], v[120:123], v[84:91], v124\n\t"
    "ds_read_b64 v[84:85], v108 offset:0\n\t"
    "v_mfma_f32_16x16x32_bf16 v[80:83], v[64:67], v[212:215], 0\n\t"
    "ds_read_b64 v[86:87], v126 offset:0\n\t"
    "v_mfma_f32_16x16x32_bf16 v[80:83], v[68:71], v[216:219], v[80:83]\n\t"
    "v_add_u32_sdwa v90, s92, v109 dst_sel:DWORD dst_unused:UNUSED_PAD src0_sel:DWORD src1_sel:WORD_1\n\t"
    "v_add_u32_sdwa v109, s92, v109 dst_sel:DWORD dst_unused:UNUSED_PAD src0_sel:DWORD src1_sel:WORD_0\n\t"
    "ds_read_b64 v[88:89], v109 offset:0\n\t"
    "v_mfma_f32_16x16x32_bf16 v[80:83], v[72:75], v[148:151], v[80:83]\n\t"
    "ds_read_b64 v[90:91], v90 offset:0\n\t"
    "v_mfma_f32_16x16x32_bf16 v[80:83], v[76:79], v[152:155], v[80:83]\n\t"
    "v_add_u32 v79, s96, v127\n\t"
    "s_add_u32 s96, s96, 0x1000\n\t"
    "s_and_b32 s96, s96, 0x3000\n\t"
    "v_add_u32 v79, 128000, v79\n\t"
    "ds_read_b128 v[64:67], v79 offset:0\n\t"
    "ds_read_b128 v[68:71], v79 offset:1024\n\t"
    "ds_read_b128 v[72:75], v79 offset:2048\n\t"
    "ds_read_b128 v[76:79], v79 offset:3072\n\t"
    "v_add_u32_sdwa v126, s92, v110 dst_sel:DWORD dst_unused:UNUSED_PAD src0_sel:DWORD src1_sel:WORD_1\n\t"
    "v_add_u32_sdwa v110, s92, v110 dst_sel:DWORD dst_unused:UNUSED_PAD src0_sel:DWORD src1_sel:WORD_0\n\t"
    "s_waitcnt vmcnt(2) lgkmcnt(8)\n\t"
    "v_smfmac_f32_16x16x64_bf16 v[116:119], v[120:123], v[92:99], v124\n\t"
    "ds_read_b64 v[92:93], v110 offset:0\n\t"
    "ds_read_b64 v[94:95], v126 offset:0\n\t"
    "v_add_u32_sdwa v98, s92, v111 dst_sel:DWORD dst_unused:UNUSED_PAD src0_sel:DWORD src1_sel:WORD_1\n\t"
    "v_add_u32_sdwa v111, s92, v111 dst_sel:DWORD dst_unused:UNUSED_PAD src0_sel:DWORD src1_sel:WORD_0\n\t"
    "ds_read_b64 v[96:97], v111 offset:0\n\t"
    "ds_read_b64 v[98:99], v98 offset:0\n\t"
    "s_add_u32 s85, s85, s94\n\t"
    "buffer_load_dwordx4 v[108:111], v127, %12, s85 offen offset:0 sc1\n\t"
    "v_add_u32_sdwa v126, s92, v112 dst_sel:DWORD dst_unused:UNUSED_PAD src0_sel:DWORD src1_sel:WORD_1\n\t"
    "v_cvt_pk_bf16_f32 v54, v116, v117\n\t"
    "v_cvt_pk_bf16_f32 v55, v118, v119\n\t"
    "v_and_b32 v119, s67, v61\n\t"
    "v_lshlrev_b32 v118, 16, v61\n\t"
    "v_and_b32 v117, s67, v60\n\t"
    "v_lshlrev_b32 v116, 16, v60\n\t"
    "v_add_u32_sdwa v112, s92, v112 dst_sel:DWORD dst_unused:UNUSED_PAD src0_sel:DWORD src1_sel:WORD_0\n\t"
    "s_waitcnt lgkmcnt(8)\n\t"
    "v_smfmac_f32_16x16x64_bf16 v[116:119], v[120:123], v[84:91], v124\n\t"
    "ds_read_b64 v[84:85], v112 offset:0\n\t"
    "ds_read_b64 v[86:87], v126 offset:0\n\t"
    "v_cvt_pk_bf16_f32 v80, v80, v81\n\t"
    "v_cvt_pk_bf16_f32 v81, v82, v83\n\t"
    "ds_write_b64 v125, v[80:81] offset:872\n\t"
    "v_add_u32_sdwa v90, s92, v113 dst_sel:DWORD dst_unused:UNUSED_PAD src0_sel:DWORD src1_sel:WORD_1\n\t"
    "v_add_u32_sdwa v113, s92, v113 dst_sel:DWORD dst_unused:UNUSED_PAD src0_sel:DWORD src1_sel:WORD_0\n\t"
    "ds_read_b64 v[88:89], v113 offset:0\n\t"
    "ds_read_b64 v[90:91], v90 offset:0\n\t"
    "v_add_u32_sdwa v126, s92, v114 dst_sel:DWORD dst_unused:UNUSED_PAD src0_sel:DWORD src1_sel:WORD_1\n\t"
    "v_add_u32_sdwa v114, s92, v114 dst_sel:DWORD dst_unused:UNUSED_PAD src0_sel:DWORD src1_sel:WORD_0\n\t"
    "s_waitcnt vmcnt(2) lgkmcnt(5)\n\t"
    "v_smfmac_f32_16x16x64_bf16 v[116:119], v[120:123], v[92:99], v124\n\t"
    "ds_read_b64 v[92:93], v114 offset:0\n\t"
    "ds_read_b64 v[94:95], v126 offset:0\n\t"
    "v_add_u32_sdwa v98, s92, v115 dst_sel:DWORD dst_unused:UNUSED_PAD src0_sel:DWORD src1_sel:WORD_1\n\t"
    "v_add_u32_sdwa v115, s92, v115 dst_sel:DWORD dst_unused:UNUSED_PAD src0_sel:DWORD src1_sel:WORD_0\n\t"
    "ds_read_b64 v[96:97], v115 offset:0\n\t"
    "ds_read_b64 v[98:99], v98 offset:0\n\t"
    "buffer_load_dwordx4 v[112:115], v127, %12, s85 offen offset:1024 sc1\n\t"
    "v_add_u32_sdwa v126, s92, v100 dst_sel:DWORD dst_unused:UNUSED_PAD src0_sel:DWORD src1_sel:WORD_1\n\t"
    "v_cvt_pk_bf16_f32 v60, v116, v117\n\t"
    "v_cvt_pk_bf16_f32 v61, v118, v119\n\t"
    "v_and_b32 v119, s67, v63\n\t"
    "v_lshlrev_b32 v118, 16, v63\n\t"
    "v_and_b32 v117, s67, v62\n\t"
    "v_lshlrev_b32 v116, 16, v62\n\t"
    "v_add_u32_sdwa v100, s92, v100 dst_sel:DWORD dst_unused:UNUSED_PAD src0_sel:DWORD src1_sel:WORD_0\n\t"
    "s_waitcnt lgkmcnt(4)\n\t"
    "v_smfmac_f32_16x16x64_bf16 v[116:119], v[120:123], v[84:91], v124\n\t"
    "ds_read_b64 v[84:85], v100 offset:0\n\t"
    "ds_read_b64 v[86:87], v126 offset:0\n\t"
    "v_add_u32_sdwa v90, s92, v101 dst_sel:DWORD dst_unused:UNUSED_PAD src0_sel:DWORD src1_sel:WORD_1\n\t"
    "v_add_u32_sdwa v101, s92, v101 dst_sel:DWORD dst_unused:UNUSED_PAD src0_sel:DWORD src1_sel:WORD_0\n\t"
    "ds_read_b64 v[88:89], v101 offset:0\n\t"
    "ds_read_b64 v[90:91], v90 offset:0\n\t"
    "v_add_u32_sdwa v126, s92, v102 dst_sel:DWORD dst_unused:UNUSED_PAD src0_sel:DWORD src1_sel:WORD_1\n\t"
    "v_add_u32_sdwa v102, s92, v102 dst_sel:DWORD dst_unused:UNUSED_PAD src0_sel:DWORD src1_sel:WORD_0\n\t"
    "s_cmp_lt_u32 %16, 4\n\t"
    "s_cbranch_scc1 L_tp31_%=\n\t"
    "s_setprio 1\n\t"
    "L_tp31_%=:\n\t"
    "s_waitcnt vmcnt(2) lgkmcnt(4)\n\t"
    "v_smfmac_f32_16x16x64_bf16 v[116:119], v[120:123], v[92:99], v124\n\t"
    "ds_read_b64 v[92:93], v102 offset:0\n\t"
    "ds_read_b64 v[94:95], v126 offset:0\n\t"
    "v_add_u32_sdwa v98, s92, v103 dst_sel:DWORD dst_unused:UNUSED_PAD src0_sel:DWORD src1_sel:WORD_1\n\t"
    "v_add_u32_sdwa v103, s92, v103 dst_sel:DWORD dst_unused:UNUSED_PAD src0_sel:DWORD src1_sel:WORD_0\n\t"
    "ds_read_b64 v[96:97], v103 offset:0\n\t"
    "ds_read_b64 v[98:99], v98 offset:0\n\t"
    "s_add_u32 s86, s86, s94\n\t"
    "buffer_load_dwordx4 v[100:103], v127, %12, s86 offen offset:0 sc1\n\t"
    "v_add_u32_sdwa v126, s92, v104 dst_sel:DWORD dst_unused:UNUSED_PAD src0_sel:DWORD src1_sel:WORD_1\n\t"
    "v_cvt_pk_bf16_f32 v62, v116, v117\n\t"
    "v_cvt_pk_bf16_f32 v63, v118, v119\n\t"
    "v_and_b32 v119, s67, v1\n\t"
    "v_lshlrev_b32 v118, 16, v1\n\t"
    "v_and_b32 v117, s67, v0\n\t"
    "v_lshlrev_b32 v116, 16, v0\n\t"
    "v_add_u32_sdwa v104, s92, v104 dst_sel:DWORD dst_unused:UNUSED_PAD src0_sel:DWORD src1_sel:WORD_0\n\t"
    "s_waitcnt lgkmcnt(4)\n\t"
    "v_smfmac_f32_16x16x64_bf16 v[116:119], v[120:123], v[84:91], v124\n\t"
    "ds_read_b64 v[84:85], v104 offset:0\n\t"
    "v_mfma_f32_16x16x32_bf16 v[80:83], v[64:67], v[164:167], 0\n\t"
    "ds_read_b64 v[86:87], v126 offset:0\n\t"
    "v_mfma_f32_16x16x32_bf16 v[80:83], v[68:71], v[168:171], v[80:83]\n\t"
    "v_add_u32_sdwa v90, s92, v105 dst_sel:DWORD dst_unused:UNUSED_PAD src0_sel:DWORD src1_sel:WORD_1\n\t"
    "v_add_u32_sdwa v105, s92, v105 dst_sel:DWORD dst_unused:UNUSED_PAD src0_sel:DWORD src1_sel:WORD_0\n\t"
    "ds_read_b64 v[88:89], v105 offset:0\n\t"
    "v_mfma_f32_16x16x32_bf16 v[80:83], v[72:75], v[220:223], v[80:83]\n\t"
    "ds_read_b64 v[90:91], v90 offset:0\n\t"
    "v_mfma_f32_16x16x32_bf16 v[80:83], v[76:79], v[224:227], v[80:83]\n\t"
    "v_add_u32_sdwa v126, s92, v106 dst_sel:DWORD dst_unused:UNUSED_PAD src0_sel:DWORD src1_sel:WORD_1\n\t"
    "v_add_u32_sdwa v106, s92, v106 dst_sel:DWORD dst_unused:UNUSED_PAD src0_sel:DWORD src1_sel:WORD_0\n\t"
    "s_waitcnt vmcnt(2) lgkmcnt(4)\n\t"
    "v_smfmac_f32_16x16x64_bf16 v[116:119], v[120:123], v[92:99], v124\n\t"
    "ds_read_b64 v[92:93], v106 offset:0\n\t"
    "ds_read_b64 v[94:95], v126 offset:0\n\t"
    "v_add_u32_sdwa v98, s92, v107 dst_sel:DWORD dst_unused:UNUSED_PAD src0_sel:DWORD src1_sel:WORD_1\n\t"
    "v_add_u32_sdwa v107, s92, v107 dst_sel:DWORD dst_unused:UNUSED_PAD src0_sel:DWORD src1_sel:WORD_0\n\t"
    "ds_read_b64 v[96:97], v107 offset:0\n\t"
    "ds_read_b64 v[98:99], v98 offset:0\n\t"
    "buffer_load_dwordx4 v[104:107], v127, %12, s86 offen offset:1024 sc1\n\t"
    "v_add_u32_sdwa v126, s92, v108 dst_sel:DWORD dst_unused:UNUSED_PAD src0_sel:DWORD src1_sel:WORD_1\n\t"
    "v_cvt_pk_bf16_f32 v0, v116, v117\n\t"
    "v_cvt_pk_bf16_f32 v1, v118, v119\n\t"
    "v_and_b32 v119, s67, v3\n\t"
    "v_lshlrev_b32 v118, 16, v3\n\t"
    "v_and_b32 v117, s67, v2\n\t"
    "v_lshlrev_b32 v116, 16, v2\n\t"
    "v_add_u32_sdwa v108, s92, v108 dst_sel:DWORD dst_unused:UNUSED_PAD src0_sel:DWORD src1_sel:WORD_0\n\t"
    "s_waitcnt lgkmcnt(4)\n\t"
    "v_smfmac_f32_16x16x64_bf16 v[116:119], v[120:123], v[84:91], v124\n\t"
    "ds_read_b64 v[84:85], v108 offset:0\n\t"
    "ds_read_b64 v[86:87], v126 offset:0\n\t"
    "v_cvt_pk_bf16_f32 v80, v80, v81\n\t"
    "v_cvt_pk_bf16_f32 v81, v82, v83\n\t"
    "ds_write_b64 v125, v[80:81] offset:32000\n\t"
    "v_mfma_f32_16x16x32_bf16 v[80:83], v[64:67], v[172:175], 0\n\t"
    "v_add_u32_sdwa v90, s92, v109 dst_sel:DWORD dst_unused:UNUSED_PAD src0_sel:DWORD src1_sel:WORD_1\n\t"
    "v_add_u32_sdwa v109, s92, v109 dst_sel:DWORD dst_unused:UNUSED_PAD src0_sel:DWORD src1_sel:WORD_0\n\t"
    "ds_read_b64 v[88:89], v109 offset:0\n\t"
    "v_mfma_f32_16x16x32_bf16 v[80:83], v[68:71], v[176:179], v[80:83]\n\t"
    "ds_read_b64 v[90:91], v90 offset:0\n\t"
    "v_mfma_f32_16x16x32_bf16 v[80:83], v[72:75], v[228:231], v[80:83]\n\t"
    "v_mfma_f32_16x16x32_bf16 v[80:83], v[76:79], v[232:235], v[80:83]\n\t"
    "v_add_u32_sdwa v126, s92, v110 dst_sel:DWORD dst_unused:UNUSED_PAD src0_sel:DWORD src1_sel:WORD_1\n\t"
    "v_add_u32_sdwa v110, s92, v110 dst_sel:DWORD dst_unused:UNUSED_PAD src0_sel:DWORD src1_sel:WORD_0\n\t"
    "s_waitcnt vmcnt(2) lgkmcnt(5)\n\t"
    "v_smfmac_f32_16x16x64_bf16 v[116:119], v[120:123], v[92:99], v124\n\t"
    "ds_read_b64 v[92:93], v110 offset:0\n\t"
    "ds_read_b64 v[94:95], v126 offset:0\n\t"
    "v_add_u32_sdwa v98, s92, v111 dst_sel:DWORD dst_unused:UNUSED_PAD src0_sel:DWORD src1_sel:WORD_1\n\t"
    "v_add_u32_sdwa v111, s92, v111 dst_sel:DWORD dst_unused:UNUSED_PAD src0_sel:DWORD src1_sel:WORD_0\n\t"
    "ds_read_b64 v[96:97], v111 offset:0\n\t"
    "ds_read_b64 v[98:99], v98 offset:0\n\t"
    "s_add_u32 s87, s87, s94\n\t"
    "buffer_load_dwordx4 v[108:111], v127, %12, s87 offen offset:0 sc1\n\t"
    "v_add_u32_sdwa v126, s92, v112 dst_sel:DWORD dst_unused:UNUSED_PAD src0_sel:DWORD src1_sel:WORD_1\n\t"
    "v_cvt_pk_bf16_f32 v2, v116, v117\n\t"
    "v_cvt_pk_bf16_f32 v3, v118, v119\n\t"
    "v_and_b32 v119, s67, v9\n\t"
    "v_lshlrev_b32 v118, 16, v9\n\t"
    "v_and_b32 v117, s67, v8\n\t"
    "v_lshlrev_b32 v116, 16, v8\n\t"
    "v_add_u32_sdwa v112, s92, v112 dst_sel:DWORD dst_unused:UNUSED_PAD src0_sel:DWORD src1_sel:WORD_0\n\t"
    "s_waitcnt lgkmcnt(4)\n\t"
    "v_smfmac_f32_16x16x64_bf16 v[116:119], v[120:123], v[84:91], v124\n\t"
    "ds_read_b64 v[84:85], v112 offset:0\n\t"
    "ds_read_b64 v[86:87], v126 offset:0\n\t"
    "v_cvt_pk_bf16_f32 v80, v80, v81\n\t"
    "v_cvt_pk_bf16_f32 v81, v82, v83\n\t"
    "ds_write_b64 v125, v[80:81] offset:32128\n\t"
    "v_add_u32_sdwa v90, s92, v113 dst_sel:DWORD dst_unused:UNUSED_PAD src0_sel:DWORD src1_sel:WORD_1\n\t"
    "v_add_u32_sdwa v113, s92, v113 dst_sel:DWORD dst_unused:UNUSED_PAD src0_sel:DWORD src1_sel:WORD_0\n\t"
    "ds_read_b64 v[88:89], v113 offset:0\n\t"
    "ds_read_b64 v[90:91], v90 offset:0\n\t"
    "v_add_u32_sdwa v126, s92, v114 dst_sel:DWORD dst_unused:UNUSED_PAD src0_sel:DWORD src1_sel:WORD_1\n\t"
    "v_add_u32_sdwa v114, s92, v114 dst_sel:DWORD dst_unused:UNUSED_PAD src0_sel:DWORD src1_sel:WORD_0\n\t"
    "s_waitcnt vmcnt(2) lgkmcnt(5)\n\t"
    "v_smfmac_f32_16x16x64_bf16 v[116:119], v[120:123], v[92:99], v124\n\t"
    "ds_read_b64 v[92:93], v114 offset:0\n\t"
    "ds_read_b64 v[94:95], v126 offset:0\n\t"
    "v_add_u32_sdwa v98, s92, v115 dst_sel:DWORD dst_unused:UNUSED_PAD src0_sel:DWORD src1_sel:WORD_1\n\t"
    "v_add_u32_sdwa v115, s92, v115 dst_sel:DWORD dst_unused:UNUSED_PAD src0_sel:DWORD src1_sel:WORD_0\n\t"
    "ds_read_b64 v[96:97], v115 offset:0\n\t"
    "ds_read_b64 v[98:99], v98 offset:0\n\t"
    "buffer_load_dwordx4 v[112:115], v127, %12, s87 offen offset:1024 sc1\n\t"
    "v_add_u32_sdwa v126, s92, v100 dst_sel:DWORD dst_unused:UNUSED_PAD src0_sel:DWORD src1_sel:WORD_1\n\t"
    "v_cvt_pk_bf16_f32 v8, v116, v117\n\t"
    "v_cvt_pk_bf16_f32 v9, v118, v119\n\t"
    "v_and_b32 v119, s67, v11\n\t"
    "v_lshlrev_b32 v118, 16, v11\n\t"
    "v_and_b32 v117, s67, v10\n\t"
    "v_lshlrev_b32 v116, 16, v10\n\t"
    "v_add_u32_sdwa v100, s92, v100 dst_sel:DWORD dst_unused:UNUSED_PAD src0_sel:DWORD src1_sel:WORD_0\n\t"
    "s_waitcnt lgkmcnt(4)\n\t"
    "v_smfmac_f32_16x16x64_bf16 v[116:119], v[120:123], v[84:91], v124\n\t"
    "ds_read_b64 v[84:85], v100 offset:0\n\t"
    "v_mfma_f32_16x16x32_bf16 v[80:83], v[64:67], v[180:183], 0\n\t"
    "ds_read_b64 v[86:87], v126 offset:0\n\t"
    "v_mfma_f32_16x16x32_bf16 v[80:83], v[68:71], v[184:187], v[80:83]\n\t"
    "v_add_u32_sdwa v90, s92, v101 dst_sel:DWORD dst_unused:UNUSED_PAD src0_sel:DWORD src1_sel:WORD_1\n\t"
    "v_add_u32_sdwa v101, s92, v101 dst_sel:DWORD dst_unused:UNUSED_PAD src0_sel:DWORD src1_sel:WORD_0\n\t"
    "ds_read_b64 v[88:89], v101 offset:0\n\t"
    "v_mfma_f32_16x16x32_bf16 v[80:83], v[72:75], v[236:239], v[80:83]\n\t"
    "ds_read_b64 v[90:91], v90 offset:0\n\t"
    "v_mfma_f32_16x16x32_bf16 v[80:83], v[76:79], v[240:243], v[80:83]\n\t"
    "v_add_u32_sdwa v126, s92, v102 dst_sel:DWORD dst_unused:UNUSED_PAD src0_sel:DWORD src1_sel:WORD_1\n\t"
    "v_add_u32_sdwa v102, s92, v102 dst_sel:DWORD dst_unused:UNUSED_PAD src0_sel:DWORD src1_sel:WORD_0\n\t"
    "s_waitcnt vmcnt(2) lgkmcnt(4)\n\t"
    "v_smfmac_f32_16x16x64_bf16 v[116:119], v[120:123], v[92:99], v124\n\t"
    "ds_read_b64 v[92:93], v102 offset:0\n\t"
    "ds_read_b64 v[94:95], v126 offset:0\n\t"
    "v_add_u32_sdwa v98, s92, v103 dst_sel:DWORD dst_unused:UNUSED_PAD src0_sel:DWORD src1_sel:WORD_1\n\t"
    "v_add_u32_sdwa v103, s92, v103 dst_sel:DWORD dst_unused:UNUSED_PAD src0_sel:DWORD src1_sel:WORD_0\n\t"
    "ds_read_b64 v[96:97], v103 offset:0\n\t"
    "ds_read_b64 v[98:99], v98 offset:0\n\t"
    "s_add_u32 s88, s88, s94\n\t"
    "buffer_load_dwordx4 v[100:103], v127, %12, s88 offen offset:0 sc1\n\t"
    "v_add_u32_sdwa v126, s92, v104 dst_sel:DWORD dst_unused:UNUSED_PAD src0_sel:DWORD src1_sel:WORD_1\n\t"
    "v_cvt_pk_bf16_f32 v10, v116, v117\n\t"
    "v_cvt_pk_bf16_f32 v11, v118, v119\n\t"
    "v_and_b32 v119, s67, v17\n\t"
    "v_lshlrev_b32 v118, 16, v17\n\t"
    "v_and_b32 v117, s67, v16\n\t"
    "v_lshlrev_b32 v116, 16, v16\n\t"
    "v_add_u32_sdwa v104, s92, v104 dst_sel:DWORD dst_unused:UNUSED_PAD src0_sel:DWORD src1_sel:WORD_0\n\t"
    "s_waitcnt lgkmcnt(4)\n\t"
    "v_smfmac_f32_16x16x64_bf16 v[116:119], v[120:123], v[84:91], v124\n\t"
    "ds_read_b64 v[84:85], v104 offset:0\n\t"
    "ds_read_b64 v[86:87], v126 offset:0\n\t"
    "v_cvt_pk_bf16_f32 v80, v80, v81\n\t"
    "v_cvt_pk_bf16_f32 v81, v82, v83\n\t"
    "ds_write_b64 v125, v[80:81] offset:32256\n\t"
    "v_add_u32_sdwa v90, s92, v105 dst_sel:DWORD dst_unused:UNUSED_PAD src0_sel:DWORD src1_sel:WORD_1\n\t"
    "v_add_u32_sdwa v105, s92, v105 dst_sel:DWORD dst_unused:UNUSED_PAD src0_sel:DWORD src1_sel:WORD_0\n\t"
    "ds_read_b64 v[88:89], v105 offset:0\n\t"
    "ds_read_b64 v[90:91], v90 offset:0\n\t"
    "v_add_u32_sdwa v126, s92, v106 dst_sel:DWORD dst_unused:UNUSED_PAD src0_sel:DWORD src1_sel:WORD_1\n\t"
    "v_add_u32_sdwa v106, s92, v106 dst_sel:DWORD dst_unused:UNUSED_PAD src0_sel:DWORD src1_sel:WORD_0\n\t"
    "s_waitcnt vmcnt(2) lgkmcnt(5)\n\t"
    "v_smfmac_f32_16x16x64_bf16 v[116:119], v[120:123], v[92:99], v124\n\t"
    "ds_read_b64 v[92:93], v106 offset:0\n\t"
    "ds_read_b64 v[94:95], v126 offset:0\n\t"
    "v_add_u32_sdwa v98, s92, v107 dst_sel:DWORD dst_unused:UNUSED_PAD src0_sel:DWORD src1_sel:WORD_1\n\t"
    "v_add_u32_sdwa v107, s92, v107 dst_sel:DWORD dst_unused:UNUSED_PAD src0_sel:DWORD src1_sel:WORD_0\n\t"
    "ds_read_b64 v[96:97], v107 offset:0\n\t"
    "ds_read_b64 v[98:99], v98 offset:0\n\t"
    "buffer_load_dwordx4 v[104:107], v127, %12, s88 offen offset:1024 sc1\n\t"
    "v_add_u32_sdwa v126, s92, v108 dst_sel:DWORD dst_unused:UNUSED_PAD src0_sel:DWORD src1_sel:WORD_1\n\t"
    "v_cvt_pk_bf16_f32 v16, v116, v117\n\t"
    "v_cvt_pk_bf16_f32 v17, v118, v119\n\t"
    "v_and_b32 v119, s67, v19\n\t"
    "v_lshlrev_b32 v118, 16, v19\n\t"
    "v_and_b32 v117, s67, v18\n\t"
    "v_lshlrev_b32 v116, 16, v18\n\t"
    "v_add_u32_sdwa v108, s92, v108 dst_sel:DWORD dst_unused:UNUSED_PAD src0_sel:DWORD src1_sel:WORD_0\n\t"
    "s_waitcnt lgkmcnt(4)\n\t"
    "v_smfmac_f32_16x16x64_bf16 v[116:119], v[120:123], v[84:91], v124\n\t"
    "ds_read_b64 v[84:85], v108 offset:0\n\t"
    "v_mfma_f32_16x16x32_bf16 v[80:83], v[64:67], v[188:191], 0\n\t"
    "ds_read_b64 v[86:87], v126 offset:0\n\t"
    "v_mfma_f32_16x16x32_bf16 v[80:83], v[68:71], v[192:195], v[80:83]\n\t"
    "v_add_u32_sdwa v90, s92, v109 dst_sel:DWORD dst_unused:UNUSED_PAD src0_sel:DWORD src1_sel:WORD_1\n\t"
    "v_add_u32_sdwa v109, s92, v109 dst_sel:DWORD dst_unused:UNUSED_PAD src0_sel:DWORD src1_sel:WORD_0\n\t"
    "ds_read_b64 v[88:89], v109 offset:0\n\t"
    "v_mfma_f32_16x16x32_bf16 v[80:83], v[72:75], v[244:247], v[80:83]\n\t"
    "ds_read_b64 v[90:91], v90 offset:0\n\t"
    "v_mfma_f32_16x16x32_bf16 v[80:83], v[76:79], v[248:251], v[80:83]\n\t"
    "v_add_u32_sdwa v126, s92, v110 dst_sel:DWORD dst_unused:UNUSED_PAD src0_sel:DWORD src1_sel:WORD_1\n\t"
    "v_add_u32_sdwa v110, s92, v110 dst_sel:DWORD dst_unused:UNUSED_PAD src0_sel:DWORD src1_sel:WORD_0\n\t"
    "s_waitcnt vmcnt(2) lgkmcnt(4)\n\t"
    "v_smfmac_f32_16x16x64_bf16 v[116:119], v[120:123], v[92:99], v124\n\t"
    "ds_read_b64 v[92:93], v110 offset:0\n\t"
    "ds_read_b64 v[94:95], v126 offset:0\n\t"
    "v_add_u32_sdwa v98, s92, v111 dst_sel:DWORD dst_unused:UNUSED_PAD src0_sel:DWORD src1_sel:WORD_1\n\t"
    "v_add_u32_sdwa v111, s92, v111 dst_sel:DWORD dst_unused:UNUSED_PAD src0_sel:DWORD src1_sel:WORD_0\n\t"
    "ds_read_b64 v[96:97], v111 offset:0\n\t"
    "ds_read_b64 v[98:99], v98 offset:0\n\t"
    "s_add_u32 s89, s89, s94\n\t"
    "buffer_load_dwordx4 v[108:111], v127, %12, s89 offen offset:0 sc1\n\t"
    "v_add_u32_sdwa v126, s92, v112 dst_sel:DWORD dst_unused:UNUSED_PAD src0_sel:DWORD src1_sel:WORD_1\n\t"
    "v_cvt_pk_bf16_f32 v18, v116, v117\n\t"
    "v_cvt_pk_bf16_f32 v19, v118, v119\n\t"
    "v_and_b32 v119, s67, v25\n\t"
    "v_lshlrev_b32 v118, 16, v25\n\t"
    "v_and_b32 v117, s67, v24\n\t"
    "v_lshlrev_b32 v116, 16, v24\n\t"
    "v_add_u32_sdwa v112, s92, v112 dst_sel:DWORD dst_unused:UNUSED_PAD src0_sel:DWORD src1_sel:WORD_0\n\t"
    "s_waitcnt lgkmcnt(4)\n\t"
    "v_smfmac_f32_16x16x64_bf16 v[116:119], v[120:123], v[84:91], v124\n\t"
    "ds_read_b64 v[84:85], v112 offset:0\n\t"
    "ds_read_b64 v[86:87], v126 offset:0\n\t"
    "v_cvt_pk_bf16_f32 v80, v80, v81\n\t"
    "v_cvt_pk_bf16_f32 v81, v82, v83\n\t"
    "ds_write_b64 v125, v[80:81] offset:32384\n\t"
    "v_add_u32_sdwa v90, s92, v113 dst_sel:DWORD dst_unused:UNUSED_PAD src0_sel:DWORD src1_sel:WORD_1\n\t"
    "v_add_u32_sdwa v113, s92, v113 dst_sel:DWORD dst_unused:UNUSED_PAD src0_sel:DWORD src1_sel:WORD_0\n\t"
    "ds_read_b64 v[88:89], v113 offset:0\n\t"
    "ds_read_b64 v[90:91], v90 offset:0\n\t"
    "v_add_u32_sdwa v126, s92, v114 dst_sel:DWORD dst_unused:UNUSED_PAD src0_sel:DWORD src1_sel:WORD_1\n\t"
    "v_add_u32_sdwa v114, s92, v114 dst_sel:DWORD dst_unused:UNUSED_PAD src0_sel:DWORD src1_sel:WORD_0\n\t"
    "s_waitcnt vmcnt(2) lgkmcnt(5)\n\t"
    "v_smfmac_f32_16x16x64_bf16 v[116:119], v[120:123], v[92:99], v124\n\t"
    "ds_read_b64 v[92:93], v114 offset:0\n\t"
    "ds_read_b64 v[94:95], v126 offset:0\n\t"
    "v_add_u32_sdwa v98, s92, v115 dst_sel:DWORD dst_unused:UNUSED_PAD src0_sel:DWORD src1_sel:WORD_1\n\t"
    "v_add_u32_sdwa v115, s92, v115 dst_sel:DWORD dst_unused:UNUSED_PAD src0_sel:DWORD src1_sel:WORD_0\n\t"
    "ds_read_b64 v[96:97], v115 offset:0\n\t"
    "ds_read_b64 v[98:99], v98 offset:0\n\t"
    "buffer_load_dwordx4 v[112:115], v127, %12, s89 offen offset:1024 sc1\n\t"
    "v_add_u32_sdwa v126, s92, v100 dst_sel:DWORD dst_unused:UNUSED_PAD src0_sel:DWORD src1_sel:WORD_1\n\t"
    "v_cvt_pk_bf16_f32 v24, v116, v117\n\t"
    "v_cvt_pk_bf16_f32 v25, v118, v119\n\t"
    "v_and_b32 v119, s67, v27\n\t"
    "v_lshlrev_b32 v118, 16, v27\n\t"
    "v_and_b32 v117, s67, v26\n\t"
    "v_lshlrev_b32 v116, 16, v26\n\t"
    "v_add_u32_sdwa v100, s92, v100 dst_sel:DWORD dst_unused:UNUSED_PAD src0_sel:DWORD src1_sel:WORD_0\n\t"
    "s_waitcnt lgkmcnt(4)\n\t"
    "v_smfmac_f32_16x16x64_bf16 v[116:119], v[120:123], v[84:91], v124\n\t"
    "ds_read_b64 v[84:85], v100 offset:0\n\t"
    "v_mfma_f32_16x16x32_bf16 v[80:83], v[64:67], v[196:199], 0\n\t"
    "ds_read_b64 v[86:87], v126 offset:0\n\t"
    "v_mfma_f32_16x16x32_bf16 v[80:83], v[68:71], v[200:203], v[80:83]\n\t"
    "v_add_u32_sdwa v90, s92, v101 dst_sel:DWORD dst_unused:UNUSED_PAD src0_sel:DWORD src1_sel:WORD_1\n\t"
    "v_add_u32_sdwa v101, s92, v101 dst_sel:DWORD dst_unused:UNUSED_PAD src0_sel:DWORD src1_sel:WORD_0\n\t"
    "ds_read_b64 v[88:89], v101 offset:0\n\t"
    "v_mfma_f32_16x16x32_bf16 v[80:83], v[72:75], v[252:255], v[80:83]\n\t"
    "ds_read_b64 v[90:91], v90 offset:0\n\t"
    "v_mfma_f32_16x16x32_bf16 v[80:83], v[76:79], v[128:131], v[80:83]\n\t"
    "v_add_u32_sdwa v126, s92, v102 dst_sel:DWORD dst_unused:UNUSED_PAD src0_sel:DWORD src1_sel:WORD_1\n\t"
    "v_add_u32_sdwa v102, s92, v102 dst_sel:DWORD dst_unused:UNUSED_PAD src0_sel:DWORD src1_sel:WORD_0\n\t"
    "s_waitcnt vmcnt(2) lgkmcnt(4)\n\t"
    "v_smfmac_f32_16x16x64_bf16 v[116:119], v[120:123], v[92:99], v124\n\t"
    "ds_read_b64 v[92:93], v102 offset:0\n\t"
    "ds_read_b64 v[94:95], v126 offset:0\n\t"
    "v_add_u32_sdwa v98, s92, v103 dst_sel:DWORD dst_unused:UNUSED_PAD src0_sel:DWORD src1_sel:WORD_1\n\t"
    "v_add_u32_sdwa v103, s92, v103 dst_sel:DWORD dst_unused:UNUSED_PAD src0_sel:DWORD src1_sel:WORD_0\n\t"
    "ds_read_b64 v[96:97], v103 offset:0\n\t"
    "ds_read_b64 v[98:99], v98 offset:0\n\t"
    "s_add_u32 s90, s90, s94\n\t"
    "buffer_load_dwordx4 v[100:103], v127, %12, s90 offen offset:0 sc1\n\t"
    "v_add_u32_sdwa v126, s92, v104 dst_sel:DWORD dst_unused:UNUSED_PAD src0_sel:DWORD src1_sel:WORD_1\n\t"
    "v_cvt_pk_bf16_f32 v26, v116, v117\n\t"
    "v_cvt_pk_bf16_f32 v27, v118, v119\n\t"
    "v_and_b32 v119, s67, v33\n\t"
    "v_lshlrev_b32 v118, 16, v33\n\t"
    "v_and_b32 v117, s67, v32\n\t"
    "v_lshlrev_b32 v116, 16, v32\n\t"
    "v_add_u32_sdwa v104, s92, v104 dst_sel:DWORD dst_unused:UNUSED_PAD src0_sel:DWORD src1_sel:WORD_0\n\t"
    "s_waitcnt lgkmcnt(4)\n\t"
    "v_smfmac_f32_16x16x64_bf16 v[116:119], v[120:123], v[84:91], v124\n\t"
    "ds_read_b64 v[84:85], v104 offset:0\n\t"
    "ds_read_b64 v[86:87], v126 offset:0\n\t"
    "v_cvt_pk_bf16_f32 v80, v80, v81\n\t"
    "v_cvt_pk_bf16_f32 v81, v82, v83\n\t"
    "ds_write_b64 v125, v[80:81] offset:32512\n\t"
    "v_add_u32_sdwa v90, s92, v105 dst_sel:DWORD dst_unused:UNUSED_PAD src0_sel:DWORD src1_sel:WORD_1\n\t"
    "v_add_u32_sdwa v105, s92, v105 dst_sel:DWORD dst_unused:UNUSED_PAD src0_sel:DWORD src1_sel:WORD_0\n\t"
    "ds_read_b64 v[88:89], v105 offset:0\n\t"
    "ds_read_b64 v[90:91], v90 offset:0\n\t"
    "v_add_u32_sdwa v126, s92, v106 dst_sel:DWORD dst_unused:UNUSED_PAD src0_sel:DWORD src1_sel:WORD_1\n\t"
    "v_add_u32_sdwa v106, s92, v106 dst_sel:DWORD dst_unused:UNUSED_PAD src0_sel:DWORD src1_sel:WORD_0\n\t"
    "s_waitcnt vmcnt(2) lgkmcnt(5)\n\t"
    "v_smfmac_f32_16x16x64_bf16 v[116:119], v[120:123], v[92:99], v124\n\t"
    "ds_read_b64 v[92:93], v106 offset:0\n\t"
    "ds_read_b64 v[94:95], v126 offset:0\n\t"
    "v_add_u32_sdwa v98, s92, v107 dst_sel:DWORD dst_unused:UNUSED_PAD src0_sel:DWORD src1_sel:WORD_1\n\t"
    "v_add_u32_sdwa v107, s92, v107 dst_sel:DWORD dst_unused:UNUSED_PAD src0_sel:DWORD src1_sel:WORD_0\n\t"
    "ds_read_b64 v[96:97], v107 offset:0\n\t"
    "ds_read_b64 v[98:99], v98 offset:0\n\t"
    "buffer_load_dwordx4 v[104:107], v127, %12, s90 offen offset:1024 sc1\n\t"
    "v_add_u32_sdwa v126, s92, v108 dst_sel:DWORD dst_unused:UNUSED_PAD src0_sel:DWORD src1_sel:WORD_1\n\t"
    "v_cvt_pk_bf16_f32 v32, v116, v117\n\t"
    "v_cvt_pk_bf16_f32 v33, v118, v119\n\t"
    "v_and_b32 v119, s67, v35\n\t"
    "v_lshlrev_b32 v118, 16, v35\n\t"
    "v_and_b32 v117, s67, v34\n\t"
    "v_lshlrev_b32 v116, 16, v34\n\t"
    "v_add_u32_sdwa v108, s92, v108 dst_sel:DWORD dst_unused:UNUSED_PAD src0_sel:DWORD src1_sel:WORD_0\n\t"
    "s_waitcnt lgkmcnt(4)\n\t"
    "v_smfmac_f32_16x16x64_bf16 v[116:119], v[120:123], v[84:91], v124\n\t"
    "ds_read_b64 v[84:85], v108 offset:0\n\t"
    "v_mfma_f32_16x16x32_bf16 v[80:83], v[64:67], v[204:207], 0\n\t"
    "ds_read_b64 v[86:87], v126 offset:0\n\t"
    "v_mfma_f32_16x16x32_bf16 v[80:83], v[68:71], v[208:211], v[80:83]\n\t"
    "v_add_u32_sdwa v90, s92, v109 dst_sel:DWORD dst_unused:UNUSED_PAD src0_sel:DWORD src1_sel:WORD_1\n\t"
    "v_add_u32_sdwa v109, s92, v109 dst_sel:DWORD dst_unused:UNUSED_PAD src0_sel:DWORD src1_sel:WORD_0\n\t"
    "ds_read_b64 v[88:89], v109 offset:0\n\t"
    "v_mfma_f32_16x16x32_bf16 v[80:83], v[72:75], v[132:135], v[80:83]\n\t"
    "ds_read_b64 v[90:91], v90 offset:0\n\t"
    "v_mfma_f32_16x16x32_bf16 v[80:83], v[76:79], v[136:139], v[80:83]\n\t"
    "v_add_u32_sdwa v126, s92, v110 dst_sel:DWORD dst_unused:UNUSED_PAD src0_sel:DWORD src1_sel:WORD_1\n\t"
    "v_add_u32_sdwa v110, s92, v110 dst_sel:DWORD dst_unused:UNUSED_PAD src0_sel:DWORD src1_sel:WORD_0\n\t"
    "s_cmp_lt_u32 %16, 4\n\t"
    "s_cbranch_scc1 L_tp51_%=\n\t"
    "s_setprio 0\n\t"
    "L_tp51_%=:\n\t"
    "s_waitcnt vmcnt(2) lgkmcnt(4)\n\t"
    "v_smfmac_f32_16x16x64_bf16 v[116:119], v[120:123], v[92:99], v124\n\t"
    "ds_read_b64 v[92:93], v110 offset:0\n\t"
    "ds_read_b64 v[94:95], v126 offset:0\n\t"
    "v_add_u32_sdwa v98, s92, v111 dst_sel:DWORD dst_unused:UNUSED_PAD src0_sel:DWORD src1_sel:WORD_1\n\t"
    "v_add_u32_sdwa v111, s92, v111 dst_sel:DWORD dst_unused:UNUSED_PAD src0_sel:DWORD src1_sel:WORD_0\n\t"
    "ds_read_b64 v[96:97], v111 offset:0\n\t"
    "ds_read_b64 v[98:99], v98 offset:0\n\t"
    "s_add_u32 s91, s91, s94\n\t"
    "buffer_load_dwordx4 v[108:111], v127, %12, s91 offen offset:0 sc1\n\t"
    "v_add_u32_sdwa v126, s92, v112 dst_sel:DWORD dst_unused:UNUSED_PAD src0_sel:DWORD src1_sel:WORD_1\n\t"
    "v_cvt_pk_bf16_f32 v34, v116, v117\n\t"
    "v_cvt_pk_bf16_f32 v35, v118, v119\n\t"
    "v_and_b32 v119, s67, v41\n\t"
    "v_lshlrev_b32 v118, 16, v41\n\t"
    "v_and_b32 v117, s67, v40\n\t"
    "v_lshlrev_b32 v116, 16, v40\n\t"
    "v_add_u32_sdwa v112, s92, v112 dst_sel:DWORD dst_unused:UNUSED_PAD src0_sel:DWORD src1_sel:WORD_0\n\t"
    "s_waitcnt lgkmcnt(4)\n\t"
    "v_smfmac_f32_16x16x64_bf16 v[116:119], v[120:123], v[84:91], v124\n\t"
    "ds_read_b64 v[84:85], v112 offset:0\n\t"
    "ds_read_b64 v[86:87], v126 offset:0\n\t"
    "v_cvt_pk_bf16_f32 v80, v80, v81\n\t"
    "v_cvt_pk_bf16_f32 v81, v82, v83\n\t"
    "ds_write_b64 v125, v[80:81] offset:32640\n\t"
    "v_add_u32_sdwa v90, s92, v113 dst_sel:DWORD dst_unused:UNUSED_PAD src0_sel:DWORD src1_sel:WORD_1\n\t"
    "v_add_u32_sdwa v113, s92, v113 dst_sel:DWORD dst_unused:UNUSED_PAD src0_sel:DWORD src1_sel:WORD_0\n\t"
    "ds_read_b64 v[88:89], v113 offset:0\n\t"
    "ds_read_b64 v[90:91], v90 offset:0\n\t"
    "v_add_u32_sdwa v126, s92, v114 dst_sel:DWORD dst_unused:UNUSED_PAD src0_sel:DWORD src1_sel:WORD_1\n\t"
    "v_add_u32_sdwa v114, s92, v114 dst_sel:DWORD dst_unused:UNUSED_PAD src0_sel:DWORD src1_sel:WORD_0\n\t"
    "s_waitcnt vmcnt(2) lgkmcnt(5)\n\t"
    "v_smfmac_f32_16x16x64_bf16 v[116:119], v[120:123], v[92:99], v124\n\t"
    "ds_read_b64 v[92:93], v114 offset:0\n\t"
    "ds_read_b64 v[94:95], v126 offset:0\n\t"
    "v_add_u32_sdwa v98, s92, v115 dst_sel:DWORD dst_unused:UNUSED_PAD src0_sel:DWORD src1_sel:WORD_1\n\t"
    "v_add_u32_sdwa v115, s92, v115 dst_sel:DWORD dst_unused:UNUSED_PAD src0_sel:DWORD src1_sel:WORD_0\n\t"
    "ds_read_b64 v[96:97], v115 offset:0\n\t"
    "ds_read_b64 v[98:99], v98 offset:0\n\t"
    "buffer_load_dwordx4 v[112:115], v127, %12, s91 offen offset:1024 sc1\n\t"
    "v_add_u32_sdwa v126, s92, v100 dst_sel:DWORD dst_unused:UNUSED_PAD src0_sel:DWORD src1_sel:WORD_1\n\t"
    "v_cvt_pk_bf16_f32 v40, v116, v117\n\t"
    "v_cvt_pk_bf16_f32 v41, v118, v119\n\t"
    "v_and_b32 v119, s67, v43\n\t"
    "v_lshlrev_b32 v118, 16, v43\n\t"
    "v_and_b32 v117, s67, v42\n\t"
    "v_lshlrev_b32 v116, 16, v42\n\t"
    "v_add_u32_sdwa v100, s92, v100 dst_sel:DWORD dst_unused:UNUSED_PAD src0_sel:DWORD src1_sel:WORD_0\n\t"
    "s_waitcnt lgkmcnt(4)\n\t"
    "v_smfmac_f32_16x16x64_bf16 v[116:119], v[120:123], v[84:91], v124\n\t"
    "ds_read_b64 v[84:85], v100 offset:0\n\t"
    "v_mfma_f32_16x16x32_bf16 v[80:83], v[64:67], v[156:159], 0\n\t"
    "ds_read_b64 v[86:87], v126 offset:0\n\t"
    "v_mfma_f32_16x16x32_bf16 v[80:83], v[68:71], v[160:163], v[80:83]\n\t"
    "v_add_u32_sdwa v90, s92, v101 dst_sel:DWORD dst_unused:UNUSED_PAD src0_sel:DWORD src1_sel:WORD_1\n\t"
    "v_add_u32_sdwa v101, s92, v101 dst_sel:DWORD dst_unused:UNUSED_PAD src0_sel:DWORD src1_sel:WORD_0\n\t"
    "ds_read_b64 v[88:89], v101 offset:0\n\t"
    "v_mfma_f32_16x16x32_bf16 v[80:83], v[72:75], v[140:143], v[80:83]\n\t"
    "ds_read_b64 v[90:91], v90 offset:0\n\t"
    "v_mfma_f32_16x16x32_bf16 v[80:83], v[76:79], v[144:147], v[80:83]\n\t"
    "v_add_u32_sdwa v126, s92, v102 dst_sel:DWORD dst_unused:UNUSED_PAD src0_sel:DWORD src1_sel:WORD_1\n\t"
    "v_add_u32_sdwa v102, s92, v102 dst_sel:DWORD dst_unused:UNUSED_PAD src0_sel:DWORD src1_sel:WORD_0\n\t"
    "s_waitcnt vmcnt(2) lgkmcnt(4)\n\t"
    "v_smfmac_f32_16x16x64_bf16 v[116:119], v[120:123], v[92:99], v124\n\t"
    "ds_read_b64 v[92:93], v102 offset:0\n\t"
    "ds_read_b64 v[94:95], v126 offset:0\n\t"
    "v_add_u32_sdwa v98, s92, v103 dst_sel:DWORD dst_unused:UNUSED_PAD src0_sel:DWORD src1_sel:WORD_1\n\t"
    "v_add_u32_sdwa v103, s92, v103 dst_sel:DWORD dst_unused:UNUSED_PAD src0_sel:DWORD src1_sel:WORD_0\n\t"
    "ds_read_b64 v[96:97], v103 offset:0\n\t"
    "ds_read_b64 v[98:99], v98 offset:0\n\t"
    "s_add_u32 s84, s84, s95\n\t"
    "buffer_load_dwordx4 v[100:103], v127, %12, s84 offen offset:0 sc1\n\t"
    "v_add_u32_sdwa v126, s92, v104 dst_sel:DWORD dst_unused:UNUSED_PAD src0_sel:DWORD src1_sel:WORD_1\n\t"
    "v_cvt_pk_bf16_f32 v42, v116, v117\n\t"
    "v_cvt_pk_bf16_f32 v43, v118, v119\n\t"
    "v_and_b32 v119, s67, v49\n\t"
    "v_lshlrev_b32 v118, 16, v49\n\t"
    "v_and_b32 v117, s67, v48\n\t"
    "v_lshlrev_b32 v116, 16, v48\n\t"
    "v_add_u32_sdwa v104, s92, v104 dst_sel:DWORD dst_unused:UNUSED_PAD src0_sel:DWORD src1_sel:WORD_0\n\t"
    "s_waitcnt lgkmcnt(4)\n\t"
    "v_smfmac_f32_16x16x64_bf16 v[116:119], v[120:123], v[84:91], v124\n\t"
    "ds_read_b64 v[84:85], v104 offset:0\n\t"
    "ds_read_b64 v[86:87], v126 offset:0\n\t"
    "v_cvt_pk_bf16_f32 v80, v80, v81\n\t"
    "v_cvt_pk_bf16_f32 v81, v82, v83\n\t"
    "ds_write_b64 v125, v[80:81] offset:32768\n\t"
    "v_add_u32_sdwa v90, s92, v105 dst_sel:DWORD dst_unused:UNUSED_PAD src0_sel:DWORD src1_sel:WORD_1\n\t"
    "v_add_u32_sdwa v105, s92, v105 dst_sel:DWORD dst_unused:UNUSED_PAD src0_sel:DWORD src1_sel:WORD_0\n\t"
    "ds_read_b64 v[88:89], v105 offset:0\n\t"
    "ds_read_b64 v[90:91], v90 offset:0\n\t"
    "v_add_u32_sdwa v126, s92, v106 dst_sel:DWORD dst_unused:UNUSED_PAD src0_sel:DWORD src1_sel:WORD_1\n\t"
    "v_add_u32_sdwa v106, s92, v106 dst_sel:DWORD dst_unused:UNUSED_PAD src0_sel:DWORD src1_sel:WORD_0\n\t"
    "s_waitcnt vmcnt(2) lgkmcnt(5)\n\t"
    "v_smfmac_f32_16x16x64_bf16 v[116:119], v[120:123], v[92:99], v124\n\t"
    "ds_read_b64 v[92:93], v106 offset:0\n\t"
    "ds_read_b64 v[94:95], v126 offset:0\n\t"
    "v_add_u32_sdwa v98, s92, v107 dst_sel:DWORD dst_unused:UNUSED_PAD src0_sel:DWORD src1_sel:WORD_1\n\t"
    "v_add_u32_sdwa v107, s92, v107 dst_sel:DWORD dst_unused:UNUSED_PAD src0_sel:DWORD src1_sel:WORD_0\n\t"
    "ds_read_b64 v[96:97], v107 offset:0\n\t"
    "ds_read_b64 v[98:99], v98 offset:0\n\t"
    "buffer_load_dwordx4 v[104:107], v127, %12, s84 offen offset:1024 sc1\n\t"
    "v_add_u32_sdwa v126, s92, v108 dst_sel:DWORD dst_unused:UNUSED_PAD src0_sel:DWORD src1_sel:WORD_1\n\t"
    "v_cvt_pk_bf16_f32 v48, v116, v117\n\t"
    "v_cvt_pk_bf16_f32 v49, v118, v119\n\t"
    "v_and_b32 v119, s67, v51\n\t"
    "v_lshlrev_b32 v118, 16, v51\n\t"
    "v_and_b32 v117, s67, v50\n\t"
    "v_lshlrev_b32 v116, 16, v50\n\t"
    "v_add_u32_sdwa v108, s92, v108 dst_sel:DWORD dst_unused:UNUSED_PAD src0_sel:DWORD src1_sel:WORD_0\n\t"
    "s_waitcnt lgkmcnt(4)\n\t"
    "v_smfmac_f32_16x16x64_bf16 v[116:119], v[120:123], v[84:91], v124\n\t"
    "ds_read_b64 v[84:85], v108 offset:0\n\t"
    "v_mfma_f32_16x16x32_bf16 v[80:83], v[64:67], v[212:215], 0\n\t"
    "ds_read_b64 v[86:87], v126 offset:0\n\t"
    "v_mfma_f32_16x16x32_bf16 v[80:83], v[68:71], v[216:219], v[80:83]\n\t"
    "v_add_u32_sdwa v90, s92, v109 dst_sel:DWORD dst_unused:UNUSED_PAD src0_sel:DWORD src1_sel:WORD_1\n\t"
    "v_add_u32_sdwa v109, s92, v109 dst_sel:DWORD dst_unused:UNUSED_PAD src0_sel:DWORD src1_sel:WORD_0\n\t"
    "ds_read_b64 v[88:89], v109 offset:0\n\t"
    "v_mfma_f32_16x16x32_bf16 v[80:83], v[72:75], v[148:151], v[80:83]\n\t"
    "ds_read_b64 v[90:91], v90 offset:0\n\t"
    "v_mfma_f32_16x16x32_bf16 v[80:83], v[76:79], v[152:155], v[80:83]\n\t"
    "v_add_u32_sdwa v126, s92, v110 dst_sel:DWORD dst_unused:UNUSED_PAD src0_sel:DWORD src1_sel:WORD_1\n\t"
    "v_add_u32_sdwa v110, s92, v110 dst_sel:DWORD dst_unused:UNUSED_PAD src0_sel:DWORD src1_sel:WORD_0\n\t"
    "s_waitcnt vmcnt(2) lgkmcnt(4)\n\t"
    "v_smfmac_f32_16x16x64_bf16 v[116:119], v[120:123], v[92:99], v124\n\t"
    "ds_read_b64 v[92:93], v110 offset:0\n\t"
    "ds_read_b64 v[94:95], v126 offset:0\n\t"
    "v_add_u32_sdwa v98, s92, v111 dst_sel:DWORD dst_unused:UNUSED_PAD src0_sel:DWORD src1_sel:WORD_1\n\t"
    "v_add_u32_sdwa v111, s92, v111 dst_sel:DWORD dst_unused:UNUSED_PAD src0_sel:DWORD src1_sel:WORD_0\n\t"
    "ds_read_b64 v[96:97], v111 offset:0\n\t"
    "ds_read_b64 v[98:99], v98 offset:0\n\t"
    "s_add_u32 s85, s85, s95\n\t"
    "buffer_load_dwordx4 v[108:111], v127, %12, s85 offen offset:0 sc1\n\t"
    "v_add_u32_sdwa v126, s92, v112 dst_sel:DWORD dst_unused:UNUSED_PAD src0_sel:DWORD src1_sel:WORD_1\n\t"
    "v_cvt_pk_bf16_f32 v50, v116, v117\n\t"
    "v_cvt_pk_bf16_f32 v51, v118, v119\n\t"
    "v_and_b32 v119, s67, v57\n\t"
    "v_lshlrev_b32 v118, 16, v57\n\t"
    "v_and_b32 v117, s67, v56\n\t"
    "v_lshlrev_b32 v116, 16, v56\n\t"
    "v_add_u32_sdwa v112, s92, v112 dst_sel:DWORD dst_unused:UNUSED_PAD src0_sel:DWORD src1_sel:WORD_0\n\t"
    "s_waitcnt lgkmcnt(4)\n\t"
    "v_smfmac_f32_16x16x64_bf16 v[116:119], v[120:123], v[84:91], v124\n\t"
    "ds_read_b64 v[84:85], v112 offset:0\n\t"
    "ds_read_b64 v[86:87], v126 offset:0\n\t"
    "v_cvt_pk_bf16_f32 v80, v80, v81\n\t"
    "v_cvt_pk_bf16_f32 v81, v82, v83\n\t"
    "ds_write_b64 v125, v[80:81] offset:32872\n\t"
    "v_add_u32_sdwa v90, s92, v113 dst_sel:DWORD dst_unused:UNUSED_PAD src0_sel:DWORD src1_sel:WORD_1\n\t"
    "v_add_u32_sdwa v113, s92, v113 dst_sel:DWORD dst_unused:UNUSED_PAD src0_sel:DWORD src1_sel:WORD_0\n\t"
    "ds_read_b64 v[88:89], v113 offset:0\n\t"
    "ds_read_b64 v[90:91], v90 offset:0\n\t"
    "v_add_u32_sdwa v126, s92, v114 dst_sel:DWORD dst_unused:UNUSED_PAD src0_sel:DWORD src1_sel:WORD_1\n\t"
    "v_add_u32_sdwa v114, s92, v114 dst_sel:DWORD dst_unused:UNUSED_PAD src0_sel:DWORD src1_sel:WORD_0\n\t"
    "s_waitcnt vmcnt(2) lgkmcnt(5)\n\t"
    "v_smfmac_f32_16x16x64_bf16 v[116:119], v[120:123], v[92:99], v124\n\t"
    "ds_read_b64 v[92:93], v114 offset:0\n\t"
    "ds_read_b64 v[94:95], v126 offset:0\n\t"
    "v_add_u32_sdwa v98, s92, v115 dst_sel:DWORD dst_unused:UNUSED_PAD src0_sel:DWORD src1_sel:WORD_1\n\t"
    "v_add_u32_sdwa v115, s92, v115 dst_sel:DWORD dst_unused:UNUSED_PAD src0_sel:DWORD src1_sel:WORD_0\n\t"
    "ds_read_b64 v[96:97], v115 offset:0\n\t"
    "ds_read_b64 v[98:99], v98 offset:0\n\t"
    "buffer_load_dwordx4 v[112:115], v127, %12, s85 offen offset:1024 sc1\n\t"
    "s_sub_u32 s92, 64000, s92\n\t"
    "v_add_u32_sdwa v126, s92, v100 dst_sel:DWORD dst_unused:UNUSED_PAD src0_sel:DWORD src1_sel:WORD_1\n\t"
    "v_cvt_pk_bf16_f32 v56, v116, v117\n\t"
    "v_cvt_pk_bf16_f32 v57, v118, v119\n\t"
    "v_and_b32 v119, s67, v59\n\t"
    "v_lshlrev_b32 v118, 16, v59\n\t"
    "v_and_b32 v117, s67, v58\n\t"
    "v_lshlrev_b32 v116, 16, v58\n\t"
    "v_add_u32_sdwa v100, s92, v100 dst_sel:DWORD dst_unused:UNUSED_PAD src0_sel:DWORD src1_sel:WORD_0\n\t"
    "s_waitcnt lgkmcnt(0)\n\t"
    "s_barrier\n\t"
    "v_add_u32 v125, s93, v125\n\t"
    "s_sub_u32 s93, 0, s93\n\t"
    "v_smfmac_f32_16x16x64_bf16 v[116:119], v[120:123], v[84:91], v124\n\t"
    "ds_read_b64 v[84:85], v100 offset:0\n\t"
    "ds_read_b64 v[86:87], v126 offset:0\n\t"
    "v_add_u32_sdwa v90, s92, v101 dst_sel:DWORD dst_unused:UNUSED_PAD src0_sel:DWORD src1_sel:WORD_1\n\t"
    "v_add_u32_sdwa v101, s92, v101 dst_sel:DWORD dst_unused:UNUSED_PAD src0_sel:DWORD src1_sel:WORD_0\n\t"
    "ds_read_b64 v[88:89], v101 offset:0\n\t"
    "ds_read_b64 v[90:91], v90 offset:0\n\t"
    "v_add_u32_sdwa v126, s92, v102 dst_sel:DWORD dst_unused:UNUSED_PAD src0_sel:DWORD src1_sel:WORD_1\n\t"
    "v_add_u32_sdwa v102, s92, v102 dst_sel:DWORD dst_unused:UNUSED_PAD src0_sel:DWORD src1_sel:WORD_0\n\t"
    "s_cmp_lt_u32 %16, 4\n\t"
    "s_cbranch_scc1 L_tp63_%=\n\t"
    "s_setprio 1\n\t"
    "L_tp63_%=:\n\t"
    "s_waitcnt vmcnt(2)\n\t"
    "v_smfmac_f32_16x16x64_bf16 v[116:119], v[120:123], v[92:99], v124\n\t"
    "ds_read_b64 v[92:93], v102 offset:0\n\t"
    "ds_read_b64 v[94:95], v126 offset:0\n\t"
    "v_add_u32_sdwa v98, s92, v103 dst_sel:DWORD dst_unused:UNUSED_PAD src0_sel:DWORD src1_sel:WORD_1\n\t"
    "v_add_u32_sdwa v103, s92, v103 dst_sel:DWORD dst_unused:UNUSED_PAD src0_sel:DWORD src1_sel:WORD_0\n\t"
    "ds_read_b64 v[96:97], v103 offset:0\n\t"
    "ds_read_b64 v[98:99], v98 offset:0\n\t"
    "s_add_u32 s86, s86, s95\n\t"
    "buffer_load_dwordx4 v[100:103], v127, %12, s86 offen offset:0 sc1\n\t"
    "v_add_u32_sdwa v126, s92, v104 dst_sel:DWORD dst_unused:UNUSED_PAD src0_sel:DWORD src1_sel:WORD_1\n\t"
    "v_cvt_pk_bf16_f32 v58, v116, v117\n\t"
    "v_cvt_pk_bf16_f32 v59, v118, v119\n\t"
    "v_and_b32 v119, s67, v5\n\t"
    "v_lshlrev_b32 v118, 16, v5\n\t"
    "v_and_b32 v117, s67, v4\n\t"
    "v_lshlrev_b32 v116, 16, v4\n\t"
    "v_add_u32_sdwa v104, s92, v104 dst_sel:DWORD dst_unused:UNUSED_PAD src0_sel:DWORD src1_sel:WORD_0\n\t"
    "v_add_u32 v79, s96, v127\n\t"
    "s_add_u32 s96, s96, 0x1000\n\t"
    "s_and_b32 s96, s96, 0x3000\n\t"
    "v_add_u32 v79, 128000, v79\n\t"
    "ds_read_b128 v[64:67], v79 offset:0\n\t"
    "ds_read_b128 v[68:71], v79 offset:1024\n\t"
    "ds_read_b128 v[72:75], v79 offset:2048\n\t"
    "ds_read_b128 v[76:79], v79 offset:3072\n\t"
    "s_sub_u32 s66, s66, 1\n\t"
    "s_cmp_lg_u32 s66, 0\n\t"
    "s_cbranch_scc1 L_loop_%=\n\t"
    "s_cmp_eq_u32 %16, 0\n\t"
    "s_cbranch_scc1 L_pub_%=\n\t"
    "L_pubb_%=:\n\t"
    "s_mov_b32 s92, 0xffff\n\t"
    "s_waitcnt lgkmcnt(8)\n\t"
    "v_smfmac_f32_16x16x64_bf16 v[116:119], v[120:123], v[84:91], v124\n\t"
    "ds_read_b64 v[84:85], v104 offset:0\n\t"
    "ds_read_b64 v[86:87], v126 offset:0\n\t"
    "v_lshrrev_b32 v90, 16, v105\n\t"
    "v_and_b32 v105, s92, v105\n\t"
    "ds_read_b64 v[88:89], v105 offset:0\n\t"
    "ds_read_b64 v[90:91], v90 offset:0\n\t"
    "v_lshrrev_b32 v126, 16, v106\n\t"
    "v_and_b32 v106, s92, v106\n\t"
    "s_waitcnt vmcnt(2) lgkmcnt(8)\n\t"
    "v_smfmac_f32_16x16x64_bf16 v[116:119], v[120:123], v[92:99], v124\n\t"
    "ds_read_b64 v[92:93], v106 offset:0\n\t"
    "s_waitcnt lgkmcnt(5)\n\t"
    "v_mfma_f32_16x16x32_bf16 v[80:83], v[64:67], v[164:167], 0\n\t"
    "ds_read_b64 v[94:95], v126 offset:0\n\t"
    "v_mfma_f32_16x16x32_bf16 v[80:83], v[68:71], v[168:171], v[80:83]\n\t"
    "v_lshrrev_b32 v98, 16, v107\n\t"
    "v_and_b32 v107, s92, v107\n\t"
    "ds_read_b64 v[96:97], v107 offset:0\n\t"
    "v_mfma_f32_16x16x32_bf16 v[80:83], v[72:75], v[220:223], v[80:83]\n\t"
    "ds_read_b64 v[98:99], v98 offset:0\n\t"
    "v_mfma_f32_16x16x32_bf16 v[80:83], v[76:79], v[224:227], v[80:83]\n\t"
    "v_lshrrev_b32 v126, 16, v108\n\t"
    "v_cvt_pk_bf16_f32 v4, v116, v117\n\t"
    "v_cvt_pk_bf16_f32 v5, v118, v119\n\t"
    "v_and_b32 v119, s67, v7\n\t"
    "v_lshlrev_b32 v118, 16, v7\n\t"
    "v_and_b32 v117, s67, v6\n\t"
    "v_lshlrev_b32 v116, 16, v6\n\t"
    "v_and_b32 v108, s92, v108\n\t"
    "s_waitcnt lgkmcnt(4)\n\t"
    "v_smfmac_f32_16x16x64_bf16 v[116:119], v[120:123], v[84:91], v124\n\t"
    "ds_read_b64 v[84:85], v108 offset:0\n\t"
    "v_mfma_f32_16x16x32_bf16 v[104:107], v[64:67], v[172:175], 0\n\t"
    "ds_read_b64 v[86:87], v126 offset:0\n\t"
    "v_mfma_f32_16x16x32_bf16 v[104:107], v[68:71], v[176:179], v[104:107]\n\t"
    "v_lshrrev_b32 v90, 16, v109\n\t"
    "v_and_b32 v109, s92, v109\n\t"
    "ds_read_b64 v[88:89], v109 offset:0\n\t"
    "v_mfma_f32_16x16x32_bf16 v[104:107], v[72:75], v[228:231], v[104:107]\n\t"
    "ds_read_b64 v[90:91], v90 offset:0\n\t"
    "v_mfma_f32_16x16x32_bf16 v[104:107], v[76:79], v[232:235], v[104:107]\n\t"
    "v_lshrrev_b32 v126, 16, v110\n\t"
    "v_cvt_pk_bf16_f32 v80, v80, v81\n\t"
    "v_cvt_pk_bf16_f32 v81, v82, v83\n\t"
    "ds_write_b64 v125, v[80:81] offset:0\n\t"
    "v_mfma_f32_16x16x32_bf16 v[80:83], v[64:67], v[180:183], 0\n\t"
    "v_and_b32 v110, s92, v110\n\t"
    "v_mfma_f32_16x16x32_bf16 v[80:83], v[68:71], v[184:187], v[80:83]\n\t"
    "v_mfma_f32_16x16x32_bf16 v[80:83], v[72:75], v[236:239], v[80:83]\n\t"
    "s_waitcnt vmcnt(1) lgkmcnt(5)\n\t"
    "v_smfmac_f32_16x16x64_bf16 v[116:119], v[120:123], v[92:99], v124\n\t"
    "ds_read_b64 v[92:93], v110 offset:0\n\t"
    "v_mfma_f32_16x16x32_bf16 v[80:83], v[76:79], v[240:243], v[80:83]\n\t"
    "ds_read_b64 v[94:95], v126 offset:0\n\t"
    "v_cvt_pk_bf16_f32 v104, v104, v105\n\t"
    "v_cvt_pk_bf16_f32 v105, v106, v107\n\t"
    "ds_write_b64 v125, v[104:105] offset:128\n\t"
    "v_mfma_f32_16x16x32_bf16 v[104:107], v[64:67], v[188:191], 0\n\t"
    "v_lshrrev_b32 v98, 16, v111\n\t"
    "v_and_b32 v111, s92, v111\n\t"
    "ds_read_b64 v[96:97], v111 offset:0\n\t"
    "v_mfma_f32_16x16x32_bf16 v[104:107], v[68:71], v[192:195], v[104:107]\n\t"
    "ds_read_b64 v[98:99], v98 offset:0\n\t"
    "v_mfma_f32_16x16x32_bf16 v[104:107], v[72:75], v[244:247], v[104:107]\n\t"
    "buffer_load_dwordx4 v[108:111], v127, %12, s86 offen offset:1024 sc1\n\t"
    "v_lshrrev_b32 v126, 16, v112\n\t"
    "v_cvt_pk_bf16_f32 v6, v116, v117\n\t"
    "v_cvt_pk_bf16_f32 v7, v118, v119\n\t"
    "v_and_b32 v119, s67, v13\n\t"
    "v_lshlrev_b32 v118, 16, v13\n\t"
    "v_and_b32 v117, s67, v12\n\t"
    "v_lshlrev_b32 v116, 16, v12\n\t"
    "v_and_b32 v112, s92, v112\n\t"
    "s_waitcnt lgkmcnt(6)\n\t"
    "v_smfmac_f32_16x16x64_bf16 v[116:119], v[120:123], v[84:91], v124\n\t"
    "ds_read_b64 v[84:85], v112 offset:0\n\t"
    "v_mfma_f32_16x16x32_bf16 v[104:107], v[76:79], v[248:251], v[104:107]\n\t"
    "ds_read_b64 v[86:87], v126 offset:0\n\t"
    "v_cvt_pk_bf16_f32 v80, v80, v81\n\t"
    "v_cvt_pk_bf16_f32 v81, v82, v83\n\t"
    "ds_write_b64 v125, v[80:81] offset:256\n\t"
    "v_mfma_f32_16x16x32_bf16 v[80:83], v[64:67], v[196:199], 0\n\t"
    "v_lshrrev_b32 v90, 16, v113\n\t"
    "v_and_b32 v113, s92, v113\n\t"
    "ds_read_b64 v[88:89], v113 offset:0\n\t"
    "v_mfma_f32_16x16x32_bf16 v[80:83], v[68:71], v[200:203], v[80:83]\n\t"
    "ds_read_b64 v[90:91], v90 offset:0\n\t"
    "v_mfma_f32_16x16x32_bf16 v[80:83], v[72:75], v[252:255], v[80:83]\n\t"
    "v_lshrrev_b32 v126, 16, v114\n\t"
    "v_mfma_f32_16x16x32_bf16 v[80:83], v[76:79], v[128:131], v[80:83]\n\t"
    "v_and_b32 v114, s92, v114\n\t"
    "v_cvt_pk_bf16_f32 v104, v104, v105\n\t"
    "v_cvt_pk_bf16_f32 v105, v106, v107\n\t"
    "ds_write_b64 v125, v[104:105] offset:384\n\t"
    "v_mfma_f32_16x16x32_bf16 v[104:107], v[64:67], v[204:207], 0\n\t"
    "s_waitcnt vmcnt(1) lgkmcnt(6)\n\t"
    "v_smfmac_f32_16x16x64_bf16 v[116:119], v[120:123], v[92:99], v124\n\t"
    "ds_read_b64 v[92:93], v114 offset:0\n\t"
    "v_mfma_f32_16x16x32_bf16 v[104:107], v[68:71], v[208:211], v[104:107]\n\t"
    "ds_read_b64 v[94:95], v126 offset:0\n\t"
    "v_mfma_f32_16x16x32_bf16 v[104:107], v[72:75], v[132:135], v[104:107]\n\t"
    "v_lshrrev_b32 v98, 16, v115\n\t"
    "v_and_b32 v115, s92, v115\n\t"
    "ds_read_b64 v[96:97], v115 offset:0\n\t"
    "v_mfma_f32_16x16x32_bf16 v[104:107], v[76:79], v[136:139], v[104:107]\n\t"
    "ds_read_b64 v[98:99], v98 offset:0\n\t"
    "v_cvt_pk_bf16_f32 v80, v80, v81\n\t"
    "v_cvt_pk_bf16_f32 v81, v82, v83\n\t"
    "ds_write_b64 v125, v[80:81] offset:512\n\t"
    "v_mfma_f32_16x16x32_bf16 v[80:83], v[64:67], v[156:159], 0\n\t"
    "s_add_u32 s87, s87, s95\n\t"
    "buffer_load_dwordx4 v[112:115], v127, %12, s87 offen offset:0 sc1\n\t"
    "v_lshrrev_b32 v126, 16, v100\n\t"
    "v_mfma_f32_16x16x32_bf16 v[80:83], v[68:71], v[160:163], v[80:83]\n\t"
    "v_cvt_pk_bf16_f32 v12, v116, v117\n\t"
    "v_cvt_pk_bf16_f32 v13, v118, v119\n\t"
    "v_and_b32 v119, s67, v15\n\t"
    "v_lshlrev_b32 v118, 16, v15\n\t"
    "v_and_b32 v117, s67, v14\n\t"
    "v_lshlrev_b32 v116, 16, v14\n\t"
    "v_and_b32 v100, s92, v100\n\t"
    "s_waitcnt lgkmcnt(6)\n\t"
    "v_smfmac_f32_16x16x64_bf16 v[116:119], v[120:123], v[84:91], v124\n\t"
    "ds_read_b64 v[84:85], v100 offset:0\n\t"
    "v_mfma_f32_16x16x32_bf16 v[80:83], v[72:75], v[140:143], v[80:83]\n\t"
    "ds_read_b64 v[86:87], v126 offset:0\n\t"
    "v_mfma_f32_16x16x32_bf16 v[80:83], v[76:79], v[144:147], v[80:83]\n\t"
    "v_lshrrev_b32 v90, 16, v101\n\t"
    "v_and_b32 v101, s92, v101\n\t"
    "ds_read_b64 v[88:89], v101 offset:0\n\t"
    "v_cvt_pk_bf16_f32 v104, v104, v105\n\t"
    "v_cvt_pk_bf16_f32 v105, v106, v107\n\t"
    "ds_write_b64 v125, v[104:105] offset:640\n\t"
    "v_mfma_f32_16x16x32_bf16 v[104:107], v[64:67], v[212:215], 0\n\t"
    "ds_read_b64 v[90:91], v90 offset:0\n\t"
    "v_mfma_f32_16x16x32_bf16 v[104:107], v[68:71], v[216:219], v[104:107]\n\t"
    "v_lshrrev_b32 v126, 16, v102\n\t"
    "v_mfma_f32_16x16x32_bf16 v[104:107], v[72:75], v[148:151], v[104:107]\n\t"
    "v_and_b32 v102, s92, v102\n\t"
    "v_mfma_f32_16x16x32_bf16 v[104:107], v[76:79], v[152:155], v[104:107]\n\t"
    "v_add_u32 v79, s96, v127\n\t"
    "s_add_u32 s96, s96, 0x1000\n\t"
    "s_and_b32 s96, s96, 0x3000\n\t"
    "v_add_u32 v79, 128000, v79\n\t"
    "ds_read_b128 v[64:67], v79 offset:0\n\t"
    "ds_read_b128 v[68:71], v79 offset:1024\n\t"
    "ds_read_b128 v[72:75], v79 offset:2048\n\t"
    "ds_read_b128 v[76:79], v79 offset:3072\n\t"
    "v_cvt_pk_bf16_f32 v80, v80, v81\n\t"
    "v_cvt_pk_bf16_f32 v81, v82, v83\n\t"
    "ds_write_b64 v125, v[80:81] offset:768\n\t"
    "s_waitcnt vmcnt(1) lgkmcnt(11)\n\t"
    "v_smfmac_f32_16x16x64_bf16 v[116:119], v[120:123], v[92:99], v124\n\t"
    "ds_read_b64 v[92:93], v102 offset:0\n\t"
    "ds_read_b64 v[94:95], v126 offset:0\n\t"
    "v_lshrrev_b32 v98, 16, v103\n\t"
    "v_and_b32 v103, s92, v103\n\t"
    "ds_read_b64 v[96:97], v103 offset:0\n\t"
    "ds_read_b64 v[98:99], v98 offset:0\n\t"
    "buffer_load_dwordx4 v[100:103], v127, %12, s87 offen offset:1024 sc1\n\t"
    "v_lshrrev_b32 v126, 16, v108\n\t"
    "v_cvt_pk_bf16_f32 v14, v116, v117\n\t"
    "v_cvt_pk_bf16_f32 v15, v118, v119\n\t"
    "v_and_b32 v119, s67, v21\n\t"
    "v_lshlrev_b32 v118, 16, v21\n\t"
    "v_and_b32 v117, s67, v20\n\t"
    "v_lshlrev_b32 v116, 16, v20\n\t"
    "v_and_b32 v108, s92, v108\n\t"
    "s_waitcnt lgkmcnt(9)\n\t"
    "v_smfmac_f32_16x16x64_bf16 v[116:119], v[120:123], v[84:91], v124\n\t"
    "ds_read_b64 v[84:85], v108 offset:0\n\t"
    "s_waitcnt lgkmcnt(6)\n\t"
    "v_mfma_f32_16x16x32_bf16 v[80:83], v[64:67], v[164:167], 0\n\t"
    "ds_read_b64 v[86:87], v126 offset:0\n\t"
    "v_mfma_f32_16x16x32_bf16 v[80:83], v[68:71], v[168:171], v[80:83]\n\t"
    "v_lshrrev_b32 v90, 16, v109\n\t"
    "v_and_b32 v109, s92, v109\n\t"
    "ds_read_b64 v[88:89], v109 offset:0\n\t"
    "v_mfma_f32_16x16x32_bf16 v[80:83], v[72:75], v[220:223], v[80:83]\n\t"
    "ds_read_b64 v[90:91], v90 offset:0\n\t"
    "v_mfma_f32_16x16x32_bf16 v[80:83], v[76:79], v[224:227], v[80:83]\n\t"
    "v_lshrrev_b32 v126, 16, v110\n\t"
    "v_cvt_pk_bf16_f32 v104, v104, v105\n\t"
    "v_cvt_pk_bf16_f32 v105, v106, v107\n\t"
    "ds_write_b64 v125, v[104:105] offset:872\n\t"
    "v_mfma_f32_16x16x32_bf16 v[104:107], v[64:67], v[172:175], 0\n\t"
    "v_and_b32 v110, s92, v110\n\t"
    "v_mfma_f32_16x16x32_bf16 v[104:107], v[68:71], v[176:179], v[104:107]\n\t"
    "s_waitcnt vmcnt(1) lgkmcnt(5)\n\t"
    "v_smfmac_f32_16x16x64_bf16 v[116:119], v[120:123], v[92:99], v124\n\t"
    "ds_read_b64 v[92:93], v110 offset:0\n\t"
    "v_mfma_f32_16x16x32_bf16 v[104:107], v[72:75], v[228:231], v[104:107]\n\t"
    "ds_read_b64 v[94:95], v126 offset:0\n\t"
    "v_mfma_f32_16x16x32_bf16 v[104:107], v[76:79], v[232:235], v[104:107]\n\t"
    "v_lshrrev_b32 v98, 16, v111\n\t"
    "v_and_b32 v111, s92, v111\n\t"
    "ds_read_b64 v[96:97], v111 offset:0\n\t"
    "v_cvt_pk_bf16_f32 v80, v80, v81\n\t"
    "v_cvt_pk_bf16_f32 v81, v82, v83\n\t"
    "ds_write_b64 v125, v[80:81] offset:32000\n\t"
    "v_mfma_f32_16x16x32_bf16 v[80:83], v[64:67], v[180:183], 0\n\t"
    "ds_read_b64 v[98:99], v98 offset:0\n\t"
    "v_mfma_f32_16x16x32_bf16 v[80:83], v[68:71], v[184:187], v[80:83]\n\t"
    "s_add_u32 s84, s84, 0x8000\n\t"
    "buffer_load_dwordx4 v[108:111], v127, %12, s84 offen offset:0 sc1\n\t"
    "v_lshrrev_b32 v126, 16, v112\n\t"
    "v_mfma_f32_16x16x32_bf16 v[80:83], v[72:75], v[236:239], v[80:83]\n\t"
    "v_cvt_pk_bf16_f32 v20, v116, v117\n\t"
    "v_cvt_pk_bf16_f32 v21, v118, v119\n\t"
    "v_and_b32 v119, s67, v23\n\t"
    "v_lshlrev_b32 v118, 16, v23\n\t"
    "v_and_b32 v117, s67, v22\n\t"
    "v_lshlrev_b32 v116, 16, v22\n\t"
    "v_and_b32 v112, s92, v112\n\t"
    "s_waitcnt lgkmcnt(6)\n\t"
    "v_smfmac_f32_16x16x64_bf16 v[116:119], v[120:123], v[84:91], v124\n\t"
    "ds_read_b64 v[84:85], v112 offset:0\n\t"
    "v_mfma_f32_16x16x32_bf16 v[80:83], v[76:79], v[240:243], v[80:83]\n\t"
    "ds_read_b64 v[86:87], v126 offset:0\n\t"
    "v_cvt_pk_bf16_f32 v104, v104, v105\n\t"
    "v_cvt_pk_bf16_f32 v105, v106, v107\n\t"
    "ds_write_b64 v125, v[104:105] offset:32128\n\t"
    "v_mfma_f32_16x16x32_bf16 v[104:107], v[64:67], v[188:191], 0\n\t"
    "v_lshrrev_b32 v90, 16, v113\n\t"
    "v_and_b32 v113, s92, v113\n\t"
    "ds_read_b64 v[88:89], v113 offset:0\n\t"
    "v_mfma_f32_16x16x32_bf16 v[104:107], v[68:71], v[192:195], v[104:107]\n\t"
    "ds_read_b64 v[90:91], v90 offset:0\n\t"
    "v_mfma_f32_16x16x32_bf16 v[104:107], v[72:75], v[244:247], v[104:107]\n\t"
    "v_lshrrev_b32 v126, 16, v114\n\t"
    "v_mfma_f32_16x16x32_bf16 v[104:107], v[76:79], v[248:251], v[104:107]\n\t"
    "v_and_b32 v114, s92, v114\n\t"
    "v_cvt_pk_bf16_f32 v80, v80, v81\n\t"
    "v_cvt_pk_bf16_f32 v81, v82, v83\n\t"
    "ds_write_b64 v125, v[80:81] offset:32256\n\t"
    "v_mfma_f32_16x16x32_bf16 v[80:83], v[64:67], v[196:199], 0\n\t"
    "s_waitcnt vmcnt(1) lgkmcnt(6)\n\t"
    "v_smfmac_f32_16x16x64_bf16 v[116:119], v[120:123], v[92:99], v124\n\t"
    "ds_read_b64 v[92:93], v114 offset:0\n\t"
    "v_mfma_f32_16x16x32_bf16 v[80:83], v[68:71], v[200:203], v[80:83]\n\t"
    "ds_read_b64 v[94:95], v126 offset:0\n\t"
    "v_mfma_f32_16x16x32_bf16 v[80:83], v[72:75], v[252:255], v[80:83]\n\t"
    "v_lshrrev_b32 v98, 16, v115\n\t"
    "v_and_b32 v115, s92, v115\n\t"
    "ds_read_b64 v[96:97], v115 offset:0\n\t"
    "v_mfma_f32_16x16x32_bf16 v[80:83], v[76:79], v[128:131], v[80:83]\n\t"
    "ds_read_b64 v[98:99], v98 offset:0\n\t"
    "v_cvt_pk_bf16_f32 v104, v104, v105\n\t"
    "v_cvt_pk_bf16_f32 v105, v106, v107\n\t"
    "ds_write_b64 v125, v[104:105] offset:32384\n\t"
    "v_mfma_f32_16x16x32_bf16 v[104:107], v[64:67], v[204:207], 0\n\t"
    "buffer_load_dwordx4 v[112:115], v127, %12, s84 offen offset:1024 sc1\n\t"
    "v_lshrrev_b32 v126, 16, v100\n\t"
    "v_cvt_pk_bf16_f32 v22, v116, v117\n\t"
    "v_cvt_pk_bf16_f32 v23, v118, v119\n\t"
    "v_and_b32 v119, s67, v29\n\t"
    "v_lshlrev_b32 v118, 16, v29\n\t"
    "v_and_b32 v117, s67, v28\n\t"
    "v_lshlrev_b32 v116, 16, v28\n\t"
    "v_and_b32 v100, s92, v100\n\t"
    "s_waitcnt lgkmcnt(6)\n\t"
    "v_smfmac_f32_16x16x64_bf16 v[116:119], v[120:123], v[84:91], v124\n\t"
    "ds_read_b64 v[84:85], v100 offset:0\n\t"
    "v_mfma_f32_16x16x32_bf16 v[104:107], v[68:71], v[208:211], v[104:107]\n\t"
    "ds_read_b64 v[86:87], v126 offset:0\n\t"
    "v_mfma_f32_16x16x32_bf16 v[104:107], v[72:75], v[132:135], v[104:107]\n\t"
    "v_lshrrev_b32 v90, 16, v101\n\t"
    "v_and_b32 v101, s92, v101\n\t"
    "ds_read_b64 v[88:89], v101 offset:0\n\t"
    "v_mfma_f32_16x16x32_bf16 v[104:107], v[76:79], v[136:139], v[104:107]\n\t"
    "ds_read_b64 v[90:91], v90 offset:0\n\t"
    "v_cvt_pk_bf16_f32 v80, v80, v81\n\t"
    "v_cvt_pk_bf16_f32 v81, v82, v83\n\t"
    "ds_write_b64 v125, v[80:81] offset:32512\n\t"
    "v_mfma_f32_16x16x32_bf16 v[80:83], v[64:67], v[156:159], 0\n\t"
    "v_lshrrev_b32 v126, 16, v102\n\t"
    "v_mfma_f32_16x16x32_bf16 v[80:83], v[68:71], v[160:163], v[80:83]\n\t"
    "v_and_b32 v102, s92, v102\n\t"
    "v_mfma_f32_16x16x32_bf16 v[80:83], v[72:75], v[140:143], v[80:83]\n\t"
    "v_mfma_f32_16x16x32_bf16 v[80:83], v[76:79], v[144:147], v[80:83]\n\t"
    "v_cvt_pk_bf16_f32 v104, v104, v105\n\t"
    "v_cvt_pk_bf16_f32 v105, v106, v107\n\t"
    "ds_write_b64 v125, v[104:105] offset:32640\n\t"
    "s_waitcnt vmcnt(1) lgkmcnt(7)\n\t"
    "v_smfmac_f32_16x16x64_bf16 v[116:119], v[120:123], v[92:99], v124\n\t"
    "ds_read_b64 v[92:93], v102 offset:0\n\t"
    "v_mfma_f32_16x16x32_bf16 v[104:107], v[64:67], v[212:215], 0\n\t"
    "ds_read_b64 v[94:95], v126 offset:0\n\t"
    "v_mfma_f32_16x16x32_bf16 v[104:107], v[68:71], v[216:219], v[104:107]\n\t"
    "v_lshrrev_b32 v98, 16, v103\n\t"
    "v_and_b32 v103, s92, v103\n\t"
    "ds_read_b64 v[96:97], v103 offset:0\n\t"
    "v_mfma_f32_16x16x32_bf16 v[104:107], v[72:75], v[148:151], v[104:107]\n\t"
    "ds_read_b64 v[98:99], v98 offset:0\n\t"
    "v_mfma_f32_16x16x32_bf16 v[104:107], v[76:79], v[152:155], v[104:107]\n\t"
    "s_add_u32 s85, s85, 0x8000\n\t"
    "buffer_load_dwordx4 v[100:103], v127, %12, s85 offen offset:0 sc1\n\t"
    "v_lshrrev_b32 v126, 16, v108\n\t"
    "v_cvt_pk_bf16_f32 v80, v80, v81\n\t"
    "v_cvt_pk_bf16_f32 v81, v82, v83\n\t"
    "ds_write_b64 v125, v[80:81] offset:32768\n\t"
    "s_nop 1\n\t"
    "v_cvt_pk_bf16_f32 v104, v104, v105\n\t"
    "v_cvt_pk_bf16_f32 v105, v106, v107\n\t"
    "ds_write_b64 v125, v[104:105] offset:32872\n\t"
    "v_cvt_pk_bf16_f32 v28, v116, v117\n\t"
    "v_cvt_pk_bf16_f32 v29, v118, v119\n\t"
    "v_and_b32 v119, s67, v31\n\t"
    "v_lshlrev_b32 v118, 16, v31\n\t"
    "v_and_b32 v117, s67, v30\n\t"
    "v_lshlrev_b32 v116, 16, v30\n\t"
    "v_and_b32 v108, s92, v108\n\t"
    "v_mov_b32 v155, 138896\n\t"
    "v_mov_b32 v144, 0\n\t"
    "v_mov_b32 v145, 0\n\t"
    "ds_write_b64 v155, v[144:145]\n\t"
    "s_waitcnt lgkmcnt(0)\n\t"
    "s_barrier\n\t"
    "v_smfmac_f32_16x16x64_bf16 v[116:119], v[120:123], v[84:91], v124\n\t"
    "ds_read_b64 v[84:85], v108 offset:64000\n\t"
    "ds_read_b64 v[86:87], v126 offset:64000\n\t"
    "v_lshrrev_b32 v90, 16, v109\n\t"
    "v_and_b32 v109, s92, v109\n\t"
    "ds_read_b64 v[88:89], v109 offset:64000\n\t"
    "ds_read_b64 v[90:91], v90 offset:64000\n\t"
    "buffer_load_dwordx4 v[104:107], v127, %12, s85 offen offset:1024 sc1\n\t"
    "v_lshrrev_b32 v126, 16, v110\n\t"
    "v_and_b32 v110, s92, v110\n\t"
    "s_waitcnt vmcnt(2)\n\t"
    "v_smfmac_f32_16x16x64_bf16 v[116:119], v[120:123], v[92:99], v124\n\t"
    "ds_read_b64 v[92:93], v110 offset:64000\n\t"
    "ds_read_b64 v[94:95], v126 offset:64000\n\t"
    "v_lshrrev_b32 v98, 16, v111\n\t"
    "v_and_b32 v111, s92, v111\n\t"
    "ds_read_b64 v[96:97], v111 offset:64000\n\t"
    "ds_read_b64 v[98:99], v98 offset:64000\n\t"
    "s_add_u32 s86, s86, 0x8000\n\t"
    "buffer_load_dwordx4 v[108:111], v127, %12, s86 offen offset:0 sc1\n\t"
    "v_lshrrev_b32 v252, 16, v112\n\t"
    "v_and_b32 v112, s92, v112\n\t"
    "v_lshrrev_b32 v253, 16, v113\n\t"
    "v_cvt_pk_bf16_f32 v30, v116, v117\n\t"
    "v_cvt_pk_bf16_f32 v31, v118, v119\n\t"
    "v_and_b32 v119, s67, v5\n\t"
    "v_lshlrev_b32 v118, 16, v5\n\t"
    "v_and_b32 v117, s67, v4\n\t"
    "v_lshlrev_b32 v116, 16, v4\n\t"
    "v_and_b32 v113, s92, v113\n\t"
    "v_mov_b32 v82, 0xbdd2d3e8\n\t"
    "v_mov_b32 v83, 0xbdd2d3e8\n\t"
    "v_mov_b32 v81, 0xc0135761\n\t"
    "v_lshrrev_b32 v125, 2, v127\n\t"
    "v_mov_b32 v80, 0xc0135761\n\t"
    "s_waitcnt lgkmcnt(4)\n\t"
    "v_smfmac_f32_16x16x64_bf16 v[116:119], v[120:123], v[84:91], v124\n\t"
    "ds_read_b64 v[84:85], v112 offset:64000\n\t"
    "ds_read_b64 v[86:87], v252 offset:64000\n\t"
    "ds_read_b64 v[88:89], v113 offset:64000\n\t"
    "ds_read_b64 v[90:91], v253 offset:64000\n\t"
    "v_lshrrev_b32 v254, 16, v114\n\t"
    "v_and_b32 v114, s92, v114\n\t"
    "v_lshrrev_b32 v255, 16, v115\n\t"
    "v_and_b32 v115, s92, v115\n\t"
    "s_lshl_b32 s65, %16, 10\n\t"
    "s_add_u32 s65, s65, 30408704\n\t"
    "s_xor_b32 s61, %14, 8\n\t"
    "s_mul_i32 s61, s61, 69632\n\t"
    "s_add_u32 s61, s61, s65\n\t"
    "s_mul_i32 s94, %14, 69632\n\t"
    "s_add_u32 s94, s94, s65\n\t"
    "s_lshl_b32 s65, %16, 9\n\t"
    "s_sub_u32 s65, 0x10000, s65\n\t"
    "s_add_u32 s93, s94, s65\n\t"
    "s_add_u32 s96, s61, s65\n\t"
    "v_mov_b32 v165, 1\n\t"
    "s_waitcnt vmcnt(2) lgkmcnt(4)\n\t"
    "v_smfmac_f32_16x16x64_bf16 v[116:119], v[120:123], v[92:99], v124\n\t"
    "ds_read_b64 v[92:93], v114 offset:64000\n\t"
    "ds_read_b64 v[94:95], v254 offset:64000\n\t"
    "ds_read_b64 v[96:97], v115 offset:64000\n\t"
    "ds_read_b64 v[98:99], v255 offset:64000\n\t"
    "buffer_load_dwordx4 v[172:175], v127, %12, s86 offen offset:1024 sc1\n\t"
    "v_lshrrev_b32 v252, 16, v100\n\t"
    "v_and_b32 v100, s92, v100\n\t"
    "v_lshrrev_b32 v253, 16, v101\n\t"
    "v_cvt_pk_bf16_f32 v4, v116, v117\n\t"
    "v_cvt_pk_bf16_f32 v5, v118, v119\n\t"
    "v_and_b32 v119, s67, v7\n\t"
    "v_lshlrev_b32 v118, 16, v7\n\t"
    "v_and_b32 v117, s67, v6\n\t"
    "v_lshlrev_b32 v116, 16, v6\n\t"
    "v_and_b32 v101, s92, v101\n\t"
    "v_mov_b32 v156, v120\n\t"
    "v_mov_b32 v160, 0\n\t"
    "v_mov_b32 v161, v121\n\t"
    "v_mov_b32 v157, 0\n\t"
    "v_mov_b32 v158, v122\n\t"
    "v_mov_b32 v162, 0\n\t"
    "v_mov_b32 v163, v123\n\t"
    "v_mov_b32 v159, 0\n\t"
    "s_waitcnt lgkmcnt(4)\n\t"
    "v_smfmac_f32_16x16x64_bf16 v[116:119], v[120:123], v[84:91], v124\n\t"
    "ds_read_b64 v[84:85], v100 offset:64000\n\t"
    "ds_read_b64 v[86:87], v252 offset:64000\n\t"
    "ds_read_b64 v[88:89], v101 offset:64000\n\t"
    "ds_read_b64 v[90:91], v253 offset:64000\n\t"
    "s_add_u32 s87, s87, 0x8000\n\t"
    "buffer_load_dwordx4 v[176:179], v127, %12, s87 offen offset:0 sc1\n\t"
    "v_lshrrev_b32 v254, 16, v102\n\t"
    "v_and_b32 v102, s92, v102\n\t"
    "v_lshrrev_b32 v255, 16, v103\n\t"
    "v_and_b32 v103, s92, v103\n\t"
    "s_cmp_lt_u32 %16, 4\n\t"
    "s_cbranch_scc1 L_tp163_%=\n\t"
    "s_setprio 0\n\t"
    "L_tp163_%=:\n\t"
    "s_waitcnt vmcnt(3) lgkmcnt(4)\n\t"
    "v_smfmac_f32_16x16x64_bf16 v[116:119], v[120:123], v[92:99], v124\n\t"
    "ds_read_b64 v[92:93], v102 offset:64000\n\t"
    "ds_read_b64 v[94:95], v254 offset:64000\n\t"
    "ds_read_b64 v[96:97], v103 offset:64000\n\t"
    "ds_read_b64 v[98:99], v255 offset:64000\n\t"
    "buffer_load_dwordx4 v[180:183], v127, %12, s87 offen offset:1024 sc1\n\t"
    "v_lshrrev_b32 v252, 16, v104\n\t"
    "v_and_b32 v104, s92, v104\n\t"
    "v_lshrrev_b32 v253, 16, v105\n\t"
    "v_cvt_pk_bf16_f32 v6, v116, v117\n\t"
    "v_cvt_pk_bf16_f32 v7, v118, v119\n\t"
    "v_and_b32 v119, s67, v13\n\t"
    "v_lshlrev_b32 v118, 16, v13\n\t"
    "v_and_b32 v117, s67, v12\n\t"
    "v_lshlrev_b32 v116, 16, v12\n\t"
    "v_and_b32 v105, s92, v105\n\t"
    "buffer_store_dwordx4 v[4:7], v127, %12, s61 offen sc1\n\t"
    "s_waitcnt lgkmcnt(4)\n\t"
    "v_smfmac_f32_16x16x64_bf16 v[116:119], v[120:123], v[84:91], v124\n\t"
    "ds_read_b64 v[84:85], v104 offset:64000\n\t"
    "ds_read_b64 v[86:87], v252 offset:64000\n\t"
    "ds_read_b64 v[88:89], v105 offset:64000\n\t"
    "ds_read_b64 v[90:91], v253 offset:64000\n\t"
    "s_add_u32 s88, s88, s95\n\t"
    "buffer_load_dwordx4 v[184:187], v127, %12, s88 offen offset:0 sc1\n\t"
    "v_lshrrev_b32 v254, 16, v106\n\t"
    "v_and_b32 v106, s92, v106\n\t"
    "v_lshrrev_b32 v255, 16, v107\n\t"
    "v_and_b32 v107, s92, v107\n\t"
    "s_waitcnt vmcnt(5) lgkmcnt(4)\n\t"
    "v_smfmac_f32_16x16x64_bf16 v[116:119], v[120:123], v[92:99], v124\n\t"
    "ds_read_b64 v[92:93], v106 offset:64000\n\t"
    "ds_read_b64 v[94:95], v254 offset:64000\n\t"
    "ds_read_b64 v[96:97], v107 offset:64000\n\t"
    "ds_read_b64 v[98:99], v255 offset:64000\n\t"
    "s_add_u32 s88, s88, 0x8000\n\t"
    "buffer_load_dwordx4 v[188:191], v127, %12, s88 offen offset:0 sc1\n\t"
    "v_lshrrev_b32 v252, 16, v108\n\t"
    "v_and_b32 v108, s92, v108\n\t"
    "v_lshrrev_b32 v253, 16, v109\n\t"
    "v_cvt_pk_bf16_f32 v12, v116, v117\n\t"
    "v_cvt_pk_bf16_f32 v13, v118, v119\n\t"
    "v_and_b32 v119, s67, v15\n\t"
    "v_lshlrev_b32 v118, 16, v15\n\t"
    "v_and_b32 v117, s67, v14\n\t"
    "v_lshlrev_b32 v116, 16, v14\n\t"
    "v_and_b32 v109, s92, v109\n\t"
    "s_waitcnt lgkmcnt(4)\n\t"
    "v_smfmac_f32_16x16x64_bf16 v[116:119], v[120:123], v[84:91], v124\n\t"
    "ds_read_b64 v[84:85], v108 offset:64000\n\t"
    "ds_read_b64 v[86:87], v252 offset:64000\n\t"
    "ds_read_b64 v[88:89], v109 offset:64000\n\t"
    "ds_read_b64 v[90:91], v253 offset:64000\n\t"
    "s_sub_u32 s88, s88, 0x8000\n\t"
    "buffer_load_dwordx4 v[236:239], v127, %12, s88 offen offset:1024 sc1\n\t"
    "v_lshrrev_b32 v254, 16, v110\n\t"
    "v_and_b32 v110, s92, v110\n\t"
    "v_lshrrev_b32 v255, 16, v111\n\t"
    "v_and_b32 v111, s92, v111\n\t"
    "s_waitcnt vmcnt(6) lgkmcnt(4)\n\t"
    "v_smfmac_f32_16x16x64_bf16 v[116:119], v[120:123], v[92:99], v124\n\t"
    "ds_read_b64 v[92:93], v110 offset:64000\n\t"
    "ds_read_b64 v[94:95], v254 offset:64000\n\t"
    "ds_read_b64 v[96:97], v111 offset:64000\n\t"
    "ds_read_b64 v[98:99], v255 offset:64000\n\t"
    "s_add_u32 s88, s88, 0x8000\n\t"
    "buffer_load_dwordx4 v[192:195], v127, %12, s88 offen offset:1024 sc1\n\t"
    "v_lshrrev_b32 v252, 16, v172\n\t"
    "v_and_b32 v172, s92, v172\n\t"
    "v_lshrrev_b32 v253, 16, v173\n\t"
    "v_cvt_pk_bf16_f32 v14, v116, v117\n\t"
    "v_cvt_pk_bf16_f32 v15, v118, v119\n\t"
    "v_and_b32 v119, s67, v21\n\t"
    "v_lshlrev_b32 v118, 16, v21\n\t"
    "v_and_b32 v117, s67, v20\n\t"
    "v_lshlrev_b32 v116, 16, v20\n\t"
    "v_and_b32 v173, s92, v173\n\t"
    "s_waitcnt lgkmcnt(4)\n\t"
    "v_smfmac_f32_16x16x64_bf16 v[116:119], v[120:123], v[84:91], v124\n\t"
    "ds_read_b64 v[84:85], v172 offset:64000\n\t"
    "ds_read_b64 v[86:87], v252 offset:64000\n\t"
    "ds_read_b64 v[88:89], v173 offset:64000\n\t"
    "ds_read_b64 v[90:91], v253 offset:64000\n\t"
    "v_lshrrev_b32 v254, 16, v174\n\t"
    "v_and_b32 v174, s92, v174\n\t"
    "v_lshrrev_b32 v255, 16, v175\n\t"
    "v_and_b32 v175, s92, v175\n\t"
    "s_waitcnt vmcnt(6) lgkmcnt(4)\n\t"
    "v_smfmac_f32_16x16x64_bf16 v[116:119], v[120:123], v[92:99], v124\n\t"
    "ds_read_b64 v[92:93], v174 offset:64000\n\t"
    "ds_read_b64 v[94:95], v254 offset:64000\n\t"
    "ds_read_b64 v[96:97], v175 offset:64000\n\t"
    "ds_read_b64 v[98:99], v255 offset:64000\n\t"
    "s_add_u32 s89, s89, s95\n\t"
    "buffer_load_dwordx4 v[196:199], v127, %12, s89 offen offset:0 sc1\n\t"
    "v_lshrrev_b32 v252, 16, v176\n\t"
    "v_and_b32 v176, s92, v176\n\t"
    "v_lshrrev_b32 v253, 16, v177\n\t"
    "v_cvt_pk_bf16_f32 v20, v116, v117\n\t"
    "v_cvt_pk_bf16_f32 v21, v118, v119\n\t"
    "v_and_b32 v119, s67, v23\n\t"
    "v_lshlrev_b32 v118, 16, v23\n\t"
    "v_and_b32 v117, s67, v22\n\t"
    "v_lshlrev_b32 v116, 16, v22\n\t"
    "v_and_b32 v177, s92, v177\n\t"
    "s_waitcnt lgkmcnt(4)\n\t"
    "v_smfmac_f32_16x16x64_bf16 v[116:119], v[120:123], v[84:91], v124\n\t"
    "ds_read_b64 v[84:85], v176 offset:64000\n\t"
    "ds_read_b64 v[86:87], v252 offset:64000\n\t"
    "ds_read_b64 v[88:89], v177 offset:64000\n\t"
    "ds_read_b64 v[90:91], v253 offset:64000\n\t"
    "v_lshrrev_b32 v254, 16, v178\n\t"
    "v_and_b32 v178, s92, v178\n\t"
    "v_lshrrev_b32 v255, 16, v179\n\t"
    "v_and_b32 v179, s92, v179\n\t"
    "s_waitcnt vmcnt(6) lgkmcnt(4)\n\t"
    "v_smfmac_f32_16x16x64_bf16 v[116:119], v[120:123], v[92:99], v124\n\t"
    "ds_read_b64 v[92:93], v178 offset:64000\n\t"
    "ds_read_b64 v[94:95], v254 offset:64000\n\t"
    "ds_read_b64 v[96:97], v179 offset:64000\n\t"
    "ds_read_b64 v[98:99], v255 offset:64000\n\t"
    "s_add_u32 s89, s89, 0x8000\n\t"
    "buffer_load_dwordx4 v[200:203], v127, %12, s89 offen offset:0 sc1\n\t"
    "v_lshrrev_b32 v252, 16, v180\n\t"
    "v_and_b32 v180, s92, v180\n\t"
    "v_lshrrev_b32 v253, 16, v181\n\t"
    "v_cvt_pk_bf16_f32 v22, v116, v117\n\t"
    "v_cvt_pk_bf16_f32 v23, v118, v119\n\t"
    "v_and_b32 v119, s67, v29\n\t"
    "v_lshlrev_b32 v118, 16, v29\n\t"
    "v_and_b32 v117, s67, v28\n\t"
    "v_lshlrev_b32 v116, 16, v28\n\t"
    "v_and_b32 v181, s92, v181\n\t"
    "s_add_u32 s61, s61, 0x2000\n\t"
    "buffer_store_dwordx4 v[12:15], v127, %12, s61 offen sc1\n\t"
    "s_waitcnt lgkmcnt(4)\n\t"
    "v_smfmac_f32_16x16x64_bf16 v[116:119], v[120:123], v[84:91], v124\n\t"
    "ds_read_b64 v[84:85], v180 offset:64000\n\t"
    "ds_read_b64 v[86:87], v252 offset:64000\n\t"
    "ds_read_b64 v[88:89], v181 offset:64000\n\t"
    "ds_read_b64 v[90:91], v253 offset:64000\n\t"
    "v_lshrrev_b32 v254, 16, v182\n\t"
    "v_and_b32 v182, s92, v182\n\t"
    "v_lshrrev_b32 v255, 16, v183\n\t"
    "v_and_b32 v183, s92, v183\n\t"
    "s_waitcnt vmcnt(6) lgkmcnt(4)\n\t"
    "v_smfmac_f32_16x16x64_bf16 v[116:119], v[120:123], v[92:99], v124\n\t"
    "ds_read_b64 v[92:93], v182 offset:64000\n\t"
    "ds_read_b64 v[94:95], v254 offset:64000\n\t"
    "ds_read_b64 v[96:97], v183 offset:64000\n\t"
    "ds_read_b64 v[98:99], v255 offset:64000\n\t"
    "s_sub_u32 s89, s89, 0x8000\n\t"
    "buffer_load_dwordx4 v[204:207], v127, %12, s89 offen offset:1024 sc1\n\t"
    "v_lshrrev_b32 v252, 16, v184\n\t"
    "v_and_b32 v184, s92, v184\n\t"
    "v_lshrrev_b32 v253, 16, v185\n\t"
    "v_cvt_pk_bf16_f32 v28, v116, v117\n\t"
    "v_cvt_pk_bf16_f32 v29, v118, v119\n\t"
    "v_and_b32 v119, s67, v31\n\t"
    "v_lshlrev_b32 v118, 16, v31\n\t"
    "v_and_b32 v117, s67, v30\n\t"
    "v_lshlrev_b32 v116, 16, v30\n\t"
    "v_and_b32 v185, s92, v185\n\t"
    "s_waitcnt lgkmcnt(4)\n\t"
    "v_smfmac_f32_16x16x64_bf16 v[116:119], v[120:123], v[84:91], v124\n\t"
    "ds_read_b64 v[84:85], v184 offset:0\n\t"
    "ds_read_b64 v[86:87], v252 offset:0\n\t"
    "ds_read_b64 v[88:89], v185 offset:0\n\t"
    "ds_read_b64 v[90:91], v253 offset:0\n\t"
    "v_lshrrev_b32 v254, 16, v186\n\t"
    "v_and_b32 v186, s92, v186\n\t"
    "v_lshrrev_b32 v255, 16, v187\n\t"
    "v_and_b32 v187, s92, v187\n\t"
    "s_cmp_lt_u32 %16, 4\n\t"
    "s_cbranch_scc1 L_tp175_%=\n\t"
    "s_setprio 1\n\t"
    "L_tp175_%=:\n\t"
    "s_waitcnt vmcnt(6) lgkmcnt(4)\n\t"
    "v_smfmac_f32_16x16x64_bf16 v[116:119], v[120:123], v[92:99], v124\n\t"
    "ds_read_b64 v[92:93], v186 offset:0\n\t"
    "ds_read_b64 v[94:95], v254 offset:0\n\t"
    "ds_read_b64 v[96:97], v187 offset:0\n\t"
    "ds_read_b64 v[98:99], v255 offset:0\n\t"
    "s_add_u32 s89, s89, 0x8000\n\t"
    "buffer_load_dwordx4 v[128:131], v127, %12, s89 offen offset:1024 sc1\n\t"
    "v_lshrrev_b32 v252, 16, v188\n\t"
    "v_and_b32 v188, s92, v188\n\t"
    "v_lshrrev_b32 v253, 16, v189\n\t"
    "v_cvt_pk_bf16_f32 v30, v116, v117\n\t"
    "v_cvt_pk_bf16_f32 v31, v118, v119\n\t"
    "v_and_b32 v119, s67, v37\n\t"
    "v_lshlrev_b32 v118, 16, v37\n\t"
    "v_and_b32 v117, s67, v36\n\t"
    "v_lshlrev_b32 v116, 16, v36\n\t"
    "v_and_b32 v189, s92, v189\n\t"
    "s_waitcnt lgkmcnt(4)\n\t"
    "v_smfmac_f32_16x16x64_bf16 v[116:119], v[120:123], v[84:91], v124\n\t"
    "ds_read_b64 v[84:85], v188 offset:64000\n\t"
    "ds_read_b64 v[86:87], v252 offset:64000\n\t"
    "ds_read_b64 v[88:89], v189 offset:64000\n\t"
    "ds_read_b64 v[90:91], v253 offset:64000\n\t"
    "v_lshrrev_b32 v254, 16, v190\n\t"
    "v_and_b32 v190, s92, v190\n\t"
    "v_lshrrev_b32 v255, 16, v191\n\t"
    "v_and_b32 v191, s92, v191\n\t"
    "s_waitcnt vmcnt(6) lgkmcnt(4)\n\t"
    "v_smfmac_f32_16x16x64_bf16 v[116:119], v[120:123], v[92:99], v124\n\t"
    "ds_read_b64 v[92:93], v190 offset:64000\n\t"
    "ds_read_b64 v[94:95], v254 offset:64000\n\t"
    "ds_read_b64 v[96:97], v191 offset:64000\n\t"
    "ds_read_b64 v[98:99], v255 offset:64000\n\t"
    "s_add_u32 s90, s90, s95\n\t"
    "buffer_load_dwordx4 v[132:135], v127, %12, s90 offen offset:0 sc1\n\t"
    "v_lshrrev_b32 v252, 16, v236\n\t"
    "v_and_b32 v236, s92, v236\n\t"
    "v_lshrrev_b32 v253, 16, v237\n\t"
    "v_and_b32 v237, s92, v237\n\t"
    "s_waitcnt lgkmcnt(4)\n\t"
    "v_smfmac_f32_16x16x64_bf16 v[116:119], v[120:123], v[84:91], v124\n\t"
    "ds_read_b64 v[84:85], v236 offset:0\n\t"
    "ds_read_b64 v[86:87], v252 offset:0\n\t"
    "ds_read_b64 v[88:89], v237 offset:0\n\t"
    "ds_read_b64 v[90:91], v253 offset:0\n\t"
    "v_lshrrev_b32 v254, 16, v238\n\t"
    "v_and_b32 v238, s92, v238\n\t"
    "v_lshrrev_b32 v255, 16, v239\n\t"
    "v_and_b32 v239, s92, v239\n\t"
    "s_waitcnt vmcnt(6) lgkmcnt(4)\n\t"
    "v_smfmac_f32_16x16x64_bf16 v[116:119], v[120:123], v[92:99], v124\n\t"
    "ds_read_b64 v[92:93], v238 offset:0\n\t"
    "ds_read_b64 v[94:95], v254 offset:0\n\t"
    "ds_read_b64 v[96:97], v239 offset:0\n\t"
    "ds_read_b64 v[98:99], v255 offset:0\n\t"
    "s_add_u32 s90, s90, 0x8000\n\t"
    "buffer_load_dwordx4 v[136:139], v127, %12, s90 offen offset:0 sc1\n\t"
    "v_lshrrev_b32 v252, 16, v192\n\t"
    "v_and_b32 v192, s92, v192\n\t"
    "v_lshrrev_b32 v253, 16, v193\n\t"
    "v_cvt_pk_bf16_f32 v36, v116, v117\n\t"
    "v_cvt_pk_bf16_f32 v37, v118, v119\n\t"
    "v_and_b32 v119, s67, v39\n\t"
    "v_lshlrev_b32 v118, 16, v39\n\t"
    "v_and_b32 v117, s67, v38\n\t"
    "v_lshlrev_b32 v116, 16, v38\n\t"
    "v_and_b32 v193, s92, v193\n\t"
    "s_waitcnt vmcnt(4)\n\t"
    "s_mov_b64 exec, 0xffffffff\n\t"
    "buffer_store_dword v165, v125, %12, s96 offen offset:0 sc1\n\t"
    "s_mov_b64 exec, -1\n\t"
    "s_add_u32 s61, s61, 0x2000\n\t"
    "buffer_store_dwordx4 v[20:23], v127, %12, s61 offen sc1\n\t"
    "s_waitcnt lgkmcnt(4)\n\t"
    "v_smfmac_f32_16x16x64_bf16 v[116:119], v[120:123], v[84:91], v124\n\t"
    "ds_read_b64 v[84:85], v192 offset:64000\n\t"
    "ds_read_b64 v[86:87], v252 offset:64000\n\t"
    "ds_read_b64 v[88:89], v193 offset:64000\n\t"
    "ds_read_b64 v[90:91], v253 offset:64000\n\t"
    "v_lshrrev_b32 v254, 16, v194\n\t"
    "v_and_b32 v194, s92, v194\n\t"
    "v_lshrrev_b32 v255, 16, v195\n\t"
    "v_and_b32 v195, s92, v195\n\t"
    "s_waitcnt lgkmcnt(4)\n\t"
    "v_smfmac_f32_16x16x64_bf16 v[116:119], v[120:123], v[92:99], v124\n\t"
    "ds_read_b64 v[92:93], v194 offset:64000\n\t"
    "ds_read_b64 v[94:95], v254 offset:64000\n\t"
    "ds_read_b64 v[96:97], v195 offset:64000\n\t"
    "ds_read_b64 v[98:99], v255 offset:64000\n\t"
    "s_sub_u32 s90, s90, 0x8000\n\t"
    "buffer_load_dwordx4 v[140:143], v127, %12, s90 offen offset:1024 sc1\n\t"
    "v_lshrrev_b32 v252, 16, v196\n\t"
    "v_and_b32 v196, s92, v196\n\t"
    "v_lshrrev_b32 v253, 16, v197\n\t"
    "v_and_b32 v197, s92, v197\n\t"
    "s_waitcnt lgkmcnt(4)\n\t"
    "v_smfmac_f32_16x16x64_bf16 v[116:119], v[120:123], v[84:91], v124\n\t"
    "ds_read_b64 v[84:85], v196 offset:0\n\t"
    "ds_read_b64 v[86:87], v252 offset:0\n\t"
    "ds_read_b64 v[88:89], v197 offset:0\n\t"
    "ds_read_b64 v[90:91], v253 offset:0\n\t"
    "v_lshrrev_b32 v254, 16, v198\n\t"
    "v_and_b32 v198, s92, v198\n\t"
    "v_lshrrev_b32 v255, 16, v199\n\t"
    "v_and_b32 v199, s92, v199\n\t"
    "s_waitcnt lgkmcnt(4)\n\t"
    "v_smfmac_f32_16x16x64_bf16 v[116:119], v[120:123], v[92:99], v124\n\t"
    "ds_read_b64 v[92:93], v198 offset:0\n\t"
    "ds_read_b64 v[94:95], v254 offset:0\n\t"
    "ds_read_b64 v[96:97], v199 offset:0\n\t"
    "ds_read_b64 v[98:99], v255 offset:0\n\t"
    "s_add_u32 s90, s90, 0x8000\n\t"
    "buffer_load_dwordx4 v[144:147], v127, %12, s90 offen offset:1024 sc1\n\t"
    "v_lshrrev_b32 v252, 16, v200\n\t"
    "v_and_b32 v200, s92, v200\n\t"
    "v_lshrrev_b32 v253, 16, v201\n\t"
    "v_cvt_pk_bf16_f32 v38, v116, v117\n\t"
    "v_cvt_pk_bf16_f32 v39, v118, v119\n\t"
    "v_and_b32 v119, s67, v45\n\t"
    "v_lshlrev_b32 v118, 16, v45\n\t"
    "v_and_b32 v117, s67, v44\n\t"
    "v_lshlrev_b32 v116, 16, v44\n\t"
    "v_and_b32 v201, s92, v201\n\t"
    "s_waitcnt lgkmcnt(4)\n\t"
    "v_smfmac_f32_16x16x64_bf16 v[116:119], v[120:123], v[84:91], v124\n\t"
    "ds_read_b64 v[84:85], v200 offset:64000\n\t"
    "ds_read_b64 v[86:87], v252 offset:64000\n\t"
    "ds_read_b64 v[88:89], v201 offset:64000\n\t"
    "ds_read_b64 v[90:91], v253 offset:64000\n\t"
    "v_lshrrev_b32 v254, 16, v202\n\t"
    "v_and_b32 v202, s92, v202\n\t"
    "v_lshrrev_b32 v255, 16, v203\n\t"
    "v_and_b32 v203, s92, v203\n\t"
    "s_waitcnt vmcnt(7) lgkmcnt(4)\n\t"
    "v_smfmac_f32_16x16x64_bf16 v[116:119], v[120:123], v[92:99], v124\n\t"
    "ds_read_b64 v[92:93], v202 offset:64000\n\t"
    "ds_read_b64 v[94:95], v254 offset:64000\n\t"
    "ds_read_b64 v[96:97], v203 offset:64000\n\t"
    "ds_read_b64 v[98:99], v255 offset:64000\n\t"
    "s_add_u32 s91, s91, s95\n\t"
    "buffer_load_dwordx4 v[148:151], v127, %12, s91 offen offset:0 sc1\n\t"
    "v_lshrrev_b32 v252, 16, v204\n\t"
    "v_and_b32 v204, s92, v204\n\t"
    "v_lshrrev_b32 v253, 16, v205\n\t"
    "v_and_b32 v205, s92, v205\n\t"
    "s_waitcnt lgkmcnt(4)\n\t"
    "v_smfmac_f32_16x16x64_bf16 v[116:119], v[120:123], v[84:91], v124\n\t"
    "ds_read_b64 v[84:85], v204 offset:0\n\t"
    "ds_read_b64 v[86:87], v252 offset:0\n\t"
    "ds_read_b64 v[88:89], v205 offset:0\n\t"
    "ds_read_b64 v[90:91], v253 offset:0\n\t"
    "v_lshrrev_b32 v254, 16, v206\n\t"
    "v_and_b32 v206, s92, v206\n\t"
    "v_lshrrev_b32 v255, 16, v207\n\t"
    "v_and_b32 v207, s92, v207\n\t"
    "s_waitcnt vmcnt(7) lgkmcnt(4)\n\t"
    "v_smfmac_f32_16x16x64_bf16 v[116:119], v[120:123], v[92:99], v124\n\t"
    "ds_read_b64 v[92:93], v206 offset:0\n\t"
    "ds_read_b64 v[94:95], v254 offset:0\n\t"
    "ds_read_b64 v[96:97], v207 offset:0\n\t"
    "ds_read_b64 v[98:99], v255 offset:0\n\t"
    "s_add_u32 s91, s91, 0x8000\n\t"
    "buffer_load_dwordx4 v[152:155], v127, %12, s91 offen offset:0 sc1\n\t"
    "v_lshrrev_b32 v252, 16, v128\n\t"
    "v_and_b32 v128, s92, v128\n\t"
    "v_lshrrev_b32 v253, 16, v129\n\t"
    "v_cvt_pk_bf16_f32 v44, v116, v117\n\t"
    "v_cvt_pk_bf16_f32 v45, v118, v119\n\t"
    "v_and_b32 v119, s67, v47\n\t"
    "v_lshlrev_b32 v118, 16, v47\n\t"
    "v_and_b32 v117, s67, v46\n\t"
    "v_lshlrev_b32 v116, 16, v46\n\t"
    "v_and_b32 v129, s92, v129\n\t"
    "s_add_u32 s61, s61, 0x2000\n\t"
    "buffer_store_dwordx4 v[28:31], v127, %12, s61 offen sc1\n\t"
    "s_waitcnt lgkmcnt(4)\n\t"
    "v_smfmac_f32_16x16x64_bf16 v[116:119], v[120:123], v[84:91], v124\n\t"
    "ds_read_b64 v[84:85], v128 offset:64000\n\t"
    "ds_read_b64 v[86:87], v252 offset:64000\n\t"
    "ds_read_b64 v[88:89], v129 offset:64000\n\t"
    "ds_read_b64 v[90:91], v253 offset:64000\n\t"
    "v_lshrrev_b32 v254, 16, v130\n\t"
    "v_and_b32 v130, s92, v130\n\t"
    "v_lshrrev_b32 v255, 16, v131\n\t"
    "v_and_b32 v131, s92, v131\n\t"
    "s_waitcnt vmcnt(8) lgkmcnt(4)\n\t"
    "v_smfmac_f32_16x16x64_bf16 v[116:119], v[120:123], v[92:99], v124\n\t"
    "ds_read_b64 v[92:93], v130 offset:64000\n\t"
    "ds_read_b64 v[94:95], v254 offset:64000\n\t"
    "ds_read_b64 v[96:97], v131 offset:64000\n\t"
    "ds_read_b64 v[98:99], v255 offset:64000\n\t"
    "s_sub_u32 s91, s91, 0x8000\n\t"
    "buffer_load_dwordx4 v[240:243], v127, %12, s91 offen offset:1024 sc1\n\t"
    "v_lshrrev_b32 v252, 16, v132\n\t"
    "v_and_b32 v132, s92, v132\n\t"
    "v_lshrrev_b32 v253, 16, v133\n\t"
    "v_and_b32 v133, s92, v133\n\t"
    "s_waitcnt lgkmcnt(4)\n\t"
    "v_smfmac_f32_16x16x64_bf16 v[116:119], v[120:123], v[84:91], v124\n\t"
    "ds_read_b64 v[84:85], v132 offset:0\n\t"
    "ds_read_b64 v[86:87], v252 offset:0\n\t"
    "ds_read_b64 v[88:89], v133 offset:0\n\t"
    "ds_read_b64 v[90:91], v253 offset:0\n\t"
    "v_lshrrev_b32 v254, 16, v134\n\t"
    "v_and_b32 v134, s92, v134\n\t"
    "v_lshrrev_b32 v255, 16, v135\n\t"
    "v_and_b32 v135, s92, v135\n\t"
    "s_waitcnt vmcnt(8) lgkmcnt(4)\n\t"
    "v_smfmac_f32_16x16x64_bf16 v[116:119], v[120:123], v[92:99], v124\n\t"
    "ds_read_b64 v[92:93], v134 offset:0\n\t"
    "ds_read_b64 v[94:95], v254 offset:0\n\t"
    "ds_read_b64 v[96:97], v135 offset:0\n\t"
    "ds_read_b64 v[98:99], v255 offset:0\n\t"
    "s_add_u32 s91, s91, 0x8000\n\t"
    "buffer_load_dwordx4 v[208:211], v127, %12, s91 offen offset:1024 sc1\n\t"
    "v_lshrrev_b32 v252, 16, v136\n\t"
    "v_and_b32 v136, s92, v136\n\t"
    "v_lshrrev_b32 v253, 16, v137\n\t"
    "v_cvt_pk_bf16_f32 v46, v116, v117\n\t"
    "v_cvt_pk_bf16_f32 v47, v118, v119\n\t"
    "v_and_b32 v119, s67, v53\n\t"
    "v_lshlrev_b32 v118, 16, v53\n\t"
    "v_and_b32 v117, s67, v52\n\t"
    "v_lshlrev_b32 v116, 16, v52\n\t"
    "v_and_b32 v137, s92, v137\n\t"
    "s_waitcnt lgkmcnt(4)\n\t"
    "v_smfmac_f32_16x16x64_bf16 v[116:119], v[120:123], v[84:91], v124\n\t"
    "ds_read_b64 v[84:85], v136 offset:64000\n\t"
    "ds_read_b64 v[86:87], v252 offset:64000\n\t"
    "ds_read_b64 v[88:89], v137 offset:64000\n\t"
    "ds_read_b64 v[90:91], v253 offset:64000\n\t"
    "v_lshrrev_b32 v254, 16, v138\n\t"
    "v_and_b32 v138, s92, v138\n\t"
    "v_lshrrev_b32 v255, 16, v139\n\t"
    "v_and_b32 v139, s92, v139\n\t"
    "s_waitcnt vmcnt(6) lgkmcnt(4)\n\t"
    "v_smfmac_f32_16x16x64_bf16 v[116:119], v[120:123], v[92:99], v124\n\t"
    "ds_read_b64 v[92:93], v138 offset:64000\n\t"
    "ds_read_b64 v[94:95], v254 offset:64000\n\t"
    "ds_read_b64 v[96:97], v139 offset:64000\n\t"
    "ds_read_b64 v[98:99], v255 offset:64000\n\t"
    "s_sub_u32 s84, s84, s95\n\t"
    "buffer_load_dwordx4 v[212:215], v127, %12, s84 offen offset:0 sc1\n\t"
    "v_lshrrev_b32 v252, 16, v140\n\t"
    "v_and_b32 v140, s92, v140\n\t"
    "v_lshrrev_b32 v253, 16, v141\n\t"
    "v_and_b32 v141, s92, v141\n\t"
    "s_waitcnt lgkmcnt(4)\n\t"
    "v_smfmac_f32_16x16x64_bf16 v[116:119], v[120:123], v[84:91], v124\n\t"
    "ds_read_b64 v[84:85], v140 offset:0\n\t"
    "ds_read_b64 v[86:87], v252 offset:0\n\t"
    "ds_read_b64 v[88:89], v141 offset:0\n\t"
    "ds_read_b64 v[90:91], v253 offset:0\n\t"
    "v_lshrrev_b32 v254, 16, v142\n\t"
    "v_and_b32 v142, s92, v142\n\t"
    "v_lshrrev_b32 v255, 16, v143\n\t"
    "v_and_b32 v143, s92, v143\n\t"
    "s_cmp_lt_u32 %16, 4\n\t"
    "s_cbranch_scc1 L_tp195_%=\n\t"
    "s_setprio 0\n\t"
    "L_tp195_%=:\n\t"
    "s_waitcnt vmcnt(6) lgkmcnt(4)\n\t"
    "v_smfmac_f32_16x16x64_bf16 v[116:119], v[120:123], v[92:99], v124\n\t"
    "ds_read_b64 v[92:93], v142 offset:0\n\t"
    "ds_read_b64 v[94:95], v254 offset:0\n\t"
    "ds_read_b64 v[96:97], v143 offset:0\n\t"
    "ds_read_b64 v[98:99], v255 offset:0\n\t"
    "buffer_load_dwordx4 v[216:219], v127, %12, s84 offen offset:1024 sc1\n\t"
    "v_lshrrev_b32 v252, 16, v144\n\t"
    "v_and_b32 v144, s92, v144\n\t"
    "v_lshrrev_b32 v253, 16, v145\n\t"
    "v_cvt_pk_bf16_f32 v52, v116, v117\n\t"
    "v_cvt_pk_bf16_f32 v53, v118, v119\n\t"
    "v_and_b32 v119, s67, v55\n\t"
    "v_lshlrev_b32 v118, 16, v55\n\t"
    "v_and_b32 v117, s67, v54\n\t"
    "v_lshlrev_b32 v116, 16, v54\n\t"
    "v_and_b32 v145, s92, v145\n\t"
    "s_waitcnt vmcnt(4)\n\t"
    "s_mov_b64 exec, 0xffffffff\n\t"
    "buffer_store_dword v165, v125, %12, s96 offen offset:128 sc1\n\t"
    "s_mov_b64 exec, -1\n\t"
    "s_add_u32 s61, s61, 0x2000\n\t"
    "buffer_store_dwordx4 v[36:39], v127, %12, s61 offen sc1\n\t"
    "s_waitcnt lgkmcnt(4)\n\t"
    "v_smfmac_f32_16x16x64_bf16 v[116:119], v[120:123], v[84:91], v124\n\t"
    "ds_read_b64 v[84:85], v144 offset:64000\n\t"
    "ds_read_b64 v[86:87], v252 offset:64000\n\t"
    "ds_read_b64 v[88:89], v145 offset:64000\n\t"
    "ds_read_b64 v[90:91], v253 offset:64000\n\t"
    "v_lshrrev_b32 v254, 16, v146\n\t"
    "v_and_b32 v146, s92, v146\n\t"
    "v_lshrrev_b32 v255, 16, v147\n\t"
    "v_and_b32 v147, s92, v147\n\t"
    "s_waitcnt lgkmcnt(4)\n\t"
    "v_smfmac_f32_16x16x64_bf16 v[116:119], v[120:123], v[92:99], v124\n\t"
    "ds_read_b64 v[92:93], v146 offset:64000\n\t"
    "ds_read_b64 v[94:95], v254 offset:64000\n\t"
    "ds_read_b64 v[96:97], v147 offset:64000\n\t"
    "ds_read_b64 v[98:99], v255 offset:64000\n\t"
    "s_sub_u32 s85, s85, s95\n\t"
    "buffer_load_dwordx4 v[220:223], v127, %12, s85 offen offset:0 sc1\n\t"
    "v_lshrrev_b32 v252, 16, v148\n\t"
    "v_and_b32 v148, s92, v148\n\t"
    "v_lshrrev_b32 v253, 16, v149\n\t"
    "v_and_b32 v149, s92, v149\n\t"
    "s_waitcnt lgkmcnt(4)\n\t"
    "v_smfmac_f32_16x16x64_bf16 v[116:119], v[120:123], v[84:91], v124\n\t"
    "ds_read_b64 v[84:85], v148 offset:0\n\t"
    "ds_read_b64 v[86:87], v252 offset:0\n\t"
    "ds_read_b64 v[88:89], v149 offset:0\n\t"
    "ds_read_b64 v[90:91], v253 offset:0\n\t"
    "v_lshrrev_b32 v254, 16, v150\n\t"
    "v_and_b32 v150, s92, v150\n\t"
    "v_lshrrev_b32 v255, 16, v151\n\t"
    "v_and_b32 v151, s92, v151\n\t"
    "s_waitcnt lgkmcnt(4)\n\t"
    "v_smfmac_f32_16x16x64_bf16 v[116:119], v[120:123], v[92:99], v124\n\t"
    "ds_read_b64 v[92:93], v150 offset:0\n\t"
    "ds_read_b64 v[94:95], v254 offset:0\n\t"
    "ds_read_b64 v[96:97], v151 offset:0\n\t"
    "ds_read_b64 v[98:99], v255 offset:0\n\t"
    "buffer_load_dwordx4 v[244:247], v127, %12, s85 offen offset:1024 sc1\n\t"
    "v_lshrrev_b32 v252, 16, v152\n\t"
    "v_and_b32 v152, s92, v152\n\t"
    "v_lshrrev_b32 v253, 16, v153\n\t"
    "v_cvt_pk_bf16_f32 v54, v116, v117\n\t"
    "v_cvt_pk_bf16_f32 v55, v118, v119\n\t"
    "v_and_b32 v119, s67, v61\n\t"
    "v_lshlrev_b32 v118, 16, v61\n\t"
    "v_and_b32 v117, s67, v60\n\t"
    "v_lshlrev_b32 v116, 16, v60\n\t"
    "v_and_b32 v153, s92, v153\n\t"
    "s_waitcnt lgkmcnt(4)\n\t"
    "v_smfmac_f32_16x16x64_bf16 v[116:119], v[120:123], v[84:91], v124\n\t"
    "ds_read_b64 v[84:85], v152 offset:64000\n\t"
    "ds_read_b64 v[86:87], v252 offset:64000\n\t"
    "ds_read_b64 v[88:89], v153 offset:64000\n\t"
    "ds_read_b64 v[90:91], v253 offset:64000\n\t"
    "v_lshrrev_b32 v254, 16, v154\n\t"
    "v_and_b32 v154, s92, v154\n\t"
    "v_lshrrev_b32 v255, 16, v155\n\t"
    "v_and_b32 v155, s92, v155\n\t"
    "s_waitcnt vmcnt(7) lgkmcnt(4)\n\t"
    "v_smfmac_f32_16x16x64_bf16 v[116:119], v[120:123], v[92:99], v124\n\t"
    "ds_read_b64 v[92:93], v154 offset:64000\n\t"
    "ds_read_b64 v[94:95], v254 offset:64000\n\t"
    "ds_read_b64 v[96:97], v155 offset:64000\n\t"
    "ds_read_b64 v[98:99], v255 offset:64000\n\t"
    "s_sub_u32 s86, s86, s95\n\t"
    "buffer_load_dwordx4 v[224:227], v127, %12, s86 offen offset:0 sc1\n\t"
    "v_lshrrev_b32 v252, 16, v240\n\t"
    "v_and_b32 v240, s92, v240\n\t"
    "v_lshrrev_b32 v253, 16, v241\n\t"
    "v_and_b32 v241, s92, v241\n\t"
    "s_waitcnt lgkmcnt(4)\n\t"
    "v_smfmac_f32_16x16x64_bf16 v[116:119], v[120:123], v[84:91], v124\n\t"
    "ds_read_b64 v[84:85], v240 offset:0\n\t"
    "ds_read_b64 v[86:87], v252 offset:0\n\t"
    "ds_read_b64 v[88:89], v241 offset:0\n\t"
    "ds_read_b64 v[90:91], v253 offset:0\n\t"
    "v_lshrrev_b32 v254, 16, v242\n\t"
    "v_and_b32 v242, s92, v242\n\t"
    "v_lshrrev_b32 v255, 16, v243\n\t"
    "v_and_b32 v243, s92, v243\n\t"
    "s_waitcnt vmcnt(7) lgkmcnt(4)\n\t"
    "v_smfmac_f32_16x16x64_bf16 v[116:119], v[120:123], v[92:99], v124\n\t"
    "ds_read_b64 v[92:93], v242 offset:0\n\t"
    "ds_read_b64 v[94:95], v254 offset:0\n\t"
    "ds_read_b64 v[96:97], v243 offset:0\n\t"
    "ds_read_b64 v[98:99], v255 offset:0\n\t"
    "s_add_u32 s84, s84, 0x8000\n\t"
    "buffer_load_dwordx4 v[228:231], v127, %12, s84 offen offset:0 sc1\n\t"
    "v_lshrrev_b32 v252, 16, v208\n\t"
    "v_and_b32 v208, s92, v208\n\t"
    "v_lshrrev_b32 v253, 16, v209\n\t"
    "v_cvt_pk_bf16_f32 v60, v116, v117\n\t"
    "v_cvt_pk_bf16_f32 v61, v118, v119\n\t"
    "v_and_b32 v119, s67, v63\n\t"
    "v_lshlrev_b32 v118, 16, v63\n\t"
    "v_and_b32 v117, s67, v62\n\t"
    "v_lshlrev_b32 v116, 16, v62\n\t"
    "v_and_b32 v209, s92, v209\n\t"
    "s_add_u32 s61, s61, 0x2000\n\t"
    "buffer_store_dwordx4 v[44:47], v127, %12, s61 offen sc1\n\t"
    "s_waitcnt lgkmcnt(4)\n\t"
    "v_smfmac_f32_16x16x64_bf16 v[116:119], v[120:123], v[84:91], v124\n\t"
    "ds_read_b64 v[84:85], v208 offset:64000\n\t"
    "ds_read_b64 v[86:87], v252 offset:64000\n\t"
    "ds_read_b64 v[88:89], v209 offset:64000\n\t"
    "ds_read_b64 v[90:91], v253 offset:64000\n\t"
    "v_lshrrev_b32 v254, 16, v210\n\t"
    "v_and_b32 v210, s92, v210\n\t"
    "v_lshrrev_b32 v255, 16, v211\n\t"
    "v_and_b32 v211, s92, v211\n\t"
    "s_mov_b64 exec, 1\n\t"
    "buffer_load_dword v164, off, %12, s93 offset:0 sc1\n\t"
    "s_mov_b64 exec, -1\n\t"
    "s_waitcnt vmcnt(9) lgkmcnt(4)\n\t"
    "v_smfmac_f32_16x16x64_bf16 v[116:119], v[120:123], v[92:99], v124\n\t"
    "ds_read_b64 v[92:93], v210 offset:64000\n\t"
    "ds_read_b64 v[94:95], v254 offset:64000\n\t"
    "ds_read_b64 v[96:97], v211 offset:64000\n\t"
    "ds_read_b64 v[98:99], v255 offset:64000\n\t"
    "buffer_load_dwordx4 v[232:235], v127, %12, s86 offen offset:1024 sc1\n\t"
    "v_lshrrev_b32 v252, 16, v212\n\t"
    "v_and_b32 v212, s92, v212\n\t"
    "v_lshrrev_b32 v253, 16, v213\n\t"
    "v_and_b32 v213, s92, v213\n\t"
    "s_waitcnt lgkmcnt(4)\n\t"
    "v_smfmac_f32_16x16x64_bf16 v[116:119], v[120:123], v[84:91], v124\n\t"
    "ds_read_b64 v[84:85], v212 offset:0\n\t"
    "ds_read_b64 v[86:87], v252 offset:0\n\t"
    "ds_read_b64 v[88:89], v213 offset:0\n\t"
    "ds_read_b64 v[90:91], v253 offset:0\n\t"
    "v_lshrrev_b32 v254, 16, v214\n\t"
    "v_and_b32 v214, s92, v214\n\t"
    "v_lshrrev_b32 v255, 16, v215\n\t"
    "v_and_b32 v215, s92, v215\n\t"
    "s_cmp_lt_u32 %16, 4\n\t"
    "s_cbranch_scc1 L_tp207_%=\n\t"
    "s_setprio 1\n\t"
    "L_tp207_%=:\n\t"
    "s_waitcnt vmcnt(9) lgkmcnt(4)\n\t"
    "v_smfmac_f32_16x16x64_bf16 v[116:119], v[120:123], v[92:99], v124\n\t"
    "ds_read_b64 v[92:93], v214 offset:0\n\t"
    "ds_read_b64 v[94:95], v254 offset:0\n\t"
    "ds_read_b64 v[96:97], v215 offset:0\n\t"
    "ds_read_b64 v[98:99], v255 offset:0\n\t"
    "buffer_load_dwordx4 v[248:251], v127, %12, s84 offen offset:1024 sc1\n\t"
    "v_lshrrev_b32 v252, 16, v216\n\t"
    "v_and_b32 v216, s92, v216\n\t"
    "v_lshrrev_b32 v253, 16, v217\n\t"
    "v_cvt_pk_bf16_f32 v62, v116, v117\n\t"
    "v_cvt_pk_bf16_f32 v63, v118, v119\n\t"
    "v_mov_b64 v[172:173], s[70:71]\n\t"
    "v_mov_b64 v[174:175], s[72:73]\n\t"
    "v_and_b32 v217, s92, v217\n\t"
    "s_waitcnt lgkmcnt(4)\n\t"
    "v_smfmac_f32_16x16x64_bf16 v[172:175], v[120:123], v[84:91], v124\n\t"
    "ds_read_b64 v[84:85], v216 offset:0\n\t"
    "ds_read_b64 v[86:87], v252 offset:0\n\t"
    "ds_read_b64 v[88:89], v217 offset:0\n\t"
    "ds_read_b64 v[90:91], v253 offset:0\n\t"
    "v_lshrrev_b32 v254, 16, v218\n\t"
    "v_and_b32 v218, s92, v218\n\t"
    "v_lshrrev_b32 v255, 16, v219\n\t"
    "v_and_b32 v219, s92, v219\n\t"
    "s_waitcnt vmcnt(7) lgkmcnt(4)\n\t"
    "v_smfmac_f32_16x16x64_bf16 v[172:175], v[120:123], v[92:99], v124\n\t"
    "ds_read_b64 v[92:93], v218 offset:0\n\t"
    "ds_read_b64 v[94:95], v254 offset:0\n\t"
    "ds_read_b64 v[96:97], v219 offset:0\n\t"
    "ds_read_b64 v[98:99], v255 offset:0\n\t"
    "s_sub_u32 s87, s87, s95\n\t"
    "buffer_load_dwordx4 v[168:171], v127, %12, s87 offen offset:0 sc1\n\t"
    "v_lshrrev_b32 v252, 16, v220\n\t"
    "v_and_b32 v220, s92, v220\n\t"
    "v_lshrrev_b32 v253, 16, v221\n\t"
    "v_mov_b64 v[176:177], s[70:71]\n\t"
    "v_mov_b64 v[178:179], s[72:73]\n\t"
    "v_and_b32 v221, s92, v221\n\t"
    "s_waitcnt lgkmcnt(4)\n\t"
    "v_smfmac_f32_16x16x64_bf16 v[176:179], v[120:123], v[84:91], v124\n\t"
    "ds_read_b64 v[84:85], v220 offset:0\n\t"
    "ds_read_b64 v[86:87], v252 offset:0\n\t"
    "ds_read_b64 v[88:89], v221 offset:0\n\t"
    "ds_read_b64 v[90:91], v253 offset:0\n\t"
    "v_lshrrev_b32 v254, 16, v222\n\t"
    "v_and_b32 v222, s92, v222\n\t"
    "v_lshrrev_b32 v255, 16, v223\n\t"
    "v_and_b32 v223, s92, v223\n\t"
    "s_waitcnt vmcnt(3)\n\t"
    "v_readfirstlane_b32 s65, v164\n\t"
    "s_nop 0\n\t"
    "s_cmp_eq_u32 s65, 0\n\t"
    "s_cbranch_scc1 L_xw0_%=\n\t"
    "L_xwb0_%=:\n\t"
    "buffer_load_dwordx4 v[4:7], v127, %12, s94 offen sc1\n\t"
    "s_waitcnt lgkmcnt(4)\n\t"
    "v_smfmac_f32_16x16x64_bf16 v[176:179], v[120:123], v[92:99], v124\n\t"
    "ds_read_b64 v[92:93], v222 offset:0\n\t"
    "ds_read_b64 v[94:95], v254 offset:0\n\t"
    "ds_read_b64 v[96:97], v223 offset:0\n\t"
    "ds_read_b64 v[98:99], v255 offset:0\n\t"
    "s_add_u32 s85, s85, 0x8000\n\t"
    "buffer_load_dwordx4 v[112:115], v127, %12, s85 offen offset:0 sc1\n\t"
    "v_lshrrev_b32 v252, 16, v244\n\t"
    "v_and_b32 v244, s92, v244\n\t"
    "v_lshrrev_b32 v253, 16, v245\n\t"
    "v_mov_b64 v[180:181], s[70:71]\n\t"
    "v_mov_b64 v[182:183], s[72:73]\n\t"
    "v_and_b32 v245, s92, v245\n\t"
    "s_mov_b64 exec, 0xffffffff\n\t"
    "buffer_store_dword v165, v125, %12, s96 offen offset:256 sc1\n\t"
    "s_mov_b64 exec, -1\n\t"
    "s_add_u32 s61, s61, 0x2000\n\t"
    "buffer_store_dwordx4 v[52:55], v127, %12, s61 offen sc1\n\t"
    "s_waitcnt lgkmcnt(4)\n\t"
    "v_smfmac_f32_16x16x64_bf16 v[180:183], v[120:123], v[84:91], v124\n\t"
    "ds_read_b64 v[84:85], v244 offset:0\n\t"
    "ds_read_b64 v[86:87], v252 offset:0\n\t"
    "ds_read_b64 v[88:89], v245 offset:0\n\t"
    "ds_read_b64 v[90:91], v253 offset:0\n\t"
    "v_lshrrev_b32 v254, 16, v246\n\t"
    "v_and_b32 v246, s92, v246\n\t"
    "v_lshrrev_b32 v255, 16, v247\n\t"
    "v_and_b32 v247, s92, v247\n\t"
    "s_waitcnt lgkmcnt(4)\n\t"
    "v_smfmac_f32_16x16x64_bf16 v[180:183], v[120:123], v[92:99], v124\n\t"
    "ds_read_b64 v[92:93], v246 offset:0\n\t"
    "ds_read_b64 v[94:95], v254 offset:0\n\t"
    "ds_read_b64 v[96:97], v247 offset:0\n\t"
    "ds_read_b64 v[98:99], v255 offset:0\n\t"
    "buffer_load_dwordx4 v[100:103], v127, %12, s87 offen offset:1024 sc1\n\t"
    "v_lshrrev_b32 v252, 16, v224\n\t"
    "v_and_b32 v224, s92, v224\n\t"
    "v_lshrrev_b32 v253, 16, v225\n\t"
    "v_mov_b64 v[184:185], s[70:71]\n\t"
    "v_mov_b64 v[186:187], s[72:73]\n\t"
    "v_and_b32 v225, s92, v225\n\t"
    "s_waitcnt lgkmcnt(4)\n\t"
    "v_smfmac_f32_16x16x64_bf16 v[184:187], v[120:123], v[84:91], v124\n\t"
    "ds_read_b64 v[84:85], v224 offset:0\n\t"
    "ds_read_b64 v[86:87], v252 offset:0\n\t"
    "ds_read_b64 v[88:89], v225 offset:0\n\t"
    "ds_read_b64 v[90:91], v253 offset:0\n\t"
    "v_lshrrev_b32 v254, 16, v226\n\t"
    "v_and_b32 v226, s92, v226\n\t"
    "v_lshrrev_b32 v255, 16, v227\n\t"
    "v_and_b32 v227, s92, v227\n\t"
    "s_add_u32 s94, s94, 0x2000\n\t"
    "buffer_load_dwordx4 v[12:15], v127, %12, s94 offen sc1\n\t"
    "s_waitcnt lgkmcnt(4)\n\t"
    "v_smfmac_f32_16x16x64_bf16 v[184:187], v[120:123], v[92:99], v124\n\t"
    "ds_read_b64 v[92:93], v226 offset:0\n\t"
    "ds_read_b64 v[94:95], v254 offset:0\n\t"
    "ds_read_b64 v[96:97], v227 offset:0\n\t"
    "ds_read_b64 v[98:99], v255 offset:0\n\t"
    "buffer_load_dwordx4 v[104:107], v127, %12, s85 offen offset:1024 sc1\n\t"
    "v_lshrrev_b32 v252, 16, v228\n\t"
    "v_and_b32 v228, s92, v228\n\t"
    "v_lshrrev_b32 v253, 16, v229\n\t"
    "v_mov_b64 v[188:189], s[70:71]\n\t"
    "v_mov_b64 v[190:191], s[72:73]\n\t"
    "v_and_b32 v229, s92, v229\n\t"
    "s_waitcnt lgkmcnt(4)\n\t"
    "v_smfmac_f32_16x16x64_bf16 v[188:191], v[120:123], v[84:91], v124\n\t"
    "ds_read_b64 v[84:85], v228 offset:64000\n\t"
    "ds_read_b64 v[86:87], v252 offset:64000\n\t"
    "ds_read_b64 v[88:89], v229 offset:64000\n\t"
    "ds_read_b64 v[90:91], v253 offset:64000\n\t"
    "v_lshrrev_b32 v254, 16, v230\n\t"
    "v_and_b32 v230, s92, v230\n\t"
    "v_lshrrev_b32 v255, 16, v231\n\t"
    "v_and_b32 v231, s92, v231\n\t"
    "s_waitcnt vmcnt(9) lgkmcnt(4)\n\t"
    "v_smfmac_f32_16x16x64_bf16 v[188:191], v[120:123], v[92:99], v124\n\t"
    "ds_read_b64 v[92:93], v230 offset:64000\n\t"
    "ds_read_b64 v[94:95], v254 offset:64000\n\t"
    "ds_read_b64 v[96:97], v231 offset:64000\n\t"
    "ds_read_b64 v[98:99], v255 offset:64000\n\t"
    "s_sub_u32 s88, s88, s95\n\t"
    "buffer_load_dwordx4 v[108:111], v127, %12, s88 offen offset:0 sc1\n\t"
    "v_lshrrev_b32 v252, 16, v232\n\t"
    "v_and_b32 v232, s92, v232\n\t"
    "v_lshrrev_b32 v253, 16, v233\n\t"
    "v_and_b32 v233, s92, v233\n\t"
    "s_waitcnt lgkmcnt(4)\n\t"
    "v_smfmac_f32_16x16x64_bf16 v[172:175], v[120:123], v[84:91], v124\n\t"
    "ds_read_b64 v[84:85], v232 offset:0\n\t"
    "ds_read_b64 v[86:87], v252 offset:0\n\t"
    "ds_read_b64 v[88:89], v233 offset:0\n\t"
    "ds_read_b64 v[90:91], v253 offset:0\n\t"
    "v_lshrrev_b32 v254, 16, v234\n\t"
    "v_and_b32 v234, s92, v234\n\t"
    "v_lshrrev_b32 v255, 16, v235\n\t"
    "v_and_b32 v235, s92, v235\n\t"
    "s_waitcnt vmcnt(7)\n\t"
    "v_smfmac_f32_16x16x64_bf16 v[172:175], v[156:159], v[0:7], v124\n\t"
    "s_waitcnt lgkmcnt(4)\n\t"
    "v_smfmac_f32_16x16x64_bf16 v[172:175], v[120:123], v[92:99], v124\n\t"
    "ds_read_b64 v[92:93], v234 offset:0\n\t"
    "ds_read_b64 v[94:95], v254 offset:0\n\t"
    "ds_read_b64 v[96:97], v235 offset:0\n\t"
    "ds_read_b64 v[98:99], v255 offset:0\n\t"
    "s_add_u32 s86, s86, 0x8000\n\t"
    "buffer_load_dwordx4 v[128:131], v127, %12, s86 offen offset:0 sc1\n\t"
    "v_lshrrev_b32 v252, 16, v248\n\t"
    "v_and_b32 v248, s92, v248\n\t"
    "v_lshrrev_b32 v253, 16, v249\n\t"
    "v_mov_b64 v[192:193], s[70:71]\n\t"
    "v_mov_b64 v[194:195], s[72:73]\n\t"
    "v_and_b32 v249, s92, v249\n\t"
    "s_add_u32 s61, s61, 0x2000\n\t"
    "buffer_store_dwordx4 v[60:63], v127, %12, s61 offen sc1\n\t"
    "v_smfmac_f32_16x16x64_bf16 v[176:179], v[160:163], v[0:7], v124\n\t"
    "v_pk_mul_f32 v[64:65], v[172:173], v[172:173]\n\t"
    "v_pk_mul_f32 v[66:67], v[174:175], v[174:175]\n\t"
    "v_pk_fma_f32 v[64:65], v[64:65], v[82:83], v[80:81]\n\t"
    "v_pk_fma_f32 v[66:67], v[66:67], v[82:83], v[80:81]\n\t"
    "v_pk_mul_f32 v[64:65], v[64:65], v[172:173]\n\t"
    "s_waitcnt lgkmcnt(4)\n\t"
    "v_smfmac_f32_16x16x64_bf16 v[192:195], v[120:123], v[84:91], v124\n\t"
    "ds_read_b64 v[84:85], v248 offset:64000\n\t"
    "ds_read_b64 v[86:87], v252 offset:64000\n\t"
    "ds_read_b64 v[88:89], v249 offset:64000\n\t"
    "ds_read_b64 v[90:91], v253 offset:64000\n\t"
    "v_lshrrev_b32 v254, 16, v250\n\t"
    "v_and_b32 v250, s92, v250\n\t"
    "v_lshrrev_b32 v255, 16, v251\n\t"
    "v_and_b32 v251, s92, v251\n\t"
    "s_mov_b64 exec, 1\n\t"
    "buffer_load_dword v164, off, %12, s93 offset:128 sc1\n\t"
    "s_mov_b64 exec, -1\n\t"
    "v_pk_mul_f32 v[66:67], v[66:67], v[174:175]\n\t"
    "v_exp_f32 v68, v64\n\t"
    "v_exp_f32 v69, v65\n\t"
    "v_exp_f32 v70, v66\n\t"
    "v_exp_f32 v71, v67\n\t"
    "s_waitcnt lgkmcnt(4)\n\t"
    "v_smfmac_f32_16x16x64_bf16 v[192:195], v[120:123], v[92:99], v124\n\t"
    "ds_read_b64 v[92:93], v250 offset:64000\n\t"
    "ds_read_b64 v[94:95], v254 offset:64000\n\t"
    "ds_read_b64 v[96:97], v251 offset:64000\n\t"
    "ds_read_b64 v[98:99], v255 offset:64000\n\t"
    "buffer_load_dwordx4 v[212:215], v127, %12, s88 offen offset:1024 sc1\n\t"
    "v_lshrrev_b32 v252, 16, v168\n\t"
    "v_and_b32 v168, s92, v168\n\t"
    "v_lshrrev_b32 v253, 16, v169\n\t"
    "v_and_b32 v169, s92, v169\n\t"
    "v_pk_fma_f32 v[68:69], v[68:69], s[40:41], s[40:41]\n\t"
    "v_pk_fma_f32 v[70:71], v[70:71], s[42:43], s[42:43]\n\t"
    "v_rcp_f32 v68, v68\n\t"
    "v_rcp_f32 v69, v69\n\t"
    "v_rcp_f32 v70, v70\n\t"
    "s_waitcnt lgkmcnt(4)\n\t"
    "v_smfmac_f32_16x16x64_bf16 v[176:179], v[120:123], v[84:91], v124\n\t"
    "ds_read_b64 v[84:85], v168 offset:0\n\t"
    "ds_read_b64 v[86:87], v252 offset:0\n\t"
    "ds_read_b64 v[88:89], v169 offset:0\n\t"
    "ds_read_b64 v[90:91], v253 offset:0\n\t"
    "v_lshrrev_b32 v254, 16, v170\n\t"
    "v_and_b32 v170, s92, v170\n\t"
    "v_lshrrev_b32 v255, 16, v171\n\t"
    "v_and_b32 v171, s92, v171\n\t"
    "s_waitcnt vmcnt(6)\n\t"
    "v_smfmac_f32_16x16x64_bf16 v[180:183], v[156:159], v[8:15], v124\n\t"
    "v_rcp_f32 v71, v71\n\t"
    "v_pk_mul_f32 v[68:69], v[68:69], v[172:173]\n\t"
    "v_pk_fma_f32 v[68:69], v[70:71], v[174:175], v[68:69]\n\t"
    "v_add_f32 v236, v68, v69\n\t"
    "s_waitcnt lgkmcnt(4)\n\t"
    "v_smfmac_f32_16x16x64_bf16 v[176:179], v[120:123], v[92:99], v124\n\t"
    "ds_read_b64 v[92:93], v170 offset:0\n\t"
    "ds_read_b64 v[94:95], v254 offset:0\n\t"
    "ds_read_b64 v[96:97], v171 offset:0\n\t"
    "ds_read_b64 v[98:99], v255 offset:0\n\t"
    "buffer_load_dwordx4 v[116:119], v127, %12, s86 offen offset:1024 sc1\n\t"
    "v_lshrrev_b32 v252, 16, v112\n\t"
    "v_and_b32 v112, s92, v112\n\t"
    "v_lshrrev_b32 v253, 16, v113\n\t"
    "v_mov_b64 v[196:197], s[70:71]\n\t"
    "v_mov_b64 v[198:199], s[72:73]\n\t"
    "v_and_b32 v113, s92, v113\n\t"
    "v_smfmac_f32_16x16x64_bf16 v[184:187], v[160:163], v[8:15], v124\n\t"
    "v_pk_mul_f32 v[72:73], v[176:177], v[176:177]\n\t"
    "v_pk_mul_f32 v[74:75], v[178:179], v[178:179]\n\t"
    "v_pk_fma_f32 v[72:73], v[72:73], v[82:83], v[80:81]\n\t"
    "v_pk_fma_f32 v[74:75], v[74:75], v[82:83], v[80:81]\n\t"
    "v_pk_mul_f32 v[72:73], v[72:73], v[176:177]\n\t"
    "s_waitcnt lgkmcnt(4)\n\t"
    "v_smfmac_f32_16x16x64_bf16 v[196:199], v[120:123], v[84:91], v124\n\t"
    "ds_read_b64 v[84:85], v112 offset:64000\n\t"
    "ds_read_b64 v[86:87], v252 offset:64000\n\t"
    "ds_read_b64 v[88:89], v113 offset:64000\n\t"
    "ds_read_b64 v[90:91], v253 offset:64000\n\t"
    "v_lshrrev_b32 v254, 16, v114\n\t"
    "v_and_b32 v114, s92, v114\n\t"
    "v_lshrrev_b32 v255, 16, v115\n\t"
    "v_and_b32 v115, s92, v115\n\t"
    "v_pk_mul_f32 v[74:75], v[74:75], v[178:179]\n\t"
    "v_exp_f32 v76, v72\n\t"
    "v_exp_f32 v77, v73\n\t"
    "v_exp_f32 v78, v74\n\t"
    "v_exp_f32 v79, v75\n\t"
    "s_waitcnt lgkmcnt(4)\n\t"
    "v_smfmac_f32_16x16x64_bf16 v[196:199], v[120:123], v[92:99], v124\n\t"
    "ds_read_b64 v[92:93], v114 offset:64000\n\t"
    "ds_read_b64 v[94:95], v254 offset:64000\n\t"
    "ds_read_b64 v[96:97], v115 offset:64000\n\t"
    "ds_read_b64 v[98:99], v255 offset:64000\n\t"
    "s_sub_u32 s89, s89, s95\n\t"
    "buffer_load_dwordx4 v[216:219], v127, %12, s89 offen offset:0 sc1\n\t"
    "v_lshrrev_b32 v252, 16, v100\n\t"
    "v_and_b32 v100, s92, v100\n\t"
    "v_lshrrev_b32 v253, 16, v101\n\t"
    "v_and_b32 v101, s92, v101\n\t"
    "v_pk_fma_f32 v[76:77], v[76:77], s[40:41], s[40:41]\n\t"
    "v_pk_fma_f32 v[78:79], v[78:79], s[42:43], s[42:43]\n\t"
    "v_rcp_f32 v76, v76\n\t"
    "v_rcp_f32 v77, v77\n\t"
    "v_rcp_f32 v78, v78\n\t"
    "s_waitcnt lgkmcnt(4)\n\t"
    "v_smfmac_f32_16x16x64_bf16 v[180:183], v[120:123], v[84:91], v124\n\t"
    "ds_read_b64 v[84:85], v100 offset:0\n\t"
    "ds_read_b64 v[86:87], v252 offset:0\n\t"
    "ds_read_b64 v[88:89], v101 offset:0\n\t"
    "ds_read_b64 v[90:91], v253 offset:0\n\t"
    "v_lshrrev_b32 v254, 16, v102\n\t"
    "v_and_b32 v102, s92, v102\n\t"
    "v_lshrrev_b32 v255, 16, v103\n\t"
    "v_and_b32 v103, s92, v103\n\t"
    "s_waitcnt vmcnt(3)\n\t"
    "v_readfirstlane_b32 s65, v164\n\t"
    "s_nop 0\n\t"
    "s_cmp_eq_u32 s65, 0\n\t"
    "s_cbranch_scc1 L_xw1_%=\n\t"
    "L_xwb1_%=:\n\t"
    "s_add_u32 s94, s94, 0x2000\n\t"
    "buffer_load_dwordx4 v[20:23], v127, %12, s94 offen sc1\n\t"
    "v_rcp_f32 v79, v79\n\t"
    "v_pk_mul_f32 v[76:77], v[76:77], v[176:177]\n\t"
    "v_pk_fma_f32 v[76:77], v[78:79], v[178:179], v[76:77]\n\t"
    "v_add_f32 v237, v76, v77\n\t"
    "s_cmp_lt_u32 %16, 4\n\t"
    "s_cbranch_scc1 L_tp227_%=\n\t"
    "s_setprio 0\n\t"
    "L_tp227_%=:\n\t"
    "s_waitcnt lgkmcnt(4)\n\t"
    "v_smfmac_f32_16x16x64_bf16 v[180:183], v[120:123], v[92:99], v124\n\t"
    "ds_read_b64 v[92:93], v102 offset:0\n\t"
    "ds_read_b64 v[94:95], v254 offset:0\n\t"
    "ds_read_b64 v[96:97], v103 offset:0\n\t"
    "ds_read_b64 v[98:99], v255 offset:0\n\t"
    "s_add_u32 s87, s87, 0x8000\n\t"
    "buffer_load_dwordx4 v[220:223], v127, %12, s87 offen offset:0 sc1\n\t"
    "v_lshrrev_b32 v252, 16, v104\n\t"
    "v_and_b32 v104, s92, v104\n\t"
    "v_lshrrev_b32 v253, 16, v105\n\t"
    "v_mov_b64 v[200:201], s[70:71]\n\t"
    "v_mov_b64 v[202:203], s[72:73]\n\t"
    "v_and_b32 v105, s92, v105\n\t"
    "s_mov_b64 exec, 0xffffffff\n\t"
    "buffer_store_dword v165, v125, %12, s96 offen offset:384 sc1\n\t"
    "s_mov_b64 exec, -1\n\t"
    "v_pk_mul_f32 v[64:65], v[180:181], v[180:181]\n\t"
    "v_pk_mul_f32 v[66:67], v[182:183], v[182:183]\n\t"
    "v_pk_fma_f32 v[64:65], v[64:65], v[82:83], v[80:81]\n\t"
    "v_pk_fma_f32 v[66:67], v[66:67], v[82:83], v[80:81]\n\t"
    "v_pk_mul_f32 v[64:65], v[64:65], v[180:181]\n\t"
    "s_waitcnt lgkmcnt(4)\n\t"
    "v_smfmac_f32_16x16x64_bf16 v[200:203], v[120:123], v[84:91], v124\n\t"
    "ds_read_b64 v[84:85], v104 offset:64000\n\t"
    "ds_read_b64 v[86:87], v252 offset:64000\n\t"
    "ds_read_b64 v[88:89], v105 offset:64000\n\t"
    "ds_read_b64 v[90:91], v253 offset:64000\n\t"
    "v_lshrrev_b32 v254, 16, v106\n\t"
    "v_and_b32 v106, s92, v106\n\t"
    "v_lshrrev_b32 v255, 16, v107\n\t"
    "v_and_b32 v107, s92, v107\n\t"
    "v_pk_mul_f32 v[66:67], v[66:67], v[182:183]\n\t"
    "v_exp_f32 v68, v64\n\t"
    "v_exp_f32 v69, v65\n\t"
    "v_exp_f32 v70, v66\n\t"
    "v_exp_f32 v71, v67\n\t"
    "s_waitcnt lgkmcnt(4)\n\t"
    "v_smfmac_f32_16x16x64_bf16 v[200:203], v[120:123], v[92:99], v124\n\t"
    "ds_read_b64 v[92:93], v106 offset:64000\n\t"
    "ds_read_b64 v[94:95], v254 offset:64000\n\t"
    "ds_read_b64 v[96:97], v107 offset:64000\n\t"
    "ds_read_b64 v[98:99], v255 offset:64000\n\t"
    "buffer_load_dwordx4 v[244:247], v127, %12, s89 offen offset:1024 sc1\n\t"
    "v_lshrrev_b32 v252, 16, v108\n\t"
    "v_and_b32 v108, s92, v108\n\t"
    "v_lshrrev_b32 v253, 16, v109\n\t"
    "v_and_b32 v109, s92, v109\n\t"
    "v_pk_fma_f32 v[68:69], v[68:69], s[40:41], s[40:41]\n\t"
    "v_pk_fma_f32 v[70:71], v[70:71], s[42:43], s[42:43]\n\t"
    "v_rcp_f32 v68, v68\n\t"
    "v_rcp_f32 v69, v69\n\t"
    "v_rcp_f32 v70, v70\n\t"
    "s_waitcnt lgkmcnt(4)\n\t"
    "v_smfmac_f32_16x16x64_bf16 v[184:187], v[120:123], v[84:91], v124\n\t"
    "ds_read_b64 v[84:85], v108 offset:0\n\t"
    "ds_read_b64 v[86:87], v252 offset:0\n\t"
    "ds_read_b64 v[88:89], v109 offset:0\n\t"
    "ds_read_b64 v[90:91], v253 offset:0\n\t"
    "v_lshrrev_b32 v254, 16, v110\n\t"
    "v_and_b32 v110, s92, v110\n\t"
    "v_lshrrev_b32 v255, 16, v111\n\t"
    "v_and_b32 v111, s92, v111\n\t"
    "s_add_u32 s94, s94, 0x2000\n\t"
    "buffer_load_dwordx4 v[28:31], v127, %12, s94 offen sc1\n\t"
    "v_rcp_f32 v71, v71\n\t"
    "v_pk_mul_f32 v[68:69], v[68:69], v[180:181]\n\t"
    "v_pk_fma_f32 v[68:69], v[70:71], v[182:183], v[68:69]\n\t"
    "v_add_f32 v238, v68, v69\n\t"
    "s_waitcnt lgkmcnt(4)\n\t"
    "v_smfmac_f32_16x16x64_bf16 v[184:187], v[120:123], v[92:99], v124\n\t"
    "ds_read_b64 v[92:93], v110 offset:0\n\t"
    "ds_read_b64 v[94:95], v254 offset:0\n\t"
    "ds_read_b64 v[96:97], v111 offset:0\n\t"
    "ds_read_b64 v[98:99], v255 offset:0\n\t"
    "buffer_load_dwordx4 v[224:227], v127, %12, s87 offen offset:1024 sc1\n\t"
    "v_lshrrev_b32 v252, 16, v128\n\t"
    "v_and_b32 v128, s92, v128\n\t"
    "v_lshrrev_b32 v253, 16, v129\n\t"
    "v_mov_b64 v[204:205], s[70:71]\n\t"
    "v_mov_b64 v[206:207], s[72:73]\n\t"
    "v_and_b32 v129, s92, v129\n\t"
    "v_pk_mul_f32 v[72:73], v[184:185], v[184:185]\n\t"
    "v_pk_mul_f32 v[74:75], v[186:187], v[186:187]\n\t"
    "v_pk_fma_f32 v[72:73], v[72:73], v[82:83], v[80:81]\n\t"
    "v_pk_fma_f32 v[74:75], v[74:75], v[82:83], v[80:81]\n\t"
    "v_pk_mul_f32 v[72:73], v[72:73], v[184:185]\n\t"
    "s_waitcnt lgkmcnt(4)\n\t"
    "v_smfmac_f32_16x16x64_bf16 v[204:207], v[120:123], v[84:91], v124\n\t"
    "ds_read_b64 v[84:85], v128 offset:64000\n\t"
    "ds_read_b64 v[86:87], v252 offset:64000\n\t"
    "ds_read_b64 v[88:89], v129 offset:64000\n\t"
    "ds_read_b64 v[90:91], v253 offset:64000\n\t"
    "v_lshrrev_b32 v254, 16, v130\n\t"
    "v_and_b32 v130, s92, v130\n\t"
    "v_lshrrev_b32 v255, 16, v131\n\t"
    "v_and_b32 v131, s92, v131\n\t"
    "v_pk_mul_f32 v[74:75], v[74:75], v[186:187]\n\t"
    "v_exp_f32 v76, v72\n\t"
    "v_exp_f32 v77, v73\n\t"
    "v_exp_f32 v78, v74\n\t"
    "v_exp_f32 v79, v75\n\t"
    "s_waitcnt vmcnt(8) lgkmcnt(4)\n\t"
    "v_smfmac_f32_16x16x64_bf16 v[204:207], v[120:123], v[92:99], v124\n\t"
    "ds_read_b64 v[92:93], v130 offset:64000\n\t"
    "ds_read_b64 v[94:95], v254 offset:64000\n\t"
    "ds_read_b64 v[96:97], v131 offset:64000\n\t"
    "ds_read_b64 v[98:99], v255 offset:64000\n\t"
    "s_sub_u32 s90, s90, s95\n\t"
    "buffer_load_dwordx4 v[228:231], v127, %12, s90 offen offset:0 sc1\n\t"
    "v_lshrrev_b32 v252, 16, v212\n\t"
    "v_and_b32 v212, s92, v212\n\t"
    "v_lshrrev_b32 v253, 16, v213\n\t"
    "v_and_b32 v213, s92, v213\n\t"
    "v_pk_fma_f32 v[76:77], v[76:77], s[40:41], s[40:41]\n\t"
    "v_pk_fma_f32 v[78:79], v[78:79], s[42:43], s[42:43]\n\t"
    "v_rcp_f32 v76, v76\n\t"
    "v_rcp_f32 v77, v77\n\t"
    "v_rcp_f32 v78, v78\n\t"
    "s_waitcnt lgkmcnt(4)\n\t"
    "v_smfmac_f32_16x16x64_bf16 v[188:191], v[120:123], v[84:91], v124\n\t"
    "ds_read_b64 v[84:85], v212 offset:0\n\t"
    "ds_read_b64 v[86:87], v252 offset:0\n\t"
    "ds_read_b64 v[88:89], v213 offset:0\n\t"
    "ds_read_b64 v[90:91], v253 offset:0\n\t"
    "v_lshrrev_b32 v254, 16, v214\n\t"
    "v_and_b32 v214, s92, v214\n\t"
    "v_lshrrev_b32 v255, 16, v215\n\t"
    "v_and_b32 v215, s92, v215\n\t"
    "s_waitcnt vmcnt(6)\n\t"
    "v_smfmac_f32_16x16x64_bf16 v[188:191], v[156:159], v[16:23], v124\n\t"
    "v_rcp_f32 v79, v79\n\t"
    "v_pk_mul_f32 v[76:77], v[76:77], v[184:185]\n\t"
    "v_pk_fma_f32 v[76:77], v[78:79], v[186:187], v[76:77]\n\t"
    "v_add_f32 v239, v76, v77\n\t"
    "v_lshrrev_b32 v166, 7, v127\n\t"
    "v_and_b32 v167, 0x70, v127\n\t"
    "v_lshl_add_u32 v126, v166, 8, v167\n\t"
    "v_add_u32 v126, 128, v126\n\t"
    "s_bfe_u32 s46, %14, 0x30003\n\t"
    "s_min_u32 s46, s46, 6\n\t"
    "s_lshl_b32 s69, s46, 3\n\t"
    "s_add_u32 s69, s69, %16\n\t"
    "v_lshrrev_b32 v147, 4, v127\n\t"
    "v_add_u32 v148, 140288, v127\n\t"
    "s_mov_b32 s46, 48244736\n\t"
    "buffer_load_dwordx4 v[128:131], v127, %12, s46 offen sc1\n\t"
    "s_lshl_b32 s65, %14, 12\n\t"
    "s_add_u32 s65, s65, 16777216\n\t"
    "buffer_load_dwordx4 v[0:3], v127, %12, s65 offen offset:0 sc1\n\t"
    "s_waitcnt lgkmcnt(4)\n\t"
    "v_smfmac_f32_16x16x64_bf16 v[188:191], v[120:123], v[92:99], v124\n\t"
    "ds_read_b64 v[92:93], v214 offset:0\n\t"
    "ds_read_b64 v[94:95], v254 offset:0\n\t"
    "ds_read_b64 v[96:97], v215 offset:0\n\t"
    "ds_read_b64 v[98:99], v255 offset:0\n\t"
    "s_add_u32 s88, s88, 0x8000\n\t"
    "buffer_load_dwordx4 v[232:235], v127, %12, s88 offen offset:0 sc1\n\t"
    "v_lshrrev_b32 v252, 16, v116\n\t"
    "v_and_b32 v116, s92, v116\n\t"
    "v_lshrrev_b32 v253, 16, v117\n\t"
    "v_mov_b64 v[208:209], s[70:71]\n\t"
    "v_mov_b64 v[210:211], s[72:73]\n\t"
    "v_and_b32 v117, s92, v117\n\t"
    "v_smfmac_f32_16x16x64_bf16 v[192:195], v[160:163], v[16:23], v124\n\t"
    "v_pk_mul_f32 v[64:65], v[188:189], v[188:189]\n\t"
    "v_pk_mul_f32 v[66:67], v[190:191], v[190:191]\n\t"
    "v_pk_fma_f32 v[64:65], v[64:65], v[82:83], v[80:81]\n\t"
    "v_pk_fma_f32 v[66:67], v[66:67], v[82:83], v[80:81]\n\t"
    "v_pk_mul_f32 v[64:65], v[64:65], v[188:189]\n\t"
    "s_lshl_b32 s65, %14, 12\n\t"
    "s_add_u32 s65, s65, 16777216\n\t"
    "buffer_load_dwordx4 v[4:7], v127, %12, s65 offen offset:1024 sc1\n\t"
    "s_waitcnt lgkmcnt(4)\n\t"
    "v_smfmac_f32_16x16x64_bf16 v[208:211], v[120:123], v[84:91], v124\n\t"
    "ds_read_b64 v[84:85], v116 offset:64000\n\t"
    "ds_read_b64 v[86:87], v252 offset:64000\n\t"
    "ds_read_b64 v[88:89], v117 offset:64000\n\t"
    "ds_read_b64 v[90:91], v253 offset:64000\n\t"
    "v_lshrrev_b32 v254, 16, v118\n\t"
    "v_and_b32 v118, s92, v118\n\t"
    "v_lshrrev_b32 v255, 16, v119\n\t"
    "v_and_b32 v119, s92, v119\n\t"
    "s_mov_b64 exec, 1\n\t"
    "buffer_load_dword v164, off, %12, s93 offset:256 sc1\n\t"
    "s_mov_b64 exec, -1\n\t"
    "v_pk_mul_f32 v[66:67], v[66:67], v[190:191]\n\t"
    "v_exp_f32 v68, v64\n\t"
    "v_exp_f32 v69, v65\n\t"
    "v_exp_f32 v70, v66\n\t"
    "v_exp_f32 v71, v67\n\t"
    "s_lshl_b32 s65, %14, 12\n\t"
    "s_add_u32 s65, s65, 16777216\n\t"
    "buffer_load_dwordx4 v[8:11], v127, %12, s65 offen offset:2048 sc1\n\t"
    "s_waitcnt lgkmcnt(4)\n\t"
    "v_smfmac_f32_16x16x64_bf16 v[208:211], v[120:123], v[92:99], v124\n\t"
    "ds_read_b64 v[92:93], v118 offset:64000\n\t"
    "ds_read_b64 v[94:95], v254 offset:64000\n\t"
    "ds_read_b64 v[96:97], v119 offset:64000\n\t"
    "ds_read_b64 v[98:99], v255 offset:64000\n\t"
    "buffer_load_dwordx4 v[248:251], v127, %12, s90 offen offset:1024 sc1\n\t"
    "v_lshrrev_b32 v252, 16, v216\n\t"
    "v_and_b32 v216, s92, v216\n\t"
    "v_lshrrev_b32 v253, 16, v217\n\t"
    "v_and_b32 v217, s92, v217\n\t"
    "v_pk_fma_f32 v[68:69], v[68:69], s[40:41], s[40:41]\n\t"
    "v_pk_fma_f32 v[70:71], v[70:71], s[42:43], s[42:43]\n\t"
    "v_rcp_f32 v68, v68\n\t"
    "v_rcp_f32 v69, v69\n\t"
    "v_rcp_f32 v70, v70\n\t"
    "s_lshl_b32 s65, %14, 12\n\t"
    "s_add_u32 s65, s65, 16777216\n\t"
    "buffer_load_dwordx4 v[12:15], v127, %12, s65 offen offset:3072 sc1\n\t"
    "s_waitcnt lgkmcnt(4)\n\t"
    "v_smfmac_f32_16x16x64_bf16 v[192:195], v[120:123], v[84:91], v124\n\t"
    "ds_read_b64 v[84:85], v216 offset:0\n\t"
    "ds_read_b64 v[86:87], v252 offset:0\n\t"
    "ds_read_b64 v[88:89], v217 offset:0\n\t"
    "ds_read_b64 v[90:91], v253 offset:0\n\t"
    "v_lshrrev_b32 v254, 16, v218\n\t"
    "v_and_b32 v218, s92, v218\n\t"
    "v_lshrrev_b32 v255, 16, v219\n\t"
    "v_and_b32 v219, s92, v219\n\t"
    "s_waitcnt vmcnt(10)\n\t"
    "v_smfmac_f32_16x16x64_bf16 v[196:199], v[156:159], v[24:31], v124\n\t"
    "v_rcp_f32 v71, v71\n\t"
    "v_pk_mul_f32 v[68:69], v[68:69], v[188:189]\n\t"
    "v_pk_fma_f32 v[68:69], v[70:71], v[190:191], v[68:69]\n\t"
    "v_add_f32 v240, v68, v69\n\t"
    "ds_read_b128 v[16:19], v148 offset:4096\n\t"
    "s_cmp_lt_u32 %16, 4\n\t"
    "s_cbranch_scc1 L_tp239_%=\n\t"
    "s_setprio 1\n\t"
    "L_tp239_%=:\n\t"
    "s_waitcnt lgkmcnt(5)\n\t"
    "v_smfmac_f32_16x16x64_bf16 v[192:195], v[120:123], v[92:99], v124\n\t"
    "ds_read_b64 v[92:93], v218 offset:0\n\t"
    "ds_read_b64 v[94:95], v254 offset:0\n\t"
    "ds_read_b64 v[96:97], v219 offset:0\n\t"
    "ds_read_b64 v[98:99], v255 offset:0\n\t"
    "buffer_load_dwordx4 v[168:171], v127, %12, s88 offen offset:1024 sc1\n\t"
    "v_lshrrev_b32 v252, 16, v220\n\t"
    "v_and_b32 v220, s92, v220\n\t"
    "v_lshrrev_b32 v253, 16, v221\n\t"
    "v_mov_b64 v[212:213], s[70:71]\n\t"
    "v_mov_b64 v[214:215], s[72:73]\n\t"
    "v_and_b32 v221, s92, v221\n\t"
    "v_smfmac_f32_16x16x64_bf16 v[200:203], v[160:163], v[24:31], v124\n\t"
    "v_pk_mul_f32 v[72:73], v[192:193], v[192:193]\n\t"
    "v_pk_mul_f32 v[74:75], v[194:195], v[194:195]\n\t"
    "v_pk_fma_f32 v[72:73], v[72:73], v[82:83], v[80:81]\n\t"
    "v_pk_fma_f32 v[74:75], v[74:75], v[82:83], v[80:81]\n\t"
    "v_pk_mul_f32 v[72:73], v[72:73], v[192:193]\n\t"
    "ds_read_b128 v[20:23], v148 offset:5120\n\t"
    "s_waitcnt lgkmcnt(6)\n\t"
    "v_smfmac_f32_16x16x64_bf16 v[212:215], v[120:123], v[84:91], v124\n\t"
    "ds_read_b64 v[84:85], v220 offset:64000\n\t"
    "ds_read_b64 v[86:87], v252 offset:64000\n\t"
    "ds_read_b64 v[88:89], v221 offset:64000\n\t"
    "ds_read_b64 v[90:91], v253 offset:64000\n\t"
    "v_lshrrev_b32 v254, 16, v222\n\t"
    "v_and_b32 v222, s92, v222\n\t"
    "v_lshrrev_b32 v255, 16, v223\n\t"
    "v_and_b32 v223, s92, v223\n\t"
    "v_pk_mul_f32 v[74:75], v[74:75], v[194:195]\n\t"
    "v_exp_f32 v76, v72\n\t"
    "v_exp_f32 v77, v73\n\t"
    "v_exp_f32 v78, v74\n\t"
    "v_exp_f32 v79, v75\n\t"
    "ds_read_b128 v[24:27], v148 offset:6144\n\t"
    "s_waitcnt lgkmcnt(6)\n\t"
    "v_smfmac_f32_16x16x64_bf16 v[212:215], v[120:123], v[92:99], v124\n\t"
    "ds_read_b64 v[92:93], v222 offset:64000\n\t"
    "ds_read_b64 v[94:95], v254 offset:64000\n\t"
    "ds_read_b64 v[96:97], v223 offset:64000\n\t"
    "ds_read_b64 v[98:99], v255 offset:64000\n\t"
    "s_sub_u32 s91, s91, s95\n\t"
    "buffer_load_dwordx4 v[112:115], v127, %12, s91 offen offset:0 sc1\n\t"
    "v_lshrrev_b32 v252, 16, v244\n\t"
    "v_and_b32 v244, s92, v244\n\t"
    "v_lshrrev_b32 v253, 16, v245\n\t"
    "v_and_b32 v245, s92, v245\n\t"
    "v_pk_fma_f32 v[76:77], v[76:77], s[40:41], s[40:41]\n\t"
    "v_pk_fma_f32 v[78:79], v[78:79], s[42:43], s[42:43]\n\t"
    "v_rcp_f32 v76, v76\n\t"
    "v_rcp_f32 v77, v77\n\t"
    "v_rcp_f32 v78, v78\n\t"
    "ds_read_b128 v[28:31], v148 offset:7168\n\t"
    "s_waitcnt lgkmcnt(6)\n\t"
    "v_smfmac_f32_16x16x64_bf16 v[196:199], v[120:123], v[84:91], v124\n\t"
    "ds_read_b64 v[84:85], v244 offset:0\n\t"
    "ds_read_b64 v[86:87], v252 offset:0\n\t"
    "ds_read_b64 v[88:89], v245 offset:0\n\t"
    "ds_read_b64 v[90:91], v253 offset:0\n\t"
    "v_lshrrev_b32 v254, 16, v246\n\t"
    "v_and_b32 v246, s92, v246\n\t"
    "v_lshrrev_b32 v255, 16, v247\n\t"
    "v_and_b32 v247, s92, v247\n\t"
    "s_waitcnt vmcnt(5)\n\t"
    "v_readfirstlane_b32 s65, v164\n\t"
    "s_nop 0\n\t"
    "s_cmp_eq_u32 s65, 0\n\t"
    "s_cbranch_scc1 L_xw2_%=\n\t"
    "L_xwb2_%=:\n\t"
    "s_add_u32 s94, s94, 0x2000\n\t"
    "buffer_load_dwordx4 v[36:39], v127, %12, s94 offen sc1\n\t"
    "v_rcp_f32 v79, v79\n\t"
    "v_pk_mul_f32 v[76:77], v[76:77], v[192:193]\n\t"
    "v_pk_fma_f32 v[76:77], v[78:79], v[194:195], v[76:77]\n\t"
    "v_add_f32 v241, v76, v77\n\t"
    "s_cmp_lg_u32 %16, 0\n\t"
    "s_cbranch_scc1 L_bend_%=\n\t"
    "s_mov_b32 s47, 1048576\n\t"
    "L_bchk_%=:\n\t"
    "v_min_u32 v132, v128, v129\n\t"
    "v_min3_u32 v132, v132, v130, v131\n\t"
    "v_cmp_eq_u32 vcc, 0, v132\n\t"
    "s_cbranch_vccz L_bok_%=\n\t"
    "s_sleep 16\n\t"
    "s_mov_b32 s46, 48244736\n\t"
    "buffer_load_dwordx4 v[128:131], v127, %12, s46 offen sc1\n\t"
    "s_sub_u32 s47, s47, 1\n\t"
    "s_waitcnt vmcnt(0)\n\t"
    "s_cmp_lg_u32 s47, 0\n\t"
    "s_cbranch_scc1 L_bchk_%=\n\t"
    "L_bok_%=:\n\t"
    "v_readfirstlane_b32 s46, v128\n\t"
    "v_lshrrev_b32 v135, 2, v127\n\t"
    "v_mov_b32 v138, -4096\n\t"
    "v_mov_b32 v139, 4096\n\t"
    "v_subrev_u32 v128, s46, v128\n\t"
    "v_subrev_u32 v129, s46, v129\n\t"
    "v_subrev_u32 v130, s46, v130\n\t"
    "v_subrev_u32 v131, s46, v131\n\t"
    "v_add3_u32 v132, v128, v129, v130\n\t"
    "v_add_u32 v132, v132, v131\n\t"
    "s_nop 1\n\t"
    "v_add_u32_dpp v132, v132, v132 row_shr:1 row_mask:0xf bank_mask:0xf\n\t"
    "s_nop 1\n\t"
    "v_add_u32_dpp v132, v132, v132 row_shr:2 row_mask:0xf bank_mask:0xf\n\t"
    "s_nop 1\n\t"
    "v_add_u32_dpp v132, v132, v132 row_shr:4 row_mask:0xf bank_mask:0xf\n\t"
    "s_nop 1\n\t"
    "v_add_u32_dpp v132, v132, v132 row_shr:8 row_mask:0xf bank_mask:0xf\n\t"
    "s_nop 1\n\t"
    "v_add_u32_dpp v132, v132, v132 row_bcast:15 row_mask:0xa bank_mask:0xf\n\t"
    "s_nop 1\n\t"
    "v_add_u32_dpp v132, v132, v132 row_bcast:31 row_mask:0xc bank_mask:0xf\n\t"
    "s_nop 0\n\t"
    "v_readlane_b32 s46, v132, 63\n\t"
    "s_movk_i32 s47, 19\n\t"
    "v_mov_b32 v134, 600\n\t"
    "s_ashr_i32 s46, s46, 8\n\t"
    "v_sub_u32 v128, s46, v128\n\t"
    "v_med3_i32 v128, v128, v138, v139\n\t"
    "v_mad_i32_i24 v128, v128, s47, v134\n\t"
    "v_ashrrev_i32 v128, 10, v128\n\t"
    "v_med3_i32 v128, v128, -8, 3\n\t"
    "v_sub_u32 v129, s46, v129\n\t"
    "v_med3_i32 v129, v129, v138, v139\n\t"
    "v_mad_i32_i24 v129, v129, s47, v134\n\t"
    "v_ashrrev_i32 v129, 10, v129\n\t"
    "v_med3_i32 v129, v129, -8, 3\n\t"
    "v_sub_u32 v130, s46, v130\n\t"
    "v_med3_i32 v130, v130, v138, v139\n\t"
    "v_mad_i32_i24 v130, v130, s47, v134\n\t"
    "v_ashrrev_i32 v130, 10, v130\n\t"
    "v_med3_i32 v130, v130, -8, 3\n\t"
    "v_sub_u32 v131, s46, v131\n\t"
    "v_med3_i32 v131, v131, v138, v139\n\t"
    "v_mad_i32_i24 v131, v131, s47, v134\n\t"
    "v_ashrrev_i32 v131, 10, v131\n\t"
    "v_med3_i32 v131, v131, -8, 3\n\t"
    "v_add3_u32 v132, v128, v129, v130\n\t"
    "v_add_u32 v132, v132, v131\n\t"
    "v_mov_b32 v133, 0\n\t"
    "v_mov_b32 v134, 0\n\t"
    "v_cmp_gt_u32 vcc, %14, v135\n\t"
    "v_cmp_ge_u32 s[46:47], %14, v135\n\t"
    "v_add_u32 v135, 1, v135\n\t"
    "v_cndmask_b32 v136, 0, v128, vcc\n\t"
    "v_cndmask_b32 v137, 0, v128, s[46:47]\n\t"
    "v_add_u32 v133, v133, v136\n\t"
    "v_add_u32 v134, v134, v137\n\t"
    "v_cmp_gt_u32 vcc, %14, v135\n\t"
    "v_cmp_ge_u32 s[46:47], %14, v135\n\t"
    "v_add_u32 v135, 1, v135\n\t"
    "v_cndmask_b32 v136, 0, v129, vcc\n\t"
    "v_cndmask_b32 v137, 0, v129, s[46:47]\n\t"
    "v_add_u32 v133, v133, v136\n\t"
    "v_add_u32 v134, v134, v137\n\t"
    "v_cmp_gt_u32 vcc, %14, v135\n\t"
    "v_cmp_ge_u32 s[46:47], %14, v135\n\t"
    "v_add_u32 v135, 1, v135\n\t"
    "v_cndmask_b32 v136, 0, v130, vcc\n\t"
    "v_cndmask_b32 v137, 0, v130, s[46:47]\n\t"
    "v_add_u32 v133, v133, v136\n\t"
    "v_add_u32 v134, v134, v137\n\t"
    "v_cmp_gt_u32 vcc, %14, v135\n\t"
    "v_cmp_ge_u32 s[46:47], %14, v135\n\t"
    "v_add_u32 v135, 1, v135\n\t"
    "v_cndmask_b32 v136, 0, v131, vcc\n\t"
    "v_cndmask_b32 v137, 0, v131, s[46:47]\n\t"
    "v_add_u32 v133, v133, v136\n\t"
    "v_add_u32 v134, v134, v137\n\t"
    "s_nop 0\n\t"
    "v_add_u32_dpp v132, v132, v132 row_shr:1 row_mask:0xf bank_mask:0xf\n\t"
    "v_add_u32_dpp v133, v133, v133 row_shr:1 row_mask:0xf bank_mask:0xf\n\t"
    "v_add_u32_dpp v134, v134, v134 row_shr:1 row_mask:0xf bank_mask:0xf\n\t"
    "v_add_u32_dpp v132, v132, v132 row_shr:2 row_mask:0xf bank_mask:0xf\n\t"
    "v_add_u32_dpp v133, v133, v133 row_shr:2 row_mask:0xf bank_mask:0xf\n\t"
    "v_add_u32_dpp v134, v134, v134 row_shr:2 row_mask:0xf bank_mask:0xf\n\t"
    "v_add_u32_dpp v132, v132, v132 row_shr:4 row_mask:0xf bank_mask:0xf\n\t"
    "v_add_u32_dpp v133, v133, v133 row_shr:4 row_mask:0xf bank_mask:0xf\n\t"
    "v_add_u32_dpp v134, v134, v134 row_shr:4 row_mask:0xf bank_mask:0xf\n\t"
    "v_add_u32_dpp v132, v132, v132 row_shr:8 row_mask:0xf bank_mask:0xf\n\t"
    "v_add_u32_dpp v133, v133, v133 row_shr:8 row_mask:0xf bank_mask:0xf\n\t"
    "v_add_u32_dpp v134, v134, v134 row_shr:8 row_mask:0xf bank_mask:0xf\n\t"
    "v_add_u32_dpp v132, v132, v132 row_bcast:15 row_mask:0xa bank_mask:0xf\n\t"
    "v_add_u32_dpp v133, v133, v133 row_bcast:15 row_mask:0xa bank_mask:0xf\n\t"
    "v_add_u32_dpp v134, v134, v134 row_bcast:15 row_mask:0xa bank_mask:0xf\n\t"
    "v_add_u32_dpp v132, v132, v132 row_bcast:31 row_mask:0xc bank_mask:0xf\n\t"
    "v_add_u32_dpp v133, v133, v133 row_bcast:31 row_mask:0xc bank_mask:0xf\n\t"
    "v_add_u32_dpp v134, v134, v134 row_bcast:31 row_mask:0xc bank_mask:0xf\n\t"
    "v_readlane_b32 s46, v132, 63\n\t"
    "v_readlane_b32 s47, v133, 63\n\t"
    "v_readlane_b32 s48, v134, 63\n\t"
    "s_add_u32 s44, %14, 1\n\t"
    "s_mul_i32 s49, %14, s46\n\t"
    "s_ashr_i32 s49, s49, 8\n\t"
    "s_sub_u32 s47, s47, s49\n\t"
    "s_lshl_b32 s47, s47, 2\n\t"
    "s_mul_i32 s49, s44, s46\n\t"
    "s_ashr_i32 s49, s49, 8\n\t"
    "s_sub_u32 s48, s48, s49\n\t"
    "s_lshl_b32 s48, s48, 2\n\t"
    "s_add_u32 s46, s46, 256\n\t"
    "s_cmp_le_u32 s46, 512\n\t"
    "s_cselect_b32 s47, s47, 0\n\t"
    "s_cselect_b32 s48, s48, 0\n\t"
    "s_lshl_b32 s49, %14, 6\n\t"
    "s_add_u32 s47, s47, s49\n\t"
    "s_add_u32 s48, s48, s49\n\t"
    "s_add_u32 s48, s48, 64\n\t"
    "v_mov_b32 v132, s47\n\t"
    "v_mov_b32 v133, s48\n\t"
    "v_mov_b32 v134, 138896\n\t"
    "ds_write_b64 v134, v[132:133]\n\t"
    "L_bend_%=:\n\t"
    "s_waitcnt lgkmcnt(5)\n\t"
    "v_smfmac_f32_16x16x64_bf16 v[196:199], v[120:123], v[92:99], v124\n\t"
    "ds_read_b64 v[92:93], v246 offset:0\n\t"
    "ds_read_b64 v[94:95], v254 offset:0\n\t"
    "ds_read_b64 v[96:97], v247 offset:0\n\t"
    "ds_read_b64 v[98:99], v255 offset:0\n\t"
    "s_add_u32 s89, s89, 0x8000\n\t"
    "buffer_load_dwordx4 v[100:103], v127, %12, s89 offen offset:0 sc1\n\t"
    "v_lshrrev_b32 v252, 16, v224\n\t"
    "v_and_b32 v224, s92, v224\n\t"
    "v_lshrrev_b32 v253, 16, v225\n\t"
    "v_mov_b64 v[216:217], s[70:71]\n\t"
    "v_mov_b64 v[218:219], s[72:73]\n\t"
    "v_and_b32 v225, s92, v225\n\t"
    "v_pk_mul_f32 v[64:65], v[196:197], v[196:197]\n\t"
    "v_pk_mul_f32 v[66:67], v[198:199], v[198:199]\n\t"
    "v_pk_fma_f32 v[64:65], v[64:65], v[82:83], v[80:81]\n\t"
    "v_pk_fma_f32 v[66:67], v[66:67], v[82:83], v[80:81]\n\t"
    "v_pk_mul_f32 v[64:65], v[64:65], v[196:197]\n\t"
    "s_waitcnt lgkmcnt(4)\n\t"
    "v_smfmac_f32_16x16x64_bf16 v[216:219], v[120:123], v[84:91], v124\n\t"
    "ds_read_b64 v[84:85], v224 offset:64000\n\t"
    "ds_read_b64 v[86:87], v252 offset:64000\n\t"
    "ds_read_b64 v[88:89], v225 offset:64000\n\t"
    "ds_read_b64 v[90:91], v253 offset:64000\n\t"
    "v_lshrrev_b32 v254, 16, v226\n\t"
    "v_and_b32 v226, s92, v226\n\t"
    "v_lshrrev_b32 v255, 16, v227\n\t"
    "v_and_b32 v227, s92, v227\n\t"
    "v_pk_mul_f32 v[66:67], v[66:67], v[198:199]\n\t"
    "v_exp_f32 v68, v64\n\t"
    "v_exp_f32 v69, v65\n\t"
    "v_exp_f32 v70, v66\n\t"
    "v_exp_f32 v71, v67\n\t"
    "s_waitcnt lgkmcnt(4)\n\t"
    "v_smfmac_f32_16x16x64_bf16 v[216:219], v[120:123], v[92:99], v124\n\t"
    "ds_read_b64 v[92:93], v226 offset:64000\n\t"
    "ds_read_b64 v[94:95], v254 offset:64000\n\t"
    "ds_read_b64 v[96:97], v227 offset:64000\n\t"
    "ds_read_b64 v[98:99], v255 offset:64000\n\t"
    "buffer_load_dwordx4 v[104:107], v127, %12, s91 offen offset:1024 sc1\n\t"
    "v_lshrrev_b32 v252, 16, v228\n\t"
    "v_and_b32 v228, s92, v228\n\t"
    "v_lshrrev_b32 v253, 16, v229\n\t"
    "v_and_b32 v229, s92, v229\n\t"
    "v_pk_fma_f32 v[68:69], v[68:69], s[40:41], s[40:41]\n\t"
    "v_pk_fma_f32 v[70:71], v[70:71], s[42:43], s[42:43]\n\t"
    "v_rcp_f32 v68, v68\n\t"
    "v_rcp_f32 v69, v69\n\t"
    "v_rcp_f32 v70, v70\n\t"
    "s_waitcnt lgkmcnt(4)\n\t"
    "v_smfmac_f32_16x16x64_bf16 v[200:203], v[120:123], v[84:91], v124\n\t"
    "ds_read_b64 v[84:85], v228 offset:0\n\t"
    "ds_read_b64 v[86:87], v252 offset:0\n\t"
    "ds_read_b64 v[88:89], v229 offset:0\n\t"
    "ds_read_b64 v[90:91], v253 offset:0\n\t"
    "v_lshrrev_b32 v254, 16, v230\n\t"
    "v_and_b32 v230, s92, v230\n\t"
    "v_lshrrev_b32 v255, 16, v231\n\t"
    "v_and_b32 v231, s92, v231\n\t"
    "s_add_u32 s94, s94, 0x2000\n\t"
    "buffer_load_dwordx4 v[44:47], v127, %12, s94 offen sc1\n\t"
    "v_rcp_f32 v71, v71\n\t"
    "v_pk_mul_f32 v[68:69], v[68:69], v[196:197]\n\t"
    "v_pk_fma_f32 v[68:69], v[70:71], v[198:199], v[68:69]\n\t"
    "v_add_f32 v242, v68, v69\n\t"
    "s_waitcnt lgkmcnt(4)\n\t"
    "v_smfmac_f32_16x16x64_bf16 v[200:203], v[120:123], v[92:99], v124\n\t"
    "ds_read_b64 v[92:93], v230 offset:0\n\t"
    "ds_read_b64 v[94:95], v254 offset:0\n\t"
    "ds_read_b64 v[96:97], v231 offset:0\n\t"
    "ds_read_b64 v[98:99], v255 offset:0\n\t"
    "buffer_load_dwordx4 v[108:111], v127, %12, s89 offen offset:1024 sc1\n\t"
    "v_lshrrev_b32 v252, 16, v232\n\t"
    "v_and_b32 v232, s92, v232\n\t"
    "v_lshrrev_b32 v253, 16, v233\n\t"
    "v_mov_b64 v[220:221], s[70:71]\n\t"
    "v_mov_b64 v[222:223], s[72:73]\n\t"
    "v_and_b32 v233, s92, v233\n\t"
    "v_pk_mul_f32 v[72:73], v[200:201], v[200:201]\n\t"
    "v_pk_mul_f32 v[74:75], v[202:203], v[202:203]\n\t"
    "v_pk_fma_f32 v[72:73], v[72:73], v[82:83], v[80:81]\n\t"
    "v_pk_fma_f32 v[74:75], v[74:75], v[82:83], v[80:81]\n\t"
    "v_pk_mul_f32 v[72:73], v[72:73], v[200:201]\n\t"
    "s_waitcnt lgkmcnt(4)\n\t"
    "v_smfmac_f32_16x16x64_bf16 v[220:223], v[120:123], v[84:91], v124\n\t"
    "ds_read_b64 v[84:85], v232 offset:64000\n\t"
    "ds_read_b64 v[86:87], v252 offset:64000\n\t"
    "ds_read_b64 v[88:89], v233 offset:64000\n\t"
    "ds_read_b64 v[90:91], v253 offset:64000\n\t"
    "v_lshrrev_b32 v254, 16, v234\n\t"
    "v_and_b32 v234, s92, v234\n\t"
    "v_lshrrev_b32 v255, 16, v235\n\t"
    "v_and_b32 v235, s92, v235\n\t"
    "v_pk_mul_f32 v[74:75], v[74:75], v[202:203]\n\t"
    "v_exp_f32 v76, v72\n\t"
    "v_exp_f32 v77, v73\n\t"
    "v_exp_f32 v78, v74\n\t"
    "v_exp_f32 v79, v75\n\t"
    "s_waitcnt vmcnt(8) lgkmcnt(4)\n\t"
    "v_smfmac_f32_16x16x64_bf16 v[220:223], v[120:123], v[92:99], v124\n\t"
    "ds_read_b64 v[92:93], v234 offset:64000\n\t"
    "ds_read_b64 v[94:95], v254 offset:64000\n\t"
    "ds_read_b64 v[96:97], v235 offset:64000\n\t"
    "ds_read_b64 v[98:99], v255 offset:64000\n\t"
    "s_add_u32 s90, s90, 0x8000\n\t"
    "buffer_load_dwordx4 v[116:119], v127, %12, s90 offen offset:0 sc1\n\t"
    "v_lshrrev_b32 v252, 16, v248\n\t"
    "v_and_b32 v248, s92, v248\n\t"
    "v_lshrrev_b32 v253, 16, v249\n\t"
    "v_and_b32 v249, s92, v249\n\t"
    "v_pk_fma_f32 v[76:77], v[76:77], s[40:41], s[40:41]\n\t"
    "v_pk_fma_f32 v[78:79], v[78:79], s[42:43], s[42:43]\n\t"
    "v_rcp_f32 v76, v76\n\t"
    "v_rcp_f32 v77, v77\n\t"
    "v_rcp_f32 v78, v78\n\t"
    "s_waitcnt lgkmcnt(4)\n\t"
    "v_smfmac_f32_16x16x64_bf16 v[204:207], v[120:123], v[84:91], v124\n\t"
    "ds_read_b64 v[84:85], v248 offset:0\n\t"
    "ds_read_b64 v[86:87], v252 offset:0\n\t"
    "ds_read_b64 v[88:89], v249 offset:0\n\t"
    "ds_read_b64 v[90:91], v253 offset:0\n\t"
    "v_lshrrev_b32 v254, 16, v250\n\t"
    "v_and_b32 v250, s92, v250\n\t"
    "v_lshrrev_b32 v255, 16, v251\n\t"
    "v_and_b32 v251, s92, v251\n\t"
    "s_mov_b64 exec, 1\n\t"
    "buffer_load_dword v164, off, %12, s93 offset:384 sc1\n\t"
    "s_mov_b64 exec, -1\n\t"
    "s_waitcnt vmcnt(6)\n\t"
    "v_smfmac_f32_16x16x64_bf16 v[204:207], v[156:159], v[32:39], v124\n\t"
    "v_rcp_f32 v79, v79\n\t"
    "v_pk_mul_f32 v[76:77], v[76:77], v[200:201]\n\t"
    "v_pk_fma_f32 v[76:77], v[78:79], v[202:203], v[76:77]\n\t"
    "v_add_f32 v243, v76, v77\n\t"
    "s_waitcnt lgkmcnt(4)\n\t"
    "v_smfmac_f32_16x16x64_bf16 v[204:207], v[120:123], v[92:99], v124\n\t"
    "ds_read_b64 v[92:93], v250 offset:0\n\t"
    "ds_read_b64 v[94:95], v254 offset:0\n\t"
    "ds_read_b64 v[96:97], v251 offset:0\n\t"
    "ds_read_b64 v[98:99], v255 offset:0\n\t"
    "v_lshrrev_b32 v252, 16, v168\n\t"
    "v_and_b32 v168, s92, v168\n\t"
    "v_lshrrev_b32 v253, 16, v169\n\t"
    "v_mov_b64 v[224:225], s[70:71]\n\t"
    "v_mov_b64 v[226:227], s[72:73]\n\t"
    "v_and_b32 v169, s92, v169\n\t"
    "v_smfmac_f32_16x16x64_bf16 v[208:211], v[160:163], v[32:39], v124\n\t"
    "v_pk_mul_f32 v[64:65], v[204:205], v[204:205]\n\t"
    "v_pk_mul_f32 v[66:67], v[206:207], v[206:207]\n\t"
    "v_pk_fma_f32 v[64:65], v[64:65], v[82:83], v[80:81]\n\t"
    "v_pk_fma_f32 v[66:67], v[66:67], v[82:83], v[80:81]\n\t"
    "v_pk_mul_f32 v[64:65], v[64:65], v[204:205]\n\t"
    "ds_read_b128 v[32:35], v148 offset:8192\n\t"
    "s_waitcnt lgkmcnt(5)\n\t"
    "v_smfmac_f32_16x16x64_bf16 v[224:227], v[120:123], v[84:91], v124\n\t"
    "ds_read_b64 v[84:85], v168 offset:64000\n\t"
    "ds_read_b64 v[86:87], v252 offset:64000\n\t"
    "ds_read_b64 v[88:89], v169 offset:64000\n\t"
    "ds_read_b64 v[90:91], v253 offset:64000\n\t"
    "v_lshrrev_b32 v254, 16, v170\n\t"
    "v_and_b32 v170, s92, v170\n\t"
    "v_lshrrev_b32 v255, 16, v171\n\t"
    "v_and_b32 v171, s92, v171\n\t"
    "v_pk_mul_f32 v[66:67], v[66:67], v[206:207]\n\t"
    "v_exp_f32 v68, v64\n\t"
    "v_exp_f32 v69, v65\n\t"
    "v_exp_f32 v70, v66\n\t"
    "v_exp_f32 v71, v67\n\t"
    "ds_read_b128 v[36:39], v148 offset:9216\n\t"
    "s_waitcnt lgkmcnt(6)\n\t"
    "v_smfmac_f32_16x16x64_bf16 v[224:227], v[120:123], v[92:99], v124\n\t"
    "ds_read_b64 v[92:93], v170 offset:64000\n\t"
    "ds_read_b64 v[94:95], v254 offset:64000\n\t"
    "ds_read_b64 v[96:97], v171 offset:64000\n\t"
    "ds_read_b64 v[98:99], v255 offset:64000\n\t"
    "buffer_load_dwordx4 v[168:171], v127, %12, s90 offen offset:1024 sc1\n\t"
    "v_lshrrev_b32 v252, 16, v112\n\t"
    "v_and_b32 v112, s92, v112\n\t"
    "v_lshrrev_b32 v253, 16, v113\n\t"
    "v_and_b32 v113, s92, v113\n\t"
    "v_pk_fma_f32 v[68:69], v[68:69], s[40:41], s[40:41]\n\t"
    "v_pk_fma_f32 v[70:71], v[70:71], s[42:43], s[42:43]\n\t"
    "v_rcp_f32 v68, v68\n\t"
    "v_rcp_f32 v69, v69\n\t"
    "v_rcp_f32 v70, v70\n\t"
    "s_waitcnt lgkmcnt(5)\n\t"
    "v_smfmac_f32_16x16x64_bf16 v[208:211], v[120:123], v[84:91], v124\n\t"
    "ds_read_b64 v[84:85], v112 offset:0\n\t"
    "ds_read_b64 v[86:87], v252 offset:0\n\t"
    "ds_read_b64 v[88:89], v113 offset:0\n\t"
    "ds_read_b64 v[90:91], v253 offset:0\n\t"
    "v_lshrrev_b32 v254, 16, v114\n\t"
    "v_and_b32 v114, s92, v114\n\t"
    "v_lshrrev_b32 v255, 16, v115\n\t"
    "v_and_b32 v115, s92, v115\n\t"
    "s_waitcnt vmcnt(4)\n\t"
    "v_smfmac_f32_16x16x64_bf16 v[212:215], v[156:159], v[40:47], v124\n\t"
    "v_rcp_f32 v71, v71\n\t"
    "v_pk_mul_f32 v[68:69], v[68:69], v[204:205]\n\t"
    "v_pk_fma_f32 v[68:69], v[70:71], v[206:207], v[68:69]\n\t"
    "v_add_f32 v244, v68, v69\n\t"
    "v_mov_b32 v155, 138896\n\t"
    "s_and_b32 s46, %16, 3\n\t"
    "s_mul_i32 s49, s46, 1040\n\t"
    "s_add_u32 s49, s49, 2064\n\t"
    "L_rng_%=:\n\t"
    "ds_read_b64 v[144:145], v155\n\t"
    "s_waitcnt lgkmcnt(0)\n\t"
    "v_readfirstlane_b32 s38, v145\n\t"
    "v_readfirstlane_b32 s37, v144\n\t"
    "s_cmp_eq_u32 s38, 0\n\t"
    "s_cbranch_scc1 L_rng_%=\n\t"
    "s_sub_u32 s38, s38, s37\n\t"
    "s_lshr_b32 s44, s38, 2\n\t"
    "s_mul_i32 s51, s46, s44\n\t"
    "s_add_u32 s48, s51, s44\n\t"
    "s_lshl_b32 s48, s48, 16\n\t"
    "s_add_u32 s48, s48, s51\n\t"
    "s_lshr_b32 s47, %16, 2\n\t"
    "s_add_u32 s48, s48, s47\n\t"
    "s_add_u32 s48, s48, 0x200\n\t"
    "s_mul_i32 s46, s37, 400\n\t"
    "s_add_u32 s96, %18, s46\n\t"
    "s_addc_u32 s97, %19, 0\n\t"
    "s_sub_u32 s98, 6553600, s46\n\t"
    "s_mov_b32 s99, 0x20000\n\t"
    "v_bfe_u32 v154, v147, 2, 2\n\t"
    "v_and_b32 v155, 3, v147\n\t"
    "v_mul_u32_u24 v154, 25, v154\n\t"
    "v_mad_u32_u24 v154, v155, 6, v154\n\t"
    "v_lshlrev_b32 v153, 2, v154\n\t"
    "s_and_b32 s53, s48, 0xff\n\t"
    "s_bfe_u32 s55, s48, 0x80008\n\t"
    "s_add_u32 s54, s53, s55\n\t"
    "s_add_u32 s55, s54, s55\n\t"
    "s_lshr_b32 s64, s48, 16\n\t"
    "s_mul_i32 s66, s53, 400\n\t"
    "buffer_load_dwordx3 v[128:130], v153, s[96:99], s66 offen\n\t"
    "buffer_load_dwordx4 v[132:135], v153, s[96:99], s66 offen offset:12\n\t"
    "s_mul_i32 s66, s54, 400\n\t"
    "buffer_load_dwordx3 v[136:138], v153, s[96:99], s66 offen\n\t"
    "buffer_load_dwordx4 v[140:143], v153, s[96:99], s66 offen offset:12\n\t"
    "v_cmp_eq_u32 s[62:63], 0, v155\n\t"
    "v_and_b32 v151, 48, v147\n\t"
    "v_or_b32 v155, 0x100000, v151\n\t"
    "v_cndmask_b32 v152, v155, v151, s[62:63]\n\t"
    "v_smfmac_f32_16x16x64_bf16 v[208:211], v[120:123], v[92:99], v124\n\t"
    "ds_read_b64 v[92:93], v114 offset:0\n\t"
    "ds_read_b64 v[94:95], v254 offset:0\n\t"
    "ds_read_b64 v[96:97], v115 offset:0\n\t"
    "ds_read_b64 v[98:99], v255 offset:0\n\t"
    "s_add_u32 s91, s91, 0x8000\n\t"
    "buffer_load_dwordx4 v[112:115], v127, %12, s91 offen offset:0 sc1\n\t"
    "v_lshrrev_b32 v252, 16, v100\n\t"
    "v_and_b32 v100, s92, v100\n\t"
    "v_lshrrev_b32 v253, 16, v101\n\t"
    "v_mov_b64 v[228:229], s[70:71]\n\t"
    "v_mov_b64 v[230:231], s[72:73]\n\t"
    "v_and_b32 v101, s92, v101\n\t"
    "v_smfmac_f32_16x16x64_bf16 v[216:219], v[160:163], v[40:47], v124\n\t"
    "v_pk_mul_f32 v[72:73], v[208:209], v[208:209]\n\t"
    "v_pk_mul_f32 v[74:75], v[210:211], v[210:211]\n\t"
    "v_pk_fma_f32 v[72:73], v[72:73], v[82:83], v[80:81]\n\t"
    "v_pk_fma_f32 v[74:75], v[74:75], v[82:83], v[80:81]\n\t"
    "v_pk_mul_f32 v[72:73], v[72:73], v[208:209]\n\t"
    "ds_read_b128 v[40:43], v148 offset:10240\n\t"
    "s_setprio 0\n\t"
    "v_smfmac_f32_16x16x64_bf16 v[228:231], v[120:123], v[84:91], v124\n\t"
    "ds_read_b64 v[84:85], v100 offset:64000\n\t"
    "ds_read_b64 v[86:87], v252 offset:64000\n\t"
    "ds_read_b64 v[88:89], v101 offset:64000\n\t"
    "ds_read_b64 v[90:91], v253 offset:64000\n\t"
    "v_lshrrev_b32 v254, 16, v102\n\t"
    "v_and_b32 v102, s92, v102\n\t"
    "v_lshrrev_b32 v255, 16, v103\n\t"
    "v_and_b32 v103, s92, v103\n\t"
    "s_waitcnt vmcnt(6)\n\t"
    "v_readfirstlane_b32 s65, v164\n\t"
    "s_nop 0\n\t"
    "s_cmp_eq_u32 s65, 0\n\t"
    "s_cbranch_scc1 L_xw3_%=\n\t"
    "L_xwb3_%=:\n\t"
    "s_add_u32 s94, s94, 0x2000\n\t"
    "buffer_load_dwordx4 v[52:55], v127, %12, s94 offen sc1\n\t"
    "v_pk_mul_f32 v[74:75], v[74:75], v[210:211]\n\t"
    "v_exp_f32 v76, v72\n\t"
    "v_exp_f32 v77, v73\n\t"
    "v_exp_f32 v78, v74\n\t"
    "v_exp_f32 v79, v75\n\t"
    "ds_read_b128 v[44:47], v148 offset:11264\n\t"
    "s_waitcnt lgkmcnt(6)\n\t"
    "v_smfmac_f32_16x16x64_bf16 v[228:231], v[120:123], v[92:99], v124\n\t"
    "ds_read_b64 v[92:93], v102 offset:64000\n\t"
    "ds_read_b64 v[94:95], v254 offset:64000\n\t"
    "ds_read_b64 v[96:97], v103 offset:64000\n\t"
    "ds_read_b64 v[98:99], v255 offset:64000\n\t"
    "buffer_load_dwordx4 v[100:103], v127, %12, s91 offen offset:1024 sc1\n\t"
    "v_lshrrev_b32 v252, 16, v104\n\t"
    "v_and_b32 v104, s92, v104\n\t"
    "v_lshrrev_b32 v253, 16, v105\n\t"
    "v_and_b32 v105, s92, v105\n\t"
    "v_pk_fma_f32 v[76:77], v[76:77], s[40:41], s[40:41]\n\t"
    "v_pk_fma_f32 v[78:79], v[78:79], s[42:43], s[42:43]\n\t"
    "v_rcp_f32 v76, v76\n\t"
    "v_rcp_f32 v77, v77\n\t"
    "v_rcp_f32 v78, v78\n\t"
    "s_waitcnt lgkmcnt(5)\n\t"
    "v_smfmac_f32_16x16x64_bf16 v[212:215], v[120:123], v[84:91], v124\n\t"
    "ds_read_b64 v[84:85], v104 offset:0\n\t"
    "ds_read_b64 v[86:87], v252 offset:0\n\t"
    "ds_read_b64 v[88:89], v105 offset:0\n\t"
    "ds_read_b64 v[90:91], v253 offset:0\n\t"
    "v_lshrrev_b32 v254, 16, v106\n\t"
    "v_and_b32 v106, s92, v106\n\t"
    "v_lshrrev_b32 v255, 16, v107\n\t"
    "v_and_b32 v107, s92, v107\n\t"
    "v_rcp_f32 v79, v79\n\t"
    "v_pk_mul_f32 v[76:77], v[76:77], v[208:209]\n\t"
    "v_pk_fma_f32 v[76:77], v[78:79], v[210:211], v[76:77]\n\t"
    "v_add_f32 v245, v76, v77\n\t"
    "s_waitcnt lgkmcnt(4)\n\t"
    "v_smfmac_f32_16x16x64_bf16 v[212:215], v[120:123], v[92:99], v124\n\t"
    "ds_read_b64 v[92:93], v106 offset:0\n\t"
    "ds_read_b64 v[94:95], v254 offset:0\n\t"
    "ds_read_b64 v[96:97], v107 offset:0\n\t"
    "ds_read_b64 v[98:99], v255 offset:0\n\t"
    "v_lshrrev_b32 v252, 16, v108\n\t"
    "v_and_b32 v108, s92, v108\n\t"
    "v_lshrrev_b32 v253, 16, v109\n\t"
    "v_mov_b64 v[232:233], s[70:71]\n\t"
    "v_mov_b64 v[234:235], s[72:73]\n\t"
    "v_and_b32 v109, s92, v109\n\t"
    "v_pk_mul_f32 v[64:65], v[212:213], v[212:213]\n\t"
    "v_pk_mul_f32 v[66:67], v[214:215], v[214:215]\n\t"
    "v_pk_fma_f32 v[64:65], v[64:65], v[82:83], v[80:81]\n\t"
    "v_pk_fma_f32 v[66:67], v[66:67], v[82:83], v[80:81]\n\t"
    "v_pk_mul_f32 v[64:65], v[64:65], v[212:213]\n\t"
    "s_waitcnt lgkmcnt(0)\n\t"
    "s_barrier\n\t"
    "v_smfmac_f32_16x16x64_bf16 v[232:235], v[120:123], v[84:91], v124\n\t"
    "ds_read_b64 v[84:85], v108 offset:64000\n\t"
    "ds_read_b64 v[86:87], v252 offset:64000\n\t"
    "ds_read_b64 v[88:89], v109 offset:64000\n\t"
    "ds_read_b64 v[90:91], v253 offset:64000\n\t"
    "v_lshrrev_b32 v254, 16, v110\n\t"
    "v_and_b32 v110, s92, v110\n\t"
    "v_lshrrev_b32 v255, 16, v111\n\t"
    "v_and_b32 v111, s92, v111\n\t"
    "s_add_u32 s94, s94, 0x2000\n\t"
    "buffer_load_dwordx4 v[60:63], v127, %12, s94 offen sc1\n\t"
    "v_pk_mul_f32 v[66:67], v[66:67], v[214:215]\n\t"
    "v_exp_f32 v68, v64\n\t"
    "v_exp_f32 v69, v65\n\t"
    "v_exp_f32 v70, v66\n\t"
    "v_exp_f32 v71, v67\n\t"
    "s_mul_i32 s44, s69, 1040\n\t"
    "s_add_u32 m0, s44, 0\n\t"
    "s_lshl_b32 s44, s69, 11\n\t"
    "buffer_load_dwordx4 v126, %15, s44 offen lds\n\t"
    "s_add_u32 s69, s69, 8\n\t"
    "s_sub_u32 s44, s69, 61\n\t"
    "s_cmp_ge_u32 s69, 61\n\t"
    "s_cselect_b32 s69, s44, s69\n\t"
    "v_smfmac_f32_16x16x64_bf16 v[232:235], v[120:123], v[92:99], v124\n\t"
    "ds_read_b64 v[92:93], v110 offset:64000\n\t"
    "ds_read_b64 v[94:95], v254 offset:64000\n\t"
    "ds_read_b64 v[96:97], v111 offset:64000\n\t"
    "ds_read_b64 v[98:99], v255 offset:64000\n\t"
    "v_lshrrev_b32 v252, 16, v116\n\t"
    "v_and_b32 v116, s92, v116\n\t"
    "v_lshrrev_b32 v253, 16, v117\n\t"
    "v_and_b32 v117, s92, v117\n\t"
    "v_pk_fma_f32 v[68:69], v[68:69], s[40:41], s[40:41]\n\t"
    "v_pk_fma_f32 v[70:71], v[70:71], s[42:43], s[42:43]\n\t"
    "v_rcp_f32 v68, v68\n\t"
    "v_rcp_f32 v69, v69\n\t"
    "v_rcp_f32 v70, v70\n\t"
    "s_mul_i32 s44, s69, 1040\n\t"
    "s_add_u32 m0, s44, 0\n\t"
    "s_lshl_b32 s44, s69, 11\n\t"
    "buffer_load_dwordx4 v126, %15, s44 offen lds\n\t"
    "s_add_u32 s69, s69, 8\n\t"
    "s_sub_u32 s44, s69, 61\n\t"
    "s_cmp_ge_u32 s69, 61\n\t"
    "s_cselect_b32 s69, s44, s69\n\t"
    "s_waitcnt lgkmcnt(4)\n\t"
    "v_smfmac_f32_16x16x64_bf16 v[216:219], v[120:123], v[84:91], v124\n\t"
    "ds_read_b64 v[84:85], v116 offset:64000\n\t"
    "ds_read_b64 v[86:87], v252 offset:64000\n\t"
    "ds_read_b64 v[88:89], v117 offset:64000\n\t"
    "ds_read_b64 v[90:91], v253 offset:64000\n\t"
    "v_lshrrev_b32 v254, 16, v118\n\t"
    "v_and_b32 v118, s92, v118\n\t"
    "v_lshrrev_b32 v255, 16, v119\n\t"
    "v_and_b32 v119, s92, v119\n\t"
    "v_rcp_f32 v71, v71\n\t"
    "v_pk_mul_f32 v[68:69], v[68:69], v[212:213]\n\t"
    "v_pk_fma_f32 v[68:69], v[70:71], v[214:215], v[68:69]\n\t"
    "v_add_f32 v246, v68, v69\n\t"
    "s_mul_i32 s44, s69, 1040\n\t"
    "s_add_u32 m0, s44, 0\n\t"
    "s_lshl_b32 s44, s69, 11\n\t"
    "buffer_load_dwordx4 v126, %15, s44 offen lds\n\t"
    "s_add_u32 s69, s69, 8\n\t"
    "s_sub_u32 s44, s69, 61\n\t"
    "s_cmp_ge_u32 s69, 61\n\t"
    "s_cselect_b32 s69, s44, s69\n\t"
    "s_waitcnt vmcnt(11) lgkmcnt(4)\n\t"
    "v_smfmac_f32_16x16x64_bf16 v[216:219], v[120:123], v[92:99], v124\n\t"
    "ds_read_b64 v[92:93], v118 offset:64000\n\t"
    "ds_read_b64 v[94:95], v254 offset:64000\n\t"
    "ds_read_b64 v[96:97], v119 offset:64000\n\t"
    "ds_read_b64 v[98:99], v255 offset:64000\n\t"
    "v_lshrrev_b32 v252, 16, v168\n\t"
    "v_and_b32 v168, s92, v168\n\t"
    "v_lshrrev_b32 v253, 16, v169\n\t"
    "v_and_b32 v169, s92, v169\n\t"
    "s_mul_i32 s44, s69, 1040\n\t"
    "s_add_u32 m0, s44, 0\n\t"
    "s_lshl_b32 s44, s69, 11\n\t"
    "buffer_load_dwordx4 v126, %15, s44 offen lds\n\t"
    "s_add_u32 s69, s69, 8\n\t"
    "s_sub_u32 s44, s69, 61\n\t"
    "s_cmp_ge_u32 s69, 61\n\t"
    "s_cselect_b32 s69, s44, s69\n\t"
    "v_lshlrev_b32 v146, 2, v147\n\t"
    "s_lshl_b32 s50, s37, 8\n\t"
    "s_add_u32 s50, s50, 26214400\n\t"
    "s_lshl_b32 s46, s53, 8\n\t"
    "s_add_u32 s46, s46, s50\n\t"
    "buffer_load_dword v144, v146, %12, s46 offen sc1\n\t"
    "buffer_load_dword v145, v146, %12, s46 offen offset:512 sc1\n\t"
    "v_mov_b32 v149, s49\n\t"
    "s_add_u32 s46, s51, 6\n\t"
    "v_mov_b32 v155, s46\n\t"
    "ds_write_b32 v149, v155\n\t"
    "s_mov_b32 s61, 0\n\t"
    "v_mov_b32 v149, 1024\n\t"
    "v_mov_b32 v150, 0\n\t"
    "ds_write_b32 v149, v150\n\t"
    "s_mov_b32 s45, 1\n\t"
    "s_waitcnt lgkmcnt(6)\n\t"
    "v_smfmac_f32_16x16x64_bf16 v[220:223], v[120:123], v[84:91], v124\n\t"
    "ds_read_b64 v[84:85], v168 offset:64000\n\t"
    "ds_read_b64 v[86:87], v252 offset:64000\n\t"
    "ds_read_b64 v[88:89], v169 offset:64000\n\t"
    "ds_read_b64 v[90:91], v253 offset:64000\n\t"
    "v_lshrrev_b32 v254, 16, v170\n\t"
    "v_and_b32 v170, s92, v170\n\t"
    "v_lshrrev_b32 v255, 16, v171\n\t"
    "v_and_b32 v171, s92, v171\n\t"
    "s_waitcnt vmcnt(8)\n\t"
    "v_smfmac_f32_16x16x64_bf16 v[220:223], v[156:159], v[48:55], v124\n\t"
    "v_pk_mul_f32 v[72:73], v[216:217], v[216:217]\n\t"
    "v_pk_mul_f32 v[74:75], v[218:219], v[218:219]\n\t"
    "v_pk_fma_f32 v[72:73], v[72:73], v[82:83], v[80:81]\n\t"
    "v_pk_fma_f32 v[74:75], v[74:75], v[82:83], v[80:81]\n\t"
    "v_pk_mul_f32 v[72:73], v[72:73], v[216:217]\n\t"
    "v_pk_mul_f32 v[74:75], v[74:75], v[218:219]\n\t"
    "v_exp_f32 v76, v72\n\t"
    "s_mul_i32 s44, s69, 1040\n\t"
    "s_add_u32 m0, s44, 0\n\t"
    "s_lshl_b32 s44, s69, 11\n\t"
    "buffer_load_dwordx4 v126, %15, s44 offen lds\n\t"
    "s_add_u32 s69, s69, 8\n\t"
    "s_sub_u32 s44, s69, 61\n\t"
    "s_cmp_ge_u32 s69, 61\n\t"
    "s_cselect_b32 s69, s44, s69\n\t"
    "s_waitcnt lgkmcnt(6)\n\t"
    "v_smfmac_f32_16x16x64_bf16 v[220:223], v[120:123], v[92:99], v124\n\t"
    "ds_read_b64 v[92:93], v170 offset:64000\n\t"
    "ds_read_b64 v[94:95], v254 offset:64000\n\t"
    "ds_read_b64 v[96:97], v171 offset:64000\n\t"
    "ds_read_b64 v[98:99], v255 offset:64000\n\t"
    "v_lshrrev_b32 v252, 16, v112\n\t"
    "v_and_b32 v112, s92, v112\n\t"
    "v_lshrrev_b32 v253, 16, v113\n\t"
    "v_and_b32 v113, s92, v113\n\t"
    "v_smfmac_f32_16x16x64_bf16 v[224:227], v[160:163], v[48:55], v124\n\t"
    "v_exp_f32 v77, v73\n\t"
    "v_exp_f32 v78, v74\n\t"
    "v_exp_f32 v79, v75\n\t"
    "v_pk_fma_f32 v[76:77], v[76:77], s[40:41], s[40:41]\n\t"
    "v_pk_fma_f32 v[78:79], v[78:79], s[42:43], s[42:43]\n\t"
    "v_rcp_f32 v76, v76\n\t"
    "v_rcp_f32 v77, v77\n\t"
    "s_mul_i32 s44, s69, 1040\n\t"
    "s_add_u32 m0, s44, 0\n\t"
    "s_lshl_b32 s44, s69, 11\n\t"
    "buffer_load_dwordx4 v126, %15, s44 offen lds\n\t"
    "s_add_u32 s69, s69, 8\n\t"
    "s_sub_u32 s44, s69, 61\n\t"
    "s_cmp_ge_u32 s69, 61\n\t"
    "s_cselect_b32 s69, s44, s69\n\t"
    "ds_read_b128 v[48:51], v148 offset:12288\n\t"
    "s_waitcnt lgkmcnt(5)\n\t"
    "v_smfmac_f32_16x16x64_bf16 v[224:227], v[120:123], v[84:91], v124\n\t"
    "ds_read_b64 v[84:85], v112 offset:64000\n\t"
    "ds_read_b64 v[86:87], v252 offset:64000\n\t"
    "ds_read_b64 v[88:89], v113 offset:64000\n\t"
    "ds_read_b64 v[90:91], v253 offset:64000\n\t"
    "v_lshrrev_b32 v254, 16, v114\n\t"
    "v_and_b32 v114, s92, v114\n\t"
    "v_lshrrev_b32 v255, 16, v115\n\t"
    "v_and_b32 v115, s92, v115\n\t"
    "v_rcp_f32 v78, v78\n\t"
    "v_rcp_f32 v79, v79\n\t"
    "v_pk_mul_f32 v[76:77], v[76:77], v[216:217]\n\t"
    "v_pk_fma_f32 v[76:77], v[78:79], v[218:219], v[76:77]\n\t"
    "v_add_f32 v247, v76, v77\n\t"
    "v_pk_mul_f32 v[64:65], v[220:221], v[220:221]\n\t"
    "v_pk_mul_f32 v[66:67], v[222:223], v[222:223]\n\t"
    "s_mul_i32 s44, s69, 1040\n\t"
    "s_add_u32 m0, s44, 0\n\t"
    "s_lshl_b32 s44, s69, 11\n\t"
    "buffer_load_dwordx4 v126, %15, s44 offen lds\n\t"
    "s_add_u32 s69, s69, 8\n\t"
    "s_sub_u32 s44, s69, 61\n\t"
    "s_cmp_ge_u32 s69, 61\n\t"
    "s_cselect_b32 s69, s44, s69\n\t"
    "ds_read_b128 v[52:55], v148 offset:13312\n\t"
    "s_waitcnt vmcnt(10) lgkmcnt(6)\n\t"
    "v_smfmac_f32_16x16x64_bf16 v[224:227], v[120:123], v[92:99], v124\n\t"
    "ds_read_b64 v[92:93], v114 offset:64000\n\t"
    "ds_read_b64 v[94:95], v254 offset:64000\n\t"
    "ds_read_b64 v[96:97], v115 offset:64000\n\t"
    "ds_read_b64 v[98:99], v255 offset:64000\n\t"
    "v_lshrrev_b32 v252, 16, v100\n\t"
    "v_and_b32 v100, s92, v100\n\t"
    "v_lshrrev_b32 v253, 16, v101\n\t"
    "v_and_b32 v101, s92, v101\n\t"
    "v_pk_fma_f32 v[64:65], v[64:65], v[82:83], v[80:81]\n\t"
    "v_pk_fma_f32 v[66:67], v[66:67], v[82:83], v[80:81]\n\t"
    "v_pk_mul_f32 v[64:65], v[64:65], v[220:221]\n\t"
    "v_pk_mul_f32 v[66:67], v[66:67], v[222:223]\n\t"
    "v_exp_f32 v68, v64\n\t"
    "v_exp_f32 v69, v65\n\t"
    "v_exp_f32 v70, v66\n\t"
    "s_mul_i32 s44, s69, 1040\n\t"
    "s_add_u32 m0, s44, 0\n\t"
    "s_lshl_b32 s44, s69, 11\n\t"
    "buffer_load_dwordx4 v126, %15, s44 offen lds\n\t"
    "s_add_u32 s69, s69, 8\n\t"
    "s_sub_u32 s44, s69, 61\n\t"
    "s_cmp_ge_u32 s69, 61\n\t"
    "s_cselect_b32 s69, s44, s69\n\t"
    "s_waitcnt lgkmcnt(5)\n\t"
    "v_smfmac_f32_16x16x64_bf16 v[228:231], v[120:123], v[84:91], v124\n\t"
    "ds_read_b64 v[84:85], v100 offset:64000\n\t"
    "ds_read_b64 v[86:87], v252 offset:64000\n\t"
    "ds_read_b64 v[88:89], v101 offset:64000\n\t"
    "ds_read_b64 v[90:91], v253 offset:64000\n\t"
    "v_lshrrev_b32 v254, 16, v102\n\t"
    "v_and_b32 v102, s92, v102\n\t"
    "v_lshrrev_b32 v255, 16, v103\n\t"
    "v_and_b32 v103, s92, v103\n\t"
    "s_waitcnt vmcnt(10)\n\t"
    "v_smfmac_f32_16x16x64_bf16 v[228:231], v[156:159], v[56:63], v124\n\t"
    "v_exp_f32 v71, v67\n\t"
    "v_pk_fma_f32 v[68:69], v[68:69], s[40:41], s[40:41]\n\t"
    "v_pk_fma_f32 v[70:71], v[70:71], s[42:43], s[42:43]\n\t"
    "v_rcp_f32 v68, v68\n\t"
    "v_rcp_f32 v69, v69\n\t"
    "v_rcp_f32 v70, v70\n\t"
    "v_rcp_f32 v71, v71\n\t"
    "s_waitcnt lgkmcnt(4)\n\t"
    "v_smfmac_f32_16x16x64_bf16 v[228:231], v[120:123], v[92:99], v124\n\t"
    "ds_read_b64 v[92:93], v102 offset:64000\n\t"
    "ds_read_b64 v[94:95], v254 offset:64000\n\t"
    "ds_read_b64 v[96:97], v103 offset:64000\n\t"
    "ds_read_b64 v[98:99], v255 offset:64000\n\t"
    "v_smfmac_f32_16x16x64_bf16 v[232:235], v[160:163], v[56:63], v124\n\t"
    "v_pk_mul_f32 v[68:69], v[68:69], v[220:221]\n\t"
    "v_pk_fma_f32 v[68:69], v[70:71], v[222:223], v[68:69]\n\t"
    "v_add_f32 v248, v68, v69\n\t"
    "v_pk_mul_f32 v[72:73], v[224:225], v[224:225]\n\t"
    "v_pk_mul_f32 v[74:75], v[226:227], v[226:227]\n\t"
    "v_pk_fma_f32 v[72:73], v[72:73], v[82:83], v[80:81]\n\t"
    "v_pk_fma_f32 v[74:75], v[74:75], v[82:83], v[80:81]\n\t"
    "ds_read_b128 v[56:59], v148 offset:14336\n\t"
    "s_waitcnt lgkmcnt(5)\n\t"
    "v_smfmac_f32_16x16x64_bf16 v[232:235], v[120:123], v[84:91], v124\n\t"
    "v_pk_mul_f32 v[72:73], v[72:73], v[224:225]\n\t"
    "v_pk_mul_f32 v[74:75], v[74:75], v[226:227]\n\t"
    "v_exp_f32 v76, v72\n\t"
    "v_exp_f32 v77, v73\n\t"
    "v_exp_f32 v78, v74\n\t"
    "v_exp_f32 v79, v75\n\t"
    "v_pk_fma_f32 v[76:77], v[76:77], s[40:41], s[40:41]\n\t"
    "ds_read_b128 v[60:63], v148 offset:15360\n\t"
    "s_waitcnt lgkmcnt(2)\n\t"
    "v_smfmac_f32_16x16x64_bf16 v[232:235], v[120:123], v[92:99], v124\n\t"
    "v_pk_fma_f32 v[78:79], v[78:79], s[42:43], s[42:43]\n\t"
    "v_rcp_f32 v76, v76\n\t"
    "v_rcp_f32 v77, v77\n\t"
    "v_rcp_f32 v78, v78\n\t"
    "v_rcp_f32 v79, v79\n\t"
    "v_pk_mul_f32 v[76:77], v[76:77], v[224:225]\n\t"
    "v_pk_fma_f32 v[76:77], v[78:79], v[226:227], v[76:77]\n\t"
    "s_barrier\n\t"
    "v_add_u32 v126, 124928, v126\n\t"
    "s_bfe_u32 s46, %14, 0x30003\n\t"
    "s_min_u32 s46, s46, 6\n\t"
    "s_lshl_b32 s69, s46, 3\n\t"
    "s_add_u32 s69, s69, %16\n\t"
    "s_mul_i32 s44, s69, 1040\n\t"
    "s_add_u32 m0, s44, 63440\n\t"
    "s_lshl_b32 s44, s69, 11\n\t"
    "buffer_load_dwordx4 v126, %15, s44 offen lds\n\t"
    "s_add_u32 s69, s69, 8\n\t"
    "s_sub_u32 s44, s69, 64\n\t"
    "s_cmp_ge_u32 s69, 64\n\t"
    "s_cselect_b32 s69, s44, s69\n\t"
    "s_mul_i32 s44, s69, 1040\n\t"
    "s_add_u32 m0, s44, 63440\n\t"
    "s_lshl_b32 s44, s69, 11\n\t"
    "buffer_load_dwordx4 v126, %15, s44 offen lds\n\t"
    "s_add_u32 s69, s69, 8\n\t"
    "s_sub_u32 s44, s69, 64\n\t"
    "s_cmp_ge_u32 s69, 64\n\t"
    "s_cselect_b32 s69, s44, s69\n\t"
    "s_mul_i32 s44, s69, 1040\n\t"
    "s_add_u32 m0, s44, 63440\n\t"
    "s_lshl_b32 s44, s69, 11\n\t"
    "buffer_load_dwordx4 v126, %15, s44 offen lds\n\t"
    "s_add_u32 s69, s69, 8\n\t"
    "s_sub_u32 s44, s69, 64\n\t"
    "s_cmp_ge_u32 s69, 64\n\t"
    "s_cselect_b32 s69, s44, s69\n\t"
    "s_mul_i32 s44, s69, 1040\n\t"
    "s_add_u32 m0, s44, 63440\n\t"
    "s_lshl_b32 s44, s69, 11\n\t"
    "buffer_load_dwordx4 v126, %15, s44 offen lds\n\t"
    "s_add_u32 s69, s69, 8\n\t"
    "s_sub_u32 s44, s69, 64\n\t"
    "s_cmp_ge_u32 s69, 64\n\t"
    "s_cselect_b32 s69, s44, s69\n\t"
    "ds_read_b128 v[84:87], v148 offset:21504\n\t"
    "ds_read_b128 v[88:91], v148 offset:22528\n\t"
    "v_add_f32 v249, v76, v77\n\t"
    "s_mul_i32 s44, s69, 1040\n\t"
    "s_add_u32 m0, s44, 63440\n\t"
    "s_lshl_b32 s44, s69, 11\n\t"
    "buffer_load_dwordx4 v126, %15, s44 offen lds\n\t"
    "s_add_u32 s69, s69, 8\n\t"
    "s_sub_u32 s44, s69, 64\n\t"
    "s_cmp_ge_u32 s69, 64\n\t"
    "s_cselect_b32 s69, s44, s69\n\t"
    "s_mul_i32 s44, s69, 1040\n\t"
    "s_add_u32 m0, s44, 63440\n\t"
    "s_lshl_b32 s44, s69, 11\n\t"
    "buffer_load_dwordx4 v126, %15, s44 offen lds\n\t"
    "s_add_u32 s69, s69, 8\n\t"
    "s_sub_u32 s44, s69, 64\n\t"
    "s_cmp_ge_u32 s69, 64\n\t"
    "s_cselect_b32 s69, s44, s69\n\t"
    "v_pk_mul_f32 v[64:65], v[228:229], v[228:229]\n\t"
    "v_pk_mul_f32 v[66:67], v[230:231], v[230:231]\n\t"
    "v_pk_fma_f32 v[64:65], v[64:65], v[82:83], v[80:81]\n\t"
    "v_pk_fma_f32 v[66:67], v[66:67], v[82:83], v[80:81]\n\t"
    "v_pk_mul_f32 v[64:65], v[64:65], v[228:229]\n\t"
    "v_pk_mul_f32 v[66:67], v[66:67], v[230:231]\n\t"
    "v_exp_f32 v68, v64\n\t"
    "v_exp_f32 v69, v65\n\t"
    "v_exp_f32 v70, v66\n\t"
    "v_exp_f32 v71, v67\n\t"
    "v_pk_fma_f32 v[68:69], v[68:69], s[40:41], s[40:41]\n\t"
    "v_pk_fma_f32 v[70:71], v[70:71], s[42:43], s[42:43]\n\t"
    "v_rcp_f32 v68, v68\n\t"
    "v_rcp_f32 v69, v69\n\t"
    "v_rcp_f32 v70, v70\n\t"
    "v_rcp_f32 v71, v71\n\t"
    "v_pk_mul_f32 v[68:69], v[68:69], v[228:229]\n\t"
    "v_pk_fma_f32 v[68:69], v[70:71], v[230:231], v[68:69]\n\t"
    "v_add_f32 v250, v68, v69\n\t"
    "s_mul_i32 s44, s69, 1040\n\t"
    "s_add_u32 m0, s44, 63440\n\t"
    "s_lshl_b32 s44, s69, 11\n\t"
    "buffer_load_dwordx4 v126, %15, s44 offen lds\n\t"
    "s_add_u32 s69, s69, 8\n\t"
    "s_sub_u32 s44, s69, 64\n\t"
    "s_cmp_ge_u32 s69, 64\n\t"
    "s_cselect_b32 s69, s44, s69\n\t"
    "s_mul_i32 s44, s69, 1040\n\t"
    "s_add_u32 m0, s44, 63440\n\t"
    "s_lshl_b32 s44, s69, 11\n\t"
    "buffer_load_dwordx4 v126, %15, s44 offen lds\n\t"
    "s_add_u32 s69, s69, 8\n\t"
    "s_sub_u32 s44, s69, 64\n\t"
    "s_cmp_ge_u32 s69, 64\n\t"
    "s_cselect_b32 s69, s44, s69\n\t"
    "v_pk_mul_f32 v[72:73], v[232:233], v[232:233]\n\t"
    "v_pk_mul_f32 v[74:75], v[234:235], v[234:235]\n\t"
    "v_pk_fma_f32 v[72:73], v[72:73], v[82:83], v[80:81]\n\t"
    "v_pk_fma_f32 v[74:75], v[74:75], v[82:83], v[80:81]\n\t"
    "v_pk_mul_f32 v[72:73], v[72:73], v[232:233]\n\t"
    "v_pk_mul_f32 v[74:75], v[74:75], v[234:235]\n\t"
    "v_exp_f32 v76, v72\n\t"
    "v_exp_f32 v77, v73\n\t"
    "v_exp_f32 v78, v74\n\t"
    "v_exp_f32 v79, v75\n\t"
    "v_pk_fma_f32 v[76:77], v[76:77], s[40:41], s[40:41]\n\t"
    "v_pk_fma_f32 v[78:79], v[78:79], s[42:43], s[42:43]\n\t"
    "v_rcp_f32 v76, v76\n\t"
    "v_rcp_f32 v77, v77\n\t"
    "v_rcp_f32 v78, v78\n\t"
    "v_rcp_f32 v79, v79\n\t"
    "v_pk_mul_f32 v[76:77], v[76:77], v[232:233]\n\t"
    "v_pk_fma_f32 v[76:77], v[78:79], v[234:235], v[76:77]\n\t"
    "v_add_f32 v251, v76, v77\n\t"
    "ds_read_b128 v[64:67], v148 offset:16384\n\t"
    "ds_read_b128 v[68:71], v148 offset:17408\n\t"
    "ds_read_b128 v[72:75], v148 offset:18432\n\t"
    "ds_read_b128 v[76:79], v148 offset:19456\n\t"
    "ds_read_b128 v[80:83], v148 offset:20480\n\t"
    "s_waitcnt vmcnt(0) lgkmcnt(0)\n\t"
    "s_mov_b32 s68, 130\n\t"
    "v_mad_u32_u24 v128, v128, s68, v152\n\t"
    "v_and_b32 v128, -16, v128\n\t"
    "v_mad_u32_u24 v129, v129, s68, v151\n\t"
    "v_and_b32 v129, -16, v129\n\t"
    "v_mad_u32_u24 v130, v130, s68, v151\n\t"
    "v_and_b32 v130, -16, v130\n\t"
    "v_mad_u32_u24 v132, v132, s68, v151\n\t"
    "v_and_b32 v132, -16, v132\n\t"
    "v_mad_u32_u24 v133, v133, s68, v151\n\t"
    "v_and_b32 v133, -16, v133\n\t"
    "v_mad_u32_u24 v134, v134, s68, v151\n\t"
    "v_and_b32 v134, -16, v134\n\t"
    "v_mad_u32_u24 v135, v135, s68, v151\n\t"
    "v_and_b32 v135, -16, v135\n\t"
    "v_lshrrev_b32 v122, 4, v147\n\t"
    "v_and_b32 v155, 15, v147\n\t"
    "v_lshlrev_b32 v155, 4, v155\n\t"
    "v_lshl_add_u32 v122, v122, 16, v155\n\t"
    "v_mov_b32 v150, 1\n\t"
    "v_mov_b32 v123, 0\n\t"
    "v_lshlrev_b32 v149, 20, v147\n\t"
    "v_add_u32 v149, s49, v149\n\t"
    "s_mov_b32 s52, s53\n\t"
    "s_cmp_ge_u32 %16, 4\n\t"
    "s_cbranch_scc1 L_st4_%=\n\t"
    "s_bfe_u32 s65, s84, 0xc0002\n\t"
    "s_add_u32 s65, s65, s36\n\t"
    "buffer_store_dword v236, v125, %12, s65 offen offset:0 sc1\n\t"
    "buffer_store_dword v237, v125, %12, s65 offen offset:256 sc1\n\t"
    "s_bfe_u32 s65, s85, 0xc0002\n\t"
    "s_add_u32 s65, s65, s36\n\t"
    "buffer_store_dword v238, v125, %12, s65 offen offset:0 sc1\n\t"
    "buffer_store_dword v239, v125, %12, s65 offen offset:256 sc1\n\t"
    "s_bfe_u32 s65, s86, 0xc0002\n\t"
    "s_add_u32 s65, s65, s36\n\t"
    "buffer_store_dword v240, v125, %12, s65 offen offset:0 sc1\n\t"
    "buffer_store_dword v241, v125, %12, s65 offen offset:256 sc1\n\t"
    "s_bfe_u32 s65, s87, 0xc0002\n\t"
    "s_add_u32 s65, s65, s36\n\t"
    "buffer_store_dword v242, v125, %12, s65 offen offset:0 sc1\n\t"
    "buffer_store_dword v243, v125, %12, s65 offen offset:256 sc1\n\t"
    "s_bfe_u32 s65, s88, 0xc0002\n\t"
    "s_add_u32 s65, s65, s36\n\t"
    "buffer_store_dword v244, v125, %12, s65 offen offset:0 sc1\n\t"
    "buffer_store_dword v245, v125, %12, s65 offen offset:256 sc1\n\t"
    "s_bfe_u32 s65, s89, 0xc0002\n\t"
    "s_add_u32 s65, s65, s36\n\t"
    "buffer_store_dword v246, v125, %12, s65 offen offset:0 sc1\n\t"
    "buffer_store_dword v247, v125, %12, s65 offen offset:256 sc1\n\t"
    "s_bfe_u32 s65, s90, 0xc0002\n\t"
    "s_add_u32 s65, s65, s36\n\t"
    "buffer_store_dword v248, v125, %12, s65 offen offset:0 sc1\n\t"
    "buffer_store_dword v249, v125, %12, s65 offen offset:256 sc1\n\t"
    "s_bfe_u32 s65, s91, 0xc0002\n\t"
    "s_add_u32 s65, s65, s36\n\t"
    "buffer_store_dword v250, v125, %12, s65 offen offset:0 sc1\n\t"
    "buffer_store_dword v251, v125, %12, s65 offen offset:256 sc1\n\t"
    "s_sub_u32 s61, s64, 3\n\t"
    "L_st4_%=:\n\t"
    "s_barrier\n\t"
    "s_mov_b32 s68, 130\n\t"
    "ds_read_b128 v[156:159], v128 offset:0\n\t"
    "ds_read_b128 v[160:163], v128 offset:64\n\t"
    "ds_read_b128 v[164:167], v129 offset:0\n\t"
    "ds_read_b128 v[168:171], v129 offset:64\n\t"
    "ds_read_b128 v[172:175], v130 offset:0\n\t"
    "ds_read_b128 v[176:179], v130 offset:64\n\t"
    "ds_read_b128 v[180:183], v132 offset:0\n\t"
    "ds_read_b128 v[184:187], v132 offset:64\n\t"
    "ds_read_b128 v[188:191], v133 offset:0\n\t"
    "ds_read_b128 v[192:195], v133 offset:64\n\t"
    "ds_read_b128 v[196:199], v134 offset:0\n\t"
    "ds_read_b128 v[200:203], v134 offset:64\n\t"
    "ds_read_b128 v[204:207], v135 offset:0\n\t"
    "ds_read_b128 v[208:211], v135 offset:64\n\t"
    "s_setprio 1\n\t"
    "L_pnn_%=:\n\t"
    "s_mul_i32 s69, s52, 272\n\t"
    "s_add_u32 s69, s69, 130048\n\t"
    "s_mul_i32 s66, s55, 400\n\t"
    "s_waitcnt lgkmcnt(8)\n\t"
    "v_mfma_f32_16x16x32_bf16 v[92:95], v[156:159], v[156:159], 0\n\t"
    "v_add_u32 v147, s69, v146\n\t"
    "v_fmac_f32 v121, v108, v72\n\t"
    "ds_add_rtn_u32 v148, v149, v150\n\t"
    "v_mfma_f32_16x16x32_bf16 v[92:95], v[160:163], v[160:163], v[92:95]\n\t"
    "v_fmac_f32 v120, v109, v73\n\t"
    "v_fmac_f32 v121, v110, v74\n\t"
    "buffer_load_dwordx3 v[128:130], v153, s[96:99], s66 offen\n\t"
    "v_mfma_f32_16x16x32_bf16 v[96:99], v[164:167], v[164:167], 0\n\t"
    "v_fmac_f32 v120, v111, v75\n\t"
    "v_fmac_f32 v121, v112, v76\n\t"
    "v_mfma_f32_16x16x32_bf16 v[96:99], v[168:171], v[168:171], v[96:99]\n\t"
    "v_fmac_f32 v120, v113, v77\n\t"
    "v_fmac_f32 v121, v114, v78\n\t"
    "buffer_load_dwordx4 v[132:135], v153, s[96:99], s66 offen offset:12\n\t"
    "v_mfma_f32_16x16x32_bf16 v[100:103], v[164:167], v[172:175], 0\n\t"
    "v_fmac_f32 v120, v115, v79\n\t"
    "v_fmac_f32 v121, v116, v80\n\t"
    "v_mfma_f32_16x16x32_bf16 v[100:103], v[168:171], v[176:179], v[100:103]\n\t"
    "v_fmac_f32 v120, v117, v81\n\t"
    "v_fmac_f32 v121, v118, v82\n\t"
    "s_waitcnt vmcnt(3)\n\t"
    "s_cmp_ge_u32 s55, s61\n\t"
    "s_cbranch_scc1 L_req0_%=\n\t"
    "L_back0_%=:\n\t"
    "v_mfma_f32_16x16x32_bf16 v[104:107], v[172:175], v[172:175], 0\n\t"
    "v_mad_u32_u24 v136, v136, s68, v152\n\t"
    "v_and_b32 v136, -16, v136\n\t"
    "v_mfma_f32_16x16x32_bf16 v[104:107], v[176:179], v[176:179], v[104:107]\n\t"
    "v_fmac_f32 v120, v119, v83\n\t"
    "v_add_f32 v154, v120, v121\n\t"
    "ds_write_b32 v147, v154\n\t"
    "v_mfma_f32_16x16x32_bf16 v[108:111], v[156:159], v[164:167], 0\n\t"
    "v_fma_f32 v120, v92, v84, v144\n\t"
    "v_mul_f32 v121, v96, v0\n\t"
    "v_mfma_f32_16x16x32_bf16 v[108:111], v[160:163], v[168:171], v[108:111]\n\t"
    "v_fmac_f32 v120, v97, v1\n\t"
    "v_fmac_f32 v121, v98, v2\n\t"
    "s_waitcnt lgkmcnt(8)\n\t"
    "v_mfma_f32_16x16x32_bf16 v[112:115], v[164:167], v[180:183], 0\n\t"
    "v_fmac_f32 v120, v99, v3\n\t"
    "v_fmac_f32 v121, v100, v4\n\t"
    "v_mfma_f32_16x16x32_bf16 v[112:115], v[168:171], v[184:187], v[112:115]\n\t"
    "v_fmac_f32 v120, v101, v5\n\t"
    "v_fmac_f32 v121, v102, v6\n\t"
    "v_mfma_f32_16x16x32_bf16 v[116:119], v[172:175], v[180:183], 0\n\t"
    "v_fmac_f32 v120, v103, v7\n\t"
    "v_fmac_f32 v121, v104, v8\n\t"
    "v_mfma_f32_16x16x32_bf16 v[116:119], v[176:179], v[184:187], v[116:119]\n\t"
    "v_fmac_f32 v120, v105, v9\n\t"
    "v_fmac_f32 v121, v106, v10\n\t"
    "ds_read_b128 v[212:215], v136 offset:0\n\t"
    "ds_read_b128 v[216:219], v136 offset:64\n\t"
    "v_mfma_f32_16x16x32_bf16 v[92:95], v[180:183], v[180:183], 0\n\t"
    "v_fmac_f32 v120, v107, v11\n\t"
    "v_mad_u32_u24 v137, v137, s68, v151\n\t"
    "v_mfma_f32_16x16x32_bf16 v[92:95], v[184:187], v[184:187], v[92:95]\n\t"
    "v_and_b32 v137, -16, v137\n\t"
    "v_fmac_f32 v121, v108, v85\n\t"
    "v_mfma_f32_16x16x32_bf16 v[96:99], v[156:159], v[172:175], 0\n\t"
    "v_fmac_f32 v120, v112, v12\n\t"
    "v_fmac_f32 v121, v113, v13\n\t"
    "v_mfma_f32_16x16x32_bf16 v[96:99], v[160:163], v[176:179], v[96:99]\n\t"
    "v_fmac_f32 v120, v114, v14\n\t"
    "v_fmac_f32 v121, v115, v15\n\t"
    "s_lshl_b32 s51, s55, 8\n\t"
    "s_add_u32 s51, s51, s50\n\t"
    "s_waitcnt lgkmcnt(8)\n\t"
    "v_mfma_f32_16x16x32_bf16 v[100:103], v[164:167], v[188:191], 0\n\t"
    "v_mad_u32_u24 v138, v138, s68, v151\n\t"
    "v_and_b32 v138, -16, v138\n\t"
    "v_mfma_f32_16x16x32_bf16 v[100:103], v[168:171], v[192:195], v[100:103]\n\t"
    "v_fmac_f32 v120, v116, v16\n\t"
    "v_fmac_f32 v121, v117, v17\n\t"
    "buffer_load_dword v144, v146, %12, s51 offen sc1\n\t"
    "v_mfma_f32_16x16x32_bf16 v[104:107], v[172:175], v[188:191], 0\n\t"
    "v_fmac_f32 v120, v118, v18\n\t"
    "v_fmac_f32 v121, v119, v19\n\t"
    "v_mfma_f32_16x16x32_bf16 v[104:107], v[176:179], v[192:195], v[104:107]\n\t"
    "v_fmac_f32 v120, v92, v20\n\t"
    "v_fmac_f32 v121, v93, v21\n\t"
    "s_waitcnt lgkmcnt(6)\n\t"
    "v_mfma_f32_16x16x32_bf16 v[108:111], v[164:167], v[196:199], 0\n\t"
    "v_fmac_f32 v120, v94, v22\n\t"
    "v_fmac_f32 v121, v95, v23\n\t"
    "v_mfma_f32_16x16x32_bf16 v[108:111], v[168:171], v[200:203], v[108:111]\n\t"
    "v_fmac_f32 v120, v96, v86\n\t"
    "v_mad_u32_u24 v140, v140, s68, v151\n\t"
    "v_mfma_f32_16x16x32_bf16 v[112:115], v[156:159], v[180:183], 0\n\t"
    "v_fmac_f32 v121, v100, v24\n\t"
    "v_fmac_f32 v120, v101, v25\n\t"
    "v_mfma_f32_16x16x32_bf16 v[112:115], v[160:163], v[184:187], v[112:115]\n\t"
    "v_fmac_f32 v121, v102, v26\n\t"
    "v_fmac_f32 v120, v103, v27\n\t"
    "s_waitcnt lgkmcnt(3)\n\t"
    "v_mfma_f32_16x16x32_bf16 v[116:119], v[164:167], v[204:207], 0\n\t"
    "v_readfirstlane_b32 s60, v148\n\t"
    "v_and_b32 v140, -16, v140\n\t"
    "v_mfma_f32_16x16x32_bf16 v[116:119], v[168:171], v[208:211], v[116:119]\n\t"
    "v_fmac_f32 v121, v104, v28\n\t"
    "v_fmac_f32 v120, v105, v29\n\t"
    "ds_read_b128 v[164:167], v137 offset:0\n\t"
    "ds_read_b128 v[168:171], v137 offset:64\n\t"
    "v_mfma_f32_16x16x32_bf16 v[92:95], v[172:175], v[196:199], 0\n\t"
    "v_fmac_f32 v121, v106, v30\n\t"
    "v_fmac_f32 v120, v107, v31\n\t"
    "v_mfma_f32_16x16x32_bf16 v[92:95], v[176:179], v[200:203], v[92:95]\n\t"
    "v_fmac_f32 v121, v108, v32\n\t"
    "v_fmac_f32 v120, v109, v33\n\t"
    "v_mfma_f32_16x16x32_bf16 v[96:99], v[172:175], v[204:207], 0\n\t"
    "v_fmac_f32 v121, v110, v34\n\t"
    "v_fmac_f32 v120, v111, v35\n\t"
    "v_mfma_f32_16x16x32_bf16 v[96:99], v[176:179], v[208:211], v[96:99]\n\t"
    "v_fmac_f32 v121, v112, v87\n\t"
    "v_mad_u32_u24 v141, v141, s68, v151\n\t"
    "ds_read_b128 v[172:175], v138 offset:0\n\t"
    "ds_read_b128 v[176:179], v138 offset:64\n\t"
    "v_mfma_f32_16x16x32_bf16 v[100:103], v[156:159], v[188:191], 0\n\t"
    "v_fmac_f32 v120, v116, v36\n\t"
    "v_fmac_f32 v121, v117, v37\n\t"
    "v_mfma_f32_16x16x32_bf16 v[100:103], v[160:163], v[192:195], v[100:103]\n\t"
    "v_fmac_f32 v120, v118, v38\n\t"
    "v_fmac_f32 v121, v119, v39\n\t"
    "v_mfma_f32_16x16x32_bf16 v[104:107], v[180:183], v[188:191], 0\n\t"
    "v_and_b32 v141, -16, v141\n\t"
    "v_fmac_f32 v120, v92, v40\n\t"
    "v_mfma_f32_16x16x32_bf16 v[104:107], v[184:187], v[192:195], v[104:107]\n\t"
    "v_fmac_f32 v121, v93, v41\n\t"
    "v_fmac_f32 v120, v94, v42\n\t"
    "v_mfma_f32_16x16x32_bf16 v[108:111], v[180:183], v[196:199], 0\n\t"
    "v_fmac_f32 v121, v95, v43\n\t"
    "v_fmac_f32 v120, v96, v44\n\t"
    "v_mfma_f32_16x16x32_bf16 v[108:111], v[184:187], v[200:203], v[108:111]\n\t"
    "v_fmac_f32 v121, v97, v45\n\t"
    "v_fmac_f32 v120, v98, v46\n\t"
    "v_mfma_f32_16x16x32_bf16 v[112:115], v[180:183], v[204:207], 0\n\t"
    "v_fmac_f32 v121, v99, v47\n\t"
    "v_fmac_f32 v120, v100, v88\n\t"
    "v_mfma_f32_16x16x32_bf16 v[112:115], v[184:187], v[208:211], v[112:115]\n\t"
    "v_mad_u32_u24 v142, v142, s68, v151\n\t"
    "v_and_b32 v142, -16, v142\n\t"
    "ds_read_b128 v[180:183], v140 offset:0\n\t"
    "ds_read_b128 v[184:187], v140 offset:64\n\t"
    "s_cmp_eq_u32 s45, 1\n\t"
    "s_cbranch_scc1 L_sig_%=\n\t"
    "L_sigb_%=:\n\t"
    "v_mfma_f32_16x16x32_bf16 v[116:119], v[156:159], v[196:199], 0\n\t"
    "v_fmac_f32 v121, v104, v48\n\t"
    "v_fmac_f32 v120, v105, v49\n\t"
    "v_mfma_f32_16x16x32_bf16 v[116:119], v[160:163], v[200:203], v[116:119]\n\t"
    "v_fmac_f32 v121, v106, v50\n\t"
    "v_fmac_f32 v120, v107, v51\n\t"
    "v_mfma_f32_16x16x32_bf16 v[92:95], v[188:191], v[188:191], 0\n\t"
    "v_mad_u32_u24 v143, v143, s68, v151\n\t"
    "v_and_b32 v143, -16, v143\n\t"
    "v_mfma_f32_16x16x32_bf16 v[92:95], v[192:195], v[192:195], v[92:95]\n\t"
    "v_fmac_f32 v121, v108, v52\n\t"
    "v_fmac_f32 v120, v109, v53\n\t"
    "v_mfma_f32_16x16x32_bf16 v[96:99], v[188:191], v[196:199], 0\n\t"
    "v_fmac_f32 v121, v110, v54\n\t"
    "v_fmac_f32 v120, v111, v55\n\t"
    "v_mfma_f32_16x16x32_bf16 v[96:99], v[192:195], v[200:203], v[96:99]\n\t"
    "v_fmac_f32 v121, v112, v56\n\t"
    "v_fmac_f32 v120, v113, v57\n\t"
    "v_mfma_f32_16x16x32_bf16 v[100:103], v[188:191], v[204:207], 0\n\t"
    "v_fmac_f32 v121, v114, v58\n\t"
    "v_fmac_f32 v120, v115, v59\n\t"
    "v_mfma_f32_16x16x32_bf16 v[100:103], v[192:195], v[208:211], v[100:103]\n\t"
    "v_fmac_f32 v121, v116, v89\n\t"
    "ds_read_b128 v[188:191], v141 offset:0\n\t"
    "ds_read_b128 v[192:195], v141 offset:64\n\t"
    "v_mfma_f32_16x16x32_bf16 v[104:107], v[156:159], v[204:207], 0\n\t"
    "v_fmac_f32 v120, v92, v60\n\t"
    "v_fmac_f32 v121, v93, v61\n\t"
    "v_mfma_f32_16x16x32_bf16 v[104:107], v[160:163], v[208:211], v[104:107]\n\t"
    "v_fmac_f32 v120, v94, v62\n\t"
    "v_fmac_f32 v121, v95, v63\n\t"
    "v_mfma_f32_16x16x32_bf16 v[108:111], v[196:199], v[196:199], 0\n\t"
    "v_fmac_f32 v120, v96, v64\n\t"
    "v_fmac_f32 v121, v97, v65\n\t"
    "v_mfma_f32_16x16x32_bf16 v[108:111], v[200:203], v[200:203], v[108:111]\n\t"
    "v_fmac_f32 v120, v98, v66\n\t"
    "v_fmac_f32 v121, v99, v67\n\t"
    "v_mfma_f32_16x16x32_bf16 v[112:115], v[196:199], v[204:207], 0\n\t"
    "v_fmac_f32 v120, v100, v68\n\t"
    "v_fmac_f32 v121, v101, v69\n\t"
    "v_mfma_f32_16x16x32_bf16 v[112:115], v[200:203], v[208:211], v[112:115]\n\t"
    "v_fmac_f32 v120, v102, v70\n\t"
    "v_fmac_f32 v121, v103, v71\n\t"
    "ds_read_b128 v[196:199], v142 offset:0\n\t"
    "ds_read_b128 v[200:203], v142 offset:64\n\t"
    "v_mfma_f32_16x16x32_bf16 v[116:119], v[204:207], v[204:207], 0\n\t"
    "v_fmac_f32 v120, v104, v90\n\t"
    "v_mfma_f32_16x16x32_bf16 v[116:119], v[208:211], v[208:211], v[116:119]\n\t"
    "ds_read_b128 v[204:207], v143 offset:0\n\t"
    "ds_read_b128 v[208:211], v143 offset:64\n\t"
    "s_mov_b32 s52, s53\n\t"
    "s_mov_b32 s53, s54\n\t"
    "s_mov_b32 s54, s55\n\t"
    "s_mov_b32 s55, s60\n\t"
    "s_cmp_lt_u32 s54, s64\n\t"
    "s_cbranch_scc1 L_pr0_%=\n\t"
    "s_setprio 0\n\t"
    "L_pr0_%=:\n\t"
    "s_cmp_lt_u32 s53, s64\n\t"
    "s_cbranch_scc0 L_done_%=\n\t"
    "s_mul_i32 s69, s52, 272\n\t"
    "s_add_u32 s69, s69, 130048\n\t"
    "s_mul_i32 s66, s55, 400\n\t"
    "s_waitcnt lgkmcnt(8)\n\t"
    "v_mfma_f32_16x16x32_bf16 v[92:95], v[212:215], v[212:215], 0\n\t"
    "v_add_u32 v147, s69, v146\n\t"
    "v_fmac_f32 v121, v108, v72\n\t"
    "ds_add_rtn_u32 v148, v149, v150\n\t"
    "v_mfma_f32_16x16x32_bf16 v[92:95], v[216:219], v[216:219], v[92:95]\n\t"
    "v_fmac_f32 v120, v109, v73\n\t"
    "v_fmac_f32 v121, v110, v74\n\t"
    "buffer_load_dwordx3 v[136:138], v153, s[96:99], s66 offen\n\t"
    "v_mfma_f32_16x16x32_bf16 v[96:99], v[164:167], v[164:167], 0\n\t"
    "v_fmac_f32 v120, v111, v75\n\t"
    "v_fmac_f32 v121, v112, v76\n\t"
    "v_mfma_f32_16x16x32_bf16 v[96:99], v[168:171], v[168:171], v[96:99]\n\t"
    "v_fmac_f32 v120, v113, v77\n\t"
    "v_fmac_f32 v121, v114, v78\n\t"
    "buffer_load_dwordx4 v[140:143], v153, s[96:99], s66 offen offset:12\n\t"
    "v_mfma_f32_16x16x32_bf16 v[100:103], v[164:167], v[172:175], 0\n\t"
    "v_fmac_f32 v120, v115, v79\n\t"
    "v_fmac_f32 v121, v116, v80\n\t"
    "v_mfma_f32_16x16x32_bf16 v[100:103], v[168:171], v[176:179], v[100:103]\n\t"
    "v_fmac_f32 v120, v117, v81\n\t"
    "v_fmac_f32 v121, v118, v82\n\t"
    "s_waitcnt vmcnt(3)\n\t"
    "s_cmp_ge_u32 s55, s61\n\t"
    "s_cbranch_scc1 L_req1_%=\n\t"
    "L_back1_%=:\n\t"
    "v_mfma_f32_16x16x32_bf16 v[104:107], v[172:175], v[172:175], 0\n\t"
    "v_mad_u32_u24 v128, v128, s68, v152\n\t"
    "v_and_b32 v128, -16, v128\n\t"
    "v_mfma_f32_16x16x32_bf16 v[104:107], v[176:179], v[176:179], v[104:107]\n\t"
    "v_fmac_f32 v120, v119, v83\n\t"
    "v_add_f32 v154, v120, v121\n\t"
    "ds_write_b32 v147, v154\n\t"
    "v_mfma_f32_16x16x32_bf16 v[108:111], v[212:215], v[164:167], 0\n\t"
    "v_fma_f32 v120, v92, v84, v145\n\t"
    "v_mul_f32 v121, v96, v0\n\t"
    "v_mfma_f32_16x16x32_bf16 v[108:111], v[216:219], v[168:171], v[108:111]\n\t"
    "v_fmac_f32 v120, v97, v1\n\t"
    "v_fmac_f32 v121, v98, v2\n\t"
    "s_waitcnt lgkmcnt(8)\n\t"
    "v_mfma_f32_16x16x32_bf16 v[112:115], v[164:167], v[180:183], 0\n\t"
    "v_fmac_f32 v120, v99, v3\n\t"
    "v_fmac_f32 v121, v100, v4\n\t"
    "v_mfma_f32_16x16x32_bf16 v[112:115], v[168:171], v[184:187], v[112:115]\n\t"
    "v_fmac_f32 v120, v101, v5\n\t"
    "v_fmac_f32 v121, v102, v6\n\t"
    "v_mfma_f32_16x16x32_bf16 v[116:119], v[172:175], v[180:183], 0\n\t"
    "v_fmac_f32 v120, v103, v7\n\t"
    "v_fmac_f32 v121, v104, v8\n\t"
    "v_mfma_f32_16x16x32_bf16 v[116:119], v[176:179], v[184:187], v[116:119]\n\t"
    "v_fmac_f32 v120, v105, v9\n\t"
    "v_fmac_f32 v121, v106, v10\n\t"
    "ds_read_b128 v[156:159], v128 offset:0\n\t"
    "ds_read_b128 v[160:163], v128 offset:64\n\t"
    "v_mfma_f32_16x16x32_bf16 v[92:95], v[180:183], v[180:183], 0\n\t"
    "v_fmac_f32 v120, v107, v11\n\t"
    "v_mad_u32_u24 v129, v129, s68, v151\n\t"
    "v_mfma_f32_16x16x32_bf16 v[92:95], v[184:187], v[184:187], v[92:95]\n\t"
    "v_and_b32 v129, -16, v129\n\t"
    "v_fmac_f32 v121, v108, v85\n\t"
    "v_mfma_f32_16x16x32_bf16 v[96:99], v[212:215], v[172:175], 0\n\t"
    "v_fmac_f32 v120, v112, v12\n\t"
    "v_fmac_f32 v121, v113, v13\n\t"
    "v_mfma_f32_16x16x32_bf16 v[96:99], v[216:219], v[176:179], v[96:99]\n\t"
    "v_fmac_f32 v120, v114, v14\n\t"
    "v_fmac_f32 v121, v115, v15\n\t"
    "s_lshl_b32 s51, s55, 8\n\t"
    "s_add_u32 s51, s51, s50\n\t"
    "s_waitcnt lgkmcnt(8)\n\t"
    "v_mfma_f32_16x16x32_bf16 v[100:103], v[164:167], v[188:191], 0\n\t"
    "v_mad_u32_u24 v130, v130, s68, v151\n\t"
    "v_and_b32 v130, -16, v130\n\t"
    "v_mfma_f32_16x16x32_bf16 v[100:103], v[168:171], v[192:195], v[100:103]\n\t"
    "v_fmac_f32 v120, v116, v16\n\t"
    "v_fmac_f32 v121, v117, v17\n\t"
    "buffer_load_dword v145, v146, %12, s51 offen sc1\n\t"
    "v_mfma_f32_16x16x32_bf16 v[104:107], v[172:175], v[188:191], 0\n\t"
    "v_fmac_f32 v120, v118, v18\n\t"
    "v_fmac_f32 v121, v119, v19\n\t"
    "v_mfma_f32_16x16x32_bf16 v[104:107], v[176:179], v[192:195], v[104:107]\n\t"
    "v_fmac_f32 v120, v92, v20\n\t"
    "v_fmac_f32 v121, v93, v21\n\t"
    "s_waitcnt lgkmcnt(6)\n\t"
    "v_mfma_f32_16x16x32_bf16 v[108:111], v[164:167], v[196:199], 0\n\t"
    "v_fmac_f32 v120, v94, v22\n\t"
    "v_fmac_f32 v121, v95, v23\n\t"
    "v_mfma_f32_16x16x32_bf16 v[108:111], v[168:171], v[200:203], v[108:111]\n\t"
    "v_fmac_f32 v120, v96, v86\n\t"
    "v_mad_u32_u24 v132, v132, s68, v151\n\t"
    "v_mfma_f32_16x16x32_bf16 v[112:115], v[212:215], v[180:183], 0\n\t"
    "v_fmac_f32 v121, v100, v24\n\t"
    "v_fmac_f32 v120, v101, v25\n\t"
    "v_mfma_f32_16x16x32_bf16 v[112:115], v[216:219], v[184:187], v[112:115]\n\t"
    "v_fmac_f32 v121, v102, v26\n\t"
    "v_fmac_f32 v120, v103, v27\n\t"
    "s_waitcnt lgkmcnt(3)\n\t"
    "v_mfma_f32_16x16x32_bf16 v[116:119], v[164:167], v[204:207], 0\n\t"
    "v_readfirstlane_b32 s60, v148\n\t"
    "v_and_b32 v132, -16, v132\n\t"
    "v_mfma_f32_16x16x32_bf16 v[116:119], v[168:171], v[208:211], v[116:119]\n\t"
    "v_fmac_f32 v121, v104, v28\n\t"
    "v_fmac_f32 v120, v105, v29\n\t"
    "ds_read_b128 v[164:167], v129 offset:0\n\t"
    "ds_read_b128 v[168:171], v129 offset:64\n\t"
    "v_mfma_f32_16x16x32_bf16 v[92:95], v[172:175], v[196:199], 0\n\t"
    "v_fmac_f32 v121, v106, v30\n\t"
    "v_fmac_f32 v120, v107, v31\n\t"
    "v_mfma_f32_16x16x32_bf16 v[92:95], v[176:179], v[200:203], v[92:95]\n\t"
    "v_fmac_f32 v121, v108, v32\n\t"
    "v_fmac_f32 v120, v109, v33\n\t"
    "v_mfma_f32_16x16x32_bf16 v[96:99], v[172:175], v[204:207], 0\n\t"
    "v_fmac_f32 v121, v110, v34\n\t"
    "v_fmac_f32 v120, v111, v35\n\t"
    "v_mfma_f32_16x16x32_bf16 v[96:99], v[176:179], v[208:211], v[96:99]\n\t"
    "v_fmac_f32 v121, v112, v87\n\t"
    "v_mad_u32_u24 v133, v133, s68, v151\n\t"
    "ds_read_b128 v[172:175], v130 offset:0\n\t"
    "ds_read_b128 v[176:179], v130 offset:64\n\t"
    "v_mfma_f32_16x16x32_bf16 v[100:103], v[212:215], v[188:191], 0\n\t"
    "v_fmac_f32 v120, v116, v36\n\t"
    "v_fmac_f32 v121, v117, v37\n\t"
    "v_mfma_f32_16x16x32_bf16 v[100:103], v[216:219], v[192:195], v[100:103]\n\t"
    "v_fmac_f32 v120, v118, v38\n\t"
    "v_fmac_f32 v121, v119, v39\n\t"
    "v_mfma_f32_16x16x32_bf16 v[104:107], v[180:183], v[188:191], 0\n\t"
    "v_and_b32 v133, -16, v133\n\t"
    "v_fmac_f32 v120, v92, v40\n\t"
    "v_mfma_f32_16x16x32_bf16 v[104:107], v[184:187], v[192:195], v[104:107]\n\t"
    "v_fmac_f32 v121, v93, v41\n\t"
    "v_fmac_f32 v120, v94, v42\n\t"
    "v_mfma_f32_16x16x32_bf16 v[108:111], v[180:183], v[196:199], 0\n\t"
    "v_fmac_f32 v121, v95, v43\n\t"
    "v_fmac_f32 v120, v96, v44\n\t"
    "v_mfma_f32_16x16x32_bf16 v[108:111], v[184:187], v[200:203], v[108:111]\n\t"
    "v_fmac_f32 v121, v97, v45\n\t"
    "v_fmac_f32 v120, v98, v46\n\t"
    "v_mfma_f32_16x16x32_bf16 v[112:115], v[180:183], v[204:207], 0\n\t"
    "v_fmac_f32 v121, v99, v47\n\t"
    "v_fmac_f32 v120, v100, v88\n\t"
    "v_mfma_f32_16x16x32_bf16 v[112:115], v[184:187], v[208:211], v[112:115]\n\t"
    "v_mad_u32_u24 v134, v134, s68, v151\n\t"
    "v_and_b32 v134, -16, v134\n\t"
    "ds_read_b128 v[180:183], v132 offset:0\n\t"
    "ds_read_b128 v[184:187], v132 offset:64\n\t"
    "v_mfma_f32_16x16x32_bf16 v[116:119], v[212:215], v[196:199], 0\n\t"
    "v_fmac_f32 v121, v104, v48\n\t"
    "v_fmac_f32 v120, v105, v49\n\t"
    "v_mfma_f32_16x16x32_bf16 v[116:119], v[216:219], v[200:203], v[116:119]\n\t"
    "v_fmac_f32 v121, v106, v50\n\t"
    "v_fmac_f32 v120, v107, v51\n\t"
    "v_mfma_f32_16x16x32_bf16 v[92:95], v[188:191], v[188:191], 0\n\t"
    "v_mad_u32_u24 v135, v135, s68, v151\n\t"
    "v_and_b32 v135, -16, v135\n\t"
    "v_mfma_f32_16x16x32_bf16 v[92:95], v[192:195], v[192:195], v[92:95]\n\t"
    "v_fmac_f32 v121, v108, v52\n\t"
    "v_fmac_f32 v120, v109, v53\n\t"
    "v_mfma_f32_16x16x32_bf16 v[96:99], v[188:191], v[196:199], 0\n\t"
    "v_fmac_f32 v121, v110, v54\n\t"
    "v_fmac_f32 v120, v111, v55\n\t"
    "v_mfma_f32_16x16x32_bf16 v[96:99], v[192:195], v[200:203], v[96:99]\n\t"
    "v_fmac_f32 v121, v112, v56\n\t"
    "v_fmac_f32 v120, v113, v57\n\t"
    "v_mfma_f32_16x16x32_bf16 v[100:103], v[188:191], v[204:207], 0\n\t"
    "v_fmac_f32 v121, v114, v58\n\t"
    "v_fmac_f32 v120, v115, v59\n\t"
    "v_mfma_f32_16x16x32_bf16 v[100:103], v[192:195], v[208:211], v[100:103]\n\t"
    "v_fmac_f32 v121, v116, v89\n\t"
    "ds_read_b128 v[188:191], v133 offset:0\n\t"
    "ds_read_b128 v[192:195], v133 offset:64\n\t"
    "v_mfma_f32_16x16x32_bf16 v[104:107], v[212:215], v[204:207], 0\n\t"
    "v_fmac_f32 v120, v92, v60\n\t"
    "v_fmac_f32 v121, v93, v61\n\t"
    "v_mfma_f32_16x16x32_bf16 v[104:107], v[216:219], v[208:211], v[104:107]\n\t"
    "v_fmac_f32 v120, v94, v62\n\t"
    "v_fmac_f32 v121, v95, v63\n\t"
    "v_mfma_f32_16x16x32_bf16 v[108:111], v[196:199], v[196:199], 0\n\t"
    "v_fmac_f32 v120, v96, v64\n\t"
    "v_fmac_f32 v121, v97, v65\n\t"
    "v_mfma_f32_16x16x32_bf16 v[108:111], v[200:203], v[200:203], v[108:111]\n\t"
    "v_fmac_f32 v120, v98, v66\n\t"
    "v_fmac_f32 v121, v99, v67\n\t"
    "v_mfma_f32_16x16x32_bf16 v[112:115], v[196:199], v[204:207], 0\n\t"
    "v_fmac_f32 v120, v100, v68\n\t"
    "v_fmac_f32 v121, v101, v69\n\t"
    "v_mfma_f32_16x16x32_bf16 v[112:115], v[200:203], v[208:211], v[112:115]\n\t"
    "v_fmac_f32 v120, v102, v70\n\t"
    "v_fmac_f32 v121, v103, v71\n\t"
    "ds_read_b128 v[196:199], v134 offset:0\n\t"
    "ds_read_b128 v[200:203], v134 offset:64\n\t"
    "v_mfma_f32_16x16x32_bf16 v[116:119], v[204:207], v[204:207], 0\n\t"
    "v_fmac_f32 v120, v104, v90\n\t"
    "v_mfma_f32_16x16x32_bf16 v[116:119], v[208:211], v[208:211], v[116:119]\n\t"
    "ds_read_b128 v[204:207], v135 offset:0\n\t"
    "ds_read_b128 v[208:211], v135 offset:64\n\t"
    "s_mov_b32 s52, s53\n\t"
    "s_mov_b32 s53, s54\n\t"
    "s_mov_b32 s54, s55\n\t"
    "s_mov_b32 s55, s60\n\t"
    "s_cmp_lt_u32 s54, s64\n\t"
    "s_cbranch_scc1 L_pr1_%=\n\t"
    "s_setprio 0\n\t"
    "L_pr1_%=:\n\t"
    "s_cmp_lt_u32 s53, s64\n\t"
    "s_cbranch_scc1 L_pnn_%=\n\t"
    "L_done_%=:\n\t"
    "s_mul_i32 s69, s52, 272\n\t"
    "s_add_u32 s69, s69, 130048\n\t"
    "v_add_u32 v147, s69, v146\n\t"
    "v_fmac_f32 v121, v108, v72\n\t"
    "v_fmac_f32 v120, v109, v73\n\t"
    "v_fmac_f32 v121, v110, v74\n\t"
    "v_fmac_f32 v120, v111, v75\n\t"
    "v_fmac_f32 v121, v112, v76\n\t"
    "v_fmac_f32 v120, v113, v77\n\t"
    "v_fmac_f32 v121, v114, v78\n\t"
    "v_fmac_f32 v120, v115, v79\n\t"
    "v_fmac_f32 v121, v116, v80\n\t"
    "v_fmac_f32 v120, v117, v81\n\t"
    "v_fmac_f32 v121, v118, v82\n\t"
    "v_fmac_f32 v120, v119, v83\n\t"
    "v_add_f32 v154, v120, v121\n\t"
    "ds_write_b32 v147, v154\n\t"
    "v_lshrrev_b32 v147, 2, v146\n\t"
    "s_mov_b64 s[92:93], %17\n\t"
    "s_and_b32 s93, s93, 0xffff\n\t"
    "s_mov_b32 s94, 32768\n\t"
    "s_mov_b32 s95, 0x20000\n\t"
    "s_lshl_b32 s44, s37, 1\n\t"
    "v_and_b32 v155, 15, v147\n\t"
    "v_lshl_add_u32 v155, %16, 4, v155\n\t"
    "v_lshrrev_b32 v154, 4, v147\n\t"
    "v_mul_u32_u24 v122, 272, v155\n\t"
    "v_lshl_add_u32 v122, v154, 6, v122\n\t"
    "v_add_u32 v122, 130048, v122\n\t"
    "v_mul_u32_u24 v154, 2560, v154\n\t"
    "v_lshl_add_u32 v154, v155, 2, v154\n\t"
    "v_add_u32 v154, 151808, v154\n\t"
    "v_cmp_le_u32 vcc, 48, v147\n\t"
    "v_cmp_gt_u32 s[46:47], s38, v155\n\t"
    "v_lshlrev_b32 v155, 1, v155\n\t"
    "s_and_b64 vcc, vcc, s[46:47]\n\t"
    "s_waitcnt lgkmcnt(0)\n\t"
    "s_barrier\n\t"
    "s_cbranch_vccz L_fin_%=\n\t"
    "ds_read_b128 v[92:95], v122 offset:0\n\t"
    "ds_read_b128 v[96:99], v122 offset:16\n\t"
    "ds_read_b128 v[100:103], v122 offset:32\n\t"
    "ds_read_b128 v[104:107], v122 offset:48\n\t"
    "ds_read2_b32 v[108:109], v154 offset0:0 offset1:80\n\t"
    "ds_read2_b32 v[110:111], v154 offset0:160 offset1:240\n\t"
    "v_add_u32 v154, 1280, v154\n\t"
    "ds_read2_b32 v[112:113], v154 offset0:0 offset1:80\n\t"
    "ds_read2_b32 v[114:115], v154 offset0:160 offset1:240\n\t"
    "s_waitcnt lgkmcnt(6)\n\t"
    "v_add_f32 v92, v92, v96\n\t"
    "v_add_f32 v93, v93, v97\n\t"
    "v_add_f32 v94, v94, v98\n\t"
    "v_add_f32 v95, v95, v99\n\t"
    "s_waitcnt lgkmcnt(5)\n\t"
    "v_add_f32 v92, v92, v100\n\t"
    "v_add_f32 v93, v93, v101\n\t"
    "v_add_f32 v94, v94, v102\n\t"
    "v_add_f32 v95, v95, v103\n\t"
    "s_waitcnt lgkmcnt(4)\n\t"
    "v_add_f32 v92, v92, v104\n\t"
    "v_add_f32 v93, v93, v105\n\t"
    "v_add_f32 v94, v94, v106\n\t"
    "v_add_f32 v95, v95, v107\n\t"
    "s_waitcnt lgkmcnt(2)\n\t"
    "v_add_f32 v92, v92, v108\n\t"
    "v_add_f32 v93, v93, v109\n\t"
    "v_add_f32 v94, v94, v110\n\t"
    "v_add_f32 v95, v95, v111\n\t"
    "s_waitcnt lgkmcnt(0)\n\t"
    "v_add_f32 v92, v92, v112\n\t"
    "v_add_f32 v93, v93, v113\n\t"
    "v_add_f32 v94, v94, v114\n\t"
    "v_add_f32 v95, v95, v115\n\t"
    "v_add_f32 v92, v92, v93\n\t"
    "v_add_f32 v94, v94, v95\n\t"
    "v_add_f32 v92, v92, v94\n\t"
    "v_mov_b32 v93, v92\n\t"
    "s_nop 1\n\t"
    "v_permlane16_swap_b32 v93, v92\n\t"
    "s_nop 1\n\t"
    "v_add_f32 v92, v92, v93\n\t"
    "v_mov_b32 v93, v92\n\t"
    "s_nop 1\n\t"
    "v_permlane32_swap_b32 v93, v92\n\t"
    "s_nop 1\n\t"
    "v_add_f32 v92, v92, v93\n\t"
    "v_add_f32 v92, s39, v92\n\t"
    "v_cvt_pk_bf16_f32 v92, v92, v92\n\t"
    "s_mov_b64 exec, vcc\n\t"
    "buffer_store_short v92, v155, s[92:95], s44 offen\n\t"
    "s_mov_b64 exec, -1\n\t"
    "s_branch L_fin_%=\n\t"
    "L_req0_%=:\n\t"
    "s_cmp_lg_u32 s61, 0\n\t"
    "s_cbranch_scc1 L_req0b_%=\n\t"
    "s_bfe_u32 s65, s84, 0xc0002\n\t"
    "s_add_u32 s65, s65, s36\n\t"
    "buffer_store_dword v236, v125, %12, s65 offen offset:0 sc1\n\t"
    "buffer_store_dword v237, v125, %12, s65 offen offset:256 sc1\n\t"
    "s_bfe_u32 s65, s85, 0xc0002\n\t"
    "s_add_u32 s65, s65, s36\n\t"
    "buffer_store_dword v238, v125, %12, s65 offen offset:0 sc1\n\t"
    "buffer_store_dword v239, v125, %12, s65 offen offset:256 sc1\n\t"
    "s_bfe_u32 s65, s86, 0xc0002\n\t"
    "s_add_u32 s65, s65, s36\n\t"
    "buffer_store_dword v240, v125, %12, s65 offen offset:0 sc1\n\t"
    "buffer_store_dword v241, v125, %12, s65 offen offset:256 sc1\n\t"
    "s_bfe_u32 s65, s87, 0xc0002\n\t"
    "s_add_u32 s65, s65, s36\n\t"
    "buffer_store_dword v242, v125, %12, s65 offen offset:0 sc1\n\t"
    "buffer_store_dword v243, v125, %12, s65 offen offset:256 sc1\n\t"
    "s_bfe_u32 s65, s88, 0xc0002\n\t"
    "s_add_u32 s65, s65, s36\n\t"
    "buffer_store_dword v244, v125, %12, s65 offen offset:0 sc1\n\t"
    "buffer_store_dword v245, v125, %12, s65 offen offset:256 sc1\n\t"
    "s_bfe_u32 s65, s89, 0xc0002\n\t"
    "s_add_u32 s65, s65, s36\n\t"
    "buffer_store_dword v246, v125, %12, s65 offen offset:0 sc1\n\t"
    "buffer_store_dword v247, v125, %12, s65 offen offset:256 sc1\n\t"
    "s_bfe_u32 s65, s90, 0xc0002\n\t"
    "s_add_u32 s65, s65, s36\n\t"
    "buffer_store_dword v248, v125, %12, s65 offen offset:0 sc1\n\t"
    "buffer_store_dword v249, v125, %12, s65 offen offset:256 sc1\n\t"
    "s_bfe_u32 s65, s91, 0xc0002\n\t"
    "s_add_u32 s65, s65, s36\n\t"
    "buffer_store_dword v250, v125, %12, s65 offen offset:0 sc1\n\t"
    "buffer_store_dword v251, v125, %12, s65 offen offset:256 sc1\n\t"
    "s_sub_u32 s61, s64, 3\n\t"
    "s_branch L_back0_%=\n\t"
    "L_req0b_%=:\n\t"
    "s_cmp_lg_u32 s61, 1\n\t"
    "s_cbranch_scc1 L_req0c_%=\n\t"
    "v_lshrrev_b32 v154, 16, v122\n\t"
    "v_lshl_add_u32 v154, %16, 2, v154\n\t"
    "v_mul_u32_u24 v154, 320, v154\n\t"
    "v_and_b32 v122, 0xf0, v122\n\t"
    "v_cmp_gt_u32 vcc, 64, v122\n\t"
    "v_add3_u32 v122, v122, v154, s45\n\t"
    "s_mov_b32 s61, -1\n\t"
    "v_add_f32 v124, v220, v224\n\t"
    "v_add_f32 v154, v228, v232\n\t"
    "v_add_f32 v124, v124, v154\n\t"
    "v_add_f32 v125, v221, v225\n\t"
    "v_add_f32 v154, v229, v233\n\t"
    "v_add_f32 v125, v125, v154\n\t"
    "v_add_f32 v126, v222, v226\n\t"
    "v_add_f32 v154, v230, v234\n\t"
    "v_add_f32 v126, v126, v154\n\t"
    "v_add_f32 v127, v223, v227\n\t"
    "v_add_f32 v154, v231, v235\n\t"
    "v_add_f32 v127, v127, v154\n\t"
    "ds_write_b128 v122, v[124:127] offset:0\n\t"
    "s_cmp_le_u32 s38, 64\n\t"
    "s_cbranch_scc1 L_back0_%=\n\t"
    "v_add_f32 v124, v236, v240\n\t"
    "v_add_f32 v154, v244, v248\n\t"
    "v_add_f32 v124, v124, v154\n\t"
    "v_add_f32 v125, v237, v241\n\t"
    "v_add_f32 v154, v245, v249\n\t"
    "v_add_f32 v125, v125, v154\n\t"
    "v_add_f32 v126, v238, v242\n\t"
    "v_add_f32 v154, v246, v250\n\t"
    "v_add_f32 v126, v126, v154\n\t"
    "v_add_f32 v127, v239, v243\n\t"
    "v_add_f32 v154, v247, v251\n\t"
    "v_add_f32 v127, v127, v154\n\t"
    "s_and_saveexec_b64 s[46:47], vcc\n\t"
    "ds_write_b128 v122, v[124:127] offset:256\n\t"
    "s_mov_b64 exec, s[46:47]\n\t"
    "s_branch L_back0_%=\n\t"
    "L_req0c_%=:\n\t"
    "s_lshl_b32 s46, %14, 7\n\t"
    "s_add_u32 s46, s46, 48245760\n\t"
    "s_mov_b32 s47, 1048576\n\t"
    "s_mov_b64 exec, 1\n\t"
    "s_cmp_ge_u32 s55, s64\n\t"
    "s_cbranch_scc1 L_pq0_%=\n\t"
    "buffer_load_dword v123, off, %12, s46 sc1\n\t"
    "s_mov_b64 exec, -1\n\t"
    "s_branch L_back0_%=\n\t"
    "L_pq0_%=:\n\t"
    "v_cmp_eq_u32 vcc, 0, v123\n\t"
    "s_cbranch_vccz L_in0_%=\n\t"
    "L_poll0_%=:\n\t"
    "buffer_load_dword v155, off, %12, s46 sc1\n\t"
    "s_sub_u32 s47, s47, 1\n\t"
    "s_cmp_eq_u32 s47, 0\n\t"
    "s_cbranch_scc1 L_in0_%=\n\t"
    "s_waitcnt vmcnt(0)\n\t"
    "v_cmp_eq_u32 vcc, 0, v155\n\t"
    "s_cbranch_vccz L_in0_%=\n\t"
    "s_sleep 8\n\t"
    "s_branch L_poll0_%=\n\t"
    "L_in0_%=:\n\t"
    "s_mov_b64 exec, -1\n\t"
    "s_lshl_b32 s46, s37, 2\n\t"
    "s_lshl_b32 s47, %16, 20\n\t"
    "s_add_u32 s46, s46, s47\n\t"
    "s_add_u32 s46, s46, 17825792\n\t"
    "v_and_b32 v155, 0xc0, v122\n\t"
    "v_lshl_add_u32 v155, v155, 24, v122\n\t"
    "buffer_load_dwordx4 v[220:223], v122, %12, s46 offen sc1\n\t"
    "s_add_u32 s46, s46, 0x40000\n\t"
    "buffer_load_dwordx4 v[224:227], v122, %12, s46 offen sc1\n\t"
    "s_add_u32 s46, s46, 0x40000\n\t"
    "buffer_load_dwordx4 v[228:231], v122, %12, s46 offen sc1\n\t"
    "s_add_u32 s46, s46, 0x40000\n\t"
    "buffer_load_dwordx4 v[232:235], v122, %12, s46 offen sc1\n\t"
    "s_add_u32 s46, s46, 0x40000\n\t"
    "s_mov_b32 s61, 1\n\t"
    "s_cmp_le_u32 s38, 64\n\t"
    "s_cbranch_scc1 L_back0_%=\n\t"
    "s_sub_u32 s46, s46, 0x40000\n\t"
    "buffer_load_dwordx4 v[248:251], v155, %12, s46 offen offset:256 sc1\n\t"
    "s_sub_u32 s46, s46, 0x40000\n\t"
    "buffer_load_dwordx4 v[244:247], v155, %12, s46 offen offset:256 sc1\n\t"
    "s_sub_u32 s46, s46, 0x40000\n\t"
    "buffer_load_dwordx4 v[240:243], v155, %12, s46 offen offset:256 sc1\n\t"
    "s_sub_u32 s46, s46, 0x40000\n\t"
    "buffer_load_dwordx4 v[236:239], v155, %12, s46 offen offset:256 sc1\n\t"
    "s_branch L_back0_%=\n\t"
    "L_req1_%=:\n\t"
    "s_cmp_lg_u32 s61, 1\n\t"
    "s_cbranch_scc1 L_req1c_%=\n\t"
    "v_lshrrev_b32 v154, 16, v122\n\t"
    "v_lshl_add_u32 v154, %16, 2, v154\n\t"
    "v_mul_u32_u24 v154, 320, v154\n\t"
    "v_and_b32 v122, 0xf0, v122\n\t"
    "v_cmp_gt_u32 vcc, 64, v122\n\t"
    "v_add3_u32 v122, v122, v154, s45\n\t"
    "s_mov_b32 s61, -1\n\t"
    "v_add_f32 v124, v220, v224\n\t"
    "v_add_f32 v154, v228, v232\n\t"
    "v_add_f32 v124, v124, v154\n\t"
    "v_add_f32 v125, v221, v225\n\t"
    "v_add_f32 v154, v229, v233\n\t"
    "v_add_f32 v125, v125, v154\n\t"
    "v_add_f32 v126, v222, v226\n\t"
    "v_add_f32 v154, v230, v234\n\t"
    "v_add_f32 v126, v126, v154\n\t"
    "v_add_f32 v127, v223, v227\n\t"
    "v_add_f32 v154, v231, v235\n\t"
    "v_add_f32 v127, v127, v154\n\t"
    "ds_write_b128 v122, v[124:127] offset:0\n\t"
    "s_cmp_le_u32 s38, 64\n\t"
    "s_cbranch_scc1 L_back1_%=\n\t"
    "v_add_f32 v124, v236, v240\n\t"
    "v_add_f32 v154, v244, v248\n\t"
    "v_add_f32 v124, v124, v154\n\t"
    "v_add_f32 v125, v237, v241\n\t"
    "v_add_f32 v154, v245, v249\n\t"
    "v_add_f32 v125, v125, v154\n\t"
    "v_add_f32 v126, v238, v242\n\t"
    "v_add_f32 v154, v246, v250\n\t"
    "v_add_f32 v126, v126, v154\n\t"
    "v_add_f32 v127, v239, v243\n\t"
    "v_add_f32 v154, v247, v251\n\t"
    "v_add_f32 v127, v127, v154\n\t"
    "s_and_saveexec_b64 s[46:47], vcc\n\t"
    "ds_write_b128 v122, v[124:127] offset:256\n\t"
    "s_mov_b64 exec, s[46:47]\n\t"
    "s_branch L_back1_%=\n\t"
    "L_req1c_%=:\n\t"
    "s_lshl_b32 s46, %14, 7\n\t"
    "s_add_u32 s46, s46, 48245760\n\t"
    "s_mov_b32 s47, 1048576\n\t"
    "s_mov_b64 exec, 1\n\t"
    "s_cmp_ge_u32 s55, s64\n\t"
    "s_cbranch_scc1 L_pq1_%=\n\t"
    "buffer_load_dword v123, off, %12, s46 sc1\n\t"
    "s_mov_b64 exec, -1\n\t"
    "s_branch L_back1_%=\n\t"
    "L_pq1_%=:\n\t"
    "v_cmp_eq_u32 vcc, 0, v123\n\t"
    "s_cbranch_vccz L_in1_%=\n\t"
    "L_poll1_%=:\n\t"
    "buffer_load_dword v155, off, %12, s46 sc1\n\t"
    "s_sub_u32 s47, s47, 1\n\t"
    "s_cmp_eq_u32 s47, 0\n\t"
    "s_cbranch_scc1 L_in1_%=\n\t"
    "s_waitcnt vmcnt(0)\n\t"
    "v_cmp_eq_u32 vcc, 0, v155\n\t"
    "s_cbranch_vccz L_in1_%=\n\t"
    "s_sleep 8\n\t"
    "s_branch L_poll1_%=\n\t"
    "L_in1_%=:\n\t"
    "s_mov_b64 exec, -1\n\t"
    "s_lshl_b32 s46, s37, 2\n\t"
    "s_lshl_b32 s47, %16, 20\n\t"
    "s_add_u32 s46, s46, s47\n\t"
    "s_add_u32 s46, s46, 17825792\n\t"
    "v_and_b32 v155, 0xc0, v122\n\t"
    "v_lshl_add_u32 v155, v155, 24, v122\n\t"
    "buffer_load_dwordx4 v[220:223], v122, %12, s46 offen sc1\n\t"
    "s_add_u32 s46, s46, 0x40000\n\t"
    "buffer_load_dwordx4 v[224:227], v122, %12, s46 offen sc1\n\t"
    "s_add_u32 s46, s46, 0x40000\n\t"
    "buffer_load_dwordx4 v[228:231], v122, %12, s46 offen sc1\n\t"
    "s_add_u32 s46, s46, 0x40000\n\t"
    "buffer_load_dwordx4 v[232:235], v122, %12, s46 offen sc1\n\t"
    "s_add_u32 s46, s46, 0x40000\n\t"
    "s_mov_b32 s61, 1\n\t"
    "s_cmp_le_u32 s38, 64\n\t"
    "s_cbranch_scc1 L_back1_%=\n\t"
    "s_sub_u32 s46, s46, 0x40000\n\t"
    "buffer_load_dwordx4 v[248:251], v155, %12, s46 offen offset:256 sc1\n\t"
    "s_sub_u32 s46, s46, 0x40000\n\t"
    "buffer_load_dwordx4 v[244:247], v155, %12, s46 offen offset:256 sc1\n\t"
    "s_sub_u32 s46, s46, 0x40000\n\t"
    "buffer_load_dwordx4 v[240:243], v155, %12, s46 offen offset:256 sc1\n\t"
    "s_sub_u32 s46, s46, 0x40000\n\t"
    "buffer_load_dwordx4 v[236:239], v155, %12, s46 offen offset:256 sc1\n\t"
    "s_branch L_back1_%=\n\t"
    "L_sig_%=:\n\t"
    "v_subrev_u32 v155, s49, v149\n\t"
    "v_add_u32 v155, 1024, v155\n\t"
    "s_waitcnt vmcnt(0)\n\t"
    "ds_add_rtn_u32 v148, v155, v150\n\t"
    "s_mov_b32 s45, 151808\n\t"
    "s_mov_b32 s46, 48234496\n\t"
    "v_mov_b32 v155, 1\n\t"
    "s_waitcnt lgkmcnt(0)\n\t"
    "v_readfirstlane_b32 s47, v148\n\t"
    "s_nop 0\n\t"
    "s_cmp_lg_u32 s47, 7\n\t"
    "s_cbranch_scc1 L_sigb_%=\n\t"
    "s_mov_b64 exec, 1\n\t"
    "buffer_atomic_add v155, off, %12, s46 sc0 sc1\n\t"
    "s_mov_b64 exec, -1\n\t"
    "s_waitcnt vmcnt(0)\n\t"
    "v_readfirstlane_b32 s47, v155\n\t"
    "v_lshlrev_b32 v155, 5, v146\n\t"
    "s_cmp_lg_u32 s47, 255\n\t"
    "s_cbranch_scc1 L_sigb_%=\n\t"
    "s_mov_b32 s46, 48245760\n\t"
    "buffer_store_dword v150, v155, %12, s46 offen sc1\n\t"
    "s_add_u32 s46, s46, 0x2000\n\t"
    "buffer_store_dword v150, v155, %12, s46 offen sc1\n\t"
    "s_add_u32 s46, s46, 0x2000\n\t"
    "buffer_store_dword v150, v155, %12, s46 offen sc1\n\t"
    "s_add_u32 s46, s46, 0x2000\n\t"
    "buffer_store_dword v150, v155, %12, s46 offen sc1\n\t"
    "s_add_u32 s46, s46, 0x2000\n\t"
    "s_branch L_sigb_%=\n\t"
    "L_fin_%=:\n\t"
    "s_branch L_pend_%=\n\t"
    "L_pub_%=:\n\t"
    "s_memrealtime s[46:47]\n\t"
    "s_lshl_b32 s49, %14, 2\n\t"
    "s_add_u32 s49, s49, 48244736\n\t"
    "s_waitcnt lgkmcnt(0)\n\t"
    "s_sub_u32 s47, s46, s44\n\t"
    "s_mul_i32 s47, s47, 236\n\t"
    "s_lshr_b32 s47, s47, 8\n\t"
    "s_add_u32 s46, s46, s47\n\t"
    "s_or_b32 s46, s46, 1\n\t"
    "v_mov_b32 v83, s46\n\t"
    "s_mov_b64 exec, 1\n\t"
    "buffer_store_dword v83, off, %12, s49 sc1\n\t"
    "s_mov_b64 exec, -1\n\t"
    "s_branch L_pubb_%=\n\t"
    "L_xw0_%=:\n\t"
    "s_mov_b32 s68, 1048576\n\t"
    "L_xv0_%=:\n\t"
    "s_sleep 4\n\t"
    "s_mov_b64 exec, 1\n\t"
    "buffer_load_dword v164, off, %12, s93 offset:0 sc1\n\t"
    "s_mov_b64 exec, -1\n\t"
    "s_sub_u32 s68, s68, 1\n\t"
    "s_cmp_eq_u32 s68, 0\n\t"
    "s_cbranch_scc1 L_xwb0_%=\n\t"
    "s_waitcnt vmcnt(0)\n\t"
    "v_readfirstlane_b32 s65, v164\n\t"
    "s_nop 0\n\t"
    "s_cmp_eq_u32 s65, 0\n\t"
    "s_cbranch_scc1 L_xv0_%=\n\t"
    "s_branch L_xwb0_%=\n\t"
    "L_xw1_%=:\n\t"
    "s_mov_b32 s68, 1048576\n\t"
    "L_xv1_%=:\n\t"
    "s_sleep 4\n\t"
    "s_mov_b64 exec, 1\n\t"
    "buffer_load_dword v164, off, %12, s93 offset:128 sc1\n\t"
    "s_mov_b64 exec, -1\n\t"
    "s_sub_u32 s68, s68, 1\n\t"
    "s_cmp_eq_u32 s68, 0\n\t"
    "s_cbranch_scc1 L_xwb1_%=\n\t"
    "s_waitcnt vmcnt(0)\n\t"
    "v_readfirstlane_b32 s65, v164\n\t"
    "s_nop 0\n\t"
    "s_cmp_eq_u32 s65, 0\n\t"
    "s_cbranch_scc1 L_xv1_%=\n\t"
    "s_branch L_xwb1_%=\n\t"
    "L_xw2_%=:\n\t"
    "s_mov_b32 s68, 1048576\n\t"
    "L_xv2_%=:\n\t"
    "s_sleep 4\n\t"
    "s_mov_b64 exec, 1\n\t"
    "buffer_load_dword v164, off, %12, s93 offset:256 sc1\n\t"
    "s_mov_b64 exec, -1\n\t"
    "s_sub_u32 s68, s68, 1\n\t"
    "s_cmp_eq_u32 s68, 0\n\t"
    "s_cbranch_scc1 L_xwb2_%=\n\t"
    "s_waitcnt vmcnt(0)\n\t"
    "v_readfirstlane_b32 s65, v164\n\t"
    "s_nop 0\n\t"
    "s_cmp_eq_u32 s65, 0\n\t"
    "s_cbranch_scc1 L_xv2_%=\n\t"
    "s_branch L_xwb2_%=\n\t"
    "L_xw3_%=:\n\t"
    "s_mov_b32 s68, 1048576\n\t"
    "L_xv3_%=:\n\t"
    "s_sleep 4\n\t"
    "s_mov_b64 exec, 1\n\t"
    "buffer_load_dword v164, off, %12, s93 offset:384 sc1\n\t"
    "s_mov_b64 exec, -1\n\t"
    "s_sub_u32 s68, s68, 1\n\t"
    "s_cmp_eq_u32 s68, 0\n\t"
    "s_cbranch_scc1 L_xwb3_%=\n\t"
    "s_waitcnt vmcnt(0)\n\t"
    "v_readfirstlane_b32 s65, v164\n\t"
    "s_nop 0\n\t"
    "s_cmp_eq_u32 s65, 0\n\t"
    "s_cbranch_scc1 L_xv3_%=\n\t"
    "s_branch L_xwb3_%=\n\t"
    "L_mp2_%=:\n\t"
    "v_lshrrev_b32 v57, 4, v127\n\t"
    "v_add_u32 v57, 32, v57\n\t"
    "s_and_b32 s65, %14, 7\n\t"
    "s_add_u32 s65, s65, 24\n\t"
    "v_writelane_b32 v57, s65, 16\n\t"
    "v_mov_b32 v58, 16\n\t"
    "v_lshlrev_b32 v57, 7, v57\n\t"
    "v_writelane_b32 v58, 32, 16\n\t"
    "s_mov_b32 s65, 48234496\n\t"
    "v_mov_b32 v56, 32\n\t"
    "s_mov_b32 s66, 1048576\n\t"
    "s_mov_b32 s65, 48234496\n\t"
    "s_mov_b64 exec, 0x1ffff\n\t"
    "buffer_load_dword v56, v57, %12, s65 offen sc1\n\t"
    "s_mov_b64 exec, -1\n\t"
    "s_branch L_mp2b_%=\n\t"
    "L_mp3_%=:\n\t"
    "v_cmp_gt_u32 vcc, v58, v56\n\t"
    "s_cbranch_vccz L_mp3b_%=\n\t"
    "s_sub_u32 s66, s66, 1\n\t"
    "s_cmp_eq_u32 s66, 0\n\t"
    "s_cbranch_scc1 L_mp3b_%=\n\t"
    "s_sleep 8\n\t"
    "s_mov_b32 s65, 48234496\n\t"
    "s_mov_b64 exec, 0x1ffff\n\t"
    "buffer_load_dword v56, v57, %12, s65 offen sc1\n\t"
    "s_mov_b64 exec, -1\n\t"
    "s_waitcnt vmcnt(0)\n\t"
    "s_branch L_mp3_%=\n\t"
    "L_pend_%=:\n\t"
    ".Ldcp_end:\n\t"
    :: "s"(s_idx), "s"(p_wp), "s"(p_w1), "n"(0), "n"(0), "n"(0), "s"(pb1), "s"(pw2), "s"(pb2), "s"(b2s),
       "n"(0),
       "n"(0), "s"(s_ws), "n"(0), "s"(bid), "s"(s_tab), "s"(w), "s"(p_out),
       "s"((unsigned)(unsigned long long)idx), "s"((unsigned)((unsigned long long)idx >> 32) & 0xFFFFu)
    : "memory", "vcc", "scc", "m0", "s36", "s37", "s38", "s39", "s40", "s41", "s42", "s43", "s44", "s45", "s46", "s47", "s48", "s49", "s50", "s51", "s52", "s53", "s54", "s55", "s56", "s57", "s58", "s59", "s60", "s61", "s62", "s63", "s64", "s65", "s66", "s67", "s68", "s69", "s70", "s71", "s72", "s73", "s74", "s75", "s76", "s77", "s78", "s79", "s80", "s81", "s82", "s83", "s84", "s85", "s86", "s87", "s88", "s89", "s90", "s91", "s92", "s93", "s94", "s95", "s96", "s97", "s98", "s99", "v0", "v1", "v2", "v3", "v4", "v5", "v6", "v7", "v8", "v9", "v10", "v11", "v12", "v13", "v14", "v15", "v16", "v17", "v18", "v19", "v20", "v21", "v22", "v23", "v24", "v25", "v26", "v27", "v28", "v29", "v30", "v31", "v32", "v33", "v34", "v35", "v36", "v37", "v38", "v39", "v40", "v41", "v42", "v43", "v44", "v45", "v46", "v47", "v48", "v49", "v50", "v51", "v52", "v53", "v54", "v55", "v56", "v57", "v58", "v59", "v60", "v61", "v62", "v63", "v64", "v65", "v66", "v67", "v68", "v69", "v70", "v71", "v72", "v73", "v74", "v75", "v76", "v77", "v78", "v79", "v80", "v81", "v82", "v83", "v84", "v85", "v86", "v87", "v88", "v89", "v90", "v91", "v92", "v93", "v94", "v95", "v96", "v97", "v98", "v99", "v100", "v101", "v102", "v103", "v104", "v105", "v106", "v107", "v108", "v109", "v110", "v111", "v112", "v113", "v114", "v115", "v116", "v117", "v118", "v119", "v120", "v121", "v122", "v123", "v124", "v125", "v126", "v127", "v128", "v129", "v130", "v131", "v132", "v133", "v134", "v135", "v136", "v137", "v138", "v139", "v140", "v141", "v142", "v143", "v144", "v145", "v146", "v147", "v148", "v149", "v150", "v151", "v152", "v153", "v154", "v155", "v156", "v157", "v158", "v159", "v160", "v161", "v162", "v163", "v164", "v165", "v166", "v167", "v168", "v169", "v170", "v171", "v172", "v173", "v174", "v175", "v176", "v177", "v178", "v179", "v180", "v181", "v182", "v183", "v184", "v185", "v186", "v187", "v188", "v189", "v190", "v191", "v192", "v193", "v194", "v195", "v196", "v197", "v198", "v199", "v200", "v201", "v202", "v203", "v204", "v205", "v206", "v207", "v208", "v209", "v210", "v211", "v212", "v213", "v214", "v215", "v216", "v217", "v218", "v219", "v220", "v221", "v222", "v223", "v224", "v225", "v226", "v227", "v228", "v229", "v230", "v231", "v232", "v233", "v234", "v235", "v236", "v237", "v238", "v239", "v240", "v241", "v242", "v243", "v244", "v245", "v246", "v247", "v248", "v249", "v250", "v251", "v252", "v253", "v254", "v255");
}

extern "C" void kernel_launch(void* const* d_in, const int* in_sizes, int n_in, void* d_out, int out_size, void* d_ws,
                              size_t ws_size, hipStream_t stream) {
  const int* idx = (const int*)d_in[0];
  const unsigned short* table = (const unsigned short*)d_in[1];
  const unsigned short* w_p = (const unsigned short*)d_in[2];
  const unsigned short* w1 = (const unsigned short*)d_in[3];
  const unsigned short* b1 = (const unsigned short*)d_in[4];
  const unsigned short* w2 = (const unsigned short*)d_in[5];
  const unsigned short* b2 = (const unsigned short*)d_in[6];
  char* ws = (char*)d_ws;
  dcp_all<<<dim3(256), dim3(512), 0, stream>>>(idx, table, w_p, w1, b1, w2, b2, ws, (__hip_bfloat16*)d_out);
}
```

```cpp
#include <hip/hip_runtime.h>
#include <hip/hip_bf16.h>
#include <stdint.h>

typedef __attribute__((ext_vector_type(2))) unsigned u32x2;

constexpr int BATCH = 16384, NF = 100, DIM = 128, VOC = 1000, HID = 512;
constexpr size_t NWIN = 13;
constexpr size_t WS_IDX = 0;
constexpr size_t IDX_STREAM = 212992;
constexpr size_t WS_W1F = 3407872;
constexpr size_t W1F_SLAB = 102400;
constexpr size_t WS_TF = 16515072;
constexpr size_t WS_WT = 16777216;
constexpr size_t WS_PART = 17825792;
constexpr size_t WS_P0 = 26214400;
constexpr size_t WS_XCH = 30408704;
constexpr size_t XCH_PITCH = 69632;
constexpr size_t WS_CNT = 48234496;
constexpr size_t WS_CH = 48242688;
constexpr size_t WS_RP = 48243712;
constexpr size_t WS_TS = 48244736;
constexpr size_t WS_RNG = 48245760;
constexpr size_t WS_RDY = 48246784;
constexpr size_t WS_DBG = 48279552;
constexpr size_t WS_END = 48672768;

__device__ __forceinline__ __amdgpu_buffer_rsrc_t mk_srd(const void* p, unsigned bytes) {
  return __builtin_amdgcn_make_buffer_rsrc((void*)p, (short)0, (int)bytes, 0x00020000);
}

__global__ __launch_bounds__(512, 2) void dcp_all(const int* __restrict__ idx, const unsigned short* __restrict__ table,
                                                  const unsigned short* __restrict__ w_p, const unsigned short* __restrict__ w1,
                                                  const unsigned short* __restrict__ b1, const unsigned short* __restrict__ w2,
                                                  const unsigned short* __restrict__ b2, char* __restrict__ ws,
                                                  __hip_bfloat16* __restrict__ out) {
  __shared__ __attribute__((aligned(1024))) char smem[163840];
  const int w = __builtin_amdgcn_readfirstlane(threadIdx.x >> 6) + (int)(uintptr_t)smem;
  const int bid = blockIdx.x;
  asm volatile(
    "s_getpc_b64 s[36:37]\n\t"
    ".Ldcp_pc:\n\t"
    "s_bfe_u32 s38, %0, 0x40003\n\t"
    "s_lshl_b32 s38, s38, 13\n\t"
    "v_and_b32 v122, 63, %1\n\t"
    "v_lshlrev_b32 v122, 7, v122\n\t"
    "v_add_u32 v122, s38, v122\n\t"
    "v_min_u32 v122, .Ldcp_end-.Ldcp_pc-4, v122\n\t"
    "s_cmp_lg_u32 %2, 3\n\t"
    "s_cbranch_scc1 L_nopf_%=\n\t"
    "global_load_dword v123, v122, s[36:37]\n\t"
    "L_nopf_%=:\n\t"
    :: "s"(bid), "v"(threadIdx.x), "s"(w) : "memory", "scc", "s36", "s37", "s38", "s74", "s75", "v122", "v123");
  const int xcd = bid & 7, loc = bid >> 3;
  const int cg = xcd * 16 + (loc >> 1);
  const __amdgpu_buffer_rsrc_t s_idx = mk_srd(idx + (size_t)bid * 64 * NF, (unsigned)((BATCH - bid * 64) * NF * 4));
  const __amdgpu_buffer_rsrc_t s_tab = mk_srd(table, VOC * DIM * 2);
  const __amdgpu_buffer_rsrc_t s_ws = mk_srd(ws, (unsigned)WS_END);
  const unsigned short* pb1 = b1 + 4 * cg;
  const unsigned short* pw2 = w2 + 4 * cg;
  const unsigned long long pb2 = (unsigned long long)b2 & ~3ull;
  const unsigned b2s = ((unsigned)(unsigned long long)b2 & 2u) * 8u;
  const unsigned long long p_wp = (unsigned long long)w_p & 0xFFFFFFFFFFFFull, p_w1 = (unsigned long long)w1 & 0xFFFFFFFFFFFFull;
  const unsigned long long p_out = (unsigned long long)out & 0xFFFFFFFFFFFFull;
  asm volatile(
    "v_mbcnt_lo_u32_b32 v147, -1, 0\n\t"
    "v_mbcnt_hi_u32_b32 v147, -1, v147\n\t"
    "s_mov_b64 s[92:93], %1\n\t"
    "s_mov_b32 s94, 20000\n\t"
    "s_mov_b32 s95, 0x20000\n\t"
    "s_lshr_b32 s46, %14, 3\n\t"
    "s_lshl_b32 s51, s46, 2\n\t"
    "v_lshlrev_b32 v122, 4, v147\n\t"
    "s_cmp_ge_u32 %16, 2\n\t"
    "s_cbranch_scc1 L_aimg_%=\n\t"
    "s_lshl_b32 s44, %16, 10\n\t"
    "s_add_u32 m0, s44, 140288\n\t"
    "s_nop 0\n\t"
    "buffer_load_dwordx4 v122, s[92:95], s44 offen lds\n\t"
    "s_add_u32 s44, s44, 0x800\n\t"
    "s_add_u32 m0, s44, 140288\n\t"
    "s_nop 0\n\t"
    "buffer_load_dwordx4 v122, s[92:95], s44 offen lds\n\t"
    "s_add_u32 s44, s44, 0x800\n\t"
    "s_add_u32 m0, s44, 140288\n\t"
    "s_nop 0\n\t"
    "buffer_load_dwordx4 v122, s[92:95], s44 offen lds\n\t"
    "s_add_u32 s44, s44, 0x800\n\t"
    "s_add_u32 m0, s44, 140288\n\t"
    "s_nop 0\n\t"
    "buffer_load_dwordx4 v122, s[92:95], s44 offen lds\n\t"
    "s_add_u32 s44, s44, 0x800\n\t"
    "s_add_u32 m0, s44, 140288\n\t"
    "s_nop 0\n\t"
    "buffer_load_dwordx4 v122, s[92:95], s44 offen lds\n\t"
    "s_add_u32 s44, s44, 0x800\n\t"
    "s_add_u32 m0, s44, 140288\n\t"
    "s_nop 0\n\t"
    "buffer_load_dwordx4 v122, s[92:95], s44 offen lds\n\t"
    "s_add_u32 s44, s44, 0x800\n\t"
    "s_add_u32 m0, s44, 140288\n\t"
    "s_nop 0\n\t"
    "buffer_load_dwordx4 v122, s[92:95], s44 offen lds\n\t"
    "s_add_u32 s44, s44, 0x800\n\t"
    "s_add_u32 m0, s44, 140288\n\t"
    "s_nop 0\n\t"
    "buffer_load_dwordx4 v122, s[92:95], s44 offen lds\n\t"
    "s_add_u32 s44, s44, 0x800\n\t"
    "s_add_u32 m0, s44, 140288\n\t"
    "s_nop 0\n\t"
    "buffer_load_dwordx4 v122, s[92:95], s44 offen lds\n\t"
    "s_add_u32 s44, s44, 0x800\n\t"
    "s_add_u32 m0, s44, 140288\n\t"
    "s_nop 0\n\t"
    "buffer_load_dwordx4 v122, s[92:95], s44 offen lds\n\t"
    "s_add_u32 s44, s44, 0x800\n\t"
    "s_mov_b32 s98, 13107200\n\t"
    "s_mov_b32 s99, 0x20000\n\t"
    "s_cmp_eq_u32 %16, 0\n\t"
    "s_cbranch_scc1 L_aside_%=\n\t"
    "s_branch L_ahelp_%=\n\t"
    "L_aimg_%=:\n\t"
    "s_cmp_lg_u32 %16, 2\n\t"
    "s_cbranch_scc1 L_anoinit_%=\n\t"
    "v_mov_b32 v0, 0\n\t"
    "v_mov_b32 v1, 0\n\t"
    "v_mov_b32 v2, 0\n\t"
    "v_mov_b32 v3, 0\n\t"
    "v_lshlrev_b32 v8, 4, v147\n\t"
    "v_add_u32 v8, 130048, v8\n\t"
    "v_mov_b32 v9, 1024\n\t"
    "ds_write_b128 v9, v[0:3]\n\t"
    "ds_write_b128 v8, v[0:3] offset:0\n\t"
    "ds_write_b128 v8, v[0:3] offset:1024\n\t"
    "ds_write_b128 v8, v[0:3] offset:2048\n\t"
    "ds_write_b32 v9, v0 offset:2084\n\t"
    "ds_write_b32 v9, v0 offset:2088\n\t"
    "ds_write_b32 v9, v0 offset:1044\n\t"
    "ds_write_b32 v9, v0 offset:1052\n\t"
    "v_mov_b32 v4, 3\n\t"
    "ds_write_b32 v9, v4 offset:1040\n\t"
    "v_mov_b32 v5, 16\n\t"
    "ds_write_b32 v9, v5 offset:2080\n\t"
    "v_mov_b32 v6, 33\n\t"
    "ds_write_b32 v9, v6 offset:3120\n\t"
    "v_mov_b32 v7, 52\n\t"
    "ds_write_b32 v9, v7 offset:4160\n\t"
    "L_anoinit_%=:\n\t"
    "s_and_b32 s46, %16, 3\n\t"
    "s_mul_i32 s49, s46, 1040\n\t"
    "s_add_u32 s49, s49, 2064\n\t"
    "s_lshl_b32 s46, s46, 3\n\t"
    "s_lshr_b32 s47, 1076763402, s46\n\t"
    "s_and_b32 s47, s47, 0xff\n\t"
    "s_lshl_b32 s48, s47, 16\n\t"
    "s_lshr_b32 s47, 773523968, s46\n\t"
    "s_and_b32 s47, s47, 0xff\n\t"
    "s_add_u32 s48, s48, s47\n\t"
    "s_lshr_b32 s47, %16, 2\n\t"
    "s_add_u32 s47, s47, 0x200\n\t"
    "s_cmp_eq_u32 s46, 0\n\t"
    "s_cselect_b32 s47, 0x100, s47\n\t"
    "s_add_u32 s48, s48, s47\n\t"
    "v_lshrrev_b32 v126, 3, v147\n\t"
    "v_and_b32 v154, 7, v147\n\t"
    "v_lshlrev_b32 v126, 8, v126\n\t"
    "v_lshl_add_u32 v126, v154, 4, v126\n\t"
    "v_lshlrev_b32 v117, 20, v147\n\t"
    "v_add_u32 v117, 1024, v117\n\t"
    "v_mov_b32 v118, 1\n\t"
    "s_and_b32 s46, %16, 6\n\t"
    "s_cmp_eq_u32 s46, 4\n\t"
    "s_cbranch_scc1 L_adma_%=\n\t"
    "s_lshr_b32 s61, %16, 2\n\t"
    "s_lshl_b32 s61, s61, 1\n\t"
    "s_and_b32 s46, %16, 1\n\t"
    "s_add_u32 s61, s61, s46\n\t"
    "s_add_u32 s46, s61, 84\n\t"
    "s_add_u32 s69, s51, s46\n\t"
    "s_sub_u32 s44, s69, 125\n\t"
    "s_cmp_ge_u32 s69, 125\n\t"
    "s_cselect_b32 s69, s44, s69\n\t"
    "s_mul_i32 m0, s69, 1040\n\t"
    "s_lshl_b32 s44, s69, 11\n\t"
    "buffer_load_dwordx4 v126, %15, s44 offen lds\n\t"
    "s_add_u32 s69, s69, 4\n\t"
    "s_sub_u32 s44, s69, 125\n\t"
    "s_cmp_ge_u32 s69, 125\n\t"
    "s_cselect_b32 s69, s44, s69\n\t"
    "s_mul_i32 m0, s69, 1040\n\t"
    "s_lshl_b32 s44, s69, 11\n\t"
    "buffer_load_dwordx4 v126, %15, s44 offen lds\n\t"
    "s_add_u32 s69, s69, 4\n\t"
    "s_sub_u32 s44, s69, 125\n\t"
    "s_cmp_ge_u32 s69, 125\n\t"
    "s_cselect_b32 s69, s44, s69\n\t"
    "s_waitcnt lgkmcnt(0)\n\t"
    "s_barrier\n\t"
    "s_load_dwordx2 s[72:73], %6, 0\n\t"
    "s_load_dwordx2 s[88:89], %7, 0\n\t"
    "s_load_dword s91, %8, 0\n\t"
    "s_mul_i32 m0, s69, 1040\n\t"
    "s_lshl_b32 s44, s69, 11\n\t"
    "buffer_load_dwordx4 v126, %15, s44 offen lds\n\t"
    "s_add_u32 s69, s69, 4\n\t"
    "s_sub_u32 s44, s69, 125\n\t"
    "s_cmp_ge_u32 s69, 125\n\t"
    "s_cselect_b32 s69, s44, s69\n\t"
    "s_mul_i32 m0, s69, 1040\n\t"
    "s_lshl_b32 s44, s69, 11\n\t"
    "buffer_load_dwordx4 v126, %15, s44 offen lds\n\t"
    "s_add_u32 s69, s69, 4\n\t"
    "s_sub_u32 s44, s69, 125\n\t"
    "s_cmp_ge_u32 s69, 125\n\t"
    "s_cselect_b32 s69, s44, s69\n\t"
    "s_mul_i32 m0, s69, 1040\n\t"
    "s_lshl_b32 s44, s69, 11\n\t"
    "buffer_load_dwordx4 v126, %15, s44 offen lds\n\t"
    "s_add_u32 s69, s69, 4\n\t"
    "s_sub_u32 s44, s69, 125\n\t"
    "s_cmp_ge_u32 s69, 125\n\t"
    "s_cselect_b32 s69, s44, s69\n\t"
    "s_mul_i32 m0, s69, 1040\n\t"
    "s_lshl_b32 s44, s69, 11\n\t"
    "buffer_load_dwordx4 v126, %15, s44 offen lds\n\t"
    "s_add_u32 s69, s69, 4\n\t"
    "s_sub_u32 s44, s69, 125\n\t"
    "s_cmp_ge_u32 s69, 125\n\t"
    "s_cselect_b32 s69, s44, s69\n\t"
    "v_bfe_u32 v154, v147, 2, 2\n\t"
    "v_and_b32 v155, 3, v147\n\t"
    "v_mul_u32_u24 v154, 25, v154\n\t"
    "v_mad_u32_u24 v154, v155, 6, v154\n\t"
    "v_lshlrev_b32 v153, 2, v154\n\t"
    "s_and_b32 s53, s48, 0xff\n\t"
    "s_bfe_u32 s55, s48, 0x80008\n\t"
    "s_add_u32 s54, s53, s55\n\t"
    "s_add_u32 s55, s54, s55\n\t"
    "s_lshr_b32 s64, s48, 16\n\t"
    "v_cmp_eq_u32 s[62:63], 0, v155\n\t"
    "v_and_b32 v151, 48, v147\n\t"
    "v_or_b32 v155, 0x100000, v151\n\t"
    "v_cndmask_b32 v152, v155, v151, s[62:63]\n\t"
    "v_lshrrev_b32 v94, 4, v147\n\t"
    "v_mul_u32_u24 v92, 0x1388, v94\n\t"
    "v_lshl_add_u32 v92, v154, 1, v92\n\t"
    "v_add_u32 v92, 140288, v92\n\t"
    "v_mul_u32_u24 v93, 200, v154\n\t"
    "v_mad_u32_u24 v93, v94, 50, v93\n\t"
    "v_add_u32 v93, 140288, v93\n\t"
    "v_add_u32 v95, 140288, v122\n\t"
    "v_mov_b32 v0, 0\n\t"
    "v_mov_b32 v28, 0\n\t"
    "v_mov_b32 v1, 0\n\t"
    "v_mov_b32 v29, 0\n\t"
    "v_mov_b32 v2, 0\n\t"
    "v_mov_b32 v30, 0\n\t"
    "v_mov_b32 v3, 0\n\t"
    "v_mov_b32 v31, 0\n\t"
    "v_mov_b32 v4, 0\n\t"
    "v_mov_b32 v32, 0\n\t"
    "v_mov_b32 v5, 0\n\t"
    "v_mov_b32 v33, 0\n\t"
    "v_mov_b32 v6, 0\n\t"
    "v_mov_b32 v34, 0\n\t"
    "v_mov_b32 v7, 0\n\t"
    "v_mov_b32 v35, 0\n\t"
    "v_mov_b32 v8, 0\n\t"
    "v_mov_b32 v36, 0\n\t"
    "v_mov_b32 v9, 0\n\t"
    "v_mov_b32 v37, 0\n\t"
    "v_mov_b32 v10, 0\n\t"
    "v_mov_b32 v38, 0\n\t"
    "v_mov_b32 v11, 0\n\t"
    "v_mov_b32 v39, 0\n\t"
    "v_mov_b32 v12, 0\n\t"
    "v_mov_b32 v40, 0\n\t"
    "v_mov_b32 v13, 0\n\t"
    "v_mov_b32 v41, 0\n\t"
    "v_mov_b32 v14, 0\n\t"
    "v_mov_b32 v42, 0\n\t"
    "v_mov_b32 v15, 0\n\t"
    "v_mov_b32 v43, 0\n\t"
    "v_mov_b32 v16, 0\n\t"
    "v_mov_b32 v44, 0\n\t"
    "v_mov_b32 v17, 0\n\t"
    "v_mov_b32 v45, 0\n\t"
    "v_mov_b32 v18, 0\n\t"
    "v_mov_b32 v46, 0\n\t"
    "v_mov_b32 v19, 0\n\t"
    "v_mov_b32 v47, 0\n\t"
    "v_mov_b32 v20, 0\n\t"
    "v_mov_b32 v48, 0\n\t"
    "v_mov_b32 v21, 0\n\t"
    "v_mov_b32 v49, 0\n\t"
    "v_mov_b32 v22, 0\n\t"
    "v_mov_b32 v50, 0\n\t"
    "v_mov_b32 v23, 0\n\t"
    "v_mov_b32 v51, 0\n\t"
    "s_cmp_eq_u32 s61, 1\n\t"
    "s_cbranch_scc1 L_afold1_%=\n\t"
    "s_cmp_eq_u32 s61, 2\n\t"
    "s_cbranch_scc1 L_afold2_%=\n\t"
    "s_cmp_eq_u32 s61, 3\n\t"
    "s_cbranch_scc1 L_afold3_%=\n\t"
    "L_afold0_%=:\n\t"
    "L_asp0_%=:\n\t"
    "ds_read_b32 v119, v117 offset:1044\n\t"
    "s_waitcnt lgkmcnt(0)\n\t"
    "v_readfirstlane_b32 s46, v119\n\t"
    "s_nop 0\n\t"
    "s_cmp_ge_u32 s46, 2\n\t"
    "s_cbranch_scc1 L_ago0_%=\n\t"
    "s_sleep 1\n\t"
    "s_branch L_asp0_%=\n\t"
    "L_ago0_%=:\n\t"
    "ds_read_u16_d16_hi v0, v92 offset:208\n\t"
    "ds_read_u16_d16_hi v28, v93 offset:802\n\t"
    "ds_read_u16_d16_hi v1, v92 offset:1408\n\t"
    "ds_read_u16_d16_hi v29, v93 offset:814\n\t"
    "ds_read_u16_d16_hi v2, v92 offset:2608\n\t"
    "ds_read_u16_d16_hi v30, v93 offset:826\n\t"
    "ds_read_u16_d16_hi v3, v92 offset:3808\n\t"
    "ds_read_u16_d16_hi v31, v93 offset:838\n\t"
    "ds_read_u16_d16_hi v4, v92 offset:410\n\t"
    "ds_read_u16_d16_hi v32, v93 offset:1004\n\t"
    "ds_read_u16_d16_hi v5, v92 offset:1610\n\t"
    "ds_read_u16_d16_hi v33, v93 offset:1016\n\t"
    "ds_read_u16_d16_hi v6, v92 offset:2810\n\t"
    "ds_read_u16_d16_hi v34, v93 offset:1028\n\t"
    "ds_read_u16_d16_hi v7, v92 offset:4010\n\t"
    "ds_read_u16_d16_hi v35, v93 offset:1040\n\t"
    "ds_read_u16_d16_hi v8, v92 offset:612\n\t"
    "ds_read_u16_d16_hi v36, v93 offset:1206\n\t"
    "ds_read_u16_d16_hi v9, v92 offset:1812\n\t"
    "ds_read_u16_d16_hi v37, v93 offset:1218\n\t"
    "ds_read_u16_d16_hi v10, v92 offset:3012\n\t"
    "ds_read_u16_d16_hi v38, v93 offset:1230\n\t"
    "ds_read_u16_d16_hi v11, v92 offset:4212\n\t"
    "ds_read_u16_d16_hi v39, v93 offset:1242\n\t"
    "ds_read_u16_d16_hi v12, v92 offset:0\n\t"
    "ds_read_u16_d16_hi v13, v92 offset:2\n\t"
    "ds_read_u16_d16_hi v41, v93 offset:200\n\t"
    "ds_read_u16_d16_hi v14, v92 offset:4\n\t"
    "ds_read_u16_d16_hi v42, v93 offset:400\n\t"
    "ds_read_u16_d16_hi v15, v92 offset:6\n\t"
    "ds_read_u16_d16_hi v43, v93 offset:600\n\t"
    "ds_read_u16_d16_hi v16, v92 offset:8\n\t"
    "ds_read_u16_d16_hi v44, v93 offset:800\n\t"
    "ds_read_u16_d16_hi v17, v92 offset:10\n\t"
    "ds_read_u16_d16_hi v45, v93 offset:1000\n\t"
    "ds_read_u16_d16_hi v18, v92 offset:12\n\t"
    "ds_read_u16_d16_hi v46, v93 offset:1200\n\t"
    "ds_add_u32 v117, v118 offset:0\n\t"
    "s_waitcnt lgkmcnt(15)\n\t"
    "v_add_f32 v0, v0, v28\n\t"
    "v_add_f32 v1, v1, v29\n\t"
    "v_add_f32 v2, v2, v30\n\t"
    "v_add_f32 v3, v3, v31\n\t"
    "v_add_f32 v4, v4, v32\n\t"
    "v_add_f32 v5, v5, v33\n\t"
    "v_add_f32 v6, v6, v34\n\t"
    "v_add_f32 v7, v7, v35\n\t"
    "s_waitcnt lgkmcnt(14)\n\t"
    "v_add_f32 v8, v8, v36\n\t"
    "v_add_f32 v9, v9, v37\n\t"
    "v_add_f32 v10, v10, v38\n\t"
    "v_add_f32 v11, v11, v39\n\t"
    "s_waitcnt lgkmcnt(7)\n\t"
    "v_cndmask_b32 v12, 0, v12, s[62:63]\n\t"
    "v_add_f32 v13, v13, v41\n\t"
    "v_add_f32 v14, v14, v42\n\t"
    "v_add_f32 v15, v15, v43\n\t"
    "s_waitcnt lgkmcnt(1)\n\t"
    "v_add_f32 v16, v16, v44\n\t"
    "v_add_f32 v17, v17, v45\n\t"
    "v_add_f32 v18, v18, v46\n\t"
    "s_call_b64 s[98:99], L_setup_%=\n\t"
    "L_asp1_%=:\n\t"
    "ds_read_b32 v119, v117 offset:0\n\t"
    "s_waitcnt lgkmcnt(0)\n\t"
    "v_readfirstlane_b32 s46, v119\n\t"
    "s_nop 0\n\t"
    "s_cmp_ge_u32 s46, 4\n\t"
    "s_cbranch_scc1 L_ago1_%=\n\t"
    "s_sleep 1\n\t"
    "s_branch L_asp1_%=\n\t"
    "L_ago1_%=:\n\t"
    "ds_write_b128 v95, v[0:3] offset:6144\n\t"
    "ds_write_b128 v95, v[4:7] offset:10240\n\t"
    "ds_write_b128 v95, v[8:11] offset:14336\n\t"
    "ds_write_b128 v95, v[12:15] offset:21504\n\t"
    "ds_write_b128 v95, v[16:19] offset:22528\n\t"
    "ds_add_u32 v117, v118 offset:4\n\t"
    "s_branch L_ab0_%=\n\t"
    "L_afold1_%=:\n\t"
    "L_asp2_%=:\n\t"
    "ds_read_b32 v119, v117 offset:1044\n\t"
    "s_waitcnt lgkmcnt(0)\n\t"
    "v_readfirstlane_b32 s46, v119\n\t"
    "s_nop 0\n\t"
    "s_cmp_ge_u32 s46, 2\n\t"
    "s_cbranch_scc1 L_ago2_%=\n\t"
    "s_sleep 1\n\t"
    "s_branch L_asp2_%=\n\t"
    "L_ago2_%=:\n\t"
    "ds_read_u16_d16_hi v0, v92 offset:204\n\t"
    "ds_read_u16_d16_hi v28, v93 offset:402\n\t"
    "ds_read_u16_d16_hi v1, v92 offset:1404\n\t"
    "ds_read_u16_d16_hi v29, v93 offset:414\n\t"
    "ds_read_u16_d16_hi v2, v92 offset:2604\n\t"
    "ds_read_u16_d16_hi v30, v93 offset:426\n\t"
    "ds_read_u16_d16_hi v3, v92 offset:3804\n\t"
    "ds_read_u16_d16_hi v31, v93 offset:438\n\t"
    "ds_read_u16_d16_hi v4, v92 offset:408\n\t"
    "ds_read_u16_d16_hi v32, v93 offset:804\n\t"
    "ds_read_u16_d16_hi v5, v92 offset:1608\n\t"
    "ds_read_u16_d16_hi v33, v93 offset:816\n\t"
    "ds_read_u16_d16_hi v6, v92 offset:2808\n\t"
    "ds_read_u16_d16_hi v34, v93 offset:828\n\t"
    "ds_read_u16_d16_hi v7, v92 offset:4008\n\t"
    "ds_read_u16_d16_hi v35, v93 offset:840\n\t"
    "ds_read_u16_d16_hi v8, v92 offset:412\n\t"
    "ds_read_u16_d16_hi v36, v93 offset:1204\n\t"
    "ds_read_u16_d16_hi v9, v92 offset:1612\n\t"
    "ds_read_u16_d16_hi v37, v93 offset:1216\n\t"
    "ds_read_u16_d16_hi v10, v92 offset:2812\n\t"
    "ds_read_u16_d16_hi v38, v93 offset:1228\n\t"
    "ds_read_u16_d16_hi v11, v92 offset:4012\n\t"
    "ds_read_u16_d16_hi v39, v93 offset:1240\n\t"
    "ds_read_u16_d16_hi v12, v92 offset:810\n\t"
    "ds_read_u16_d16_hi v40, v93 offset:1008\n\t"
    "ds_read_u16_d16_hi v13, v92 offset:2010\n\t"
    "ds_read_u16_d16_hi v41, v93 offset:1020\n\t"
    "ds_read_u16_d16_hi v14, v92 offset:3210\n\t"
    "ds_read_u16_d16_hi v42, v93 offset:1032\n\t"
    "ds_read_u16_d16_hi v15, v92 offset:4410\n\t"
    "ds_read_u16_d16_hi v43, v93 offset:1044\n\t"
    "ds_read_u16_d16_hi v16, v92 offset:202\n\t"
    "ds_read_u16_d16_hi v17, v92 offset:1402\n\t"
    "ds_read_u16_d16_hi v18, v92 offset:2602\n\t"
    "ds_read_u16_d16_hi v19, v92 offset:3802\n\t"
    "ds_read_u16_d16_hi v20, v92 offset:808\n\t"
    "ds_read_u16_d16_hi v21, v92 offset:2008\n\t"
    "ds_read_u16_d16_hi v22, v92 offset:3208\n\t"
    "ds_read_u16_d16_hi v23, v92 offset:4408\n\t"
    "ds_add_u32 v117, v118 offset:0\n\t"
    "s_waitcnt lgkmcnt(15)\n\t"
    "v_add_f32 v0, v0, v28\n\t"
    "v_add_f32 v1, v1, v29\n\t"
    "v_add_f32 v2, v2, v30\n\t"
    "v_add_f32 v3, v3, v31\n\t"
    "v_add_f32 v4, v4, v32\n\t"
    "v_add_f32 v5, v5, v33\n\t"
    "v_add_f32 v6, v6, v34\n\t"
    "v_add_f32 v7, v7, v35\n\t"
    "v_add_f32 v8, v8, v36\n\t"
    "v_add_f32 v9, v9, v37\n\t"
    "v_add_f32 v10, v10, v38\n\t"
    "v_add_f32 v11, v11, v39\n\t"
    "s_waitcnt lgkmcnt(9)\n\t"
    "v_add_f32 v12, v12, v40\n\t"
    "v_add_f32 v13, v13, v41\n\t"
    "v_add_f32 v14, v14, v42\n\t"
    "v_add_f32 v15, v15, v43\n\t"
    "s_waitcnt lgkmcnt(5)\n\t"
    "s_waitcnt lgkmcnt(1)\n\t"
    "s_call_b64 s[98:99], L_setup_%=\n\t"
    "L_asp3_%=:\n\t"
    "ds_read_b32 v119, v117 offset:0\n\t"
    "s_waitcnt lgkmcnt(0)\n\t"
    "v_readfirstlane_b32 s46, v119\n\t"
    "s_nop 0\n\t"
    "s_cmp_ge_u32 s46, 4\n\t"
    "s_cbranch_scc1 L_ago3_%=\n\t"
    "s_sleep 1\n\t"
    "s_branch L_asp3_%=\n\t"
    "L_ago3_%=:\n\t"
    "ds_write_b128 v95, v[0:3] offset:1024\n\t"
    "ds_write_b128 v95, v[4:7] offset:7168\n\t"
    "ds_write_b128 v95, v[8:11] offset:11264\n\t"
    "ds_write_b128 v95, v[12:15] offset:16384\n\t"
    "ds_write_b128 v95, v[16:19] offset:0\n\t"
    "ds_write_b128 v95, v[20:23] offset:15360\n\t"
    "ds_add_u32 v117, v118 offset:4\n\t"
    "s_branch L_ab0_%=\n\t"
    "L_afold2_%=:\n\t"
    "s_call_b64 s[98:99], L_setup_%=\n\t"
    "L_asp4_%=:\n\t"
    "ds_read_b32 v119, v117 offset:1044\n\t"
    "s_waitcnt lgkmcnt(0)\n\t"
    "v_readfirstlane_b32 s46, v119\n\t"
    "s_nop 0\n\t"
    "s_cmp_ge_u32 s46, 2\n\t"
    "s_cbranch_scc1 L_ago4_%=\n\t"
    "s_sleep 1\n\t"
    "s_branch L_asp4_%=\n\t"
    "L_ago4_%=:\n\t"
    "ds_read_u16_d16_hi v0, v92 offset:206\n\t"
    "ds_read_u16_d16_hi v28, v93 offset:602\n\t"
    "ds_read_u16_d16_hi v1, v92 offset:1406\n\t"
    "ds_read_u16_d16_hi v29, v93 offset:614\n\t"
    "ds_read_u16_d16_hi v2, v92 offset:2606\n\t"
    "ds_read_u16_d16_hi v30, v93 offset:626\n\t"
    "ds_read_u16_d16_hi v3, v92 offset:3806\n\t"
    "ds_read_u16_d16_hi v31, v93 offset:638\n\t"
    "ds_read_u16_d16_hi v4, v92 offset:210\n\t"
    "ds_read_u16_d16_hi v32, v93 offset:1002\n\t"
    "ds_read_u16_d16_hi v5, v92 offset:1410\n\t"
    "ds_read_u16_d16_hi v33, v93 offset:1014\n\t"
    "ds_read_u16_d16_hi v6, v92 offset:2610\n\t"
    "ds_read_u16_d16_hi v34, v93 offset:1026\n\t"
    "ds_read_u16_d16_hi v7, v92 offset:3810\n\t"
    "ds_read_u16_d16_hi v35, v93 offset:1038\n\t"
    "ds_read_u16_d16_hi v8, v92 offset:608\n\t"
    "ds_read_u16_d16_hi v36, v93 offset:806\n\t"
    "ds_read_u16_d16_hi v9, v92 offset:1808\n\t"
    "ds_read_u16_d16_hi v37, v93 offset:818\n\t"
    "ds_read_u16_d16_hi v10, v92 offset:3008\n\t"
    "ds_read_u16_d16_hi v38, v93 offset:830\n\t"
    "ds_read_u16_d16_hi v11, v92 offset:4208\n\t"
    "ds_read_u16_d16_hi v39, v93 offset:842\n\t"
    "ds_read_u16_d16_hi v12, v92 offset:812\n\t"
    "ds_read_u16_d16_hi v40, v93 offset:1208\n\t"
    "ds_read_u16_d16_hi v13, v92 offset:2012\n\t"
    "ds_read_u16_d16_hi v41, v93 offset:1220\n\t"
    "ds_read_u16_d16_hi v14, v92 offset:3212\n\t"
    "ds_read_u16_d16_hi v42, v93 offset:1232\n\t"
    "ds_read_u16_d16_hi v15, v92 offset:4412\n\t"
    "ds_read_u16_d16_hi v43, v93 offset:1244\n\t"
    "ds_read_u16_d16_hi v16, v92 offset:404\n\t"
    "ds_read_u16_d16_hi v17, v92 offset:1604\n\t"
    "ds_read_u16_d16_hi v18, v92 offset:2804\n\t"
    "ds_read_u16_d16_hi v19, v92 offset:4004\n\t"
    "ds_read_u16_d16_hi v20, v92 offset:1010\n\t"
    "ds_read_u16_d16_hi v21, v92 offset:2210\n\t"
    "ds_read_u16_d16_hi v22, v92 offset:3410\n\t"
    "ds_read_u16_d16_hi v23, v92 offset:4610\n\t"
    "ds_add_u32 v117, v118 offset:0\n\t"
    "s_waitcnt lgkmcnt(15)\n\t"
    "v_add_f32 v0, v0, v28\n\t"
    "v_add_f32 v1, v1, v29\n\t"
    "v_add_f32 v2, v2, v30\n\t"
    "v_add_f32 v3, v3, v31\n\t"
    "v_add_f32 v4, v4, v32\n\t"
    "v_add_f32 v5, v5, v33\n\t"
    "v_add_f32 v6, v6, v34\n\t"
    "v_add_f32 v7, v7, v35\n\t"
    "v_add_f32 v8, v8, v36\n\t"
    "v_add_f32 v9, v9, v37\n\t"
    "v_add_f32 v10, v10, v38\n\t"
    "v_add_f32 v11, v11, v39\n\t"
    "s_waitcnt lgkmcnt(9)\n\t"
    "v_add_f32 v12, v12, v40\n\t"
    "v_add_f32 v13, v13, v41\n\t"
    "v_add_f32 v14, v14, v42\n\t"
    "v_add_f32 v15, v15, v43\n\t"
    "s_waitcnt lgkmcnt(5)\n\t"
    "s_waitcnt lgkmcnt(1)\n\t"
    "L_asp5_%=:\n\t"
    "ds_read_b32 v119, v117 offset:0\n\t"
    "s_waitcnt lgkmcnt(0)\n\t"
    "v_readfirstlane_b32 s46, v119\n\t"
    "s_nop 0\n\t"
    "s_cmp_ge_u32 s46, 4\n\t"
    "s_cbranch_scc1 L_ago5_%=\n\t"
    "s_sleep 1\n\t"
    "s_branch L_asp5_%=\n\t"
    "L_ago5_%=:\n\t"
    "ds_write_b128 v95, v[0:3] offset:3072\n\t"
    "ds_write_b128 v95, v[4:7] offset:8192\n\t"
    "ds_write_b128 v95, v[8:11] offset:12288\n\t"
    "ds_write_b128 v95, v[12:15] offset:17408\n\t"
    "ds_write_b128 v95, v[16:19] offset:2048\n\t"
    "ds_write_b128 v95, v[20:23] offset:18432\n\t"
    "ds_add_u32 v117, v118 offset:4\n\t"
    "s_branch L_ab0_%=\n\t"
    "L_afold3_%=:\n\t"
    "s_call_b64 s[98:99], L_setup_%=\n\t"
    "L_asp6_%=:\n\t"
    "ds_read_b32 v119, v117 offset:1044\n\t"
    "s_waitcnt lgkmcnt(0)\n\t"
    "v_readfirstlane_b32 s46, v119\n\t"
    "s_nop 0\n\t"
    "s_cmp_ge_u32 s46, 2\n\t"
    "s_cbranch_scc1 L_ago6_%=\n\t"
    "s_sleep 1\n\t"
    "s_branch L_asp6_%=\n\t"
    "L_ago6_%=:\n\t"
    "ds_read_u16_d16_hi v0, v92 offset:406\n\t"
    "ds_read_u16_d16_hi v28, v93 offset:604\n\t"
    "ds_read_u16_d16_hi v1, v92 offset:1606\n\t"
    "ds_read_u16_d16_hi v29, v93 offset:616\n\t"
    "ds_read_u16_d16_hi v2, v92 offset:2806\n\t"
    "ds_read_u16_d16_hi v30, v93 offset:628\n\t"
    "ds_read_u16_d16_hi v3, v92 offset:4006\n\t"
    "ds_read_u16_d16_hi v31, v93 offset:640\n\t"
    "ds_read_u16_d16_hi v4, v92 offset:212\n\t"
    "ds_read_u16_d16_hi v32, v93 offset:1202\n\t"
    "ds_read_u16_d16_hi v5, v92 offset:1412\n\t"
    "ds_read_u16_d16_hi v33, v93 offset:1214\n\t"
    "ds_read_u16_d16_hi v6, v92 offset:2612\n\t"
    "ds_read_u16_d16_hi v34, v93 offset:1226\n\t"
    "ds_read_u16_d16_hi v7, v92 offset:3812\n\t"
    "ds_read_u16_d16_hi v35, v93 offset:1238\n\t"
    "ds_read_u16_d16_hi v8, v92 offset:610\n\t"
    "ds_read_u16_d16_hi v36, v93 offset:1006\n\t"
    "ds_read_u16_d16_hi v9, v92 offset:1810\n\t"
    "ds_read_u16_d16_hi v37, v93 offset:1018\n\t"
    "ds_read_u16_d16_hi v10, v92 offset:3010\n\t"
    "ds_read_u16_d16_hi v38, v93 offset:1030\n\t"
    "ds_read_u16_d16_hi v11, v92 offset:4210\n\t"
    "ds_read_u16_d16_hi v39, v93 offset:1042\n\t"
    "ds_read_u16_d16_hi v12, v92 offset:1012\n\t"
    "ds_read_u16_d16_hi v40, v93 offset:1210\n\t"
    "ds_read_u16_d16_hi v13, v92 offset:2212\n\t"
    "ds_read_u16_d16_hi v41, v93 offset:1222\n\t"
    "ds_read_u16_d16_hi v14, v92 offset:3412\n\t"
    "ds_read_u16_d16_hi v42, v93 offset:1234\n\t"
    "ds_read_u16_d16_hi v15, v92 offset:4612\n\t"
    "ds_read_u16_d16_hi v43, v93 offset:1246\n\t"
    "ds_read_u16_d16_hi v16, v92 offset:606\n\t"
    "ds_read_u16_d16_hi v17, v92 offset:1806\n\t"
    "ds_read_u16_d16_hi v18, v92 offset:3006\n\t"
    "ds_read_u16_d16_hi v19, v92 offset:4206\n\t"
    "ds_read_u16_d16_hi v20, v92 offset:1212\n\t"
    "ds_read_u16_d16_hi v21, v92 offset:2412\n\t"
    "ds_read_u16_d16_hi v22, v92 offset:3612\n\t"
    "ds_read_u16_d16_hi v23, v92 offset:4812\n\t"
    "ds_add_u32 v117, v118 offset:0\n\t"
    "s_waitcnt lgkmcnt(15)\n\t"
    "v_add_f32 v0, v0, v28\n\t"
    "v_add_f32 v1, v1, v29\n\t"
    "v_add_f32 v2, v2, v30\n\t"
    "v_add_f32 v3, v3, v31\n\t"
    "v_add_f32 v4, v4, v32\n\t"
    "v_add_f32 v5, v5, v33\n\t"
    "v_add_f32 v6, v6, v34\n\t"
    "v_add_f32 v7, v7, v35\n\t"
    "v_add_f32 v8, v8, v36\n\t"
    "v_add_f32 v9, v9, v37\n\t"
    "v_add_f32 v10, v10, v38\n\t"
    "v_add_f32 v11, v11, v39\n\t"
    "s_waitcnt lgkmcnt(9)\n\t"
    "v_add_f32 v12, v12, v40\n\t"
    "v_add_f32 v13, v13, v41\n\t"
    "v_add_f32 v14, v14, v42\n\t"
    "v_add_f32 v15, v15, v43\n\t"
    "s_waitcnt lgkmcnt(5)\n\t"
    "s_waitcnt lgkmcnt(1)\n\t"
    "L_asp7_%=:\n\t"
    "ds_read_b32 v119, v117 offset:0\n\t"
    "s_waitcnt lgkmcnt(0)\n\t"
    "v_readfirstlane_b32 s46, v119\n\t"
    "s_nop 0\n\t"
    "s_cmp_ge_u32 s46, 4\n\t"
    "s_cbranch_scc1 L_ago7_%=\n\t"
    "s_sleep 1\n\t"
    "s_branch L_asp7_%=\n\t"
    "L_ago7_%=:\n\t"
    "ds_write_b128 v95, v[0:3] offset:4096\n\t"
    "ds_write_b128 v95, v[4:7] offset:9216\n\t"
    "ds_write_b128 v95, v[8:11] offset:13312\n\t"
    "ds_write_b128 v95, v[12:15] offset:19456\n\t"
    "ds_write_b128 v95, v[16:19] offset:5120\n\t"
    "ds_write_b128 v95, v[20:23] offset:20480\n\t"
    "ds_add_u32 v117, v118 offset:4\n\t"
    "L_ab0_%=:\n\t"
    "s_mul_i32 m0, s69, 1040\n\t"
    "s_lshl_b32 s44, s69, 11\n\t"
    "buffer_load_dwordx4 v126, %15, s44 offen lds\n\t"
    "s_add_u32 s69, s69, 4\n\t"
    "s_sub_u32 s44, s69, 125\n\t"
    "s_cmp_ge_u32 s69, 125\n\t"
    "s_cselect_b32 s69, s44, s69\n\t"
    "s_mul_i32 m0, s69, 1040\n\t"
    "s_lshl_b32 s44, s69, 11\n\t"
    "buffer_load_dwordx4 v126, %15, s44 offen lds\n\t"
    "s_add_u32 s69, s69, 4\n\t"
    "s_sub_u32 s44, s69, 125\n\t"
    "s_cmp_ge_u32 s69, 125\n\t"
    "s_cselect_b32 s69, s44, s69\n\t"
    "s_mul_i32 m0, s69, 1040\n\t"
    "s_lshl_b32 s44, s69, 11\n\t"
    "buffer_load_dwordx4 v126, %15, s44 offen lds\n\t"
    "s_add_u32 s69, s69, 4\n\t"
    "s_sub_u32 s44, s69, 125\n\t"
    "s_cmp_ge_u32 s69, 125\n\t"
    "s_cselect_b32 s69, s44, s69\n\t"
    "s_mul_i32 m0, s69, 1040\n\t"
    "s_lshl_b32 s44, s69, 11\n\t"
    "buffer_load_dwordx4 v126, %15, s44 offen lds\n\t"
    "s_add_u32 s69, s69, 4\n\t"
    "s_sub_u32 s44, s69, 125\n\t"
    "s_cmp_ge_u32 s69, 125\n\t"
    "s_cselect_b32 s69, s44, s69\n\t"
    "s_mul_i32 m0, s69, 1040\n\t"
    "s_lshl_b32 s44, s69, 11\n\t"
    "buffer_load_dwordx4 v126, %15, s44 offen lds\n\t"
    "s_add_u32 s69, s69, 4\n\t"
    "s_sub_u32 s44, s69, 125\n\t"
    "s_cmp_ge_u32 s69, 125\n\t"
    "s_cselect_b32 s69, s44, s69\n\t"
    "s_branch L_ab_%=\n\t"
    "L_adma_%=:\n\t"
    "s_sub_u32 s46, %16, 4\n\t"
    "s_add_u32 s69, s51, s46\n\t"
    "s_sub_u32 s44, s69, 125\n\t"
    "s_cmp_ge_u32 s69, 125\n\t"
    "s_cselect_b32 s69, s44, s69\n\t"
    "s_mul_i32 m0, s69, 1040\n\t"
    "s_lshl_b32 s44, s69, 11\n\t"
    "buffer_load_dwordx4 v126, %15, s44 offen lds\n\t"
    "s_add_u32 s69, s69, 2\n\t"
    "s_sub_u32 s44, s69, 125\n\t"
    "s_cmp_ge_u32 s69, 125\n\t"
    "s_cselect_b32 s69, s44, s69\n\t"
    "s_mul_i32 m0, s69, 1040\n\t"
    "s_lshl_b32 s44, s69, 11\n\t"
    "buffer_load_dwordx4 v126, %15, s44 offen lds\n\t"
    "s_add_u32 s69, s69, 2\n\t"
    "s_sub_u32 s44, s69, 125\n\t"
    "s_cmp_ge_u32 s69, 125\n\t"
    "s_cselect_b32 s69, s44, s69\n\t"
    "s_barrier\n\t"
    "s_load_dwordx2 s[72:73], %6, 0\n\t"
    "s_load_dwordx2 s[88:89], %7, 0\n\t"
    "s_load_dword s91, %8, 0\n\t"
    "s_mul_i32 m0, s69, 1040\n\t"
    "s_lshl_b32 s44, s69, 11\n\t"
    "buffer_load_dwordx4 v126, %15, s44 offen lds\n\t"
    "s_add_u32 s69, s69, 2\n\t"
    "s_sub_u32 s44, s69, 125\n\t"
    "s_cmp_ge_u32 s69, 125\n\t"
    "s_cselect_b32 s69, s44, s69\n\t"
    "s_mul_i32 m0, s69, 1040\n\t"
    "s_lshl_b32 s44, s69, 11\n\t"
    "buffer_load_dwordx4 v126, %15, s44 offen lds\n\t"
    "s_add_u32 s69, s69, 2\n\t"
    "s_sub_u32 s44, s69, 125\n\t"
    "s_cmp_ge_u32 s69, 125\n\t"
    "s_cselect_b32 s69, s44, s69\n\t"
    "s_mul_i32 m0, s69, 1040\n\t"
    "s_lshl_b32 s44, s69, 11\n\t"
    "buffer_load_dwordx4 v126, %15, s44 offen lds\n\t"
    "s_add_u32 s69, s69, 2\n\t"
    "s_sub_u32 s44, s69, 125\n\t"
    "s_cmp_ge_u32 s69, 125\n\t"
    "s_cselect_b32 s69, s44, s69\n\t"
    "s_mul_i32 m0, s69, 1040\n\t"
    "s_lshl_b32 s44, s69, 11\n\t"
    "buffer_load_dwordx4 v126, %15, s44 offen lds\n\t"
    "s_add_u32 s69, s69, 2\n\t"
    "s_sub_u32 s44, s69, 125\n\t"
    "s_cmp_ge_u32 s69, 125\n\t"
    "s_cselect_b32 s69, s44, s69\n\t"
    "s_mul_i32 m0, s69, 1040\n\t"
    "s_lshl_b32 s44, s69, 11\n\t"
    "buffer_load_dwordx4 v126, %15, s44 offen lds\n\t"
    "s_add_u32 s69, s69, 2\n\t"
    "s_sub_u32 s44, s69, 125\n\t"
    "s_cmp_ge_u32 s69, 125\n\t"
    "s_cselect_b32 s69, s44, s69\n\t"
    "s_mul_i32 m0, s69, 1040\n\t"
    "s_lshl_b32 s44, s69, 11\n\t"
    "buffer_load_dwordx4 v126, %15, s44 offen lds\n\t"
    "s_add_u32 s69, s69, 2\n\t"
    "s_sub_u32 s44, s69, 125\n\t"
    "s_cmp_ge_u32 s69, 125\n\t"
    "s_cselect_b32 s69, s44, s69\n\t"
    "s_mul_i32 m0, s69, 1040\n\t"
    "s_lshl_b32 s44, s69, 11\n\t"
    "buffer_load_dwordx4 v126, %15, s44 offen lds\n\t"
    "s_add_u32 s69, s69, 2\n\t"
    "s_sub_u32 s44, s69, 125\n\t"
    "s_cmp_ge_u32 s69, 125\n\t"
    "s_cselect_b32 s69, s44, s69\n\t"
    "s_mul_i32 m0, s69, 1040\n\t"
    "s_lshl_b32 s44, s69, 11\n\t"
    "buffer_load_dwordx4 v126, %15, s44 offen lds\n\t"
    "s_add_u32 s69, s69, 2\n\t"
    "s_sub_u32 s44, s69, 125\n\t"
    "s_cmp_ge_u32 s69, 125\n\t"
    "s_cselect_b32 s69, s44, s69\n\t"
    "s_mul_i32 m0, s69, 1040\n\t"
    "s_lshl_b32 s44, s69, 11\n\t"
    "buffer_load_dwordx4 v126, %15, s44 offen lds\n\t"
    "s_add_u32 s69, s69, 2\n\t"
    "s_sub_u32 s44, s69, 125\n\t"
    "s_cmp_ge_u32 s69, 125\n\t"
    "s_cselect_b32 s69, s44, s69\n\t"
    "s_mul_i32 m0, s69, 1040\n\t"
    "s_lshl_b32 s44, s69, 11\n\t"
    "buffer_load_dwordx4 v126, %15, s44 offen lds\n\t"
    "s_add_u32 s69, s69, 2\n\t"
    "s_sub_u32 s44, s69, 125\n\t"
    "s_cmp_ge_u32 s69, 125\n\t"
    "s_cselect_b32 s69, s44, s69\n\t"
    "v_bfe_u32 v154, v147, 2, 2\n\t"
    "v_and_b32 v155, 3, v147\n\t"
    "v_mul_u32_u24 v154, 25, v154\n\t"
    "v_mad_u32_u24 v154, v155, 6, v154\n\t"
    "v_lshlrev_b32 v153, 2, v154\n\t"
    "s_and_b32 s53, s48, 0xff\n\t"
    "s_bfe_u32 s55, s48, 0x80008\n\t"
    "s_add_u32 s54, s53, s55\n\t"
    "s_add_u32 s55, s54, s55\n\t"
    "s_lshr_b32 s64, s48, 16\n\t"
    "v_cmp_eq_u32 s[62:63], 0, v155\n\t"
    "v_and_b32 v151, 48, v147\n\t"
    "v_or_b32 v155, 0x100000, v151\n\t"
    "v_cndmask_b32 v152, v155, v151, s[62:63]\n\t"
    "v_add_u32 v95, 140288, v122\n\t"
    "s_mul_i32 m0, s69, 1040\n\t"
    "s_lshl_b32 s44, s69, 11\n\t"
    "buffer_load_dwordx4 v126, %15, s44 offen lds\n\t"
    "s_add_u32 s69, s69, 2\n\t"
    "s_sub_u32 s44, s69, 125\n\t"
    "s_cmp_ge_u32 s69, 125\n\t"
    "s_cselect_b32 s69, s44, s69\n\t"
    "s_mul_i32 m0, s69, 1040\n\t"
    "s_lshl_b32 s44, s69, 11\n\t"
    "buffer_load_dwordx4 v126, %15, s44 offen lds\n\t"
    "s_add_u32 s69, s69, 2\n\t"
    "s_sub_u32 s44, s69, 125\n\t"
    "s_cmp_ge_u32 s69, 125\n\t"
    "s_cselect_b32 s69, s44, s69\n\t"
    "s_mul_i32 m0, s69, 1040\n\t"
    "s_lshl_b32 s44, s69, 11\n\t"
    "buffer_load_dwordx4 v126, %15, s44 offen lds\n\t"
    "s_add_u32 s69, s69, 2\n\t"
    "s_sub_u32 s44, s69, 125\n\t"
    "s_cmp_ge_u32 s69, 125\n\t"
    "s_cselect_b32 s69, s44, s69\n\t"
    "s_mul_i32 m0, s69, 1040\n\t"
    "s_lshl_b32 s44, s69, 11\n\t"
    "buffer_load_dwordx4 v126, %15, s44 offen lds\n\t"
    "s_add_u32 s69, s69, 2\n\t"
    "s_sub_u32 s44, s69, 125\n\t"
    "s_cmp_ge_u32 s69, 125\n\t"
    "s_cselect_b32 s69, s44, s69\n\t"
    "s_mul_i32 m0, s69, 1040\n\t"
    "s_lshl_b32 s44, s69, 11\n\t"
    "buffer_load_dwordx4 v126, %15, s44 offen lds\n\t"
    "s_add_u32 s69, s69, 2\n\t"
    "s_sub_u32 s44, s69, 125\n\t"
    "s_cmp_ge_u32 s69, 125\n\t"
    "s_cselect_b32 s69, s44, s69\n\t"
    "s_mul_i32 m0, s69, 1040\n\t"
    "s_lshl_b32 s44, s69, 11\n\t"
    "buffer_load_dwordx4 v126, %15, s44 offen lds\n\t"
    "s_add_u32 s69, s69, 2\n\t"
    "s_sub_u32 s44, s69, 125\n\t"
    "s_cmp_ge_u32 s69, 125\n\t"
    "s_cselect_b32 s69, s44, s69\n\t"
    "s_mul_i32 m0, s69, 1040\n\t"
    "s_lshl_b32 s44, s69, 11\n\t"
    "buffer_load_dwordx4 v126, %15, s44 offen lds\n\t"
    "s_add_u32 s69, s69, 2\n\t"
    "s_sub_u32 s44, s69, 125\n\t"
    "s_cmp_ge_u32 s69, 125\n\t"
    "s_cselect_b32 s69, s44, s69\n\t"
    "s_mul_i32 m0, s69, 1040\n\t"
    "s_lshl_b32 s44, s69, 11\n\t"
    "buffer_load_dwordx4 v126, %15, s44 offen lds\n\t"
    "s_add_u32 s69, s69, 2\n\t"
    "s_sub_u32 s44, s69, 125\n\t"
    "s_cmp_ge_u32 s69, 125\n\t"
    "s_cselect_b32 s69, s44, s69\n\t"
    "s_mul_i32 m0, s69, 1040\n\t"
    "s_lshl_b32 s44, s69, 11\n\t"
    "buffer_load_dwordx4 v126, %15, s44 offen lds\n\t"
    "s_add_u32 s69, s69, 2\n\t"
    "s_sub_u32 s44, s69, 125\n\t"
    "s_cmp_ge_u32 s69, 125\n\t"
    "s_cselect_b32 s69, s44, s69\n\t"
    "s_mul_i32 m0, s69, 1040\n\t"
    "s_lshl_b32 s44, s69, 11\n\t"
    "buffer_load_dwordx4 v126, %15, s44 offen lds\n\t"
    "s_add_u32 s69, s69, 2\n\t"
    "s_sub_u32 s44, s69, 125\n\t"
    "s_cmp_ge_u32 s69, 125\n\t"
    "s_cselect_b32 s69, s44, s69\n\t"
    "s_mul_i32 m0, s69, 1040\n\t"
    "s_lshl_b32 s44, s69, 11\n\t"
    "buffer_load_dwordx4 v126, %15, s44 offen lds\n\t"
    "s_add_u32 s69, s69, 2\n\t"
    "s_sub_u32 s44, s69, 125\n\t"
    "s_cmp_ge_u32 s69, 125\n\t"
    "s_cselect_b32 s69, s44, s69\n\t"
    "s_mul_i32 m0, s69, 1040\n\t"
    "s_lshl_b32 s44, s69, 11\n\t"
    "buffer_load_dwordx4 v126, %15, s44 offen lds\n\t"
    "s_add_u32 s69, s69, 2\n\t"
    "s_sub_u32 s44, s69, 125\n\t"
    "s_cmp_ge_u32 s69, 125\n\t"
    "s_cselect_b32 s69, s44, s69\n\t"
    "s_mul_i32 m0, s69, 1040\n\t"
    "s_lshl_b32 s44, s69, 11\n\t"
    "buffer_load_dwordx4 v126, %15, s44 offen lds\n\t"
    "s_add_u32 s69, s69, 2\n\t"
    "s_sub_u32 s44, s69, 125\n\t"
    "s_cmp_ge_u32 s69, 125\n\t"
    "s_cselect_b32 s69, s44, s69\n\t"
    "s_mul_i32 m0, s69, 1040\n\t"
    "s_lshl_b32 s44, s69, 11\n\t"
    "buffer_load_dwordx4 v126, %15, s44 offen lds\n\t"
    "s_add_u32 s69, s69, 2\n\t"
    "s_sub_u32 s44, s69, 125\n\t"
    "s_cmp_ge_u32 s69, 125\n\t"
    "s_cselect_b32 s69, s44, s69\n\t"
    "s_mul_i32 m0, s69, 1040\n\t"
    "s_lshl_b32 s44, s69, 11\n\t"
    "buffer_load_dwordx4 v126, %15, s44 offen lds\n\t"
    "s_add_u32 s69, s69, 2\n\t"
    "s_sub_u32 s44, s69, 125\n\t"
    "s_cmp_ge_u32 s69, 125\n\t"
    "s_cselect_b32 s69, s44, s69\n\t"
    "s_mul_i32 m0, s69, 1040\n\t"
    "s_lshl_b32 s44, s69, 11\n\t"
    "buffer_load_dwordx4 v126, %15, s44 offen lds\n\t"
    "s_add_u32 s69, s69, 2\n\t"
    "s_sub_u32 s44, s69, 125\n\t"
    "s_cmp_ge_u32 s69, 125\n\t"
    "s_cselect_b32 s69, s44, s69\n\t"
    "s_mul_i32 m0, s69, 1040\n\t"
    "s_lshl_b32 s44, s69, 11\n\t"
    "buffer_load_dwordx4 v126, %15, s44 offen lds\n\t"
    "s_add_u32 s69, s69, 2\n\t"
    "s_sub_u32 s44, s69, 125\n\t"
    "s_cmp_ge_u32 s69, 125\n\t"
    "s_cselect_b32 s69, s44, s69\n\t"
    "s_mul_i32 m0, s69, 1040\n\t"
    "s_lshl_b32 s44, s69, 11\n\t"
    "buffer_load_dwordx4 v126, %15, s44 offen lds\n\t"
    "s_add_u32 s69, s69, 2\n\t"
    "s_sub_u32 s44, s69, 125\n\t"
    "s_cmp_ge_u32 s69, 125\n\t"
    "s_cselect_b32 s69, s44, s69\n\t"
    "s_mul_i32 m0, s69, 1040\n\t"
    "s_lshl_b32 s44, s69, 11\n\t"
    "buffer_load_dwordx4 v126, %15, s44 offen lds\n\t"
    "s_add_u32 s69, s69, 2\n\t"
    "s_sub_u32 s44, s69, 125\n\t"
    "s_cmp_ge_u32 s69, 125\n\t"
    "s_cselect_b32 s69, s44, s69\n\t"
    "s_mul_i32 m0, s69, 1040\n\t"
    "s_lshl_b32 s44, s69, 11\n\t"
    "buffer_load_dwordx4 v126, %15, s44 offen lds\n\t"
    "s_add_u32 s69, s69, 2\n\t"
    "s_sub_u32 s44, s69, 125\n\t"
    "s_cmp_ge_u32 s69, 125\n\t"
    "s_cselect_b32 s69, s44, s69\n\t"
    "s_mul_i32 m0, s69, 1040\n\t"
    "s_lshl_b32 s44, s69, 11\n\t"
    "buffer_load_dwordx4 v126, %15, s44 offen lds\n\t"
    "s_add_u32 s69, s69, 2\n\t"
    "s_sub_u32 s44, s69, 125\n\t"
    "s_cmp_ge_u32 s69, 125\n\t"
    "s_cselect_b32 s69, s44, s69\n\t"
    "s_mul_i32 m0, s69, 1040\n\t"
    "s_lshl_b32 s44, s69, 11\n\t"
    "buffer_load_dwordx4 v126, %15, s44 offen lds\n\t"
    "s_add_u32 s69, s69, 2\n\t"
    "s_sub_u32 s44, s69, 125\n\t"
    "s_cmp_ge_u32 s69, 125\n\t"
    "s_cselect_b32 s69, s44, s69\n\t"
    "s_mul_i32 m0, s69, 1040\n\t"
    "s_lshl_b32 s44, s69, 11\n\t"
    "buffer_load_dwordx4 v126, %15, s44 offen lds\n\t"
    "s_add_u32 s69, s69, 2\n\t"
    "s_sub_u32 s44, s69, 125\n\t"
    "s_cmp_ge_u32 s69, 125\n\t"
    "s_cselect_b32 s69, s44, s69\n\t"
    "s_mul_i32 m0, s69, 1040\n\t"
    "s_lshl_b32 s44, s69, 11\n\t"
    "buffer_load_dwordx4 v126, %15, s44 offen lds\n\t"
    "s_add_u32 s69, s69, 2\n\t"
    "s_sub_u32 s44, s69, 125\n\t"
    "s_cmp_ge_u32 s69, 125\n\t"
    "s_cselect_b32 s69, s44, s69\n\t"
    "s_mul_i32 m0, s69, 1040\n\t"
    "s_lshl_b32 s44, s69, 11\n\t"
    "buffer_load_dwordx4 v126, %15, s44 offen lds\n\t"
    "s_add_u32 s69, s69, 2\n\t"
    "s_sub_u32 s44, s69, 125\n\t"
    "s_cmp_ge_u32 s69, 125\n\t"
    "s_cselect_b32 s69, s44, s69\n\t"
    "s_mul_i32 m0, s69, 1040\n\t"
    "s_lshl_b32 s44, s69, 11\n\t"
    "buffer_load_dwordx4 v126, %15, s44 offen lds\n\t"
    "s_add_u32 s69, s69, 2\n\t"
    "s_sub_u32 s44, s69, 125\n\t"
    "s_cmp_ge_u32 s69, 125\n\t"
    "s_cselect_b32 s69, s44, s69\n\t"
    "s_mul_i32 m0, s69, 1040\n\t"
    "s_lshl_b32 s44, s69, 11\n\t"
    "buffer_load_dwordx4 v126, %15, s44 offen lds\n\t"
    "s_add_u32 s69, s69, 2\n\t"
    "s_sub_u32 s44, s69, 125\n\t"
    "s_cmp_ge_u32 s69, 125\n\t"
    "s_cselect_b32 s69, s44, s69\n\t"
    "s_mul_i32 m0, s69, 1040\n\t"
    "s_lshl_b32 s44, s69, 11\n\t"
    "buffer_load_dwordx4 v126, %15, s44 offen lds\n\t"
    "s_add_u32 s69, s69, 2\n\t"
    "s_sub_u32 s44, s69, 125\n\t"
    "s_cmp_ge_u32 s69, 125\n\t"
    "s_cselect_b32 s69, s44, s69\n\t"
    "s_mul_i32 m0, s69, 1040\n\t"
    "s_lshl_b32 s44, s69, 11\n\t"
    "buffer_load_dwordx4 v126, %15, s44 offen lds\n\t"
    "s_add_u32 s69, s69, 2\n\t"
    "s_sub_u32 s44, s69, 125\n\t"
    "s_cmp_ge_u32 s69, 125\n\t"
    "s_cselect_b32 s69, s44, s69\n\t"
    "s_mul_i32 m0, s69, 1040\n\t"
    "s_lshl_b32 s44, s69, 11\n\t"
    "buffer_load_dwordx4 v126, %15, s44 offen lds\n\t"
    "s_add_u32 s69, s69, 2\n\t"
    "s_sub_u32 s44, s69, 125\n\t"
    "s_cmp_ge_u32 s69, 125\n\t"
    "s_cselect_b32 s69, s44, s69\n\t"
    "s_call_b64 s[98:99], L_setup_%=\n\t"
    "L_ab_%=:\n\t"
    "s_mul_i32 s66, s53, 400\n\t"
    "buffer_load_dwordx3 v[128:130], v153, %0, s66 offen\n\t"
    "buffer_load_dwordx4 v[132:135], v153, %0, s66 offen offset:12\n\t"
    "s_mul_i32 s66, s54, 400\n\t"
    "buffer_load_dwordx3 v[136:138], v153, %0, s66 offen\n\t"
    "buffer_load_dwordx4 v[140:143], v153, %0, s66 offen offset:12\n\t"
    "L_asp8_%=:\n\t"
    "ds_read_b32 v119, v117 offset:4\n\t"
    "s_waitcnt lgkmcnt(0)\n\t"
    "v_readfirstlane_b32 s46, v119\n\t"
    "s_nop 0\n\t"
    "s_cmp_ge_u32 s46, 4\n\t"
    "s_cbranch_scc1 L_ago8_%=\n\t"
    "s_sleep 1\n\t"
    "s_branch L_asp8_%=\n\t"
    "L_ago8_%=:\n\t"
    "ds_read_b128 v[0:3], v95 offset:0\n\t"
    "ds_read_b128 v[4:7], v95 offset:1024\n\t"
    "ds_read_b128 v[8:11], v95 offset:2048\n\t"
    "ds_read_b128 v[12:15], v95 offset:3072\n\t"
    "ds_read_b128 v[16:19], v95 offset:4096\n\t"
    "ds_read_b128 v[20:23], v95 offset:5120\n\t"
    "ds_read_b128 v[24:27], v95 offset:6144\n\t"
    "ds_read_b128 v[28:31], v95 offset:7168\n\t"
    "ds_read_b128 v[32:35], v95 offset:8192\n\t"
    "ds_read_b128 v[36:39], v95 offset:9216\n\t"
    "ds_read_b128 v[40:43], v95 offset:10240\n\t"
    "ds_read_b128 v[44:47], v95 offset:11264\n\t"
    "ds_read_b128 v[48:51], v95 offset:12288\n\t"
    "ds_read_b128 v[52:55], v95 offset:13312\n\t"
    "ds_read_b128 v[56:59], v95 offset:14336\n\t"
    "ds_read_b128 v[60:63], v95 offset:15360\n\t"
    "ds_read_b128 v[64:67], v95 offset:16384\n\t"
    "ds_read_b128 v[68:71], v95 offset:17408\n\t"
    "ds_read_b128 v[72:75], v95 offset:18432\n\t"
    "ds_read_b128 v[76:79], v95 offset:19456\n\t"
    "ds_read_b128 v[80:83], v95 offset:20480\n\t"
    "ds_read_b128 v[84:87], v95 offset:21504\n\t"
    "ds_read_b128 v[88:91], v95 offset:22528\n\t"
    "s_waitcnt vmcnt(4)\n\t"
    "ds_add_u32 v117, v118 offset:8\n\t"
    "s_waitcnt vmcnt(0)\n\t"
    "s_branch L_awd_%=\n\t"
    "s_waitcnt vmcnt(4)\n\t"
    "ds_add_u32 v117, v118 offset:8\n\t"
    "s_branch L_await_%=\n\t"
    "L_setup_%=:\n\t"
    "v_mbcnt_lo_u32_b32 v100, -1, 0\n\t"
    "v_mbcnt_hi_u32_b32 v100, -1, v100\n\t"
    "s_lshl_b32 s61, %16, 15\n\t"
    "s_add_u32 s61, s61, 16515072\n\t"
    "v_lshlrev_b32 v127, 4, v100\n\t"
    "s_lshr_b32 s37, %14, 4\n\t"
    "s_and_b32 s39, %14, 7\n\t"
    "v_and_b32 v101, 15, v100\n\t"
    "s_lshl_b32 s39, s39, 4\n\t"
    "s_add_u32 s39, s39, s37\n\t"
    "s_mul_i32 s65, %16, 125\n\t"
    "v_add_u32 v155, s65, v101\n\t"
    "s_mul_i32 s63, s39, 102400\n\t"
    "s_add_u32 s63, s63, 3407872\n\t"
    "v_lshrrev_b32 v102, 2, v101\n\t"
    "s_and_b32 s37, s37, 7\n\t"
    "s_lshl_b32 s37, s37, 11\n\t"
    "v_lshrrev_b32 v103, 3, v100\n\t"
    "s_lshl_b32 s36, s39, 16\n\t"
    "s_bfe_u32 s65, %14, 0x10003\n\t"
    "v_and_b32 v103, 2, v103\n\t"
    "s_lshl_b32 s38, s65, 15\n\t"
    "s_add_u32 s36, s36, s38\n\t"
    "v_and_b32 v104, 1, v100\n\t"
    "s_lshl_b32 s38, %16, 12\n\t"
    "s_add_u32 s36, s36, s38\n\t"
    "v_lshlrev_b32 v104, 4, v104\n\t"
    "s_add_u32 s36, s36, 17825792\n\t"
    "v_mov_b32 v101, 0x3f80\n\t"
    "v_lshlrev_b32 v104, v104, v101\n\t"
    "v_cmp_eq_u32 vcc, v102, v103\n\t"
    "s_waitcnt lgkmcnt(0)\n\t"
    "s_lshr_b32 s39, s91, %9\n\t"
    "s_lshl_b32 s39, s39, 16\n\t"
    "v_cndmask_b32 v120, 0, v104, vcc\n\t"
    "v_cndmask_b32 v121, 0, v104, vcc\n\t"
    "v_add_u32 v103, 1, v103\n\t"
    "v_cmp_eq_u32 vcc, v102, v103\n\t"
    "s_lshl_b32 s40, s88, 16\n\t"
    "s_and_b32 s41, s88, 0xffff0000\n\t"
    "v_cndmask_b32 v122, 0, v104, vcc\n\t"
    "v_cndmask_b32 v123, 0, v104, vcc\n\t"
    "v_and_b32 v101, 2, v100\n\t"
    "v_mov_b32 v102, 0x44444444\n\t"
    "v_mov_b32 v103, 0xeeeeeeee\n\t"
    "v_cmp_eq_u32 vcc, 0, v101\n\t"
    "s_lshl_b32 s42, s89, 16\n\t"
    "s_and_b32 s43, s89, 0xffff0000\n\t"
    "v_cndmask_b32 v124, v103, v102, vcc\n\t"
    "v_lshrrev_b32 v101, 4, v100\n\t"
    "v_mul_u32_u24 v101, 8000, v101\n\t"
    "v_lshl_add_u32 v125, v155, 3, v101\n\t"
    "v_add_u32 v125, 64000, v125\n\t"
    "s_lshl_b32 s70, s72, 16\n\t"
    "s_and_b32 s71, s72, 0xffff0000\n\t"
    "s_lshl_b32 s72, s73, 16\n\t"
    "s_and_b32 s73, s73, 0xffff0000\n\t"
    "s_mov_b32 s67, 0xffff0000\n\t"
    "s_and_b32 s65, %14, 8\n\t"
    "s_add_u32 s38, s65, %16\n\t"
    "s_mul_i32 s38, s38, 212992\n\t"
    "s_add_u32 s38, s38, 196608\n\t"
    "s_add_u32 s84, s37, s38\n\t"
    "s_add_u32 s37, s37, 0x800\n\t"
    "s_and_b32 s37, s37, 0x3800\n\t"
    "s_add_u32 s85, s37, s38\n\t"
    "s_add_u32 s37, s37, 0x800\n\t"
    "s_and_b32 s37, s37, 0x3800\n\t"
    "s_add_u32 s86, s37, s38\n\t"
    "s_add_u32 s37, s37, 0x800\n\t"
    "s_and_b32 s37, s37, 0x3800\n\t"
    "s_add_u32 s87, s37, s38\n\t"
    "s_add_u32 s37, s37, 0x800\n\t"
    "s_and_b32 s37, s37, 0x3800\n\t"
    "s_add_u32 s88, s37, s38\n\t"
    "s_add_u32 s37, s37, 0x800\n\t"
    "s_and_b32 s37, s37, 0x3800\n\t"
    "s_add_u32 s89, s37, s38\n\t"
    "s_add_u32 s37, s37, 0x800\n\t"
    "s_and_b32 s37, s37, 0x3800\n\t"
    "s_add_u32 s90, s37, s38\n\t"
    "s_add_u32 s37, s37, 0x800\n\t"
    "s_and_b32 s37, s37, 0x3800\n\t"
    "s_add_u32 s91, s37, s38\n\t"
    "s_mul_i32 s94, s65, 425984\n\t"
    "s_sub_u32 s94, s94, 1703936\n\t"
    "s_sub_u32 s95, 0x8000, s94\n\t"
    "s_lshl_b32 s97, s65, 11\n\t"
    "s_sub_u32 s97, s97, 229376\n\t"
    "s_mov_b32 s92, 64000\n\t"
    "s_mov_b32 s93, -64000\n\t"
    "s_mov_b32 s96, 0x2000\n\t"
    "s_mov_b32 s62, 2\n\t"
    "v_rcp_f32 v101, s40\n\t"
    "v_rcp_f32 v102, s41\n\t"
    "v_rcp_f32 v103, s42\n\t"
    "v_rcp_f32 v104, s43\n\t"
    "v_mov_b32 v100, 0x7f7fffff\n\t"
    "v_med3_f32 v101, v101, -v100, v100\n\t"
    "v_med3_f32 v102, v102, -v100, v100\n\t"
    "v_med3_f32 v103, v103, -v100, v100\n\t"
    "v_med3_f32 v104, v104, -v100, v100\n\t"
    "v_readfirstlane_b32 s40, v101\n\t"
    "v_readfirstlane_b32 s41, v102\n\t"
    "v_readfirstlane_b32 s42, v103\n\t"
    "v_readfirstlane_b32 s43, v104\n\t"
    "s_setpc_b64 s[98:99]\n\t"
    "L_aside_%=:\n\t"
    "s_setprio 3\n\t"
    "v_cmp_gt_u32 vcc, 48, v147\n\t"
    "v_mov_b32 v133, 0x40000000\n\t"
    "v_lshrrev_b32 v134, 3, v147\n\t"
    "v_bfe_u32 v135, v147, 2, 1\n\t"
    "v_and_b32 v130, 3, v147\n\t"
    "v_lshl_add_u32 v132, v135, 5, v130\n\t"
    "v_mul_u32_u24 v64, 400, v132\n\t"
    "v_lshl_add_u32 v64, v134, 5, v64\n\t"
    "s_movk_i32 s50, 0\n\t"
    "buffer_load_dwordx4 v[0:3], v64, %0, s50 offen offset:16\n\t"
    "buffer_load_dwordx4 v[4:7], v64, %0, s50 offen offset:32\n\t"
    "v_lshlrev_b32 v66, 14, v134\n\t"
    "v_lshl_add_u32 v66, v132, 4, v66\n\t"
    "v_lshlrev_b32 v68, 8, v134\n\t"
    "v_lshl_add_u32 v68, v135, 2, v68\n\t"
    "v_add_u32 v68, 130048, v68\n\t"
    "v_cndmask_b32 v68, v133, v68, vcc\n\t"
    "v_cndmask_b32 v66, v133, v66, vcc\n\t"
    "v_lshrrev_b32 v134, 2, v147\n\t"
    "v_bfe_u32 v135, v147, 1, 1\n\t"
    "v_and_b32 v130, 1, v147\n\t"
    "v_lshl_add_u32 v132, v135, 5, v130\n\t"
    "v_mul_u32_u24 v65, 400, v132\n\t"
    "v_lshl_add_u32 v65, v134, 5, v65\n\t"
    "v_lshlrev_b32 v67, 14, v134\n\t"
    "v_lshl_add_u32 v67, v132, 4, v67\n\t"
    "v_lshlrev_b32 v69, 8, v134\n\t"
    "v_lshl_add_u32 v69, v135, 2, v69\n\t"
    "v_add_u32 v69, 130048, v69\n\t"
    "v_cndmask_b32 v69, v133, v69, vcc\n\t"
    "v_cndmask_b32 v67, v133, v67, vcc\n\t"
    "s_barrier\n\t"
    "s_load_dwordx2 s[72:73], %6, 0\n\t"
    "s_load_dwordx2 s[88:89], %7, 0\n\t"
    "s_load_dword s91, %8, 0\n\t"
    "v_lshlrev_b32 v128, 4, v147\n\t"
    "v_add_u32 v128, 133120, v128\n\t"
    "v_and_b32 v130, 1, v147\n\t"
    "v_cndmask_b32 v128, v133, v128, vcc\n\t"
    "v_mul_u32_u24 v127, 7, v130\n\t"
    "v_cmp_eq_u32 vcc, 1, v130\n\t"
    "v_mov_b32 v126, 0x07060100\n\t"
    "v_mov_b32 v133, 0x05040100\n\t"
    "v_mov_b32 v136, 1\n\t"
    "v_cndmask_b32 v126, v126, v133, vcc\n\t"
    "s_mov_b32 s36, 0\n\t"
    "s_mov_b32 s37, 8000\n\t"
    "s_mov_b32 s38, 16000\n\t"
    "s_mov_b32 s39, 24000\n\t"
    "s_mov_b32 s40, 32000\n\t"
    "s_mov_b32 s41, 40000\n\t"
    "s_mov_b32 s42, 48000\n\t"
    "s_mov_b32 s43, 56000\n\t"
    "s_movk_i32 s44, 0xf8\n\t"
    "s_mov_b32 s45, 0x01010000\n\t"
    "s_mov_b32 s46, 0x020c000c\n\t"
    "s_lshr_b32 s49, %14, 4\n\t"
    "s_mul_i32 s49, s49, 212992\n\t"
    "s_and_b32 s50, %14, 15\n\t"
    "s_lshl_b32 s50, s50, 10\n\t"
    "s_add_u32 s49, s49, s50\n\t"
    "v_lshrrev_b32 v134, 4, v147\n\t"
    "v_bfe_u32 v135, v147, 2, 2\n\t"
    "v_and_b32 v130, 3, v147\n\t"
    "v_lshlrev_b32 v137, 17, v134\n\t"
    "v_lshl_add_u32 v137, v135, 10, v137\n\t"
    "v_lshl_add_u32 v137, v130, 4, v137\n\t"
    "v_add_u32 v138, 0x1000, v137\n\t"
    "v_mul_u32_u24 v141, 0x210, v134\n\t"
    "v_lshl_add_u32 v141, v135, 6, v141\n\t"
    "v_lshl_add_u32 v141, v130, 4, v141\n\t"
    "v_add_u32 v141, 134656, v141\n\t"
    "v_mul_u32_u24 v142, 0x210, v130\n\t"
    "v_lshl_add_u32 v142, v135, 6, v142\n\t"
    "v_lshl_add_u32 v142, v134, 3, v142\n\t"
    "v_add_u32 v142, 134656, v142\n\t"
    "s_mov_b32 s50, 102400\n\t"
    "v_mul_u32_u24 v139, s50, v134\n\t"
    "v_and_b32 v132, 15, v147\n\t"
    "v_lshl_add_u32 v139, v132, 4, v139\n\t"
    "v_add_u32 v140, 409600, v139\n\t"
    "s_lshr_b32 s50, %14, 4\n\t"
    "s_lshl_b32 s47, s50, 13\n\t"
    "s_lshl_b32 s48, s50, 8\n\t"
    "s_and_b32 s50, %14, 7\n\t"
    "s_lshl_b32 s51, s50, 7\n\t"
    "s_add_u32 s47, s47, s51\n\t"
    "s_mul_i32 s51, s50, 1638400\n\t"
    "s_add_u32 s48, s48, s51\n\t"
    "s_bfe_u32 s50, %14, 0x10003\n\t"
    "s_lshl_b32 s51, s50, 19\n\t"
    "s_add_u32 s47, s47, s51\n\t"
    "s_lshl_b32 s51, s50, 12\n\t"
    "s_add_u32 s48, s48, s51\n\t"
    "s_add_u32 s48, s48, 3407872\n\t"
    "s_waitcnt vmcnt(2)\n\t"
    "v_lshlrev_b32 v134, 20, v147\n\t"
    "v_add_u32 v134, 2068, v134\n\t"
    "ds_add_u32 v134, v136\n\t"
    "v_mov_b32 v110, 65664\n\t"
    "v_mov_b32 v111, 131136\n\t"
    "v_mov_b32 v112, 262176\n\t"
    "v_mov_b32 v113, 524304\n\t"
    "v_mov_b32 v114, 1048584\n\t"
    "v_mov_b32 v115, 2097156\n\t"
    "v_mov_b32 v116, 4194306\n\t"
    "v_mov_b32 v117, 8388609\n\t"
    "s_waitcnt vmcnt(0)\n\t"
    "v_lshl_add_u32 v76, v0, 3, s36\n\t"
    "v_lshl_add_u32 v77, v1, 3, s37\n\t"
    "v_lshl_add_u32 v78, v2, 3, s38\n\t"
    "v_lshl_add_u32 v79, v3, 3, s39\n\t"
    "v_lshl_add_u32 v80, v4, 3, s40\n\t"
    "v_lshl_add_u32 v81, v5, 3, s41\n\t"
    "v_lshl_add_u32 v82, v6, 3, s42\n\t"
    "v_lshl_add_u32 v83, v7, 3, s43\n\t"
    "v_and_or_b32 v92, v76, s44, v68\n\t"
    "ds_or_rtn_b32 v118, v92, v110\n\t"
    "v_and_or_b32 v93, v77, s44, v68\n\t"
    "ds_or_rtn_b32 v119, v93, v111\n\t"
    "v_and_or_b32 v94, v78, s44, v68\n\t"
    "ds_or_rtn_b32 v120, v94, v112\n\t"
    "v_and_or_b32 v95, v79, s44, v68\n\t"
    "ds_or_rtn_b32 v121, v95, v113\n\t"
    "v_and_or_b32 v96, v80, s44, v68\n\t"
    "ds_or_rtn_b32 v122, v96, v114\n\t"
    "v_and_or_b32 v97, v81, s44, v68\n\t"
    "ds_or_rtn_b32 v123, v97, v115\n\t"
    "v_and_or_b32 v98, v82, s44, v68\n\t"
    "ds_or_rtn_b32 v124, v98, v116\n\t"
    "v_and_or_b32 v99, v83, s44, v68\n\t"
    "ds_or_rtn_b32 v125, v99, v117\n\t"
    "v_lshl_or_b32 v164, v77, 16, v76\n\t"
    "v_lshl_or_b32 v165, v79, 16, v78\n\t"
    "v_lshl_or_b32 v166, v81, 16, v80\n\t"
    "v_lshl_or_b32 v167, v83, 16, v82\n\t"
    "s_movk_i32 s50, 0\n\t"
    "buffer_load_dwordx4 v[8:11], v64, %0, s50 offen offset:1616\n\t"
    "buffer_load_dwordx4 v[12:15], v64, %0, s50 offen offset:1632\n\t"
    "s_movk_i32 s50, 3200\n\t"
    "buffer_load_dwordx4 v[16:19], v64, %0, s50 offen offset:16\n\t"
    "buffer_load_dwordx4 v[20:23], v64, %0, s50 offen offset:32\n\t"
    "s_waitcnt vmcnt(2)\n\t"
    "v_lshl_add_u32 v84, v8, 3, s36\n\t"
    "v_lshl_add_u32 v85, v9, 3, s37\n\t"
    "v_lshl_add_u32 v86, v10, 3, s38\n\t"
    "v_lshl_add_u32 v87, v11, 3, s39\n\t"
    "v_lshl_add_u32 v88, v12, 3, s40\n\t"
    "v_lshl_add_u32 v89, v13, 3, s41\n\t"
    "v_lshl_add_u32 v90, v14, 3, s42\n\t"
    "v_lshl_add_u32 v91, v15, 3, s43\n\t"
    "s_waitcnt lgkmcnt(4)\n\t"
    "v_and_b32 v118, v118, v110\n\t"
    "v_perm_b32 v118, v118, v118, s46\n\t"
    "ds_or_b32 v92, v118\n\t"
    "v_and_b32 v119, v119, v111\n\t"
    "v_perm_b32 v119, v119, v119, s46\n\t"
    "ds_or_b32 v93, v119\n\t"
    "v_and_b32 v120, v120, v112\n\t"
    "v_perm_b32 v120, v120, v120, s46\n\t"
    "ds_or_b32 v94, v120\n\t"
    "v_and_b32 v121, v121, v113\n\t"
    "v_perm_b32 v121, v121, v121, s46\n\t"
    "ds_or_b32 v95, v121\n\t"
    "s_waitcnt lgkmcnt(4)\n\t"
    "v_and_b32 v122, v122, v114\n\t"
    "v_perm_b32 v122, v122, v122, s46\n\t"
    "ds_or_b32 v96, v122\n\t"
    "v_and_b32 v123, v123, v115\n\t"
    "v_perm_b32 v123, v123, v123, s46\n\t"
    "ds_or_b32 v97, v123\n\t"
    "v_and_b32 v124, v124, v116\n\t"
    "v_perm_b32 v124, v124, v124, s46\n\t"
    "ds_or_b32 v98, v124\n\t"
    "v_and_b32 v125, v125, v117\n\t"
    "v_perm_b32 v125, v125, v125, s46\n\t"
    "ds_or_b32 v99, v125\n\t"
    "buffer_store_dwordx4 v[164:167], v66, %12, s49 offen offset:0 sc1\n\t"
    "v_and_or_b32 v100, v84, s44, v68\n\t"
    "ds_read_b32 v100, v100\n\t"
    "v_and_or_b32 v101, v85, s44, v68\n\t"
    "ds_read_b32 v101, v101\n\t"
    "v_and_or_b32 v102, v86, s44, v68\n\t"
    "ds_read_b32 v102, v102\n\t"
    "v_and_or_b32 v103, v87, s44, v68\n\t"
    "ds_read_b32 v103, v103\n\t"
    "v_and_or_b32 v104, v88, s44, v68\n\t"
    "ds_read_b32 v104, v104\n\t"
    "v_and_or_b32 v105, v89, s44, v68\n\t"
    "ds_read_b32 v105, v105\n\t"
    "v_and_or_b32 v106, v90, s44, v68\n\t"
    "ds_read_b32 v106, v106\n\t"
    "v_and_or_b32 v107, v91, s44, v68\n\t"
    "ds_read_b32 v107, v107\n\t"
    "s_waitcnt vmcnt(1)\n\t"
    "v_lshl_add_u32 v76, v16, 3, s36\n\t"
    "v_lshl_add_u32 v77, v17, 3, s37\n\t"
    "v_lshl_add_u32 v78, v18, 3, s38\n\t"
    "v_lshl_add_u32 v79, v19, 3, s39\n\t"
    "v_lshl_add_u32 v80, v20, 3, s40\n\t"
    "v_lshl_add_u32 v81, v21, 3, s41\n\t"
    "v_lshl_add_u32 v82, v22, 3, s42\n\t"
    "v_lshl_add_u32 v83, v23, 3, s43\n\t"
    "s_movk_i32 s50, 3200\n\t"
    "buffer_load_dwordx4 v[24:27], v64, %0, s50 offen offset:1616\n\t"
    "buffer_load_dwordx4 v[28:31], v64, %0, s50 offen offset:1632\n\t"
    "s_movk_i32 s50, 6400\n\t"
    "buffer_load_dwordx4 v[32:35], v64, %0, s50 offen offset:16\n\t"
    "buffer_load_dwordx4 v[36:39], v64, %0, s50 offen offset:32\n\t"
    "v_mov_b32 v129, 0xffff0000\n\t"
    "s_waitcnt lgkmcnt(4)\n\t"
    "v_perm_b32 v100, v100, v84, v126\n\t"
    "v_perm_b32 v101, v101, v85, v126\n\t"
    "v_perm_b32 v102, v102, v86, v126\n\t"
    "v_perm_b32 v103, v103, v87, v126\n\t"
    "s_waitcnt lgkmcnt(0)\n\t"
    "v_perm_b32 v104, v104, v88, v126\n\t"
    "v_perm_b32 v105, v105, v89, v126\n\t"
    "v_perm_b32 v106, v106, v90, v126\n\t"
    "v_perm_b32 v107, v107, v91, v126\n\t"
    "v_max_u32 v108, v100, v101\n\t"
    "v_min_u32 v101, v100, v101\n\t"
    "v_max_u32 v100, v102, v103\n\t"
    "v_min_u32 v103, v102, v103\n\t"
    "v_max_u32 v102, v104, v105\n\t"
    "v_min_u32 v105, v104, v105\n\t"
    "v_max_u32 v104, v106, v107\n\t"
    "v_min_u32 v107, v106, v107\n\t"
    "v_max_u32 v106, v108, v100\n\t"
    "v_min_u32 v100, v108, v100\n\t"
    "v_max_u32 v108, v101, v103\n\t"
    "v_min_u32 v103, v101, v103\n\t"
    "v_max_u32 v101, v102, v104\n\t"
    "v_min_u32 v104, v102, v104\n\t"
    "v_max_u32 v102, v105, v107\n\t"
    "v_min_u32 v107, v105, v107\n\t"
    "v_bfi_b32 v130, v106, 0, v129\n\t"
    "v_cmp_eq_u32 vcc, 0, v130\n\t"
    "v_and_or_b32 v92, v106, s44, v68\n\t"
    "s_nop 0\n\t"
    "v_cndmask_b32 v130, v130, v129, vcc\n\t"
    "v_ffbl_b32 v131, v130\n\t"
    "v_and_b32 v134, 7, v131\n\t"
    "v_lshlrev_b32 v134, v134, s45\n\t"
    "v_xor_b32 v129, v129, v134\n\t"
    "v_xor_b32 v132, v131, v127\n\t"
    "v_bfi_b32 v130, v101, 0, v129\n\t"
    "v_cmp_eq_u32 vcc, 0, v130\n\t"
    "v_and_or_b32 v93, v101, s44, v68\n\t"
    "v_xor_b32 v133, 23, v132\n\t"
    "v_lshlrev_b32 v110, v132, v136\n\t"
    "v_and_b32 v132, 7, v132\n\t"
    "v_lshl_or_b32 v110, v136, v133, v110\n\t"
    "v_lshl_add_u32 v132, v132, 1, v128\n\t"
    "ds_or_rtn_b32 v118, v92, v110\n\t"
    "ds_write_b16 v132, v106\n\t"
    "v_cndmask_b32 v130, v130, v129, vcc\n\t"
    "v_ffbl_b32 v131, v130\n\t"
    "v_and_b32 v134, 7, v131\n\t"
    "v_lshlrev_b32 v134, v134, s45\n\t"
    "v_xor_b32 v129, v129, v134\n\t"
    "v_xor_b32 v132, v131, v127\n\t"
    "v_bfi_b32 v130, v108, 0, v129\n\t"
    "v_cmp_eq_u32 vcc, 0, v130\n\t"
    "v_and_or_b32 v94, v108, s44, v68\n\t"
    "v_xor_b32 v133, 23, v132\n\t"
    "v_lshlrev_b32 v111, v132, v136\n\t"
    "v_and_b32 v132, 7, v132\n\t"
    "v_lshl_or_b32 v111, v136, v133, v111\n\t"
    "v_lshl_add_u32 v132, v132, 1, v128\n\t"
    "ds_or_rtn_b32 v119, v93, v111\n\t"
    "ds_write_b16 v132, v101\n\t"
    "v_cndmask_b32 v130, v130, v129, vcc\n\t"
    "v_ffbl_b32 v131, v130\n\t"
    "v_and_b32 v134, 7, v131\n\t"
    "v_lshlrev_b32 v134, v134, s45\n\t"
    "v_xor_b32 v129, v129, v134\n\t"
    "v_xor_b32 v132, v131, v127\n\t"
    "v_bfi_b32 v130, v100, 0, v129\n\t"
    "v_cmp_eq_u32 vcc, 0, v130\n\t"
    "v_and_or_b32 v95, v100, s44, v68\n\t"
    "v_xor_b32 v133, 23, v132\n\t"
    "v_lshlrev_b32 v112, v132, v136\n\t"
    "v_and_b32 v132, 7, v132\n\t"
    "v_lshl_or_b32 v112, v136, v133, v112\n\t"
    "v_lshl_add_u32 v132, v132, 1, v128\n\t"
    "ds_or_rtn_b32 v120, v94, v112\n\t"
    "ds_write_b16 v132, v108\n\t"
    "v_cndmask_b32 v130, v130, v129, vcc\n\t"
    "v_ffbl_b32 v131, v130\n\t"
    "v_and_b32 v134, 7, v131\n\t"
    "v_lshlrev_b32 v134, v134, s45\n\t"
    "v_xor_b32 v129, v129, v134\n\t"
    "v_xor_b32 v132, v131, v127\n\t"
    "v_bfi_b32 v130, v102, 0, v129\n\t"
    "v_cmp_eq_u32 vcc, 0, v130\n\t"
    "v_and_or_b32 v96, v102, s44, v68\n\t"
    "v_xor_b32 v133, 23, v132\n\t"
    "v_lshlrev_b32 v113, v132, v136\n\t"
    "v_and_b32 v132, 7, v132\n\t"
    "v_lshl_or_b32 v113, v136, v133, v113\n\t"
    "v_lshl_add_u32 v132, v132, 1, v128\n\t"
    "ds_or_rtn_b32 v121, v95, v113\n\t"
    "ds_write_b16 v132, v100\n\t"
    "v_cndmask_b32 v130, v130, v129, vcc\n\t"
    "v_ffbl_b32 v131, v130\n\t"
    "v_and_b32 v134, 7, v131\n\t"
    "v_lshlrev_b32 v134, v134, s45\n\t"
    "v_xor_b32 v129, v129, v134\n\t"
    "v_xor_b32 v132, v131, v127\n\t"
    "s_waitcnt lgkmcnt(7)\n\t"
    "v_and_b32 v118, v118, v110\n\t"
    "v_perm_b32 v118, v118, v118, s46\n\t"
    "ds_or_b32 v92, v118\n\t"
    "v_bfi_b32 v130, v104, 0, v129\n\t"
    "v_cmp_eq_u32 vcc, 0, v130\n\t"
    "v_and_or_b32 v97, v104, s44, v68\n\t"
    "v_xor_b32 v133, 23, v132\n\t"
    "v_lshlrev_b32 v114, v132, v136\n\t"
    "v_and_b32 v132, 7, v132\n\t"
    "v_lshl_or_b32 v114, v136, v133, v114\n\t"
    "v_lshl_add_u32 v132, v132, 1, v128\n\t"
    "ds_or_rtn_b32 v122, v96, v114\n\t"
    "ds_write_b16 v132, v102\n\t"
    "v_cndmask_b32 v130, v130, v129, vcc\n\t"
    "v_ffbl_b32 v131, v130\n\t"
    "v_and_b32 v134, 7, v131\n\t"
    "v_lshlrev_b32 v134, v134, s45\n\t"
    "v_xor_b32 v129, v129, v134\n\t"
    "v_xor_b32 v132, v131, v127\n\t"
    "s_waitcnt lgkmcnt(8)\n\t"
    "v_and_b32 v119, v119, v111\n\t"
    "v_perm_b32 v119, v119, v119, s46\n\t"
    "ds_or_b32 v93, v119\n\t"
    "v_bfi_b32 v130, v103, 0, v129\n\t"
    "v_cmp_eq_u32 vcc, 0, v130\n\t"
    "v_and_or_b32 v98, v103, s44, v68\n\t"
    "v_xor_b32 v133, 23, v132\n\t"
    "v_lshlrev_b32 v115, v132, v136\n\t"
    "v_and_b32 v132, 7, v132\n\t"
    "v_lshl_or_b32 v115, v136, v133, v115\n\t"
    "v_lshl_add_u32 v132, v132, 1, v128\n\t"
    "ds_or_rtn_b32 v123, v97, v115\n\t"
    "ds_write_b16 v132, v104\n\t"
    "v_cndmask_b32 v130, v130, v129, vcc\n\t"
    "v_ffbl_b32 v131, v130\n\t"
    "v_and_b32 v134, 7, v131\n\t"
    "v_lshlrev_b32 v134, v134, s45\n\t"
    "v_xor_b32 v129, v129, v134\n\t"
    "v_xor_b32 v132, v131, v127\n\t"
    "s_waitcnt lgkmcnt(9)\n\t"
    "v_and_b32 v120, v120, v112\n\t"
    "v_perm_b32 v120, v120, v120, s46\n\t"
    "ds_or_b32 v94, v120\n\t"
    "v_bfi_b32 v130, v107, 0, v129\n\t"
    "v_cmp_eq_u32 vcc, 0, v130\n\t"
    "v_and_or_b32 v99, v107, s44, v68\n\t"
    "v_xor_b32 v133, 23, v132\n\t"
    "v_lshlrev_b32 v116, v132, v136\n\t"
    "v_and_b32 v132, 7, v132\n\t"
    "v_lshl_or_b32 v116, v136, v133, v116\n\t"
    "v_lshl_add_u32 v132, v132, 1, v128\n\t"
    "ds_or_rtn_b32 v124, v98, v116\n\t"
    "ds_write_b16 v132, v103\n\t"
    "v_cndmask_b32 v130, v130, v129, vcc\n\t"
    "v_ffbl_b32 v131, v130\n\t"
    "v_xor_b32 v132, v131, v127\n\t"
    "s_waitcnt lgkmcnt(10)\n\t"
    "v_and_b32 v121, v121, v113\n\t"
    "v_perm_b32 v121, v121, v121, s46\n\t"
    "ds_or_b32 v95, v121\n\t"
    "v_xor_b32 v133, 23, v132\n\t"
    "v_lshlrev_b32 v117, v132, v136\n\t"
    "v_and_b32 v132, 7, v132\n\t"
    "v_lshl_or_b32 v117, v136, v133, v117\n\t"
    "v_lshl_add_u32 v132, v132, 1, v128\n\t"
    "ds_or_rtn_b32 v125, v99, v117\n\t"
    "ds_write_b16 v132, v107\n\t"
    "s_waitcnt lgkmcnt(10)\n\t"
    "v_and_b32 v122, v122, v114\n\t"
    "v_perm_b32 v122, v122, v122, s46\n\t"
    "ds_or_b32 v96, v122\n\t"
    "ds_read_b128 v[72:75], v128\n\t"
    "v_and_or_b32 v100, v76, s44, v68\n\t"
    "ds_read_b32 v100, v100\n\t"
    "v_and_or_b32 v101, v77, s44, v68\n\t"
    "ds_read_b32 v101, v101\n\t"
    "v_and_or_b32 v102, v78, s44, v68\n\t"
    "ds_read_b32 v102, v102\n\t"
    "v_and_or_b32 v103, v79, s44, v68\n\t"
    "ds_read_b32 v103, v103\n\t"
    "v_and_or_b32 v104, v80, s44, v68\n\t"
    "ds_read_b32 v104, v104\n\t"
    "v_and_or_b32 v105, v81, s44, v68\n\t"
    "ds_read_b32 v105, v105\n\t"
    "v_and_or_b32 v106, v82, s44, v68\n\t"
    "ds_read_b32 v106, v106\n\t"
    "v_and_or_b32 v107, v83, s44, v68\n\t"
    "ds_read_b32 v107, v107\n\t"
    "s_waitcnt vmcnt(2)\n\t"
    "v_lshl_add_u32 v84, v24, 3, s36\n\t"
    "v_lshl_add_u32 v85, v25, 3, s37\n\t"
    "v_lshl_add_u32 v86, v26, 3, s38\n\t"
    "v_lshl_add_u32 v87, v27, 3, s39\n\t"
    "v_lshl_add_u32 v88, v28, 3, s40\n\t"
    "v_lshl_add_u32 v89, v29, 3, s41\n\t"
    "v_lshl_add_u32 v90, v30, 3, s42\n\t"
    "v_lshl_add_u32 v91, v31, 3, s43\n\t"
    "s_movk_i32 s50, 6400\n\t"
    "buffer_load_dwordx4 v[40:43], v65, %0, s50 offen offset:1616\n\t"
    "buffer_load_dwordx4 v[44:47], v65, %0, s50 offen offset:1632\n\t"
    "s_movk_i32 s50, 6400\n\t"
    "buffer_load_dwordx4 v[48:51], v65, %0, s50 offen offset:2416\n\t"
    "buffer_load_dwordx4 v[52:55], v65, %0, s50 offen offset:2432\n\t"
    "s_movk_i32 s50, 9600\n\t"
    "buffer_load_dwordx4 v[56:59], v65, %0, s50 offen offset:16\n\t"
    "buffer_load_dwordx4 v[60:63], v65, %0, s50 offen offset:32\n\t"
    "s_movk_i32 s50, 9600\n\t"
    "buffer_load_dwordx4 v[0:3], v65, %0, s50 offen offset:816\n\t"
    "buffer_load_dwordx4 v[4:7], v65, %0, s50 offen offset:832\n\t"
    "s_movk_i32 s50, 9600\n\t"
    "buffer_load_dwordx4 v[8:11], v65, %0, s50 offen offset:1616\n\t"
    "buffer_load_dwordx4 v[12:15], v65, %0, s50 offen offset:1632\n\t"
    "v_mov_b32 v134, 1032\n\t"
    "L_asvsp8_%=:\n\t"
    "ds_read_b32 v130, v134\n\t"
    "s_waitcnt lgkmcnt(0)\n\t"
    "v_readfirstlane_b32 s50, v130\n\t"
    "s_nop 0\n\t"
    "s_cmp_lt_u32 s50, 6\n\t"
    "s_cbranch_scc1 L_asvsp8_%=\n\t"
    "s_mov_b64 s[96:97], %2\n\t"
    "s_and_b32 s97, s97, 0xffff\n\t"
    "buffer_load_dwordx4 v[156:159], v137, s[96:99], s47 offen offset:0\n\t"
    "buffer_load_dwordx4 v[160:163], v138, s[96:99], s47 offen offset:0\n\t"
    "buffer_load_dwordx4 v[164:167], v137, s[96:99], s47 offen offset:64\n\t"
    "buffer_load_dwordx4 v[168:171], v138, s[96:99], s47 offen offset:64\n\t"
    "s_add_u32 s47, s47, 0x100000\n\t"
    "buffer_load_dwordx4 v[172:175], v137, s[96:99], s47 offen offset:0\n\t"
    "buffer_load_dwordx4 v[176:179], v138, s[96:99], s47 offen offset:0\n\t"
    "buffer_load_dwordx4 v[180:183], v137, s[96:99], s47 offen offset:64\n\t"
    "buffer_load_dwordx4 v[184:187], v138, s[96:99], s47 offen offset:64\n\t"
    "s_add_u32 s47, s47, 0x100000\n\t"
    "buffer_load_dwordx4 v[188:191], v137, s[96:99], s47 offen offset:0\n\t"
    "buffer_load_dwordx4 v[192:195], v138, s[96:99], s47 offen offset:0\n\t"
    "buffer_load_dwordx4 v[196:199], v137, s[96:99], s47 offen offset:64\n\t"
    "buffer_load_dwordx4 v[200:203], v138, s[96:99], s47 offen offset:64\n\t"
    "s_add_u32 s47, s47, 0x100000\n\t"
    "v_and_b32 v123, v123, v115\n\t"
    "v_perm_b32 v123, v123, v123, s46\n\t"
    "ds_or_b32 v97, v123\n\t"
    "v_and_b32 v124, v124, v116\n\t"
    "v_perm_b32 v124, v124, v124, s46\n\t"
    "ds_or_b32 v98, v124\n\t"
    "v_and_b32 v125, v125, v117\n\t"
    "v_perm_b32 v125, v125, v125, s46\n\t"
    "ds_or_b32 v99, v125\n\t"
    "buffer_store_dwordx4 v[72:75], v66, %12, s49 offen offset:64 sc1\n\t"
    "v_mov_b32 v129, 0xffff0000\n\t"
    "v_perm_b32 v100, v100, v76, v126\n\t"
    "v_perm_b32 v101, v101, v77, v126\n\t"
    "v_perm_b32 v102, v102, v78, v126\n\t"
    "v_perm_b32 v103, v103, v79, v126\n\t"
    "v_perm_b32 v104, v104, v80, v126\n\t"
    "v_perm_b32 v105, v105, v81, v126\n\t"
    "v_perm_b32 v106, v106, v82, v126\n\t"
    "v_perm_b32 v107, v107, v83, v126\n\t"
    "v_max_u32 v108, v100, v101\n\t"
    "v_min_u32 v101, v100, v101\n\t"
    "v_max_u32 v100, v102, v103\n\t"
    "v_min_u32 v103, v102, v103\n\t"
    "v_max_u32 v102, v104, v105\n\t"
    "v_min_u32 v105, v104, v105\n\t"
    "v_max_u32 v104, v106, v107\n\t"
    "v_min_u32 v107, v106, v107\n\t"
    "v_max_u32 v106, v108, v100\n\t"
    "v_min_u32 v100, v108, v100\n\t"
    "v_max_u32 v108, v101, v103\n\t"
    "v_min_u32 v103, v101, v103\n\t"
    "v_max_u32 v101, v102, v104\n\t"
    "v_min_u32 v104, v102, v104\n\t"
    "v_max_u32 v102, v105, v107\n\t"
    "v_min_u32 v107, v105, v107\n\t"
    "v_bfi_b32 v130, v106, 0, v129\n\t"
    "v_cmp_eq_u32 vcc, 0, v130\n\t"
    "v_and_or_b32 v92, v106, s44, v68\n\t"
    "s_nop 0\n\t"
    "v_cndmask_b32 v130, v130, v129, vcc\n\t"
    "v_ffbl_b32 v131, v130\n\t"
    "v_and_b32 v134, 7, v131\n\t"
    "v_lshlrev_b32 v134, v134, s45\n\t"
    "v_xor_b32 v129, v129, v134\n\t"
    "v_xor_b32 v132, v131, v127\n\t"
    "v_bfi_b32 v130, v101, 0, v129\n\t"
    "v_cmp_eq_u32 vcc, 0, v130\n\t"
    "v_and_or_b32 v93, v101, s44, v68\n\t"
    "v_xor_b32 v133, 23, v132\n\t"
    "v_lshlrev_b32 v110, v132, v136\n\t"
    "v_and_b32 v132, 7, v132\n\t"
    "v_lshl_or_b32 v110, v136, v133, v110\n\t"
    "v_lshl_add_u32 v132, v132, 1, v128\n\t"
    "ds_or_rtn_b32 v118, v92, v110\n\t"
    "ds_write_b16 v132, v106\n\t"
    "v_cndmask_b32 v130, v130, v129, vcc\n\t"
    "v_ffbl_b32 v131, v130\n\t"
    "v_and_b32 v134, 7, v131\n\t"
    "v_lshlrev_b32 v134, v134, s45\n\t"
    "v_xor_b32 v129, v129, v134\n\t"
    "v_xor_b32 v132, v131, v127\n\t"
    "v_bfi_b32 v130, v108, 0, v129\n\t"
    "v_cmp_eq_u32 vcc, 0, v130\n\t"
    "v_and_or_b32 v94, v108, s44, v68\n\t"
    "v_xor_b32 v133, 23, v132\n\t"
    "v_lshlrev_b32 v111, v132, v136\n\t"
    "v_and_b32 v132, 7, v132\n\t"
    "v_lshl_or_b32 v111, v136, v133, v111\n\t"
    "v_lshl_add_u32 v132, v132, 1, v128\n\t"
    "ds_or_rtn_b32 v119, v93, v111\n\t"
    "ds_write_b16 v132, v101\n\t"
    "v_cndmask_b32 v130, v130, v129, vcc\n\t"
    "v_ffbl_b32 v131, v130\n\t"
    "v_and_b32 v134, 7, v131\n\t"
    "v_lshlrev_b32 v134, v134, s45\n\t"
    "v_xor_b32 v129, v129, v134\n\t"
    "v_xor_b32 v132, v131, v127\n\t"
    "v_bfi_b32 v130, v100, 0, v129\n\t"
    "v_cmp_eq_u32 vcc, 0, v130\n\t"
    "v_and_or_b32 v95, v100, s44, v68\n\t"
    "v_xor_b32 v133, 23, v132\n\t"
    "v_lshlrev_b32 v112, v132, v136\n\t"
    "v_and_b32 v132, 7, v132\n\t"
    "v_lshl_or_b32 v112, v136, v133, v112\n\t"
    "v_lshl_add_u32 v132, v132, 1, v128\n\t"
    "ds_or_rtn_b32 v120, v94, v112\n\t"
    "ds_write_b16 v132, v108\n\t"
    "v_cndmask_b32 v130, v130, v129, vcc\n\t"
    "v_ffbl_b32 v131, v130\n\t"
    "v_and_b32 v134, 7, v131\n\t"
    "v_lshlrev_b32 v134, v134, s45\n\t"
    "v_xor_b32 v129, v129, v134\n\t"
    "v_xor_b32 v132, v131, v127\n\t"
    "v_bfi_b32 v130, v102, 0, v129\n\t"
    "v_cmp_eq_u32 vcc, 0, v130\n\t"
    "v_and_or_b32 v96, v102, s44, v68\n\t"
    "v_xor_b32 v133, 23, v132\n\t"
    "v_lshlrev_b32 v113, v132, v136\n\t"
    "v_and_b32 v132, 7, v132\n\t"
    "v_lshl_or_b32 v113, v136, v133, v113\n\t"
    "v_lshl_add_u32 v132, v132, 1, v128\n\t"
    "ds_or_rtn_b32 v121, v95, v113\n\t"
    "ds_write_b16 v132, v100\n\t"
    "v_cndmask_b32 v130, v130, v129, vcc\n\t"
    "v_ffbl_b32 v131, v130\n\t"
    "v_and_b32 v134, 7, v131\n\t"
    "v_lshlrev_b32 v134, v134, s45\n\t"
    "v_xor_b32 v129, v129, v134\n\t"
    "v_xor_b32 v132, v131, v127\n\t"
    "s_waitcnt lgkmcnt(7)\n\t"
    "v_and_b32 v118, v118, v110\n\t"
    "v_perm_b32 v118, v118, v118, s46\n\t"
    "ds_or_b32 v92, v118\n\t"
    "v_bfi_b32 v130, v104, 0, v129\n\t"
    "v_cmp_eq_u32 vcc, 0, v130\n\t"
    "v_and_or_b32 v97, v104, s44, v68\n\t"
    "v_xor_b32 v133, 23, v132\n\t"
    "v_lshlrev_b32 v114, v132, v136\n\t"
    "v_and_b32 v132, 7, v132\n\t"
    "v_lshl_or_b32 v114, v136, v133, v114\n\t"
    "v_lshl_add_u32 v132, v132, 1, v128\n\t"
    "ds_or_rtn_b32 v122, v96, v114\n\t"
    "ds_write_b16 v132, v102\n\t"
    "v_cndmask_b32 v130, v130, v129, vcc\n\t"
    "v_ffbl_b32 v131, v130\n\t"
    "v_and_b32 v134, 7, v131\n\t"
    "v_lshlrev_b32 v134, v134, s45\n\t"
    "v_xor_b32 v129, v129, v134\n\t"
    "v_xor_b32 v132, v131, v127\n\t"
    "s_waitcnt lgkmcnt(8)\n\t"
    "v_and_b32 v119, v119, v111\n\t"
    "v_perm_b32 v119, v119, v119, s46\n\t"
    "ds_or_b32 v93, v119\n\t"
    "v_bfi_b32 v130, v103, 0, v129\n\t"
    "v_cmp_eq_u32 vcc, 0, v130\n\t"
    "v_and_or_b32 v98, v103, s44, v68\n\t"
    "v_xor_b32 v133, 23, v132\n\t"
    "v_lshlrev_b32 v115, v132, v136\n\t"
    "v_and_b32 v132, 7, v132\n\t"
    "v_lshl_or_b32 v115, v136, v133, v115\n\t"
    "v_lshl_add_u32 v132, v132, 1, v128\n\t"
    "ds_or_rtn_b32 v123, v97, v115\n\t"
    "ds_write_b16 v132, v104\n\t"
    "v_cndmask_b32 v130, v130, v129, vcc\n\t"
    "v_ffbl_b32 v131, v130\n\t"
    "v_and_b32 v134, 7, v131\n\t"
    "v_lshlrev_b32 v134, v134, s45\n\t"
    "v_xor_b32 v129, v129, v134\n\t"
    "v_xor_b32 v132, v131, v127\n\t"
    "s_waitcnt lgkmcnt(9)\n\t"
    "v_and_b32 v120, v120, v112\n\t"
    "v_perm_b32 v120, v120, v120, s46\n\t"
    "ds_or_b32 v94, v120\n\t"
    "v_bfi_b32 v130, v107, 0, v129\n\t"
    "v_cmp_eq_u32 vcc, 0, v130\n\t"
    "v_and_or_b32 v99, v107, s44, v68\n\t"
    "v_xor_b32 v133, 23, v132\n\t"
    "v_lshlrev_b32 v116, v132, v136\n\t"
    "v_and_b32 v132, 7, v132\n\t"
    "v_lshl_or_b32 v116, v136, v133, v116\n\t"
    "v_lshl_add_u32 v132, v132, 1, v128\n\t"
    "ds_or_rtn_b32 v124, v98, v116\n\t"
    "ds_write_b16 v132, v103\n\t"
    "v_cndmask_b32 v130, v130, v129, vcc\n\t"
    "v_ffbl_b32 v131, v130\n\t"
    "v_xor_b32 v132, v131, v127\n\t"
    "s_waitcnt lgkmcnt(10)\n\t"
    "v_and_b32 v121, v121, v113\n\t"
    "v_perm_b32 v121, v121, v121, s46\n\t"
    "ds_or_b32 v95, v121\n\t"
    "v_xor_b32 v133, 23, v132\n\t"
    "v_lshlrev_b32 v117, v132, v136\n\t"
    "v_and_b32 v132, 7, v132\n\t"
    "v_lshl_or_b32 v117, v136, v133, v117\n\t"
    "v_lshl_add_u32 v132, v132, 1, v128\n\t"
    "ds_or_rtn_b32 v125, v99, v117\n\t"
    "ds_write_b16 v132, v107\n\t"
    "s_waitcnt lgkmcnt(10)\n\t"
    "v_and_b32 v122, v122, v114\n\t"
    "v_perm_b32 v122, v122, v122, s46\n\t"
    "ds_or_b32 v96, v122\n\t"
    "ds_read_b128 v[72:75], v128\n\t"
    "v_and_or_b32 v100, v84, s44, v68\n\t"
    "ds_read_b32 v100, v100\n\t"
    "v_and_or_b32 v101, v85, s44, v68\n\t"
    "ds_read_b32 v101, v101\n\t"
    "v_and_or_b32 v102, v86, s44, v68\n\t"
    "ds_read_b32 v102, v102\n\t"
    "v_and_or_b32 v103, v87, s44, v68\n\t"
    "ds_read_b32 v103, v103\n\t"
    "v_and_or_b32 v104, v88, s44, v68\n\t"
    "ds_read_b32 v104, v104\n\t"
    "v_and_or_b32 v105, v89, s44, v68\n\t"
    "ds_read_b32 v105, v105\n\t"
    "v_and_or_b32 v106, v90, s44, v68\n\t"
    "ds_read_b32 v106, v106\n\t"
    "v_and_or_b32 v107, v91, s44, v68\n\t"
    "ds_read_b32 v107, v107\n\t"
    "s_waitcnt vmcnt(23)\n\t"
    "v_lshl_add_u32 v76, v32, 3, s36\n\t"
    "v_lshl_add_u32 v77, v33, 3, s37\n\t"
    "v_lshl_add_u32 v78, v34, 3, s38\n\t"
    "v_lshl_add_u32 v79, v35, 3, s39\n\t"
    "v_lshl_add_u32 v80, v36, 3, s40\n\t"
    "v_lshl_add_u32 v81, v37, 3, s41\n\t"
    "v_lshl_add_u32 v82, v38, 3, s42\n\t"
    "v_lshl_add_u32 v83, v39, 3, s43\n\t"
    "s_movk_i32 s50, 9600\n\t"
    "buffer_load_dwordx4 v[16:19], v65, %0, s50 offen offset:2416\n\t"
    "buffer_load_dwordx4 v[20:23], v65, %0, s50 offen offset:2432\n\t"
    "buffer_load_dwordx4 v[204:207], v137, s[96:99], s47 offen offset:0\n\t"
    "buffer_load_dwordx4 v[208:211], v138, s[96:99], s47 offen offset:0\n\t"
    "buffer_load_dwordx4 v[212:215], v137, s[96:99], s47 offen offset:64\n\t"
    "buffer_load_dwordx4 v[216:219], v138, s[96:99], s47 offen offset:64\n\t"
    "s_add_u32 s47, s47, 0x100000\n\t"
    "buffer_load_dwordx4 v[220:223], v137, s[96:99], s47 offen offset:0\n\t"
    "buffer_load_dwordx4 v[224:227], v138, s[96:99], s47 offen offset:0\n\t"
    "buffer_load_dwordx4 v[228:231], v137, s[96:99], s47 offen offset:64\n\t"
    "buffer_load_dwordx4 v[232:235], v138, s[96:99], s47 offen offset:64\n\t"
    "s_add_u32 s47, s47, 0x100000\n\t"
    "buffer_load_dwordx4 v[236:239], v137, s[96:99], s47 offen offset:0\n\t"
    "buffer_load_dwordx4 v[240:243], v138, s[96:99], s47 offen offset:0\n\t"
    "buffer_load_dwordx4 v[244:247], v137, s[96:99], s47 offen offset:64\n\t"
    "buffer_load_dwordx4 v[248:251], v138, s[96:99], s47 offen offset:64\n\t"
    "s_add_u32 s47, s47, 0x100000\n\t"
    "s_waitcnt lgkmcnt(11)\n\t"
    "v_and_b32 v123, v123, v115\n\t"
    "v_perm_b32 v123, v123, v123, s46\n\t"
    "ds_or_b32 v97, v123\n\t"
    "v_and_b32 v124, v124, v116\n\t"
    "v_perm_b32 v124, v124, v124, s46\n\t"
    "ds_or_b32 v98, v124\n\t"
    "v_and_b32 v125, v125, v117\n\t"
    "v_perm_b32 v125, v125, v125, s46\n\t"
    "ds_or_b32 v99, v125\n\t"
    "s_waitcnt lgkmcnt(11)\n\t"
    "buffer_store_dwordx4 v[72:75], v66, %12, s49 offen offset:128 sc1\n\t"
    "v_mov_b32 v129, 0xffff0000\n\t"
    "s_waitcnt lgkmcnt(7)\n\t"
    "v_perm_b32 v100, v100, v84, v126\n\t"
    "v_perm_b32 v101, v101, v85, v126\n\t"
    "v_perm_b32 v102, v102, v86, v126\n\t"
    "v_perm_b32 v103, v103, v87, v126\n\t"
    "s_waitcnt lgkmcnt(3)\n\t"
    "v_perm_b32 v104, v104, v88, v126\n\t"
    "v_perm_b32 v105, v105, v89, v126\n\t"
    "v_perm_b32 v106, v106, v90, v126\n\t"
    "v_perm_b32 v107, v107, v91, v126\n\t"
    "v_max_u32 v108, v100, v101\n\t"
    "v_min_u32 v101, v100, v101\n\t"
    "v_max_u32 v100, v102, v103\n\t"
    "v_min_u32 v103, v102, v103\n\t"
    "v_max_u32 v102, v104, v105\n\t"
    "v_min_u32 v105, v104, v105\n\t"
    "v_max_u32 v104, v106, v107\n\t"
    "v_min_u32 v107, v106, v107\n\t"
    "v_max_u32 v106, v108, v100\n\t"
    "v_min_u32 v100, v108, v100\n\t"
    "v_max_u32 v108, v101, v103\n\t"
    "v_min_u32 v103, v101, v103\n\t"
    "v_max_u32 v101, v102, v104\n\t"
    "v_min_u32 v104, v102, v104\n\t"
    "v_max_u32 v102, v105, v107\n\t"
    "v_min_u32 v107, v105, v107\n\t"
    "v_bfi_b32 v130, v106, 0, v129\n\t"
    "v_cmp_eq_u32 vcc, 0, v130\n\t"
    "v_and_or_b32 v92, v106, s44, v68\n\t"
    "s_nop 0\n\t"
    "v_cndmask_b32 v130, v130, v129, vcc\n\t"
    "v_ffbl_b32 v131, v130\n\t"
    "v_and_b32 v134, 7, v131\n\t"
    "v_lshlrev_b32 v134, v134, s45\n\t"
    "v_xor_b32 v129, v129, v134\n\t"
    "v_xor_b32 v132, v131, v127\n\t"
    "v_bfi_b32 v130, v101, 0, v129\n\t"
    "v_cmp_eq_u32 vcc, 0, v130\n\t"
    "v_and_or_b32 v93, v101, s44, v68\n\t"
    "v_xor_b32 v133, 23, v132\n\t"
    "v_lshlrev_b32 v110, v132, v136\n\t"
    "v_and_b32 v132, 7, v132\n\t"
    "v_lshl_or_b32 v110, v136, v133, v110\n\t"
    "v_lshl_add_u32 v132, v132, 1, v128\n\t"
    "ds_or_rtn_b32 v118, v92, v110\n\t"
    "ds_write_b16 v132, v106\n\t"
    "v_cndmask_b32 v130, v130, v129, vcc\n\t"
    "v_ffbl_b32 v131, v130\n\t"
    "v_and_b32 v134, 7, v131\n\t"
    "v_lshlrev_b32 v134, v134, s45\n\t"
    "v_xor_b32 v129, v129, v134\n\t"
    "v_xor_b32 v132, v131, v127\n\t"
    "v_bfi_b32 v130, v108, 0, v129\n\t"
    "v_cmp_eq_u32 vcc, 0, v130\n\t"
    "v_and_or_b32 v94, v108, s44, v68\n\t"
    "v_xor_b32 v133, 23, v132\n\t"
    "v_lshlrev_b32 v111, v132, v136\n\t"
    "v_and_b32 v132, 7, v132\n\t"
    "v_lshl_or_b32 v111, v136, v133, v111\n\t"
    "v_lshl_add_u32 v132, v132, 1, v128\n\t"
    "ds_or_rtn_b32 v119, v93, v111\n\t"
    "ds_write_b16 v132, v101\n\t"
    "v_cndmask_b32 v130, v130, v129, vcc\n\t"
    "v_ffbl_b32 v131, v130\n\t"
    "v_and_b32 v134, 7, v131\n\t"
    "v_lshlrev_b32 v134, v134, s45\n\t"
    "v_xor_b32 v129, v129, v134\n\t"
    "v_xor_b32 v132, v131, v127\n\t"
    "v_bfi_b32 v130, v100, 0, v129\n\t"
    "v_cmp_eq_u32 vcc, 0, v130\n\t"
    "v_and_or_b32 v95, v100, s44, v68\n\t"
    "v_xor_b32 v133, 23, v132\n\t"
    "v_lshlrev_b32 v112, v132, v136\n\t"
    "v_and_b32 v132, 7, v132\n\t"
    "v_lshl_or_b32 v112, v136, v133, v112\n\t"
    "v_lshl_add_u32 v132, v132, 1, v128\n\t"
    "ds_or_rtn_b32 v120, v94, v112\n\t"
    "ds_write_b16 v132, v108\n\t"
    "v_cndmask_b32 v130, v130, v129, vcc\n\t"
    "v_ffbl_b32 v131, v130\n\t"
    "v_and_b32 v134, 7, v131\n\t"
    "v_lshlrev_b32 v134, v134, s45\n\t"
    "v_xor_b32 v129, v129, v134\n\t"
    "v_xor_b32 v132, v131, v127\n\t"
    "v_bfi_b32 v130, v102, 0, v129\n\t"
    "v_cmp_eq_u32 vcc, 0, v130\n\t"
    "v_and_or_b32 v96, v102, s44, v68\n\t"
    "v_xor_b32 v133, 23, v132\n\t"
    "v_lshlrev_b32 v113, v132, v136\n\t"
    "v_and_b32 v132, 7, v132\n\t"
    "v_lshl_or_b32 v113, v136, v133, v113\n\t"
    "v_lshl_add_u32 v132, v132, 1, v128\n\t"
    "ds_or_rtn_b32 v121, v95, v113\n\t"
    "ds_write_b16 v132, v100\n\t"
    "v_cndmask_b32 v130, v130, v129, vcc\n\t"
    "v_ffbl_b32 v131, v130\n\t"
    "v_and_b32 v134, 7, v131\n\t"
    "v_lshlrev_b32 v134, v134, s45\n\t"
    "v_xor_b32 v129, v129, v134\n\t"
    "v_xor_b32 v132, v131, v127\n\t"
    "s_waitcnt lgkmcnt(7)\n\t"
    "v_and_b32 v118, v118, v110\n\t"
    "v_perm_b32 v118, v118, v118, s46\n\t"
    "ds_or_b32 v92, v118\n\t"
    "v_bfi_b32 v130, v104, 0, v129\n\t"
    "v_cmp_eq_u32 vcc, 0, v130\n\t"
    "v_and_or_b32 v97, v104, s44, v68\n\t"
    "v_xor_b32 v133, 23, v132\n\t"
    "v_lshlrev_b32 v114, v132, v136\n\t"
    "v_and_b32 v132, 7, v132\n\t"
    "v_lshl_or_b32 v114, v136, v133, v114\n\t"
    "v_lshl_add_u32 v132, v132, 1, v128\n\t"
    "ds_or_rtn_b32 v122, v96, v114\n\t"
    "ds_write_b16 v132, v102\n\t"
    "v_cndmask_b32 v130, v130, v129, vcc\n\t"
    "v_ffbl_b32 v131, v130\n\t"
    "v_and_b32 v134, 7, v131\n\t"
    "v_lshlrev_b32 v134, v134, s45\n\t"
    "v_xor_b32 v129, v129, v134\n\t"
    "v_xor_b32 v132, v131, v127\n\t"
    "s_waitcnt lgkmcnt(8)\n\t"
    "v_and_b32 v119, v119, v111\n\t"
    "v_perm_b32 v119, v119, v119, s46\n\t"
    "ds_or_b32 v93, v119\n\t"
    "v_bfi_b32 v130, v103, 0, v129\n\t"
    "v_cmp_eq_u32 vcc, 0, v130\n\t"
    "v_and_or_b32 v98, v103, s44, v68\n\t"
    "v_xor_b32 v133, 23, v132\n\t"
    "v_lshlrev_b32 v115, v132, v136\n\t"
    "v_and_b32 v132, 7, v132\n\t"
    "v_lshl_or_b32 v115, v136, v133, v115\n\t"
    "v_lshl_add_u32 v132, v132, 1, v128\n\t"
    "ds_or_rtn_b32 v123, v97, v115\n\t"
    "ds_write_b16 v132, v104\n\t"
    "v_cndmask_b32 v130, v130, v129, vcc\n\t"
    "v_ffbl_b32 v131, v130\n\t"
    "v_and_b32 v134, 7, v131\n\t"
    "v_lshlrev_b32 v134, v134, s45\n\t"
    "v_xor_b32 v129, v129, v134\n\t"
    "v_xor_b32 v132, v131, v127\n\t"
    "s_waitcnt lgkmcnt(9)\n\t"
    "v_and_b32 v120, v120, v112\n\t"
    "v_perm_b32 v120, v120, v120, s46\n\t"
    "ds_or_b32 v94, v120\n\t"
    "v_bfi_b32 v130, v107, 0, v129\n\t"
    "v_cmp_eq_u32 vcc, 0, v130\n\t"
    "v_and_or_b32 v99, v107, s44, v68\n\t"
    "v_xor_b32 v133, 23, v132\n\t"
    "v_lshlrev_b32 v116, v132, v136\n\t"
    "v_and_b32 v132, 7, v132\n\t"
    "v_lshl_or_b32 v116, v136, v133, v116\n\t"
    "v_lshl_add_u32 v132, v132, 1, v128\n\t"
    "ds_or_rtn_b32 v124, v98, v116\n\t"
    "ds_write_b16 v132, v103\n\t"
    "v_cndmask_b32 v130, v130, v129, vcc\n\t"
    "v_ffbl_b32 v131, v130\n\t"
    "v_xor_b32 v132, v131, v127\n\t"
    "s_waitcnt lgkmcnt(10)\n\t"
    "v_and_b32 v121, v121, v113\n\t"
    "v_perm_b32 v121, v121, v121, s46\n\t"
    "ds_or_b32 v95, v121\n\t"
    "v_xor_b32 v133, 23, v132\n\t"
    "v_lshlrev_b32 v117, v132, v136\n\t"
    "v_and_b32 v132, 7, v132\n\t"
    "v_lshl_or_b32 v117, v136, v133, v117\n\t"
    "v_lshl_add_u32 v132, v132, 1, v128\n\t"
    "ds_or_rtn_b32 v125, v99, v117\n\t"
    "ds_write_b16 v132, v107\n\t"
    "s_waitcnt lgkmcnt(10)\n\t"
    "v_and_b32 v122, v122, v114\n\t"
    "v_perm_b32 v122, v122, v122, s46\n\t"
    "ds_or_b32 v96, v122\n\t"
    "ds_read_b128 v[72:75], v128\n\t"
    "v_and_or_b32 v100, v76, s44, v68\n\t"
    "ds_read_b32 v100, v100\n\t"
    "v_and_or_b32 v101, v77, s44, v68\n\t"
    "ds_read_b32 v101, v101\n\t"
    "v_and_or_b32 v102, v78, s44, v68\n\t"
    "ds_read_b32 v102, v102\n\t"
    "v_and_or_b32 v103, v79, s44, v68\n\t"
    "ds_read_b32 v103, v103\n\t"
    "v_and_or_b32 v104, v80, s44, v68\n\t"
    "ds_read_b32 v104, v104\n\t"
    "v_and_or_b32 v105, v81, s44, v68\n\t"
    "ds_read_b32 v105, v105\n\t"
    "v_and_or_b32 v106, v82, s44, v68\n\t"
    "ds_read_b32 v106, v106\n\t"
    "v_and_or_b32 v107, v83, s44, v68\n\t"
    "ds_read_b32 v107, v107\n\t"
    "s_waitcnt vmcnt(36)\n\t"
    "v_lshl_add_u32 v84, v40, 3, s36\n\t"
    "v_lshl_add_u32 v85, v41, 3, s37\n\t"
    "v_lshl_add_u32 v86, v42, 3, s38\n\t"
    "v_lshl_add_u32 v87, v43, 3, s39\n\t"
    "v_lshl_add_u32 v88, v44, 3, s40\n\t"
    "v_lshl_add_u32 v89, v45, 3, s41\n\t"
    "v_lshl_add_u32 v90, v46, 3, s42\n\t"
    "v_lshl_add_u32 v91, v47, 3, s43\n\t"
    "buffer_load_dwordx4 v[24:27], v137, s[96:99], s47 offen offset:0\n\t"
    "buffer_load_dwordx4 v[28:31], v138, s[96:99], s47 offen offset:0\n\t"
    "buffer_load_dwordx4 v[32:35], v137, s[96:99], s47 offen offset:64\n\t"
    "buffer_load_dwordx4 v[36:39], v138, s[96:99], s47 offen offset:64\n\t"
    "s_add_u32 s47, s47, 0x100000\n\t"
    "buffer_load_dwordx4 v[40:43], v137, s[96:99], s47 offen offset:0\n\t"
    "buffer_load_dwordx4 v[44:47], v138, s[96:99], s47 offen offset:0\n\t"
    "s_waitcnt lgkmcnt(11)\n\t"
    "v_and_b32 v123, v123, v115\n\t"
    "v_perm_b32 v123, v123, v123, s46\n\t"
    "ds_or_b32 v97, v123\n\t"
    "v_and_b32 v124, v124, v116\n\t"
    "v_perm_b32 v124, v124, v124, s46\n\t"
    "ds_or_b32 v98, v124\n\t"
    "v_and_b32 v125, v125, v117\n\t"
    "v_perm_b32 v125, v125, v125, s46\n\t"
    "ds_or_b32 v99, v125\n\t"
    "s_waitcnt lgkmcnt(11)\n\t"
    "buffer_store_dwordx4 v[72:75], v66, %12, s49 offen offset:192 sc1\n\t"
    "v_mov_b32 v129, 0xffff0000\n\t"
    "s_waitcnt lgkmcnt(7)\n\t"
    "v_perm_b32 v100, v100, v76, v126\n\t"
    "v_perm_b32 v101, v101, v77, v126\n\t"
    "v_perm_b32 v102, v102, v78, v126\n\t"
    "v_perm_b32 v103, v103, v79, v126\n\t"
    "s_waitcnt lgkmcnt(3)\n\t"
    "v_perm_b32 v104, v104, v80, v126\n\t"
    "v_perm_b32 v105, v105, v81, v126\n\t"
    "v_perm_b32 v106, v106, v82, v126\n\t"
    "v_perm_b32 v107, v107, v83, v126\n\t"
    "s_waitcnt vmcnt(33)\n\t"
    "ds_write_b128 v141, v[156:159] offset:0\n\t"
    "ds_write_b128 v141, v[160:163] offset:256\n\t"
    "ds_read_b64_tr_b16 v[156:157], v142 offset:0\n\t"
    "ds_read_b64_tr_b16 v[158:159], v142 offset:256\n\t"
    "ds_read_b64_tr_b16 v[160:161], v142 offset:32\n\t"
    "ds_read_b64_tr_b16 v[162:163], v142 offset:288\n\t"
    "s_waitcnt vmcnt(31)\n\t"
    "ds_write_b128 v141, v[164:167] offset:2112\n\t"
    "ds_write_b128 v141, v[168:171] offset:2368\n\t"
    "ds_read_b64_tr_b16 v[164:165], v142 offset:2112\n\t"
    "ds_read_b64_tr_b16 v[166:167], v142 offset:2368\n\t"
    "ds_read_b64_tr_b16 v[168:169], v142 offset:2144\n\t"
    "ds_read_b64_tr_b16 v[170:171], v142 offset:2400\n\t"
    "v_max_u32 v108, v100, v101\n\t"
    "v_min_u32 v101, v100, v101\n\t"
    "v_max_u32 v100, v102, v103\n\t"
    "v_min_u32 v103, v102, v103\n\t"
    "v_max_u32 v102, v104, v105\n\t"
    "v_min_u32 v105, v104, v105\n\t"
    "v_max_u32 v104, v106, v107\n\t"
    "v_min_u32 v107, v106, v107\n\t"
    "v_max_u32 v106, v108, v100\n\t"
    "v_min_u32 v100, v108, v100\n\t"
    "v_max_u32 v108, v101, v103\n\t"
    "v_min_u32 v103, v101, v103\n\t"
    "v_max_u32 v101, v102, v104\n\t"
    "v_min_u32 v104, v102, v104\n\t"
    "v_max_u32 v102, v105, v107\n\t"
    "v_min_u32 v107, v105, v107\n\t"
    "v_bfi_b32 v130, v106, 0, v129\n\t"
    "v_cmp_eq_u32 vcc, 0, v130\n\t"
    "v_and_or_b32 v92, v106, s44, v68\n\t"
    "s_nop 0\n\t"
    "v_cndmask_b32 v130, v130, v129, vcc\n\t"
    "v_ffbl_b32 v131, v130\n\t"
    "v_and_b32 v134, 7, v131\n\t"
    "v_lshlrev_b32 v134, v134, s45\n\t"
    "v_xor_b32 v129, v129, v134\n\t"
    "v_xor_b32 v132, v131, v127\n\t"
    "v_bfi_b32 v130, v101, 0, v129\n\t"
    "v_cmp_eq_u32 vcc, 0, v130\n\t"
    "v_and_or_b32 v93, v101, s44, v68\n\t"
    "v_xor_b32 v133, 23, v132\n\t"
    "v_lshlrev_b32 v110, v132, v136\n\t"
    "v_and_b32 v132, 7, v132\n\t"
    "v_lshl_or_b32 v110, v136, v133, v110\n\t"
    "v_lshl_add_u32 v132, v132, 1, v128\n\t"
    "ds_or_rtn_b32 v118, v92, v110\n\t"
    "ds_write_b16 v132, v106\n\t"
    "v_cndmask_b32 v130, v130, v129, vcc\n\t"
    "v_ffbl_b32 v131, v130\n\t"
    "v_and_b32 v134, 7, v131\n\t"
    "v_lshlrev_b32 v134, v134, s45\n\t"
    "v_xor_b32 v129, v129, v134\n\t"
    "v_xor_b32 v132, v131, v127\n\t"
    "v_bfi_b32 v130, v108, 0, v129\n\t"
    "v_cmp_eq_u32 vcc, 0, v130\n\t"
    "v_and_or_b32 v94, v108, s44, v68\n\t"
    "v_xor_b32 v133, 23, v132\n\t"
    "v_lshlrev_b32 v111, v132, v136\n\t"
    "v_and_b32 v132, 7, v132\n\t"
    "v_lshl_or_b32 v111, v136, v133, v111\n\t"
    "v_lshl_add_u32 v132, v132, 1, v128\n\t"
    "ds_or_rtn_b32 v119, v93, v111\n\t"
    "ds_write_b16 v132, v101\n\t"
    "v_cndmask_b32 v130, v130, v129, vcc\n\t"
    "v_ffbl_b32 v131, v130\n\t"
    "v_and_b32 v134, 7, v131\n\t"
    "v_lshlrev_b32 v134, v134, s45\n\t"
    "v_xor_b32 v129, v129, v134\n\t"
    "v_xor_b32 v132, v131, v127\n\t"
    "s_waitcnt lgkmcnt(10)\n\t"
    "buffer_store_dwordx4 v[156:159], v139, %12, s48 offen sc1\n\t"
    "buffer_store_dwordx4 v[160:163], v140, %12, s48 offen sc1\n\t"
    "s_add_u32 s48, s48, 819200\n\t"
    "s_waitcnt lgkmcnt(4)\n\t"
    "buffer_store_dwordx4 v[164:167], v139, %12, s48 offen sc1\n\t"
    "buffer_store_dwordx4 v[168:171], v140, %12, s48 offen sc1\n\t"
    "s_add_u32 s48, s48, -811008\n\t"
    "s_waitcnt vmcnt(33)\n\t"
    "ds_write_b128 v141, v[172:175] offset:0\n\t"
    "ds_write_b128 v141, v[176:179] offset:256\n\t"
    "ds_read_b64_tr_b16 v[172:173], v142 offset:0\n\t"
    "ds_read_b64_tr_b16 v[174:175], v142 offset:256\n\t"
    "ds_read_b64_tr_b16 v[176:177], v142 offset:32\n\t"
    "ds_read_b64_tr_b16 v[178:179], v142 offset:288\n\t"
    "s_waitcnt vmcnt(31)\n\t"
    "ds_write_b128 v141, v[180:183] offset:2112\n\t"
    "ds_write_b128 v141, v[184:187] offset:2368\n\t"
    "ds_read_b64_tr_b16 v[180:181], v142 offset:2112\n\t"
    "ds_read_b64_tr_b16 v[182:183], v142 offset:2368\n\t"
    "ds_read_b64_tr_b16 v[184:185], v142 offset:2144\n\t"
    "ds_read_b64_tr_b16 v[186:187], v142 offset:2400\n\t"
    "v_bfi_b32 v130, v100, 0, v129\n\t"
    "v_cmp_eq_u32 vcc, 0, v130\n\t"
    "v_and_or_b32 v95, v100, s44, v68\n\t"
    "v_xor_b32 v133, 23, v132\n\t"
    "v_lshlrev_b32 v112, v132, v136\n\t"
    "v_and_b32 v132, 7, v132\n\t"
    "v_lshl_or_b32 v112, v136, v133, v112\n\t"
    "v_lshl_add_u32 v132, v132, 1, v128\n\t"
    "ds_or_rtn_b32 v120, v94, v112\n\t"
    "ds_write_b16 v132, v108\n\t"
    "v_cndmask_b32 v130, v130, v129, vcc\n\t"
    "v_ffbl_b32 v131, v130\n\t"
    "v_and_b32 v134, 7, v131\n\t"
    "v_lshlrev_b32 v134, v134, s45\n\t"
    "v_xor_b32 v129, v129, v134\n\t"
    "v_xor_b32 v132, v131, v127\n\t"
    "v_bfi_b32 v130, v102, 0, v129\n\t"
    "v_cmp_eq_u32 vcc, 0, v130\n\t"
    "v_and_or_b32 v96, v102, s44, v68\n\t"
    "v_xor_b32 v133, 23, v132\n\t"
    "v_lshlrev_b32 v113, v132, v136\n\t"
    "v_and_b32 v132, 7, v132\n\t"
    "v_lshl_or_b32 v113, v136, v133, v113\n\t"
    "v_lshl_add_u32 v132, v132, 1, v128\n\t"
    "ds_or_rtn_b32 v121, v95, v113\n\t"
    "ds_write_b16 v132, v100\n\t"
    "v_cndmask_b32 v130, v130, v129, vcc\n\t"
    "v_ffbl_b32 v131, v130\n\t"
    "v_and_b32 v134, 7, v131\n\t"
    "v_lshlrev_b32 v134, v134, s45\n\t"
    "v_xor_b32 v129, v129, v134\n\t"
    "v_xor_b32 v132, v131, v127\n\t"
    "s_waitcnt lgkmcnt(15)\n\t"
    "v_and_b32 v118, v118, v110\n\t"
    "v_perm_b32 v118, v118, v118, s46\n\t"
    "ds_or_b32 v92, v118\n\t"
    "s_lshl_b32 s52, %14, 2\n\t"
    "s_add_u32 s52, s52, 48242688\n\t"
    "buffer_load_dword v71, off, %12, s52 offset:1024 sc1\n\t"
    "v_bfi_b32 v130, v104, 0, v129\n\t"
    "v_cmp_eq_u32 vcc, 0, v130\n\t"
    "v_and_or_b32 v97, v104, s44, v68\n\t"
    "v_xor_b32 v133, 23, v132\n\t"
    "v_lshlrev_b32 v114, v132, v136\n\t"
    "v_and_b32 v132, 7, v132\n\t"
    "v_lshl_or_b32 v114, v136, v133, v114\n\t"
    "v_lshl_add_u32 v132, v132, 1, v128\n\t"
    "ds_or_rtn_b32 v122, v96, v114\n\t"
    "ds_write_b16 v132, v102\n\t"
    "v_cndmask_b32 v130, v130, v129, vcc\n\t"
    "v_ffbl_b32 v131, v130\n\t"
    "v_and_b32 v134, 7, v131\n\t"
    "v_lshlrev_b32 v134, v134, s45\n\t"
    "v_xor_b32 v129, v129, v134\n\t"
    "v_xor_b32 v132, v131, v127\n\t"
    "v_and_b32 v119, v119, v111\n\t"
    "v_perm_b32 v119, v119, v119, s46\n\t"
    "ds_or_b32 v93, v119\n\t"
    "s_waitcnt lgkmcnt(14)\n\t"
    "buffer_store_dwordx4 v[172:175], v139, %12, s48 offen sc1\n\t"
    "buffer_store_dwordx4 v[176:179], v140, %12, s48 offen sc1\n\t"
    "s_add_u32 s48, s48, 819200\n\t"
    "s_waitcnt lgkmcnt(8)\n\t"
    "buffer_store_dwordx4 v[180:183], v139, %12, s48 offen sc1\n\t"
    "buffer_store_dwordx4 v[184:187], v140, %12, s48 offen sc1\n\t"
    "s_add_u32 s48, s48, -811008\n\t"
    "s_waitcnt vmcnt(34)\n\t"
    "ds_write_b128 v141, v[188:191] offset:0\n\t"
    "ds_write_b128 v141, v[192:195] offset:256\n\t"
    "ds_read_b64_tr_b16 v[188:189], v142 offset:0\n\t"
    "ds_read_b64_tr_b16 v[190:191], v142 offset:256\n\t"
    "ds_read_b64_tr_b16 v[192:193], v142 offset:32\n\t"
    "ds_read_b64_tr_b16 v[194:195], v142 offset:288\n\t"
    "s_waitcnt vmcnt(32)\n\t"
    "ds_write_b128 v141, v[196:199] offset:2112\n\t"
    "ds_write_b128 v141, v[200:203] offset:2368\n\t"
    "ds_read_b64_tr_b16 v[196:197], v142 offset:2112\n\t"
    "ds_read_b64_tr_b16 v[198:199], v142 offset:2368\n\t"
    "ds_read_b64_tr_b16 v[200:201], v142 offset:2144\n\t"
    "ds_read_b64_tr_b16 v[202:203], v142 offset:2400\n\t"
    "v_bfi_b32 v130, v103, 0, v129\n\t"
    "v_cmp_eq_u32 vcc, 0, v130\n\t"
    "v_and_or_b32 v98, v103, s44, v68\n\t"
    "v_xor_b32 v133, 23, v132\n\t"
    "v_lshlrev_b32 v115, v132, v136\n\t"
    "v_and_b32 v132, 7, v132\n\t"
    "v_lshl_or_b32 v115, v136, v133, v115\n\t"
    "v_lshl_add_u32 v132, v132, 1, v128\n\t"
    "ds_or_rtn_b32 v123, v97, v115\n\t"
    "ds_write_b16 v132, v104\n\t"
    "v_cndmask_b32 v130, v130, v129, vcc\n\t"
    "v_ffbl_b32 v131, v130\n\t"
    "v_and_b32 v134, 7, v131\n\t"
    "v_lshlrev_b32 v134, v134, s45\n\t"
    "v_xor_b32 v129, v129, v134\n\t"
    "v_xor_b32 v132, v131, v127\n\t"
    "s_waitcnt lgkmcnt(15)\n\t"
    "v_and_b32 v120, v120, v112\n\t"
    "v_perm_b32 v120, v120, v120, s46\n\t"
    "ds_or_b32 v94, v120\n\t"
    "v_bfi_b32 v130, v107, 0, v129\n\t"
    "v_cmp_eq_u32 vcc, 0, v130\n\t"
    "v_and_or_b32 v99, v107, s44, v68\n\t"
    "v_xor_b32 v133, 23, v132\n\t"
    "v_lshlrev_b32 v116, v132, v136\n\t"
    "v_and_b32 v132, 7, v132\n\t"
    "v_lshl_or_b32 v116, v136, v133, v116\n\t"
    "v_lshl_add_u32 v132, v132, 1, v128\n\t"
    "ds_or_rtn_b32 v124, v98, v116\n\t"
    "ds_write_b16 v132, v103\n\t"
    "v_cndmask_b32 v130, v130, v129, vcc\n\t"
    "v_ffbl_b32 v131, v130\n\t"
    "v_xor_b32 v132, v131, v127\n\t"
    "v_and_b32 v121, v121, v113\n\t"
    "v_perm_b32 v121, v121, v121, s46\n\t"
    "ds_or_b32 v95, v121\n\t"
    "v_xor_b32 v133, 23, v132\n\t"
    "v_lshlrev_b32 v117, v132, v136\n\t"
    "v_and_b32 v132, 7, v132\n\t"
    "v_lshl_or_b32 v117, v136, v133, v117\n\t"
    "v_lshl_add_u32 v132, v132, 1, v128\n\t"
    "ds_or_rtn_b32 v125, v99, v117\n\t"
    "ds_write_b16 v132, v107\n\t"
    "v_and_b32 v122, v122, v114\n\t"
    "v_perm_b32 v122, v122, v122, s46\n\t"
    "ds_or_b32 v96, v122\n\t"
    "ds_read_b128 v[72:75], v128\n\t"
    "s_waitcnt lgkmcnt(15)\n\t"
    "buffer_store_dwordx4 v[188:191], v139, %12, s48 offen sc1\n\t"
    "buffer_store_dwordx4 v[192:195], v140, %12, s48 offen sc1\n\t"
    "s_add_u32 s48, s48, 819200\n\t"
    "s_waitcnt lgkmcnt(10)\n\t"
    "buffer_store_dwordx4 v[196:199], v139, %12, s48 offen sc1\n\t"
    "buffer_store_dwordx4 v[200:203], v140, %12, s48 offen sc1\n\t"
    "s_add_u32 s48, s48, -811008\n\t"
    "v_mov_b32 v134, 1036\n\t"
    "L_asvsp12_%=:\n\t"
    "ds_read_b32 v130, v134\n\t"
    "s_waitcnt lgkmcnt(0)\n\t"
    "v_readfirstlane_b32 s50, v130\n\t"
    "s_nop 0\n\t"
    "s_cmp_lt_u32 s50, 1\n\t"
    "s_cbranch_scc1 L_asvsp12_%=\n\t"
    "v_and_or_b32 v100, v84, s44, v69\n\t"
    "ds_read_b32 v100, v100\n\t"
    "v_and_or_b32 v101, v85, s44, v69\n\t"
    "ds_read_b32 v101, v101\n\t"
    "v_and_or_b32 v102, v86, s44, v69\n\t"
    "ds_read_b32 v102, v102\n\t"
    "v_and_or_b32 v103, v87, s44, v69\n\t"
    "ds_read_b32 v103, v103\n\t"
    "v_and_or_b32 v104, v88, s44, v69\n\t"
    "ds_read_b32 v104, v104\n\t"
    "v_and_or_b32 v105, v89, s44, v69\n\t"
    "ds_read_b32 v105, v105\n\t"
    "v_and_or_b32 v106, v90, s44, v69\n\t"
    "ds_read_b32 v106, v106\n\t"
    "v_and_or_b32 v107, v91, s44, v69\n\t"
    "ds_read_b32 v107, v107\n\t"
    "v_lshl_add_u32 v76, v48, 3, s36\n\t"
    "v_lshl_add_u32 v77, v49, 3, s37\n\t"
    "v_lshl_add_u32 v78, v50, 3, s38\n\t"
    "v_lshl_add_u32 v79, v51, 3, s39\n\t"
    "v_lshl_add_u32 v80, v52, 3, s40\n\t"
    "v_lshl_add_u32 v81, v53, 3, s41\n\t"
    "v_lshl_add_u32 v82, v54, 3, s42\n\t"
    "v_lshl_add_u32 v83, v55, 3, s43\n\t"
    "v_mov_b32 v134, 2076\n\t"
    "L_asvsp1052_%=:\n\t"
    "ds_read_b32 v130, v134\n\t"
    "s_waitcnt lgkmcnt(0)\n\t"
    "v_readfirstlane_b32 s50, v130\n\t"
    "s_nop 0\n\t"
    "s_cmp_lt_u32 s50, 1\n\t"
    "s_cbranch_scc1 L_asvsp1052_%=\n\t"
    "v_mov_b32 v70, 2072\n\t"
    "ds_read_b32 v70, v70\n\t"
    "s_and_b32 s50, %14, 7\n\t"
    "s_lshr_b32 s51, %14, 5\n\t"
    "s_add_u32 s50, s50, 8\n\t"
    "s_lshl_b32 s50, s50, 7\n\t"
    "s_lshl_b32 s51, s51, 7\n\t"
    "v_mov_b32 v132, s51\n\t"
    "v_writelane_b32 v132, s50, 0\n\t"
    "s_waitcnt vmcnt(8)\n\t"
    "s_waitcnt vmcnt(0)\n\t"
    "s_waitcnt lgkmcnt(0)\n\t"
    "v_cmp_ne_u32 vcc, v71, v70\n\t"
    "s_cbranch_vccz L_ahsok_%=\n\t"
    "s_mov_b32 s51, 1048576\n\t"
    "L_ahs_%=:\n\t"
    "buffer_load_dword v71, off, %12, s52 offset:1024 sc1\n\t"
    "s_sub_u32 s51, s51, 1\n\t"
    "s_waitcnt vmcnt(0)\n\t"
    "s_cmp_eq_u32 s51, 0\n\t"
    "s_cbranch_scc1 L_ahsok_%=\n\t"
    "v_cmp_ne_u32 vcc, v71, v70\n\t"
    "s_cbranch_vccnz L_ahs_%=\n\t"
    "L_ahsok_%=:\n\t"
    "s_mov_b32 s50, 48235520\n\t"
    "s_mov_b64 exec, 3\n\t"
    "buffer_atomic_add v136, v132, %12, s50 offen sc1\n\t"
    "s_mov_b64 exec, -1\n\t"
    "buffer_load_dwordx4 v[156:159], v137, s[96:99], s47 offen offset:64\n\t"
    "buffer_load_dwordx4 v[160:163], v138, s[96:99], s47 offen offset:64\n\t"
    "s_add_u32 s47, s47, 0x100000\n\t"
    "buffer_load_dwordx4 v[164:167], v137, s[96:99], s47 offen offset:0\n\t"
    "buffer_load_dwordx4 v[168:171], v138, s[96:99], s47 offen offset:0\n\t"
    "buffer_load_dwordx4 v[172:175], v137, s[96:99], s47 offen offset:64\n\t"
    "buffer_load_dwordx4 v[176:179], v138, s[96:99], s47 offen offset:64\n\t"
    "s_add_u32 s47, s47, 0x100000\n\t"
    "buffer_load_dwordx4 v[180:183], v137, s[96:99], s47 offen offset:0\n\t"
    "buffer_load_dwordx4 v[184:187], v138, s[96:99], s47 offen offset:0\n\t"
    "buffer_load_dwordx4 v[188:191], v137, s[96:99], s47 offen offset:64\n\t"
    "buffer_load_dwordx4 v[192:195], v138, s[96:99], s47 offen offset:64\n\t"
    "s_add_u32 s47, s47, 0x100000\n\t"
    "buffer_load_dwordx4 v[196:199], v137, s[96:99], s47 offen offset:0\n\t"
    "buffer_load_dwordx4 v[200:203], v138, s[96:99], s47 offen offset:0\n\t"
    "v_and_b32 v123, v123, v115\n\t"
    "v_perm_b32 v123, v123, v123, s46\n\t"
    "ds_or_b32 v97, v123\n\t"
    "v_and_b32 v124, v124, v116\n\t"
    "v_perm_b32 v124, v124, v124, s46\n\t"
    "ds_or_b32 v98, v124\n\t"
    "v_and_b32 v125, v125, v117\n\t"
    "v_perm_b32 v125, v125, v125, s46\n\t"
    "ds_or_b32 v99, v125\n\t"
    "buffer_store_dwordx4 v[72:75], v66, %12, s49 offen offset:256 sc1\n\t"
    "v_mov_b32 v129, 0xffff0000\n\t"
    "v_perm_b32 v100, v100, v84, v126\n\t"
    "v_perm_b32 v101, v101, v85, v126\n\t"
    "v_perm_b32 v102, v102, v86, v126\n\t"
    "v_perm_b32 v103, v103, v87, v126\n\t"
    "v_perm_b32 v104, v104, v88, v126\n\t"
    "v_perm_b32 v105, v105, v89, v126\n\t"
    "v_perm_b32 v106, v106, v90, v126\n\t"
    "v_perm_b32 v107, v107, v91, v126\n\t"
    "ds_write_b128 v141, v[204:207] offset:0\n\t"
    "ds_write_b128 v141, v[208:211] offset:256\n\t"
    "ds_read_b64_tr_b16 v[204:205], v142 offset:0\n\t"
    "ds_read_b64_tr_b16 v[206:207], v142 offset:256\n\t"
    "ds_read_b64_tr_b16 v[208:209], v142 offset:32\n\t"
    "ds_read_b64_tr_b16 v[210:211], v142 offset:288\n\t"
    "ds_write_b128 v141, v[212:215] offset:2112\n\t"
    "ds_write_b128 v141, v[216:219] offset:2368\n\t"
    "ds_read_b64_tr_b16 v[212:213], v142 offset:2112\n\t"
    "ds_read_b64_tr_b16 v[214:215], v142 offset:2368\n\t"
    "ds_read_b64_tr_b16 v[216:217], v142 offset:2144\n\t"
    "ds_read_b64_tr_b16 v[218:219], v142 offset:2400\n\t"
    "v_max_u32 v108, v100, v101\n\t"
    "v_min_u32 v101, v100, v101\n\t"
    "v_max_u32 v100, v102, v103\n\t"
    "v_min_u32 v103, v102, v103\n\t"
    "v_max_u32 v102, v104, v105\n\t"
    "v_min_u32 v105, v104, v105\n\t"
    "v_max_u32 v104, v106, v107\n\t"
    "v_min_u32 v107, v106, v107\n\t"
    "v_max_u32 v106, v108, v100\n\t"
    "v_min_u32 v100, v108, v100\n\t"
    "v_max_u32 v108, v101, v103\n\t"
    "v_min_u32 v103, v101, v103\n\t"
    "v_max_u32 v101, v102, v104\n\t"
    "v_min_u32 v104, v102, v104\n\t"
    "v_max_u32 v102, v105, v107\n\t"
    "v_min_u32 v107, v105, v107\n\t"
    "v_bfi_b32 v130, v106, 0, v129\n\t"
    "v_cmp_eq_u32 vcc, 0, v130\n\t"
    "v_and_or_b32 v92, v106, s44, v69\n\t"
    "s_nop 0\n\t"
    "v_cndmask_b32 v130, v130, v129, vcc\n\t"
    "v_ffbl_b32 v131, v130\n\t"
    "v_and_b32 v134, 7, v131\n\t"
    "v_lshlrev_b32 v134, v134, s45\n\t"
    "v_xor_b32 v129, v129, v134\n\t"
    "v_xor_b32 v132, v131, v127\n\t"
    "v_bfi_b32 v130, v101, 0, v129\n\t"
    "v_cmp_eq_u32 vcc, 0, v130\n\t"
    "v_and_or_b32 v93, v101, s44, v69\n\t"
    "v_xor_b32 v133, 23, v132\n\t"
    "v_lshlrev_b32 v110, v132, v136\n\t"
    "v_and_b32 v132, 7, v132\n\t"
    "v_lshl_or_b32 v110, v136, v133, v110\n\t"
    "v_lshl_add_u32 v132, v132, 1, v128\n\t"
    "ds_or_rtn_b32 v118, v92, v110\n\t"
    "ds_write_b16 v132, v106\n\t"
    "v_cndmask_b32 v130, v130, v129, vcc\n\t"
    "v_ffbl_b32 v131, v130\n\t"
    "v_and_b32 v134, 7, v131\n\t"
    "v_lshlrev_b32 v134, v134, s45\n\t"
    "v_xor_b32 v129, v129, v134\n\t"
    "v_xor_b32 v132, v131, v127\n\t"
    "v_bfi_b32 v130, v108, 0, v129\n\t"
    "v_cmp_eq_u32 vcc, 0, v130\n\t"
    "v_and_or_b32 v94, v108, s44, v69\n\t"
    "v_xor_b32 v133, 23, v132\n\t"
    "v_lshlrev_b32 v111, v132, v136\n\t"
    "v_and_b32 v132, 7, v132\n\t"
    "v_lshl_or_b32 v111, v136, v133, v111\n\t"
    "v_lshl_add_u32 v132, v132, 1, v128\n\t"
    "ds_or_rtn_b32 v119, v93, v111\n\t"
    "ds_write_b16 v132, v101\n\t"
    "v_cndmask_b32 v130, v130, v129, vcc\n\t"
    "v_ffbl_b32 v131, v130\n\t"
    "v_and_b32 v134, 7, v131\n\t"
    "v_lshlrev_b32 v134, v134, s45\n\t"
    "v_xor_b32 v129, v129, v134\n\t"
    "v_xor_b32 v132, v131, v127\n\t"
    "s_waitcnt lgkmcnt(10)\n\t"
    "buffer_store_dwordx4 v[204:207], v139, %12, s48 offen sc1\n\t"
    "buffer_store_dwordx4 v[208:211], v140, %12, s48 offen sc1\n\t"
    "s_add_u32 s48, s48, 819200\n\t"
    "s_waitcnt lgkmcnt(4)\n\t"
    "buffer_store_dwordx4 v[212:215], v139, %12, s48 offen sc1\n\t"
    "buffer_store_dwordx4 v[216:219], v140, %12, s48 offen sc1\n\t"
    "s_add_u32 s48, s48, -811008\n\t"
    "ds_write_b128 v141, v[220:223] offset:0\n\t"
    "ds_write_b128 v141, v[224:227] offset:256\n\t"
    "ds_read_b64_tr_b16 v[220:221], v142 offset:0\n\t"
    "ds_read_b64_tr_b16 v[222:223], v142 offset:256\n\t"
    "ds_read_b64_tr_b16 v[224:225], v142 offset:32\n\t"
    "ds_read_b64_tr_b16 v[226:227], v142 offset:288\n\t"
    "ds_write_b128 v141, v[228:231] offset:2112\n\t"
    "ds_write_b128 v141, v[232:235] offset:2368\n\t"
    "ds_read_b64_tr_b16 v[228:229], v142 offset:2112\n\t"
    "ds_read_b64_tr_b16 v[230:231], v142 offset:2368\n\t"
    "ds_read_b64_tr_b16 v[232:233], v142 offset:2144\n\t"
    "ds_read_b64_tr_b16 v[234:235], v142 offset:2400\n\t"
    "v_bfi_b32 v130, v100, 0, v129\n\t"
    "v_cmp_eq_u32 vcc, 0, v130\n\t"
    "v_and_or_b32 v95, v100, s44, v69\n\t"
    "v_xor_b32 v133, 23, v132\n\t"
    "v_lshlrev_b32 v112, v132, v136\n\t"
    "v_and_b32 v132, 7, v132\n\t"
    "v_lshl_or_b32 v112, v136, v133, v112\n\t"
    "v_lshl_add_u32 v132, v132, 1, v128\n\t"
    "ds_or_rtn_b32 v120, v94, v112\n\t"
    "ds_write_b16 v132, v108\n\t"
    "v_cndmask_b32 v130, v130, v129, vcc\n\t"
    "v_ffbl_b32 v131, v130\n\t"
    "v_and_b32 v134, 7, v131\n\t"
    "v_lshlrev_b32 v134, v134, s45\n\t"
    "v_xor_b32 v129, v129, v134\n\t"
    "v_xor_b32 v132, v131, v127\n\t"
    "v_bfi_b32 v130, v102, 0, v129\n\t"
    "v_cmp_eq_u32 vcc, 0, v130\n\t"
    "v_and_or_b32 v96, v102, s44, v69\n\t"
    "v_xor_b32 v133, 23, v132\n\t"
    "v_lshlrev_b32 v113, v132, v136\n\t"
    "v_and_b32 v132, 7, v132\n\t"
    "v_lshl_or_b32 v113, v136, v133, v113\n\t"
    "v_lshl_add_u32 v132, v132, 1, v128\n\t"
    "ds_or_rtn_b32 v121, v95, v113\n\t"
    "ds_write_b16 v132, v100\n\t"
    "v_cndmask_b32 v130, v130, v129, vcc\n\t"
    "v_ffbl_b32 v131, v130\n\t"
    "v_and_b32 v134, 7, v131\n\t"
    "v_lshlrev_b32 v134, v134, s45\n\t"
    "v_xor_b32 v129, v129, v134\n\t"
    "v_xor_b32 v132, v131, v127\n\t"
    "s_waitcnt lgkmcnt(15)\n\t"
    "v_and_b32 v118, v118, v110\n\t"
    "v_perm_b32 v118, v118, v118, s46\n\t"
    "ds_or_b32 v92, v118\n\t"
    "v_bfi_b32 v130, v104, 0, v129\n\t"
    "v_cmp_eq_u32 vcc, 0, v130\n\t"
    "v_and_or_b32 v97, v104, s44, v69\n\t"
    "v_xor_b32 v133, 23, v132\n\t"
    "v_lshlrev_b32 v114, v132, v136\n\t"
    "v_and_b32 v132, 7, v132\n\t"
    "v_lshl_or_b32 v114, v136, v133, v114\n\t"
    "v_lshl_add_u32 v132, v132, 1, v128\n\t"
    "ds_or_rtn_b32 v122, v96, v114\n\t"
    "ds_write_b16 v132, v102\n\t"
    "v_cndmask_b32 v130, v130, v129, vcc\n\t"
    "v_ffbl_b32 v131, v130\n\t"
    "v_and_b32 v134, 7, v131\n\t"
    "v_lshlrev_b32 v134, v134, s45\n\t"
    "v_xor_b32 v129, v129, v134\n\t"
    "v_xor_b32 v132, v131, v127\n\t"
    "v_and_b32 v119, v119, v111\n\t"
    "v_perm_b32 v119, v119, v119, s46\n\t"
    "ds_or_b32 v93, v119\n\t"
    "s_waitcnt lgkmcnt(14)\n\t"
    "buffer_store_dwordx4 v[220:223], v139, %12, s48 offen sc1\n\t"
    "buffer_store_dwordx4 v[224:227], v140, %12, s48 offen sc1\n\t"
    "s_add_u32 s48, s48, 819200\n\t"
    "s_waitcnt lgkmcnt(8)\n\t"
    "buffer_store_dwordx4 v[228:231], v139, %12, s48 offen sc1\n\t"
    "buffer_store_dwordx4 v[232:235], v140, %12, s48 offen sc1\n\t"
    "s_add_u32 s48, s48, -811008\n\t"
    "ds_write_b128 v141, v[236:239] offset:0\n\t"
    "ds_write_b128 v141, v[240:243] offset:256\n\t"
    "ds_read_b64_tr_b16 v[236:237], v142 offset:0\n\t"
    "ds_read_b64_tr_b16 v[238:239], v142 offset:256\n\t"
    "ds_read_b64_tr_b16 v[240:241], v142 offset:32\n\t"
    "ds_read_b64_tr_b16 v[242:243], v142 offset:288\n\t"
    "ds_write_b128 v141, v[244:247] offset:2112\n\t"
    "ds_write_b128 v141, v[248:251] offset:2368\n\t"
    "ds_read_b64_tr_b16 v[244:245], v142 offset:2112\n\t"
    "ds_read_b64_tr_b16 v[246:247], v142 offset:2368\n\t"
    "ds_read_b64_tr_b16 v[248:249], v142 offset:2144\n\t"
    "ds_read_b64_tr_b16 v[250:251], v142 offset:2400\n\t"
    "v_bfi_b32 v130, v103, 0, v129\n\t"
    "v_cmp_eq_u32 vcc, 0, v130\n\t"
    "v_and_or_b32 v98, v103, s44, v69\n\t"
    "v_xor_b32 v133, 23, v132\n\t"
    "v_lshlrev_b32 v115, v132, v136\n\t"
    "v_and_b32 v132, 7, v132\n\t"
    "v_lshl_or_b32 v115, v136, v133, v115\n\t"
    "v_lshl_add_u32 v132, v132, 1, v128\n\t"
    "ds_or_rtn_b32 v123, v97, v115\n\t"
    "ds_write_b16 v132, v104\n\t"
    "v_cndmask_b32 v130, v130, v129, vcc\n\t"
    "v_ffbl_b32 v131, v130\n\t"
    "v_and_b32 v134, 7, v131\n\t"
    "v_lshlrev_b32 v134, v134, s45\n\t"
    "v_xor_b32 v129, v129, v134\n\t"
    "v_xor_b32 v132, v131, v127\n\t"
    "s_waitcnt lgkmcnt(15)\n\t"
    "v_and_b32 v120, v120, v112\n\t"
    "v_perm_b32 v120, v120, v120, s46\n\t"
    "ds_or_b32 v94, v120\n\t"
    "v_bfi_b32 v130, v107, 0, v129\n\t"
    "v_cmp_eq_u32 vcc, 0, v130\n\t"
    "v_and_or_b32 v99, v107, s44, v69\n\t"
    "v_xor_b32 v133, 23, v132\n\t"
    "v_lshlrev_b32 v116, v132, v136\n\t"
    "v_and_b32 v132, 7, v132\n\t"
    "v_lshl_or_b32 v116, v136, v133, v116\n\t"
    "v_lshl_add_u32 v132, v132, 1, v128\n\t"
    "ds_or_rtn_b32 v124, v98, v116\n\t"
    "ds_write_b16 v132, v103\n\t"
    "v_cndmask_b32 v130, v130, v129, vcc\n\t"
    "v_ffbl_b32 v131, v130\n\t"
    "v_xor_b32 v132, v131, v127\n\t"
    "v_and_b32 v121, v121, v113\n\t"
    "v_perm_b32 v121, v121, v121, s46\n\t"
    "ds_or_b32 v95, v121\n\t"
    "v_xor_b32 v133, 23, v132\n\t"
    "v_lshlrev_b32 v117, v132, v136\n\t"
    "v_and_b32 v132, 7, v132\n\t"
    "v_lshl_or_b32 v117, v136, v133, v117\n\t"
    "v_lshl_add_u32 v132, v132, 1, v128\n\t"
    "ds_or_rtn_b32 v125, v99, v117\n\t"
    "ds_write_b16 v132, v107\n\t"
    "v_and_b32 v122, v122, v114\n\t"
    "v_perm_b32 v122, v122, v122, s46\n\t"
    "ds_or_b32 v96, v122\n\t"
    "ds_read_b128 v[72:75], v128\n\t"
    "v_and_or_b32 v100, v76, s44, v69\n\t"
    "ds_read_b32 v100, v100\n\t"
    "v_and_or_b32 v101, v77, s44, v69\n\t"
    "ds_read_b32 v101, v101\n\t"
    "v_and_or_b32 v102, v78, s44, v69\n\t"
    "ds_read_b32 v102, v102\n\t"
    "v_and_or_b32 v103, v79, s44, v69\n\t"
    "ds_read_b32 v103, v103\n\t"
    "v_and_or_b32 v104, v80, s44, v69\n\t"
    "ds_read_b32 v104, v104\n\t"
    "v_and_or_b32 v105, v81, s44, v69\n\t"
    "ds_read_b32 v105, v105\n\t"
    "v_and_or_b32 v106, v82, s44, v69\n\t"
    "ds_read_b32 v106, v106\n\t"
    "v_and_or_b32 v107, v83, s44, v69\n\t"
    "ds_read_b32 v107, v107\n\t"
    "v_lshl_add_u32 v84, v56, 3, s36\n\t"
    "v_lshl_add_u32 v85, v57, 3, s37\n\t"
    "v_lshl_add_u32 v86, v58, 3, s38\n\t"
    "v_lshl_add_u32 v87, v59, 3, s39\n\t"
    "v_lshl_add_u32 v88, v60, 3, s40\n\t"
    "v_lshl_add_u32 v89, v61, 3, s41\n\t"
    "v_lshl_add_u32 v90, v62, 3, s42\n\t"
    "v_lshl_add_u32 v91, v63, 3, s43\n\t"
    "s_waitcnt lgkmcnt(15)\n\t"
    "buffer_store_dwordx4 v[236:239], v139, %12, s48 offen sc1\n\t"
    "buffer_store_dwordx4 v[240:243], v140, %12, s48 offen sc1\n\t"
    "s_add_u32 s48, s48, 819200\n\t"
    "buffer_store_dwordx4 v[244:247], v139, %12, s48 offen sc1\n\t"
    "buffer_store_dwordx4 v[248:251], v140, %12, s48 offen sc1\n\t"
    "s_add_u32 s48, s48, -811008\n\t"
    "buffer_load_dwordx4 v[48:51], v137, s[96:99], s47 offen offset:64\n\t"
    "buffer_load_dwordx4 v[52:55], v138, s[96:99], s47 offen offset:64\n\t"
    "s_add_u32 s47, s47, 0x100000\n\t"
    "buffer_load_dwordx4 v[204:207], v137, s[96:99], s47 offen offset:0\n\t"
    "buffer_load_dwordx4 v[208:211], v138, s[96:99], s47 offen offset:0\n\t"
    "buffer_load_dwordx4 v[212:215], v137, s[96:99], s47 offen offset:64\n\t"
    "buffer_load_dwordx4 v[216:219], v138, s[96:99], s47 offen offset:64\n\t"
    "s_add_u32 s47, s47, 0x100000\n\t"
    "buffer_load_dwordx4 v[220:223], v137, s[96:99], s47 offen offset:0\n\t"
    "buffer_load_dwordx4 v[224:227], v138, s[96:99], s47 offen offset:0\n\t"
    "buffer_load_dwordx4 v[228:231], v137, s[96:99], s47 offen offset:64\n\t"
    "buffer_load_dwordx4 v[232:235], v138, s[96:99], s47 offen offset:64\n\t"
    "s_add_u32 s47, s47, 0x100000\n\t"
    "s_waitcnt lgkmcnt(11)\n\t"
    "v_and_b32 v123, v123, v115\n\t"
    "v_perm_b32 v123, v123, v123, s46\n\t"
    "ds_or_b32 v97, v123\n\t"
    "v_and_b32 v124, v124, v116\n\t"
    "v_perm_b32 v124, v124, v124, s46\n\t"
    "ds_or_b32 v98, v124\n\t"
    "v_and_b32 v125, v125, v117\n\t"
    "v_perm_b32 v125, v125, v125, s46\n\t"
    "ds_or_b32 v99, v125\n\t"
    "s_waitcnt lgkmcnt(11)\n\t"
    "buffer_store_dwordx4 v[72:75], v67, %12, s49 offen offset:320 sc1\n\t"
    "v_mov_b32 v129, 0xffff0000\n\t"
    "s_waitcnt lgkmcnt(7)\n\t"
    "v_perm_b32 v100, v100, v76, v126\n\t"
    "v_perm_b32 v101, v101, v77, v126\n\t"
    "v_perm_b32 v102, v102, v78, v126\n\t"
    "v_perm_b32 v103, v103, v79, v126\n\t"
    "s_waitcnt lgkmcnt(3)\n\t"
    "v_perm_b32 v104, v104, v80, v126\n\t"
    "v_perm_b32 v105, v105, v81, v126\n\t"
    "v_perm_b32 v106, v106, v82, v126\n\t"
    "v_perm_b32 v107, v107, v83, v126\n\t"
    "ds_write_b128 v141, v[24:27] offset:0\n\t"
    "ds_write_b128 v141, v[28:31] offset:256\n\t"
    "ds_read_b64_tr_b16 v[24:25], v142 offset:0\n\t"
    "ds_read_b64_tr_b16 v[26:27], v142 offset:256\n\t"
    "ds_read_b64_tr_b16 v[28:29], v142 offset:32\n\t"
    "ds_read_b64_tr_b16 v[30:31], v142 offset:288\n\t"
    "ds_write_b128 v141, v[32:35] offset:2112\n\t"
    "ds_write_b128 v141, v[36:39] offset:2368\n\t"
    "ds_read_b64_tr_b16 v[32:33], v142 offset:2112\n\t"
    "ds_read_b64_tr_b16 v[34:35], v142 offset:2368\n\t"
    "ds_read_b64_tr_b16 v[36:37], v142 offset:2144\n\t"
    "ds_read_b64_tr_b16 v[38:39], v142 offset:2400\n\t"
    "v_max_u32 v108, v100, v101\n\t"
    "v_min_u32 v101, v100, v101\n\t"
    "v_max_u32 v100, v102, v103\n\t"
    "v_min_u32 v103, v102, v103\n\t"
    "v_max_u32 v102, v104, v105\n\t"
    "v_min_u32 v105, v104, v105\n\t"
    "v_max_u32 v104, v106, v107\n\t"
    "v_min_u32 v107, v106, v107\n\t"
    "v_max_u32 v106, v108, v100\n\t"
    "v_min_u32 v100, v108, v100\n\t"
    "v_max_u32 v108, v101, v103\n\t"
    "v_min_u32 v103, v101, v103\n\t"
    "v_max_u32 v101, v102, v104\n\t"
    "v_min_u32 v104, v102, v104\n\t"
    "v_max_u32 v102, v105, v107\n\t"
    "v_min_u32 v107, v105, v107\n\t"
    "v_bfi_b32 v130, v106, 0, v129\n\t"
    "v_cmp_eq_u32 vcc, 0, v130\n\t"
    "v_and_or_b32 v92, v106, s44, v69\n\t"
    "s_nop 0\n\t"
    "v_cndmask_b32 v130, v130, v129, vcc\n\t"
    "v_ffbl_b32 v131, v130\n\t"
    "v_and_b32 v134, 7, v131\n\t"
    "v_lshlrev_b32 v134, v134, s45\n\t"
    "v_xor_b32 v129, v129, v134\n\t"
    "v_xor_b32 v132, v131, v127\n\t"
    "v_bfi_b32 v130, v101, 0, v129\n\t"
    "v_cmp_eq_u32 vcc, 0, v130\n\t"
    "v_and_or_b32 v93, v101, s44, v69\n\t"
    "v_xor_b32 v133, 23, v132\n\t"
    "v_lshlrev_b32 v110, v132, v136\n\t"
    "v_and_b32 v132, 7, v132\n\t"
    "v_lshl_or_b32 v110, v136, v133, v110\n\t"
    "v_lshl_add_u32 v132, v132, 1, v128\n\t"
    "ds_or_rtn_b32 v118, v92, v110\n\t"
    "ds_write_b16 v132, v106\n\t"
    "v_cndmask_b32 v130, v130, v129, vcc\n\t"
    "v_ffbl_b32 v131, v130\n\t"
    "v_and_b32 v134, 7, v131\n\t"
    "v_lshlrev_b32 v134, v134, s45\n\t"
    "v_xor_b32 v129, v129, v134\n\t"
    "v_xor_b32 v132, v131, v127\n\t"
    "v_bfi_b32 v130, v108, 0, v129\n\t"
    "v_cmp_eq_u32 vcc, 0, v130\n\t"
    "v_and_or_b32 v94, v108, s44, v69\n\t"
    "v_xor_b32 v133, 23, v132\n\t"
    "v_lshlrev_b32 v111, v132, v136\n\t"
    "v_and_b32 v132, 7, v132\n\t"
    "v_lshl_or_b32 v111, v136, v133, v111\n\t"
    "v_lshl_add_u32 v132, v132, 1, v128\n\t"
    "ds_or_rtn_b32 v119, v93, v111\n\t"
    "ds_write_b16 v132, v101\n\t"
    "v_cndmask_b32 v130, v130, v129, vcc\n\t"
    "v_ffbl_b32 v131, v130\n\t"
    "v_and_b32 v134, 7, v131\n\t"
    "v_lshlrev_b32 v134, v134, s45\n\t"
    "v_xor_b32 v129, v129, v134\n\t"
    "v_xor_b32 v132, v131, v127\n\t"
    "s_waitcnt lgkmcnt(10)\n\t"
    "buffer_store_dwordx4 v[24:27], v139, %12, s48 offen sc1\n\t"
    "buffer_store_dwordx4 v[28:31], v140, %12, s48 offen sc1\n\t"
    "s_add_u32 s48, s48, 819200\n\t"
    "s_waitcnt lgkmcnt(4)\n\t"
    "buffer_store_dwordx4 v[32:35], v139, %12, s48 offen sc1\n\t"
    "buffer_store_dwordx4 v[36:39], v140, %12, s48 offen sc1\n\t"
    "s_add_u32 s48, s48, -811008\n\t"
    "ds_write_b128 v141, v[40:43] offset:0\n\t"
    "ds_write_b128 v141, v[44:47] offset:256\n\t"
    "ds_read_b64_tr_b16 v[40:41], v142 offset:0\n\t"
    "ds_read_b64_tr_b16 v[42:43], v142 offset:256\n\t"
    "ds_read_b64_tr_b16 v[44:45], v142 offset:32\n\t"
    "ds_read_b64_tr_b16 v[46:47], v142 offset:288\n\t"
    "v_bfi_b32 v130, v100, 0, v129\n\t"
    "v_cmp_eq_u32 vcc, 0, v130\n\t"
    "v_and_or_b32 v95, v100, s44, v69\n\t"
    "v_xor_b32 v133, 23, v132\n\t"
    "v_lshlrev_b32 v112, v132, v136\n\t"
    "v_and_b32 v132, 7, v132\n\t"
    "v_lshl_or_b32 v112, v136, v133, v112\n\t"
    "v_lshl_add_u32 v132, v132, 1, v128\n\t"
    "ds_or_rtn_b32 v120, v94, v112\n\t"
    "ds_write_b16 v132, v108\n\t"
    "v_cndmask_b32 v130, v130, v129, vcc\n\t"
    "v_ffbl_b32 v131, v130\n\t"
    "v_and_b32 v134, 7, v131\n\t"
    "v_lshlrev_b32 v134, v134, s45\n\t"
    "v_xor_b32 v129, v129, v134\n\t"
    "v_xor_b32 v132, v131, v127\n\t"
    "v_bfi_b32 v130, v102, 0, v129\n\t"
    "v_cmp_eq_u32 vcc, 0, v130\n\t"
    "v_and_or_b32 v96, v102, s44, v69\n\t"
    "v_xor_b32 v133, 23, v132\n\t"
    "v_lshlrev_b32 v113, v132, v136\n\t"
    "v_and_b32 v132, 7, v132\n\t"
    "v_lshl_or_b32 v113, v136, v133, v113\n\t"
    "v_lshl_add_u32 v132, v132, 1, v128\n\t"
    "ds_or_rtn_b32 v121, v95, v113\n\t"
    "ds_write_b16 v132, v100\n\t"
    "v_cndmask_b32 v130, v130, v129, vcc\n\t"
    "v_ffbl_b32 v131, v130\n\t"
    "v_and_b32 v134, 7, v131\n\t"
    "v_lshlrev_b32 v134, v134, s45\n\t"
    "v_xor_b32 v129, v129, v134\n\t"
    "v_xor_b32 v132, v131, v127\n\t"
    "s_waitcnt lgkmcnt(13)\n\t"
    "v_and_b32 v118, v118, v110\n\t"
    "v_perm_b32 v118, v118, v118, s46\n\t"
    "ds_or_b32 v92, v118\n\t"
    "v_bfi_b32 v130, v104, 0, v129\n\t"
    "v_cmp_eq_u32 vcc, 0, v130\n\t"
    "v_and_or_b32 v97, v104, s44, v69\n\t"
    "v_xor_b32 v133, 23, v132\n\t"
    "v_lshlrev_b32 v114, v132, v136\n\t"
    "v_and_b32 v132, 7, v132\n\t"
    "v_lshl_or_b32 v114, v136, v133, v114\n\t"
    "v_lshl_add_u32 v132, v132, 1, v128\n\t"
    "ds_or_rtn_b32 v122, v96, v114\n\t"
    "ds_write_b16 v132, v102\n\t"
    "v_cndmask_b32 v130, v130, v129, vcc\n\t"
    "v_ffbl_b32 v131, v130\n\t"
    "v_and_b32 v134, 7, v131\n\t"
    "v_lshlrev_b32 v134, v134, s45\n\t"
    "v_xor_b32 v129, v129, v134\n\t"
    "v_xor_b32 v132, v131, v127\n\t"
    "s_waitcnt lgkmcnt(14)\n\t"
    "v_and_b32 v119, v119, v111\n\t"
    "v_perm_b32 v119, v119, v119, s46\n\t"
    "ds_or_b32 v93, v119\n\t"
    "s_waitcnt lgkmcnt(8)\n\t"
    "buffer_store_dwordx4 v[40:43], v139, %12, s48 offen sc1\n\t"
    "buffer_store_dwordx4 v[44:47], v140, %12, s48 offen sc1\n\t"
    "s_add_u32 s48, s48, 819200\n\t"
    "v_bfi_b32 v130, v103, 0, v129\n\t"
    "v_cmp_eq_u32 vcc, 0, v130\n\t"
    "v_and_or_b32 v98, v103, s44, v69\n\t"
    "v_xor_b32 v133, 23, v132\n\t"
    "v_lshlrev_b32 v115, v132, v136\n\t"
    "v_and_b32 v132, 7, v132\n\t"
    "v_lshl_or_b32 v115, v136, v133, v115\n\t"
    "v_lshl_add_u32 v132, v132, 1, v128\n\t"
    "ds_or_rtn_b32 v123, v97, v115\n\t"
    "ds_write_b16 v132, v104\n\t"
    "v_cndmask_b32 v130, v130, v129, vcc\n\t"
    "v_ffbl_b32 v131, v130\n\t"
    "v_and_b32 v134, 7, v131\n\t"
    "v_lshlrev_b32 v134, v134, s45\n\t"
    "v_xor_b32 v129, v129, v134\n\t"
    "v_xor_b32 v132, v131, v127\n\t"
    "s_waitcnt lgkmcnt(9)\n\t"
    "v_and_b32 v120, v120, v112\n\t"
    "v_perm_b32 v120, v120, v120, s46\n\t"
    "ds_or_b32 v94, v120\n\t"
    "v_bfi_b32 v130, v107, 0, v129\n\t"
    "v_cmp_eq_u32 vcc, 0, v130\n\t"
    "v_and_or_b32 v99, v107, s44, v69\n\t"
    "v_xor_b32 v133, 23, v132\n\t"
    "v_lshlrev_b32 v116, v132, v136\n\t"
    "v_and_b32 v132, 7, v132\n\t"
    "v_lshl_or_b32 v116, v136, v133, v116\n\t"
    "v_lshl_add_u32 v132, v132, 1, v128\n\t"
    "ds_or_rtn_b32 v124, v98, v116\n\t"
    "ds_write_b16 v132, v103\n\t"
    "v_cndmask_b32 v130, v130, v129, vcc\n\t"
    "v_ffbl_b32 v131, v130\n\t"
    "v_xor_b32 v132, v131, v127\n\t"
    "s_waitcnt lgkmcnt(10)\n\t"
    "v_and_b32 v121, v121, v113\n\t"
    "v_perm_b32 v121, v121, v121, s46\n\t"
    "ds_or_b32 v95, v121\n\t"
    "v_xor_b32 v133, 23, v132\n\t"
    "v_lshlrev_b32 v117, v132, v136\n\t"
    "v_and_b32 v132, 7, v132\n\t"
    "v_lshl_or_b32 v117, v136, v133, v117\n\t"
    "v_lshl_add_u32 v132, v132, 1, v128\n\t"
    "ds_or_rtn_b32 v125, v99, v117\n\t"
    "ds_write_b16 v132, v107\n\t"
    "s_waitcnt lgkmcnt(10)\n\t"
    "v_and_b32 v122, v122, v114\n\t"
    "v_perm_b32 v122, v122, v122, s46\n\t"
    "ds_or_b32 v96, v122\n\t"
    "ds_read_b128 v[72:75], v128\n\t"
    "v_and_or_b32 v100, v84, s44, v69\n\t"
    "ds_read_b32 v100, v100\n\t"
    "v_and_or_b32 v101, v85, s44, v69\n\t"
    "ds_read_b32 v101, v101\n\t"
    "v_and_or_b32 v102, v86, s44, v69\n\t"
    "ds_read_b32 v102, v102\n\t"
    "v_and_or_b32 v103, v87, s44, v69\n\t"
    "ds_read_b32 v103, v103\n\t"
    "v_and_or_b32 v104, v88, s44, v69\n\t"
    "ds_read_b32 v104, v104\n\t"
    "v_and_or_b32 v105, v89, s44, v69\n\t"
    "ds_read_b32 v105, v105\n\t"
    "v_and_or_b32 v106, v90, s44, v69\n\t"
    "ds_read_b32 v106, v106\n\t"
    "v_and_or_b32 v107, v91, s44, v69\n\t"
    "ds_read_b32 v107, v107\n\t"
    "v_lshl_add_u32 v76, v0, 3, s36\n\t"
    "v_lshl_add_u32 v77, v1, 3, s37\n\t"
    "v_lshl_add_u32 v78, v2, 3, s38\n\t"
    "v_lshl_add_u32 v79, v3, 3, s39\n\t"
    "v_lshl_add_u32 v80, v4, 3, s40\n\t"
    "v_lshl_add_u32 v81, v5, 3, s41\n\t"
    "v_lshl_add_u32 v82, v6, 3, s42\n\t"
    "v_lshl_add_u32 v83, v7, 3, s43\n\t"
    "v_mbcnt_lo_u32_b32 v70, -1, 0\n\t"
    "v_mbcnt_hi_u32_b32 v70, -1, v70\n\t"
    "v_add_u32 v70, 8, v70\n\t"
    "s_and_b32 s50, %14, 7\n\t"
    "s_add_u32 s50, s50, 16\n\t"
    "v_writelane_b32 v70, s50, 8\n\t"
    "v_mov_b32 v148, 32\n\t"
    "v_lshlrev_b32 v70, 7, v70\n\t"
    "s_mov_b32 s52, 48234496\n\t"
    "s_mov_b64 exec, 0x1ff\n\t"
    "buffer_load_dword v71, v70, %12, s52 offen sc1\n\t"
    "s_mov_b64 exec, -1\n\t"
    "s_mov_b32 s54, 0x40000000\n\t"
    "s_mov_b32 s55, 0\n\t"
    "s_waitcnt lgkmcnt(11)\n\t"
    "v_and_b32 v123, v123, v115\n\t"
    "v_perm_b32 v123, v123, v123, s46\n\t"
    "ds_or_b32 v97, v123\n\t"
    "v_and_b32 v124, v124, v116\n\t"
    "v_perm_b32 v124, v124, v124, s46\n\t"
    "ds_or_b32 v98, v124\n\t"
    "v_and_b32 v125, v125, v117\n\t"
    "v_perm_b32 v125, v125, v125, s46\n\t"
    "ds_or_b32 v99, v125\n\t"
    "s_waitcnt lgkmcnt(11)\n\t"
    "buffer_store_dwordx4 v[72:75], v67, %12, s49 offen offset:352 sc1\n\t"
    "v_mov_b32 v129, 0xffff0000\n\t"
    "s_waitcnt lgkmcnt(7)\n\t"
    "v_perm_b32 v100, v100, v84, v126\n\t"
    "v_perm_b32 v101, v101, v85, v126\n\t"
    "v_perm_b32 v102, v102, v86, v126\n\t"
    "v_perm_b32 v103, v103, v87, v126\n\t"
    "s_waitcnt lgkmcnt(3)\n\t"
    "v_perm_b32 v104, v104, v88, v126\n\t"
    "v_perm_b32 v105, v105, v89, v126\n\t"
    "v_perm_b32 v106, v106, v90, v126\n\t"
    "v_perm_b32 v107, v107, v91, v126\n\t"
    "s_waitcnt vmcnt(42)\n\t"
    "ds_write_b128 v141, v[156:159] offset:2112\n\t"
    "ds_write_b128 v141, v[160:163] offset:2368\n\t"
    "ds_read_b64_tr_b16 v[156:157], v142 offset:2112\n\t"
    "ds_read_b64_tr_b16 v[158:159], v142 offset:2368\n\t"
    "ds_read_b64_tr_b16 v[160:161], v142 offset:2144\n\t"
    "ds_read_b64_tr_b16 v[162:163], v142 offset:2400\n\t"
    "s_waitcnt vmcnt(40)\n\t"
    "ds_write_b128 v141, v[164:167] offset:0\n\t"
    "ds_write_b128 v141, v[168:171] offset:256\n\t"
    "ds_read_b64_tr_b16 v[164:165], v142 offset:0\n\t"
    "ds_read_b64_tr_b16 v[166:167], v142 offset:256\n\t"
    "ds_read_b64_tr_b16 v[168:169], v142 offset:32\n\t"
    "ds_read_b64_tr_b16 v[170:171], v142 offset:288\n\t"
    "v_max_u32 v108, v100, v101\n\t"
    "v_min_u32 v101, v100, v101\n\t"
    "v_max_u32 v100, v102, v103\n\t"
    "v_min_u32 v103, v102, v103\n\t"
    "v_max_u32 v102, v104, v105\n\t"
    "v_min_u32 v105, v104, v105\n\t"
    "v_max_u32 v104, v106, v107\n\t"
    "v_min_u32 v107, v106, v107\n\t"
    "v_max_u32 v106, v108, v100\n\t"
    "v_min_u32 v100, v108, v100\n\t"
    "v_max_u32 v108, v101, v103\n\t"
    "v_min_u32 v103, v101, v103\n\t"
    "v_max_u32 v101, v102, v104\n\t"
    "v_min_u32 v104, v102, v104\n\t"
    "v_max_u32 v102, v105, v107\n\t"
    "v_min_u32 v107, v105, v107\n\t"
    "v_bfi_b32 v130, v106, 0, v129\n\t"
    "v_cmp_eq_u32 vcc, 0, v130\n\t"
    "v_and_or_b32 v92, v106, s44, v69\n\t"
    "s_nop 0\n\t"
    "v_cndmask_b32 v130, v130, v129, vcc\n\t"
    "v_ffbl_b32 v131, v130\n\t"
    "v_and_b32 v134, 7, v131\n\t"
    "v_lshlrev_b32 v134, v134, s45\n\t"
    "v_xor_b32 v129, v129, v134\n\t"
    "v_xor_b32 v132, v131, v127\n\t"
    "v_bfi_b32 v130, v101, 0, v129\n\t"
    "v_cmp_eq_u32 vcc, 0, v130\n\t"
    "v_and_or_b32 v93, v101, s44, v69\n\t"
    "v_xor_b32 v133, 23, v132\n\t"
    "v_lshlrev_b32 v110, v132, v136\n\t"
    "v_and_b32 v132, 7, v132\n\t"
    "v_lshl_or_b32 v110, v136, v133, v110\n\t"
    "v_lshl_add_u32 v132, v132, 1, v128\n\t"
    "ds_or_b32 v92, v110\n\t"
    "ds_write_b16 v132, v106\n\t"
    "v_cndmask_b32 v130, v130, v129, vcc\n\t"
    "v_ffbl_b32 v131, v130\n\t"
    "v_and_b32 v134, 7, v131\n\t"
    "v_lshlrev_b32 v134, v134, s45\n\t"
    "v_xor_b32 v129, v129, v134\n\t"
    "v_xor_b32 v132, v131, v127\n\t"
    "v_bfi_b32 v130, v108, 0, v129\n\t"
    "v_cmp_eq_u32 vcc, 0, v130\n\t"
    "v_and_or_b32 v94, v108, s44, v69\n\t"
    "v_xor_b32 v133, 23, v132\n\t"
    "v_lshlrev_b32 v111, v132, v136\n\t"
    "v_and_b32 v132, 7, v132\n\t"
    "v_lshl_or_b32 v111, v136, v133, v111\n\t"
    "v_lshl_add_u32 v132, v132, 1, v128\n\t"
    "ds_or_b32 v93, v111\n\t"
    "ds_write_b16 v132, v101\n\t"
    "v_cndmask_b32 v130, v130, v129, vcc\n\t"
    "v_ffbl_b32 v131, v130\n\t"
    "v_and_b32 v134, 7, v131\n\t"
    "v_lshlrev_b32 v134, v134, s45\n\t"
    "v_xor_b32 v129, v129, v134\n\t"
    "v_xor_b32 v132, v131, v127\n\t"
    "s_waitcnt lgkmcnt(10)\n\t"
    "buffer_store_dwordx4 v[156:159], v139, %12, s48 offen sc1\n\t"
    "buffer_store_dwordx4 v[160:163], v140, %12, s48 offen sc1\n\t"
    "s_add_u32 s48, s48, -811008\n\t"
    "s_waitcnt lgkmcnt(4)\n\t"
    "buffer_store_dwordx4 v[164:167], v139, %12, s48 offen sc1\n\t"
    "buffer_store_dwordx4 v[168:171], v140, %12, s48 offen sc1\n\t"
    "s_add_u32 s48, s48, 819200\n\t"
    "s_waitcnt vmcnt(42)\n\t"
    "ds_write_b128 v141, v[172:175] offset:2112\n\t"
    "ds_write_b128 v141, v[176:179] offset:2368\n\t"
    "ds_read_b64_tr_b16 v[172:173], v142 offset:2112\n\t"
    "ds_read_b64_tr_b16 v[174:175], v142 offset:2368\n\t"
    "ds_read_b64_tr_b16 v[176:177], v142 offset:2144\n\t"
    "ds_read_b64_tr_b16 v[178:179], v142 offset:2400\n\t"
    "s_waitcnt vmcnt(40)\n\t"
    "ds_write_b128 v141, v[180:183] offset:0\n\t"
    "ds_write_b128 v141, v[184:187] offset:256\n\t"
    "ds_read_b64_tr_b16 v[180:181], v142 offset:0\n\t"
    "ds_read_b64_tr_b16 v[182:183], v142 offset:256\n\t"
    "ds_read_b64_tr_b16 v[184:185], v142 offset:32\n\t"
    "ds_read_b64_tr_b16 v[186:187], v142 offset:288\n\t"
    "v_bfi_b32 v130, v100, 0, v129\n\t"
    "v_cmp_eq_u32 vcc, 0, v130\n\t"
    "v_and_or_b32 v95, v100, s44, v69\n\t"
    "v_xor_b32 v133, 23, v132\n\t"
    "v_lshlrev_b32 v112, v132, v136\n\t"
    "v_and_b32 v132, 7, v132\n\t"
    "v_lshl_or_b32 v112, v136, v133, v112\n\t"
    "v_lshl_add_u32 v132, v132, 1, v128\n\t"
    "ds_or_b32 v94, v112\n\t"
    "ds_write_b16 v132, v108\n\t"
    "v_cndmask_b32 v130, v130, v129, vcc\n\t"
    "v_ffbl_b32 v131, v130\n\t"
    "v_and_b32 v134, 7, v131\n\t"
    "v_lshlrev_b32 v134, v134, s45\n\t"
    "v_xor_b32 v129, v129, v134\n\t"
    "v_xor_b32 v132, v131, v127\n\t"
    "v_bfi_b32 v130, v102, 0, v129\n\t"
    "v_cmp_eq_u32 vcc, 0, v130\n\t"
    "v_and_or_b32 v96, v102, s44, v69\n\t"
    "v_xor_b32 v133, 23, v132\n\t"
    "v_lshlrev_b32 v113, v132, v136\n\t"
    "v_and_b32 v132, 7, v132\n\t"
    "v_lshl_or_b32 v113, v136, v133, v113\n\t"
    "v_lshl_add_u32 v132, v132, 1, v128\n\t"
    "ds_or_b32 v95, v113\n\t"
    "ds_write_b16 v132, v100\n\t"
    "v_cndmask_b32 v130, v130, v129, vcc\n\t"
    "v_ffbl_b32 v131, v130\n\t"
    "v_and_b32 v134, 7, v131\n\t"
    "v_lshlrev_b32 v134, v134, s45\n\t"
    "v_xor_b32 v129, v129, v134\n\t"
    "v_xor_b32 v132, v131, v127\n\t"
    "s_waitcnt vmcnt(5)\n\t"
    "s_call_b64 s[54:55], L_af1w_%=\n\t"
    "s_mov_b64 s[54:55], 0\n\t"
    "L_af1n15_%=:\n\t"
    "v_bfi_b32 v130, v104, 0, v129\n\t"
    "v_cmp_eq_u32 vcc, 0, v130\n\t"
    "v_and_or_b32 v97, v104, s44, v69\n\t"
    "v_xor_b32 v133, 23, v132\n\t"
    "v_lshlrev_b32 v114, v132, v136\n\t"
    "v_and_b32 v132, 7, v132\n\t"
    "v_lshl_or_b32 v114, v136, v133, v114\n\t"
    "v_lshl_add_u32 v132, v132, 1, v128\n\t"
    "ds_or_b32 v96, v114\n\t"
    "ds_write_b16 v132, v102\n\t"
    "v_cndmask_b32 v130, v130, v129, vcc\n\t"
    "v_ffbl_b32 v131, v130\n\t"
    "v_and_b32 v134, 7, v131\n\t"
    "v_lshlrev_b32 v134, v134, s45\n\t"
    "v_xor_b32 v129, v129, v134\n\t"
    "v_xor_b32 v132, v131, v127\n\t"
    "s_waitcnt lgkmcnt(12)\n\t"
    "buffer_store_dwordx4 v[172:175], v139, %12, s48 offen sc1\n\t"
    "buffer_store_dwordx4 v[176:179], v140, %12, s48 offen sc1\n\t"
    "s_add_u32 s48, s48, -811008\n\t"
    "s_waitcnt lgkmcnt(6)\n\t"
    "buffer_store_dwordx4 v[180:183], v139, %12, s48 offen sc1\n\t"
    "buffer_store_dwordx4 v[184:187], v140, %12, s48 offen sc1\n\t"
    "s_add_u32 s48, s48, 819200\n\t"
    "ds_write_b128 v141, v[188:191] offset:2112\n\t"
    "ds_write_b128 v141, v[192:195] offset:2368\n\t"
    "ds_read_b64_tr_b16 v[188:189], v142 offset:2112\n\t"
    "ds_read_b64_tr_b16 v[190:191], v142 offset:2368\n\t"
    "ds_read_b64_tr_b16 v[192:193], v142 offset:2144\n\t"
    "ds_read_b64_tr_b16 v[194:195], v142 offset:2400\n\t"
    "ds_write_b128 v141, v[196:199] offset:0\n\t"
    "ds_write_b128 v141, v[200:203] offset:256\n\t"
    "ds_read_b64_tr_b16 v[196:197], v142 offset:0\n\t"
    "ds_read_b64_tr_b16 v[198:199], v142 offset:256\n\t"
    "ds_read_b64_tr_b16 v[200:201], v142 offset:32\n\t"
    "ds_read_b64_tr_b16 v[202:203], v142 offset:288\n\t"
    "v_bfi_b32 v130, v103, 0, v129\n\t"
    "v_cmp_eq_u32 vcc, 0, v130\n\t"
    "v_and_or_b32 v98, v103, s44, v69\n\t"
    "v_xor_b32 v133, 23, v132\n\t"
    "v_lshlrev_b32 v115, v132, v136\n\t"
    "v_and_b32 v132, 7, v132\n\t"
    "v_lshl_or_b32 v115, v136, v133, v115\n\t"
    "v_lshl_add_u32 v132, v132, 1, v128\n\t"
    "ds_or_b32 v97, v115\n\t"
    "ds_write_b16 v132, v104\n\t"
    "v_cndmask_b32 v130, v130, v129, vcc\n\t"
    "v_ffbl_b32 v131, v130\n\t"
    "v_and_b32 v134, 7, v131\n\t"
    "v_lshlrev_b32 v134, v134, s45\n\t"
    "v_xor_b32 v129, v129, v134\n\t"
    "v_xor_b32 v132, v131, v127\n\t"
    "v_bfi_b32 v130, v107, 0, v129\n\t"
    "v_cmp_eq_u32 vcc, 0, v130\n\t"
    "v_and_or_b32 v99, v107, s44, v69\n\t"
    "v_xor_b32 v133, 23, v132\n\t"
    "v_lshlrev_b32 v116, v132, v136\n\t"
    "v_and_b32 v132, 7, v132\n\t"
    "v_lshl_or_b32 v116, v136, v133, v116\n\t"
    "v_lshl_add_u32 v132, v132, 1, v128\n\t"
    "ds_or_b32 v98, v116\n\t"
    "ds_write_b16 v132, v103\n\t"
    "v_cndmask_b32 v130, v130, v129, vcc\n\t"
    "v_ffbl_b32 v131, v130\n\t"
    "v_xor_b32 v132, v131, v127\n\t"
    "v_xor_b32 v133, 23, v132\n\t"
    "v_lshlrev_b32 v117, v132, v136\n\t"
    "v_and_b32 v132, 7, v132\n\t"
    "v_lshl_or_b32 v117, v136, v133, v117\n\t"
    "v_lshl_add_u32 v132, v132, 1, v128\n\t"
    "ds_or_b32 v99, v117\n\t"
    "ds_write_b16 v132, v107\n\t"
    "ds_read_b128 v[72:75], v128\n\t"
    "v_and_or_b32 v100, v76, s44, v69\n\t"
    "ds_read_b32 v100, v100\n\t"
    "v_and_or_b32 v101, v77, s44, v69\n\t"
    "ds_read_b32 v101, v101\n\t"
    "v_and_or_b32 v102, v78, s44, v69\n\t"
    "ds_read_b32 v102, v102\n\t"
    "v_and_or_b32 v103, v79, s44, v69\n\t"
    "ds_read_b32 v103, v103\n\t"
    "v_and_or_b32 v104, v80, s44, v69\n\t"
    "ds_read_b32 v104, v104\n\t"
    "v_and_or_b32 v105, v81, s44, v69\n\t"
    "ds_read_b32 v105, v105\n\t"
    "v_and_or_b32 v106, v82, s44, v69\n\t"
    "ds_read_b32 v106, v106\n\t"
    "v_and_or_b32 v107, v83, s44, v69\n\t"
    "ds_read_b32 v107, v107\n\t"
    "v_lshl_add_u32 v84, v8, 3, s36\n\t"
    "v_lshl_add_u32 v85, v9, 3, s37\n\t"
    "v_lshl_add_u32 v86, v10, 3, s38\n\t"
    "v_lshl_add_u32 v87, v11, 3, s39\n\t"
    "v_lshl_add_u32 v88, v12, 3, s40\n\t"
    "v_lshl_add_u32 v89, v13, 3, s41\n\t"
    "v_lshl_add_u32 v90, v14, 3, s42\n\t"
    "v_lshl_add_u32 v91, v15, 3, s43\n\t"
    "s_waitcnt lgkmcnt(15)\n\t"
    "buffer_store_dwordx4 v[188:191], v139, %12, s48 offen sc1\n\t"
    "buffer_store_dwordx4 v[192:195], v140, %12, s48 offen sc1\n\t"
    "s_add_u32 s48, s48, -811008\n\t"
    "buffer_store_dwordx4 v[196:199], v139, %12, s48 offen sc1\n\t"
    "buffer_store_dwordx4 v[200:203], v140, %12, s48 offen sc1\n\t"
    "s_add_u32 s48, s48, 819200\n\t"
    "s_waitcnt lgkmcnt(8)\n\t"
    "buffer_store_dwordx4 v[72:75], v67, %12, s49 offen offset:384 sc1\n\t"
    "v_mov_b32 v129, 0xffff0000\n\t"
    "s_waitcnt lgkmcnt(4)\n\t"
    "v_perm_b32 v100, v100, v76, v126\n\t"
    "v_perm_b32 v101, v101, v77, v126\n\t"
    "v_perm_b32 v102, v102, v78, v126\n\t"
    "v_perm_b32 v103, v103, v79, v126\n\t"
    "s_waitcnt lgkmcnt(0)\n\t"
    "v_perm_b32 v104, v104, v80, v126\n\t"
    "v_perm_b32 v105, v105, v81, v126\n\t"
    "v_perm_b32 v106, v106, v82, v126\n\t"
    "v_perm_b32 v107, v107, v83, v126\n\t"
    "ds_write_b128 v141, v[48:51] offset:2112\n\t"
    "ds_write_b128 v141, v[52:55] offset:2368\n\t"
    "ds_read_b64_tr_b16 v[48:49], v142 offset:2112\n\t"
    "ds_read_b64_tr_b16 v[50:51], v142 offset:2368\n\t"
    "ds_read_b64_tr_b16 v[52:53], v142 offset:2144\n\t"
    "ds_read_b64_tr_b16 v[54:55], v142 offset:2400\n\t"
    "ds_write_b128 v141, v[204:207] offset:0\n\t"
    "ds_write_b128 v141, v[208:211] offset:256\n\t"
    "ds_read_b64_tr_b16 v[204:205], v142 offset:0\n\t"
    "ds_read_b64_tr_b16 v[206:207], v142 offset:256\n\t"
    "ds_read_b64_tr_b16 v[208:209], v142 offset:32\n\t"
    "ds_read_b64_tr_b16 v[210:211], v142 offset:288\n\t"
    "v_max_u32 v108, v100, v101\n\t"
    "v_min_u32 v101, v100, v101\n\t"
    "v_max_u32 v100, v102, v103\n\t"
    "v_min_u32 v103, v102, v103\n\t"
    "v_max_u32 v102, v104, v105\n\t"
    "v_min_u32 v105, v104, v105\n\t"
    "v_max_u32 v104, v106, v107\n\t"
    "v_min_u32 v107, v106, v107\n\t"
    "v_max_u32 v106, v108, v100\n\t"
    "v_min_u32 v100, v108, v100\n\t"
    "v_max_u32 v108, v101, v103\n\t"
    "v_min_u32 v103, v101, v103\n\t"
    "v_max_u32 v101, v102, v104\n\t"
    "v_min_u32 v104, v102, v104\n\t"
    "v_max_u32 v102, v105, v107\n\t"
    "v_min_u32 v107, v105, v107\n\t"
    "v_bfi_b32 v130, v106, 0, v129\n\t"
    "v_cmp_eq_u32 vcc, 0, v130\n\t"
    "v_and_or_b32 v92, v106, s44, v69\n\t"
    "s_nop 0\n\t"
    "v_cndmask_b32 v130, v130, v129, vcc\n\t"
    "v_ffbl_b32 v131, v130\n\t"
    "v_and_b32 v134, 7, v131\n\t"
    "v_lshlrev_b32 v134, v134, s45\n\t"
    "v_xor_b32 v129, v129, v134\n\t"
    "v_xor_b32 v132, v131, v127\n\t"
    "v_bfi_b32 v130, v101, 0, v129\n\t"
    "v_cmp_eq_u32 vcc, 0, v130\n\t"
    "v_and_or_b32 v93, v101, s44, v69\n\t"
    "v_xor_b32 v133, 23, v132\n\t"
    "v_lshlrev_b32 v110, v132, v136\n\t"
    "v_and_b32 v132, 7, v132\n\t"
    "v_lshl_or_b32 v110, v136, v133, v110\n\t"
    "v_lshl_add_u32 v132, v132, 1, v128\n\t"
    "ds_or_b32 v92, v110\n\t"
    "ds_write_b16 v132, v106\n\t"
    "v_cndmask_b32 v130, v130, v129, vcc\n\t"
    "v_ffbl_b32 v131, v130\n\t"
    "v_and_b32 v134, 7, v131\n\t"
    "v_lshlrev_b32 v134, v134, s45\n\t"
    "v_xor_b32 v129, v129, v134\n\t"
    "v_xor_b32 v132, v131, v127\n\t"
    "v_bfi_b32 v130, v108, 0, v129\n\t"
    "v_cmp_eq_u32 vcc, 0, v130\n\t"
    "v_and_or_b32 v94, v108, s44, v69\n\t"
    "v_xor_b32 v133, 23, v132\n\t"
    "v_lshlrev_b32 v111, v132, v136\n\t"
    "v_and_b32 v132, 7, v132\n\t"
    "v_lshl_or_b32 v111, v136, v133, v111\n\t"
    "v_lshl_add_u32 v132, v132, 1, v128\n\t"
    "ds_or_b32 v93, v111\n\t"
    "ds_write_b16 v132, v101\n\t"
    "v_cndmask_b32 v130, v130, v129, vcc\n\t"
    "v_ffbl_b32 v131, v130\n\t"
    "v_and_b32 v134, 7, v131\n\t"
    "v_lshlrev_b32 v134, v134, s45\n\t"
    "v_xor_b32 v129, v129, v134\n\t"
    "v_xor_b32 v132, v131, v127\n\t"
    "s_waitcnt lgkmcnt(10)\n\t"
    "buffer_store_dwordx4 v[48:51], v139, %12, s48 offen sc1\n\t"
    "buffer_store_dwordx4 v[52:55], v140, %12, s48 offen sc1\n\t"
    "s_add_u32 s48, s48, -811008\n\t"
    "s_waitcnt lgkmcnt(4)\n\t"
    "buffer_store_dwordx4 v[204:207], v139, %12, s48 offen sc1\n\t"
    "buffer_store_dwordx4 v[208:211], v140, %12, s48 offen sc1\n\t"
    "s_add_u32 s48, s48, 819200\n\t"
    "ds_write_b128 v141, v[212:215] offset:2112\n\t"
    "ds_write_b128 v141, v[216:219] offset:2368\n\t"
    "ds_read_b64_tr_b16 v[212:213], v142 offset:2112\n\t"
    "ds_read_b64_tr_b16 v[214:215], v142 offset:2368\n\t"
    "ds_read_b64_tr_b16 v[216:217], v142 offset:2144\n\t"
    "ds_read_b64_tr_b16 v[218:219], v142 offset:2400\n\t"
    "ds_write_b128 v141, v[220:223] offset:0\n\t"
    "ds_write_b128 v141, v[224:227] offset:256\n\t"
    "ds_read_b64_tr_b16 v[220:221], v142 offset:0\n\t"
    "ds_read_b64_tr_b16 v[222:223], v142 offset:256\n\t"
    "ds_read_b64_tr_b16 v[224:225], v142 offset:32\n\t"
    "ds_read_b64_tr_b16 v[226:227], v142 offset:288\n\t"
    "v_bfi_b32 v130, v100, 0, v129\n\t"
    "v_cmp_eq_u32 vcc, 0, v130\n\t"
    "v_and_or_b32 v95, v100, s44, v69\n\t"
    "v_xor_b32 v133, 23, v132\n\t"
    "v_lshlrev_b32 v112, v132, v136\n\t"
    "v_and_b32 v132, 7, v132\n\t"
    "v_lshl_or_b32 v112, v136, v133, v112\n\t"
    "v_lshl_add_u32 v132, v132, 1, v128\n\t"
    "ds_or_b32 v94, v112\n\t"
    "ds_write_b16 v132, v108\n\t"
    "v_cndmask_b32 v130, v130, v129, vcc\n\t"
    "v_ffbl_b32 v131, v130\n\t"
    "v_and_b32 v134, 7, v131\n\t"
    "v_lshlrev_b32 v134, v134, s45\n\t"
    "v_xor_b32 v129, v129, v134\n\t"
    "v_xor_b32 v132, v131, v127\n\t"
    "v_bfi_b32 v130, v102, 0, v129\n\t"
    "v_cmp_eq_u32 vcc, 0, v130\n\t"
    "v_and_or_b32 v96, v102, s44, v69\n\t"
    "v_xor_b32 v133, 23, v132\n\t"
    "v_lshlrev_b32 v113, v132, v136\n\t"
    "v_and_b32 v132, 7, v132\n\t"
    "v_lshl_or_b32 v113, v136, v133, v113\n\t"
    "v_lshl_add_u32 v132, v132, 1, v128\n\t"
    "ds_or_b32 v95, v113\n\t"
    "ds_write_b16 v132, v100\n\t"
    "v_cndmask_b32 v130, v130, v129, vcc\n\t"
    "v_ffbl_b32 v131, v130\n\t"
    "v_and_b32 v134, 7, v131\n\t"
    "v_lshlrev_b32 v134, v134, s45\n\t"
    "v_xor_b32 v129, v129, v134\n\t"
    "v_xor_b32 v132, v131, v127\n\t"
    "v_bfi_b32 v130, v104, 0, v129\n\t"
    "v_cmp_eq_u32 vcc, 0, v130\n\t"
    "v_and_or_b32 v97, v104, s44, v69\n\t"
    "v_xor_b32 v133, 23, v132\n\t"
    "v_lshlrev_b32 v114, v132, v136\n\t"
    "v_and_b32 v132, 7, v132\n\t"
    "v_lshl_or_b32 v114, v136, v133, v114\n\t"
    "v_lshl_add_u32 v132, v132, 1, v128\n\t"
    "ds_or_b32 v96, v114\n\t"
    "ds_write_b16 v132, v102\n\t"
    "v_cndmask_b32 v130, v130, v129, vcc\n\t"
    "v_ffbl_b32 v131, v130\n\t"
    "v_and_b32 v134, 7, v131\n\t"
    "v_lshlrev_b32 v134, v134, s45\n\t"
    "v_xor_b32 v129, v129, v134\n\t"
    "v_xor_b32 v132, v131, v127\n\t"
    "s_waitcnt lgkmcnt(12)\n\t"
    "buffer_store_dwordx4 v[212:215], v139, %12, s48 offen sc1\n\t"
    "buffer_store_dwordx4 v[216:219], v140, %12, s48 offen sc1\n\t"
    "s_add_u32 s48, s48, -811008\n\t"
    "s_waitcnt lgkmcnt(6)\n\t"
    "s_bitcmp1_b32 %14, 3\n\t"
    "s_cselect_b32 s48, 0x40000000, s48\n\t"
    "buffer_store_dwordx4 v[220:223], v139, %12, s48 offen sc1\n\t"
    "buffer_store_dwordx4 v[224:227], v140, %12, s48 offen sc1\n\t"
    "s_add_u32 s48, s48, 819200\n\t"
    "ds_write_b128 v141, v[228:231] offset:2112\n\t"
    "ds_write_b128 v141, v[232:235] offset:2368\n\t"
    "ds_read_b64_tr_b16 v[228:229], v142 offset:2112\n\t"
    "ds_read_b64_tr_b16 v[230:231], v142 offset:2368\n\t"
    "ds_read_b64_tr_b16 v[232:233], v142 offset:2144\n\t"
    "ds_read_b64_tr_b16 v[234:235], v142 offset:2400\n\t"
    "v_bfi_b32 v130, v103, 0, v129\n\t"
    "v_cmp_eq_u32 vcc, 0, v130\n\t"
    "v_and_or_b32 v98, v103, s44, v69\n\t"
    "v_xor_b32 v133, 23, v132\n\t"
    "v_lshlrev_b32 v115, v132, v136\n\t"
    "v_and_b32 v132, 7, v132\n\t"
    "v_lshl_or_b32 v115, v136, v133, v115\n\t"
    "v_lshl_add_u32 v132, v132, 1, v128\n\t"
    "ds_or_b32 v97, v115\n\t"
    "ds_write_b16 v132, v104\n\t"
    "v_cndmask_b32 v130, v130, v129, vcc\n\t"
    "v_ffbl_b32 v131, v130\n\t"
    "v_and_b32 v134, 7, v131\n\t"
    "v_lshlrev_b32 v134, v134, s45\n\t"
    "v_xor_b32 v129, v129, v134\n\t"
    "v_xor_b32 v132, v131, v127\n\t"
    "v_bfi_b32 v130, v107, 0, v129\n\t"
    "v_cmp_eq_u32 vcc, 0, v130\n\t"
    "v_and_or_b32 v99, v107, s44, v69\n\t"
    "v_xor_b32 v133, 23, v132\n\t"
    "v_lshlrev_b32 v116, v132, v136\n\t"
    "v_and_b32 v132, 7, v132\n\t"
    "v_lshl_or_b32 v116, v136, v133, v116\n\t"
    "v_lshl_add_u32 v132, v132, 1, v128\n\t"
    "ds_or_b32 v98, v116\n\t"
    "ds_write_b16 v132, v103\n\t"
    "v_cndmask_b32 v130, v130, v129, vcc\n\t"
    "v_ffbl_b32 v131, v130\n\t"
    "v_xor_b32 v132, v131, v127\n\t"
    "v_xor_b32 v133, 23, v132\n\t"
    "v_lshlrev_b32 v117, v132, v136\n\t"
    "v_and_b32 v132, 7, v132\n\t"
    "v_lshl_or_b32 v117, v136, v133, v117\n\t"
    "v_lshl_add_u32 v132, v132, 1, v128\n\t"
    "ds_or_b32 v99, v117\n\t"
    "ds_write_b16 v132, v107\n\t"
    "ds_read_b128 v[72:75], v128\n\t"
    "v_and_or_b32 v100, v84, s44, v69\n\t"
    "ds_read_b32 v100, v100\n\t"
    "v_and_or_b32 v101, v85, s44, v69\n\t"
    "ds_read_b32 v101, v101\n\t"
    "v_and_or_b32 v102, v86, s44, v69\n\t"
    "ds_read_b32 v102, v102\n\t"
    "v_and_or_b32 v103, v87, s44, v69\n\t"
    "ds_read_b32 v103, v103\n\t"
    "v_and_or_b32 v104, v88, s44, v69\n\t"
    "ds_read_b32 v104, v104\n\t"
    "v_and_or_b32 v105, v89, s44, v69\n\t"
    "ds_read_b32 v105, v105\n\t"
    "v_and_or_b32 v106, v90, s44, v69\n\t"
    "ds_read_b32 v106, v106\n\t"
    "v_and_or_b32 v107, v91, s44, v69\n\t"
    "ds_read_b32 v107, v107\n\t"
    "v_lshl_add_u32 v76, v16, 3, s36\n\t"
    "v_lshl_add_u32 v77, v17, 3, s37\n\t"
    "v_lshl_add_u32 v78, v18, 3, s38\n\t"
    "v_lshl_add_u32 v79, v19, 3, s39\n\t"
    "v_lshl_add_u32 v80, v20, 3, s40\n\t"
    "v_lshl_add_u32 v81, v21, 3, s41\n\t"
    "v_lshl_add_u32 v82, v22, 3, s42\n\t"
    "v_lshl_add_u32 v83, v23, 3, s43\n\t"
    "s_waitcnt lgkmcnt(15)\n\t"
    "buffer_store_dwordx4 v[228:231], v139, %12, s48 offen sc1\n\t"
    "buffer_store_dwordx4 v[232:235], v140, %12, s48 offen sc1\n\t"
    "s_add_u32 s48, s48, -811008\n\t"
    "s_waitcnt lgkmcnt(8)\n\t"
    "buffer_store_dwordx4 v[72:75], v67, %12, s49 offen offset:416 sc1\n\t"
    "v_mov_b32 v129, 0xffff0000\n\t"
    "s_waitcnt lgkmcnt(4)\n\t"
    "v_perm_b32 v100, v100, v84, v126\n\t"
    "v_perm_b32 v101, v101, v85, v126\n\t"
    "v_perm_b32 v102, v102, v86, v126\n\t"
    "v_perm_b32 v103, v103, v87, v126\n\t"
    "s_waitcnt lgkmcnt(0)\n\t"
    "v_perm_b32 v104, v104, v88, v126\n\t"
    "v_perm_b32 v105, v105, v89, v126\n\t"
    "v_perm_b32 v106, v106, v90, v126\n\t"
    "v_perm_b32 v107, v107, v91, v126\n\t"
    "v_max_u32 v108, v100, v101\n\t"
    "v_min_u32 v101, v100, v101\n\t"
    "v_max_u32 v100, v102, v103\n\t"
    "v_min_u32 v103, v102, v103\n\t"
    "v_max_u32 v102, v104, v105\n\t"
    "v_min_u32 v105, v104, v105\n\t"
    "v_max_u32 v104, v106, v107\n\t"
    "v_min_u32 v107, v106, v107\n\t"
    "v_max_u32 v106, v108, v100\n\t"
    "v_min_u32 v100, v108, v100\n\t"
    "v_max_u32 v108, v101, v103\n\t"
    "v_min_u32 v103, v101, v103\n\t"
    "v_max_u32 v101, v102, v104\n\t"
    "v_min_u32 v104, v102, v104\n\t"
    "v_max_u32 v102, v105, v107\n\t"
    "v_min_u32 v107, v105, v107\n\t"
    "v_bfi_b32 v130, v106, 0, v129\n\t"
    "v_cmp_eq_u32 vcc, 0, v130\n\t"
    "v_and_or_b32 v92, v106, s44, v69\n\t"
    "s_nop 0\n\t"
    "v_cndmask_b32 v130, v130, v129, vcc\n\t"
    "v_ffbl_b32 v131, v130\n\t"
    "v_and_b32 v134, 7, v131\n\t"
    "v_lshlrev_b32 v134, v134, s45\n\t"
    "v_xor_b32 v129, v129, v134\n\t"
    "v_xor_b32 v132, v131, v127\n\t"
    "v_bfi_b32 v130, v101, 0, v129\n\t"
    "v_cmp_eq_u32 vcc, 0, v130\n\t"
    "v_and_or_b32 v93, v101, s44, v69\n\t"
    "v_xor_b32 v133, 23, v132\n\t"
    "v_lshlrev_b32 v110, v132, v136\n\t"
    "v_and_b32 v132, 7, v132\n\t"
    "v_lshl_or_b32 v110, v136, v133, v110\n\t"
    "v_lshl_add_u32 v132, v132, 1, v128\n\t"
    "ds_or_b32 v92, v110\n\t"
    "ds_write_b16 v132, v106\n\t"
    "v_cndmask_b32 v130, v130, v129, vcc\n\t"
    "v_ffbl_b32 v131, v130\n\t"
    "v_and_b32 v134, 7, v131\n\t"
    "v_lshlrev_b32 v134, v134, s45\n\t"
    "v_xor_b32 v129, v129, v134\n\t"
    "v_xor_b32 v132, v131, v127\n\t"
    "v_bfi_b32 v130, v108, 0, v129\n\t"
    "v_cmp_eq_u32 vcc, 0, v130\n\t"
    "v_and_or_b32 v94, v108, s44, v69\n\t"
    "v_xor_b32 v133, 23, v132\n\t"
    "v_lshlrev_b32 v111, v132, v136\n\t"
    "v_and_b32 v132, 7, v132\n\t"
    "v_lshl_or_b32 v111, v136, v133, v111\n\t"
    "v_lshl_add_u32 v132, v132, 1, v128\n\t"
    "ds_or_b32 v93, v111\n\t"
    "ds_write_b16 v132, v101\n\t"
    "v_cndmask_b32 v130, v130, v129, vcc\n\t"
    "v_ffbl_b32 v131, v130\n\t"
    "v_and_b32 v134, 7, v131\n\t"
    "v_lshlrev_b32 v134, v134, s45\n\t"
    "v_xor_b32 v129, v129, v134\n\t"
    "v_xor_b32 v132, v131, v127\n\t"
    "v_bfi_b32 v130, v100, 0, v129\n\t"
    "v_cmp_eq_u32 vcc, 0, v130\n\t"
    "v_and_or_b32 v95, v100, s44, v69\n\t"
    "v_xor_b32 v133, 23, v132\n\t"
    "v_lshlrev_b32 v112, v132, v136\n\t"
    "v_and_b32 v132, 7, v132\n\t"
    "v_lshl_or_b32 v112, v136, v133, v112\n\t"
    "v_lshl_add_u32 v132, v132, 1, v128\n\t"
    "ds_or_b32 v94, v112\n\t"
    "ds_write_b16 v132, v108\n\t"
    "v_cndmask_b32 v130, v130, v129, vcc\n\t"
    "v_ffbl_b32 v131, v130\n\t"
    "v_and_b32 v134, 7, v131\n\t"
    "v_lshlrev_b32 v134, v134, s45\n\t"
    "v_xor_b32 v129, v129, v134\n\t"
    "v_xor_b32 v132, v131, v127\n\t"
    "v_bfi_b32 v130, v102, 0, v129\n\t"
    "v_cmp_eq_u32 vcc, 0, v130\n\t"
    "v_and_or_b32 v96, v102, s44, v69\n\t"
    "v_xor_b32 v133, 23, v132\n\t"
    "v_lshlrev_b32 v113, v132, v136\n\t"
    "v_and_b32 v132, 7, v132\n\t"
    "v_lshl_or_b32 v113, v136, v133, v113\n\t"
    "v_lshl_add_u32 v132, v132, 1, v128\n\t"
    "ds_or_b32 v95, v113\n\t"
    "ds_write_b16 v132, v100\n\t"
    "v_cndmask_b32 v130, v130, v129, vcc\n\t"
    "v_ffbl_b32 v131, v130\n\t"
    "v_and_b32 v134, 7, v131\n\t"
    "v_lshlrev_b32 v134, v134, s45\n\t"
    "v_xor_b32 v129, v129, v134\n\t"
    "v_xor_b32 v132, v131, v127\n\t"
    "v_bfi_b32 v130, v104, 0, v129\n\t"
    "v_cmp_eq_u32 vcc, 0, v130\n\t"
    "v_and_or_b32 v97, v104, s44, v69\n\t"
    "v_xor_b32 v133, 23, v132\n\t"
    "v_lshlrev_b32 v114, v132, v136\n\t"
    "v_and_b32 v132, 7, v132\n\t"
    "v_lshl_or_b32 v114, v136, v133, v114\n\t"
    "v_lshl_add_u32 v132, v132, 1, v128\n\t"
    "ds_or_b32 v96, v114\n\t"
    "ds_write_b16 v132, v102\n\t"
    "v_cndmask_b32 v130, v130, v129, vcc\n\t"
    "v_ffbl_b32 v131, v130\n\t"
    "v_and_b32 v134, 7, v131\n\t"
    "v_lshlrev_b32 v134, v134, s45\n\t"
    "v_xor_b32 v129, v129, v134\n\t"
    "v_xor_b32 v132, v131, v127\n\t"
    "v_bfi_b32 v130, v103, 0, v129\n\t"
    "v_cmp_eq_u32 vcc, 0, v130\n\t"
    "v_and_or_b32 v98, v103, s44, v69\n\t"
    "v_xor_b32 v133, 23, v132\n\t"
    "v_lshlrev_b32 v115, v132, v136\n\t"
    "v_and_b32 v132, 7, v132\n\t"
    "v_lshl_or_b32 v115, v136, v133, v115\n\t"
    "v_lshl_add_u32 v132, v132, 1, v128\n\t"
    "ds_or_b32 v97, v115\n\t"
    "ds_write_b16 v132, v104\n\t"
    "v_cndmask_b32 v130, v130, v129, vcc\n\t"
    "v_ffbl_b32 v131, v130\n\t"
    "v_and_b32 v134, 7, v131\n\t"
    "v_lshlrev_b32 v134, v134, s45\n\t"
    "v_xor_b32 v129, v129, v134\n\t"
    "v_xor_b32 v132, v131, v127\n\t"
    "v_bfi_b32 v130, v107, 0, v129\n\t"
    "v_cmp_eq_u32 vcc, 0, v130\n\t"
    "v_and_or_b32 v99, v107, s44, v69\n\t"
    "v_xor_b32 v133, 23, v132\n\t"
    "v_lshlrev_b32 v116, v132, v136\n\t"
    "v_and_b32 v132, 7, v132\n\t"
    "v_lshl_or_b32 v116, v136, v133, v116\n\t"
    "v_lshl_add_u32 v132, v132, 1, v128\n\t"
    "ds_or_b32 v98, v116\n\t"
    "ds_write_b16 v132, v103\n\t"
    "v_cndmask_b32 v130, v130, v129, vcc\n\t"
    "v_ffbl_b32 v131, v130\n\t"
    "v_xor_b32 v132, v131, v127\n\t"
    "v_xor_b32 v133, 23, v132\n\t"
    "v_lshlrev_b32 v117, v132, v136\n\t"
    "v_and_b32 v132, 7, v132\n\t"
    "v_lshl_or_b32 v117, v136, v133, v117\n\t"
    "v_lshl_add_u32 v132, v132, 1, v128\n\t"
    "ds_or_b32 v99, v117\n\t"
    "ds_write_b16 v132, v107\n\t"
    "ds_read_b128 v[72:75], v128\n\t"
    "v_and_or_b32 v100, v76, s44, v69\n\t"
    "ds_read_b32 v100, v100\n\t"
    "v_and_or_b32 v101, v77, s44, v69\n\t"
    "ds_read_b32 v101, v101\n\t"
    "v_and_or_b32 v102, v78, s44, v69\n\t"
    "ds_read_b32 v102, v102\n\t"
    "v_and_or_b32 v103, v79, s44, v69\n\t"
    "ds_read_b32 v103, v103\n\t"
    "v_and_or_b32 v104, v80, s44, v69\n\t"
    "ds_read_b32 v104, v104\n\t"
    "v_and_or_b32 v105, v81, s44, v69\n\t"
    "ds_read_b32 v105, v105\n\t"
    "v_and_or_b32 v106, v82, s44, v69\n\t"
    "ds_read_b32 v106, v106\n\t"
    "v_and_or_b32 v107, v83, s44, v69\n\t"
    "ds_read_b32 v107, v107\n\t"
    "s_waitcnt lgkmcnt(8)\n\t"
    "buffer_store_dwordx4 v[72:75], v67, %12, s49 offen offset:448 sc1\n\t"
    "v_mov_b32 v129, 0xffff0000\n\t"
    "s_waitcnt lgkmcnt(4)\n\t"
    "v_perm_b32 v100, v100, v76, v126\n\t"
    "v_perm_b32 v101, v101, v77, v126\n\t"
    "v_perm_b32 v102, v102, v78, v126\n\t"
    "v_perm_b32 v103, v103, v79, v126\n\t"
    "s_waitcnt lgkmcnt(0)\n\t"
    "v_perm_b32 v104, v104, v80, v126\n\t"
    "v_perm_b32 v105, v105, v81, v126\n\t"
    "v_perm_b32 v106, v106, v82, v126\n\t"
    "v_perm_b32 v107, v107, v83, v126\n\t"
    "v_max_u32 v108, v100, v101\n\t"
    "v_min_u32 v101, v100, v101\n\t"
    "v_max_u32 v100, v102, v103\n\t"
    "v_min_u32 v103, v102, v103\n\t"
    "v_max_u32 v102, v104, v105\n\t"
    "v_min_u32 v105, v104, v105\n\t"
    "v_max_u32 v104, v106, v107\n\t"
    "v_min_u32 v107, v106, v107\n\t"
    "v_max_u32 v106, v108, v100\n\t"
    "v_min_u32 v100, v108, v100\n\t"
    "v_max_u32 v108, v101, v103\n\t"
    "v_min_u32 v103, v101, v103\n\t"
    "v_max_u32 v101, v102, v104\n\t"
    "v_min_u32 v104, v102, v104\n\t"
    "v_max_u32 v102, v105, v107\n\t"
    "v_min_u32 v107, v105, v107\n\t"
    "v_bfi_b32 v130, v106, 0, v129\n\t"
    "v_cmp_eq_u32 vcc, 0, v130\n\t"
    "v_and_or_b32 v92, v106, s44, v69\n\t"
    "s_nop 0\n\t"
    "v_cndmask_b32 v130, v130, v129, vcc\n\t"
    "v_ffbl_b32 v131, v130\n\t"
    "v_and_b32 v134, 7, v131\n\t"
    "v_lshlrev_b32 v134, v134, s45\n\t"
    "v_xor_b32 v129, v129, v134\n\t"
    "v_xor_b32 v132, v131, v127\n\t"
    "v_bfi_b32 v130, v101, 0, v129\n\t"
    "v_cmp_eq_u32 vcc, 0, v130\n\t"
    "v_and_b32 v132, 7, v132\n\t"
    "v_lshl_add_u32 v132, v132, 1, v128\n\t"
    "ds_write_b16 v132, v106\n\t"
    "v_cndmask_b32 v130, v130, v129, vcc\n\t"
    "v_ffbl_b32 v131, v130\n\t"
    "v_and_b32 v134, 7, v131\n\t"
    "v_lshlrev_b32 v134, v134, s45\n\t"
    "v_xor_b32 v129, v129, v134\n\t"
    "v_xor_b32 v132, v131, v127\n\t"
    "v_bfi_b32 v130, v108, 0, v129\n\t"
    "v_cmp_eq_u32 vcc, 0, v130\n\t"
    "v_and_b32 v132, 7, v132\n\t"
    "v_lshl_add_u32 v132, v132, 1, v128\n\t"
    "ds_write_b16 v132, v101\n\t"
    "v_cndmask_b32 v130, v130, v129, vcc\n\t"
    "v_ffbl_b32 v131, v130\n\t"
    "v_and_b32 v134, 7, v131\n\t"
    "v_lshlrev_b32 v134, v134, s45\n\t"
    "v_xor_b32 v129, v129, v134\n\t"
    "v_xor_b32 v132, v131, v127\n\t"
    "v_bfi_b32 v130, v100, 0, v129\n\t"
    "v_cmp_eq_u32 vcc, 0, v130\n\t"
    "v_and_b32 v132, 7, v132\n\t"
    "v_lshl_add_u32 v132, v132, 1, v128\n\t"
    "ds_write_b16 v132, v108\n\t"
    "v_cndmask_b32 v130, v130, v129, vcc\n\t"
    "v_ffbl_b32 v131, v130\n\t"
    "v_and_b32 v134, 7, v131\n\t"
    "v_lshlrev_b32 v134, v134, s45\n\t"
    "v_xor_b32 v129, v129, v134\n\t"
    "v_xor_b32 v132, v131, v127\n\t"
    "v_bfi_b32 v130, v102, 0, v129\n\t"
    "v_cmp_eq_u32 vcc, 0, v130\n\t"
    "v_and_b32 v132, 7, v132\n\t"
    "v_lshl_add_u32 v132, v132, 1, v128\n\t"
    "ds_write_b16 v132, v100\n\t"
    "v_cndmask_b32 v130, v130, v129, vcc\n\t"
    "v_ffbl_b32 v131, v130\n\t"
    "v_and_b32 v134, 7, v131\n\t"
    "v_lshlrev_b32 v134, v134, s45\n\t"
    "v_xor_b32 v129, v129, v134\n\t"
    "v_xor_b32 v132, v131, v127\n\t"
    "v_bfi_b32 v130, v104, 0, v129\n\t"
    "v_cmp_eq_u32 vcc, 0, v130\n\t"
    "v_and_b32 v132, 7, v132\n\t"
    "v_lshl_add_u32 v132, v132, 1, v128\n\t"
    "ds_write_b16 v132, v102\n\t"
    "v_cndmask_b32 v130, v130, v129, vcc\n\t"
    "v_ffbl_b32 v131, v130\n\t"
    "v_and_b32 v134, 7, v131\n\t"
    "v_lshlrev_b32 v134, v134, s45\n\t"
    "v_xor_b32 v129, v129, v134\n\t"
    "v_xor_b32 v132, v131, v127\n\t"
    "v_bfi_b32 v130, v103, 0, v129\n\t"
    "v_cmp_eq_u32 vcc, 0, v130\n\t"
    "v_and_b32 v132, 7, v132\n\t"
    "v_lshl_add_u32 v132, v132, 1, v128\n\t"
    "ds_write_b16 v132, v104\n\t"
    "v_cndmask_b32 v130, v130, v129, vcc\n\t"
    "v_ffbl_b32 v131, v130\n\t"
    "v_and_b32 v134, 7, v131\n\t"
    "v_lshlrev_b32 v134, v134, s45\n\t"
    "v_xor_b32 v129, v129, v134\n\t"
    "v_xor_b32 v132, v131, v127\n\t"
    "v_bfi_b32 v130, v107, 0, v129\n\t"
    "v_cmp_eq_u32 vcc, 0, v130\n\t"
    "v_and_b32 v132, 7, v132\n\t"
    "v_lshl_add_u32 v132, v132, 1, v128\n\t"
    "ds_write_b16 v132, v103\n\t"
    "v_cndmask_b32 v130, v130, v129, vcc\n\t"
    "v_ffbl_b32 v131, v130\n\t"
    "v_xor_b32 v132, v131, v127\n\t"
    "v_and_b32 v132, 7, v132\n\t"
    "v_lshl_add_u32 v132, v132, 1, v128\n\t"
    "ds_write_b16 v132, v107\n\t"
    "ds_read_b128 v[72:75], v128\n\t"
    "s_waitcnt lgkmcnt(0)\n\t"
    "buffer_store_dwordx4 v[72:75], v67, %12, s49 offen offset:480 sc1\n\t"
    "s_setprio 0\n\t"
    "v_mov_b32 v134, 1036\n\t"
    "L_asvsp12d_%=:\n\t"
    "ds_read_b32 v130, v134\n\t"
    "s_waitcnt lgkmcnt(0)\n\t"
    "v_readfirstlane_b32 s50, v130\n\t"
    "s_nop 0\n\t"
    "s_cmp_lt_u32 s50, 4\n\t"
    "s_cbranch_scc1 L_asvsp12d_%=\n\t"
    "s_call_b64 s[98:99], L_setup_%=\n\t"
    "v_lshrrev_b32 v105, 4, v127\n\t"
    "v_and_b32 v105, 48, v105\n\t"
    "s_movk_i32 s65, 130\n\t"
    "v_mad_u32_u24 v104, v155, s65, v105\n\t"
    "v_add_u32 v105, 14170, v104\n\t"
    "v_and_b32 v104, -16, v104\n\t"
    "v_and_b32 v105, -16, v105\n\t"
    "ds_read_b128 v[164:167], v104 offset:0\n\t"
    "ds_read_b128 v[168:171], v104 offset:64\n\t"
    "ds_read_b128 v[172:175], v104 offset:2080\n\t"
    "ds_read_b128 v[176:179], v104 offset:2144\n\t"
    "ds_read_b128 v[180:183], v104 offset:4160\n\t"
    "ds_read_b128 v[184:187], v104 offset:4224\n\t"
    "ds_read_b128 v[188:191], v104 offset:6240\n\t"
    "ds_read_b128 v[192:195], v104 offset:6304\n\t"
    "ds_read_b128 v[196:199], v104 offset:8320\n\t"
    "ds_read_b128 v[200:203], v104 offset:8384\n\t"
    "ds_read_b128 v[204:207], v104 offset:10400\n\t"
    "ds_read_b128 v[208:211], v104 offset:10464\n\t"
    "s_cmp_lt_u32 s62, 2\n\t"
    "s_cbranch_scc1 L_mel0_%=\n\t"
    "buffer_load_dwordx4 v[220:223], v127, %12, s61 offen offset:2048 sc1\n\t"
    "buffer_load_dwordx4 v[224:227], v127, %12, s61 offen offset:3072 sc1\n\t"
    "s_add_u32 s61, s61, 0x1000\n\t"
    "buffer_load_dwordx4 v[228:231], v127, %12, s61 offen offset:2048 sc1\n\t"
    "buffer_load_dwordx4 v[232:235], v127, %12, s61 offen offset:3072 sc1\n\t"
    "s_add_u32 s61, s61, 0x1000\n\t"
    "buffer_load_dwordx4 v[236:239], v127, %12, s61 offen offset:2048 sc1\n\t"
    "buffer_load_dwordx4 v[240:243], v127, %12, s61 offen offset:3072 sc1\n\t"
    "s_add_u32 s61, s61, 0x1000\n\t"
    "buffer_load_dwordx4 v[244:247], v127, %12, s61 offen offset:2048 sc1\n\t"
    "buffer_load_dwordx4 v[248:251], v127, %12, s61 offen offset:3072 sc1\n\t"
    "s_add_u32 s61, s61, 0x1000\n\t"
    "buffer_load_dwordx4 v[252:255], v127, %12, s61 offen offset:2048 sc1\n\t"
    "s_mov_b32 s62, 1\n\t"
    "L_mel0_%=:\n\t"
    "buffer_load_dwordx4 v[0:3], v127, %12, s63 offen offset:0 sc1\n\t"
    "buffer_load_dwordx4 v[4:7], v127, %12, s63 offen offset:1024 sc1\n\t"
    "buffer_load_dwordx4 v[8:11], v127, %12, s63 offen offset:2048 sc1\n\t"
    "buffer_load_dwordx4 v[12:15], v127, %12, s63 offen offset:3072 sc1\n\t"
    "s_add_u32 s63, s63, 0x1000\n\t"
    "s_mov_b32 s62, 0\n\t"
    "s_and_b32 s65, %14, 8\n\t"
    "s_lshl_b32 s65, s65, 10\n\t"
    "s_add_u32 s65, s65, s63\n\t"
    "s_mov_b32 m0, 136192\n\t"
    "s_add_u32 s65, s65, 0\n\t"
    "buffer_load_dwordx4 v127, %12, s65 offen offset:0 sc1 lds\n\t"
    "buffer_load_dwordx4 v127, %12, s65 offen offset:1024 sc1 lds\n\t"
    "buffer_load_dwordx4 v127, %12, s65 offen offset:2048 sc1 lds\n\t"
    "buffer_load_dwordx4 v127, %12, s65 offen offset:3072 sc1 lds\n\t"
    "s_mov_b32 m0, 140288\n\t"
    "s_add_u32 s65, s65, 4096\n\t"
    "buffer_load_dwordx4 v127, %12, s65 offen offset:0 sc1 lds\n\t"
    "buffer_load_dwordx4 v127, %12, s65 offen offset:1024 sc1 lds\n\t"
    "buffer_load_dwordx4 v127, %12, s65 offen offset:2048 sc1 lds\n\t"
    "buffer_load_dwordx4 v127, %12, s65 offen offset:3072 sc1 lds\n\t"
    "s_waitcnt vmcnt(21)\n\t"
    "s_and_b32 s50, %14, 7\n\t"
    "s_lshr_b32 s51, %14, 4\n\t"
    "s_add_u32 s51, s51, 8\n\t"
    "s_lshl_b32 s50, s50, 7\n\t"
    "s_lshl_b32 s51, s51, 7\n\t"
    "v_mov_b32 v132, s51\n\t"
    "v_writelane_b32 v132, s50, 0\n\t"
    "s_mov_b32 s50, 48237568\n\t"
    "s_mov_b64 exec, 3\n\t"
    "buffer_atomic_add v136, v132, %12, s50 offen sc1\n\t"
    "s_mov_b64 exec, -1\n\t"
    "s_branch L_af1x_%=\n\t"
    "L_af1w_%=:\n\t"
    "s_mov_b32 s51, 1048576\n\t"
    "s_mov_b64 exec, 0x1ff\n\t"
    "L_af1_%=:\n\t"
    "v_cmp_gt_u32 vcc, v148, v71\n\t"
    "s_cbranch_vccz L_af1ok_%=\n\t"
    "s_sleep 8\n\t"
    "buffer_load_dword v71, v70, %12, s52 offen sc1\n\t"
    "s_sub_u32 s51, s51, 1\n\t"
    "s_waitcnt vmcnt(0)\n\t"
    "s_cmp_lg_u32 s51, 0\n\t"
    "s_cbranch_scc1 L_af1_%=\n\t"
    "L_af1ok_%=:\n\t"
    "s_mov_b64 exec, -1\n\t"
    "v_mov_b32 v134, 3112\n\t"
    "ds_write_b32 v134, v136\n\t"
    "v_mbcnt_lo_u32_b32 v70, -1, 0\n\t"
    "v_mbcnt_hi_u32_b32 v70, -1, v70\n\t"
    "s_and_b32 s50, %14, 7\n\t"
    "s_lshl_b32 s50, s50, 4\n\t"
    "s_lshr_b32 s51, %14, 4\n\t"
    "s_add_u32 s50, s50, s51\n\t"
    "s_mul_i32 s50, s50, 102400\n\t"
    "s_add_u32 s50, s50, 3407872\n\t"
    "v_lshlrev_b32 v148, 7, v70\n\t"
    "buffer_load_dword v71, v148, %12, s50 offen sc1\n\t"
    "s_and_b32 s51, %14, 8\n\t"
    "s_lshl_b32 s51, s51, 10\n\t"
    "s_add_u32 s50, s50, s51\n\t"
    "s_add_u32 s50, s50, 0x1000\n\t"
    "v_lshrrev_b32 v149, 4, v70\n\t"
    "v_lshl_add_u32 v149, v149, 11, v148\n\t"
    "buffer_load_dword v71, v148, %12, s50 offen sc1\n\t"
    "s_bfe_u32 s50, %14, 0x40003\n\t"
    "s_lshl_b32 s50, s50, 14\n\t"
    "s_add_u32 s50, s50, 16517120\n\t"
    "v_lshrrev_b32 v150, 3, v70\n\t"
    "v_lshl_add_u32 v150, v150, 10, v148\n\t"
    "buffer_load_dword v71, v149, %12, s50 offen sc1\n\t"
    "s_and_b32 s50, %14, 8\n\t"
    "s_bfe_u32 s51, %14, 0x30004\n\t"
    "s_add_u32 s50, s50, s51\n\t"
    "s_mul_i32 s50, s50, 212992\n\t"
    "s_add_u32 s50, s50, 196608\n\t"
    "buffer_load_dword v71, v150, %12, s50 offen sc1\n\t"
    "s_setpc_b64 s[54:55]\n\t"
    "L_af1x_%=:\n\t"
    "s_mov_b32 s69, 0x40000000\n\t"
    "s_branch L_aend2_%=\n\t"
    "L_ahelp_%=:\n\t"
    "s_setprio 3\n\t"
    "v_cmp_gt_u32 vcc, 48, v147\n\t"
    "v_mov_b32 v133, 0x40000000\n\t"
    "v_lshrrev_b32 v134, 3, v147\n\t"
    "v_add_u32 v134, 6, v134\n\t"
    "v_bfe_u32 v135, v147, 2, 1\n\t"
    "v_and_b32 v130, 3, v147\n\t"
    "v_lshl_add_u32 v132, v135, 5, v130\n\t"
    "v_mul_u32_u24 v64, 400, v132\n\t"
    "v_lshl_add_u32 v64, v134, 5, v64\n\t"
    "s_movk_i32 s50, 0\n\t"
    "buffer_load_dwordx4 v[0:3], v64, %0, s50 offen offset:16\n\t"
    "buffer_load_dwordx4 v[4:7], v64, %0, s50 offen offset:32\n\t"
    "v_lshlrev_b32 v66, 14, v134\n\t"
    "v_lshl_add_u32 v66, v132, 4, v66\n\t"
    "v_lshlrev_b32 v68, 8, v134\n\t"
    "v_lshl_add_u32 v68, v135, 2, v68\n\t"
    "v_add_u32 v68, 130048, v68\n\t"
    "v_cndmask_b32 v68, v133, v68, vcc\n\t"
    "v_cndmask_b32 v66, v133, v66, vcc\n\t"
    "s_barrier\n\t"
    "s_load_dwordx2 s[72:73], %6, 0\n\t"
    "s_load_dwordx2 s[88:89], %7, 0\n\t"
    "s_load_dword s91, %8, 0\n\t"
    "v_lshlrev_b32 v128, 4, v147\n\t"
    "v_add_u32 v128, 133888, v128\n\t"
    "v_and_b32 v130, 1, v147\n\t"
    "v_cndmask_b32 v128, v133, v128, vcc\n\t"
    "v_mul_u32_u24 v127, 7, v130\n\t"
    "v_cmp_eq_u32 vcc, 1, v130\n\t"
    "v_mov_b32 v126, 0x07060100\n\t"
    "v_mov_b32 v133, 0x05040100\n\t"
    "v_mov_b32 v136, 1\n\t"
    "v_cndmask_b32 v126, v126, v133, vcc\n\t"
    "s_mov_b32 s36, 0\n\t"
    "s_mov_b32 s37, 8000\n\t"
    "s_mov_b32 s38, 16000\n\t"
    "s_mov_b32 s39, 24000\n\t"
    "s_mov_b32 s40, 32000\n\t"
    "s_mov_b32 s41, 40000\n\t"
    "s_mov_b32 s42, 48000\n\t"
    "s_mov_b32 s43, 56000\n\t"
    "s_movk_i32 s44, 0xf8\n\t"
    "s_mov_b32 s45, 0x01010000\n\t"
    "s_mov_b32 s46, 0x020c000c\n\t"
    "s_lshr_b32 s49, %14, 4\n\t"
    "s_mul_i32 s49, s49, 212992\n\t"
    "s_and_b32 s50, %14, 15\n\t"
    "s_lshl_b32 s50, s50, 10\n\t"
    "s_add_u32 s49, s49, s50\n\t"
    "s_lshl_b32 s52, %14, 2\n\t"
    "s_add_u32 s52, s52, 48242688\n\t"
    "buffer_load_dword v70, off, %12, s52 offset:1024 sc1\n\t"
    "s_cmp_eq_u32 %14, 0\n\t"
    "s_cselect_b32 s47, 48242688, 0x40000000\n\t"
    "s_cselect_b32 s48, 48234496, 0x40000000\n\t"
    "v_lshlrev_b32 v152, 4, v147\n\t"
    "v_lshlrev_b32 v132, 7, v147\n\t"
    "v_mov_b32 v71, 0\n\t"
    "buffer_store_dword v71, v132, %12, s48 offen sc1\n\t"
    "v_mov_b32 v148, 0\n\t"
    "v_mov_b32 v149, 0\n\t"
    "v_mov_b32 v150, 0\n\t"
    "v_mov_b32 v151, 0\n\t"
    "buffer_store_dwordx4 v[148:151], v152, %12, s47 offen offset:2048 sc1\n\t"
    "buffer_store_dwordx4 v[148:151], v152, %12, s47 offen offset:3072 sc1\n\t"
    "s_add_u32 s50, s48, 0x1000\n\t"
    "s_waitcnt vmcnt(6)\n\t"
    "v_lshlrev_b32 v134, 20, v147\n\t"
    "v_add_u32 v134, 2068, v134\n\t"
    "ds_add_u32 v134, v136\n\t"
    "v_mov_b32 v110, 65664\n\t"
    "v_mov_b32 v111, 131136\n\t"
    "v_mov_b32 v112, 262176\n\t"
    "v_mov_b32 v113, 524304\n\t"
    "v_mov_b32 v114, 1048584\n\t"
    "v_mov_b32 v115, 2097156\n\t"
    "v_mov_b32 v116, 4194306\n\t"
    "v_mov_b32 v117, 8388609\n\t"
    "s_waitcnt vmcnt(4)\n\t"
    "v_lshl_add_u32 v76, v0, 3, s36\n\t"
    "v_lshl_add_u32 v77, v1, 3, s37\n\t"
    "v_lshl_add_u32 v78, v2, 3, s38\n\t"
    "v_lshl_add_u32 v79, v3, 3, s39\n\t"
    "v_lshl_add_u32 v80, v4, 3, s40\n\t"
    "v_lshl_add_u32 v81, v5, 3, s41\n\t"
    "v_lshl_add_u32 v82, v6, 3, s42\n\t"
    "v_lshl_add_u32 v83, v7, 3, s43\n\t"
    "v_and_or_b32 v92, v76, s44, v68\n\t"
    "ds_or_rtn_b32 v118, v92, v110\n\t"
    "v_and_or_b32 v93, v77, s44, v68\n\t"
    "ds_or_rtn_b32 v119, v93, v111\n\t"
    "v_and_or_b32 v94, v78, s44, v68\n\t"
    "ds_or_rtn_b32 v120, v94, v112\n\t"
    "v_and_or_b32 v95, v79, s44, v68\n\t"
    "ds_or_rtn_b32 v121, v95, v113\n\t"
    "v_and_or_b32 v96, v80, s44, v68\n\t"
    "ds_or_rtn_b32 v122, v96, v114\n\t"
    "v_and_or_b32 v97, v81, s44, v68\n\t"
    "ds_or_rtn_b32 v123, v97, v115\n\t"
    "v_and_or_b32 v98, v82, s44, v68\n\t"
    "ds_or_rtn_b32 v124, v98, v116\n\t"
    "v_and_or_b32 v99, v83, s44, v68\n\t"
    "ds_or_rtn_b32 v125, v99, v117\n\t"
    "v_lshl_or_b32 v164, v77, 16, v76\n\t"
    "v_lshl_or_b32 v165, v79, 16, v78\n\t"
    "v_lshl_or_b32 v166, v81, 16, v80\n\t"
    "v_lshl_or_b32 v167, v83, 16, v82\n\t"
    "s_movk_i32 s50, 0\n\t"
    "buffer_load_dwordx4 v[8:11], v64, %0, s50 offen offset:1616\n\t"
    "buffer_load_dwordx4 v[12:15], v64, %0, s50 offen offset:1632\n\t"
    "s_movk_i32 s50, 3200\n\t"
    "buffer_load_dwordx4 v[16:19], v64, %0, s50 offen offset:16\n\t"
    "buffer_load_dwordx4 v[20:23], v64, %0, s50 offen offset:32\n\t"
    "s_waitcnt vmcnt(7)\n\t"
    "v_add_u32 v70, 1, v70\n\t"
    "buffer_store_dword v70, off, %12, s52 sc1\n\t"
    "v_mov_b32 v71, 2072\n\t"
    "ds_write_b32 v71, v70\n\t"
    "s_waitcnt vmcnt(3)\n\t"
    "v_lshl_add_u32 v84, v8, 3, s36\n\t"
    "v_lshl_add_u32 v85, v9, 3, s37\n\t"
    "v_lshl_add_u32 v86, v10, 3, s38\n\t"
    "v_lshl_add_u32 v87, v11, 3, s39\n\t"
    "v_lshl_add_u32 v88, v12, 3, s40\n\t"
    "v_lshl_add_u32 v89, v13, 3, s41\n\t"
    "v_lshl_add_u32 v90, v14, 3, s42\n\t"
    "v_lshl_add_u32 v91, v15, 3, s43\n\t"
    "s_waitcnt lgkmcnt(5)\n\t"
    "v_and_b32 v118, v118, v110\n\t"
    "v_perm_b32 v118, v118, v118, s46\n\t"
    "ds_or_b32 v92, v118\n\t"
    "v_and_b32 v119, v119, v111\n\t"
    "v_perm_b32 v119, v119, v119, s46\n\t"
    "ds_or_b32 v93, v119\n\t"
    "v_and_b32 v120, v120, v112\n\t"
    "v_perm_b32 v120, v120, v120, s46\n\t"
    "ds_or_b32 v94, v120\n\t"
    "v_and_b32 v121, v121, v113\n\t"
    "v_perm_b32 v121, v121, v121, s46\n\t"
    "ds_or_b32 v95, v121\n\t"
    "s_waitcnt lgkmcnt(5)\n\t"
    "v_and_b32 v122, v122, v114\n\t"
    "v_perm_b32 v122, v122, v122, s46\n\t"
    "ds_or_b32 v96, v122\n\t"
    "v_and_b32 v123, v123, v115\n\t"
    "v_perm_b32 v123, v123, v123, s46\n\t"
    "ds_or_b32 v97, v123\n\t"
    "v_and_b32 v124, v124, v116\n\t"
    "v_perm_b32 v124, v124, v124, s46\n\t"
    "ds_or_b32 v98, v124\n\t"
    "v_and_b32 v125, v125, v117\n\t"
    "v_perm_b32 v125, v125, v125, s46\n\t"
    "ds_or_b32 v99, v125\n\t"
    "buffer_store_dwordx4 v[164:167], v66, %12, s49 offen offset:0 sc1\n\t"
    "v_and_or_b32 v100, v84, s44, v68\n\t"
    "ds_read_b32 v100, v100\n\t"
    "v_and_or_b32 v101, v85, s44, v68\n\t"
    "ds_read_b32 v101, v101\n\t"
    "v_and_or_b32 v102, v86, s44, v68\n\t"
    "ds_read_b32 v102, v102\n\t"
    "v_and_or_b32 v103, v87, s44, v68\n\t"
    "ds_read_b32 v103, v103\n\t"
    "v_and_or_b32 v104, v88, s44, v68\n\t"
    "ds_read_b32 v104, v104\n\t"
    "v_and_or_b32 v105, v89, s44, v68\n\t"
    "ds_read_b32 v105, v105\n\t"
    "v_and_or_b32 v106, v90, s44, v68\n\t"
    "ds_read_b32 v106, v106\n\t"
    "v_and_or_b32 v107, v91, s44, v68\n\t"
    "ds_read_b32 v107, v107\n\t"
    "s_waitcnt vmcnt(2)\n\t"
    "v_lshl_add_u32 v76, v16, 3, s36\n\t"
    "v_lshl_add_u32 v77, v17, 3, s37\n\t"
    "v_lshl_add_u32 v78, v18, 3, s38\n\t"
    "v_lshl_add_u32 v79, v19, 3, s39\n\t"
    "v_lshl_add_u32 v80, v20, 3, s40\n\t"
    "v_lshl_add_u32 v81, v21, 3, s41\n\t"
    "v_lshl_add_u32 v82, v22, 3, s42\n\t"
    "v_lshl_add_u32 v83, v23, 3, s43\n\t"
    "s_movk_i32 s50, 3200\n\t"
    "buffer_load_dwordx4 v[24:27], v64, %0, s50 offen offset:1616\n\t"
    "buffer_load_dwordx4 v[28:31], v64, %0, s50 offen offset:1632\n\t"
    "s_movk_i32 s50, 6400\n\t"
    "buffer_load_dwordx4 v[32:35], v64, %0, s50 offen offset:16\n\t"
    "buffer_load_dwordx4 v[36:39], v64, %0, s50 offen offset:32\n\t"
    "s_cmp_lg_u32 %14, 0\n\t"
    "s_cbranch_scc1 L_ahe184_%=\n\t"
    "buffer_load_dwordx4 v[148:151], v152, %12, s47 offen sc1\n\t"
    "L_ahe184_%=:\n\t"
    "v_mov_b32 v129, 0xffff0000\n\t"
    "s_waitcnt lgkmcnt(4)\n\t"
    "v_perm_b32 v100, v100, v84, v126\n\t"
    "v_perm_b32 v101, v101, v85, v126\n\t"
    "v_perm_b32 v102, v102, v86, v126\n\t"
    "v_perm_b32 v103, v103, v87, v126\n\t"
    "s_waitcnt lgkmcnt(0)\n\t"
    "v_perm_b32 v104, v104, v88, v126\n\t"
    "v_perm_b32 v105, v105, v89, v126\n\t"
    "v_perm_b32 v106, v106, v90, v126\n\t"
    "v_perm_b32 v107, v107, v91, v126\n\t"
    "v_max_u32 v108, v100, v101\n\t"
    "v_min_u32 v101, v100, v101\n\t"
    "v_max_u32 v100, v102, v103\n\t"
    "v_min_u32 v103, v102, v103\n\t"
    "v_max_u32 v102, v104, v105\n\t"
    "v_min_u32 v105, v104, v105\n\t"
    "v_max_u32 v104, v106, v107\n\t"
    "v_min_u32 v107, v106, v107\n\t"
    "v_max_u32 v106, v108, v100\n\t"
    "v_min_u32 v100, v108, v100\n\t"
    "v_max_u32 v108, v101, v103\n\t"
    "v_min_u32 v103, v101, v103\n\t"
    "v_max_u32 v101, v102, v104\n\t"
    "v_min_u32 v104, v102, v104\n\t"
    "v_max_u32 v102, v105, v107\n\t"
    "v_min_u32 v107, v105, v107\n\t"
    "v_bfi_b32 v130, v106, 0, v129\n\t"
    "v_cmp_eq_u32 vcc, 0, v130\n\t"
    "v_and_or_b32 v92, v106, s44, v68\n\t"
    "s_nop 0\n\t"
    "v_cndmask_b32 v130, v130, v129, vcc\n\t"
    "v_ffbl_b32 v131, v130\n\t"
    "v_and_b32 v134, 7, v131\n\t"
    "v_lshlrev_b32 v134, v134, s45\n\t"
    "v_xor_b32 v129, v129, v134\n\t"
    "v_xor_b32 v132, v131, v127\n\t"
    "v_bfi_b32 v130, v101, 0, v129\n\t"
    "v_cmp_eq_u32 vcc, 0, v130\n\t"
    "v_and_or_b32 v93, v101, s44, v68\n\t"
    "v_xor_b32 v133, 23, v132\n\t"
    "v_lshlrev_b32 v110, v132, v136\n\t"
    "v_and_b32 v132, 7, v132\n\t"
    "v_lshl_or_b32 v110, v136, v133, v110\n\t"
    "v_lshl_add_u32 v132, v132, 1, v128\n\t"
    "ds_or_rtn_b32 v118, v92, v110\n\t"
    "ds_write_b16 v132, v106\n\t"
    "v_cndmask_b32 v130, v130, v129, vcc\n\t"
    "v_ffbl_b32 v131, v130\n\t"
    "v_and_b32 v134, 7, v131\n\t"
    "v_lshlrev_b32 v134, v134, s45\n\t"
    "v_xor_b32 v129, v129, v134\n\t"
    "v_xor_b32 v132, v131, v127\n\t"
    "v_bfi_b32 v130, v108, 0, v129\n\t"
    "v_cmp_eq_u32 vcc, 0, v130\n\t"
    "v_and_or_b32 v94, v108, s44, v68\n\t"
    "v_xor_b32 v133, 23, v132\n\t"
    "v_lshlrev_b32 v111, v132, v136\n\t"
    "v_and_b32 v132, 7, v132\n\t"
    "v_lshl_or_b32 v111, v136, v133, v111\n\t"
    "v_lshl_add_u32 v132, v132, 1, v128\n\t"
    "ds_or_rtn_b32 v119, v93, v111\n\t"
    "ds_write_b16 v132, v101\n\t"
    "v_cndmask_b32 v130, v130, v129, vcc\n\t"
    "v_ffbl_b32 v131, v130\n\t"
    "v_and_b32 v134, 7, v131\n\t"
    "v_lshlrev_b32 v134, v134, s45\n\t"
    "v_xor_b32 v129, v129, v134\n\t"
    "v_xor_b32 v132, v131, v127\n\t"
    "v_bfi_b32 v130, v100, 0, v129\n\t"
    "v_cmp_eq_u32 vcc, 0, v130\n\t"
    "v_and_or_b32 v95, v100, s44, v68\n\t"
    "v_xor_b32 v133, 23, v132\n\t"
    "v_lshlrev_b32 v112, v132, v136\n\t"
    "v_and_b32 v132, 7, v132\n\t"
    "v_lshl_or_b32 v112, v136, v133, v112\n\t"
    "v_lshl_add_u32 v132, v132, 1, v128\n\t"
    "ds_or_rtn_b32 v120, v94, v112\n\t"
    "ds_write_b16 v132, v108\n\t"
    "v_cndmask_b32 v130, v130, v129, vcc\n\t"
    "v_ffbl_b32 v131, v130\n\t"
    "v_and_b32 v134, 7, v131\n\t"
    "v_lshlrev_b32 v134, v134, s45\n\t"
    "v_xor_b32 v129, v129, v134\n\t"
    "v_xor_b32 v132, v131, v127\n\t"
    "v_bfi_b32 v130, v102, 0, v129\n\t"
    "v_cmp_eq_u32 vcc, 0, v130\n\t"
    "v_and_or_b32 v96, v102, s44, v68\n\t"
    "v_xor_b32 v133, 23, v132\n\t"
    "v_lshlrev_b32 v113, v132, v136\n\t"
    "v_and_b32 v132, 7, v132\n\t"
    "v_lshl_or_b32 v113, v136, v133, v113\n\t"
    "v_lshl_add_u32 v132, v132, 1, v128\n\t"
    "ds_or_rtn_b32 v121, v95, v113\n\t"
    "ds_write_b16 v132, v100\n\t"
    "v_cndmask_b32 v130, v130, v129, vcc\n\t"
    "v_ffbl_b32 v131, v130\n\t"
    "v_and_b32 v134, 7, v131\n\t"
    "v_lshlrev_b32 v134, v134, s45\n\t"
    "v_xor_b32 v129, v129, v134\n\t"
    "v_xor_b32 v132, v131, v127\n\t"
    "s_waitcnt lgkmcnt(7)\n\t"
    "v_and_b32 v118, v118, v110\n\t"
    "v_perm_b32 v118, v118, v118, s46\n\t"
    "ds_or_b32 v92, v118\n\t"
    "v_bfi_b32 v130, v104, 0, v129\n\t"
    "v_cmp_eq_u32 vcc, 0, v130\n\t"
    "v_and_or_b32 v97, v104, s44, v68\n\t"
    "v_xor_b32 v133, 23, v132\n\t"
    "v_lshlrev_b32 v114, v132, v136\n\t"
    "v_and_b32 v132, 7, v132\n\t"
    "v_lshl_or_b32 v114, v136, v133, v114\n\t"
    "v_lshl_add_u32 v132, v132, 1, v128\n\t"
    "ds_or_rtn_b32 v122, v96, v114\n\t"
    "ds_write_b16 v132, v102\n\t"
    "v_cndmask_b32 v130, v130, v129, vcc\n\t"
    "v_ffbl_b32 v131, v130\n\t"
    "v_and_b32 v134, 7, v131\n\t"
    "v_lshlrev_b32 v134, v134, s45\n\t"
    "v_xor_b32 v129, v129, v134\n\t"
    "v_xor_b32 v132, v131, v127\n\t"
    "s_waitcnt lgkmcnt(8)\n\t"
    "v_and_b32 v119, v119, v111\n\t"
    "v_perm_b32 v119, v119, v119, s46\n\t"
    "ds_or_b32 v93, v119\n\t"
    "v_bfi_b32 v130, v103, 0, v129\n\t"
    "v_cmp_eq_u32 vcc, 0, v130\n\t"
    "v_and_or_b32 v98, v103, s44, v68\n\t"
    "v_xor_b32 v133, 23, v132\n\t"
    "v_lshlrev_b32 v115, v132, v136\n\t"
    "v_and_b32 v132, 7, v132\n\t"
    "v_lshl_or_b32 v115, v136, v133, v115\n\t"
    "v_lshl_add_u32 v132, v132, 1, v128\n\t"
    "ds_or_rtn_b32 v123, v97, v115\n\t"
    "ds_write_b16 v132, v104\n\t"
    "v_cndmask_b32 v130, v130, v129, vcc\n\t"
    "v_ffbl_b32 v131, v130\n\t"
    "v_and_b32 v134, 7, v131\n\t"
    "v_lshlrev_b32 v134, v134, s45\n\t"
    "v_xor_b32 v129, v129, v134\n\t"
    "v_xor_b32 v132, v131, v127\n\t"
    "s_waitcnt lgkmcnt(9)\n\t"
    "v_and_b32 v120, v120, v112\n\t"
    "v_perm_b32 v120, v120, v120, s46\n\t"
    "ds_or_b32 v94, v120\n\t"
    "v_bfi_b32 v130, v107, 0, v129\n\t"
    "v_cmp_eq_u32 vcc, 0, v130\n\t"
    "v_and_or_b32 v99, v107, s44, v68\n\t"
    "v_xor_b32 v133, 23, v132\n\t"
    "v_lshlrev_b32 v116, v132, v136\n\t"
    "v_and_b32 v132, 7, v132\n\t"
    "v_lshl_or_b32 v116, v136, v133, v116\n\t"
    "v_lshl_add_u32 v132, v132, 1, v128\n\t"
    "ds_or_rtn_b32 v124, v98, v116\n\t"
    "ds_write_b16 v132, v103\n\t"
    "v_cndmask_b32 v130, v130, v129, vcc\n\t"
    "v_ffbl_b32 v131, v130\n\t"
    "v_xor_b32 v132, v131, v127\n\t"
    "s_waitcnt lgkmcnt(10)\n\t"
    "v_and_b32 v121, v121, v113\n\t"
    "v_perm_b32 v121, v121, v121, s46\n\t"
    "ds_or_b32 v95, v121\n\t"
    "v_xor_b32 v133, 23, v132\n\t"
    "v_lshlrev_b32 v117, v132, v136\n\t"
    "v_and_b32 v132, 7, v132\n\t"
    "v_lshl_or_b32 v117, v136, v133, v117\n\t"
    "v_lshl_add_u32 v132, v132, 1, v128\n\t"
    "ds_or_rtn_b32 v125, v99, v117\n\t"
    "ds_write_b16 v132, v107\n\t"
    "s_waitcnt lgkmcnt(10)\n\t"
    "v_and_b32 v122, v122, v114\n\t"
    "v_perm_b32 v122, v122, v122, s46\n\t"
    "ds_or_b32 v96, v122\n\t"
    "ds_read_b128 v[72:75], v128\n\t"
    "v_and_or_b32 v100, v76, s44, v68\n\t"
    "ds_read_b32 v100, v100\n\t"
    "v_and_or_b32 v101, v77, s44, v68\n\t"
    "ds_read_b32 v101, v101\n\t"
    "v_and_or_b32 v102, v78, s44, v68\n\t"
    "ds_read_b32 v102, v102\n\t"
    "v_and_or_b32 v103, v79, s44, v68\n\t"
    "ds_read_b32 v103, v103\n\t"
    "v_and_or_b32 v104, v80, s44, v68\n\t"
    "ds_read_b32 v104, v104\n\t"
    "v_and_or_b32 v105, v81, s44, v68\n\t"
    "ds_read_b32 v105, v105\n\t"
    "v_and_or_b32 v106, v82, s44, v68\n\t"
    "ds_read_b32 v106, v106\n\t"
    "v_and_or_b32 v107, v83, s44, v68\n\t"
    "ds_read_b32 v107, v107\n\t"
    "s_cmp_lg_u32 %14, 0\n\t"
    "s_cbranch_scc1 L_ahe378_%=\n\t"
    "s_waitcnt vmcnt(0)\n\t"
    "buffer_store_dwordx4 v[148:151], v152, %12, s47 offen offset:1024 sc1\n\t"
    "L_ahe378_%=:\n\t"
    "s_waitcnt vmcnt(2)\n\t"
    "v_lshl_add_u32 v84, v24, 3, s36\n\t"
    "v_lshl_add_u32 v85, v25, 3, s37\n\t"
    "v_lshl_add_u32 v86, v26, 3, s38\n\t"
    "v_lshl_add_u32 v87, v27, 3, s39\n\t"
    "v_lshl_add_u32 v88, v28, 3, s40\n\t"
    "v_lshl_add_u32 v89, v29, 3, s41\n\t"
    "v_lshl_add_u32 v90, v30, 3, s42\n\t"
    "v_lshl_add_u32 v91, v31, 3, s43\n\t"
    "v_mul_u32_u24 v58, 400, v147\n\t"
    "buffer_load_dwordx4 v[48:51], v58, %0, 0 offen\n\t"
    "s_lshr_b32 s50, %14, 5\n\t"
    "s_mul_i32 s50, s50, 125\n\t"
    "s_bfe_u32 s51, %14, 0x30002\n\t"
    "s_lshl_b32 s51, s51, 4\n\t"
    "s_min_u32 s51, s51, 109\n\t"
    "s_add_u32 s50, s50, s51\n\t"
    "s_lshl_b32 s50, s50, 8\n\t"
    "s_and_b32 s51, %14, 3\n\t"
    "s_lshl_b32 s51, s51, 6\n\t"
    "s_add_u32 s50, s50, s51\n\t"
    "s_bitcmp1_b32 %14, 1\n\t"
    "s_cselect_b32 s50, s50, 0x40000000\n\t"
    "v_and_b32 v59, 48, v147\n\t"
    "v_and_b32 v60, 15, v147\n\t"
    "v_lshl_add_u32 v59, v60, 8, v59\n\t"
    "buffer_load_dwordx4 v[52:55], v59, %15, s50 offen\n\t"
    "s_cmp_lg_u32 %14, 0\n\t"
    "s_cbranch_scc1 L_ahe410_%=\n\t"
    "buffer_load_dwordx4 v[148:151], v152, %12, s47 offen sc1\n\t"
    "L_ahe410_%=:\n\t"
    "s_waitcnt lgkmcnt(11)\n\t"
    "v_and_b32 v123, v123, v115\n\t"
    "v_perm_b32 v123, v123, v123, s46\n\t"
    "ds_or_b32 v97, v123\n\t"
    "v_and_b32 v124, v124, v116\n\t"
    "v_perm_b32 v124, v124, v124, s46\n\t"
    "ds_or_b32 v98, v124\n\t"
    "v_and_b32 v125, v125, v117\n\t"
    "v_perm_b32 v125, v125, v125, s46\n\t"
    "ds_or_b32 v99, v125\n\t"
    "s_waitcnt lgkmcnt(11)\n\t"
    "buffer_store_dwordx4 v[72:75], v66, %12, s49 offen offset:64 sc1\n\t"
    "v_mov_b32 v129, 0xffff0000\n\t"
    "s_waitcnt lgkmcnt(7)\n\t"
    "v_perm_b32 v100, v100, v76, v126\n\t"
    "v_perm_b32 v101, v101, v77, v126\n\t"
    "v_perm_b32 v102, v102, v78, v126\n\t"
    "v_perm_b32 v103, v103, v79, v126\n\t"
    "s_waitcnt lgkmcnt(3)\n\t"
    "v_perm_b32 v104, v104, v80, v126\n\t"
    "v_perm_b32 v105, v105, v81, v126\n\t"
    "v_perm_b32 v106, v106, v82, v126\n\t"
    "v_perm_b32 v107, v107, v83, v126\n\t"
    "v_max_u32 v108, v100, v101\n\t"
    "v_min_u32 v101, v100, v101\n\t"
    "v_max_u32 v100, v102, v103\n\t"
    "v_min_u32 v103, v102, v103\n\t"
    "v_max_u32 v102, v104, v105\n\t"
    "v_min_u32 v105, v104, v105\n\t"
    "v_max_u32 v104, v106, v107\n\t"
    "v_min_u32 v107, v106, v107\n\t"
    "v_max_u32 v106, v108, v100\n\t"
    "v_min_u32 v100, v108, v100\n\t"
    "v_max_u32 v108, v101, v103\n\t"
    "v_min_u32 v103, v101, v103\n\t"
    "v_max_u32 v101, v102, v104\n\t"
    "v_min_u32 v104, v102, v104\n\t"
    "v_max_u32 v102, v105, v107\n\t"
    "v_min_u32 v107, v105, v107\n\t"
    "v_bfi_b32 v130, v106, 0, v129\n\t"
    "v_cmp_eq_u32 vcc, 0, v130\n\t"
    "v_and_or_b32 v92, v106, s44, v68\n\t"
    "s_nop 0\n\t"
    "v_cndmask_b32 v130, v130, v129, vcc\n\t"
    "v_ffbl_b32 v131, v130\n\t"
    "v_and_b32 v134, 7, v131\n\t"
    "v_lshlrev_b32 v134, v134, s45\n\t"
    "v_xor_b32 v129, v129, v134\n\t"
    "v_xor_b32 v132, v131, v127\n\t"
    "v_bfi_b32 v130, v101, 0, v129\n\t"
    "v_cmp_eq_u32 vcc, 0, v130\n\t"
    "v_and_or_b32 v93, v101, s44, v68\n\t"
    "v_xor_b32 v133, 23, v132\n\t"
    "v_lshlrev_b32 v110, v132, v136\n\t"
    "v_and_b32 v132, 7, v132\n\t"
    "v_lshl_or_b32 v110, v136, v133, v110\n\t"
    "v_lshl_add_u32 v132, v132, 1, v128\n\t"
    "ds_or_rtn_b32 v118, v92, v110\n\t"
    "ds_write_b16 v132, v106\n\t"
    "v_cndmask_b32 v130, v130, v129, vcc\n\t"
    "v_ffbl_b32 v131, v130\n\t"
    "v_and_b32 v134, 7, v131\n\t"
    "v_lshlrev_b32 v134, v134, s45\n\t"
    "v_xor_b32 v129, v129, v134\n\t"
    "v_xor_b32 v132, v131, v127\n\t"
    "v_bfi_b32 v130, v108, 0, v129\n\t"
    "v_cmp_eq_u32 vcc, 0, v130\n\t"
    "v_and_or_b32 v94, v108, s44, v68\n\t"
    "v_xor_b32 v133, 23, v132\n\t"
    "v_lshlrev_b32 v111, v132, v136\n\t"
    "v_and_b32 v132, 7, v132\n\t"
    "v_lshl_or_b32 v111, v136, v133, v111\n\t"
    "v_lshl_add_u32 v132, v132, 1, v128\n\t"
    "ds_or_rtn_b32 v119, v93, v111\n\t"
    "ds_write_b16 v132, v101\n\t"
    "v_cndmask_b32 v130, v130, v129, vcc\n\t"
    "v_ffbl_b32 v131, v130\n\t"
    "v_and_b32 v134, 7, v131\n\t"
    "v_lshlrev_b32 v134, v134, s45\n\t"
    "v_xor_b32 v129, v129, v134\n\t"
    "v_xor_b32 v132, v131, v127\n\t"
    "v_bfi_b32 v130, v100, 0, v129\n\t"
    "v_cmp_eq_u32 vcc, 0, v130\n\t"
    "v_and_or_b32 v95, v100, s44, v68\n\t"
    "v_xor_b32 v133, 23, v132\n\t"
    "v_lshlrev_b32 v112, v132, v136\n\t"
    "v_and_b32 v132, 7, v132\n\t"
    "v_lshl_or_b32 v112, v136, v133, v112\n\t"
    "v_lshl_add_u32 v132, v132, 1, v128\n\t"
    "ds_or_rtn_b32 v120, v94, v112\n\t"
    "ds_write_b16 v132, v108\n\t"
    "v_cndmask_b32 v130, v130, v129, vcc\n\t"
    "v_ffbl_b32 v131, v130\n\t"
    "v_and_b32 v134, 7, v131\n\t"
    "v_lshlrev_b32 v134, v134, s45\n\t"
    "v_xor_b32 v129, v129, v134\n\t"
    "v_xor_b32 v132, v131, v127\n\t"
    "v_bfi_b32 v130, v102, 0, v129\n\t"
    "v_cmp_eq_u32 vcc, 0, v130\n\t"
    "v_and_or_b32 v96, v102, s44, v68\n\t"
    "v_xor_b32 v133, 23, v132\n\t"
    "v_lshlrev_b32 v113, v132, v136\n\t"
    "v_and_b32 v132, 7, v132\n\t"
    "v_lshl_or_b32 v113, v136, v133, v113\n\t"
    "v_lshl_add_u32 v132, v132, 1, v128\n\t"
    "ds_or_rtn_b32 v121, v95, v113\n\t"
    "ds_write_b16 v132, v100\n\t"
    "v_cndmask_b32 v130, v130, v129, vcc\n\t"
    "v_ffbl_b32 v131, v130\n\t"
    "v_and_b32 v134, 7, v131\n\t"
    "v_lshlrev_b32 v134, v134, s45\n\t"
    "v_xor_b32 v129, v129, v134\n\t"
    "v_xor_b32 v132, v131, v127\n\t"
    "s_waitcnt lgkmcnt(7)\n\t"
    "v_and_b32 v118, v118, v110\n\t"
    "v_perm_b32 v118, v118, v118, s46\n\t"
    "ds_or_b32 v92, v118\n\t"
    "v_bfi_b32 v130, v104, 0, v129\n\t"
    "v_cmp_eq_u32 vcc, 0, v130\n\t"
    "v_and_or_b32 v97, v104, s44, v68\n\t"
    "v_xor_b32 v133, 23, v132\n\t"
    "v_lshlrev_b32 v114, v132, v136\n\t"
    "v_and_b32 v132, 7, v132\n\t"
    "v_lshl_or_b32 v114, v136, v133, v114\n\t"
    "v_lshl_add_u32 v132, v132, 1, v128\n\t"
    "ds_or_rtn_b32 v122, v96, v114\n\t"
    "ds_write_b16 v132, v102\n\t"
    "v_cndmask_b32 v130, v130, v129, vcc\n\t"
    "v_ffbl_b32 v131, v130\n\t"
    "v_and_b32 v134, 7, v131\n\t"
    "v_lshlrev_b32 v134, v134, s45\n\t"
    "v_xor_b32 v129, v129, v134\n\t"
    "v_xor_b32 v132, v131, v127\n\t"
    "s_waitcnt lgkmcnt(8)\n\t"
    "v_and_b32 v119, v119, v111\n\t"
    "v_perm_b32 v119, v119, v119, s46\n\t"
    "ds_or_b32 v93, v119\n\t"
    "v_bfi_b32 v130, v103, 0, v129\n\t"
    "v_cmp_eq_u32 vcc, 0, v130\n\t"
    "v_and_or_b32 v98, v103, s44, v68\n\t"
    "v_xor_b32 v133, 23, v132\n\t"
    "v_lshlrev_b32 v115, v132, v136\n\t"
    "v_and_b32 v132, 7, v132\n\t"
    "v_lshl_or_b32 v115, v136, v133, v115\n\t"
    "v_lshl_add_u32 v132, v132, 1, v128\n\t"
    "ds_or_rtn_b32 v123, v97, v115\n\t"
    "ds_write_b16 v132, v104\n\t"
    "v_cndmask_b32 v130, v130, v129, vcc\n\t"
    "v_ffbl_b32 v131, v130\n\t"
    "v_and_b32 v134, 7, v131\n\t"
    "v_lshlrev_b32 v134, v134, s45\n\t"
    "v_xor_b32 v129, v129, v134\n\t"
    "v_xor_b32 v132, v131, v127\n\t"
    "s_waitcnt lgkmcnt(9)\n\t"
    "v_and_b32 v120, v120, v112\n\t"
    "v_perm_b32 v120, v120, v120, s46\n\t"
    "ds_or_b32 v94, v120\n\t"
    "v_bfi_b32 v130, v107, 0, v129\n\t"
    "v_cmp_eq_u32 vcc, 0, v130\n\t"
    "v_and_or_b32 v99, v107, s44, v68\n\t"
    "v_xor_b32 v133, 23, v132\n\t"
    "v_lshlrev_b32 v116, v132, v136\n\t"
    "v_and_b32 v132, 7, v132\n\t"
    "v_lshl_or_b32 v116, v136, v133, v116\n\t"
    "v_lshl_add_u32 v132, v132, 1, v128\n\t"
    "ds_or_rtn_b32 v124, v98, v116\n\t"
    "ds_write_b16 v132, v103\n\t"
    "v_cndmask_b32 v130, v130, v129, vcc\n\t"
    "v_ffbl_b32 v131, v130\n\t"
    "v_xor_b32 v132, v131, v127\n\t"
    "s_waitcnt lgkmcnt(10)\n\t"
    "v_and_b32 v121, v121, v113\n\t"
    "v_perm_b32 v121, v121, v121, s46\n\t"
    "ds_or_b32 v95, v121\n\t"
    "v_xor_b32 v133, 23, v132\n\t"
    "v_lshlrev_b32 v117, v132, v136\n\t"
    "v_and_b32 v132, 7, v132\n\t"
    "v_lshl_or_b32 v117, v136, v133, v117\n\t"
    "v_lshl_add_u32 v132, v132, 1, v128\n\t"
    "ds_or_rtn_b32 v125, v99, v117\n\t"
    "ds_write_b16 v132, v107\n\t"
    "s_waitcnt lgkmcnt(10)\n\t"
    "v_and_b32 v122, v122, v114\n\t"
    "v_perm_b32 v122, v122, v122, s46\n\t"
    "ds_or_b32 v96, v122\n\t"
    "ds_read_b128 v[72:75], v128\n\t"
    "v_and_or_b32 v100, v84, s44, v68\n\t"
    "ds_read_b32 v100, v100\n\t"
    "v_and_or_b32 v101, v85, s44, v68\n\t"
    "ds_read_b32 v101, v101\n\t"
    "v_and_or_b32 v102, v86, s44, v68\n\t"
    "ds_read_b32 v102, v102\n\t"
    "v_and_or_b32 v103, v87, s44, v68\n\t"
    "ds_read_b32 v103, v103\n\t"
    "v_and_or_b32 v104, v88, s44, v68\n\t"
    "ds_read_b32 v104, v104\n\t"
    "v_and_or_b32 v105, v89, s44, v68\n\t"
    "ds_read_b32 v105, v105\n\t"
    "v_and_or_b32 v106, v90, s44, v68\n\t"
    "ds_read_b32 v106, v106\n\t"
    "v_and_or_b32 v107, v91, s44, v68\n\t"
    "ds_read_b32 v107, v107\n\t"
    "s_cmp_lg_u32 %14, 0\n\t"
    "s_cbranch_scc1 L_ahe616_%=\n\t"
    "s_waitcnt vmcnt(0)\n\t"
    "buffer_store_dwordx4 v[148:151], v152, %12, s47 offen offset:1024 sc1\n\t"
    "L_ahe616_%=:\n\t"
    "s_waitcnt vmcnt(3)\n\t"
    "v_lshl_add_u32 v76, v32, 3, s36\n\t"
    "v_lshl_add_u32 v77, v33, 3, s37\n\t"
    "v_lshl_add_u32 v78, v34, 3, s38\n\t"
    "v_lshl_add_u32 v79, v35, 3, s39\n\t"
    "v_lshl_add_u32 v80, v36, 3, s40\n\t"
    "v_lshl_add_u32 v81, v37, 3, s41\n\t"
    "v_lshl_add_u32 v82, v38, 3, s42\n\t"
    "v_lshl_add_u32 v83, v39, 3, s43\n\t"
    "s_waitcnt vmcnt(1)\n\t"
    "v_lshl_add_u32 v48, v48, 3, s36\n\t"
    "v_lshl_add_u32 v49, v49, 3, s37\n\t"
    "v_lshl_add_u32 v50, v50, 3, s38\n\t"
    "v_lshl_add_u32 v51, v51, 3, s39\n\t"
    "v_lshl_or_b32 v56, v49, 16, v48\n\t"
    "v_lshl_or_b32 v57, v51, 16, v50\n\t"
    "s_and_b32 s51, %14, 1\n\t"
    "s_mul_i32 s51, s51, 1016\n\t"
    "s_sub_u32 s50, s49, s51\n\t"
    "s_add_u32 s50, s50, 196608\n\t"
    "buffer_store_dwordx2 v[56:57], v152, %12, s50 offen sc1\n\t"
    "s_lshl_b32 s50, %14, 10\n\t"
    "s_add_u32 s50, s50, 16515072\n\t"
    "s_bitcmp1_b32 %14, 1\n\t"
    "s_cselect_b32 s50, s50, 0x40000000\n\t"
    "buffer_store_dwordx4 v[52:55], v152, %12, s50 offen sc1\n\t"
    "s_mul_i32 s50, %14, 69632\n\t"
    "s_add_u32 s50, s50, 30474240\n\t"
    "v_mov_b32 v60, 0\n\t"
    "v_lshlrev_b32 v61, 3, v152\n\t"
    "s_mov_b64 exec, 0xffffffff\n\t"
    "buffer_store_dword v60, v61, %12, s50 offen sc1\n\t"
    "s_lshl_b32 s50, %14, 7\n\t"
    "s_add_u32 s50, s50, 48246784\n\t"
    "v_lshrrev_b32 v62, 2, v152\n\t"
    "buffer_store_dword v60, v62, %12, s50 offen sc1\n\t"
    "s_mov_b64 exec, -1\n\t"
    "s_cmp_lg_u32 %14, 0\n\t"
    "s_cbranch_scc1 L_ahe658_%=\n\t"
    "buffer_load_dwordx4 v[148:151], v152, %12, s47 offen sc1\n\t"
    "L_ahe658_%=:\n\t"
    "s_waitcnt lgkmcnt(11)\n\t"
    "v_and_b32 v123, v123, v115\n\t"
    "v_perm_b32 v123, v123, v123, s46\n\t"
    "ds_or_b32 v97, v123\n\t"
    "v_and_b32 v124, v124, v116\n\t"
    "v_perm_b32 v124, v124, v124, s46\n\t"
    "ds_or_b32 v98, v124\n\t"
    "v_and_b32 v125, v125, v117\n\t"
    "v_perm_b32 v125, v125, v125, s46\n\t"
    "ds_or_b32 v99, v125\n\t"
    "s_waitcnt lgkmcnt(11)\n\t"
    "buffer_store_dwordx4 v[72:75], v66, %12, s49 offen offset:128 sc1\n\t"
    "v_mov_b32 v129, 0xffff0000\n\t"
    "s_waitcnt lgkmcnt(7)\n\t"
    "v_perm_b32 v100, v100, v84, v126\n\t"
    "v_perm_b32 v101, v101, v85, v126\n\t"
    "v_perm_b32 v102, v102, v86, v126\n\t"
    "v_perm_b32 v103, v103, v87, v126\n\t"
    "s_waitcnt lgkmcnt(3)\n\t"
    "v_perm_b32 v104, v104, v88, v126\n\t"
    "v_perm_b32 v105, v105, v89, v126\n\t"
    "v_perm_b32 v106, v106, v90, v126\n\t"
    "v_perm_b32 v107, v107, v91, v126\n\t"
    "v_max_u32 v108, v100, v101\n\t"
    "v_min_u32 v101, v100, v101\n\t"
    "v_max_u32 v100, v102, v103\n\t"
    "v_min_u32 v103, v102, v103\n\t"
    "v_max_u32 v102, v104, v105\n\t"
    "v_min_u32 v105, v104, v105\n\t"
    "v_max_u32 v104, v106, v107\n\t"
    "v_min_u32 v107, v106, v107\n\t"
    "v_max_u32 v106, v108, v100\n\t"
    "v_min_u32 v100, v108, v100\n\t"
    "v_max_u32 v108, v101, v103\n\t"
    "v_min_u32 v103, v101, v103\n\t"
    "v_max_u32 v101, v102, v104\n\t"
    "v_min_u32 v104, v102, v104\n\t"
    "v_max_u32 v102, v105, v107\n\t"
    "v_min_u32 v107, v105, v107\n\t"
    "v_bfi_b32 v130, v106, 0, v129\n\t"
    "v_cmp_eq_u32 vcc, 0, v130\n\t"
    "v_and_or_b32 v92, v106, s44, v68\n\t"
    "s_nop 0\n\t"
    "v_cndmask_b32 v130, v130, v129, vcc\n\t"
    "v_ffbl_b32 v131, v130\n\t"
    "v_and_b32 v134, 7, v131\n\t"
    "v_lshlrev_b32 v134, v134, s45\n\t"
    "v_xor_b32 v129, v129, v134\n\t"
    "v_xor_b32 v132, v131, v127\n\t"
    "v_bfi_b32 v130, v101, 0, v129\n\t"
    "v_cmp_eq_u32 vcc, 0, v130\n\t"
    "v_and_or_b32 v93, v101, s44, v68\n\t"
    "v_xor_b32 v133, 23, v132\n\t"
    "v_lshlrev_b32 v110, v132, v136\n\t"
    "v_and_b32 v132, 7, v132\n\t"
    "v_lshl_or_b32 v110, v136, v133, v110\n\t"
    "v_lshl_add_u32 v132, v132, 1, v128\n\t"
    "ds_or_rtn_b32 v118, v92, v110\n\t"
    "ds_write_b16 v132, v106\n\t"
    "v_cndmask_b32 v130, v130, v129, vcc\n\t"
    "v_ffbl_b32 v131, v130\n\t"
    "v_and_b32 v134, 7, v131\n\t"
    "v_lshlrev_b32 v134, v134, s45\n\t"
    "v_xor_b32 v129, v129, v134\n\t"
    "v_xor_b32 v132, v131, v127\n\t"
    "v_bfi_b32 v130, v108, 0, v129\n\t"
    "v_cmp_eq_u32 vcc, 0, v130\n\t"
    "v_and_or_b32 v94, v108, s44, v68\n\t"
    "v_xor_b32 v133, 23, v132\n\t"
    "v_lshlrev_b32 v111, v132, v136\n\t"
    "v_and_b32 v132, 7, v132\n\t"
    "v_lshl_or_b32 v111, v136, v133, v111\n\t"
    "v_lshl_add_u32 v132, v132, 1, v128\n\t"
    "ds_or_rtn_b32 v119, v93, v111\n\t"
    "ds_write_b16 v132, v101\n\t"
    "v_cndmask_b32 v130, v130, v129, vcc\n\t"
    "v_ffbl_b32 v131, v130\n\t"
    "v_and_b32 v134, 7, v131\n\t"
    "v_lshlrev_b32 v134, v134, s45\n\t"
    "v_xor_b32 v129, v129, v134\n\t"
    "v_xor_b32 v132, v131, v127\n\t"
    "v_bfi_b32 v130, v100, 0, v129\n\t"
    "v_cmp_eq_u32 vcc, 0, v130\n\t"
    "v_and_or_b32 v95, v100, s44, v68\n\t"
    "v_xor_b32 v133, 23, v132\n\t"
    "v_lshlrev_b32 v112, v132, v136\n\t"
    "v_and_b32 v132, 7, v132\n\t"
    "v_lshl_or_b32 v112, v136, v133, v112\n\t"
    "v_lshl_add_u32 v132, v132, 1, v128\n\t"
    "ds_or_rtn_b32 v120, v94, v112\n\t"
    "ds_write_b16 v132, v108\n\t"
    "v_cndmask_b32 v130, v130, v129, vcc\n\t"
    "v_ffbl_b32 v131, v130\n\t"
    "v_and_b32 v134, 7, v131\n\t"
    "v_lshlrev_b32 v134, v134, s45\n\t"
    "v_xor_b32 v129, v129, v134\n\t"
    "v_xor_b32 v132, v131, v127\n\t"
    "v_bfi_b32 v130, v102, 0, v129\n\t"
    "v_cmp_eq_u32 vcc, 0, v130\n\t"
    "v_and_or_b32 v96, v102, s44, v68\n\t"
    "v_xor_b32 v133, 23, v132\n\t"
    "v_lshlrev_b32 v113, v132, v136\n\t"
    "v_and_b32 v132, 7, v132\n\t"
    "v_lshl_or_b32 v113, v136, v133, v113\n\t"
    "v_lshl_add_u32 v132, v132, 1, v128\n\t"
    "ds_or_rtn_b32 v121, v95, v113\n\t"
    "ds_write_b16 v132, v100\n\t"
    "v_cndmask_b32 v130, v130, v129, vcc\n\t"
    "v_ffbl_b32 v131, v130\n\t"
    "v_and_b32 v134, 7, v131\n\t"
    "v_lshlrev_b32 v134, v134, s45\n\t"
    "v_xor_b32 v129, v129, v134\n\t"
    "v_xor_b32 v132, v131, v127\n\t"
    "s_waitcnt lgkmcnt(7)\n\t"
    "v_and_b32 v118, v118, v110\n\t"
    "v_perm_b32 v118, v118, v118, s46\n\t"
    "ds_or_b32 v92, v118\n\t"
    "v_bfi_b32 v130, v104, 0, v129\n\t"
    "v_cmp_eq_u32 vcc, 0, v130\n\t"
    "v_and_or_b32 v97, v104, s44, v68\n\t"
    "v_xor_b32 v133, 23, v132\n\t"
    "v_lshlrev_b32 v114, v132, v136\n\t"
    "v_and_b32 v132, 7, v132\n\t"
    "v_lshl_or_b32 v114, v136, v133, v114\n\t"
    "v_lshl_add_u32 v132, v132, 1, v128\n\t"
    "ds_or_rtn_b32 v122, v96, v114\n\t"
    "ds_write_b16 v132, v102\n\t"
    "v_cndmask_b32 v130, v130, v129, vcc\n\t"
    "v_ffbl_b32 v131, v130\n\t"
    "v_and_b32 v134, 7, v131\n\t"
    "v_lshlrev_b32 v134, v134, s45\n\t"
    "v_xor_b32 v129, v129, v134\n\t"
    "v_xor_b32 v132, v131, v127\n\t"
    "s_waitcnt lgkmcnt(8)\n\t"
    "v_and_b32 v119, v119, v111\n\t"
    "v_perm_b32 v119, v119, v119, s46\n\t"
    "ds_or_b32 v93, v119\n\t"
    "v_bfi_b32 v130, v103, 0, v129\n\t"
    "v_cmp_eq_u32 vcc, 0, v130\n\t"
    "v_and_or_b32 v98, v103, s44, v68\n\t"
    "v_xor_b32 v133, 23, v132\n\t"
    "v_lshlrev_b32 v115, v132, v136\n\t"
    "v_and_b32 v132, 7, v132\n\t"
    "v_lshl_or_b32 v115, v136, v133, v115\n\t"
    "v_lshl_add_u32 v132, v132, 1, v128\n\t"
    "ds_or_rtn_b32 v123, v97, v115\n\t"
    "ds_write_b16 v132, v104\n\t"
    "v_cndmask_b32 v130, v130, v129, vcc\n\t"
    "v_ffbl_b32 v131, v130\n\t"
    "v_and_b32 v134, 7, v131\n\t"
    "v_lshlrev_b32 v134, v134, s45\n\t"
    "v_xor_b32 v129, v129, v134\n\t"
    "v_xor_b32 v132, v131, v127\n\t"
    "s_waitcnt lgkmcnt(9)\n\t"
    "v_and_b32 v120, v120, v112\n\t"
    "v_perm_b32 v120, v120, v120, s46\n\t"
    "ds_or_b32 v94, v120\n\t"
    "v_bfi_b32 v130, v107, 0, v129\n\t"
    "v_cmp_eq_u32 vcc, 0, v130\n\t"
    "v_and_or_b32 v99, v107, s44, v68\n\t"
    "v_xor_b32 v133, 23, v132\n\t"
    "v_lshlrev_b32 v116, v132, v136\n\t"
    "v_and_b32 v132, 7, v132\n\t"
    "v_lshl_or_b32 v116, v136, v133, v116\n\t"
    "v_lshl_add_u32 v132, v132, 1, v128\n\t"
    "ds_or_rtn_b32 v124, v98, v116\n\t"
    "ds_write_b16 v132, v103\n\t"
    "v_cndmask_b32 v130, v130, v129, vcc\n\t"
    "v_ffbl_b32 v131, v130\n\t"
    "v_xor_b32 v132, v131, v127\n\t"
    "s_waitcnt lgkmcnt(10)\n\t"
    "v_and_b32 v121, v121, v113\n\t"
    "v_perm_b32 v121, v121, v121, s46\n\t"
    "ds_or_b32 v95, v121\n\t"
    "v_xor_b32 v133, 23, v132\n\t"
    "v_lshlrev_b32 v117, v132, v136\n\t"
    "v_and_b32 v132, 7, v132\n\t"
    "v_lshl_or_b32 v117, v136, v133, v117\n\t"
    "v_lshl_add_u32 v132, v132, 1, v128\n\t"
    "ds_or_rtn_b32 v125, v99, v117\n\t"
    "ds_write_b16 v132, v107\n\t"
    "s_waitcnt lgkmcnt(10)\n\t"
    "v_and_b32 v122, v122, v114\n\t"
    "v_perm_b32 v122, v122, v122, s46\n\t"
    "ds_or_b32 v96, v122\n\t"
    "ds_read_b128 v[72:75], v128\n\t"
    "v_and_or_b32 v100, v76, s44, v68\n\t"
    "ds_read_b32 v100, v100\n\t"
    "v_and_or_b32 v101, v77, s44, v68\n\t"
    "ds_read_b32 v101, v101\n\t"
    "v_and_or_b32 v102, v78, s44, v68\n\t"
    "ds_read_b32 v102, v102\n\t"
    "v_and_or_b32 v103, v79, s44, v68\n\t"
    "ds_read_b32 v103, v103\n\t"
    "v_and_or_b32 v104, v80, s44, v68\n\t"
    "ds_read_b32 v104, v104\n\t"
    "v_and_or_b32 v105, v81, s44, v68\n\t"
    "ds_read_b32 v105, v105\n\t"
    "v_and_or_b32 v106, v82, s44, v68\n\t"
    "ds_read_b32 v106, v106\n\t"
    "v_and_or_b32 v107, v83, s44, v68\n\t"
    "ds_read_b32 v107, v107\n\t"
    "s_cmp_lg_u32 %14, 0\n\t"
    "s_cbranch_scc1 L_ahe864_%=\n\t"
    "s_waitcnt vmcnt(0)\n\t"
    "buffer_store_dwordx4 v[148:151], v152, %12, s47 offen offset:1024 sc1\n\t"
    "L_ahe864_%=:\n\t"
    "s_cmp_lg_u32 %14, 0\n\t"
    "s_cbranch_scc1 L_ahe869_%=\n\t"
    "buffer_load_dwordx4 v[148:151], v152, %12, s47 offen sc1\n\t"
    "L_ahe869_%=:\n\t"
    "s_waitcnt lgkmcnt(11)\n\t"
    "v_and_b32 v123, v123, v115\n\t"
    "v_perm_b32 v123, v123, v123, s46\n\t"
    "ds_or_b32 v97, v123\n\t"
    "v_and_b32 v124, v124, v116\n\t"
    "v_perm_b32 v124, v124, v124, s46\n\t"
    "ds_or_b32 v98, v124\n\t"
    "v_and_b32 v125, v125, v117\n\t"
    "v_perm_b32 v125, v125, v125, s46\n\t"
    "ds_or_b32 v99, v125\n\t"
    "s_waitcnt lgkmcnt(11)\n\t"
    "buffer_store_dwordx4 v[72:75], v66, %12, s49 offen offset:192 sc1\n\t"
    "v_mov_b32 v129, 0xffff0000\n\t"
    "s_waitcnt lgkmcnt(7)\n\t"
    "v_perm_b32 v100, v100, v76, v126\n\t"
    "v_perm_b32 v101, v101, v77, v126\n\t"
    "v_perm_b32 v102, v102, v78, v126\n\t"
    "v_perm_b32 v103, v103, v79, v126\n\t"
    "s_waitcnt lgkmcnt(3)\n\t"
    "v_perm_b32 v104, v104, v80, v126\n\t"
    "v_perm_b32 v105, v105, v81, v126\n\t"
    "v_perm_b32 v106, v106, v82, v126\n\t"
    "v_perm_b32 v107, v107, v83, v126\n\t"
    "v_max_u32 v108, v100, v101\n\t"
    "v_min_u32 v101, v100, v101\n\t"
    "v_max_u32 v100, v102, v103\n\t"
    "v_min_u32 v103, v102, v103\n\t"
    "v_max_u32 v102, v104, v105\n\t"
    "v_min_u32 v105, v104, v105\n\t"
    "v_max_u32 v104, v106, v107\n\t"
    "v_min_u32 v107, v106, v107\n\t"
    "v_max_u32 v106, v108, v100\n\t"
    "v_min_u32 v100, v108, v100\n\t"
    "v_max_u32 v108, v101, v103\n\t"
    "v_min_u32 v103, v101, v103\n\t"
    "v_max_u32 v101, v102, v104\n\t"
    "v_min_u32 v104, v102, v104\n\t"
    "v_max_u32 v102, v105, v107\n\t"
    "v_min_u32 v107, v105, v107\n\t"
    "v_bfi_b32 v130, v106, 0, v129\n\t"
    "v_cmp_eq_u32 vcc, 0, v130\n\t"
    "v_and_or_b32 v92, v106, s44, v68\n\t"
    "s_nop 0\n\t"
    "v_cndmask_b32 v130, v130, v129, vcc\n\t"
    "v_ffbl_b32 v131, v130\n\t"
    "v_and_b32 v134, 7, v131\n\t"
    "v_lshlrev_b32 v134, v134, s45\n\t"
    "v_xor_b32 v129, v129, v134\n\t"
    "v_xor_b32 v132, v131, v127\n\t"
    "v_bfi_b32 v130, v101, 0, v129\n\t"
    "v_cmp_eq_u32 vcc, 0, v130\n\t"
    "v_and_or_b32 v93, v101, s44, v68\n\t"
    "v_xor_b32 v133, 23, v132\n\t"
    "v_lshlrev_b32 v110, v132, v136\n\t"
    "v_and_b32 v132, 7, v132\n\t"
    "v_lshl_or_b32 v110, v136, v133, v110\n\t"
    "v_lshl_add_u32 v132, v132, 1, v128\n\t"
    "ds_or_rtn_b32 v118, v92, v110\n\t"
    "ds_write_b16 v132, v106\n\t"
    "v_cndmask_b32 v130, v130, v129, vcc\n\t"
    "v_ffbl_b32 v131, v130\n\t"
    "v_and_b32 v134, 7, v131\n\t"
    "v_lshlrev_b32 v134, v134, s45\n\t"
    "v_xor_b32 v129, v129, v134\n\t"
    "v_xor_b32 v132, v131, v127\n\t"
    "v_bfi_b32 v130, v108, 0, v129\n\t"
    "v_cmp_eq_u32 vcc, 0, v130\n\t"
    "v_and_or_b32 v94, v108, s44, v68\n\t"
    "v_xor_b32 v133, 23, v132\n\t"
    "v_lshlrev_b32 v111, v132, v136\n\t"
    "v_and_b32 v132, 7, v132\n\t"
    "v_lshl_or_b32 v111, v136, v133, v111\n\t"
    "v_lshl_add_u32 v132, v132, 1, v128\n\t"
    "ds_or_rtn_b32 v119, v93, v111\n\t"
    "ds_write_b16 v132, v101\n\t"
    "v_cndmask_b32 v130, v130, v129, vcc\n\t"
    "v_ffbl_b32 v131, v130\n\t"
    "v_and_b32 v134, 7, v131\n\t"
    "v_lshlrev_b32 v134, v134, s45\n\t"
    "v_xor_b32 v129, v129, v134\n\t"
    "v_xor_b32 v132, v131, v127\n\t"
    "v_bfi_b32 v130, v100, 0, v129\n\t"
    "v_cmp_eq_u32 vcc, 0, v130\n\t"
    "v_and_or_b32 v95, v100, s44, v68\n\t"
    "v_xor_b32 v133, 23, v132\n\t"
    "v_lshlrev_b32 v112, v132, v136\n\t"
    "v_and_b32 v132, 7, v132\n\t"
    "v_lshl_or_b32 v112, v136, v133, v112\n\t"
    "v_lshl_add_u32 v132, v132, 1, v128\n\t"
    "ds_or_rtn_b32 v120, v94, v112\n\t"
    "ds_write_b16 v132, v108\n\t"
    "v_cndmask_b32 v130, v130, v129, vcc\n\t"
    "v_ffbl_b32 v131, v130\n\t"
    "v_and_b32 v134, 7, v131\n\t"
    "v_lshlrev_b32 v134, v134, s45\n\t"
    "v_xor_b32 v129, v129, v134\n\t"
    "v_xor_b32 v132, v131, v127\n\t"
    "v_bfi_b32 v130, v102, 0, v129\n\t"
    "v_cmp_eq_u32 vcc, 0, v130\n\t"
    "v_and_or_b32 v96, v102, s44, v68\n\t"
    "v_xor_b32 v133, 23, v132\n\t"
    "v_lshlrev_b32 v113, v132, v136\n\t"
    "v_and_b32 v132, 7, v132\n\t"
    "v_lshl_or_b32 v113, v136, v133, v113\n\t"
    "v_lshl_add_u32 v132, v132, 1, v128\n\t"
    "ds_or_rtn_b32 v121, v95, v113\n\t"
    "ds_write_b16 v132, v100\n\t"
    "v_cndmask_b32 v130, v130, v129, vcc\n\t"
    "v_ffbl_b32 v131, v130\n\t"
    "v_and_b32 v134, 7, v131\n\t"
    "v_lshlrev_b32 v134, v134, s45\n\t"
    "v_xor_b32 v129, v129, v134\n\t"
    "v_xor_b32 v132, v131, v127\n\t"
    "s_waitcnt lgkmcnt(7)\n\t"
    "v_and_b32 v118, v118, v110\n\t"
    "v_perm_b32 v118, v118, v118, s46\n\t"
    "ds_or_b32 v92, v118\n\t"
    "s_waitcnt vmcnt(2)\n\t"
    "ds_write_b32 v71, v136 offset:4\n\t"
    "v_bfi_b32 v130, v104, 0, v129\n\t"
    "v_cmp_eq_u32 vcc, 0, v130\n\t"
    "v_and_or_b32 v97, v104, s44, v68\n\t"
    "v_xor_b32 v133, 23, v132\n\t"
    "v_lshlrev_b32 v114, v132, v136\n\t"
    "v_and_b32 v132, 7, v132\n\t"
    "v_lshl_or_b32 v114, v136, v133, v114\n\t"
    "v_lshl_add_u32 v132, v132, 1, v128\n\t"
    "ds_or_rtn_b32 v122, v96, v114\n\t"
    "ds_write_b16 v132, v102\n\t"
    "v_cndmask_b32 v130, v130, v129, vcc\n\t"
    "v_ffbl_b32 v131, v130\n\t"
    "v_and_b32 v134, 7, v131\n\t"
    "v_lshlrev_b32 v134, v134, s45\n\t"
    "v_xor_b32 v129, v129, v134\n\t"
    "v_xor_b32 v132, v131, v127\n\t"
    "s_waitcnt lgkmcnt(9)\n\t"
    "v_and_b32 v119, v119, v111\n\t"
    "v_perm_b32 v119, v119, v119, s46\n\t"
    "ds_or_b32 v93, v119\n\t"
    "v_bfi_b32 v130, v103, 0, v129\n\t"
    "v_cmp_eq_u32 vcc, 0, v130\n\t"
    "v_and_or_b32 v98, v103, s44, v68\n\t"
    "v_xor_b32 v133, 23, v132\n\t"
    "v_lshlrev_b32 v115, v132, v136\n\t"
    "v_and_b32 v132, 7, v132\n\t"
    "v_lshl_or_b32 v115, v136, v133, v115\n\t"
    "v_lshl_add_u32 v132, v132, 1, v128\n\t"
    "ds_or_rtn_b32 v123, v97, v115\n\t"
    "ds_write_b16 v132, v104\n\t"
    "v_cndmask_b32 v130, v130, v129, vcc\n\t"
    "v_ffbl_b32 v131, v130\n\t"
    "v_and_b32 v134, 7, v131\n\t"
    "v_lshlrev_b32 v134, v134, s45\n\t"
    "v_xor_b32 v129, v129, v134\n\t"
    "v_xor_b32 v132, v131, v127\n\t"
    "s_waitcnt lgkmcnt(10)\n\t"
    "v_and_b32 v120, v120, v112\n\t"
    "v_perm_b32 v120, v120, v120, s46\n\t"
    "ds_or_b32 v94, v120\n\t"
    "v_bfi_b32 v130, v107, 0, v129\n\t"
    "v_cmp_eq_u32 vcc, 0, v130\n\t"
    "v_and_or_b32 v99, v107, s44, v68\n\t"
    "v_xor_b32 v133, 23, v132\n\t"
    "v_lshlrev_b32 v116, v132, v136\n\t"
    "v_and_b32 v132, 7, v132\n\t"
    "v_lshl_or_b32 v116, v136, v133, v116\n\t"
    "v_lshl_add_u32 v132, v132, 1, v128\n\t"
    "ds_or_rtn_b32 v124, v98, v116\n\t"
    "ds_write_b16 v132, v103\n\t"
    "v_cndmask_b32 v130, v130, v129, vcc\n\t"
    "v_ffbl_b32 v131, v130\n\t"
    "v_xor_b32 v132, v131, v127\n\t"
    "s_waitcnt lgkmcnt(11)\n\t"
    "v_and_b32 v121, v121, v113\n\t"
    "v_perm_b32 v121, v121, v121, s46\n\t"
    "ds_or_b32 v95, v121\n\t"
    "v_xor_b32 v133, 23, v132\n\t"
    "v_lshlrev_b32 v117, v132, v136\n\t"
    "v_and_b32 v132, 7, v132\n\t"
    "v_lshl_or_b32 v117, v136, v133, v117\n\t"
    "v_lshl_add_u32 v132, v132, 1, v128\n\t"
    "ds_or_rtn_b32 v125, v99, v117\n\t"
    "ds_write_b16 v132, v107\n\t"
    "s_waitcnt lgkmcnt(10)\n\t"
    "v_and_b32 v122, v122, v114\n\t"
    "v_perm_b32 v122, v122, v122, s46\n\t"
    "ds_or_b32 v96, v122\n\t"
    "ds_read_b128 v[72:75], v128\n\t"
    "s_waitcnt lgkmcnt(3)\n\t"
    "v_and_b32 v123, v123, v115\n\t"
    "v_perm_b32 v123, v123, v123, s46\n\t"
    "ds_or_b32 v97, v123\n\t"
    "v_and_b32 v124, v124, v116\n\t"
    "v_perm_b32 v124, v124, v124, s46\n\t"
    "ds_or_b32 v98, v124\n\t"
    "v_and_b32 v125, v125, v117\n\t"
    "v_perm_b32 v125, v125, v125, s46\n\t"
    "ds_or_b32 v99, v125\n\t"
    "s_waitcnt lgkmcnt(3)\n\t"
    "buffer_store_dwordx4 v[72:75], v66, %12, s49 offen offset:256 sc1\n\t"
    "v_mov_b32 v134, 1036\n\t"
    "ds_write_b32 v134, v136\n\t"
    "s_cmp_lg_u32 %14, 0\n\t"
    "s_cbranch_scc1 L_ahsend_%=\n\t"
    "s_waitcnt vmcnt(0)\n\t"
    "buffer_store_dwordx4 v[148:151], v152, %12, s47 offen offset:1024 sc1\n\t"
    "s_setprio 0\n\t"
    "v_and_b32 v132, 0x70, v152\n\t"
    "v_lshlrev_b32 v132, 3, v132\n\t"
    "s_mov_b32 s51, 1048576\n\t"
    "L_ahs2_%=:\n\t"
    "s_sleep 8\n\t"
    "buffer_load_dword v71, v132, %12, s48 offen offset:1024 sc1\n\t"
    "buffer_load_dwordx4 v[148:151], v152, %12, s47 offen sc1\n\t"
    "s_sub_u32 s51, s51, 1\n\t"
    "s_cmp_eq_u32 s51, 0\n\t"
    "s_cbranch_scc1 L_ahsend_%=\n\t"
    "s_waitcnt vmcnt(0)\n\t"
    "buffer_store_dwordx4 v[148:151], v152, %12, s47 offen offset:1024 sc1\n\t"
    "v_cmp_gt_u32 vcc, 32, v71\n\t"
    "s_cbranch_vccnz L_ahs2_%=\n\t"
    "L_ahsend_%=:\n\t"
    "s_setprio 0\n\t"
    "s_and_b32 s46, %16, 3\n\t"
    "s_mul_i32 s49, s46, 1040\n\t"
    "s_add_u32 s49, s49, 2064\n\t"
    "s_lshl_b32 s46, s46, 3\n\t"
    "s_lshr_b32 s47, 1076763402, s46\n\t"
    "s_and_b32 s47, s47, 0xff\n\t"
    "s_lshl_b32 s48, s47, 16\n\t"
    "s_lshr_b32 s47, 773523968, s46\n\t"
    "s_and_b32 s47, s47, 0xff\n\t"
    "s_add_u32 s48, s48, s47\n\t"
    "s_lshr_b32 s47, %16, 2\n\t"
    "s_add_u32 s47, s47, 0x200\n\t"
    "s_cmp_eq_u32 s46, 0\n\t"
    "s_cselect_b32 s47, 0x100, s47\n\t"
    "s_add_u32 s48, s48, s47\n\t"
    "v_bfe_u32 v154, v147, 2, 2\n\t"
    "v_and_b32 v155, 3, v147\n\t"
    "v_mul_u32_u24 v154, 25, v154\n\t"
    "v_mad_u32_u24 v154, v155, 6, v154\n\t"
    "v_lshlrev_b32 v153, 2, v154\n\t"
    "s_and_b32 s53, s48, 0xff\n\t"
    "s_bfe_u32 s55, s48, 0x80008\n\t"
    "s_add_u32 s54, s53, s55\n\t"
    "s_add_u32 s55, s54, s55\n\t"
    "s_lshr_b32 s64, s48, 16\n\t"
    "s_mul_i32 s66, s53, 400\n\t"
    "buffer_load_dwordx3 v[128:130], v153, %0, s66 offen\n\t"
    "buffer_load_dwordx4 v[132:135], v153, %0, s66 offen offset:12\n\t"
    "s_mul_i32 s66, s54, 400\n\t"
    "buffer_load_dwordx3 v[136:138], v153, %0, s66 offen\n\t"
    "buffer_load_dwordx4 v[140:143], v153, %0, s66 offen offset:12\n\t"
    "v_cmp_eq_u32 s[62:63], 0, v155\n\t"
    "v_and_b32 v151, 48, v147\n\t"
    "v_or_b32 v155, 0x100000, v151\n\t"
    "v_cndmask_b32 v152, v155, v151, s[62:63]\n\t"
    "v_lshlrev_b32 v117, 20, v147\n\t"
    "v_add_u32 v117, 1024, v117\n\t"
    "v_mov_b32 v118, 1\n\t"
    "v_lshlrev_b32 v95, 4, v147\n\t"
    "v_add_u32 v95, 140288, v95\n\t"
    "s_call_b64 s[98:99], L_setup_%=\n\t"
    "L_await_%=:\n\t"
    "L_asp9_%=:\n\t"
    "ds_read_b32 v118, v117 offset:8\n\t"
    "ds_read_b32 v119, v117 offset:4\n\t"
    "s_waitcnt lgkmcnt(0)\n\t"
    "v_readfirstlane_b32 s46, v119\n\t"
    "s_nop 0\n\t"
    "s_cmp_ge_u32 s46, 4\n\t"
    "s_cbranch_scc1 L_ago9_%=\n\t"
    "s_sleep 1\n\t"
    "s_branch L_asp9_%=\n\t"
    "L_ago9_%=:\n\t"
    "v_readfirstlane_b32 s47, v118\n\t"
    "ds_read_b128 v[0:3], v95 offset:0\n\t"
    "ds_read_b128 v[4:7], v95 offset:1024\n\t"
    "ds_read_b128 v[8:11], v95 offset:2048\n\t"
    "ds_read_b128 v[12:15], v95 offset:3072\n\t"
    "ds_read_b128 v[16:19], v95 offset:4096\n\t"
    "ds_read_b128 v[20:23], v95 offset:5120\n\t"
    "ds_read_b128 v[24:27], v95 offset:6144\n\t"
    "ds_read_b128 v[28:31], v95 offset:7168\n\t"
    "ds_read_b128 v[32:35], v95 offset:8192\n\t"
    "ds_read_b128 v[36:39], v95 offset:9216\n\t"
    "ds_read_b128 v[40:43], v95 offset:10240\n\t"
    "ds_read_b128 v[44:47], v95 offset:11264\n\t"
    "ds_read_b128 v[48:51], v95 offset:12288\n\t"
    "ds_read_b128 v[52:55], v95 offset:13312\n\t"
    "ds_read_b128 v[56:59], v95 offset:14336\n\t"
    "ds_read_b128 v[60:63], v95 offset:15360\n\t"
    "ds_read_b128 v[64:67], v95 offset:16384\n\t"
    "ds_read_b128 v[68:71], v95 offset:17408\n\t"
    "ds_read_b128 v[72:75], v95 offset:18432\n\t"
    "ds_read_b128 v[76:79], v95 offset:19456\n\t"
    "ds_read_b128 v[80:83], v95 offset:20480\n\t"
    "ds_read_b128 v[84:87], v95 offset:21504\n\t"
    "ds_read_b128 v[88:91], v95 offset:22528\n\t"
    "s_waitcnt vmcnt(0)\n\t"
    "s_cmp_lg_u32 %16, 1\n\t"
    "s_cbranch_scc1 L_anh3_%=\n\t"
    "v_mov_b32 v119, 4\n\t"
    "ds_write_b32 v117, v119 offset:12\n\t"
    "s_lshl_b32 s65, %14, 12\n\t"
    "s_add_u32 s65, s65, 16777216\n\t"
    "s_waitcnt lgkmcnt(15)\n\t"
    "buffer_store_dwordx4 v[0:3], v127, %12, s65 offen offset:0 sc1\n\t"
    "buffer_store_dwordx4 v[4:7], v127, %12, s65 offen offset:1024 sc1\n\t"
    "buffer_store_dwordx4 v[8:11], v127, %12, s65 offen offset:2048 sc1\n\t"
    "buffer_store_dwordx4 v[12:15], v127, %12, s65 offen offset:3072 sc1\n\t"
    "L_anh3_%=:\n\t"
    "s_cmp_ge_u32 s47, 6\n\t"
    "s_cbranch_scc1 L_adok_%=\n\t"
    "L_awd_%=:\n\t"
    "L_asp10_%=:\n\t"
    "ds_read_b32 v119, v117 offset:8\n\t"
    "s_waitcnt lgkmcnt(0)\n\t"
    "v_readfirstlane_b32 s46, v119\n\t"
    "s_nop 0\n\t"
    "s_cmp_ge_u32 s46, 6\n\t"
    "s_cbranch_scc1 L_ago10_%=\n\t"
    "s_sleep 1\n\t"
    "s_branch L_asp10_%=\n\t"
    "L_ago10_%=:\n\t"
    "L_adok_%=:\n\t"
    "L_amain_%=:\n\t"
    "s_mov_b32 s68, 130\n\t"
    "v_mad_u32_u24 v128, v128, s68, v152\n\t"
    "v_and_b32 v128, -16, v128\n\t"
    "ds_read_b128 v[156:159], v128 offset:0\n\t"
    "ds_read_b128 v[160:163], v128 offset:64\n\t"
    "v_mad_u32_u24 v129, v129, s68, v151\n\t"
    "v_and_b32 v129, -16, v129\n\t"
    "ds_read_b128 v[164:167], v129 offset:0\n\t"
    "ds_read_b128 v[168:171], v129 offset:64\n\t"
    "v_mad_u32_u24 v130, v130, s68, v151\n\t"
    "v_and_b32 v130, -16, v130\n\t"
    "ds_read_b128 v[172:175], v130 offset:0\n\t"
    "ds_read_b128 v[176:179], v130 offset:64\n\t"
    "s_mul_i32 s66, s55, 400\n\t"
    "buffer_load_dwordx3 v[128:130], v153, %0, s66 offen\n\t"
    "v_mad_u32_u24 v132, v132, s68, v151\n\t"
    "v_and_b32 v132, -16, v132\n\t"
    "ds_read_b128 v[180:183], v132 offset:0\n\t"
    "ds_read_b128 v[184:187], v132 offset:64\n\t"
    "v_mad_u32_u24 v133, v133, s68, v151\n\t"
    "v_and_b32 v133, -16, v133\n\t"
    "ds_read_b128 v[188:191], v133 offset:0\n\t"
    "ds_read_b128 v[192:195], v133 offset:64\n\t"
    "v_mad_u32_u24 v134, v134, s68, v151\n\t"
    "v_and_b32 v134, -16, v134\n\t"
    "ds_read_b128 v[196:199], v134 offset:0\n\t"
    "ds_read_b128 v[200:203], v134 offset:64\n\t"
    "v_mad_u32_u24 v135, v135, s68, v151\n\t"
    "v_and_b32 v135, -16, v135\n\t"
    "ds_read_b128 v[204:207], v135 offset:0\n\t"
    "ds_read_b128 v[208:211], v135 offset:64\n\t"
    "v_mov_b32 v150, 1\n\t"
    "v_lshlrev_b32 v149, 20, v147\n\t"
    "v_add_u32 v149, s49, v149\n\t"
    "v_lshlrev_b32 v146, 2, v147\n\t"
    "s_mov_b32 s52, s53\n\t"
    "s_lshl_b32 s50, %14, 14\n\t"
    "s_add_u32 s50, s50, 26214400\n\t"
    "L_apnn_%=:\n\t"
    "s_mul_i32 s69, s52, 256\n\t"
    "s_add_u32 s69, s69, s50\n\t"
    "s_waitcnt lgkmcnt(8)\n\t"
    "v_mfma_f32_16x16x32_bf16 v[92:95], v[156:159], v[156:159], 0\n\t"
    "v_fmac_f32 v145, v108, v72\n\t"
    "v_fmac_f32 v144, v109, v73\n\t"
    "ds_add_rtn_u32 v148, v149, v150\n\t"
    "v_mfma_f32_16x16x32_bf16 v[92:95], v[160:163], v[160:163], v[92:95]\n\t"
    "v_fmac_f32 v145, v110, v74\n\t"
    "v_fmac_f32 v144, v111, v75\n\t"
    "buffer_load_dwordx4 v[132:135], v153, %0, s66 offen offset:12\n\t"
    "v_mfma_f32_16x16x32_bf16 v[96:99], v[164:167], v[164:167], 0\n\t"
    "v_fmac_f32 v145, v112, v76\n\t"
    "v_fmac_f32 v144, v113, v77\n\t"
    "v_mfma_f32_16x16x32_bf16 v[96:99], v[168:171], v[168:171], v[96:99]\n\t"
    "v_fmac_f32 v145, v114, v78\n\t"
    "v_fmac_f32 v144, v115, v79\n\t"
    "v_mfma_f32_16x16x32_bf16 v[100:103], v[164:167], v[172:175], 0\n\t"
    "v_fmac_f32 v145, v116, v80\n\t"
    "v_fmac_f32 v144, v117, v81\n\t"
    "v_mfma_f32_16x16x32_bf16 v[100:103], v[168:171], v[176:179], v[100:103]\n\t"
    "v_fmac_f32 v145, v118, v82\n\t"
    "v_fmac_f32 v144, v119, v83\n\t"
    "s_waitcnt vmcnt(4)\n\t"
    "s_cmp_ge_u32 s55, s64\n\t"
    "s_cbranch_scc1 L_ahk00_%=\n\t"
    "L_ahb00_%=:\n\t"
    "v_mfma_f32_16x16x32_bf16 v[104:107], v[172:175], v[172:175], 0\n\t"
    "v_mad_u32_u24 v136, v136, s68, v152\n\t"
    "v_and_b32 v136, -16, v136\n\t"
    "v_mfma_f32_16x16x32_bf16 v[104:107], v[176:179], v[176:179], v[104:107]\n\t"
    "v_add_f32 v154, v144, v145\n\t"
    "buffer_store_dword v154, v146, %12, s69 offen sc1\n\t"
    "v_mul_f32 v144, v92, v84\n\t"
    "v_mfma_f32_16x16x32_bf16 v[108:111], v[156:159], v[164:167], 0\n\t"
    "v_mul_f32 v145, v96, v0\n\t"
    "v_fmac_f32 v144, v97, v1\n\t"
    "v_mfma_f32_16x16x32_bf16 v[108:111], v[160:163], v[168:171], v[108:111]\n\t"
    "v_fmac_f32 v145, v98, v2\n\t"
    "v_fmac_f32 v144, v99, v3\n\t"
    "s_waitcnt lgkmcnt(7)\n\t"
    "v_mfma_f32_16x16x32_bf16 v[112:115], v[164:167], v[180:183], 0\n\t"
    "v_fmac_f32 v145, v100, v4\n\t"
    "v_fmac_f32 v144, v101, v5\n\t"
    "v_mfma_f32_16x16x32_bf16 v[112:115], v[168:171], v[184:187], v[112:115]\n\t"
    "v_fmac_f32 v145, v102, v6\n\t"
    "v_fmac_f32 v144, v103, v7\n\t"
    "v_mfma_f32_16x16x32_bf16 v[116:119], v[172:175], v[180:183], 0\n\t"
    "v_fmac_f32 v145, v104, v8\n\t"
    "v_fmac_f32 v144, v105, v9\n\t"
    "v_mfma_f32_16x16x32_bf16 v[116:119], v[176:179], v[184:187], v[116:119]\n\t"
    "v_fmac_f32 v145, v106, v10\n\t"
    "v_fmac_f32 v144, v107, v11\n\t"
    "ds_read_b128 v[212:215], v136 offset:0\n\t"
    "ds_read_b128 v[216:219], v136 offset:64\n\t"
    "v_mfma_f32_16x16x32_bf16 v[92:95], v[180:183], v[180:183], 0\n\t"
    "v_mad_u32_u24 v137, v137, s68, v151\n\t"
    "v_and_b32 v137, -16, v137\n\t"
    "v_mfma_f32_16x16x32_bf16 v[92:95], v[184:187], v[184:187], v[92:95]\n\t"
    "v_fmac_f32 v145, v108, v85\n\t"
    "v_mad_u32_u24 v138, v138, s68, v151\n\t"
    "v_mfma_f32_16x16x32_bf16 v[96:99], v[156:159], v[172:175], 0\n\t"
    "v_fmac_f32 v144, v112, v12\n\t"
    "v_fmac_f32 v145, v113, v13\n\t"
    "v_mfma_f32_16x16x32_bf16 v[96:99], v[160:163], v[176:179], v[96:99]\n\t"
    "v_fmac_f32 v144, v114, v14\n\t"
    "v_fmac_f32 v145, v115, v15\n\t"
    "s_waitcnt lgkmcnt(7)\n\t"
    "v_mfma_f32_16x16x32_bf16 v[100:103], v[164:167], v[188:191], 0\n\t"
    "v_and_b32 v138, -16, v138\n\t"
    "v_fmac_f32 v144, v116, v16\n\t"
    "v_mfma_f32_16x16x32_bf16 v[100:103], v[168:171], v[192:195], v[100:103]\n\t"
    "v_fmac_f32 v145, v117, v17\n\t"
    "v_fmac_f32 v144, v118, v18\n\t"
    "v_mfma_f32_16x16x32_bf16 v[104:107], v[172:175], v[188:191], 0\n\t"
    "v_fmac_f32 v145, v119, v19\n\t"
    "v_fmac_f32 v144, v92, v20\n\t"
    "v_mfma_f32_16x16x32_bf16 v[104:107], v[176:179], v[192:195], v[104:107]\n\t"
    "v_fmac_f32 v145, v93, v21\n\t"
    "v_fmac_f32 v144, v94, v22\n\t"
    "s_waitcnt lgkmcnt(5)\n\t"
    "v_mfma_f32_16x16x32_bf16 v[108:111], v[164:167], v[196:199], 0\n\t"
    "v_fmac_f32 v145, v95, v23\n\t"
    "v_fmac_f32 v144, v96, v86\n\t"
    "v_mfma_f32_16x16x32_bf16 v[108:111], v[168:171], v[200:203], v[108:111]\n\t"
    "v_mfma_f32_16x16x32_bf16 v[112:115], v[156:159], v[180:183], 0\n\t"
    "v_fmac_f32 v145, v100, v24\n\t"
    "v_fmac_f32 v144, v101, v25\n\t"
    "v_mfma_f32_16x16x32_bf16 v[112:115], v[160:163], v[184:187], v[112:115]\n\t"
    "v_fmac_f32 v145, v102, v26\n\t"
    "v_fmac_f32 v144, v103, v27\n\t"
    "s_waitcnt lgkmcnt(2)\n\t"
    "v_mfma_f32_16x16x32_bf16 v[116:119], v[164:167], v[204:207], 0\n\t"
    "v_readfirstlane_b32 s60, v148\n\t"
    "v_fmac_f32 v145, v104, v28\n\t"
    "v_mfma_f32_16x16x32_bf16 v[116:119], v[168:171], v[208:211], v[116:119]\n\t"
    "v_fmac_f32 v144, v105, v29\n\t"
    "v_fmac_f32 v145, v106, v30\n\t"
    "ds_read_b128 v[164:167], v137 offset:0\n\t"
    "ds_read_b128 v[168:171], v137 offset:64\n\t"
    "v_mfma_f32_16x16x32_bf16 v[92:95], v[172:175], v[196:199], 0\n\t"
    "v_fmac_f32 v144, v107, v31\n\t"
    "v_fmac_f32 v145, v108, v32\n\t"
    "v_mfma_f32_16x16x32_bf16 v[92:95], v[176:179], v[200:203], v[92:95]\n\t"
    "v_fmac_f32 v144, v109, v33\n\t"
    "v_fmac_f32 v145, v110, v34\n\t"
    "s_waitcnt vmcnt(4)\n\t"
    "s_cmp_ge_u32 s55, s64\n\t"
    "s_cbranch_scc1 L_ahk10_%=\n\t"
    "L_ahb10_%=:\n\t"
    "v_mfma_f32_16x16x32_bf16 v[96:99], v[172:175], v[204:207], 0\n\t"
    "v_mad_u32_u24 v140, v140, s68, v151\n\t"
    "v_and_b32 v140, -16, v140\n\t"
    "v_mfma_f32_16x16x32_bf16 v[96:99], v[176:179], v[208:211], v[96:99]\n\t"
    "v_fmac_f32 v144, v111, v35\n\t"
    "v_fmac_f32 v145, v112, v87\n\t"
    "ds_read_b128 v[172:175], v138 offset:0\n\t"
    "ds_read_b128 v[176:179], v138 offset:64\n\t"
    "s_mul_i32 s66, s60, 400\n\t"
    "v_mfma_f32_16x16x32_bf16 v[100:103], v[156:159], v[188:191], 0\n\t"
    "v_fmac_f32 v144, v116, v36\n\t"
    "v_fmac_f32 v145, v117, v37\n\t"
    "v_mfma_f32_16x16x32_bf16 v[100:103], v[160:163], v[192:195], v[100:103]\n\t"
    "v_fmac_f32 v144, v118, v38\n\t"
    "v_fmac_f32 v145, v119, v39\n\t"
    "buffer_load_dwordx3 v[136:138], v153, %0, s66 offen\n\t"
    "v_mfma_f32_16x16x32_bf16 v[104:107], v[180:183], v[188:191], 0\n\t"
    "v_mad_u32_u24 v141, v141, s68, v151\n\t"
    "v_and_b32 v141, -16, v141\n\t"
    "v_mfma_f32_16x16x32_bf16 v[104:107], v[184:187], v[192:195], v[104:107]\n\t"
    "v_fmac_f32 v144, v92, v40\n\t"
    "v_fmac_f32 v145, v93, v41\n\t"
    "v_mfma_f32_16x16x32_bf16 v[108:111], v[180:183], v[196:199], 0\n\t"
    "v_fmac_f32 v144, v94, v42\n\t"
    "v_fmac_f32 v145, v95, v43\n\t"
    "v_mfma_f32_16x16x32_bf16 v[108:111], v[184:187], v[200:203], v[108:111]\n\t"
    "v_fmac_f32 v144, v96, v44\n\t"
    "v_fmac_f32 v145, v97, v45\n\t"
    "v_mfma_f32_16x16x32_bf16 v[112:115], v[180:183], v[204:207], 0\n\t"
    "v_fmac_f32 v144, v98, v46\n\t"
    "v_fmac_f32 v145, v99, v47\n\t"
    "v_mfma_f32_16x16x32_bf16 v[112:115], v[184:187], v[208:211], v[112:115]\n\t"
    "v_fmac_f32 v144, v100, v88\n\t"
    "v_mad_u32_u24 v142, v142, s68, v151\n\t"
    "ds_read_b128 v[180:183], v140 offset:0\n\t"
    "ds_read_b128 v[184:187], v140 offset:64\n\t"
    "v_mfma_f32_16x16x32_bf16 v[116:119], v[156:159], v[196:199], 0\n\t"
    "v_and_b32 v142, -16, v142\n\t"
    "v_fmac_f32 v145, v104, v48\n\t"
    "v_mfma_f32_16x16x32_bf16 v[116:119], v[160:163], v[200:203], v[116:119]\n\t"
    "v_fmac_f32 v144, v105, v49\n\t"
    "v_fmac_f32 v145, v106, v50\n\t"
    "v_mfma_f32_16x16x32_bf16 v[92:95], v[188:191], v[188:191], 0\n\t"
    "v_fmac_f32 v144, v107, v51\n\t"
    "v_mad_u32_u24 v143, v143, s68, v151\n\t"
    "v_mfma_f32_16x16x32_bf16 v[92:95], v[192:195], v[192:195], v[92:95]\n\t"
    "v_and_b32 v143, -16, v143\n\t"
    "v_fmac_f32 v145, v108, v52\n\t"
    "v_mfma_f32_16x16x32_bf16 v[96:99], v[188:191], v[196:199], 0\n\t"
    "v_fmac_f32 v144, v109, v53\n\t"
    "v_fmac_f32 v145, v110, v54\n\t"
    "v_mfma_f32_16x16x32_bf16 v[96:99], v[192:195], v[200:203], v[96:99]\n\t"
    "v_fmac_f32 v144, v111, v55\n\t"
    "v_fmac_f32 v145, v112, v56\n\t"
    "v_mfma_f32_16x16x32_bf16 v[100:103], v[188:191], v[204:207], 0\n\t"
    "v_fmac_f32 v144, v113, v57\n\t"
    "v_fmac_f32 v145, v114, v58\n\t"
    "v_mfma_f32_16x16x32_bf16 v[100:103], v[192:195], v[208:211], v[100:103]\n\t"
    "v_fmac_f32 v144, v115, v59\n\t"
    "v_fmac_f32 v145, v116, v89\n\t"
    "ds_read_b128 v[188:191], v141 offset:0\n\t"
    "ds_read_b128 v[192:195], v141 offset:64\n\t"
    "v_mfma_f32_16x16x32_bf16 v[104:107], v[156:159], v[204:207], 0\n\t"
    "v_fmac_f32 v144, v92, v60\n\t"
    "v_fmac_f32 v145, v93, v61\n\t"
    "v_mfma_f32_16x16x32_bf16 v[104:107], v[160:163], v[208:211], v[104:107]\n\t"
    "v_fmac_f32 v144, v94, v62\n\t"
    "v_fmac_f32 v145, v95, v63\n\t"
    "v_mfma_f32_16x16x32_bf16 v[108:111], v[196:199], v[196:199], 0\n\t"
    "v_fmac_f32 v144, v96, v64\n\t"
    "v_fmac_f32 v145, v97, v65\n\t"
    "v_mfma_f32_16x16x32_bf16 v[108:111], v[200:203], v[200:203], v[108:111]\n\t"
    "v_fmac_f32 v144, v98, v66\n\t"
    "v_fmac_f32 v145, v99, v67\n\t"
    "v_mfma_f32_16x16x32_bf16 v[112:115], v[196:199], v[204:207], 0\n\t"
    "v_fmac_f32 v144, v100, v68\n\t"
    "v_fmac_f32 v145, v101, v69\n\t"
    "v_mfma_f32_16x16x32_bf16 v[112:115], v[200:203], v[208:211], v[112:115]\n\t"
    "v_fmac_f32 v144, v102, v70\n\t"
    "v_fmac_f32 v145, v103, v71\n\t"
    "ds_read_b128 v[196:199], v142 offset:0\n\t"
    "ds_read_b128 v[200:203], v142 offset:64\n\t"
    "v_mfma_f32_16x16x32_bf16 v[116:119], v[204:207], v[204:207], 0\n\t"
    "v_fmac_f32 v144, v104, v90\n\t"
    "v_mfma_f32_16x16x32_bf16 v[116:119], v[208:211], v[208:211], v[116:119]\n\t"
    "ds_read_b128 v[204:207], v143 offset:0\n\t"
    "ds_read_b128 v[208:211], v143 offset:64\n\t"
    "s_mov_b32 s52, s53\n\t"
    "s_mov_b32 s53, s54\n\t"
    "s_mov_b32 s54, s55\n\t"
    "s_mov_b32 s55, s60\n\t"
    "s_cmp_lt_u32 s53, s64\n\t"
    "s_cbranch_scc0 L_adone_%=\n\t"
    "s_mul_i32 s69, s52, 256\n\t"
    "s_add_u32 s69, s69, s50\n\t"
    "s_waitcnt lgkmcnt(8)\n\t"
    "v_mfma_f32_16x16x32_bf16 v[92:95], v[212:215], v[212:215], 0\n\t"
    "v_fmac_f32 v145, v108, v72\n\t"
    "v_fmac_f32 v144, v109, v73\n\t"
    "ds_add_rtn_u32 v148, v149, v150\n\t"
    "v_mfma_f32_16x16x32_bf16 v[92:95], v[216:219], v[216:219], v[92:95]\n\t"
    "v_fmac_f32 v145, v110, v74\n\t"
    "v_fmac_f32 v144, v111, v75\n\t"
    "buffer_load_dwordx4 v[140:143], v153, %0, s66 offen offset:12\n\t"
    "v_mfma_f32_16x16x32_bf16 v[96:99], v[164:167], v[164:167], 0\n\t"
    "v_fmac_f32 v145, v112, v76\n\t"
    "v_fmac_f32 v144, v113, v77\n\t"
    "v_mfma_f32_16x16x32_bf16 v[96:99], v[168:171], v[168:171], v[96:99]\n\t"
    "v_fmac_f32 v145, v114, v78\n\t"
    "v_fmac_f32 v144, v115, v79\n\t"
    "v_mfma_f32_16x16x32_bf16 v[100:103], v[164:167], v[172:175], 0\n\t"
    "v_fmac_f32 v145, v116, v80\n\t"
    "v_fmac_f32 v144, v117, v81\n\t"
    "v_mfma_f32_16x16x32_bf16 v[100:103], v[168:171], v[176:179], v[100:103]\n\t"
    "v_fmac_f32 v145, v118, v82\n\t"
    "v_fmac_f32 v144, v119, v83\n\t"
    "s_waitcnt vmcnt(4)\n\t"
    "s_cmp_ge_u32 s55, s64\n\t"
    "s_cbranch_scc1 L_ahk01_%=\n\t"
    "L_ahb01_%=:\n\t"
    "v_mfma_f32_16x16x32_bf16 v[104:107], v[172:175], v[172:175], 0\n\t"
    "v_mad_u32_u24 v128, v128, s68, v152\n\t"
    "v_and_b32 v128, -16, v128\n\t"
    "v_mfma_f32_16x16x32_bf16 v[104:107], v[176:179], v[176:179], v[104:107]\n\t"
    "v_add_f32 v154, v144, v145\n\t"
    "buffer_store_dword v154, v146, %12, s69 offen sc1\n\t"
    "v_mul_f32 v144, v92, v84\n\t"
    "v_mfma_f32_16x16x32_bf16 v[108:111], v[212:215], v[164:167], 0\n\t"
    "v_mul_f32 v145, v96, v0\n\t"
    "v_fmac_f32 v144, v97, v1\n\t"
    "v_mfma_f32_16x16x32_bf16 v[108:111], v[216:219], v[168:171], v[108:111]\n\t"
    "v_fmac_f32 v145, v98, v2\n\t"
    "v_fmac_f32 v144, v99, v3\n\t"
    "s_waitcnt lgkmcnt(7)\n\t"
    "v_mfma_f32_16x16x32_bf16 v[112:115], v[164:167], v[180:183], 0\n\t"
    "v_fmac_f32 v145, v100, v4\n\t"
    "v_fmac_f32 v144, v101, v5\n\t"
    "v_mfma_f32_16x16x32_bf16 v[112:115], v[168:171], v[184:187], v[112:115]\n\t"
    "v_fmac_f32 v145, v102, v6\n\t"
    "v_fmac_f32 v144, v103, v7\n\t"
    "v_mfma_f32_16x16x32_bf16 v[116:119], v[172:175], v[180:183], 0\n\t"
    "v_fmac_f32 v145, v104, v8\n\t"
    "v_fmac_f32 v144, v105, v9\n\t"
    "v_mfma_f32_16x16x32_bf16 v[116:119], v[176:179], v[184:187], v[116:119]\n\t"
    "v_fmac_f32 v145, v106, v10\n\t"
    "v_fmac_f32 v144, v107, v11\n\t"
    "ds_read_b128 v[156:159], v128 offset:0\n\t"
    "ds_read_b128 v[160:163], v128 offset:64\n\t"
    "v_mfma_f32_16x16x32_bf16 v[92:95], v[180:183], v[180:183], 0\n\t"
    "v_mad_u32_u24 v129, v129, s68, v151\n\t"
    "v_and_b32 v129, -16, v129\n\t"
    "v_mfma_f32_16x16x32_bf16 v[92:95], v[184:187], v[184:187], v[92:95]\n\t"
    "v_fmac_f32 v145, v108, v85\n\t"
    "v_mad_u32_u24 v130, v130, s68, v151\n\t"
    "v_mfma_f32_16x16x32_bf16 v[96:99], v[212:215], v[172:175], 0\n\t"
    "v_fmac_f32 v144, v112, v12\n\t"
    "v_fmac_f32 v145, v113, v13\n\t"
    "v_mfma_f32_16x16x32_bf16 v[96:99], v[216:219], v[176:179], v[96:99]\n\t"
    "v_fmac_f32 v144, v114, v14\n\t"
    "v_fmac_f32 v145, v115, v15\n\t"
    "s_waitcnt lgkmcnt(7)\n\t"
    "v_mfma_f32_16x16x32_bf16 v[100:103], v[164:167], v[188:191], 0\n\t"
    "v_and_b32 v130, -16, v130\n\t"
    "v_fmac_f32 v144, v116, v16\n\t"
    "v_mfma_f32_16x16x32_bf16 v[100:103], v[168:171], v[192:195], v[100:103]\n\t"
    "v_fmac_f32 v145, v117, v17\n\t"
    "v_fmac_f32 v144, v118, v18\n\t"
    "v_mfma_f32_16x16x32_bf16 v[104:107], v[172:175], v[188:191], 0\n\t"
    "v_fmac_f32 v145, v119, v19\n\t"
    "v_fmac_f32 v144, v92, v20\n\t"
    "v_mfma_f32_16x16x32_bf16 v[104:107], v[176:179], v[192:195], v[104:107]\n\t"
    "v_fmac_f32 v145, v93, v21\n\t"
    "v_fmac_f32 v144, v94, v22\n\t"
    "s_waitcnt lgkmcnt(5)\n\t"
    "v_mfma_f32_16x16x32_bf16 v[108:111], v[164:167], v[196:199], 0\n\t"
    "v_fmac_f32 v145, v95, v23\n\t"
    "v_fmac_f32 v144, v96, v86\n\t"
    "v_mfma_f32_16x16x32_bf16 v[108:111], v[168:171], v[200:203], v[108:111]\n\t"
    "v_mfma_f32_16x16x32_bf16 v[112:115], v[212:215], v[180:183], 0\n\t"
    "v_fmac_f32 v145, v100, v24\n\t"
    "v_fmac_f32 v144, v101, v25\n\t"
    "v_mfma_f32_16x16x32_bf16 v[112:115], v[216:219], v[184:187], v[112:115]\n\t"
    "v_fmac_f32 v145, v102, v26\n\t"
    "v_fmac_f32 v144, v103, v27\n\t"
    "s_waitcnt lgkmcnt(2)\n\t"
    "v_mfma_f32_16x16x32_bf16 v[116:119], v[164:167], v[204:207], 0\n\t"
    "v_readfirstlane_b32 s60, v148\n\t"
    "v_fmac_f32 v145, v104, v28\n\t"
    "v_mfma_f32_16x16x32_bf16 v[116:119], v[168:171], v[208:211], v[116:119]\n\t"
    "v_fmac_f32 v144, v105, v29\n\t"
    "v_fmac_f32 v145, v106, v30\n\t"
    "ds_read_b128 v[164:167], v129 offset:0\n\t"
    "ds_read_b128 v[168:171], v129 offset:64\n\t"
    "v_mfma_f32_16x16x32_bf16 v[92:95], v[172:175], v[196:199], 0\n\t"
    "v_fmac_f32 v144, v107, v31\n\t"
    "v_fmac_f32 v145, v108, v32\n\t"
    "v_mfma_f32_16x16x32_bf16 v[92:95], v[176:179], v[200:203], v[92:95]\n\t"
    "v_fmac_f32 v144, v109, v33\n\t"
    "v_fmac_f32 v145, v110, v34\n\t"
    "s_waitcnt vmcnt(4)\n\t"
    "s_cmp_ge_u32 s55, s64\n\t"
    "s_cbranch_scc1 L_ahk11_%=\n\t"
    "L_ahb11_%=:\n\t"
    "v_mfma_f32_16x16x32_bf16 v[96:99], v[172:175], v[204:207], 0\n\t"
    "v_mad_u32_u24 v132, v132, s68, v151\n\t"
    "v_and_b32 v132, -16, v132\n\t"
    "v_mfma_f32_16x16x32_bf16 v[96:99], v[176:179], v[208:211], v[96:99]\n\t"
    "v_fmac_f32 v144, v111, v35\n\t"
    "v_fmac_f32 v145, v112, v87\n\t"
    "ds_read_b128 v[172:175], v130 offset:0\n\t"
    "ds_read_b128 v[176:179], v130 offset:64\n\t"
    "s_mul_i32 s66, s60, 400\n\t"
    "v_mfma_f32_16x16x32_bf16 v[100:103], v[212:215], v[188:191], 0\n\t"
    "v_fmac_f32 v144, v116, v36\n\t"
    "v_fmac_f32 v145, v117, v37\n\t"
    "v_mfma_f32_16x16x32_bf16 v[100:103], v[216:219], v[192:195], v[100:103]\n\t"
    "v_fmac_f32 v144, v118, v38\n\t"
    "v_fmac_f32 v145, v119, v39\n\t"
    "buffer_load_dwordx3 v[128:130], v153, %0, s66 offen\n\t"
    "v_mfma_f32_16x16x32_bf16 v[104:107], v[180:183], v[188:191], 0\n\t"
    "v_mad_u32_u24 v133, v133, s68, v151\n\t"
    "v_and_b32 v133, -16, v133\n\t"
    "v_mfma_f32_16x16x32_bf16 v[104:107], v[184:187], v[192:195], v[104:107]\n\t"
    "v_fmac_f32 v144, v92, v40\n\t"
    "v_fmac_f32 v145, v93, v41\n\t"
    "v_mfma_f32_16x16x32_bf16 v[108:111], v[180:183], v[196:199], 0\n\t"
    "v_fmac_f32 v144, v94, v42\n\t"
    "v_fmac_f32 v145, v95, v43\n\t"
    "v_mfma_f32_16x16x32_bf16 v[108:111], v[184:187], v[200:203], v[108:111]\n\t"
    "v_fmac_f32 v144, v96, v44\n\t"
    "v_fmac_f32 v145, v97, v45\n\t"
    "v_mfma_f32_16x16x32_bf16 v[112:115], v[180:183], v[204:207], 0\n\t"
    "v_fmac_f32 v144, v98, v46\n\t"
    "v_fmac_f32 v145, v99, v47\n\t"
    "v_mfma_f32_16x16x32_bf16 v[112:115], v[184:187], v[208:211], v[112:115]\n\t"
    "v_fmac_f32 v144, v100, v88\n\t"
    "v_mad_u32_u24 v134, v134, s68, v151\n\t"
    "ds_read_b128 v[180:183], v132 offset:0\n\t"
    "ds_read_b128 v[184:187], v132 offset:64\n\t"
    "v_mfma_f32_16x16x32_bf16 v[116:119], v[212:215], v[196:199], 0\n\t"
    "v_and_b32 v134, -16, v134\n\t"
    "v_fmac_f32 v145, v104, v48\n\t"
    "v_mfma_f32_16x16x32_bf16 v[116:119], v[216:219], v[200:203], v[116:119]\n\t"
    "v_fmac_f32 v144, v105, v49\n\t"
    "v_fmac_f32 v145, v106, v50\n\t"
    "v_mfma_f32_16x16x32_bf16 v[92:95], v[188:191], v[188:191], 0\n\t"
    "v_fmac_f32 v144, v107, v51\n\t"
    "v_mad_u32_u24 v135, v135, s68, v151\n\t"
    "v_mfma_f32_16x16x32_bf16 v[92:95], v[192:195], v[192:195], v[92:95]\n\t"
    "v_and_b32 v135, -16, v135\n\t"
    "v_fmac_f32 v145, v108, v52\n\t"
    "v_mfma_f32_16x16x32_bf16 v[96:99], v[188:191], v[196:199], 0\n\t"
    "v_fmac_f32 v144, v109, v53\n\t"
    "v_fmac_f32 v145, v110, v54\n\t"
    "v_mfma_f32_16x16x32_bf16 v[96:99], v[192:195], v[200:203], v[96:99]\n\t"
    "v_fmac_f32 v144, v111, v55\n\t"
    "v_fmac_f32 v145, v112, v56\n\t"
    "v_mfma_f32_16x16x32_bf16 v[100:103], v[188:191], v[204:207], 0\n\t"
    "v_fmac_f32 v144, v113, v57\n\t"
    "v_fmac_f32 v145, v114, v58\n\t"
    "v_mfma_f32_16x16x32_bf16 v[100:103], v[192:195], v[208:211], v[100:103]\n\t"
    "v_fmac_f32 v144, v115, v59\n\t"
    "v_fmac_f32 v145, v116, v89\n\t"
    "ds_read_b128 v[188:191], v133 offset:0\n\t"
    "ds_read_b128 v[192:195], v133 offset:64\n\t"
    "v_mfma_f32_16x16x32_bf16 v[104:107], v[212:215], v[204:207], 0\n\t"
    "v_fmac_f32 v144, v92, v60\n\t"
    "v_fmac_f32 v145, v93, v61\n\t"
    "v_mfma_f32_16x16x32_bf16 v[104:107], v[216:219], v[208:211], v[104:107]\n\t"
    "v_fmac_f32 v144, v94, v62\n\t"
    "v_fmac_f32 v145, v95, v63\n\t"
    "v_mfma_f32_16x16x32_bf16 v[108:111], v[196:199], v[196:199], 0\n\t"
    "v_fmac_f32 v144, v96, v64\n\t"
    "v_fmac_f32 v145, v97, v65\n\t"
    "v_mfma_f32_16x16x32_bf16 v[108:111], v[200:203], v[200:203], v[108:111]\n\t"
    "v_fmac_f32 v144, v98, v66\n\t"
    "v_fmac_f32 v145, v99, v67\n\t"
    "v_mfma_f32_16x16x32_bf16 v[112:115], v[196:199], v[204:207], 0\n\t"
    "v_fmac_f32 v144, v100, v68\n\t"
    "v_fmac_f32 v145, v101, v69\n\t"
    "v_mfma_f32_16x16x32_bf16 v[112:115], v[200:203], v[208:211], v[112:115]\n\t"
    "v_fmac_f32 v144, v102, v70\n\t"
    "v_fmac_f32 v145, v103, v71\n\t"
    "ds_read_b128 v[196:199], v134 offset:0\n\t"
    "ds_read_b128 v[200:203], v134 offset:64\n\t"
    "v_mfma_f32_16x16x32_bf16 v[116:119], v[204:207], v[204:207], 0\n\t"
    "v_fmac_f32 v144, v104, v90\n\t"
    "v_mfma_f32_16x16x32_bf16 v[116:119], v[208:211], v[208:211], v[116:119]\n\t"
    "ds_read_b128 v[204:207], v135 offset:0\n\t"
    "ds_read_b128 v[208:211], v135 offset:64\n\t"
    "s_mov_b32 s52, s53\n\t"
    "s_mov_b32 s53, s54\n\t"
    "s_mov_b32 s54, s55\n\t"
    "s_mov_b32 s55, s60\n\t"
    "s_cmp_lt_u32 s53, s64\n\t"
    "s_cbranch_scc1 L_apnn_%=\n\t"
    "L_adone_%=:\n\t"
    "s_mul_i32 s69, s52, 256\n\t"
    "s_add_u32 s69, s69, s50\n\t"
    "v_fmac_f32 v145, v108, v72\n\t"
    "v_fmac_f32 v144, v109, v73\n\t"
    "v_fmac_f32 v145, v110, v74\n\t"
    "v_fmac_f32 v144, v111, v75\n\t"
    "v_fmac_f32 v145, v112, v76\n\t"
    "v_fmac_f32 v144, v113, v77\n\t"
    "v_fmac_f32 v145, v114, v78\n\t"
    "v_fmac_f32 v144, v115, v79\n\t"
    "v_fmac_f32 v145, v116, v80\n\t"
    "v_fmac_f32 v144, v117, v81\n\t"
    "v_fmac_f32 v145, v118, v82\n\t"
    "v_fmac_f32 v144, v119, v83\n\t"
    "v_add_f32 v40, v144, v145\n\t"
    "L_aend_%=:\n\t"
    "s_branch L_aend2_%=\n\t"
    "L_ahk00_%=:\n\t"
    "v_mov_b32 v147, 3112\n\t"
    "s_cmp_lt_u32 s54, s64\n\t"
    "ds_read_b32 v147, v147\n\t"
    "s_cbranch_scc1 L_ahb00_%=\n\t"
    "s_waitcnt vmcnt(3)\n\t"
    "v_add_u32 v137, 0, v155\n\t"
    "v_add_u32 v138, 16, v155\n\t"
    "v_add_u32 v140, 32, v155\n\t"
    "v_add_u32 v141, 48, v155\n\t"
    "v_add_u32 v142, 64, v155\n\t"
    "v_add_u32 v143, 80, v155\n\t"
    "s_branch L_ahb00_%=\n\t"
    "L_ahk10_%=:\n\t"
    "s_waitcnt lgkmcnt(4)\n\t"
    "v_readfirstlane_b32 s65, v147\n\t"
    "s_nop 0\n\t"
    "s_cmp_eq_u32 s65, 0\n\t"
    "s_cbranch_scc1 L_ahb10_%=\n\t"
    "s_cmp_lt_u32 s62, 2\n\t"
    "s_cbranch_scc1 L_mel1_%=\n\t"
    "buffer_load_dwordx4 v[220:223], v127, %12, s61 offen offset:2048 sc1\n\t"
    "buffer_load_dwordx4 v[224:227], v127, %12, s61 offen offset:3072 sc1\n\t"
    "s_add_u32 s61, s61, 0x1000\n\t"
    "buffer_load_dwordx4 v[228:231], v127, %12, s61 offen offset:2048 sc1\n\t"
    "buffer_load_dwordx4 v[232:235], v127, %12, s61 offen offset:3072 sc1\n\t"
    "s_add_u32 s61, s61, 0x1000\n\t"
    "buffer_load_dwordx4 v[236:239], v127, %12, s61 offen offset:2048 sc1\n\t"
    "buffer_load_dwordx4 v[240:243], v127, %12, s61 offen offset:3072 sc1\n\t"
    "s_add_u32 s61, s61, 0x1000\n\t"
    "buffer_load_dwordx4 v[244:247], v127, %12, s61 offen offset:2048 sc1\n\t"
    "buffer_load_dwordx4 v[248:251], v127, %12, s61 offen offset:3072 sc1\n\t"
    "s_add_u32 s61, s61, 0x1000\n\t"
    "buffer_load_dwordx4 v[252:255], v127, %12, s61 offen offset:2048 sc1\n\t"
    "s_mov_b32 s62, 1\n\t"
    "L_mel1_%=:\n\t"
    "s_cmp_lt_u32 s54, s64\n\t"
    "s_cbranch_scc1 L_ahb10_%=\n\t"
    "buffer_load_dwordx4 v[0:3], v127, %12, s63 offen offset:0 sc1\n\t"
    "buffer_load_dwordx4 v[4:7], v127, %12, s63 offen offset:1024 sc1\n\t"
    "buffer_load_dwordx4 v[8:11], v127, %12, s63 offen offset:2048 sc1\n\t"
    "buffer_load_dwordx4 v[12:15], v127, %12, s63 offen offset:3072 sc1\n\t"
    "s_add_u32 s63, s63, 0x1000\n\t"
    "s_mov_b32 s62, 0\n\t"
    "s_branch L_ahb10_%=\n\t"
    "L_ahk01_%=:\n\t"
    "v_mov_b32 v147, 3112\n\t"
    "s_cmp_lt_u32 s54, s64\n\t"
    "ds_read_b32 v147, v147\n\t"
    "s_cbranch_scc1 L_ahb01_%=\n\t"
    "s_waitcnt vmcnt(3)\n\t"
    "v_add_u32 v129, 0, v155\n\t"
    "v_add_u32 v130, 16, v155\n\t"
    "v_add_u32 v132, 32, v155\n\t"
    "v_add_u32 v133, 48, v155\n\t"
    "v_add_u32 v134, 64, v155\n\t"
    "v_add_u32 v135, 80, v155\n\t"
    "s_branch L_ahb01_%=\n\t"
    "L_ahk11_%=:\n\t"
    "s_waitcnt lgkmcnt(4)\n\t"
    "v_readfirstlane_b32 s65, v147\n\t"
    "s_nop 0\n\t"
    "s_cmp_eq_u32 s65, 0\n\t"
    "s_cbranch_scc1 L_ahb11_%=\n\t"
    "s_cmp_lt_u32 s62, 2\n\t"
    "s_cbranch_scc1 L_mel3_%=\n\t"
    "buffer_load_dwordx4 v[220:223], v127, %12, s61 offen offset:2048 sc1\n\t"
    "buffer_load_dwordx4 v[224:227], v127, %12, s61 offen offset:3072 sc1\n\t"
    "s_add_u32 s61, s61, 0x1000\n\t"
    "buffer_load_dwordx4 v[228:231], v127, %12, s61 offen offset:2048 sc1\n\t"
    "buffer_load_dwordx4 v[232:235], v127, %12, s61 offen offset:3072 sc1\n\t"
    "s_add_u32 s61, s61, 0x1000\n\t"
    "buffer_load_dwordx4 v[236:239], v127, %12, s61 offen offset:2048 sc1\n\t"
    "buffer_load_dwordx4 v[240:243], v127, %12, s61 offen offset:3072 sc1\n\t"
    "s_add_u32 s61, s61, 0x1000\n\t"
    "buffer_load_dwordx4 v[244:247], v127, %12, s61 offen offset:2048 sc1\n\t"
    "buffer_load_dwordx4 v[248:251], v127, %12, s61 offen offset:3072 sc1\n\t"
    "s_add_u32 s61, s61, 0x1000\n\t"
    "buffer_load_dwordx4 v[252:255], v127, %12, s61 offen offset:2048 sc1\n\t"
    "s_mov_b32 s62, 1\n\t"
    "L_mel3_%=:\n\t"
    "s_cmp_lt_u32 s54, s64\n\t"
    "s_cbranch_scc1 L_ahb11_%=\n\t"
    "buffer_load_dwordx4 v[0:3], v127, %12, s63 offen offset:0 sc1\n\t"
    "buffer_load_dwordx4 v[4:7], v127, %12, s63 offen offset:1024 sc1\n\t"
    "buffer_load_dwordx4 v[8:11], v127, %12, s63 offen offset:2048 sc1\n\t"
    "buffer_load_dwordx4 v[12:15], v127, %12, s63 offen offset:3072 sc1\n\t"
    "s_add_u32 s63, s63, 0x1000\n\t"
    "s_mov_b32 s62, 0\n\t"
    "s_branch L_ahb11_%=\n\t"
    "L_aend2_%=:\n\t"
    "s_setprio 0\n\t"
    "v_lshlrev_b32 v113, 16, v127\n\t"
    "v_add_u32 v113, 3108, v113\n\t"
    "s_cmp_eq_u32 s62, 0\n\t"
    "s_cbranch_scc1 L_mpf_%=\n\t"
    "L_ml0_%=:\n\t"
    "ds_read_b32 v114, v113 offset:4\n\t"
    "s_waitcnt lgkmcnt(0)\n\t"
    "v_readfirstlane_b32 s65, v114\n\t"
    "s_nop 0\n\t"
    "s_cmp_lg_u32 s65, 0\n\t"
    "s_cbranch_scc1 L_mm0_%=\n\t"
    "s_sleep 2\n\t"
    "s_branch L_ml0_%=\n\t"
    "L_mm0_%=:\n\t"
    "s_cmp_lt_u32 s62, 2\n\t"
    "s_cbranch_scc1 L_mel5_%=\n\t"
    "buffer_load_dwordx4 v[220:223], v127, %12, s61 offen offset:2048 sc1\n\t"
    "buffer_load_dwordx4 v[224:227], v127, %12, s61 offen offset:3072 sc1\n\t"
    "s_add_u32 s61, s61, 0x1000\n\t"
    "buffer_load_dwordx4 v[228:231], v127, %12, s61 offen offset:2048 sc1\n\t"
    "buffer_load_dwordx4 v[232:235], v127, %12, s61 offen offset:3072 sc1\n\t"
    "s_add_u32 s61, s61, 0x1000\n\t"
    "buffer_load_dwordx4 v[236:239], v127, %12, s61 offen offset:2048 sc1\n\t"
    "buffer_load_dwordx4 v[240:243], v127, %12, s61 offen offset:3072 sc1\n\t"
    "s_add_u32 s61, s61, 0x1000\n\t"
    "buffer_load_dwordx4 v[244:247], v127, %12, s61 offen offset:2048 sc1\n\t"
    "buffer_load_dwordx4 v[248:251], v127, %12, s61 offen offset:3072 sc1\n\t"
    "s_add_u32 s61, s61, 0x1000\n\t"
    "buffer_load_dwordx4 v[252:255], v127, %12, s61 offen offset:2048 sc1\n\t"
    "s_mov_b32 s62, 1\n\t"
    "L_mel5_%=:\n\t"
    "buffer_load_dwordx4 v[0:3], v127, %12, s63 offen offset:0 sc1\n\t"
    "buffer_load_dwordx4 v[4:7], v127, %12, s63 offen offset:1024 sc1\n\t"
    "buffer_load_dwordx4 v[8:11], v127, %12, s63 offen offset:2048 sc1\n\t"
    "buffer_load_dwordx4 v[12:15], v127, %12, s63 offen offset:3072 sc1\n\t"
    "s_add_u32 s63, s63, 0x1000\n\t"
    "s_mov_b32 s62, 0\n\t"
    "L_mpf_%=:\n\t"
    "v_lshrrev_b32 v105, 4, v127\n\t"
    "v_and_b32 v105, 48, v105\n\t"
    "s_movk_i32 s65, 130\n\t"
    "v_mad_u32_u24 v104, v155, s65, v105\n\t"
    "v_add_u32 v105, 14170, v104\n\t"
    "v_and_b32 v104, -16, v104\n\t"
    "v_and_b32 v105, -16, v105\n\t"
    "buffer_load_dwordx4 v[128:131], v127, %12, s61 offen offset:3072 sc1\n\t"
    "s_add_u32 s61, s61, 0x1000\n\t"
    "buffer_load_dwordx4 v[132:135], v127, %12, s61 offen offset:2048 sc1\n\t"
    "ds_read_b128 v[156:159], v104 offset:12480\n\t"
    "ds_read_b128 v[160:163], v104 offset:12544\n\t"
    "buffer_load_dwordx4 v[136:139], v127, %12, s61 offen offset:3072 sc1\n\t"
    "s_add_u32 s61, s61, 0x1000\n\t"
    "buffer_load_dwordx4 v[140:143], v127, %12, s61 offen offset:2048 sc1\n\t"
    "ds_read_b128 v[212:215], v105 offset:0\n\t"
    "ds_read_b128 v[216:219], v105 offset:64\n\t"
    "buffer_load_dwordx4 v[144:147], v127, %12, s61 offen offset:3072 sc1\n\t"
    "s_add_u32 s61, s61, 0x1000\n\t"
    "buffer_load_dwordx4 v[148:151], v127, %12, s61 offen offset:2048 sc1\n\t"
    "buffer_load_dwordx4 v[152:155], v127, %12, s61 offen offset:3072 sc1\n\t"
    "s_add_u32 s61, s61, 0x1000\n\t"
    "v_mov_b32 v114, 1\n\t"
    "ds_add_u32 v113, v114\n\t"
    "s_waitcnt lgkmcnt(5)\n\t"
    "s_waitcnt vmcnt(7)\n\t"
    "v_mfma_f32_16x16x32_bf16 v[16:19], v[0:3], v[164:167], 0\n\t"
    "v_mfma_f32_16x16x32_bf16 v[16:19], v[4:7], v[168:171], v[16:19]\n\t"
    "v_mfma_f32_16x16x32_bf16 v[16:19], v[8:11], v[220:223], v[16:19]\n\t"
    "v_mfma_f32_16x16x32_bf16 v[16:19], v[12:15], v[224:227], v[16:19]\n\t"
    "v_mfma_f32_16x16x32_bf16 v[20:23], v[0:3], v[172:175], 0\n\t"
    "v_mfma_f32_16x16x32_bf16 v[20:23], v[4:7], v[176:179], v[20:23]\n\t"
    "v_mfma_f32_16x16x32_bf16 v[20:23], v[8:11], v[228:231], v[20:23]\n\t"
    "v_mfma_f32_16x16x32_bf16 v[20:23], v[12:15], v[232:235], v[20:23]\n\t"
    "v_mfma_f32_16x16x32_bf16 v[80:83], v[0:3], v[180:183], 0\n\t"
    "v_mfma_f32_16x16x32_bf16 v[80:83], v[4:7], v[184:187], v[80:83]\n\t"
    "v_mfma_f32_16x16x32_bf16 v[80:83], v[8:11], v[236:239], v[80:83]\n\t"
    "v_mfma_f32_16x16x32_bf16 v[80:83], v[12:15], v[240:243], v[80:83]\n\t"
    "v_cvt_pk_bf16_f32 v24, v16, v17\n\t"
    "v_cvt_pk_bf16_f32 v25, v18, v19\n\t"
    "v_mfma_f32_16x16x32_bf16 v[16:19], v[0:3], v[188:191], 0\n\t"
    "v_mfma_f32_16x16x32_bf16 v[16:19], v[4:7], v[192:195], v[16:19]\n\t"
    "v_mfma_f32_16x16x32_bf16 v[16:19], v[8:11], v[244:247], v[16:19]\n\t"
    "v_mfma_f32_16x16x32_bf16 v[16:19], v[12:15], v[248:251], v[16:19]\n\t"
    "v_cvt_pk_bf16_f32 v26, v20, v21\n\t"
    "v_cvt_pk_bf16_f32 v27, v22, v23\n\t"
    "s_waitcnt vmcnt(6)\n\t"
    "v_mfma_f32_16x16x32_bf16 v[20:23], v[0:3], v[196:199], 0\n\t"
    "v_mfma_f32_16x16x32_bf16 v[20:23], v[4:7], v[200:203], v[20:23]\n\t"
    "v_mfma_f32_16x16x32_bf16 v[20:23], v[8:11], v[252:255], v[20:23]\n\t"
    "v_mfma_f32_16x16x32_bf16 v[20:23], v[12:15], v[128:131], v[20:23]\n\t"
    "v_cvt_pk_bf16_f32 v28, v80, v81\n\t"
    "v_cvt_pk_bf16_f32 v29, v82, v83\n\t"
    "s_waitcnt lgkmcnt(0)\n\t"
    "s_waitcnt vmcnt(4)\n\t"
    "v_mfma_f32_16x16x32_bf16 v[80:83], v[0:3], v[204:207], 0\n\t"
    "v_mfma_f32_16x16x32_bf16 v[80:83], v[4:7], v[208:211], v[80:83]\n\t"
    "v_mfma_f32_16x16x32_bf16 v[80:83], v[8:11], v[132:135], v[80:83]\n\t"
    "v_mfma_f32_16x16x32_bf16 v[80:83], v[12:15], v[136:139], v[80:83]\n\t"
    "v_cvt_pk_bf16_f32 v30, v16, v17\n\t"
    "v_cvt_pk_bf16_f32 v31, v18, v19\n\t"
    "s_waitcnt vmcnt(2)\n\t"
    "v_mfma_f32_16x16x32_bf16 v[16:19], v[0:3], v[156:159], 0\n\t"
    "buffer_load_dwordx4 v[100:103], v127, %12, s84 offen offset:0 sc1\n\t"
    "s_add_u32 s84, s84, s97\n\t"
    "v_mfma_f32_16x16x32_bf16 v[16:19], v[4:7], v[160:163], v[16:19]\n\t"
    "buffer_load_dwordx4 v[104:107], v127, %12, s85 offen offset:0 sc1\n\t"
    "s_add_u32 s85, s85, s97\n\t"
    "v_mfma_f32_16x16x32_bf16 v[16:19], v[8:11], v[140:143], v[16:19]\n\t"
    "v_mfma_f32_16x16x32_bf16 v[16:19], v[12:15], v[144:147], v[16:19]\n\t"
    "v_cvt_pk_bf16_f32 v32, v20, v21\n\t"
    "v_cvt_pk_bf16_f32 v33, v22, v23\n\t"
    "s_waitcnt vmcnt(2)\n\t"
    "v_mfma_f32_16x16x32_bf16 v[20:23], v[0:3], v[212:215], 0\n\t"
    "buffer_load_dwordx4 v[108:111], v127, %12, s86 offen offset:0 sc1\n\t"
    "s_add_u32 s86, s86, s97\n\t"
    "v_mfma_f32_16x16x32_bf16 v[20:23], v[4:7], v[216:219], v[20:23]\n\t"
    "buffer_load_dwordx4 v[112:115], v127, %12, s87 offen offset:0 sc1\n\t"
    "s_add_u32 s87, s87, s97\n\t"
    "v_mfma_f32_16x16x32_bf16 v[20:23], v[8:11], v[148:151], v[20:23]\n\t"
    "v_mfma_f32_16x16x32_bf16 v[20:23], v[12:15], v[152:155], v[20:23]\n\t"
    "v_cvt_pk_bf16_f32 v34, v80, v81\n\t"
    "v_cvt_pk_bf16_f32 v35, v82, v83\n\t"
    "v_cvt_pk_bf16_f32 v36, v16, v17\n\t"
    "v_cvt_pk_bf16_f32 v37, v18, v19\n\t"
    "s_nop 3\n\t"
    "v_cvt_pk_bf16_f32 v38, v20, v21\n\t"
    "v_cvt_pk_bf16_f32 v39, v22, v23\n\t"
    "v_lshrrev_b32 v41, 2, v127\n\t"
    "s_and_b32 s65, %14, 8\n\t"
    "s_lshl_b32 s65, s65, 10\n\t"
    "s_add_u32 s63, s63, s65\n\t"
    "buffer_store_dword v40, v41, %12, s69 offen sc1\n\t"
    "v_mov_b32 v82, 3108\n\t"
    "s_cmp_eq_u32 %16, 0\n\t"
    "s_cselect_b32 s47, 0x1ff, 0\n\t"
    "L_my_%=:\n\t"
    "ds_read_b32 v81, v82\n\t"
    "s_waitcnt lgkmcnt(0)\n\t"
    "v_cmp_gt_u32 vcc, 8, v81\n\t"
    "s_cbranch_vccz L_mz_%=\n\t"
    "s_sleep 2\n\t"
    "s_branch L_my_%=\n\t"
    "L_mz_%=:\n\t"
    "ds_write_b64 v125, v[24:25] offset:0\n\t"
    "v_mov_b64 v[0:1], 0\n\t"
    "ds_write_b64 v125, v[26:27] offset:128\n\t"
    "v_mov_b64 v[2:3], 0\n\t"
    "ds_write_b64 v125, v[28:29] offset:256\n\t"
    "v_mov_b64 v[4:5], 0\n\t"
    "ds_write_b64 v125, v[30:31] offset:384\n\t"
    "v_mov_b64 v[6:7], 0\n\t"
    "ds_write_b64 v125, v[32:33] offset:512\n\t"
    "ds_write_b64 v125, v[34:35] offset:640\n\t"
    "ds_write_b64 v125, v[36:37] offset:768\n\t"
    "ds_write_b64 v125, v[38:39] offset:872\n\t"
    "v_add_u32 v125, s93, v125\n\t"
    "s_sub_u32 s93, 0, s93\n\t"
    "s_waitcnt lgkmcnt(0)\n\t"
    "s_barrier\n\t"
    "s_memrealtime s[44:45]\n\t"
    "s_waitcnt lgkmcnt(0)\n\t"
    "s_waitcnt vmcnt(4)\n\t"
    "v_add_u32_sdwa v126, s92, v100 dst_sel:DWORD dst_unused:UNUSED_PAD src0_sel:DWORD src1_sel:WORD_1\n\t"
    "v_add_u32_sdwa v100, s92, v100 dst_sel:DWORD dst_unused:UNUSED_PAD src0_sel:DWORD src1_sel:WORD_0\n\t"
    "ds_read_b64 v[84:85], v100 offset:0\n\t"
    "ds_read_b64 v[86:87], v126 offset:0\n\t"
    "v_add_u32_sdwa v90, s92, v101 dst_sel:DWORD dst_unused:UNUSED_PAD src0_sel:DWORD src1_sel:WORD_1\n\t"
    "v_add_u32_sdwa v101, s92, v101 dst_sel:DWORD dst_unused:UNUSED_PAD src0_sel:DWORD src1_sel:WORD_0\n\t"
    "ds_read_b64 v[88:89], v101 offset:0\n\t"
    "ds_read_b64 v[90:91], v90 offset:0\n\t"
    "v_add_u32 v63, s96, v127\n\t"
    "s_add_u32 s96, s96, 0x1000\n\t"
    "s_and_b32 s96, s96, 0x3000\n\t"
    "v_add_u32 v63, 128000, v63\n\t"
    "ds_read_b128 v[48:51], v63 offset:0\n\t"
    "ds_read_b128 v[52:55], v63 offset:1024\n\t"
    "ds_read_b128 v[56:59], v63 offset:2048\n\t"
    "ds_read_b128 v[60:63], v63 offset:3072\n\t"
    "v_add_u32_sdwa v126, s92, v102 dst_sel:DWORD dst_unused:UNUSED_PAD src0_sel:DWORD src1_sel:WORD_1\n\t"
    "v_add_u32_sdwa v102, s92, v102 dst_sel:DWORD dst_unused:UNUSED_PAD src0_sel:DWORD src1_sel:WORD_0\n\t"
    "ds_read_b64 v[92:93], v102 offset:0\n\t"
    "ds_read_b64 v[94:95], v126 offset:0\n\t"
    "v_add_u32_sdwa v98, s92, v103 dst_sel:DWORD dst_unused:UNUSED_PAD src0_sel:DWORD src1_sel:WORD_1\n\t"
    "v_add_u32_sdwa v103, s92, v103 dst_sel:DWORD dst_unused:UNUSED_PAD src0_sel:DWORD src1_sel:WORD_0\n\t"
    "ds_read_b64 v[96:97], v103 offset:0\n\t"
    "ds_read_b64 v[98:99], v98 offset:0\n\t"
    "buffer_load_dwordx4 v[100:103], v127, %12, s88 offen offset:0 sc1\n\t"
    "s_add_u32 s88, s88, s97\n\t"
    "s_waitcnt vmcnt(4)\n\t"
    "v_add_u32_sdwa v126, s92, v104 dst_sel:DWORD dst_unused:UNUSED_PAD src0_sel:DWORD src1_sel:WORD_1\n\t"
    "v_add_u32_sdwa v104, s92, v104 dst_sel:DWORD dst_unused:UNUSED_PAD src0_sel:DWORD src1_sel:WORD_0\n\t"
    "v_mov_b64 v[8:9], 0\n\t"
    "v_mov_b64 v[10:11], 0\n\t"
    "v_mov_b64 v[12:13], 0\n\t"
    "v_mov_b64 v[14:15], 0\n\t"
    "v_mov_b64 v[16:17], 0\n\t"
    "v_mov_b64 v[18:19], 0\n\t"
    "s_waitcnt lgkmcnt(8)\n\t"
    "v_smfmac_f32_16x16x64_bf16 v[0:3], v[120:123], v[84:91], v124\n\t"
    "ds_read_b64 v[84:85], v104 offset:0\n\t"
    "s_waitcnt lgkmcnt(5)\n\t"
    "v_mfma_f32_16x16x32_bf16 v[116:119], v[48:51], v[164:167], 0\n\t"
    "ds_read_b64 v[86:87], v126 offset:0\n\t"
    "v_mfma_f32_16x16x32_bf16 v[116:119], v[52:55], v[168:171], v[116:119]\n\t"
    "v_add_u32_sdwa v90, s92, v105 dst_sel:DWORD dst_unused:UNUSED_PAD src0_sel:DWORD src1_sel:WORD_1\n\t"
    "v_add_u32_sdwa v105, s92, v105 dst_sel:DWORD dst_unused:UNUSED_PAD src0_sel:DWORD src1_sel:WORD_0\n\t"
    "ds_read_b64 v[88:89], v105 offset:0\n\t"
    "v_mfma_f32_16x16x32_bf16 v[116:119], v[56:59], v[220:223], v[116:119]\n\t"
    "ds_read_b64 v[90:91], v90 offset:0\n\t"
    "v_mfma_f32_16x16x32_bf16 v[116:119], v[60:63], v[224:227], v[116:119]\n\t"
    "v_add_u32_sdwa v126, s92, v106 dst_sel:DWORD dst_unused:UNUSED_PAD src0_sel:DWORD src1_sel:WORD_1\n\t"
    "v_add_u32_sdwa v106, s92, v106 dst_sel:DWORD dst_unused:UNUSED_PAD src0_sel:DWORD src1_sel:WORD_0\n\t"
    "v_mfma_f32_16x16x32_bf16 v[80:83], v[48:51], v[172:175], 0\n\t"
    "v_mov_b64 v[20:21], 0\n\t"
    "v_mov_b64 v[22:23], 0\n\t"
    "v_mov_b64 v[24:25], 0\n\t"
    "v_mov_b64 v[26:27], 0\n\t"
    "v_mov_b64 v[28:29], 0\n\t"
    "v_mov_b64 v[30:31], 0\n\t"
    "s_waitcnt vmcnt(3) lgkmcnt(4)\n\t"
    "v_smfmac_f32_16x16x64_bf16 v[4:7], v[120:123], v[92:99], v124\n\t"
    "ds_read_b64 v[92:93], v106 offset:0\n\t"
    "v_mfma_f32_16x16x32_bf16 v[80:83], v[52:55], v[176:179], v[80:83]\n\t"
    "ds_read_b64 v[94:95], v126 offset:0\n\t"
    "v_mfma_f32_16x16x32_bf16 v[80:83], v[56:59], v[228:231], v[80:83]\n\t"
    "v_add_u32_sdwa v98, s92, v107 dst_sel:DWORD dst_unused:UNUSED_PAD src0_sel:DWORD src1_sel:WORD_1\n\t"
    "v_add_u32_sdwa v107, s92, v107 dst_sel:DWORD dst_unused:UNUSED_PAD src0_sel:DWORD src1_sel:WORD_0\n\t"
    "ds_read_b64 v[96:97], v107 offset:0\n\t"
    "v_mfma_f32_16x16x32_bf16 v[80:83], v[60:63], v[232:235], v[80:83]\n\t"
    "ds_read_b64 v[98:99], v98 offset:0\n\t"
    "v_cvt_pk_bf16_f32 v116, v116, v117\n\t"
    "v_cvt_pk_bf16_f32 v117, v118, v119\n\t"
    "ds_write_b64 v125, v[116:117] offset:0\n\t"
    "v_mfma_f32_16x16x32_bf16 v[116:119], v[48:51], v[180:183], 0\n\t"
    "buffer_load_dwordx4 v[104:107], v127, %12, s89 offen offset:0 sc1\n\t"
    "s_add_u32 s89, s89, s97\n\t"
    "v_add_u32_sdwa v126, s92, v108 dst_sel:DWORD dst_unused:UNUSED_PAD src0_sel:DWORD src1_sel:WORD_1\n\t"
    "v_add_u32_sdwa v108, s92, v108 dst_sel:DWORD dst_unused:UNUSED_PAD src0_sel:DWORD src1_sel:WORD_0\n\t"
    "v_mfma_f32_16x16x32_bf16 v[116:119], v[52:55], v[184:187], v[116:119]\n\t"
    "v_mov_b64 v[32:33], 0\n\t"
    "v_mov_b64 v[34:35], 0\n\t"
    "v_mov_b64 v[36:37], 0\n\t"
    "v_mov_b64 v[38:39], 0\n\t"
    "v_mov_b64 v[40:41], 0\n\t"
    "v_mov_b64 v[42:43], 0\n\t"
    "v_mov_b64 v[44:45], 0\n\t"
    "v_mov_b64 v[46:47], 0\n\t"
    "s_waitcnt lgkmcnt(5)\n\t"
    "v_smfmac_f32_16x16x64_bf16 v[8:11], v[120:123], v[84:91], v124\n\t"
    "ds_read_b64 v[84:85], v108 offset:0\n\t"
    "v_mfma_f32_16x16x32_bf16 v[116:119], v[56:59], v[236:239], v[116:119]\n\t"
    "ds_read_b64 v[86:87], v126 offset:0\n\t"
    "v_mfma_f32_16x16x32_bf16 v[116:119], v[60:63], v[240:243], v[116:119]\n\t"
    "v_add_u32_sdwa v90, s92, v109 dst_sel:DWORD dst_unused:UNUSED_PAD src0_sel:DWORD src1_sel:WORD_1\n\t"
    "v_add_u32_sdwa v109, s92, v109 dst_sel:DWORD dst_unused:UNUSED_PAD src0_sel:DWORD src1_sel:WORD_0\n\t"
    "ds_read_b64 v[88:89], v109 offset:0\n\t"
    "v_cvt_pk_bf16_f32 v80, v80, v81\n\t"
    "v_cvt_pk_bf16_f32 v81, v82, v83\n\t"
    "ds_write_b64 v125, v[80:81] offset:128\n\t"
    "v_mfma_f32_16x16x32_bf16 v[80:83], v[48:51], v[188:191], 0\n\t"
    "ds_read_b64 v[90:91], v90 offset:0\n\t"
    "v_mfma_f32_16x16x32_bf16 v[80:83], v[52:55], v[192:195], v[80:83]\n\t"
    "v_add_u32_sdwa v126, s92, v110 dst_sel:DWORD dst_unused:UNUSED_PAD src0_sel:DWORD src1_sel:WORD_1\n\t"
    "v_add_u32_sdwa v110, s92, v110 dst_sel:DWORD dst_unused:UNUSED_PAD src0_sel:DWORD src1_sel:WORD_0\n\t"
    "v_mfma_f32_16x16x32_bf16 v[80:83], v[56:59], v[244:247], v[80:83]\n\t"
    "s_waitcnt vmcnt(3) lgkmcnt(6)\n\t"
    "v_smfmac_f32_16x16x64_bf16 v[12:15], v[120:123], v[92:99], v124\n\t"
    "ds_read_b64 v[92:93], v110 offset:0\n\t"
    "v_mfma_f32_16x16x32_bf16 v[80:83], v[60:63], v[248:251], v[80:83]\n\t"
    "ds_read_b64 v[94:95], v126 offset:0\n\t"
    "v_cvt_pk_bf16_f32 v116, v116, v117\n\t"
    "v_cvt_pk_bf16_f32 v117, v118, v119\n\t"
    "ds_write_b64 v125, v[116:117] offset:256\n\t"
    "v_mfma_f32_16x16x32_bf16 v[116:119], v[48:51], v[196:199], 0\n\t"
    "v_add_u32_sdwa v98, s92, v111 dst_sel:DWORD dst_unused:UNUSED_PAD src0_sel:DWORD src1_sel:WORD_1\n\t"
    "v_add_u32_sdwa v111, s92, v111 dst_sel:DWORD dst_unused:UNUSED_PAD src0_sel:DWORD src1_sel:WORD_0\n\t"
    "ds_read_b64 v[96:97], v111 offset:0\n\t"
    "v_mfma_f32_16x16x32_bf16 v[116:119], v[52:55], v[200:203], v[116:119]\n\t"
    "ds_read_b64 v[98:99], v98 offset:0\n\t"
    "v_mfma_f32_16x16x32_bf16 v[116:119], v[56:59], v[252:255], v[116:119]\n\t"
    "buffer_load_dwordx4 v[108:111], v127, %12, s90 offen offset:0 sc1\n\t"
    "s_add_u32 s90, s90, s97\n\t"
    "v_add_u32_sdwa v126, s92, v112 dst_sel:DWORD dst_unused:UNUSED_PAD src0_sel:DWORD src1_sel:WORD_1\n\t"
    "v_add_u32_sdwa v112, s92, v112 dst_sel:DWORD dst_unused:UNUSED_PAD src0_sel:DWORD src1_sel:WORD_0\n\t"
    "v_mfma_f32_16x16x32_bf16 v[116:119], v[60:63], v[128:131], v[116:119]\n\t"
    "v_cvt_pk_bf16_f32 v0, v0, v1\n\t"
    "v_cvt_pk_bf16_f32 v1, v2, v3\n\t"
    "v_cvt_pk_bf16_f32 v80, v80, v81\n\t"
    "v_cvt_pk_bf16_f32 v81, v82, v83\n\t"
    "ds_write_b64 v125, v[80:81] offset:384\n\t"
    "v_add_u32 v79, s96, v127\n\t"
    "s_add_u32 s96, s96, 0x1000\n\t"
    "s_and_b32 s96, s96, 0x3000\n\t"
    "v_add_u32 v79, 128000, v79\n\t"
    "ds_read_b128 v[64:67], v79 offset:0\n\t"
    "ds_read_b128 v[68:71], v79 offset:1024\n\t"
    "ds_read_b128 v[72:75], v79 offset:2048\n\t"
    "ds_read_b128 v[76:79], v79 offset:3072\n\t"
    "s_waitcnt lgkmcnt(10)\n\t"
    "v_smfmac_f32_16x16x64_bf16 v[16:19], v[120:123], v[84:91], v124\n\t"
    "ds_read_b64 v[84:85], v112 offset:0\n\t"
    "v_mfma_f32_16x16x32_bf16 v[80:83], v[48:51], v[204:207], 0\n\t"
    "ds_read_b64 v[86:87], v126 offset:0\n\t"
    "v_mfma_f32_16x16x32_bf16 v[80:83], v[52:55], v[208:211], v[80:83]\n\t"
    "v_add_u32_sdwa v90, s92, v113 dst_sel:DWORD dst_unused:UNUSED_PAD src0_sel:DWORD src1_sel:WORD_1\n\t"
    "v_add_u32_sdwa v113, s92, v113 dst_sel:DWORD dst_unused:UNUSED_PAD src0_sel:DWORD src1_sel:WORD_0\n\t"
    "ds_read_b64 v[88:89], v113 offset:0\n\t"
    "v_mfma_f32_16x16x32_bf16 v[80:83], v[56:59], v[132:135], v[80:83]\n\t"
    "ds_read_b64 v[90:91], v90 offset:0\n\t"
    "v_mfma_f32_16x16x32_bf16 v[80:83], v[60:63], v[136:139], v[80:83]\n\t"
    "v_add_u32_sdwa v126, s92, v114 dst_sel:DWORD dst_unused:UNUSED_PAD src0_sel:DWORD src1_sel:WORD_1\n\t"
    "v_add_u32_sdwa v114, s92, v114 dst_sel:DWORD dst_unused:UNUSED_PAD src0_sel:DWORD src1_sel:WORD_0\n\t"
    "v_cvt_pk_bf16_f32 v116, v116, v117\n\t"
    "v_cvt_pk_bf16_f32 v117, v118, v119\n\t"
    "ds_write_b64 v125, v[116:117] offset:512\n\t"
    "v_mfma_f32_16x16x32_bf16 v[116:119], v[48:51], v[156:159], 0\n\t"
    "v_cvt_pk_bf16_f32 v2, v4, v5\n\t"
    "v_cvt_pk_bf16_f32 v3, v6, v7\n\t"
    "v_mov_b64 v[4:5], 0\n\t"
    "v_mov_b64 v[6:7], 0\n\t"
    "s_waitcnt vmcnt(2) lgkmcnt(10)\n\t"
    "v_smfmac_f32_16x16x64_bf16 v[20:23], v[120:123], v[92:99], v124\n\t"
    "ds_read_b64 v[92:93], v114 offset:0\n\t"
    "v_mfma_f32_16x16x32_bf16 v[116:119], v[52:55], v[160:163], v[116:119]\n\t"
    "ds_read_b64 v[94:95], v126 offset:0\n\t"
    "v_mfma_f32_16x16x32_bf16 v[116:119], v[56:59], v[140:143], v[116:119]\n\t"
    "v_add_u32_sdwa v98, s92, v115 dst_sel:DWORD dst_unused:UNUSED_PAD src0_sel:DWORD src1_sel:WORD_1\n\t"
    "v_add_u32_sdwa v115, s92, v115 dst_sel:DWORD dst_unused:UNUSED_PAD src0_sel:DWORD src1_sel:WORD_0\n\t"
    "ds_read_b64 v[96:97], v115 offset:0\n\t"
    "v_mfma_f32_16x16x32_bf16 v[116:119], v[60:63], v[144:147], v[116:119]\n\t"
    "ds_read_b64 v[98:99], v98 offset:0\n\t"
    "v_cvt_pk_bf16_f32 v80, v80, v81\n\t"
    "v_cvt_pk_bf16_f32 v81, v82, v83\n\t"
    "ds_write_b64 v125, v[80:81] offset:640\n\t"
    "v_mfma_f32_16x16x32_bf16 v[80:83], v[48:51], v[212:215], 0\n\t"
    "buffer_load_dwordx4 v[112:115], v127, %12, s91 offen offset:0 sc1\n\t"
    "s_add_u32 s91, s91, s97\n\t"
    "v_add_u32_sdwa v126, s92, v100 dst_sel:DWORD dst_unused:UNUSED_PAD src0_sel:DWORD src1_sel:WORD_1\n\t"
    "v_add_u32_sdwa v100, s92, v100 dst_sel:DWORD dst_unused:UNUSED_PAD src0_sel:DWORD src1_sel:WORD_0\n\t"
    "v_mfma_f32_16x16x32_bf16 v[80:83], v[52:55], v[216:219], v[80:83]\n\t"
    "v_cvt_pk_bf16_f32 v8, v8, v9\n\t"
    "v_cvt_pk_bf16_f32 v9, v10, v11\n\t"
    "s_waitcnt lgkmcnt(6)\n\t"
    "v_smfmac_f32_16x16x64_bf16 v[24:27], v[120:123], v[84:91], v124\n\t"
    "ds_read_b64 v[84:85], v100 offset:0\n\t"
    "v_mfma_f32_16x16x32_bf16 v[80:83], v[56:59], v[148:151], v[80:83]\n\t"
    "ds_read_b64 v[86:87], v126 offset:0\n\t"
    "v_mfma_f32_16x16x32_bf16 v[80:83], v[60:63], v[152:155], v[80:83]\n\t"
    "v_add_u32_sdwa v90, s92, v101 dst_sel:DWORD dst_unused:UNUSED_PAD src0_sel:DWORD src1_sel:WORD_1\n\t"
    "v_add_u32_sdwa v101, s92, v101 dst_sel:DWORD dst_unused:UNUSED_PAD src0_sel:DWORD src1_sel:WORD_0\n\t"
    "ds_read_b64 v[88:89], v101 offset:0\n\t"
    "v_cvt_pk_bf16_f32 v116, v116, v117\n\t"
    "v_cvt_pk_bf16_f32 v117, v118, v119\n\t"
    "ds_write_b64 v125, v[116:117] offset:768\n\t"
    "v_mfma_f32_16x16x32_bf16 v[116:119], v[64:67], v[164:167], 0\n\t"
    "ds_read_b64 v[90:91], v90 offset:0\n\t"
    "v_mfma_f32_16x16x32_bf16 v[116:119], v[68:71], v[168:171], v[116:119]\n\t"
    "v_add_u32_sdwa v126, s92, v102 dst_sel:DWORD dst_unused:UNUSED_PAD src0_sel:DWORD src1_sel:WORD_1\n\t"
    "v_add_u32_sdwa v102, s92, v102 dst_sel:DWORD dst_unused:UNUSED_PAD src0_sel:DWORD src1_sel:WORD_0\n\t"
    "v_mfma_f32_16x16x32_bf16 v[116:119], v[72:75], v[220:223], v[116:119]\n\t"
    "v_cvt_pk_bf16_f32 v10, v12, v13\n\t"
    "v_cvt_pk_bf16_f32 v11, v14, v15\n\t"
    "v_mov_b64 v[12:13], 0\n\t"
    "v_mov_b64 v[14:15], 0\n\t"
    "s_cmp_lg_u32 s47, 0\n\t"
    "s_cbranch_scc1 L_mp2_%=\n\t"
    "L_mp2b_%=:\n\t"
    "s_waitcnt vmcnt(2) lgkmcnt(6)\n\t"
    "v_smfmac_f32_16x16x64_bf16 v[28:31], v[120:123], v[92:99], v124\n\t"
    "ds_read_b64 v[92:93], v102 offset:0\n\t"
    "v_mfma_f32_16x16x32_bf16 v[116:119], v[76:79], v[224:227], v[116:119]\n\t"
    "ds_read_b64 v[94:95], v126 offset:0\n\t"
    "v_cvt_pk_bf16_f32 v80, v80, v81\n\t"
    "v_cvt_pk_bf16_f32 v81, v82, v83\n\t"
    "ds_write_b64 v125, v[80:81] offset:872\n\t"
    "v_mfma_f32_16x16x32_bf16 v[80:83], v[64:67], v[172:175], 0\n\t"
    "v_add_u32_sdwa v98, s92, v103 dst_sel:DWORD dst_unused:UNUSED_PAD src0_sel:DWORD src1_sel:WORD_1\n\t"
    "v_add_u32_sdwa v103, s92, v103 dst_sel:DWORD dst_unused:UNUSED_PAD src0_sel:DWORD src1_sel:WORD_0\n\t"
    "ds_read_b64 v[96:97], v103 offset:0\n\t"
    "v_mfma_f32_16x16x32_bf16 v[80:83], v[68:71], v[176:179], v[80:83]\n\t"
    "ds_read_b64 v[98:99], v98 offset:0\n\t"
    "v_mfma_f32_16x16x32_bf16 v[80:83], v[72:75], v[228:231], v[80:83]\n\t"
    "v_add_u32_sdwa v126, s92, v104 dst_sel:DWORD dst_unused:UNUSED_PAD src0_sel:DWORD src1_sel:WORD_1\n\t"
    "v_add_u32_sdwa v104, s92, v104 dst_sel:DWORD dst_unused:UNUSED_PAD src0_sel:DWORD src1_sel:WORD_0\n\t"
    "v_mfma_f32_16x16x32_bf16 v[80:83], v[76:79], v[232:235], v[80:83]\n\t"
    "v_cvt_pk_bf16_f32 v16, v16, v17\n\t"
    "v_cvt_pk_bf16_f32 v17, v18, v19\n\t"
    "v_cvt_pk_bf16_f32 v116, v116, v117\n\t"
    "v_cvt_pk_bf16_f32 v117, v118, v119\n\t"
    "ds_write_b64 v125, v[116:117] offset:32000\n\t"
    "s_waitcnt lgkmcnt(6)\n\t"
    "v_smfmac_f32_16x16x64_bf16 v[32:35], v[120:123], v[84:91], v124\n\t"
    "ds_read_b64 v[84:85], v104 offset:0\n\t"
    "v_mfma_f32_16x16x32_bf16 v[116:119], v[64:67], v[180:183], 0\n\t"
    "ds_read_b64 v[86:87], v126 offset:0\n\t"
    "v_mfma_f32_16x16x32_bf16 v[116:119], v[68:71], v[184:187], v[116:119]\n\t"
    "v_add_u32_sdwa v90, s92, v105 dst_sel:DWORD dst_unused:UNUSED_PAD src0_sel:DWORD src1_sel:WORD_1\n\t"
    "v_add_u32_sdwa v105, s92, v105 dst_sel:DWORD dst_unused:UNUSED_PAD src0_sel:DWORD src1_sel:WORD_0\n\t"
    "ds_read_b64 v[88:89], v105 offset:0\n\t"
    "v_mfma_f32_16x16x32_bf16 v[116:119], v[72:75], v[236:239], v[116:119]\n\t"
    "ds_read_b64 v[90:91], v90 offset:0\n\t"
    "v_mfma_f32_16x16x32_bf16 v[116:119], v[76:79], v[240:243], v[116:119]\n\t"
    "v_add_u32_sdwa v126, s92, v106 dst_sel:DWORD dst_unused:UNUSED_PAD src0_sel:DWORD src1_sel:WORD_1\n\t"
    "v_add_u32_sdwa v106, s92, v106 dst_sel:DWORD dst_unused:UNUSED_PAD src0_sel:DWORD src1_sel:WORD_0\n\t"
    "v_cvt_pk_bf16_f32 v80, v80, v81\n\t"
    "v_cvt_pk_bf16_f32 v81, v82, v83\n\t"
    "ds_write_b64 v125, v[80:81] offset:32128\n\t"
    "v_mfma_f32_16x16x32_bf16 v[80:83], v[64:67], v[188:191], 0\n\t"
    "v_cvt_pk_bf16_f32 v18, v20, v21\n\t"
    "v_cvt_pk_bf16_f32 v19, v22, v23\n\t"
    "v_mov_b64 v[20:21], 0\n\t"
    "v_mov_b64 v[22:23], 0\n\t"
    "s_waitcnt vmcnt(0)\n\t"
    "s_cmp_lg_u32 s47, 0\n\t"
    "s_cbranch_scc1 L_mp3_%=\n\t"
    "L_mp3b_%=:\n\t"
    "s_barrier\n\t"
    "s_add_u32 s84, s84, s95\n\t"
    "buffer_load_dwordx4 v[100:103], v127, %12, s84 offen offset:0 sc1\n\t"
    "s_lshl_b32 s50, %16, 10\n\t"
    "s_add_u32 s63, s63, s50\n\t"
    "s_add_u32 s50, s50, 0x2000\n\t"
    "s_xor_b32 s50, s50, 0x2000\n\t"
    "s_add_u32 m0, s50, 128000\n\t"
    "s_add_u32 s63, s63, 0x4000\n\t"
    "buffer_load_dwordx4 v127, %12, s63 offen sc1 lds\n\t"
    "s_cmp_lt_u32 %16, 4\n\t"
    "s_cbranch_scc1 L_sp1_%=\n\t"
    "s_setprio 1\n\t"
    "L_sp1_%=:\n\t"
    "s_waitcnt lgkmcnt(6)\n\t"
    "v_smfmac_f32_16x16x64_bf16 v[36:39], v[120:123], v[92:99], v124\n\t"
    "ds_read_b64 v[92:93], v106 offset:0\n\t"
    "v_mfma_f32_16x16x32_bf16 v[80:83], v[68:71], v[192:195], v[80:83]\n\t"
    "ds_read_b64 v[94:95], v126 offset:0\n\t"
    "v_mfma_f32_16x16x32_bf16 v[80:83], v[72:75], v[244:247], v[80:83]\n\t"
    "v_add_u32_sdwa v98, s92, v107 dst_sel:DWORD dst_unused:UNUSED_PAD src0_sel:DWORD src1_sel:WORD_1\n\t"
    "v_add_u32_sdwa v107, s92, v107 dst_sel:DWORD dst_unused:UNUSED_PAD src0_sel:DWORD src1_sel:WORD_0\n\t"
    "ds_read_b64 v[96:97], v107 offset:0\n\t"
    "v_mfma_f32_16x16x32_bf16 v[80:83], v[76:79], v[248:251], v[80:83]\n\t"
    "ds_read_b64 v[98:99], v98 offset:0\n\t"
    "v_cvt_pk_bf16_f32 v116, v116, v117\n\t"
    "v_cvt_pk_bf16_f32 v117, v118, v119\n\t"
    "ds_write_b64 v125, v[116:117] offset:32256\n\t"
    "v_mfma_f32_16x16x32_bf16 v[116:119], v[64:67], v[196:199], 0\n\t"
    "buffer_load_dwordx4 v[104:107], v127, %12, s84 offen offset:1024 sc1\n\t"
    "v_add_u32_sdwa v126, s92, v108 dst_sel:DWORD dst_unused:UNUSED_PAD src0_sel:DWORD src1_sel:WORD_1\n\t"
    "v_add_u32_sdwa v108, s92, v108 dst_sel:DWORD dst_unused:UNUSED_PAD src0_sel:DWORD src1_sel:WORD_0\n\t"
    "v_mfma_f32_16x16x32_bf16 v[116:119], v[68:71], v[200:203], v[116:119]\n\t"
    "v_cvt_pk_bf16_f32 v24, v24, v25\n\t"
    "v_cvt_pk_bf16_f32 v25, v26, v27\n\t"
    "v_mov_b64 v[48:49], 0\n\t"
    "v_mov_b64 v[50:51], 0\n\t"
    "v_mov_b64 v[52:53], 0\n\t"
    "v_mov_b64 v[54:55], 0\n\t"
    "s_waitcnt lgkmcnt(6)\n\t"
    "v_smfmac_f32_16x16x64_bf16 v[40:43], v[120:123], v[84:91], v124\n\t"
    "ds_read_b64 v[84:85], v108 offset:0\n\t"
    "v_mfma_f32_16x16x32_bf16 v[116:119], v[72:75], v[252:255], v[116:119]\n\t"
    "ds_read_b64 v[86:87], v126 offset:0\n\t"
    "v_mfma_f32_16x16x32_bf16 v[116:119], v[76:79], v[128:131], v[116:119]\n\t"
    "v_add_u32_sdwa v90, s92, v109 dst_sel:DWORD dst_unused:UNUSED_PAD src0_sel:DWORD src1_sel:WORD_1\n\t"
    "v_add_u32_sdwa v109, s92, v109 dst_sel:DWORD dst_unused:UNUSED_PAD src0_sel:DWORD src1_sel:WORD_0\n\t"
    "ds_read_b64 v[88:89], v109 offset:0\n\t"
    "v_cvt_pk_bf16_f32 v80, v80, v81\n\t"
    "v_cvt_pk_bf16_f32 v81, v82, v83\n\t"
    "ds_write_b64 v125, v[80:81] offset:32384\n\t"
    "v_mfma_f32_16x16x32_bf16 v[80:83], v[64:67], v[204:207], 0\n\t"
    "ds_read_b64 v[90:91], v90 offset:0\n\t"
    "v_mfma_f32_16x16x32_bf16 v[80:83], v[68:71], v[208:211], v[80:83]\n\t"
    "v_add_u32_sdwa v126, s92, v110 dst_sel:DWORD dst_unused:UNUSED_PAD src0_sel:DWORD src1_sel:WORD_1\n\t"
    "v_add_u32_sdwa v110, s92, v110 dst_sel:DWORD dst_unused:UNUSED_PAD src0_sel:DWORD src1_sel:WORD_0\n\t"
    "v_mfma_f32_16x16x32_bf16 v[80:83], v[72:75], v[132:135], v[80:83]\n\t"
    "v_cvt_pk_bf16_f32 v26, v28, v29\n\t"
    "v_cvt_pk_bf16_f32 v27, v30, v31\n\t"
    "v_mov_b64 v[28:29], 0\n\t"
    "v_mov_b64 v[30:31], 0\n\t"
    "s_waitcnt lgkmcnt(6)\n\t"
    "v_smfmac_f32_16x16x64_bf16 v[44:47], v[120:123], v[92:99], v124\n\t"
    "ds_read_b64 v[92:93], v110 offset:0\n\t"
    "v_mfma_f32_16x16x32_bf16 v[80:83], v[76:79], v[136:139], v[80:83]\n\t"
    "ds_read_b64 v[94:95], v126 offset:0\n\t"
    "v_cvt_pk_bf16_f32 v116, v116, v117\n\t"
    "v_cvt_pk_bf16_f32 v117, v118, v119\n\t"
    "ds_write_b64 v125, v[116:117] offset:32512\n\t"
    "v_mfma_f32_16x16x32_bf16 v[116:119], v[64:67], v[156:159], 0\n\t"
    "v_add_u32_sdwa v98, s92, v111 dst_sel:DWORD dst_unused:UNUSED_PAD src0_sel:DWORD src1_sel:WORD_1\n\t"
    "v_add_u32_sdwa v111, s92, v111 dst_sel:DWORD dst_unused:UNUSED_PAD src0_sel:DWORD src1_sel:WORD_0\n\t"
    "ds_read_b64 v[96:97], v111 offset:0\n\t"
    "v_mfma_f32_16x16x32_bf16 v[116:119], v[68:71], v[160:163], v[116:119]\n\t"
    "ds_read_b64 v[98:99], v98 offset:0\n\t"
    "v_mfma_f32_16x16x32_bf16 v[116:119], v[72:75], v[140:143], v[116:119]\n\t"
    "s_add_u32 s85, s85, s95\n\t"
    "buffer_load_dwordx4 v[108:111], v127, %12, s85 offen offset:0 sc1\n\t"
    "v_add_u32_sdwa v126, s92, v112 dst_sel:DWORD dst_unused:UNUSED_PAD src0_sel:DWORD src1_sel:WORD_1\n\t"
    "v_add_u32_sdwa v112, s92, v112 dst_sel:DWORD dst_unused:UNUSED_PAD src0_sel:DWORD src1_sel:WORD_0\n\t"
    "v_mfma_f32_16x16x32_bf16 v[116:119], v[76:79], v[144:147], v[116:119]\n\t"
    "v_cvt_pk_bf16_f32 v32, v32, v33\n\t"
    "v_cvt_pk_bf16_f32 v33, v34, v35\n\t"
    "v_cvt_pk_bf16_f32 v80, v80, v81\n\t"
    "v_cvt_pk_bf16_f32 v81, v82, v83\n\t"
    "ds_write_b64 v125, v[80:81] offset:32640\n\t"
    "v_mov_b64 v[56:57], 0\n\t"
    "v_mov_b64 v[58:59], 0\n\t"
    "v_mov_b64 v[60:61], 0\n\t"
    "v_mov_b64 v[62:63], 0\n\t"
    "s_waitcnt lgkmcnt(6)\n\t"
    "v_smfmac_f32_16x16x64_bf16 v[48:51], v[120:123], v[84:91], v124\n\t"
    "ds_read_b64 v[84:85], v112 offset:0\n\t"
    "v_mfma_f32_16x16x32_bf16 v[80:83], v[64:67], v[212:215], 0\n\t"
    "ds_read_b64 v[86:87], v126 offset:0\n\t"
    "v_mfma_f32_16x16x32_bf16 v[80:83], v[68:71], v[216:219], v[80:83]\n\t"
    "v_add_u32_sdwa v90, s92, v113 dst_sel:DWORD dst_unused:UNUSED_PAD src0_sel:DWORD src1_sel:WORD_1\n\t"
    "v_add_u32_sdwa v113, s92, v113 dst_sel:DWORD dst_unused:UNUSED_PAD src0_sel:DWORD src1_sel:WORD_0\n\t"
    "ds_read_b64 v[88:89], v113 offset:0\n\t"
    "v_mfma_f32_16x16x32_bf16 v[80:83], v[72:75], v[148:151], v[80:83]\n\t"
    "ds_read_b64 v[90:91], v90 offset:0\n\t"
    "v_mfma_f32_16x16x32_bf16 v[80:83], v[76:79], v[152:155], v[80:83]\n\t"
    "v_add_u32_sdwa v126, s92, v114 dst_sel:DWORD dst_unused:UNUSED_PAD src0_sel:DWORD src1_sel:WORD_1\n\t"
    "v_add_u32_sdwa v114, s92, v114 dst_sel:DWORD dst_unused:UNUSED_PAD src0_sel:DWORD src1_sel:WORD_0\n\t"
    "v_cvt_pk_bf16_f32 v116, v116, v117\n\t"
    "v_cvt_pk_bf16_f32 v117, v118, v119\n\t"
    "ds_write_b64 v125, v[116:117] offset:32768\n\t"
    "s_nop 2\n\t"
    "v_cvt_pk_bf16_f32 v80, v80, v81\n\t"
    "v_cvt_pk_bf16_f32 v81, v82, v83\n\t"
    "ds_write_b64 v125, v[80:81] offset:32872\n\t"
    "v_cvt_pk_bf16_f32 v34, v36, v37\n\t"
    "v_cvt_pk_bf16_f32 v35, v38, v39\n\t"
    "v_mov_b64 v[36:37], 0\n\t"
    "v_mov_b64 v[38:39], 0\n\t"
    "v_cvt_pk_bf16_f32 v40, v40, v41\n\t"
    "v_cvt_pk_bf16_f32 v41, v42, v43\n\t"
    "v_cvt_pk_bf16_f32 v42, v44, v45\n\t"
    "v_cvt_pk_bf16_f32 v43, v46, v47\n\t"
    "v_mov_b64 v[44:45], 0\n\t"
    "v_mov_b64 v[46:47], 0\n\t"
    "s_waitcnt vmcnt(3) lgkmcnt(7)\n\t"
    "v_smfmac_f32_16x16x64_bf16 v[52:55], v[120:123], v[92:99], v124\n\t"
    "ds_read_b64 v[92:93], v114 offset:0\n\t"
    "ds_read_b64 v[94:95], v126 offset:0\n\t"
    "v_add_u32_sdwa v98, s92, v115 dst_sel:DWORD dst_unused:UNUSED_PAD src0_sel:DWORD src1_sel:WORD_1\n\t"
    "v_add_u32_sdwa v115, s92, v115 dst_sel:DWORD dst_unused:UNUSED_PAD src0_sel:DWORD src1_sel:WORD_0\n\t"
    "ds_read_b64 v[96:97], v115 offset:0\n\t"
    "ds_read_b64 v[98:99], v98 offset:0\n\t"
    "buffer_load_dwordx4 v[112:115], v127, %12, s85 offen offset:1024 sc1\n\t"
    "s_sub_u32 s92, 64000, s92\n\t"
    "v_add_u32_sdwa v126, s92, v100 dst_sel:DWORD dst_unused:UNUSED_PAD src0_sel:DWORD src1_sel:WORD_1\n\t"
    "v_add_u32_sdwa v100, s92, v100 dst_sel:DWORD dst_unused:UNUSED_PAD src0_sel:DWORD src1_sel:WORD_0\n\t"
    "v_cvt_pk_bf16_f32 v48, v48, v49\n\t"
    "v_cvt_pk_bf16_f32 v49, v50, v51\n\t"
    "s_waitcnt lgkmcnt(4)\n\t"
    "s_waitcnt lgkmcnt(0)\n\t"
    "s_waitcnt vmcnt(3)\n\t"
    "s_barrier\n\t"
    "v_add_u32 v125, s93, v125\n\t"
    "s_sub_u32 s93, 0, s93\n\t"
    "v_smfmac_f32_16x16x64_bf16 v[56:59], v[120:123], v[84:91], v124\n\t"
    "ds_read_b64 v[84:85], v100 offset:0\n\t"
    "ds_read_b64 v[86:87], v126 offset:0\n\t"
    "v_add_u32_sdwa v90, s92, v101 dst_sel:DWORD dst_unused:UNUSED_PAD src0_sel:DWORD src1_sel:WORD_1\n\t"
    "v_add_u32_sdwa v101, s92, v101 dst_sel:DWORD dst_unused:UNUSED_PAD src0_sel:DWORD src1_sel:WORD_0\n\t"
    "ds_read_b64 v[88:89], v101 offset:0\n\t"
    "ds_read_b64 v[90:91], v90 offset:0\n\t"
    "v_add_u32_sdwa v126, s92, v102 dst_sel:DWORD dst_unused:UNUSED_PAD src0_sel:DWORD src1_sel:WORD_1\n\t"
    "v_add_u32_sdwa v102, s92, v102 dst_sel:DWORD dst_unused:UNUSED_PAD src0_sel:DWORD src1_sel:WORD_0\n\t"
    "v_cvt_pk_bf16_f32 v50, v52, v53\n\t"
    "v_cvt_pk_bf16_f32 v51, v54, v55\n\t"
    "v_mov_b64 v[52:53], 0\n\t"
    "v_mov_b64 v[54:55], 0\n\t"
    "s_waitcnt vmcnt(2)\n\t"
    "v_smfmac_f32_16x16x64_bf16 v[60:63], v[120:123], v[92:99], v124\n\t"
    "ds_read_b64 v[92:93], v102 offset:0\n\t"
    "ds_read_b64 v[94:95], v126 offset:0\n\t"
    "v_add_u32_sdwa v98, s92, v103 dst_sel:DWORD dst_unused:UNUSED_PAD src0_sel:DWORD src1_sel:WORD_1\n\t"
    "v_add_u32_sdwa v103, s92, v103 dst_sel:DWORD dst_unused:UNUSED_PAD src0_sel:DWORD src1_sel:WORD_0\n\t"
    "ds_read_b64 v[96:97], v103 offset:0\n\t"
    "ds_read_b64 v[98:99], v98 offset:0\n\t"
    "s_add_u32 s86, s86, s95\n\t"
    "buffer_load_dwordx4 v[100:103], v127, %12, s86 offen offset:0 sc1\n\t"
    "v_add_u32_sdwa v126, s92, v104 dst_sel:DWORD dst_unused:UNUSED_PAD src0_sel:DWORD src1_sel:WORD_1\n\t"
    "v_add_u32_sdwa v104, s92, v104 dst_sel:DWORD dst_unused:UNUSED_PAD src0_sel:DWORD src1_sel:WORD_0\n\t"
    "v_cvt_pk_bf16_f32 v56, v56, v57\n\t"
    "v_cvt_pk_bf16_f32 v57, v58, v59\n\t"
    "v_add_u32 v79, s96, v127\n\t"
    "s_add_u32 s96, s96, 0x1000\n\t"
    "s_and_b32 s96, s96, 0x3000\n\t"
    "v_add_u32 v79, 128000, v79\n\t"
    "ds_read_b128 v[64:67], v79 offset:0\n\t"
    "ds_read_b128 v[68:71], v79 offset:1024\n\t"
    "ds_read_b128 v[72:75], v79 offset:2048\n\t"
    "ds_read_b128 v[76:79], v79 offset:3072\n\t"
    "v_mov_b64 v[116:117], 0\n\t"
    "v_mov_b64 v[118:119], 0\n\t"
    "v_cvt_pk_bf16_f32 v58, v60, v61\n\t"
    "v_cvt_pk_bf16_f32 v59, v62, v63\n\t"
    "v_mov_b64 v[60:61], 0\n\t"
    "v_mov_b64 v[62:63], 0\n\t"
    "s_cmp_eq_u32 %16, 0\n\t"
    "s_cselect_b32 s48, 2, -1\n\t"
    "s_mov_b32 s66, 4\n\t"
    "s_branch L_loop_%=\n\t"
    ".p2align 9\n\t"
    "s_nop 0\n\t"
    "s_nop 0\n\t"
    "s_nop 0\n\t"
    "s_nop 0\n\t"
    "s_nop 0\n\t"
    "s_nop 0\n\t"
    "s_nop 0\n\t"
    "s_nop 0\n\t"
    "s_nop 0\n\t"
    "s_nop 0\n\t"
    "s_nop 0\n\t"
    "s_nop 0\n\t"
    "s_nop 0\n\t"
    "s_nop 0\n\t"
    "s_nop 0\n\t"
    "s_nop 0\n\t"
    "s_nop 0\n\t"
    "s_nop 0\n\t"
    "s_nop 0\n\t"
    "s_nop 0\n\t"
    "s_nop 0\n\t"
    "s_nop 0\n\t"
    "s_nop 0\n\t"
    "s_nop 0\n\t"
    "s_nop 0\n\t"
    "s_nop 0\n\t"
    "s_nop 0\n\t"
    "s_nop 0\n\t"
    "s_nop 0\n\t"
    "s_nop 0\n\t"
    "s_nop 0\n\t"
    "s_nop 0\n\t"
    "s_nop 0\n\t"
    "L_loop_%=:\n\t"
    "s_waitcnt lgkmcnt(8)\n\t"
    "v_smfmac_f32_16x16x64_bf16 v[116:119], v[120:123], v[84:91], v124\n\t"
    "ds_read_b64 v[84:85], v104 offset:0\n\t"
    "ds_read_b64 v[86:87], v126 offset:0\n\t"
    "v_add_u32_sdwa v90, s92, v105 dst_sel:DWORD dst_unused:UNUSED_PAD src0_sel:DWORD src1_sel:WORD_1\n\t"
    "v_add_u32_sdwa v105, s92, v105 dst_sel:DWORD dst_unused:UNUSED_PAD src0_sel:DWORD src1_sel:WORD_0\n\t"
    "ds_read_b64 v[88:89], v105 offset:0\n\t"
    "ds_read_b64 v[90:91], v90 offset:0\n\t"
    "v_add_u32_sdwa v126, s92, v106 dst_sel:DWORD dst_unused:UNUSED_PAD src0_sel:DWORD src1_sel:WORD_1\n\t"
    "v_add_u32_sdwa v106, s92, v106 dst_sel:DWORD dst_unused:UNUSED_PAD src0_sel:DWORD src1_sel:WORD_0\n\t"
    "s_waitcnt vmcnt(2) lgkmcnt(8)\n\t"
    "v_smfmac_f32_16x16x64_bf16 v[116:119], v[120:123], v[92:99], v124\n\t"
    "ds_read_b64 v[92:93], v106 offset:0\n\t"
    "ds_read_b64 v[94:95], v126 offset:0\n\t"
    "v_add_u32_sdwa v98, s92, v107 dst_sel:DWORD dst_unused:UNUSED_PAD src0_sel:DWORD src1_sel:WORD_1\n\t"
    "v_add_u32_sdwa v107, s92, v107 dst_sel:DWORD dst_unused:UNUSED_PAD src0_sel:DWORD src1_sel:WORD_0\n\t"
    "ds_read_b64 v[96:97], v107 offset:0\n\t"
    "ds_read_b64 v[98:99], v98 offset:0\n\t"
    "buffer_load_dwordx4 v[104:107], v127, %12, s86 offen offset:1024 sc1\n\t"
    "v_add_u32_sdwa v126, s92, v108 dst_sel:DWORD dst_unused:UNUSED_PAD src0_sel:DWORD src1_sel:WORD_1\n\t"
    "v_cvt_pk_bf16_f32 v4, v116, v117\n\t"
    "v_cvt_pk_bf16_f32 v5, v118, v119\n\t"
    "v_and_b32 v119, s67, v7\n\t"
    "v_lshlrev_b32 v118, 16, v7\n\t"
    "v_and_b32 v117, s67, v6\n\t"
    "v_lshlrev_b32 v116, 16, v6\n\t"
    "v_add_u32_sdwa v108, s92, v108 dst_sel:DWORD dst_unused:UNUSED_PAD src0_sel:DWORD src1_sel:WORD_0\n\t"
    "s_waitcnt lgkmcnt(4)\n\t"
    "v_smfmac_f32_16x16x64_bf16 v[116:119], v[120:123], v[84:91], v124\n\t"
    "ds_read_b64 v[84:85], v108 offset:0\n\t"
    "v_mfma_f32_16x16x32_bf16 v[80:83], v[64:67], v[164:167], 0\n\t"
    "ds_read_b64 v[86:87], v126 offset:0\n\t"
    "v_mfma_f32_16x16x32_bf16 v[80:83], v[68:71], v[168:171], v[80:83]\n\t"
    "v_add_u32_sdwa v90, s92, v109 dst_sel:DWORD dst_unused:UNUSED_PAD src0_sel:DWORD src1_sel:WORD_1\n\t"
    "v_add_u32_sdwa v109, s92, v109 dst_sel:DWORD dst_unused:UNUSED_PAD src0_sel:DWORD src1_sel:WORD_0\n\t"
    "ds_read_b64 v[88:89], v109 offset:0\n\t"
    "v_mfma_f32_16x16x32_bf16 v[80:83], v[72:75], v[220:223], v[80:83]\n\t"
    "ds_read_b64 v[90:91], v90 offset:0\n\t"
    "v_mfma_f32_16x16x32_bf16 v[80:83], v[76:79], v[224:227], v[80:83]\n\t"
    "v_add_u32_sdwa v126, s92, v110 dst_sel:DWORD dst_unused:UNUSED_PAD src0_sel:DWORD src1_sel:WORD_1\n\t"
    "v_add_u32_sdwa v110, s92, v110 dst_sel:DWORD dst_unused:UNUSED_PAD src0_sel:DWORD src1_sel:WORD_0\n\t"
    "s_waitcnt vmcnt(2) lgkmcnt(4)\n\t"
    "v_smfmac_f32_16x16x64_bf16 v[116:119], v[120:123], v[92:99], v124\n\t"
    "ds_read_b64 v[92:93], v110 offset:0\n\t"
    "ds_read_b64 v[94:95], v126 offset:0\n\t"
    "v_add_u32_sdwa v98, s92, v111 dst_sel:DWORD dst_unused:UNUSED_PAD src0_sel:DWORD src1_sel:WORD_1\n\t"
    "v_add_u32_sdwa v111, s92, v111 dst_sel:DWORD dst_unused:UNUSED_PAD src0_sel:DWORD src1_sel:WORD_0\n\t"
    "ds_read_b64 v[96:97], v111 offset:0\n\t"
    "ds_read_b64 v[98:99], v98 offset:0\n\t"
    "s_add_u32 s87, s87, s95\n\t"
    "buffer_load_dwordx4 v[108:111], v127, %12, s87 offen offset:0 sc1\n\t"
    "v_add_u32_sdwa v126, s92, v112 dst_sel:DWORD dst_unused:UNUSED_PAD src0_sel:DWORD src1_sel:WORD_1\n\t"
    "v_cvt_pk_bf16_f32 v6, v116, v117\n\t"
    "v_cvt_pk_bf16_f32 v7, v118, v119\n\t"
    "v_and_b32 v119, s67, v13\n\t"
    "v_lshlrev_b32 v118, 16, v13\n\t"
    "v_and_b32 v117, s67, v12\n\t"
    "v_lshlrev_b32 v116, 16, v12\n\t"
    "v_add_u32_sdwa v112, s92, v112 dst_sel:DWORD dst_unused:UNUSED_PAD src0_sel:DWORD src1_sel:WORD_0\n\t"
    "s_waitcnt lgkmcnt(4)\n\t"
    "v_smfmac_f32_16x16x64_bf16 v[116:119], v[120:123], v[84:91], v124\n\t"
    "ds_read_b64 v[84:85], v112 offset:0\n\t"
    "ds_read_b64 v[86:87], v126 offset:0\n\t"
    "v_cvt_pk_bf16_f32 v80, v80, v81\n\t"
    "v_cvt_pk_bf16_f32 v81, v82, v83\n\t"
    "ds_write_b64 v125, v[80:81] offset:0\n\t"
    "v_mfma_f32_16x16x32_bf16 v[80:83], v[64:67], v[172:175], 0\n\t"
    "v_add_u32_sdwa v90, s92, v113 dst_sel:DWORD dst_unused:UNUSED_PAD src0_sel:DWORD src1_sel:WORD_1\n\t"
    "v_add_u32_sdwa v113, s92, v113 dst_sel:DWORD dst_unused:UNUSED_PAD src0_sel:DWORD src1_sel:WORD_0\n\t"
    "ds_read_b64 v[88:89], v113 offset:0\n\t"
    "v_mfma_f32_16x16x32_bf16 v[80:83], v[68:71], v[176:179], v[80:83]\n\t"
    "ds_read_b64 v[90:91], v90 offset:0\n\t"
    "v_mfma_f32_16x16x32_bf16 v[80:83], v[72:75], v[228:231], v[80:83]\n\t"
    "v_mfma_f32_16x16x32_bf16 v[80:83], v[76:79], v[232:235], v[80:83]\n\t"
    "v_add_u32_sdwa v126, s92, v114 dst_sel:DWORD dst_unused:UNUSED_PAD src0_sel:DWORD src1_sel:WORD_1\n\t"
    "v_add_u32_sdwa v114, s92, v114 dst_sel:DWORD dst_unused:UNUSED_PAD src0_sel:DWORD src1_sel:WORD_0\n\t"
    "s_waitcnt vmcnt(2) lgkmcnt(5)\n\t"
    "v_smfmac_f32_16x16x64_bf16 v[116:119], v[120:123], v[92:99], v124\n\t"
    "ds_read_b64 v[92:93], v114 offset:0\n\t"
    "ds_read_b64 v[94:95], v126 offset:0\n\t"
    "v_add_u32_sdwa v98, s92, v115 dst_sel:DWORD dst_unused:UNUSED_PAD src0_sel:DWORD src1_sel:WORD_1\n\t"
    "v_add_u32_sdwa v115, s92, v115 dst_sel:DWORD dst_unused:UNUSED_PAD src0_sel:DWORD src1_sel:WORD_0\n\t"
    "ds_read_b64 v[96:97], v115 offset:0\n\t"
    "ds_read_b64 v[98:99], v98 offset:0\n\t"
    "buffer_load_dwordx4 v[112:115], v127, %12, s87 offen offset:1024 sc1\n\t"
    "v_add_u32_sdwa v126, s92, v100 dst_sel:DWORD dst_unused:UNUSED_PAD src0_sel:DWORD src1_sel:WORD_1\n\t"
    "v_cvt_pk_bf16_f32 v12, v116, v117\n\t"
    "v_cvt_pk_bf16_f32 v13, v118, v119\n\t"
    "v_and_b32 v119, s67, v15\n\t"
    "v_lshlrev_b32 v118, 16, v15\n\t"
    "v_and_b32 v117, s67, v14\n\t"
    "v_lshlrev_b32 v116, 16, v14\n\t"
    "v_add_u32_sdwa v100, s92, v100 dst_sel:DWORD dst_unused:UNUSED_PAD src0_sel:DWORD src1_sel:WORD_0\n\t"
    "s_waitcnt lgkmcnt(4)\n\t"
    "v_smfmac_f32_16x16x64_bf16 v[116:119], v[120:123], v[84:91], v124\n\t"
    "ds_read_b64 v[84:85], v100 offset:0\n\t"
    "ds_read_b64 v[86:87], v126 offset:0\n\t"
    "v_cvt_pk_bf16_f32 v80, v80, v81\n\t"
    "v_cvt_pk_bf16_f32 v81, v82, v83\n\t"
    "ds_write_b64 v125, v[80:81] offset:128\n\t"
    "v_mfma_f32_16x16x32_bf16 v[80:83], v[64:67], v[180:183], 0\n\t"
    "v_add_u32_sdwa v90, s92, v101 dst_sel:DWORD dst_unused:UNUSED_PAD src0_sel:DWORD src1_sel:WORD_1\n\t"
    "v_add_u32_sdwa v101, s92, v101 dst_sel:DWORD dst_unused:UNUSED_PAD src0_sel:DWORD src1_sel:WORD_0\n\t"
    "ds_read_b64 v[88:89], v101 offset:0\n\t"
    "v_mfma_f32_16x16x32_bf16 v[80:83], v[68:71], v[184:187], v[80:83]\n\t"
    "ds_read_b64 v[90:91], v90 offset:0\n\t"
    "v_mfma_f32_16x16x32_bf16 v[80:83], v[72:75], v[236:239], v[80:83]\n\t"
    "v_mfma_f32_16x16x32_bf16 v[80:83], v[76:79], v[240:243], v[80:83]\n\t"
    "v_add_u32_sdwa v126, s92, v102 dst_sel:DWORD dst_unused:UNUSED_PAD src0_sel:DWORD src1_sel:WORD_1\n\t"
    "v_add_u32_sdwa v102, s92, v102 dst_sel:DWORD dst_unused:UNUSED_PAD src0_sel:DWORD src1_sel:WORD_0\n\t"
    "s_waitcnt vmcnt(2) lgkmcnt(5)\n\t"
    "v_smfmac_f32_16x16x64_bf16 v[116:119], v[120:123], v[92:99], v124\n\t"
    "ds_read_b64 v[92:93], v102 offset:0\n\t"
    "ds_read_b64 v[94:95], v126 offset:0\n\t"
    "v_add_u32_sdwa v98, s92, v103 dst_sel:DWORD dst_unused:UNUSED_PAD src0_sel:DWORD src1_sel:WORD_1\n\t"
    "v_add_u32_sdwa v103, s92, v103 dst_sel:DWORD dst_unused:UNUSED_PAD src0_sel:DWORD src1_sel:WORD_0\n\t"
    "ds_read_b64 v[96:97], v103 offset:0\n\t"
    "ds_read_b64 v[98:99], v98 offset:0\n\t"
    "s_add_u32 s88, s88, s95\n\t"
    "buffer_load_dwordx4 v[100:103], v127, %12, s88 offen offset:0 sc1\n\t"
    "v_add_u32_sdwa v126, s92, v104 dst_sel:DWORD dst_unused:UNUSED_PAD src0_sel:DWORD src1_sel:WORD_1\n\t"
    "v_cvt_pk_bf16_f32 v14, v116, v117\n\t"
    "v_cvt_pk_bf16_f32 v15, v118, v119\n\t"
    "v_and_b32 v119, s67, v21\n\t"
    "v_lshlrev_b32 v118, 16, v21\n\t"
    "v_and_b32 v117, s67, v20\n\t"
    "v_lshlrev_b32 v116, 16, v20\n\t"
    "v_add_u32_sdwa v104, s92, v104 dst_sel:DWORD dst_unused:UNUSED_PAD src0_sel:DWORD src1_sel:WORD_0\n\t"
    "s_waitcnt lgkmcnt(4)\n\t"
    "v_smfmac_f32_16x16x64_bf16 v[116:119], v[120:123], v[84:91], v124\n\t"
    "ds_read_b64 v[84:85], v104 offset:0\n\t"
    "ds_read_b64 v[86:87], v126 offset:0\n\t"
    "v_cvt_pk_bf16_f32 v80, v80, v81\n\t"
    "v_cvt_pk_bf16_f32 v81, v82, v83\n\t"
    "ds_write_b64 v125, v[80:81] offset:256\n\t"
    "v_add_u32_sdwa v90, s92, v105 dst_sel:DWORD dst_unused:UNUSED_PAD src0_sel:DWORD src1_sel:WORD_1\n\t"
    "v_add_u32_sdwa v105, s92, v105 dst_sel:DWORD dst_unused:UNUSED_PAD src0_sel:DWORD src1_sel:WORD_0\n\t"
    "ds_read_b64 v[88:89], v105 offset:0\n\t"
    "ds_read_b64 v[90:91], v90 offset:0\n\t"
    "v_add_u32_sdwa v126, s92, v106 dst_sel:DWORD dst_unused:UNUSED_PAD src0_sel:DWORD src1_sel:WORD_1\n\t"
    "v_add_u32_sdwa v106, s92, v106 dst_sel:DWORD dst_unused:UNUSED_PAD src0_sel:DWORD src1_sel:WORD_0\n\t"
    "s_waitcnt vmcnt(2) lgkmcnt(5)\n\t"
    "v_smfmac_f32_16x16x64_bf16 v[116:119], v[120:123], v[92:99], v124\n\t"
    "ds_read_b64 v[92:93], v106 offset:0\n\t"
    "ds_read_b64 v[94:95], v126 offset:0\n\t"
    "v_add_u32_sdwa v98, s92, v107 dst_sel:DWORD dst_unused:UNUSED_PAD src0_sel:DWORD src1_sel:WORD_1\n\t"
    "v_add_u32_sdwa v107, s92, v107 dst_sel:DWORD dst_unused:UNUSED_PAD src0_sel:DWORD src1_sel:WORD_0\n\t"
    "ds_read_b64 v[96:97], v107 offset:0\n\t"
    "ds_read_b64 v[98:99], v98 offset:0\n\t"
    "buffer_load_dwordx4 v[104:107], v127, %12, s88 offen offset:1024 sc1\n\t"
    "v_add_u32_sdwa v126, s92, v108 dst_sel:DWORD dst_unused:UNUSED_PAD src0_sel:DWORD src1_sel:WORD_1\n\t"
    "v_cvt_pk_bf16_f32 v20, v116, v117\n\t"
    "v_cvt_pk_bf16_f32 v21, v118, v119\n\t"
    "v_and_b32 v119, s67, v23\n\t"
    "v_lshlrev_b32 v118, 16, v23\n\t"
    "v_and_b32 v117, s67, v22\n\t"
    "v_lshlrev_b32 v116, 16, v22\n\t"
    "v_add_u32_sdwa v108, s92, v108 dst_sel:DWORD dst_unused:UNUSED_PAD src0_sel:DWORD src1_sel:WORD_0\n\t"
    "s_waitcnt lgkmcnt(4)\n\t"
    "v_smfmac_f32_16x16x64_bf16 v[116:119], v[120:123], v[84:91], v124\n\t"
    "ds_read_b64 v[84:85], v108 offset:0\n\t"
    "v_mfma_f32_16x16x32_bf16 v[80:83], v[64:67], v[188:191], 0\n\t"
    "ds_read_b64 v[86:87], v126 offset:0\n\t"
    "v_mfma_f32_16x16x32_bf16 v[80:83], v[68:71], v[192:195], v[80:83]\n\t"
    "v_add_u32_sdwa v90, s92, v109 dst_sel:DWORD dst_unused:UNUSED_PAD src0_sel:DWORD src1_sel:WORD_1\n\t"
    "v_add_u32_sdwa v109, s92, v109 dst_sel:DWORD dst_unused:UNUSED_PAD src0_sel:DWORD src1_sel:WORD_0\n\t"
    "ds_read_b64 v[88:89], v109 offset:0\n\t"
    "v_mfma_f32_16x16x32_bf16 v[80:83], v[72:75], v[244:247], v[80:83]\n\t"
    "ds_read_b64 v[90:91], v90 offset:0\n\t"
    "v_mfma_f32_16x16x32_bf16 v[80:83], v[76:79], v[248:251], v[80:83]\n\t"
    "v_add_u32_sdwa v126, s92, v110 dst_sel:DWORD dst_unused:UNUSED_PAD src0_sel:DWORD src1_sel:WORD_1\n\t"
    "v_add_u32_sdwa v110, s92, v110 dst_sel:DWORD dst_unused:UNUSED_PAD src0_sel:DWORD src1_sel:WORD_0\n\t"
    "s_waitcnt vmcnt(2) lgkmcnt(4)\n\t"
    "v_smfmac_f32_16x16x64_bf16 v[116:119], v[120:123], v[92:99], v124\n\t"
    "ds_read_b64 v[92:93], v110 offset:0\n\t"
    "ds_read_b64 v[94:95], v126 offset:0\n\t"
    "v_add_u32_sdwa v98, s92, v111 dst_sel:DWORD dst_unused:UNUSED_PAD src0_sel:DWORD src1_sel:WORD_1\n\t"
    "v_add_u32_sdwa v111, s92, v111 dst_sel:DWORD dst_unused:UNUSED_PAD src0_sel:DWORD src1_sel:WORD_0\n\t"
    "ds_read_b64 v[96:97], v111 offset:0\n\t"
    "ds_read_b64 v[98:99], v98 offset:0\n\t"
    "s_add_u32 s89, s89, s95\n\t"
    "buffer_load_dwordx4 v[108:111], v127, %12, s89 offen offset:0 sc1\n\t"
    "v_add_u32_sdwa v126, s92, v112 dst_sel:DWORD dst_unused:UNUSED_PAD src0_sel:DWORD src1_sel:WORD_1\n\t"
    "v_cvt_pk_bf16_f32 v22, v116, v117\n\t"
    "v_cvt_pk_bf16_f32 v23, v118, v119\n\t"
    "v_and_b32 v119, s67, v29\n\t"
    "v_lshlrev_b32 v118, 16, v29\n\t"
    "v_and_b32 v117, s67, v28\n\t"
    "v_lshlrev_b32 v116, 16, v28\n\t"
    "v_add_u32_sdwa v112, s92, v112 dst_sel:DWORD dst_unused:UNUSED_PAD src0_sel:DWORD src1_sel:WORD_0\n\t"
    "s_waitcnt lgkmcnt(4)\n\t"
    "v_smfmac_f32_16x16x64_bf16 v[116:119], v[120:123], v[84:91], v124\n\t"
    "ds_read_b64 v[84:85], v112 offset:0\n\t"
    "ds_read_b64 v[86:87], v126 offset:0\n\t"
    "v_cvt_pk_bf16_f32 v80, v80, v81\n\t"
    "v_cvt_pk_bf16_f32 v81, v82, v83\n\t"
    "ds_write_b64 v125, v[80:81] offset:384\n\t"
    "v_add_u32_sdwa v90, s92, v113 dst_sel:DWORD dst_unused:UNUSED_PAD src0_sel:DWORD src1_sel:WORD_1\n\t"
    "v_add_u32_sdwa v113, s92, v113 dst_sel:DWORD dst_unused:UNUSED_PAD src0_sel:DWORD src1_sel:WORD_0\n\t"
    "ds_read_b64 v[88:89], v113 offset:0\n\t"
    "ds_read_b64 v[90:91], v90 offset:0\n\t"
    "v_add_u32_sdwa v126, s92, v114 dst_sel:DWORD dst_unused:UNUSED_PAD src0_sel:DWORD src1_sel:WORD_1\n\t"
    "v_add_u32_sdwa v114, s92, v114 dst_sel:DWORD dst_unused:UNUSED_PAD src0_sel:DWORD src1_sel:WORD_0\n\t"
    "s_waitcnt vmcnt(2) lgkmcnt(5)\n\t"
    "v_smfmac_f32_16x16x64_bf16 v[116:119], v[120:123], v[92:99], v124\n\t"
    "ds_read_b64 v[92:93], v114 offset:0\n\t"
    "ds_read_b64 v[94:95], v126 offset:0\n\t"
    "v_add_u32_sdwa v98, s92, v115 dst_sel:DWORD dst_unused:UNUSED_PAD src0_sel:DWORD src1_sel:WORD_1\n\t"
    "v_add_u32_sdwa v115, s92, v115 dst_sel:DWORD dst_unused:UNUSED_PAD src0_sel:DWORD src1_sel:WORD_0\n\t"
    "ds_read_b64 v[96:97], v115 offset:0\n\t"
    "ds_read_b64 v[98:99], v98 offset:0\n\t"
    "buffer_load_dwordx4 v[112:115], v127, %12, s89 offen offset:1024 sc1\n\t"
    "v_add_u32_sdwa v126, s92, v100 dst_sel:DWORD dst_unused:UNUSED_PAD src0_sel:DWORD src1_sel:WORD_1\n\t"
    "v_cvt_pk_bf16_f32 v28, v116, v117\n\t"
    "v_cvt_pk_bf16_f32 v29, v118, v119\n\t"
    "v_and_b32 v119, s67, v31\n\t"
    "v_lshlrev_b32 v118, 16, v31\n\t"
    "v_and_b32 v117, s67, v30\n\t"
    "v_lshlrev_b32 v116, 16, v30\n\t"
    "v_add_u32_sdwa v100, s92, v100 dst_sel:DWORD dst_unused:UNUSED_PAD src0_sel:DWORD src1_sel:WORD_0\n\t"
    "s_waitcnt lgkmcnt(4)\n\t"
    "v_smfmac_f32_16x16x64_bf16 v[116:119], v[120:123], v[84:91], v124\n\t"
    "ds_read_b64 v[84:85], v100 offset:0\n\t"
    "v_mfma_f32_16x16x32_bf16 v[80:83], v[64:67], v[196:199], 0\n\t"
    "ds_read_b64 v[86:87], v126 offset:0\n\t"
    "v_mfma_f32_16x16x32_bf16 v[80:83], v[68:71], v[200:203], v[80:83]\n\t"
    "v_add_u32_sdwa v90, s92, v101 dst_sel:DWORD dst_unused:UNUSED_PAD src0_sel:DWORD src1_sel:WORD_1\n\t"
    "v_add_u32_sdwa v101, s92, v101 dst_sel:DWORD dst_unused:UNUSED_PAD src0_sel:DWORD src1_sel:WORD_0\n\t"
    "ds_read_b64 v[88:89], v101 offset:0\n\t"
    "v_mfma_f32_16x16x32_bf16 v[80:83], v[72:75], v[252:255], v[80:83]\n\t"
    "ds_read_b64 v[90:91], v90 offset:0\n\t"
    "v_mfma_f32_16x16x32_bf16 v[80:83], v[76:79], v[128:131], v[80:83]\n\t"
    "v_add_u32_sdwa v126, s92, v102 dst_sel:DWORD dst_unused:UNUSED_PAD src0_sel:DWORD src1_sel:WORD_1\n\t"
    "v_add_u32_sdwa v102, s92, v102 dst_sel:DWORD dst_unused:UNUSED_PAD src0_sel:DWORD src1_sel:WORD_0\n\t"
    "s_xor_b32 s50, s50, 0x2000\n\t"
    "s_add_u32 m0, s50, 128000\n\t"
    "s_add_u32 s63, s63, 0x4000\n\t"
    "buffer_load_dwordx4 v127, %12, s63 offen sc1 lds\n\t"
    "s_waitcnt vmcnt(3) lgkmcnt(4)\n\t"
    "v_smfmac_f32_16x16x64_bf16 v[116:119], v[120:123], v[92:99], v124\n\t"
    "ds_read_b64 v[92:93], v102 offset:0\n\t"
    "ds_read_b64 v[94:95], v126 offset:0\n\t"
    "v_add_u32_sdwa v98, s92, v103 dst_sel:DWORD dst_unused:UNUSED_PAD src0_sel:DWORD src1_sel:WORD_1\n\t"
    "v_add_u32_sdwa v103, s92, v103 dst_sel:DWORD dst_unused:UNUSED_PAD src0_sel:DWORD src1_sel:WORD_0\n\t"
    "ds_read_b64 v[96:97], v103 offset:0\n\t"
    "ds_read_b64 v[98:99], v98 offset:0\n\t"
    "s_add_u32 s90, s90, s95\n\t"
    "buffer_load_dwordx4 v[100:103], v127, %12, s90 offen offset:0 sc1\n\t"
    "v_add_u32_sdwa v126, s92, v104 dst_sel:DWORD dst_unused:UNUSED_PAD src0_sel:DWORD src1_sel:WORD_1\n\t"
    "v_cvt_pk_bf16_f32 v30, v116, v117\n\t"
    "v_cvt_pk_bf16_f32 v31, v118, v119\n\t"
    "v_and_b32 v119, s67, v37\n\t"
    "v_lshlrev_b32 v118, 16, v37\n\t"
    "v_and_b32 v117, s67, v36\n\t"
    "v_lshlrev_b32 v116, 16, v36\n\t"
    "v_add_u32_sdwa v104, s92, v104 dst_sel:DWORD dst_unused:UNUSED_PAD src0_sel:DWORD src1_sel:WORD_0\n\t"
    "s_waitcnt lgkmcnt(4)\n\t"
    "v_smfmac_f32_16x16x64_bf16 v[116:119], v[120:123], v[84:91], v124\n\t"
    "ds_read_b64 v[84:85], v104 offset:0\n\t"
    "ds_read_b64 v[86:87], v126 offset:0\n\t"
    "v_cvt_pk_bf16_f32 v80, v80, v81\n\t"
    "v_cvt_pk_bf16_f32 v81, v82, v83\n\t"
    "ds_write_b64 v125, v[80:81] offset:512\n\t"
    "v_add_u32_sdwa v90, s92, v105 dst_sel:DWORD dst_unused:UNUSED_PAD src0_sel:DWORD src1_sel:WORD_1\n\t"
    "v_add_u32_sdwa v105, s92, v105 dst_sel:DWORD dst_unused:UNUSED_PAD src0_sel:DWORD src1_sel:WORD_0\n\t"
    "ds_read_b64 v[88:89], v105 offset:0\n\t"
    "ds_read_b64 v[90:91], v90 offset:0\n\t"
    "v_add_u32_sdwa v126, s92, v106 dst_sel:DWORD dst_unused:UNUSED_PAD src0_sel:DWORD src1_sel:WORD_1\n\t"
    "v_add_u32_sdwa v106, s92, v106 dst_sel:DWORD dst_unused:UNUSED_PAD src0_sel:DWORD src1_sel:WORD_0\n\t"
    "s_waitcnt vmcnt(3) lgkmcnt(5)\n\t"
    "v_smfmac_f32_16x16x64_bf16 v[116:119], v[120:123], v[92:99], v124\n\t"
    "ds_read_b64 v[92:93], v106 offset:0\n\t"
    "ds_read_b64 v[94:95], v126 offset:0\n\t"
    "v_add_u32_sdwa v98, s92, v107 dst_sel:DWORD dst_unused:UNUSED_PAD src0_sel:DWORD src1_sel:WORD_1\n\t"
    "v_add_u32_sdwa v107, s92, v107 dst_sel:DWORD dst_unused:UNUSED_PAD src0_sel:DWORD src1_sel:WORD_0\n\t"
    "ds_read_b64 v[96:97], v107 offset:0\n\t"
    "ds_read_b64 v[98:99], v98 offset:0\n\t"
    "buffer_load_dwordx4 v[104:107], v127, %12, s90 offen offset:1024 sc1\n\t"
    "v_add_u32_sdwa v126, s92, v108 dst_sel:DWORD dst_unused:UNUSED_PAD src0_sel:DWORD src1_sel:WORD_1\n\t"
    "v_cvt_pk_bf16_f32 v36, v116, v117\n\t"
    "v_cvt_pk_bf16_f32 v37, v118, v119\n\t"
    "v_and_b32 v119, s67, v39\n\t"
    "v_lshlrev_b32 v118, 16, v39\n\t"
    "v_and_b32 v117, s67, v38\n\t"
    "v_lshlrev_b32 v116, 16, v38\n\t"
    "v_add_u32_sdwa v108, s92, v108 dst_sel:DWORD dst_unused:UNUSED_PAD src0_sel:DWORD src1_sel:WORD_0\n\t"
    "s_waitcnt lgkmcnt(4)\n\t"
    "v_smfmac_f32_16x16x64_bf16 v[116:119], v[120:123], v[84:91], v124\n\t"
    "ds_read_b64 v[84:85], v108 offset:0\n\t"
    "v_mfma_f32_16x16x32_bf16 v[80:83], v[64:67], v[204:207], 0\n\t"
    "ds_read_b64 v[86:87], v126 offset:0\n\t"
    "v_mfma_f32_16x16x32_bf16 v[80:83], v[68:71], v[208:211], v[80:83]\n\t"
    "v_add_u32_sdwa v90, s92, v109 dst_sel:DWORD dst_unused:UNUSED_PAD src0_sel:DWORD src1_sel:WORD_1\n\t"
    "v_add_u32_sdwa v109, s92, v109 dst_sel:DWORD dst_unused:UNUSED_PAD src0_sel:DWORD src1_sel:WORD_0\n\t"
    "ds_read_b64 v[88:89], v109 offset:0\n\t"
    "v_mfma_f32_16x16x32_bf16 v[80:83], v[72:75], v[132:135], v[80:83]\n\t"
    "ds_read_b64 v[90:91], v90 offset:0\n\t"
    "v_mfma_f32_16x16x32_bf16 v[80:83], v[76:79], v[136:139], v[80:83]\n\t"
    "v_add_u32_sdwa v126, s92, v110 dst_sel:DWORD dst_unused:UNUSED_PAD src0_sel:DWORD src1_sel:WORD_1\n\t"
    "v_add_u32_sdwa v110, s92, v110 dst_sel:DWORD dst_unused:UNUSED_PAD src0_sel:DWORD src1_sel:WORD_0\n\t"
    "s_cmp_lt_u32 %16, 4\n\t"
    "s_cbranch_scc1 L_tp19_%=\n\t"
    "s_setprio 0\n\t"
    "L_tp19_%=:\n\t"
    "s_waitcnt vmcnt(3) lgkmcnt(4)\n\t"
    "v_smfmac_f32_16x16x64_bf16 v[116:119], v[120:123], v[92:99], v124\n\t"
    "ds_read_b64 v[92:93], v110 offset:0\n\t"
    "ds_read_b64 v[94:95], v126 offset:0\n\t"
    "v_add_u32_sdwa v98, s92, v111 dst_sel:DWORD dst_unused:UNUSED_PAD src0_sel:DWORD src1_sel:WORD_1\n\t"
    "v_add_u32_sdwa v111, s92, v111 dst_sel:DWORD dst_unused:UNUSED_PAD src0_sel:DWORD src1_sel:WORD_0\n\t"
    "ds_read_b64 v[96:97], v111 offset:0\n\t"
    "ds_read_b64 v[98:99], v98 offset:0\n\t"
    "s_add_u32 s91, s91, s95\n\t"
    "buffer_load_dwordx4 v[108:111], v127, %12, s91 offen offset:0 sc1\n\t"
    "v_add_u32_sdwa v126, s92, v112 dst_sel:DWORD dst_unused:UNUSED_PAD src0_sel:DWORD src1_sel:WORD_1\n\t"
    "v_cvt_pk_bf16_f32 v38, v116, v117\n\t"
    "v_cvt_pk_bf16_f32 v39, v118, v119\n\t"
    "v_and_b32 v119, s67, v45\n\t"
    "v_lshlrev_b32 v118, 16, v45\n\t"
    "v_and_b32 v117, s67, v44\n\t"
    "v_lshlrev_b32 v116, 16, v44\n\t"
    "v_add_u32_sdwa v112, s92, v112 dst_sel:DWORD dst_unused:UNUSED_PAD src0_sel:DWORD src1_sel:WORD_0\n\t"
    "s_waitcnt lgkmcnt(4)\n\t"
    "v_smfmac_f32_16x16x64_bf16 v[116:119], v[120:123], v[84:91], v124\n\t"
    "ds_read_b64 v[84:85], v112 offset:0\n\t"
    "ds_read_b64 v[86:87], v126 offset:0\n\t"
    "v_cvt_pk_bf16_f32 v80, v80, v81\n\t"
    "v_cvt_pk_bf16_f32 v81, v82, v83\n\t"
    "ds_write_b64 v125, v[80:81] offset:640\n\t"
    "v_add_u32_sdwa v90, s92, v113 dst_sel:DWORD dst_unused:UNUSED_PAD src0_sel:DWORD src1_sel:WORD_1\n\t"
    "v_add_u32_sdwa v113, s92, v113 dst_sel:DWORD dst_unused:UNUSED_PAD src0_sel:DWORD src1_sel:WORD_0\n\t"
    "ds_read_b64 v[88:89], v113 offset:0\n\t"
    "ds_read_b64 v[90:91], v90 offset:0\n\t"
    "v_add_u32_sdwa v126, s92, v114 dst_sel:DWORD dst_unused:UNUSED_PAD src0_sel:DWORD src1_sel:WORD_1\n\t"
    "v_add_u32_sdwa v114, s92, v114 dst_sel:DWORD dst_unused:UNUSED_PAD src0_sel:DWORD src1_sel:WORD_0\n\t"
    "s_waitcnt vmcnt(2) lgkmcnt(5)\n\t"
    "v_smfmac_f32_16x16x64_bf16 v[116:119], v[120:123], v[92:99], v124\n\t"
    "ds_read_b64 v[92:93], v114 offset:0\n\t"
    "ds_read_b64 v[94:95], v126 offset:0\n\t"
    "v_add_u32_sdwa v98, s92, v115 dst_sel:DWORD dst_unused:UNUSED_PAD src0_sel:DWORD src1_sel:WORD_1\n\t"
    "v_add_u32_sdwa v115, s92, v115 dst_sel:DWORD dst_unused:UNUSED_PAD src0_sel:DWORD src1_sel:WORD_0\n\t"
    "ds_read_b64 v[96:97], v115 offset:0\n\t"
    "ds_read_b64 v[98:99], v98 offset:0\n\t"
    "buffer_load_dwordx4 v[112:115], v127, %12, s91 offen offset:1024 sc1\n\t"
    "v_add_u32_sdwa v126, s92, v100 dst_sel:DWORD dst_unused:UNUSED_PAD src0_sel:DWORD src1_sel:WORD_1\n\t"
    "v_cvt_pk_bf16_f32 v44, v116, v117\n\t"
    "v_cvt_pk_bf16_f32 v45, v118, v119\n\t"
    "v_and_b32 v119, s67, v47\n\t"
    "v_lshlrev_b32 v118, 16, v47\n\t"
    "v_and_b32 v117, s67, v46\n\t"
    "v_lshlrev_b32 v116, 16, v46\n\t"
    "v_add_u32_sdwa v100, s92, v100 dst_sel:DWORD dst_unused:UNUSED_PAD src0_sel:DWORD src1_sel:WORD_0\n\t"
    "s_waitcnt lgkmcnt(4)\n\t"
    "v_smfmac_f32_16x16x64_bf16 v[116:119], v[120:123], v[84:91], v124\n\t"
    "ds_read_b64 v[84:85], v100 offset:0\n\t"
    "v_mfma_f32_16x16x32_bf16 v[80:83], v[64:67], v[156:159], 0\n\t"
    "ds_read_b64 v[86:87], v126 offset:0\n\t"
    "v_mfma_f32_16x16x32_bf16 v[80:83], v[68:71], v[160:163], v[80:83]\n\t"
    "v_add_u32_sdwa v90, s92, v101 dst_sel:DWORD dst_unused:UNUSED_PAD src0_sel:DWORD src1_sel:WORD_1\n\t"
    "v_add_u32_sdwa v101, s92, v101 dst_sel:DWORD dst_unused:UNUSED_PAD src0_sel:DWORD src1_sel:WORD_0\n\t"
    "ds_read_b64 v[88:89], v101 offset:0\n\t"
    "v_mfma_f32_16x16x32_bf16 v[80:83], v[72:75], v[140:143], v[80:83]\n\t"
    "ds_read_b64 v[90:91], v90 offset:0\n\t"
    "v_mfma_f32_16x16x32_bf16 v[80:83], v[76:79], v[144:147], v[80:83]\n\t"
    "v_add_u32_sdwa v126, s92, v102 dst_sel:DWORD dst_unused:UNUSED_PAD src0_sel:DWORD src1_sel:WORD_1\n\t"
    "v_add_u32_sdwa v102, s92, v102 dst_sel:DWORD dst_unused:UNUSED_PAD src0_sel:DWORD src1_sel:WORD_0\n\t"
    "s_waitcnt vmcnt(2) lgkmcnt(4)\n\t"
    "v_smfmac_f32_16x16x64_bf16 v[116:119], v[120:123], v[92:99], v124\n\t"
    "ds_read_b64 v[92:93], v102 offset:0\n\t"
    "ds_read_b64 v[94:95], v126 offset:0\n\t"
    "v_add_u32_sdwa v98, s92, v103 dst_sel:DWORD dst_unused:UNUSED_PAD src0_sel:DWORD src1_sel:WORD_1\n\t"
    "v_add_u32_sdwa v103, s92, v103 dst_sel:DWORD dst_unused:UNUSED_PAD src0_sel:DWORD src1_sel:WORD_0\n\t"
    "ds_read_b64 v[96:97], v103 offset:0\n\t"
    "ds_read_b64 v[98:99], v98 offset:0\n\t"
    "s_add_u32 s84, s84, s94\n\t"
    "buffer_load_dwordx4 v[100:103], v127, %12, s84 offen offset:0 sc1\n\t"
    "v_add_u32_sdwa v126, s92, v104 dst_sel:DWORD dst_unused:UNUSED_PAD src0_sel:DWORD src1_sel:WORD_1\n\t"
    "v_cvt_pk_bf16_f32 v46, v116, v117\n\t"
    "v_cvt_pk_bf16_f32 v47, v118, v119\n\t"
    "v_and_b32 v119, s67, v53\n\t"
    "v_lshlrev_b32 v118, 16, v53\n\t"
    "v_and_b32 v117, s67, v52\n\t"
    "v_lshlrev_b32 v116, 16, v52\n\t"
    "v_add_u32_sdwa v104, s92, v104 dst_sel:DWORD dst_unused:UNUSED_PAD src0_sel:DWORD src1_sel:WORD_0\n\t"
    "s_waitcnt lgkmcnt(4)\n\t"
    "v_smfmac_f32_16x16x64_bf16 v[116:119], v[120:123], v[84:91], v124\n\t"
    "ds_read_b64 v[84:85], v104 offset:0\n\t"
    "ds_read_b64 v[86:87], v126 offset:0\n\t"
    "v_cvt_pk_bf16_f32 v80, v80, v81\n\t"
    "v_cvt_pk_bf16_f32 v81, v82, v83\n\t"
    "ds_write_b64 v125, v[80:81] offset:768\n\t"
    "v_add_u32_sdwa v90, s92, v105 dst_sel:DWORD dst_unused:UNUSED_PAD src0_sel:DWORD src1_sel:WORD_1\n\t"
    "v_add_u32_sdwa v105, s92, v105 dst_sel:DWORD dst_unused:UNUSED_PAD src0_sel:DWORD src1_sel:WORD_0\n\t"
    "ds_read_b64 v[88:89], v105 offset:0\n\t"
    "ds_read_b64 v[90:91], v90 offset:0\n\t"
    "v_add_u32_sdwa v126, s92, v106 dst_sel:DWORD dst_unused:UNUSED_PAD src0_sel:DWORD src1_sel:WORD_1\n\t"
    "v_add_u32_sdwa v106, s92, v106 dst_sel:DWORD dst_unused:UNUSED_PAD src0_sel:DWORD src1_sel:WORD_0\n\t"
    "s_waitcnt vmcnt(2) lgkmcnt(5)\n\t"
    "v_smfmac_f32_16x16x64_bf16 v[116:119], v[120:123], v[92:99], v124\n\t"
    "ds_read_b64 v[92:93], v106 offset:0\n\t"
    "ds_read_b64 v[94:95], v126 offset:0\n\t"
    "v_add_u32_sdwa v98, s92, v107 dst_sel:DWORD dst_unused:UNUSED_PAD src0_sel:DWORD src1_sel:WORD_1\n\t"
    "v_add_u32_sdwa v107, s92, v107 dst_sel:DWORD dst_unused:UNUSED_PAD src0_sel:DWORD src1_sel:WORD_0\n\t"
    "ds_read_b64 v[96:97], v107 offset:0\n\t"
    "ds_read_b64 v[98:99], v98 offset:0\n\t"
    "buffer_load_dwordx4 v[104:107], v127, %12, s84 offen offset:1024 sc1\n\t"
    "v_add_u32_sdwa v126, s92, v108 dst_sel:DWORD dst_unused:UNUSED_PAD src0_sel:DWORD src1_sel:WORD_1\n\t"
    "v_cvt_pk_bf16_f32 v52, v116, v117\n\t"
    "v_cvt_pk_bf16_f32 v53, v118, v119\n\t"
    "v_and_b32 v119, s67, v55\n\t"
    "v_lshlrev_b32 v118, 16, v55\n\t"
    "v_and_b32 v117, s67, v54\n\t"
    "v_lshlrev_b32 v116, 16, v54\n\t"
    "v_add_u32_sdwa v108, s92, v108 dst_sel:DWORD dst_unused:UNUSED_PAD src0_sel:DWORD src1_sel:WORD_0\n\t"
    "s_waitcnt lgkmcnt(4)\n\t"
    "v_smfmac_f32_16x16x64_bf16 v[116:119], v[120:123], v[84:91], v124\n\t"
    "ds_read_b64 v[84:85], v108 offset:0\n\t"
    "v_mfma_f32_16x16x32_bf16 v[80:83], v[64:67], v[212:215], 0\n\t"
    "ds_read_b64 v[86:87], v126 offset:0\n\t"
    "v_mfma_f32_16x16x32_bf16 v[80:83], v[68:71], v[216:219], v[80:83]\n\t"
    "v_add_u32_sdwa v90, s92, v109 dst_sel:DWORD dst_unused:UNUSED_PAD src0_sel:DWORD src1_sel:WORD_1\n\t"
    "v_add_u32_sdwa v109, s92, v109 dst_sel:DWORD dst_unused:UNUSED_PAD src0_sel:DWORD src1_sel:WORD_0\n\t"
    "ds_read_b64 v[88:89], v109 offset:0\n\t"
    "v_mfma_f32_16x16x32_bf16 v[80:83], v[72:75], v[148:151], v[80:83]\n\t"
    "ds_read_b64 v[90:91], v90 offset:0\n\t"
    "v_mfma_f32_16x16x32_bf16 v[80:83], v[76:79], v[152:155], v[80:83]\n\t"
    "v_add_u32 v79, s96, v127\n\t"
    "s_add_u32 s96, s96, 0x1000\n\t"
    "s_and_b32 s96, s96, 0x3000\n\t"
    "v_add_u32 v79, 128000, v79\n\t"
    "ds_read_b128 v[64:67], v79 offset:0\n\t"
    "ds_read_b128 v[68:71], v79 offset:1024\n\t"
    "ds_read_b128 v[72:75], v79 offset:2048\n\t"
    "ds_read_b128 v[76:79], v79 offset:3072\n\t"
    "v_add_u32_sdwa v126, s92, v110 dst_sel:DWORD dst_unused:UNUSED_PAD src0_sel:DWORD src1_sel:WORD_1\n\t"
    "v_add_u32_sdwa v110, s92, v110 dst_sel:DWORD dst_unused:UNUSED_PAD src0_sel:DWORD src1_sel:WORD_0\n\t"
    "s_waitcnt vmcnt(2) lgkmcnt(8)\n\t"
    "v_smfmac_f32_16x16x64_bf16 v[116:119], v[120:123], v[92:99], v124\n\t"
    "ds_read_b64 v[92:93], v110 offset:0\n\t"
    "ds_read_b64 v[94:95], v126 offset:0\n\t"
    "v_add_u32_sdwa v98, s92, v111 dst_sel:DWORD dst_unused:UNUSED_PAD src0_sel:DWORD src1_sel:WORD_1\n\t"
    "v_add_u32_sdwa v111, s92, v111 dst_sel:DWORD dst_unused:UNUSED_PAD src0_sel:DWORD src1_sel:WORD_0\n\t"
    "ds_read_b64 v[96:97], v111 offset:0\n\t"
    "ds_read_b64 v[98:99], v98 offset:0\n\t"
    "s_add_u32 s85, s85, s94\n\t"
    "buffer_load_dwordx4 v[108:111], v127, %12, s85 offen offset:0 sc1\n\t"
    "v_add_u32_sdwa v126, s92, v112 dst_sel:DWORD dst_unused:UNUSED_PAD src0_sel:DWORD src1_sel:WORD_1\n\t"
    "v_cvt_pk_bf16_f32 v54, v116, v117\n\t"
    "v_cvt_pk_bf16_f32 v55, v118, v119\n\t"
    "v_and_b32 v119, s67, v61\n\t"
    "v_lshlrev_b32 v118, 16, v61\n\t"
    "v_and_b32 v117, s67, v60\n\t"
    "v_lshlrev_b32 v116, 16, v60\n\t"
    "v_add_u32_sdwa v112, s92, v112 dst_sel:DWORD dst_unused:UNUSED_PAD src0_sel:DWORD src1_sel:WORD_0\n\t"
    "s_waitcnt lgkmcnt(8)\n\t"
    "v_smfmac_f32_16x16x64_bf16 v[116:119], v[120:123], v[84:91], v124\n\t"
    "ds_read_b64 v[84:85], v112 offset:0\n\t"
    "ds_read_b64 v[86:87], v126 offset:0\n\t"
    "v_cvt_pk_bf16_f32 v80, v80, v81\n\t"
    "v_cvt_pk_bf16_f32 v81, v82, v83\n\t"
    "ds_write_b64 v125, v[80:81] offset:872\n\t"
    "v_add_u32_sdwa v90, s92, v113 dst_sel:DWORD dst_unused:UNUSED_PAD src0_sel:DWORD src1_sel:WORD_1\n\t"
    "v_add_u32_sdwa v113, s92, v113 dst_sel:DWORD dst_unused:UNUSED_PAD src0_sel:DWORD src1_sel:WORD_0\n\t"
    "ds_read_b64 v[88:89], v113 offset:0\n\t"
    "ds_read_b64 v[90:91], v90 offset:0\n\t"
    "v_add_u32_sdwa v126, s92, v114 dst_sel:DWORD dst_unused:UNUSED_PAD src0_sel:DWORD src1_sel:WORD_1\n\t"
    "v_add_u32_sdwa v114, s92, v114 dst_sel:DWORD dst_unused:UNUSED_PAD src0_sel:DWORD src1_sel:WORD_0\n\t"
    "s_waitcnt vmcnt(2) lgkmcnt(5)\n\t"
    "v_smfmac_f32_16x16x64_bf16 v[116:119], v[120:123], v[92:99], v124\n\t"
    "ds_read_b64 v[92:93], v114 offset:0\n\t"
    "ds_read_b64 v[94:95], v126 offset:0\n\t"
    "v_add_u32_sdwa v98, s92, v115 dst_sel:DWORD dst_unused:UNUSED_PAD src0_sel:DWORD src1_sel:WORD_1\n\t"
    "v_add_u32_sdwa v115, s92, v115 dst_sel:DWORD dst_unused:UNUSED_PAD src0_sel:DWORD src1_sel:WORD_0\n\t"
    "ds_read_b64 v[96:97], v115 offset:0\n\t"
    "ds_read_b64 v[98:99], v98 offset:0\n\t"
    "buffer_load_dwordx4 v[112:115], v127, %12, s85 offen offset:1024 sc1\n\t"
    "v_add_u32_sdwa v126, s92, v100 dst_sel:DWORD dst_unused:UNUSED_PAD src0_sel:DWORD src1_sel:WORD_1\n\t"
    "v_cvt_pk_bf16_f32 v60, v116, v117\n\t"
    "v_cvt_pk_bf16_f32 v61, v118, v119\n\t"
    "v_and_b32 v119, s67, v63\n\t"
    "v_lshlrev_b32 v118, 16, v63\n\t"
    "v_and_b32 v117, s67, v62\n\t"
    "v_lshlrev_b32 v116, 16, v62\n\t"
    "v_add_u32_sdwa v100, s92, v100 dst_sel:DWORD dst_unused:UNUSED_PAD src0_sel:DWORD src1_sel:WORD_0\n\t"
    "s_waitcnt lgkmcnt(4)\n\t"
    "v_smfmac_f32_16x16x64_bf16 v[116:119], v[120:123], v[84:91], v124\n\t"
    "ds_read_b64 v[84:85], v100 offset:0\n\t"
    "ds_read_b64 v[86:87], v126 offset:0\n\t"
    "v_add_u32_sdwa v90, s92, v101 dst_sel:DWORD dst_unused:UNUSED_PAD src0_sel:DWORD src1_sel:WORD_1\n\t"
    "v_add_u32_sdwa v101, s92, v101 dst_sel:DWORD dst_unused:UNUSED_PAD src0_sel:DWORD src1_sel:WORD_0\n\t"
    "ds_read_b64 v[88:89], v101 offset:0\n\t"
    "ds_read_b64 v[90:91], v90 offset:0\n\t"
    "v_add_u32_sdwa v126, s92, v102 dst_sel:DWORD dst_unused:UNUSED_PAD src0_sel:DWORD src1_sel:WORD_1\n\t"
    "v_add_u32_sdwa v102, s92, v102 dst_sel:DWORD dst_unused:UNUSED_PAD src0_sel:DWORD src1_sel:WORD_0\n\t"
    "s_cmp_lt_u32 %16, 4\n\t"
    "s_cbranch_scc1 L_tp31_%=\n\t"
    "s_setprio 1\n\t"
    "L_tp31_%=:\n\t"
    "s_waitcnt vmcnt(2) lgkmcnt(4)\n\t"
    "v_smfmac_f32_16x16x64_bf16 v[116:119], v[120:123], v[92:99], v124\n\t"
    "ds_read_b64 v[92:93], v102 offset:0\n\t"
    "ds_read_b64 v[94:95], v126 offset:0\n\t"
    "v_add_u32_sdwa v98, s92, v103 dst_sel:DWORD dst_unused:UNUSED_PAD src0_sel:DWORD src1_sel:WORD_1\n\t"
    "v_add_u32_sdwa v103, s92, v103 dst_sel:DWORD dst_unused:UNUSED_PAD src0_sel:DWORD src1_sel:WORD_0\n\t"
    "ds_read_b64 v[96:97], v103 offset:0\n\t"
    "ds_read_b64 v[98:99], v98 offset:0\n\t"
    "s_add_u32 s86, s86, s94\n\t"
    "buffer_load_dwordx4 v[100:103], v127, %12, s86 offen offset:0 sc1\n\t"
    "v_add_u32_sdwa v126, s92, v104 dst_sel:DWORD dst_unused:UNUSED_PAD src0_sel:DWORD src1_sel:WORD_1\n\t"
    "v_cvt_pk_bf16_f32 v62, v116, v117\n\t"
    "v_cvt_pk_bf16_f32 v63, v118, v119\n\t"
    "v_and_b32 v119, s67, v1\n\t"
    "v_lshlrev_b32 v118, 16, v1\n\t"
    "v_and_b32 v117, s67, v0\n\t"
    "v_lshlrev_b32 v116, 16, v0\n\t"
    "v_add_u32_sdwa v104, s92, v104 dst_sel:DWORD dst_unused:UNUSED_PAD src0_sel:DWORD src1_sel:WORD_0\n\t"
    "s_waitcnt lgkmcnt(4)\n\t"
    "v_smfmac_f32_16x16x64_bf16 v[116:119], v[120:123], v[84:91], v124\n\t"
    "ds_read_b64 v[84:85], v104 offset:0\n\t"
    "v_mfma_f32_16x16x32_bf16 v[80:83], v[64:67], v[164:167], 0\n\t"
    "ds_read_b64 v[86:87], v126 offset:0\n\t"
    "v_mfma_f32_16x16x32_bf16 v[80:83], v[68:71], v[168:171], v[80:83]\n\t"
    "v_add_u32_sdwa v90, s92, v105 dst_sel:DWORD dst_unused:UNUSED_PAD src0_sel:DWORD src1_sel:WORD_1\n\t"
    "v_add_u32_sdwa v105, s92, v105 dst_sel:DWORD dst_unused:UNUSED_PAD src0_sel:DWORD src1_sel:WORD_0\n\t"
    "ds_read_b64 v[88:89], v105 offset:0\n\t"
    "v_mfma_f32_16x16x32_bf16 v[80:83], v[72:75], v[220:223], v[80:83]\n\t"
    "ds_read_b64 v[90:91], v90 offset:0\n\t"
    "v_mfma_f32_16x16x32_bf16 v[80:83], v[76:79], v[224:227], v[80:83]\n\t"
    "v_add_u32_sdwa v126, s92, v106 dst_sel:DWORD dst_unused:UNUSED_PAD src0_sel:DWORD src1_sel:WORD_1\n\t"
    "v_add_u32_sdwa v106, s92, v106 dst_sel:DWORD dst_unused:UNUSED_PAD src0_sel:DWORD src1_sel:WORD_0\n\t"
    "s_waitcnt vmcnt(2) lgkmcnt(4)\n\t"
    "v_smfmac_f32_16x16x64_bf16 v[116:119], v[120:123], v[92:99], v124\n\t"
    "ds_read_b64 v[92:93], v106 offset:0\n\t"
    "ds_read_b64 v[94:95], v126 offset:0\n\t"
    "v_add_u32_sdwa v98, s92, v107 dst_sel:DWORD dst_unused:UNUSED_PAD src0_sel:DWORD src1_sel:WORD_1\n\t"
    "v_add_u32_sdwa v107, s92, v107 dst_sel:DWORD dst_unused:UNUSED_PAD src0_sel:DWORD src1_sel:WORD_0\n\t"
    "ds_read_b64 v[96:97], v107 offset:0\n\t"
    "ds_read_b64 v[98:99], v98 offset:0\n\t"
    "buffer_load_dwordx4 v[104:107], v127, %12, s86 offen offset:1024 sc1\n\t"
    "v_add_u32_sdwa v126, s92, v108 dst_sel:DWORD dst_unused:UNUSED_PAD src0_sel:DWORD src1_sel:WORD_1\n\t"
    "v_cvt_pk_bf16_f32 v0, v116, v117\n\t"
    "v_cvt_pk_bf16_f32 v1, v118, v119\n\t"
    "v_and_b32 v119, s67, v3\n\t"
    "v_lshlrev_b32 v118, 16, v3\n\t"
    "v_and_b32 v117, s67, v2\n\t"
    "v_lshlrev_b32 v116, 16, v2\n\t"
    "v_add_u32_sdwa v108, s92, v108 dst_sel:DWORD dst_unused:UNUSED_PAD src0_sel:DWORD src1_sel:WORD_0\n\t"
    "s_waitcnt lgkmcnt(4)\n\t"
    "v_smfmac_f32_16x16x64_bf16 v[116:119], v[120:123], v[84:91], v124\n\t"
    "ds_read_b64 v[84:85], v108 offset:0\n\t"
    "ds_read_b64 v[86:87], v126 offset:0\n\t"
    "v_cvt_pk_bf16_f32 v80, v80, v81\n\t"
    "v_cvt_pk_bf16_f32 v81, v82, v83\n\t"
    "ds_write_b64 v125, v[80:81] offset:32000\n\t"
    "v_mfma_f32_16x16x32_bf16 v[80:83], v[64:67], v[172:175], 0\n\t"
    "v_add_u32_sdwa v90, s92, v109 dst_sel:DWORD dst_unused:UNUSED_PAD src0_sel:DWORD src1_sel:WORD_1\n\t"
    "v_add_u32_sdwa v109, s92, v109 dst_sel:DWORD dst_unused:UNUSED_PAD src0_sel:DWORD src1_sel:WORD_0\n\t"
    "ds_read_b64 v[88:89], v109 offset:0\n\t"
    "v_mfma_f32_16x16x32_bf16 v[80:83], v[68:71], v[176:179], v[80:83]\n\t"
    "ds_read_b64 v[90:91], v90 offset:0\n\t"
    "v_mfma_f32_16x16x32_bf16 v[80:83], v[72:75], v[228:231], v[80:83]\n\t"
    "v_mfma_f32_16x16x32_bf16 v[80:83], v[76:79], v[232:235], v[80:83]\n\t"
    "v_add_u32_sdwa v126, s92, v110 dst_sel:DWORD dst_unused:UNUSED_PAD src0_sel:DWORD src1_sel:WORD_1\n\t"
    "v_add_u32_sdwa v110, s92, v110 dst_sel:DWORD dst_unused:UNUSED_PAD src0_sel:DWORD src1_sel:WORD_0\n\t"
    "s_waitcnt vmcnt(2) lgkmcnt(5)\n\t"
    "v_smfmac_f32_16x16x64_bf16 v[116:119], v[120:123], v[92:99], v124\n\t"
    "ds_read_b64 v[92:93], v110 offset:0\n\t"
    "ds_read_b64 v[94:95], v126 offset:0\n\t"
    "v_add_u32_sdwa v98, s92, v111 dst_sel:DWORD dst_unused:UNUSED_PAD src0_sel:DWORD src1_sel:WORD_1\n\t"
    "v_add_u32_sdwa v111, s92, v111 dst_sel:DWORD dst_unused:UNUSED_PAD src0_sel:DWORD src1_sel:WORD_0\n\t"
    "ds_read_b64 v[96:97], v111 offset:0\n\t"
    "ds_read_b64 v[98:99], v98 offset:0\n\t"
    "s_add_u32 s87, s87, s94\n\t"
    "buffer_load_dwordx4 v[108:111], v127, %12, s87 offen offset:0 sc1\n\t"
    "v_add_u32_sdwa v126, s92, v112 dst_sel:DWORD dst_unused:UNUSED_PAD src0_sel:DWORD src1_sel:WORD_1\n\t"
    "v_cvt_pk_bf16_f32 v2, v116, v117\n\t"
    "v_cvt_pk_bf16_f32 v3, v118, v119\n\t"
    "v_and_b32 v119, s67, v9\n\t"
    "v_lshlrev_b32 v118, 16, v9\n\t"
    "v_and_b32 v117, s67, v8\n\t"
    "v_lshlrev_b32 v116, 16, v8\n\t"
    "v_add_u32_sdwa v112, s92, v112 dst_sel:DWORD dst_unused:UNUSED_PAD src0_sel:DWORD src1_sel:WORD_0\n\t"
    "s_waitcnt lgkmcnt(4)\n\t"
    "v_smfmac_f32_16x16x64_bf16 v[116:119], v[120:123], v[84:91], v124\n\t"
    "ds_read_b64 v[84:85], v112 offset:0\n\t"
    "ds_read_b64 v[86:87], v126 offset:0\n\t"
    "v_cvt_pk_bf16_f32 v80, v80, v81\n\t"
    "v_cvt_pk_bf16_f32 v81, v82, v83\n\t"
    "ds_write_b64 v125, v[80:81] offset:32128\n\t"
    "v_add_u32_sdwa v90, s92, v113 dst_sel:DWORD dst_unused:UNUSED_PAD src0_sel:DWORD src1_sel:WORD_1\n\t"
    "v_add_u32_sdwa v113, s92, v113 dst_sel:DWORD dst_unused:UNUSED_PAD src0_sel:DWORD src1_sel:WORD_0\n\t"
    "ds_read_b64 v[88:89], v113 offset:0\n\t"
    "ds_read_b64 v[90:91], v90 offset:0\n\t"
    "v_add_u32_sdwa v126, s92, v114 dst_sel:DWORD dst_unused:UNUSED_PAD src0_sel:DWORD src1_sel:WORD_1\n\t"
    "v_add_u32_sdwa v114, s92, v114 dst_sel:DWORD dst_unused:UNUSED_PAD src0_sel:DWORD src1_sel:WORD_0\n\t"
    "s_waitcnt vmcnt(2) lgkmcnt(5)\n\t"
    "v_smfmac_f32_16x16x64_bf16 v[116:119], v[120:123], v[92:99], v124\n\t"
    "ds_read_b64 v[92:93], v114 offset:0\n\t"
    "ds_read_b64 v[94:95], v126 offset:0\n\t"
    "v_add_u32_sdwa v98, s92, v115 dst_sel:DWORD dst_unused:UNUSED_PAD src0_sel:DWORD src1_sel:WORD_1\n\t"
    "v_add_u32_sdwa v115, s92, v115 dst_sel:DWORD dst_unused:UNUSED_PAD src0_sel:DWORD src1_sel:WORD_0\n\t"
    "ds_read_b64 v[96:97], v115 offset:0\n\t"
    "ds_read_b64 v[98:99], v98 offset:0\n\t"
    "buffer_load_dwordx4 v[112:115], v127, %12, s87 offen offset:1024 sc1\n\t"
    "v_add_u32_sdwa v126, s92, v100 dst_sel:DWORD dst_unused:UNUSED_PAD src0_sel:DWORD src1_sel:WORD_1\n\t"
    "v_cvt_pk_bf16_f32 v8, v116, v117\n\t"
    "v_cvt_pk_bf16_f32 v9, v118, v119\n\t"
    "v_and_b32 v119, s67, v11\n\t"
    "v_lshlrev_b32 v118, 16, v11\n\t"
    "v_and_b32 v117, s67, v10\n\t"
    "v_lshlrev_b32 v116, 16, v10\n\t"
    "v_add_u32_sdwa v100, s92, v100 dst_sel:DWORD dst_unused:UNUSED_PAD src0_sel:DWORD src1_sel:WORD_0\n\t"
    "s_waitcnt lgkmcnt(4)\n\t"
    "v_smfmac_f32_16x16x64_bf16 v[116:119], v[120:123], v[84:91], v124\n\t"
    "ds_read_b64 v[84:85], v100 offset:0\n\t"
    "v_mfma_f32_16x16x32_bf16 v[80:83], v[64:67], v[180:183], 0\n\t"
    "ds_read_b64 v[86:87], v126 offset:0\n\t"
    "v_mfma_f32_16x16x32_bf16 v[80:83], v[68:71], v[184:187], v[80:83]\n\t"
    "v_add_u32_sdwa v90, s92, v101 dst_sel:DWORD dst_unused:UNUSED_PAD src0_sel:DWORD src1_sel:WORD_1\n\t"
    "v_add_u32_sdwa v101, s92, v101 dst_sel:DWORD dst_unused:UNUSED_PAD src0_sel:DWORD src1_sel:WORD_0\n\t"
    "ds_read_b64 v[88:89], v101 offset:0\n\t"
    "v_mfma_f32_16x16x32_bf16 v[80:83], v[72:75], v[236:239], v[80:83]\n\t"
    "ds_read_b64 v[90:91], v90 offset:0\n\t"
    "v_mfma_f32_16x16x32_bf16 v[80:83], v[76:79], v[240:243], v[80:83]\n\t"
    "v_add_u32_sdwa v126, s92, v102 dst_sel:DWORD dst_unused:UNUSED_PAD src0_sel:DWORD src1_sel:WORD_1\n\t"
    "v_add_u32_sdwa v102, s92, v102 dst_sel:DWORD dst_unused:UNUSED_PAD src0_sel:DWORD src1_sel:WORD_0\n\t"
    "s_waitcnt vmcnt(2) lgkmcnt(4)\n\t"
    "v_smfmac_f32_16x16x64_bf16 v[116:119], v[120:123], v[92:99], v124\n\t"
    "ds_read_b64 v[92:93], v102 offset:0\n\t"
    "ds_read_b64 v[94:95], v126 offset:0\n\t"
    "v_add_u32_sdwa v98, s92, v103 dst_sel:DWORD dst_unused:UNUSED_PAD src0_sel:DWORD src1_sel:WORD_1\n\t"
    "v_add_u32_sdwa v103, s92, v103 dst_sel:DWORD dst_unused:UNUSED_PAD src0_sel:DWORD src1_sel:WORD_0\n\t"
    "ds_read_b64 v[96:97], v103 offset:0\n\t"
    "ds_read_b64 v[98:99], v98 offset:0\n\t"
    "s_add_u32 s88, s88, s94\n\t"
    "buffer_load_dwordx4 v[100:103], v127, %12, s88 offen offset:0 sc1\n\t"
    "v_add_u32_sdwa v126, s92, v104 dst_sel:DWORD dst_unused:UNUSED_PAD src0_sel:DWORD src1_sel:WORD_1\n\t"
    "v_cvt_pk_bf16_f32 v10, v116, v117\n\t"
    "v_cvt_pk_bf16_f32 v11, v118, v119\n\t"
    "v_and_b32 v119, s67, v17\n\t"
    "v_lshlrev_b32 v118, 16, v17\n\t"
    "v_and_b32 v117, s67, v16\n\t"
    "v_lshlrev_b32 v116, 16, v16\n\t"
    "v_add_u32_sdwa v104, s92, v104 dst_sel:DWORD dst_unused:UNUSED_PAD src0_sel:DWORD src1_sel:WORD_0\n\t"
    "s_waitcnt lgkmcnt(4)\n\t"
    "v_smfmac_f32_16x16x64_bf16 v[116:119], v[120:123], v[84:91], v124\n\t"
    "ds_read_b64 v[84:85], v104 offset:0\n\t"
    "ds_read_b64 v[86:87], v126 offset:0\n\t"
    "v_cvt_pk_bf16_f32 v80, v80, v81\n\t"
    "v_cvt_pk_bf16_f32 v81, v82, v83\n\t"
    "ds_write_b64 v125, v[80:81] offset:32256\n\t"
    "v_add_u32_sdwa v90, s92, v105 dst_sel:DWORD dst_unused:UNUSED_PAD src0_sel:DWORD src1_sel:WORD_1\n\t"
    "v_add_u32_sdwa v105, s92, v105 dst_sel:DWORD dst_unused:UNUSED_PAD src0_sel:DWORD src1_sel:WORD_0\n\t"
    "ds_read_b64 v[88:89], v105 offset:0\n\t"
    "ds_read_b64 v[90:91], v90 offset:0\n\t"
    "v_add_u32_sdwa v126, s92, v106 dst_sel:DWORD dst_unused:UNUSED_PAD src0_sel:DWORD src1_sel:WORD_1\n\t"
    "v_add_u32_sdwa v106, s92, v106 dst_sel:DWORD dst_unused:UNUSED_PAD src0_sel:DWORD src1_sel:WORD_0\n\t"
    "s_waitcnt vmcnt(2) lgkmcnt(5)\n\t"
    "v_smfmac_f32_16x16x64_bf16 v[116:119], v[120:123], v[92:99], v124\n\t"
    "ds_read_b64 v[92:93], v106 offset:0\n\t"
    "ds_read_b64 v[94:95], v126 offset:0\n\t"
    "v_add_u32_sdwa v98, s92, v107 dst_sel:DWORD dst_unused:UNUSED_PAD src0_sel:DWORD src1_sel:WORD_1\n\t"
    "v_add_u32_sdwa v107, s92, v107 dst_sel:DWORD dst_unused:UNUSED_PAD src0_sel:DWORD src1_sel:WORD_0\n\t"
    "ds_read_b64 v[96:97], v107 offset:0\n\t"
    "ds_read_b64 v[98:99], v98 offset:0\n\t"
    "buffer_load_dwordx4 v[104:107], v127, %12, s88 offen offset:1024 sc1\n\t"
    "v_add_u32_sdwa v126, s92, v108 dst_sel:DWORD dst_unused:UNUSED_PAD src0_sel:DWORD src1_sel:WORD_1\n\t"
    "v_cvt_pk_bf16_f32 v16, v116, v117\n\t"
    "v_cvt_pk_bf16_f32 v17, v118, v119\n\t"
    "v_and_b32 v119, s67, v19\n\t"
    "v_lshlrev_b32 v118, 16, v19\n\t"
    "v_and_b32 v117, s67, v18\n\t"
    "v_lshlrev_b32 v116, 16, v18\n\t"
    "v_add_u32_sdwa v108, s92, v108 dst_sel:DWORD dst_unused:UNUSED_PAD src0_sel:DWORD src1_sel:WORD_0\n\t"
    "s_waitcnt lgkmcnt(4)\n\t"
    "v_smfmac_f32_16x16x64_bf16 v[116:119], v[120:123], v[84:91], v124\n\t"
    "ds_read_b64 v[84:85], v108 offset:0\n\t"
    "v_mfma_f32_16x16x32_bf16 v[80:83], v[64:67], v[188:191], 0\n\t"
    "ds_read_b64 v[86:87], v126 offset:0\n\t"
    "v_mfma_f32_16x16x32_bf16 v[80:83], v[68:71], v[192:195], v[80:83]\n\t"
    "v_add_u32_sdwa v90, s92, v109 dst_sel:DWORD dst_unused:UNUSED_PAD src0_sel:DWORD src1_sel:WORD_1\n\t"
    "v_add_u32_sdwa v109, s92, v109 dst_sel:DWORD dst_unused:UNUSED_PAD src0_sel:DWORD src1_sel:WORD_0\n\t"
    "ds_read_b64 v[88:89], v109 offset:0\n\t"
    "v_mfma_f32_16x16x32_bf16 v[80:83], v[72:75], v[244:247], v[80:83]\n\t"
    "ds_read_b64 v[90:91], v90 offset:0\n\t"
    "v_mfma_f32_16x16x32_bf16 v[80:83], v[76:79], v[248:251], v[80:83]\n\t"
    "v_add_u32_sdwa v126, s92, v110 dst_sel:DWORD dst_unused:UNUSED_PAD src0_sel:DWORD src1_sel:WORD_1\n\t"
    "v_add_u32_sdwa v110, s92, v110 dst_sel:DWORD dst_unused:UNUSED_PAD src0_sel:DWORD src1_sel:WORD_0\n\t"
    "s_waitcnt vmcnt(2) lgkmcnt(4)\n\t"
    "v_smfmac_f32_16x16x64_bf16 v[116:119], v[120:123], v[92:99], v124\n\t"
    "ds_read_b64 v[92:93], v110 offset:0\n\t"
    "ds_read_b64 v[94:95], v126 offset:0\n\t"
    "v_add_u32_sdwa v98, s92, v111 dst_sel:DWORD dst_unused:UNUSED_PAD src0_sel:DWORD src1_sel:WORD_1\n\t"
    "v_add_u32_sdwa v111, s92, v111 dst_sel:DWORD dst_unused:UNUSED_PAD src0_sel:DWORD src1_sel:WORD_0\n\t"
    "ds_read_b64 v[96:97], v111 offset:0\n\t"
    "ds_read_b64 v[98:99], v98 offset:0\n\t"
    "s_add_u32 s89, s89, s94\n\t"
    "buffer_load_dwordx4 v[108:111], v127, %12, s89 offen offset:0 sc1\n\t"
    "v_add_u32_sdwa v126, s92, v112 dst_sel:DWORD dst_unused:UNUSED_PAD src0_sel:DWORD src1_sel:WORD_1\n\t"
    "v_cvt_pk_bf16_f32 v18, v116, v117\n\t"
    "v_cvt_pk_bf16_f32 v19, v118, v119\n\t"
    "v_and_b32 v119, s67, v25\n\t"
    "v_lshlrev_b32 v118, 16, v25\n\t"
    "v_and_b32 v117, s67, v24\n\t"
    "v_lshlrev_b32 v116, 16, v24\n\t"
    "v_add_u32_sdwa v112, s92, v112 dst_sel:DWORD dst_unused:UNUSED_PAD src0_sel:DWORD src1_sel:WORD_0\n\t"
    "s_waitcnt lgkmcnt(4)\n\t"
    "v_smfmac_f32_16x16x64_bf16 v[116:119], v[120:123], v[84:91], v124\n\t"
    "ds_read_b64 v[84:85], v112 offset:0\n\t"
    "ds_read_b64 v[86:87], v126 offset:0\n\t"
    "v_cvt_pk_bf16_f32 v80, v80, v81\n\t"
    "v_cvt_pk_bf16_f32 v81, v82, v83\n\t"
    "ds_write_b64 v125, v[80:81] offset:32384\n\t"
    "v_add_u32_sdwa v90, s92, v113 dst_sel:DWORD dst_unused:UNUSED_PAD src0_sel:DWORD src1_sel:WORD_1\n\t"
    "v_add_u32_sdwa v113, s92, v113 dst_sel:DWORD dst_unused:UNUSED_PAD src0_sel:DWORD src1_sel:WORD_0\n\t"
    "ds_read_b64 v[88:89], v113 offset:0\n\t"
    "ds_read_b64 v[90:91], v90 offset:0\n\t"
    "v_add_u32_sdwa v126, s92, v114 dst_sel:DWORD dst_unused:UNUSED_PAD src0_sel:DWORD src1_sel:WORD_1\n\t"
    "v_add_u32_sdwa v114, s92, v114 dst_sel:DWORD dst_unused:UNUSED_PAD src0_sel:DWORD src1_sel:WORD_0\n\t"
    "s_waitcnt vmcnt(2) lgkmcnt(5)\n\t"
    "v_smfmac_f32_16x16x64_bf16 v[116:119], v[120:123], v[92:99], v124\n\t"
    "ds_read_b64 v[92:93], v114 offset:0\n\t"
    "ds_read_b64 v[94:95], v126 offset:0\n\t"
    "v_add_u32_sdwa v98, s92, v115 dst_sel:DWORD dst_unused:UNUSED_PAD src0_sel:DWORD src1_sel:WORD_1\n\t"
    "v_add_u32_sdwa v115, s92, v115 dst_sel:DWORD dst_unused:UNUSED_PAD src0_sel:DWORD src1_sel:WORD_0\n\t"
    "ds_read_b64 v[96:97], v115 offset:0\n\t"
    "ds_read_b64 v[98:99], v98 offset:0\n\t"
    "buffer_load_dwordx4 v[112:115], v127, %12, s89 offen offset:1024 sc1\n\t"
    "v_add_u32_sdwa v126, s92, v100 dst_sel:DWORD dst_unused:UNUSED_PAD src0_sel:DWORD src1_sel:WORD_1\n\t"
    "v_cvt_pk_bf16_f32 v24, v116, v117\n\t"
    "v_cvt_pk_bf16_f32 v25, v118, v119\n\t"
    "v_and_b32 v119, s67, v27\n\t"
    "v_lshlrev_b32 v118, 16, v27\n\t"
    "v_and_b32 v117, s67, v26\n\t"
    "v_lshlrev_b32 v116, 16, v26\n\t"
    "v_add_u32_sdwa v100, s92, v100 dst_sel:DWORD dst_unused:UNUSED_PAD src0_sel:DWORD src1_sel:WORD_0\n\t"
    "s_waitcnt lgkmcnt(4)\n\t"
    "v_smfmac_f32_16x16x64_bf16 v[116:119], v[120:123], v[84:91], v124\n\t"
    "ds_read_b64 v[84:85], v100 offset:0\n\t"
    "v_mfma_f32_16x16x32_bf16 v[80:83], v[64:67], v[196:199], 0\n\t"
    "ds_read_b64 v[86:87], v126 offset:0\n\t"
    "v_mfma_f32_16x16x32_bf16 v[80:83], v[68:71], v[200:203], v[80:83]\n\t"
    "v_add_u32_sdwa v90, s92, v101 dst_sel:DWORD dst_unused:UNUSED_PAD src0_sel:DWORD src1_sel:WORD_1\n\t"
    "v_add_u32_sdwa v101, s92, v101 dst_sel:DWORD dst_unused:UNUSED_PAD src0_sel:DWORD src1_sel:WORD_0\n\t"
    "ds_read_b64 v[88:89], v101 offset:0\n\t"
    "v_mfma_f32_16x16x32_bf16 v[80:83], v[72:75], v[252:255], v[80:83]\n\t"
    "ds_read_b64 v[90:91], v90 offset:0\n\t"
    "v_mfma_f32_16x16x32_bf16 v[80:83], v[76:79], v[128:131], v[80:83]\n\t"
    "v_add_u32_sdwa v126, s92, v102 dst_sel:DWORD dst_unused:UNUSED_PAD src0_sel:DWORD src1_sel:WORD_1\n\t"
    "v_add_u32_sdwa v102, s92, v102 dst_sel:DWORD dst_unused:UNUSED_PAD src0_sel:DWORD src1_sel:WORD_0\n\t"
    "s_waitcnt vmcnt(2) lgkmcnt(4)\n\t"
    "v_smfmac_f32_16x16x64_bf16 v[116:119], v[120:123], v[92:99], v124\n\t"
    "ds_read_b64 v[92:93], v102 offset:0\n\t"
    "ds_read_b64 v[94:95], v126 offset:0\n\t"
    "v_add_u32_sdwa v98, s92, v103 dst_sel:DWORD dst_unused:UNUSED_PAD src0_sel:DWORD src1_sel:WORD_1\n\t"
    "v_add_u32_sdwa v103, s92, v103 dst_sel:DWORD dst_unused:UNUSED_PAD src0_sel:DWORD src1_sel:WORD_0\n\t"
    "ds_read_b64 v[96:97], v103 offset:0\n\t"
    "ds_read_b64 v[98:99], v98 offset:0\n\t"
    "s_add_u32 s90, s90, s94\n\t"
    "buffer_load_dwordx4 v[100:103], v127, %12, s90 offen offset:0 sc1\n\t"
    "v_add_u32_sdwa v126, s92, v104 dst_sel:DWORD dst_unused:UNUSED_PAD src0_sel:DWORD src1_sel:WORD_1\n\t"
    "v_cvt_pk_bf16_f32 v26, v116, v117\n\t"
    "v_cvt_pk_bf16_f32 v27, v118, v119\n\t"
    "v_and_b32 v119, s67, v33\n\t"
    "v_lshlrev_b32 v118, 16, v33\n\t"
    "v_and_b32 v117, s67, v32\n\t"
    "v_lshlrev_b32 v116, 16, v32\n\t"
    "v_add_u32_sdwa v104, s92, v104 dst_sel:DWORD dst_unused:UNUSED_PAD src0_sel:DWORD src1_sel:WORD_0\n\t"
    "s_waitcnt lgkmcnt(4)\n\t"
    "v_smfmac_f32_16x16x64_bf16 v[116:119], v[120:123], v[84:91], v124\n\t"
    "ds_read_b64 v[84:85], v104 offset:0\n\t"
    "ds_read_b64 v[86:87], v126 offset:0\n\t"
    "v_cvt_pk_bf16_f32 v80, v80, v81\n\t"
    "v_cvt_pk_bf16_f32 v81, v82, v83\n\t"
    "ds_write_b64 v125, v[80:81] offset:32512\n\t"
    "v_add_u32_sdwa v90, s92, v105 dst_sel:DWORD dst_unused:UNUSED_PAD src0_sel:DWORD src1_sel:WORD_1\n\t"
    "v_add_u32_sdwa v105, s92, v105 dst_sel:DWORD dst_unused:UNUSED_PAD src0_sel:DWORD src1_sel:WORD_0\n\t"
    "ds_read_b64 v[88:89], v105 offset:0\n\t"
    "ds_read_b64 v[90:91], v90 offset:0\n\t"
    "v_add_u32_sdwa v126, s92, v106 dst_sel:DWORD dst_unused:UNUSED_PAD src0_sel:DWORD src1_sel:WORD_1\n\t"
    "v_add_u32_sdwa v106, s92, v106 dst_sel:DWORD dst_unused:UNUSED_PAD src0_sel:DWORD src1_sel:WORD_0\n\t"
    "s_waitcnt vmcnt(2) lgkmcnt(5)\n\t"
    "v_smfmac_f32_16x16x64_bf16 v[116:119], v[120:123], v[92:99], v124\n\t"
    "ds_read_b64 v[92:93], v106 offset:0\n\t"
    "ds_read_b64 v[94:95], v126 offset:0\n\t"
    "v_add_u32_sdwa v98, s92, v107 dst_sel:DWORD dst_unused:UNUSED_PAD src0_sel:DWORD src1_sel:WORD_1\n\t"
    "v_add_u32_sdwa v107, s92, v107 dst_sel:DWORD dst_unused:UNUSED_PAD src0_sel:DWORD src1_sel:WORD_0\n\t"
    "ds_read_b64 v[96:97], v107 offset:0\n\t"
    "ds_read_b64 v[98:99], v98 offset:0\n\t"
    "buffer_load_dwordx4 v[104:107], v127, %12, s90 offen offset:1024 sc1\n\t"
    "v_add_u32_sdwa v126, s92, v108 dst_sel:DWORD dst_unused:UNUSED_PAD src0_sel:DWORD src1_sel:WORD_1\n\t"
    "v_cvt_pk_bf16_f32 v32, v116, v117\n\t"
    "v_cvt_pk_bf16_f32 v33, v118, v119\n\t"
    "v_and_b32 v119, s67, v35\n\t"
    "v_lshlrev_b32 v118, 16, v35\n\t"
    "v_and_b32 v117, s67, v34\n\t"
    "v_lshlrev_b32 v116, 16, v34\n\t"
    "v_add_u32_sdwa v108, s92, v108 dst_sel:DWORD dst_unused:UNUSED_PAD src0_sel:DWORD src1_sel:WORD_0\n\t"
    "s_waitcnt lgkmcnt(4)\n\t"
    "v_smfmac_f32_16x16x64_bf16 v[116:119], v[120:123], v[84:91], v124\n\t"
    "ds_read_b64 v[84:85], v108 offset:0\n\t"
    "v_mfma_f32_16x16x32_bf16 v[80:83], v[64:67], v[204:207], 0\n\t"
    "ds_read_b64 v[86:87], v126 offset:0\n\t"
    "v_mfma_f32_16x16x32_bf16 v[80:83], v[68:71], v[208:211], v[80:83]\n\t"
    "v_add_u32_sdwa v90, s92, v109 dst_sel:DWORD dst_unused:UNUSED_PAD src0_sel:DWORD src1_sel:WORD_1\n\t"
    "v_add_u32_sdwa v109, s92, v109 dst_sel:DWORD dst_unused:UNUSED_PAD src0_sel:DWORD src1_sel:WORD_0\n\t"
    "ds_read_b64 v[88:89], v109 offset:0\n\t"
    "v_mfma_f32_16x16x32_bf16 v[80:83], v[72:75], v[132:135], v[80:83]\n\t"
    "ds_read_b64 v[90:91], v90 offset:0\n\t"
    "v_mfma_f32_16x16x32_bf16 v[80:83], v[76:79], v[136:139], v[80:83]\n\t"
    "v_add_u32_sdwa v126, s92, v110 dst_sel:DWORD dst_unused:UNUSED_PAD src0_sel:DWORD src1_sel:WORD_1\n\t"
    "v_add_u32_sdwa v110, s92, v110 dst_sel:DWORD dst_unused:UNUSED_PAD src0_sel:DWORD src1_sel:WORD_0\n\t"
    "s_cmp_lt_u32 %16, 4\n\t"
    "s_cbranch_scc1 L_tp51_%=\n\t"
    "s_setprio 0\n\t"
    "L_tp51_%=:\n\t"
    "s_waitcnt vmcnt(2) lgkmcnt(4)\n\t"
    "v_smfmac_f32_16x16x64_bf16 v[116:119], v[120:123], v[92:99], v124\n\t"
    "ds_read_b64 v[92:93], v110 offset:0\n\t"
    "ds_read_b64 v[94:95], v126 offset:0\n\t"
    "v_add_u32_sdwa v98, s92, v111 dst_sel:DWORD dst_unused:UNUSED_PAD src0_sel:DWORD src1_sel:WORD_1\n\t"
    "v_add_u32_sdwa v111, s92, v111 dst_sel:DWORD dst_unused:UNUSED_PAD src0_sel:DWORD src1_sel:WORD_0\n\t"
    "ds_read_b64 v[96:97], v111 offset:0\n\t"
    "ds_read_b64 v[98:99], v98 offset:0\n\t"
    "s_add_u32 s91, s91, s94\n\t"
    "buffer_load_dwordx4 v[108:111], v127, %12, s91 offen offset:0 sc1\n\t"
    "v_add_u32_sdwa v126, s92, v112 dst_sel:DWORD dst_unused:UNUSED_PAD src0_sel:DWORD src1_sel:WORD_1\n\t"
    "v_cvt_pk_bf16_f32 v34, v116, v117\n\t"
    "v_cvt_pk_bf16_f32 v35, v118, v119\n\t"
    "v_and_b32 v119, s67, v41\n\t"
    "v_lshlrev_b32 v118, 16, v41\n\t"
    "v_and_b32 v117, s67, v40\n\t"
    "v_lshlrev_b32 v116, 16, v40\n\t"
    "v_add_u32_sdwa v112, s92, v112 dst_sel:DWORD dst_unused:UNUSED_PAD src0_sel:DWORD src1_sel:WORD_0\n\t"
    "s_waitcnt lgkmcnt(4)\n\t"
    "v_smfmac_f32_16x16x64_bf16 v[116:119], v[120:123], v[84:91], v124\n\t"
    "ds_read_b64 v[84:85], v112 offset:0\n\t"
    "ds_read_b64 v[86:87], v126 offset:0\n\t"
    "v_cvt_pk_bf16_f32 v80, v80, v81\n\t"
    "v_cvt_pk_bf16_f32 v81, v82, v83\n\t"
    "ds_write_b64 v125, v[80:81] offset:32640\n\t"
    "v_add_u32_sdwa v90, s92, v113 dst_sel:DWORD dst_unused:UNUSED_PAD src0_sel:DWORD src1_sel:WORD_1\n\t"
    "v_add_u32_sdwa v113, s92, v113 dst_sel:DWORD dst_unused:UNUSED_PAD src0_sel:DWORD src1_sel:WORD_0\n\t"
    "ds_read_b64 v[88:89], v113 offset:0\n\t"
    "ds_read_b64 v[90:91], v90 offset:0\n\t"
    "v_add_u32_sdwa v126, s92, v114 dst_sel:DWORD dst_unused:UNUSED_PAD src0_sel:DWORD src1_sel:WORD_1\n\t"
    "v_add_u32_sdwa v114, s92, v114 dst_sel:DWORD dst_unused:UNUSED_PAD src0_sel:DWORD src1_sel:WORD_0\n\t"
    "s_waitcnt vmcnt(2) lgkmcnt(5)\n\t"
    "v_smfmac_f32_16x16x64_bf16 v[116:119], v[120:123], v[92:99], v124\n\t"
    "ds_read_b64 v[92:93], v114 offset:0\n\t"
    "ds_read_b64 v[94:95], v126 offset:0\n\t"
    "v_add_u32_sdwa v98, s92, v115 dst_sel:DWORD dst_unused:UNUSED_PAD src0_sel:DWORD src1_sel:WORD_1\n\t"
    "v_add_u32_sdwa v115, s92, v115 dst_sel:DWORD dst_unused:UNUSED_PAD src0_sel:DWORD src1_sel:WORD_0\n\t"
    "ds_read_b64 v[96:97], v115 offset:0\n\t"
    "ds_read_b64 v[98:99], v98 offset:0\n\t"
    "buffer_load_dwordx4 v[112:115], v127, %12, s91 offen offset:1024 sc1\n\t"
    "v_add_u32_sdwa v126, s92, v100 dst_sel:DWORD dst_unused:UNUSED_PAD src0_sel:DWORD src1_sel:WORD_1\n\t"
    "v_cvt_pk_bf16_f32 v40, v116, v117\n\t"
    "v_cvt_pk_bf16_f32 v41, v118, v119\n\t"
    "v_and_b32 v119, s67, v43\n\t"
    "v_lshlrev_b32 v118, 16, v43\n\t"
    "v_and_b32 v117, s67, v42\n\t"
    "v_lshlrev_b32 v116, 16, v42\n\t"
    "v_add_u32_sdwa v100, s92, v100 dst_sel:DWORD dst_unused:UNUSED_PAD src0_sel:DWORD src1_sel:WORD_0\n\t"
    "s_waitcnt lgkmcnt(4)\n\t"
    "v_smfmac_f32_16x16x64_bf16 v[116:119], v[120:123], v[84:91], v124\n\t"
    "ds_read_b64 v[84:85], v100 offset:0\n\t"
    "v_mfma_f32_16x16x32_bf16 v[80:83], v[64:67], v[156:159], 0\n\t"
    "ds_read_b64 v[86:87], v126 offset:0\n\t"
    "v_mfma_f32_16x16x32_bf16 v[80:83], v[68:71], v[160:163], v[80:83]\n\t"
    "v_add_u32_sdwa v90, s92, v101 dst_sel:DWORD dst_unused:UNUSED_PAD src0_sel:DWORD src1_sel:WORD_1\n\t"
    "v_add_u32_sdwa v101, s92, v101 dst_sel:DWORD dst_unused:UNUSED_PAD src0_sel:DWORD src1_sel:WORD_0\n\t"
    "ds_read_b64 v[88:89], v101 offset:0\n\t"
    "v_mfma_f32_16x16x32_bf16 v[80:83], v[72:75], v[140:143], v[80:83]\n\t"
    "ds_read_b64 v[90:91], v90 offset:0\n\t"
    "v_mfma_f32_16x16x32_bf16 v[80:83], v[76:79], v[144:147], v[80:83]\n\t"
    "v_add_u32_sdwa v126, s92, v102 dst_sel:DWORD dst_unused:UNUSED_PAD src0_sel:DWORD src1_sel:WORD_1\n\t"
    "v_add_u32_sdwa v102, s92, v102 dst_sel:DWORD dst_unused:UNUSED_PAD src0_sel:DWORD src1_sel:WORD_0\n\t"
    "s_waitcnt vmcnt(2) lgkmcnt(4)\n\t"
    "v_smfmac_f32_16x16x64_bf16 v[116:119], v[120:123], v[92:99], v124\n\t"
    "ds_read_b64 v[92:93], v102 offset:0\n\t"
    "ds_read_b64 v[94:95], v126 offset:0\n\t"
    "v_add_u32_sdwa v98, s92, v103 dst_sel:DWORD dst_unused:UNUSED_PAD src0_sel:DWORD src1_sel:WORD_1\n\t"
    "v_add_u32_sdwa v103, s92, v103 dst_sel:DWORD dst_unused:UNUSED_PAD src0_sel:DWORD src1_sel:WORD_0\n\t"
    "ds_read_b64 v[96:97], v103 offset:0\n\t"
    "ds_read_b64 v[98:99], v98 offset:0\n\t"
    "s_add_u32 s84, s84, s95\n\t"
    "buffer_load_dwordx4 v[100:103], v127, %12, s84 offen offset:0 sc1\n\t"
    "v_add_u32_sdwa v126, s92, v104 dst_sel:DWORD dst_unused:UNUSED_PAD src0_sel:DWORD src1_sel:WORD_1\n\t"
    "v_cvt_pk_bf16_f32 v42, v116, v117\n\t"
    "v_cvt_pk_bf16_f32 v43, v118, v119\n\t"
    "v_and_b32 v119, s67, v49\n\t"
    "v_lshlrev_b32 v118, 16, v49\n\t"
    "v_and_b32 v117, s67, v48\n\t"
    "v_lshlrev_b32 v116, 16, v48\n\t"
    "v_add_u32_sdwa v104, s92, v104 dst_sel:DWORD dst_unused:UNUSED_PAD src0_sel:DWORD src1_sel:WORD_0\n\t"
    "s_waitcnt lgkmcnt(4)\n\t"
    "v_smfmac_f32_16x16x64_bf16 v[116:119], v[120:123], v[84:91], v124\n\t"
    "ds_read_b64 v[84:85], v104 offset:0\n\t"
    "ds_read_b64 v[86:87], v126 offset:0\n\t"
    "v_cvt_pk_bf16_f32 v80, v80, v81\n\t"
    "v_cvt_pk_bf16_f32 v81, v82, v83\n\t"
    "ds_write_b64 v125, v[80:81] offset:32768\n\t"
    "v_add_u32_sdwa v90, s92, v105 dst_sel:DWORD dst_unused:UNUSED_PAD src0_sel:DWORD src1_sel:WORD_1\n\t"
    "v_add_u32_sdwa v105, s92, v105 dst_sel:DWORD dst_unused:UNUSED_PAD src0_sel:DWORD src1_sel:WORD_0\n\t"
    "ds_read_b64 v[88:89], v105 offset:0\n\t"
    "ds_read_b64 v[90:91], v90 offset:0\n\t"
    "v_add_u32_sdwa v126, s92, v106 dst_sel:DWORD dst_unused:UNUSED_PAD src0_sel:DWORD src1_sel:WORD_1\n\t"
    "v_add_u32_sdwa v106, s92, v106 dst_sel:DWORD dst_unused:UNUSED_PAD src0_sel:DWORD src1_sel:WORD_0\n\t"
    "s_waitcnt vmcnt(2) lgkmcnt(5)\n\t"
    "v_smfmac_f32_16x16x64_bf16 v[116:119], v[120:123], v[92:99], v124\n\t"
    "ds_read_b64 v[92:93], v106 offset:0\n\t"
    "ds_read_b64 v[94:95], v126 offset:0\n\t"
    "v_add_u32_sdwa v98, s92, v107 dst_sel:DWORD dst_unused:UNUSED_PAD src0_sel:DWORD src1_sel:WORD_1\n\t"
    "v_add_u32_sdwa v107, s92, v107 dst_sel:DWORD dst_unused:UNUSED_PAD src0_sel:DWORD src1_sel:WORD_0\n\t"
    "ds_read_b64 v[96:97], v107 offset:0\n\t"
    "ds_read_b64 v[98:99], v98 offset:0\n\t"
    "buffer_load_dwordx4 v[104:107], v127, %12, s84 offen offset:1024 sc1\n\t"
    "v_add_u32_sdwa v126, s92, v108 dst_sel:DWORD dst_unused:UNUSED_PAD src0_sel:DWORD src1_sel:WORD_1\n\t"
    "v_cvt_pk_bf16_f32 v48, v116, v117\n\t"
    "v_cvt_pk_bf16_f32 v49, v118, v119\n\t"
    "v_and_b32 v119, s67, v51\n\t"
    "v_lshlrev_b32 v118, 16, v51\n\t"
    "v_and_b32 v117, s67, v50\n\t"
    "v_lshlrev_b32 v116, 16, v50\n\t"
    "v_add_u32_sdwa v108, s92, v108 dst_sel:DWORD dst_unused:UNUSED_PAD src0_sel:DWORD src1_sel:WORD_0\n\t"
    "s_waitcnt lgkmcnt(4)\n\t"
    "v_smfmac_f32_16x16x64_bf16 v[116:119], v[120:123], v[84:91], v124\n\t"
    "ds_read_b64 v[84:85], v108 offset:0\n\t"
    "v_mfma_f32_16x16x32_bf16 v[80:83], v[64:67], v[212:215], 0\n\t"
    "ds_read_b64 v[86:87], v126 offset:0\n\t"
    "v_mfma_f32_16x16x32_bf16 v[80:83], v[68:71], v[216:219], v[80:83]\n\t"
    "v_add_u32_sdwa v90, s92, v109 dst_sel:DWORD dst_unused:UNUSED_PAD src0_sel:DWORD src1_sel:WORD_1\n\t"
    "v_add_u32_sdwa v109, s92, v109 dst_sel:DWORD dst_unused:UNUSED_PAD src0_sel:DWORD src1_sel:WORD_0\n\t"
    "ds_read_b64 v[88:89], v109 offset:0\n\t"
    "v_mfma_f32_16x16x32_bf16 v[80:83], v[72:75], v[148:151], v[80:83]\n\t"
    "ds_read_b64 v[90:91], v90 offset:0\n\t"
    "v_mfma_f32_16x16x32_bf16 v[80:83], v[76:79], v[152:155], v[80:83]\n\t"
    "v_add_u32_sdwa v126, s92, v110 dst_sel:DWORD dst_unused:UNUSED_PAD src0_sel:DWORD src1_sel:WORD_1\n\t"
    "v_add_u32_sdwa v110, s92, v110 dst_sel:DWORD dst_unused:UNUSED_PAD src0_sel:DWORD src1_sel:WORD_0\n\t"
    "s_waitcnt vmcnt(2) lgkmcnt(4)\n\t"
    "v_smfmac_f32_16x16x64_bf16 v[116:119], v[120:123], v[92:99], v124\n\t"
    "ds_read_b64 v[92:93], v110 offset:0\n\t"
    "ds_read_b64 v[94:95], v126 offset:0\n\t"
    "v_add_u32_sdwa v98, s92, v111 dst_sel:DWORD dst_unused:UNUSED_PAD src0_sel:DWORD src1_sel:WORD_1\n\t"
    "v_add_u32_sdwa v111, s92, v111 dst_sel:DWORD dst_unused:UNUSED_PAD src0_sel:DWORD src1_sel:WORD_0\n\t"
    "ds_read_b64 v[96:97], v111 offset:0\n\t"
    "ds_read_b64 v[98:99], v98 offset:0\n\t"
    "s_add_u32 s85, s85, s95\n\t"
    "buffer_load_dwordx4 v[108:111], v127, %12, s85 offen offset:0 sc1\n\t"
    "v_add_u32_sdwa v126, s92, v112 dst_sel:DWORD dst_unused:UNUSED_PAD src0_sel:DWORD src1_sel:WORD_1\n\t"
    "v_cvt_pk_bf16_f32 v50, v116, v117\n\t"
    "v_cvt_pk_bf16_f32 v51, v118, v119\n\t"
    "v_and_b32 v119, s67, v57\n\t"
    "v_lshlrev_b32 v118, 16, v57\n\t"
    "v_and_b32 v117, s67, v56\n\t"
    "v_lshlrev_b32 v116, 16, v56\n\t"
    "v_add_u32_sdwa v112, s92, v112 dst_sel:DWORD dst_unused:UNUSED_PAD src0_sel:DWORD src1_sel:WORD_0\n\t"
    "s_waitcnt lgkmcnt(4)\n\t"
    "v_smfmac_f32_16x16x64_bf16 v[116:119], v[120:123], v[84:91], v124\n\t"
    "ds_read_b64 v[84:85], v112 offset:0\n\t"
    "ds_read_b64 v[86:87], v126 offset:0\n\t"
    "v_cvt_pk_bf16_f32 v80, v80, v81\n\t"
    "v_cvt_pk_bf16_f32 v81, v82, v83\n\t"
    "ds_write_b64 v125, v[80:81] offset:32872\n\t"
    "v_add_u32_sdwa v90, s92, v113 dst_sel:DWORD dst_unused:UNUSED_PAD src0_sel:DWORD src1_sel:WORD_1\n\t"
    "v_add_u32_sdwa v113, s92, v113 dst_sel:DWORD dst_unused:UNUSED_PAD src0_sel:DWORD src1_sel:WORD_0\n\t"
    "ds_read_b64 v[88:89], v113 offset:0\n\t"
    "ds_read_b64 v[90:91], v90 offset:0\n\t"
    "v_add_u32_sdwa v126, s92, v114 dst_sel:DWORD dst_unused:UNUSED_PAD src0_sel:DWORD src1_sel:WORD_1\n\t"
    "v_add_u32_sdwa v114, s92, v114 dst_sel:DWORD dst_unused:UNUSED_PAD src0_sel:DWORD src1_sel:WORD_0\n\t"
    "s_waitcnt vmcnt(2) lgkmcnt(5)\n\t"
    "v_smfmac_f32_16x16x64_bf16 v[116:119], v[120:123], v[92:99], v124\n\t"
    "ds_read_b64 v[92:93], v114 offset:0\n\t"
    "ds_read_b64 v[94:95], v126 offset:0\n\t"
    "v_add_u32_sdwa v98, s92, v115 dst_sel:DWORD dst_unused:UNUSED_PAD src0_sel:DWORD src1_sel:WORD_1\n\t"
    "v_add_u32_sdwa v115, s92, v115 dst_sel:DWORD dst_unused:UNUSED_PAD src0_sel:DWORD src1_sel:WORD_0\n\t"
    "ds_read_b64 v[96:97], v115 offset:0\n\t"
    "ds_read_b64 v[98:99], v98 offset:0\n\t"
    "buffer_load_dwordx4 v[112:115], v127, %12, s85 offen offset:1024 sc1\n\t"
    "s_sub_u32 s92, 64000, s92\n\t"
    "v_add_u32_sdwa v126, s92, v100 dst_sel:DWORD dst_unused:UNUSED_PAD src0_sel:DWORD src1_sel:WORD_1\n\t"
    "v_cvt_pk_bf16_f32 v56, v116, v117\n\t"
    "v_cvt_pk_bf16_f32 v57, v118, v119\n\t"
    "v_and_b32 v119, s67, v59\n\t"
    "v_lshlrev_b32 v118, 16, v59\n\t"
    "v_and_b32 v117, s67, v58\n\t"
    "v_lshlrev_b32 v116, 16, v58\n\t"
    "v_add_u32_sdwa v100, s92, v100 dst_sel:DWORD dst_unused:UNUSED_PAD src0_sel:DWORD src1_sel:WORD_0\n\t"
    "s_waitcnt lgkmcnt(0)\n\t"
    "s_barrier\n\t"
    "v_add_u32 v125, s93, v125\n\t"
    "s_sub_u32 s93, 0, s93\n\t"
    "v_smfmac_f32_16x16x64_bf16 v[116:119], v[120:123], v[84:91], v124\n\t"
    "ds_read_b64 v[84:85], v100 offset:0\n\t"
    "ds_read_b64 v[86:87], v126 offset:0\n\t"
    "v_add_u32_sdwa v90, s92, v101 dst_sel:DWORD dst_unused:UNUSED_PAD src0_sel:DWORD src1_sel:WORD_1\n\t"
    "v_add_u32_sdwa v101, s92, v101 dst_sel:DWORD dst_unused:UNUSED_PAD src0_sel:DWORD src1_sel:WORD_0\n\t"
    "ds_read_b64 v[88:89], v101 offset:0\n\t"
    "ds_read_b64 v[90:91], v90 offset:0\n\t"
    "v_add_u32_sdwa v126, s92, v102 dst_sel:DWORD dst_unused:UNUSED_PAD src0_sel:DWORD src1_sel:WORD_1\n\t"
    "v_add_u32_sdwa v102, s92, v102 dst_sel:DWORD dst_unused:UNUSED_PAD src0_sel:DWORD src1_sel:WORD_0\n\t"
    "s_cmp_lt_u32 %16, 4\n\t"
    "s_cbranch_scc1 L_tp63_%=\n\t"
    "s_setprio 1\n\t"
    "L_tp63_%=:\n\t"
    "s_waitcnt vmcnt(2)\n\t"
    "v_smfmac_f32_16x16x64_bf16 v[116:119], v[120:123], v[92:99], v124\n\t"
    "ds_read_b64 v[92:93], v102 offset:0\n\t"
    "ds_read_b64 v[94:95], v126 offset:0\n\t"
    "v_add_u32_sdwa v98, s92, v103 dst_sel:DWORD dst_unused:UNUSED_PAD src0_sel:DWORD src1_sel:WORD_1\n\t"
    "v_add_u32_sdwa v103, s92, v103 dst_sel:DWORD dst_unused:UNUSED_PAD src0_sel:DWORD src1_sel:WORD_0\n\t"
    "ds_read_b64 v[96:97], v103 offset:0\n\t"
    "ds_read_b64 v[98:99], v98 offset:0\n\t"
    "s_add_u32 s86, s86, s95\n\t"
    "buffer_load_dwordx4 v[100:103], v127, %12, s86 offen offset:0 sc1\n\t"
    "v_add_u32_sdwa v126, s92, v104 dst_sel:DWORD dst_unused:UNUSED_PAD src0_sel:DWORD src1_sel:WORD_1\n\t"
    "v_cvt_pk_bf16_f32 v58, v116, v117\n\t"
    "v_cvt_pk_bf16_f32 v59, v118, v119\n\t"
    "v_and_b32 v119, s67, v5\n\t"
    "v_lshlrev_b32 v118, 16, v5\n\t"
    "v_and_b32 v117, s67, v4\n\t"
    "v_lshlrev_b32 v116, 16, v4\n\t"
    "v_add_u32_sdwa v104, s92, v104 dst_sel:DWORD dst_unused:UNUSED_PAD src0_sel:DWORD src1_sel:WORD_0\n\t"
    "v_add_u32 v79, s96, v127\n\t"
    "s_add_u32 s96, s96, 0x1000\n\t"
    "s_and_b32 s96, s96, 0x3000\n\t"
    "v_add_u32 v79, 128000, v79\n\t"
    "ds_read_b128 v[64:67], v79 offset:0\n\t"
    "ds_read_b128 v[68:71], v79 offset:1024\n\t"
    "ds_read_b128 v[72:75], v79 offset:2048\n\t"
    "ds_read_b128 v[76:79], v79 offset:3072\n\t"
    "s_cmp_eq_u32 s66, s48\n\t"
    "s_cbranch_scc1 L_pub_%=\n\t"
    "L_pubb_%=:\n\t"
    "s_sub_u32 s66, s66, 1\n\t"
    "s_cmp_lg_u32 s66, 0\n\t"
    "s_cbranch_scc1 L_loop_%=\n\t"
    "s_mov_b32 s92, 0xffff\n\t"
    "s_waitcnt lgkmcnt(8)\n\t"
    "v_smfmac_f32_16x16x64_bf16 v[116:119], v[120:123], v[84:91], v124\n\t"
    "ds_read_b64 v[84:85], v104 offset:0\n\t"
    "ds_read_b64 v[86:87], v126 offset:0\n\t"
    "v_lshrrev_b32 v90, 16, v105\n\t"
    "v_and_b32 v105, s92, v105\n\t"
    "ds_read_b64 v[88:89], v105 offset:0\n\t"
    "ds_read_b64 v[90:91], v90 offset:0\n\t"
    "v_lshrrev_b32 v126, 16, v106\n\t"
    "v_and_b32 v106, s92, v106\n\t"
    "s_waitcnt vmcnt(2) lgkmcnt(8)\n\t"
    "v_smfmac_f32_16x16x64_bf16 v[116:119], v[120:123], v[92:99], v124\n\t"
    "ds_read_b64 v[92:93], v106 offset:0\n\t"
    "s_waitcnt lgkmcnt(5)\n\t"
    "v_mfma_f32_16x16x32_bf16 v[80:83], v[64:67], v[164:167], 0\n\t"
    "ds_read_b64 v[94:95], v126 offset:0\n\t"
    "v_mfma_f32_16x16x32_bf16 v[80:83], v[68:71], v[168:171], v[80:83]\n\t"
    "v_lshrrev_b32 v98, 16, v107\n\t"
    "v_and_b32 v107, s92, v107\n\t"
    "ds_read_b64 v[96:97], v107 offset:0\n\t"
    "v_mfma_f32_16x16x32_bf16 v[80:83], v[72:75], v[220:223], v[80:83]\n\t"
    "ds_read_b64 v[98:99], v98 offset:0\n\t"
    "v_mfma_f32_16x16x32_bf16 v[80:83], v[76:79], v[224:227], v[80:83]\n\t"
    "v_lshrrev_b32 v126, 16, v108\n\t"
    "v_cvt_pk_bf16_f32 v4, v116, v117\n\t"
    "v_cvt_pk_bf16_f32 v5, v118, v119\n\t"
    "v_and_b32 v119, s67, v7\n\t"
    "v_lshlrev_b32 v118, 16, v7\n\t"
    "v_and_b32 v117, s67, v6\n\t"
    "v_lshlrev_b32 v116, 16, v6\n\t"
    "v_and_b32 v108, s92, v108\n\t"
    "s_waitcnt lgkmcnt(4)\n\t"
    "v_smfmac_f32_16x16x64_bf16 v[116:119], v[120:123], v[84:91], v124\n\t"
    "ds_read_b64 v[84:85], v108 offset:0\n\t"
    "v_mfma_f32_16x16x32_bf16 v[104:107], v[64:67], v[172:175], 0\n\t"
    "ds_read_b64 v[86:87], v126 offset:0\n\t"
    "v_mfma_f32_16x16x32_bf16 v[104:107], v[68:71], v[176:179], v[104:107]\n\t"
    "v_lshrrev_b32 v90, 16, v109\n\t"
    "v_and_b32 v109, s92, v109\n\t"
    "ds_read_b64 v[88:89], v109 offset:0\n\t"
    "v_mfma_f32_16x16x32_bf16 v[104:107], v[72:75], v[228:231], v[104:107]\n\t"
    "ds_read_b64 v[90:91], v90 offset:0\n\t"
    "v_mfma_f32_16x16x32_bf16 v[104:107], v[76:79], v[232:235], v[104:107]\n\t"
    "v_lshrrev_b32 v126, 16, v110\n\t"
    "v_cvt_pk_bf16_f32 v80, v80, v81\n\t"
    "v_cvt_pk_bf16_f32 v81, v82, v83\n\t"
    "ds_write_b64 v125, v[80:81] offset:0\n\t"
    "v_mfma_f32_16x16x32_bf16 v[80:83], v[64:67], v[180:183], 0\n\t"
    "v_and_b32 v110, s92, v110\n\t"
    "v_mfma_f32_16x16x32_bf16 v[80:83], v[68:71], v[184:187], v[80:83]\n\t"
    "v_mfma_f32_16x16x32_bf16 v[80:83], v[72:75], v[236:239], v[80:83]\n\t"
    "s_waitcnt vmcnt(1) lgkmcnt(5)\n\t"
    "v_smfmac_f32_16x16x64_bf16 v[116:119], v[120:123], v[92:99], v124\n\t"
    "ds_read_b64 v[92:93], v110 offset:0\n\t"
    "v_mfma_f32_16x16x32_bf16 v[80:83], v[76:79], v[240:243], v[80:83]\n\t"
    "ds_read_b64 v[94:95], v126 offset:0\n\t"
    "v_cvt_pk_bf16_f32 v104, v104, v105\n\t"
    "v_cvt_pk_bf16_f32 v105, v106, v107\n\t"
    "ds_write_b64 v125, v[104:105] offset:128\n\t"
    "v_mfma_f32_16x16x32_bf16 v[104:107], v[64:67], v[188:191], 0\n\t"
    "v_lshrrev_b32 v98, 16, v111\n\t"
    "v_and_b32 v111, s92, v111\n\t"
    "ds_read_b64 v[96:97], v111 offset:0\n\t"
    "v_mfma_f32_16x16x32_bf16 v[104:107], v[68:71], v[192:195], v[104:107]\n\t"
    "ds_read_b64 v[98:99], v98 offset:0\n\t"
    "v_mfma_f32_16x16x32_bf16 v[104:107], v[72:75], v[244:247], v[104:107]\n\t"
    "buffer_load_dwordx4 v[108:111], v127, %12, s86 offen offset:1024 sc1\n\t"
    "v_lshrrev_b32 v126, 16, v112\n\t"
    "v_cvt_pk_bf16_f32 v6, v116, v117\n\t"
    "v_cvt_pk_bf16_f32 v7, v118, v119\n\t"
    "v_and_b32 v119, s67, v13\n\t"
    "v_lshlrev_b32 v118, 16, v13\n\t"
    "v_and_b32 v117, s67, v12\n\t"
    "v_lshlrev_b32 v116, 16, v12\n\t"
    "v_and_b32 v112, s92, v112\n\t"
    "s_waitcnt lgkmcnt(6)\n\t"
    "v_smfmac_f32_16x16x64_bf16 v[116:119], v[120:123], v[84:91], v124\n\t"
    "ds_read_b64 v[84:85], v112 offset:0\n\t"
    "v_mfma_f32_16x16x32_bf16 v[104:107], v[76:79], v[248:251], v[104:107]\n\t"
    "ds_read_b64 v[86:87], v126 offset:0\n\t"
    "v_cvt_pk_bf16_f32 v80, v80, v81\n\t"
    "v_cvt_pk_bf16_f32 v81, v82, v83\n\t"
    "ds_write_b64 v125, v[80:81] offset:256\n\t"
    "v_mfma_f32_16x16x32_bf16 v[80:83], v[64:67], v[196:199], 0\n\t"
    "v_lshrrev_b32 v90, 16, v113\n\t"
    "v_and_b32 v113, s92, v113\n\t"
    "ds_read_b64 v[88:89], v113 offset:0\n\t"
    "v_mfma_f32_16x16x32_bf16 v[80:83], v[68:71], v[200:203], v[80:83]\n\t"
    "ds_read_b64 v[90:91], v90 offset:0\n\t"
    "v_mfma_f32_16x16x32_bf16 v[80:83], v[72:75], v[252:255], v[80:83]\n\t"
    "v_lshrrev_b32 v126, 16, v114\n\t"
    "v_mfma_f32_16x16x32_bf16 v[80:83], v[76:79], v[128:131], v[80:83]\n\t"
    "v_and_b32 v114, s92, v114\n\t"
    "v_cvt_pk_bf16_f32 v104, v104, v105\n\t"
    "v_cvt_pk_bf16_f32 v105, v106, v107\n\t"
    "ds_write_b64 v125, v[104:105] offset:384\n\t"
    "v_mfma_f32_16x16x32_bf16 v[104:107], v[64:67], v[204:207], 0\n\t"
    "s_waitcnt vmcnt(1) lgkmcnt(6)\n\t"
    "v_smfmac_f32_16x16x64_bf16 v[116:119], v[120:123], v[92:99], v124\n\t"
    "ds_read_b64 v[92:93], v114 offset:0\n\t"
    "v_mfma_f32_16x16x32_bf16 v[104:107], v[68:71], v[208:211], v[104:107]\n\t"
    "ds_read_b64 v[94:95], v126 offset:0\n\t"
    "v_mfma_f32_16x16x32_bf16 v[104:107], v[72:75], v[132:135], v[104:107]\n\t"
    "v_lshrrev_b32 v98, 16, v115\n\t"
    "v_and_b32 v115, s92, v115\n\t"
    "ds_read_b64 v[96:97], v115 offset:0\n\t"
    "v_mfma_f32_16x16x32_bf16 v[104:107], v[76:79], v[136:139], v[104:107]\n\t"
    "ds_read_b64 v[98:99], v98 offset:0\n\t"
    "v_cvt_pk_bf16_f32 v80, v80, v81\n\t"
    "v_cvt_pk_bf16_f32 v81, v82, v83\n\t"
    "ds_write_b64 v125, v[80:81] offset:512\n\t"
    "v_mfma_f32_16x16x32_bf16 v[80:83], v[64:67], v[156:159], 0\n\t"
    "s_add_u32 s87, s87, s95\n\t"
    "buffer_load_dwordx4 v[112:115], v127, %12, s87 offen offset:0 sc1\n\t"
    "v_lshrrev_b32 v126, 16, v100\n\t"
    "v_mfma_f32_16x16x32_bf16 v[80:83], v[68:71], v[160:163], v[80:83]\n\t"
    "v_cvt_pk_bf16_f32 v12, v116, v117\n\t"
    "v_cvt_pk_bf16_f32 v13, v118, v119\n\t"
    "v_and_b32 v119, s67, v15\n\t"
    "v_lshlrev_b32 v118, 16, v15\n\t"
    "v_and_b32 v117, s67, v14\n\t"
    "v_lshlrev_b32 v116, 16, v14\n\t"
    "v_and_b32 v100, s92, v100\n\t"
    "s_waitcnt lgkmcnt(6)\n\t"
    "v_smfmac_f32_16x16x64_bf16 v[116:119], v[120:123], v[84:91], v124\n\t"
    "ds_read_b64 v[84:85], v100 offset:0\n\t"
    "v_mfma_f32_16x16x32_bf16 v[80:83], v[72:75], v[140:143], v[80:83]\n\t"
    "ds_read_b64 v[86:87], v126 offset:0\n\t"
    "v_mfma_f32_16x16x32_bf16 v[80:83], v[76:79], v[144:147], v[80:83]\n\t"
    "v_lshrrev_b32 v90, 16, v101\n\t"
    "v_and_b32 v101, s92, v101\n\t"
    "ds_read_b64 v[88:89], v101 offset:0\n\t"
    "v_cvt_pk_bf16_f32 v104, v104, v105\n\t"
    "v_cvt_pk_bf16_f32 v105, v106, v107\n\t"
    "ds_write_b64 v125, v[104:105] offset:640\n\t"
    "v_mfma_f32_16x16x32_bf16 v[104:107], v[64:67], v[212:215], 0\n\t"
    "ds_read_b64 v[90:91], v90 offset:0\n\t"
    "v_mfma_f32_16x16x32_bf16 v[104:107], v[68:71], v[216:219], v[104:107]\n\t"
    "v_lshrrev_b32 v126, 16, v102\n\t"
    "v_mfma_f32_16x16x32_bf16 v[104:107], v[72:75], v[148:151], v[104:107]\n\t"
    "v_and_b32 v102, s92, v102\n\t"
    "v_mfma_f32_16x16x32_bf16 v[104:107], v[76:79], v[152:155], v[104:107]\n\t"
    "v_add_u32 v79, s96, v127\n\t"
    "s_add_u32 s96, s96, 0x1000\n\t"
    "s_and_b32 s96, s96, 0x3000\n\t"
    "v_add_u32 v79, 128000, v79\n\t"
    "ds_read_b128 v[64:67], v79 offset:0\n\t"
    "ds_read_b128 v[68:71], v79 offset:1024\n\t"
    "ds_read_b128 v[72:75], v79 offset:2048\n\t"
    "ds_read_b128 v[76:79], v79 offset:3072\n\t"
    "v_cvt_pk_bf16_f32 v80, v80, v81\n\t"
    "v_cvt_pk_bf16_f32 v81, v82, v83\n\t"
    "ds_write_b64 v125, v[80:81] offset:768\n\t"
    "s_waitcnt vmcnt(1) lgkmcnt(11)\n\t"
    "v_smfmac_f32_16x16x64_bf16 v[116:119], v[120:123], v[92:99], v124\n\t"
    "ds_read_b64 v[92:93], v102 offset:0\n\t"
    "ds_read_b64 v[94:95], v126 offset:0\n\t"
    "v_lshrrev_b32 v98, 16, v103\n\t"
    "v_and_b32 v103, s92, v103\n\t"
    "ds_read_b64 v[96:97], v103 offset:0\n\t"
    "ds_read_b64 v[98:99], v98 offset:0\n\t"
    "buffer_load_dwordx4 v[100:103], v127, %12, s87 offen offset:1024 sc1\n\t"
    "v_lshrrev_b32 v126, 16, v108\n\t"
    "v_cvt_pk_bf16_f32 v14, v116, v117\n\t"
    "v_cvt_pk_bf16_f32 v15, v118, v119\n\t"
    "v_and_b32 v119, s67, v21\n\t"
    "v_lshlrev_b32 v118, 16, v21\n\t"
    "v_and_b32 v117, s67, v20\n\t"
    "v_lshlrev_b32 v116, 16, v20\n\t"
    "v_and_b32 v108, s92, v108\n\t"
    "s_waitcnt lgkmcnt(9)\n\t"
    "v_smfmac_f32_16x16x64_bf16 v[116:119], v[120:123], v[84:91], v124\n\t"
    "ds_read_b64 v[84:85], v108 offset:0\n\t"
    "s_waitcnt lgkmcnt(6)\n\t"
    "v_mfma_f32_16x16x32_bf16 v[80:83], v[64:67], v[164:167], 0\n\t"
    "ds_read_b64 v[86:87], v126 offset:0\n\t"
    "v_mfma_f32_16x16x32_bf16 v[80:83], v[68:71], v[168:171], v[80:83]\n\t"
    "v_lshrrev_b32 v90, 16, v109\n\t"
    "v_and_b32 v109, s92, v109\n\t"
    "ds_read_b64 v[88:89], v109 offset:0\n\t"
    "v_mfma_f32_16x16x32_bf16 v[80:83], v[72:75], v[220:223], v[80:83]\n\t"
    "ds_read_b64 v[90:91], v90 offset:0\n\t"
    "v_mfma_f32_16x16x32_bf16 v[80:83], v[76:79], v[224:227], v[80:83]\n\t"
    "v_lshrrev_b32 v126, 16, v110\n\t"
    "v_cvt_pk_bf16_f32 v104, v104, v105\n\t"
    "v_cvt_pk_bf16_f32 v105, v106, v107\n\t"
    "ds_write_b64 v125, v[104:105] offset:872\n\t"
    "v_mfma_f32_16x16x32_bf16 v[104:107], v[64:67], v[172:175], 0\n\t"
    "v_and_b32 v110, s92, v110\n\t"
    "v_mfma_f32_16x16x32_bf16 v[104:107], v[68:71], v[176:179], v[104:107]\n\t"
    "s_waitcnt vmcnt(1) lgkmcnt(5)\n\t"
    "v_smfmac_f32_16x16x64_bf16 v[116:119], v[120:123], v[92:99], v124\n\t"
    "ds_read_b64 v[92:93], v110 offset:0\n\t"
    "v_mfma_f32_16x16x32_bf16 v[104:107], v[72:75], v[228:231], v[104:107]\n\t"
    "ds_read_b64 v[94:95], v126 offset:0\n\t"
    "v_mfma_f32_16x16x32_bf16 v[104:107], v[76:79], v[232:235], v[104:107]\n\t"
    "v_lshrrev_b32 v98, 16, v111\n\t"
    "v_and_b32 v111, s92, v111\n\t"
    "ds_read_b64 v[96:97], v111 offset:0\n\t"
    "v_cvt_pk_bf16_f32 v80, v80, v81\n\t"
    "v_cvt_pk_bf16_f32 v81, v82, v83\n\t"
    "ds_write_b64 v125, v[80:81] offset:32000\n\t"
    "v_mfma_f32_16x16x32_bf16 v[80:83], v[64:67], v[180:183], 0\n\t"
    "ds_read_b64 v[98:99], v98 offset:0\n\t"
    "v_mfma_f32_16x16x32_bf16 v[80:83], v[68:71], v[184:187], v[80:83]\n\t"
    "s_add_u32 s84, s84, 0x8000\n\t"
    "buffer_load_dwordx4 v[108:111], v127, %12, s84 offen offset:0 sc1\n\t"
    "v_lshrrev_b32 v126, 16, v112\n\t"
    "v_mfma_f32_16x16x32_bf16 v[80:83], v[72:75], v[236:239], v[80:83]\n\t"
    "v_cvt_pk_bf16_f32 v20, v116, v117\n\t"
    "v_cvt_pk_bf16_f32 v21, v118, v119\n\t"
    "v_and_b32 v119, s67, v23\n\t"
    "v_lshlrev_b32 v118, 16, v23\n\t"
    "v_and_b32 v117, s67, v22\n\t"
    "v_lshlrev_b32 v116, 16, v22\n\t"
    "v_and_b32 v112, s92, v112\n\t"
    "s_waitcnt lgkmcnt(6)\n\t"
    "v_smfmac_f32_16x16x64_bf16 v[116:119], v[120:123], v[84:91], v124\n\t"
    "ds_read_b64 v[84:85], v112 offset:0\n\t"
    "v_mfma_f32_16x16x32_bf16 v[80:83], v[76:79], v[240:243], v[80:83]\n\t"
    "ds_read_b64 v[86:87], v126 offset:0\n\t"
    "v_cvt_pk_bf16_f32 v104, v104, v105\n\t"
    "v_cvt_pk_bf16_f32 v105, v106, v107\n\t"
    "ds_write_b64 v125, v[104:105] offset:32128\n\t"
    "v_mfma_f32_16x16x32_bf16 v[104:107], v[64:67], v[188:191], 0\n\t"
    "v_lshrrev_b32 v90, 16, v113\n\t"
    "v_and_b32 v113, s92, v113\n\t"
    "ds_read_b64 v[88:89], v113 offset:0\n\t"
    "v_mfma_f32_16x16x32_bf16 v[104:107], v[68:71], v[192:195], v[104:107]\n\t"
    "ds_read_b64 v[90:91], v90 offset:0\n\t"
    "v_mfma_f32_16x16x32_bf16 v[104:107], v[72:75], v[244:247], v[104:107]\n\t"
    "v_lshrrev_b32 v126, 16, v114\n\t"
    "v_mfma_f32_16x16x32_bf16 v[104:107], v[76:79], v[248:251], v[104:107]\n\t"
    "v_and_b32 v114, s92, v114\n\t"
    "v_cvt_pk_bf16_f32 v80, v80, v81\n\t"
    "v_cvt_pk_bf16_f32 v81, v82, v83\n\t"
    "ds_write_b64 v125, v[80:81] offset:32256\n\t"
    "v_mfma_f32_16x16x32_bf16 v[80:83], v[64:67], v[196:199], 0\n\t"
    "s_waitcnt vmcnt(1) lgkmcnt(6)\n\t"
    "v_smfmac_f32_16x16x64_bf16 v[116:119], v[120:123], v[92:99], v124\n\t"
    "ds_read_b64 v[92:93], v114 offset:0\n\t"
    "v_mfma_f32_16x16x32_bf16 v[80:83], v[68:71], v[200:203], v[80:83]\n\t"
    "ds_read_b64 v[94:95], v126 offset:0\n\t"
    "v_mfma_f32_16x16x32_bf16 v[80:83], v[72:75], v[252:255], v[80:83]\n\t"
    "v_lshrrev_b32 v98, 16, v115\n\t"
    "v_and_b32 v115, s92, v115\n\t"
    "ds_read_b64 v[96:97], v115 offset:0\n\t"
    "v_mfma_f32_16x16x32_bf16 v[80:83], v[76:79], v[128:131], v[80:83]\n\t"
    "ds_read_b64 v[98:99], v98 offset:0\n\t"
    "v_cvt_pk_bf16_f32 v104, v104, v105\n\t"
    "v_cvt_pk_bf16_f32 v105, v106, v107\n\t"
    "ds_write_b64 v125, v[104:105] offset:32384\n\t"
    "v_mfma_f32_16x16x32_bf16 v[104:107], v[64:67], v[204:207], 0\n\t"
    "buffer_load_dwordx4 v[112:115], v127, %12, s84 offen offset:1024 sc1\n\t"
    "v_lshrrev_b32 v126, 16, v100\n\t"
    "v_cvt_pk_bf16_f32 v22, v116, v117\n\t"
    "v_cvt_pk_bf16_f32 v23, v118, v119\n\t"
    "v_and_b32 v119, s67, v29\n\t"
    "v_lshlrev_b32 v118, 16, v29\n\t"
    "v_and_b32 v117, s67, v28\n\t"
    "v_lshlrev_b32 v116, 16, v28\n\t"
    "v_and_b32 v100, s92, v100\n\t"
    "s_waitcnt lgkmcnt(6)\n\t"
    "v_smfmac_f32_16x16x64_bf16 v[116:119], v[120:123], v[84:91], v124\n\t"
    "ds_read_b64 v[84:85], v100 offset:0\n\t"
    "v_mfma_f32_16x16x32_bf16 v[104:107], v[68:71], v[208:211], v[104:107]\n\t"
    "ds_read_b64 v[86:87], v126 offset:0\n\t"
    "v_mfma_f32_16x16x32_bf16 v[104:107], v[72:75], v[132:135], v[104:107]\n\t"
    "v_lshrrev_b32 v90, 16, v101\n\t"
    "v_and_b32 v101, s92, v101\n\t"
    "ds_read_b64 v[88:89], v101 offset:0\n\t"
    "v_mfma_f32_16x16x32_bf16 v[104:107], v[76:79], v[136:139], v[104:107]\n\t"
    "ds_read_b64 v[90:91], v90 offset:0\n\t"
    "v_cvt_pk_bf16_f32 v80, v80, v81\n\t"
    "v_cvt_pk_bf16_f32 v81, v82, v83\n\t"
    "ds_write_b64 v125, v[80:81] offset:32512\n\t"
    "v_mfma_f32_16x16x32_bf16 v[80:83], v[64:67], v[156:159], 0\n\t"
    "v_lshrrev_b32 v126, 16, v102\n\t"
    "v_mfma_f32_16x16x32_bf16 v[80:83], v[68:71], v[160:163], v[80:83]\n\t"
    "v_and_b32 v102, s92, v102\n\t"
    "v_mfma_f32_16x16x32_bf16 v[80:83], v[72:75], v[140:143], v[80:83]\n\t"
    "v_mfma_f32_16x16x32_bf16 v[80:83], v[76:79], v[144:147], v[80:83]\n\t"
    "v_cvt_pk_bf16_f32 v104, v104, v105\n\t"
    "v_cvt_pk_bf16_f32 v105, v106, v107\n\t"
    "ds_write_b64 v125, v[104:105] offset:32640\n\t"
    "s_waitcnt vmcnt(1) lgkmcnt(7)\n\t"
    "v_smfmac_f32_16x16x64_bf16 v[116:119], v[120:123], v[92:99], v124\n\t"
    "ds_read_b64 v[92:93], v102 offset:0\n\t"
    "v_mfma_f32_16x16x32_bf16 v[104:107], v[64:67], v[212:215], 0\n\t"
    "ds_read_b64 v[94:95], v126 offset:0\n\t"
    "v_mfma_f32_16x16x32_bf16 v[104:107], v[68:71], v[216:219], v[104:107]\n\t"
    "v_lshrrev_b32 v98, 16, v103\n\t"
    "v_and_b32 v103, s92, v103\n\t"
    "ds_read_b64 v[96:97], v103 offset:0\n\t"
    "v_mfma_f32_16x16x32_bf16 v[104:107], v[72:75], v[148:151], v[104:107]\n\t"
    "ds_read_b64 v[98:99], v98 offset:0\n\t"
    "v_mfma_f32_16x16x32_bf16 v[104:107], v[76:79], v[152:155], v[104:107]\n\t"
    "s_add_u32 s85, s85, 0x8000\n\t"
    "buffer_load_dwordx4 v[100:103], v127, %12, s85 offen offset:0 sc1\n\t"
    "v_lshrrev_b32 v126, 16, v108\n\t"
    "v_cvt_pk_bf16_f32 v80, v80, v81\n\t"
    "v_cvt_pk_bf16_f32 v81, v82, v83\n\t"
    "ds_write_b64 v125, v[80:81] offset:32768\n\t"
    "s_nop 1\n\t"
    "v_cvt_pk_bf16_f32 v104, v104, v105\n\t"
    "v_cvt_pk_bf16_f32 v105, v106, v107\n\t"
    "ds_write_b64 v125, v[104:105] offset:32872\n\t"
    "v_cvt_pk_bf16_f32 v28, v116, v117\n\t"
    "v_cvt_pk_bf16_f32 v29, v118, v119\n\t"
    "v_and_b32 v119, s67, v31\n\t"
    "v_lshlrev_b32 v118, 16, v31\n\t"
    "v_and_b32 v117, s67, v30\n\t"
    "v_lshlrev_b32 v116, 16, v30\n\t"
    "v_and_b32 v108, s92, v108\n\t"
    "s_waitcnt lgkmcnt(0)\n\t"
    "s_barrier\n\t"
    "v_smfmac_f32_16x16x64_bf16 v[116:119], v[120:123], v[84:91], v124\n\t"
    "ds_read_b64 v[84:85], v108 offset:64000\n\t"
    "ds_read_b64 v[86:87], v126 offset:64000\n\t"
    "v_lshrrev_b32 v90, 16, v109\n\t"
    "v_and_b32 v109, s92, v109\n\t"
    "ds_read_b64 v[88:89], v109 offset:64000\n\t"
    "ds_read_b64 v[90:91], v90 offset:64000\n\t"
    "buffer_load_dwordx4 v[104:107], v127, %12, s85 offen offset:1024 sc1\n\t"
    "v_lshrrev_b32 v126, 16, v110\n\t"
    "v_and_b32 v110, s92, v110\n\t"
    "s_waitcnt vmcnt(2)\n\t"
    "v_smfmac_f32_16x16x64_bf16 v[116:119], v[120:123], v[92:99], v124\n\t"
    "ds_read_b64 v[92:93], v110 offset:64000\n\t"
    "ds_read_b64 v[94:95], v126 offset:64000\n\t"
    "v_lshrrev_b32 v98, 16, v111\n\t"
    "v_and_b32 v111, s92, v111\n\t"
    "ds_read_b64 v[96:97], v111 offset:64000\n\t"
    "ds_read_b64 v[98:99], v98 offset:64000\n\t"
    "s_add_u32 s86, s86, 0x8000\n\t"
    "buffer_load_dwordx4 v[108:111], v127, %12, s86 offen offset:0 sc1\n\t"
    "v_lshrrev_b32 v252, 16, v112\n\t"
    "v_and_b32 v112, s92, v112\n\t"
    "v_lshrrev_b32 v253, 16, v113\n\t"
    "v_cvt_pk_bf16_f32 v30, v116, v117\n\t"
    "v_cvt_pk_bf16_f32 v31, v118, v119\n\t"
    "v_and_b32 v119, s67, v5\n\t"
    "v_lshlrev_b32 v118, 16, v5\n\t"
    "v_and_b32 v117, s67, v4\n\t"
    "v_lshlrev_b32 v116, 16, v4\n\t"
    "v_and_b32 v113, s92, v113\n\t"
    "v_mov_b32 v82, 0xbdd2d3e8\n\t"
    "v_mov_b32 v83, 0xbdd2d3e8\n\t"
    "v_mov_b32 v81, 0xc0135761\n\t"
    "v_lshrrev_b32 v125, 2, v127\n\t"
    "v_mov_b32 v80, 0xc0135761\n\t"
    "s_waitcnt lgkmcnt(4)\n\t"
    "v_smfmac_f32_16x16x64_bf16 v[116:119], v[120:123], v[84:91], v124\n\t"
    "ds_read_b64 v[84:85], v112 offset:64000\n\t"
    "ds_read_b64 v[86:87], v252 offset:64000\n\t"
    "ds_read_b64 v[88:89], v113 offset:64000\n\t"
    "ds_read_b64 v[90:91], v253 offset:64000\n\t"
    "v_lshrrev_b32 v254, 16, v114\n\t"
    "v_and_b32 v114, s92, v114\n\t"
    "v_lshrrev_b32 v255, 16, v115\n\t"
    "v_and_b32 v115, s92, v115\n\t"
    "s_lshl_b32 s65, %16, 10\n\t"
    "s_add_u32 s65, s65, 30408704\n\t"
    "s_xor_b32 s61, %14, 8\n\t"
    "s_mul_i32 s61, s61, 69632\n\t"
    "s_add_u32 s61, s61, s65\n\t"
    "s_mul_i32 s94, %14, 69632\n\t"
    "s_add_u32 s94, s94, s65\n\t"
    "s_lshl_b32 s65, %16, 9\n\t"
    "s_sub_u32 s65, 0x10000, s65\n\t"
    "s_add_u32 s93, s94, s65\n\t"
    "s_add_u32 s96, s61, s65\n\t"
    "v_mov_b32 v165, 1\n\t"
    "s_or_b32 s97, %14, %16\n\t"
    "s_cbranch_scc0 L_part0_%=\n\t"
    "L_partb0_%=:\n\t"
    "s_waitcnt vmcnt(2) lgkmcnt(4)\n\t"
    "v_smfmac_f32_16x16x64_bf16 v[116:119], v[120:123], v[92:99], v124\n\t"
    "ds_read_b64 v[92:93], v114 offset:64000\n\t"
    "ds_read_b64 v[94:95], v254 offset:64000\n\t"
    "ds_read_b64 v[96:97], v115 offset:64000\n\t"
    "ds_read_b64 v[98:99], v255 offset:64000\n\t"
    "buffer_load_dwordx4 v[172:175], v127, %12, s86 offen offset:1024 sc1\n\t"
    "v_lshrrev_b32 v252, 16, v100\n\t"
    "v_and_b32 v100, s92, v100\n\t"
    "v_lshrrev_b32 v253, 16, v101\n\t"
    "v_cvt_pk_bf16_f32 v4, v116, v117\n\t"
    "v_cvt_pk_bf16_f32 v5, v118, v119\n\t"
    "v_and_b32 v119, s67, v7\n\t"
    "v_lshlrev_b32 v118, 16, v7\n\t"
    "v_and_b32 v117, s67, v6\n\t"
    "v_lshlrev_b32 v116, 16, v6\n\t"
    "v_and_b32 v101, s92, v101\n\t"
    "v_mov_b32 v156, v120\n\t"
    "v_mov_b32 v160, 0\n\t"
    "v_mov_b32 v161, v121\n\t"
    "v_mov_b32 v157, 0\n\t"
    "v_mov_b32 v158, v122\n\t"
    "v_mov_b32 v162, 0\n\t"
    "v_mov_b32 v163, v123\n\t"
    "v_mov_b32 v159, 0\n\t"
    "s_waitcnt lgkmcnt(4)\n\t"
    "v_smfmac_f32_16x16x64_bf16 v[116:119], v[120:123], v[84:91], v124\n\t"
    "ds_read_b64 v[84:85], v100 offset:64000\n\t"
    "ds_read_b64 v[86:87], v252 offset:64000\n\t"
    "ds_read_b64 v[88:89], v101 offset:64000\n\t"
    "ds_read_b64 v[90:91], v253 offset:64000\n\t"
    "s_add_u32 s87, s87, 0x8000\n\t"
    "buffer_load_dwordx4 v[176:179], v127, %12, s87 offen offset:0 sc1\n\t"
    "v_lshrrev_b32 v254, 16, v102\n\t"
    "v_and_b32 v102, s92, v102\n\t"
    "v_lshrrev_b32 v255, 16, v103\n\t"
    "v_and_b32 v103, s92, v103\n\t"
    "s_cmp_lt_u32 %16, 4\n\t"
    "s_cbranch_scc1 L_tp163_%=\n\t"
    "s_setprio 0\n\t"
    "L_tp163_%=:\n\t"
    "s_waitcnt vmcnt(3) lgkmcnt(4)\n\t"
    "v_smfmac_f32_16x16x64_bf16 v[116:119], v[120:123], v[92:99], v124\n\t"
    "ds_read_b64 v[92:93], v102 offset:64000\n\t"
    "ds_read_b64 v[94:95], v254 offset:64000\n\t"
    "ds_read_b64 v[96:97], v103 offset:64000\n\t"
    "ds_read_b64 v[98:99], v255 offset:64000\n\t"
    "buffer_load_dwordx4 v[180:183], v127, %12, s87 offen offset:1024 sc1\n\t"
    "v_lshrrev_b32 v252, 16, v104\n\t"
    "v_and_b32 v104, s92, v104\n\t"
    "v_lshrrev_b32 v253, 16, v105\n\t"
    "v_cvt_pk_bf16_f32 v6, v116, v117\n\t"
    "v_cvt_pk_bf16_f32 v7, v118, v119\n\t"
    "v_and_b32 v119, s67, v13\n\t"
    "v_lshlrev_b32 v118, 16, v13\n\t"
    "v_and_b32 v117, s67, v12\n\t"
    "v_lshlrev_b32 v116, 16, v12\n\t"
    "v_and_b32 v105, s92, v105\n\t"
    "buffer_store_dwordx4 v[4:7], v127, %12, s61 offen sc1\n\t"
    "s_waitcnt lgkmcnt(4)\n\t"
    "v_smfmac_f32_16x16x64_bf16 v[116:119], v[120:123], v[84:91], v124\n\t"
    "ds_read_b64 v[84:85], v104 offset:64000\n\t"
    "ds_read_b64 v[86:87], v252 offset:64000\n\t"
    "ds_read_b64 v[88:89], v105 offset:64000\n\t"
    "ds_read_b64 v[90:91], v253 offset:64000\n\t"
    "s_add_u32 s88, s88, s95\n\t"
    "buffer_load_dwordx4 v[184:187], v127, %12, s88 offen offset:0 sc1\n\t"
    "v_lshrrev_b32 v254, 16, v106\n\t"
    "v_and_b32 v106, s92, v106\n\t"
    "v_lshrrev_b32 v255, 16, v107\n\t"
    "v_and_b32 v107, s92, v107\n\t"
    "s_waitcnt vmcnt(5) lgkmcnt(4)\n\t"
    "v_smfmac_f32_16x16x64_bf16 v[116:119], v[120:123], v[92:99], v124\n\t"
    "ds_read_b64 v[92:93], v106 offset:64000\n\t"
    "ds_read_b64 v[94:95], v254 offset:64000\n\t"
    "ds_read_b64 v[96:97], v107 offset:64000\n\t"
    "ds_read_b64 v[98:99], v255 offset:64000\n\t"
    "s_add_u32 s88, s88, 0x8000\n\t"
    "buffer_load_dwordx4 v[188:191], v127, %12, s88 offen offset:0 sc1\n\t"
    "v_lshrrev_b32 v252, 16, v108\n\t"
    "v_and_b32 v108, s92, v108\n\t"
    "v_lshrrev_b32 v253, 16, v109\n\t"
    "v_cvt_pk_bf16_f32 v12, v116, v117\n\t"
    "v_cvt_pk_bf16_f32 v13, v118, v119\n\t"
    "v_and_b32 v119, s67, v15\n\t"
    "v_lshlrev_b32 v118, 16, v15\n\t"
    "v_and_b32 v117, s67, v14\n\t"
    "v_lshlrev_b32 v116, 16, v14\n\t"
    "v_and_b32 v109, s92, v109\n\t"
    "s_waitcnt lgkmcnt(4)\n\t"
    "v_smfmac_f32_16x16x64_bf16 v[116:119], v[120:123], v[84:91], v124\n\t"
    "ds_read_b64 v[84:85], v108 offset:64000\n\t"
    "ds_read_b64 v[86:87], v252 offset:64000\n\t"
    "ds_read_b64 v[88:89], v109 offset:64000\n\t"
    "ds_read_b64 v[90:91], v253 offset:64000\n\t"
    "s_sub_u32 s88, s88, 0x8000\n\t"
    "buffer_load_dwordx4 v[236:239], v127, %12, s88 offen offset:1024 sc1\n\t"
    "v_lshrrev_b32 v254, 16, v110\n\t"
    "v_and_b32 v110, s92, v110\n\t"
    "v_lshrrev_b32 v255, 16, v111\n\t"
    "v_and_b32 v111, s92, v111\n\t"
    "s_waitcnt vmcnt(6) lgkmcnt(4)\n\t"
    "v_smfmac_f32_16x16x64_bf16 v[116:119], v[120:123], v[92:99], v124\n\t"
    "ds_read_b64 v[92:93], v110 offset:64000\n\t"
    "ds_read_b64 v[94:95], v254 offset:64000\n\t"
    "ds_read_b64 v[96:97], v111 offset:64000\n\t"
    "ds_read_b64 v[98:99], v255 offset:64000\n\t"
    "s_add_u32 s88, s88, 0x8000\n\t"
    "buffer_load_dwordx4 v[192:195], v127, %12, s88 offen offset:1024 sc1\n\t"
    "v_lshrrev_b32 v252, 16, v172\n\t"
    "v_and_b32 v172, s92, v172\n\t"
    "v_lshrrev_b32 v253, 16, v173\n\t"
    "v_cvt_pk_bf16_f32 v14, v116, v117\n\t"
    "v_cvt_pk_bf16_f32 v15, v118, v119\n\t"
    "v_and_b32 v119, s67, v21\n\t"
    "v_lshlrev_b32 v118, 16, v21\n\t"
    "v_and_b32 v117, s67, v20\n\t"
    "v_lshlrev_b32 v116, 16, v20\n\t"
    "v_and_b32 v173, s92, v173\n\t"
    "s_waitcnt lgkmcnt(4)\n\t"
    "v_smfmac_f32_16x16x64_bf16 v[116:119], v[120:123], v[84:91], v124\n\t"
    "ds_read_b64 v[84:85], v172 offset:64000\n\t"
    "ds_read_b64 v[86:87], v252 offset:64000\n\t"
    "ds_read_b64 v[88:89], v173 offset:64000\n\t"
    "ds_read_b64 v[90:91], v253 offset:64000\n\t"
    "v_lshrrev_b32 v254, 16, v174\n\t"
    "v_and_b32 v174, s92, v174\n\t"
    "v_lshrrev_b32 v255, 16, v175\n\t"
    "v_and_b32 v175, s92, v175\n\t"
    "s_cmp_lg_u32 s97, 0\n\t"
    "s_cbranch_scc0 L_part1_%=\n\t"
    "L_partb1_%=:\n\t"
    "s_waitcnt vmcnt(6) lgkmcnt(4)\n\t"
    "v_smfmac_f32_16x16x64_bf16 v[116:119], v[120:123], v[92:99], v124\n\t"
    "ds_read_b64 v[92:93], v174 offset:64000\n\t"
    "ds_read_b64 v[94:95], v254 offset:64000\n\t"
    "ds_read_b64 v[96:97], v175 offset:64000\n\t"
    "ds_read_b64 v[98:99], v255 offset:64000\n\t"
    "s_add_u32 s89, s89, s95\n\t"
    "buffer_load_dwordx4 v[196:199], v127, %12, s89 offen offset:0 sc1\n\t"
    "v_lshrrev_b32 v252, 16, v176\n\t"
    "v_and_b32 v176, s92, v176\n\t"
    "v_lshrrev_b32 v253, 16, v177\n\t"
    "v_cvt_pk_bf16_f32 v20, v116, v117\n\t"
    "v_cvt_pk_bf16_f32 v21, v118, v119\n\t"
    "v_and_b32 v119, s67, v23\n\t"
    "v_lshlrev_b32 v118, 16, v23\n\t"
    "v_and_b32 v117, s67, v22\n\t"
    "v_lshlrev_b32 v116, 16, v22\n\t"
    "v_and_b32 v177, s92, v177\n\t"
    "s_waitcnt lgkmcnt(4)\n\t"
    "v_smfmac_f32_16x16x64_bf16 v[116:119], v[120:123], v[84:91], v124\n\t"
    "ds_read_b64 v[84:85], v176 offset:64000\n\t"
    "ds_read_b64 v[86:87], v252 offset:64000\n\t"
    "ds_read_b64 v[88:89], v177 offset:64000\n\t"
    "ds_read_b64 v[90:91], v253 offset:64000\n\t"
    "v_lshrrev_b32 v254, 16, v178\n\t"
    "v_and_b32 v178, s92, v178\n\t"
    "v_lshrrev_b32 v255, 16, v179\n\t"
    "v_and_b32 v179, s92, v179\n\t"
    "s_waitcnt vmcnt(6) lgkmcnt(4)\n\t"
    "v_smfmac_f32_16x16x64_bf16 v[116:119], v[120:123], v[92:99], v124\n\t"
    "ds_read_b64 v[92:93], v178 offset:64000\n\t"
    "ds_read_b64 v[94:95], v254 offset:64000\n\t"
    "ds_read_b64 v[96:97], v179 offset:64000\n\t"
    "ds_read_b64 v[98:99], v255 offset:64000\n\t"
    "s_add_u32 s89, s89, 0x8000\n\t"
    "buffer_load_dwordx4 v[200:203], v127, %12, s89 offen offset:0 sc1\n\t"
    "v_lshrrev_b32 v252, 16, v180\n\t"
    "v_and_b32 v180, s92, v180\n\t"
    "v_lshrrev_b32 v253, 16, v181\n\t"
    "v_cvt_pk_bf16_f32 v22, v116, v117\n\t"
    "v_cvt_pk_bf16_f32 v23, v118, v119\n\t"
    "v_and_b32 v119, s67, v29\n\t"
    "v_lshlrev_b32 v118, 16, v29\n\t"
    "v_and_b32 v117, s67, v28\n\t"
    "v_lshlrev_b32 v116, 16, v28\n\t"
    "v_and_b32 v181, s92, v181\n\t"
    "s_add_u32 s61, s61, 0x2000\n\t"
    "buffer_store_dwordx4 v[12:15], v127, %12, s61 offen sc1\n\t"
    "s_waitcnt lgkmcnt(4)\n\t"
    "v_smfmac_f32_16x16x64_bf16 v[116:119], v[120:123], v[84:91], v124\n\t"
    "ds_read_b64 v[84:85], v180 offset:64000\n\t"
    "ds_read_b64 v[86:87], v252 offset:64000\n\t"
    "ds_read_b64 v[88:89], v181 offset:64000\n\t"
    "ds_read_b64 v[90:91], v253 offset:64000\n\t"
    "v_lshrrev_b32 v254, 16, v182\n\t"
    "v_and_b32 v182, s92, v182\n\t"
    "v_lshrrev_b32 v255, 16, v183\n\t"
    "v_and_b32 v183, s92, v183\n\t"
    "s_waitcnt vmcnt(6) lgkmcnt(4)\n\t"
    "v_smfmac_f32_16x16x64_bf16 v[116:119], v[120:123], v[92:99], v124\n\t"
    "ds_read_b64 v[92:93], v182 offset:64000\n\t"
    "ds_read_b64 v[94:95], v254 offset:64000\n\t"
    "ds_read_b64 v[96:97], v183 offset:64000\n\t"
    "ds_read_b64 v[98:99], v255 offset:64000\n\t"
    "s_sub_u32 s89, s89, 0x8000\n\t"
    "buffer_load_dwordx4 v[204:207], v127, %12, s89 offen offset:1024 sc1\n\t"
    "v_lshrrev_b32 v252, 16, v184\n\t"
    "v_and_b32 v184, s92, v184\n\t"
    "v_lshrrev_b32 v253, 16, v185\n\t"
    "v_cvt_pk_bf16_f32 v28, v116, v117\n\t"
    "v_cvt_pk_bf16_f32 v29, v118, v119\n\t"
    "v_and_b32 v119, s67, v31\n\t"
    "v_lshlrev_b32 v118, 16, v31\n\t"
    "v_and_b32 v117, s67, v30\n\t"
    "v_lshlrev_b32 v116, 16, v30\n\t"
    "v_and_b32 v185, s92, v185\n\t"
    "s_waitcnt lgkmcnt(4)\n\t"
    "v_smfmac_f32_16x16x64_bf16 v[116:119], v[120:123], v[84:91], v124\n\t"
    "ds_read_b64 v[84:85], v184 offset:0\n\t"
    "ds_read_b64 v[86:87], v252 offset:0\n\t"
    "ds_read_b64 v[88:89], v185 offset:0\n\t"
    "ds_read_b64 v[90:91], v253 offset:0\n\t"
    "v_lshrrev_b32 v254, 16, v186\n\t"
    "v_and_b32 v186, s92, v186\n\t"
    "v_lshrrev_b32 v255, 16, v187\n\t"
    "v_and_b32 v187, s92, v187\n\t"
    "s_cmp_lt_u32 %16, 4\n\t"
    "s_cbranch_scc1 L_tp175_%=\n\t"
    "s_setprio 1\n\t"
    "L_tp175_%=:\n\t"
    "s_waitcnt vmcnt(6) lgkmcnt(4)\n\t"
    "v_smfmac_f32_16x16x64_bf16 v[116:119], v[120:123], v[92:99], v124\n\t"
    "ds_read_b64 v[92:93], v186 offset:0\n\t"
    "ds_read_b64 v[94:95], v254 offset:0\n\t"
    "ds_read_b64 v[96:97], v187 offset:0\n\t"
    "ds_read_b64 v[98:99], v255 offset:0\n\t"
    "s_add_u32 s89, s89, 0x8000\n\t"
    "buffer_load_dwordx4 v[144:147], v127, %12, s89 offen offset:1024 sc1\n\t"
    "v_lshrrev_b32 v252, 16, v188\n\t"
    "v_and_b32 v188, s92, v188\n\t"
    "v_lshrrev_b32 v253, 16, v189\n\t"
    "v_cvt_pk_bf16_f32 v30, v116, v117\n\t"
    "v_cvt_pk_bf16_f32 v31, v118, v119\n\t"
    "v_and_b32 v119, s67, v37\n\t"
    "v_lshlrev_b32 v118, 16, v37\n\t"
    "v_and_b32 v117, s67, v36\n\t"
    "v_lshlrev_b32 v116, 16, v36\n\t"
    "v_and_b32 v189, s92, v189\n\t"
    "s_waitcnt lgkmcnt(4)\n\t"
    "v_smfmac_f32_16x16x64_bf16 v[116:119], v[120:123], v[84:91], v124\n\t"
    "ds_read_b64 v[84:85], v188 offset:64000\n\t"
    "ds_read_b64 v[86:87], v252 offset:64000\n\t"
    "ds_read_b64 v[88:89], v189 offset:64000\n\t"
    "ds_read_b64 v[90:91], v253 offset:64000\n\t"
    "v_lshrrev_b32 v254, 16, v190\n\t"
    "v_and_b32 v190, s92, v190\n\t"
    "v_lshrrev_b32 v255, 16, v191\n\t"
    "v_and_b32 v191, s92, v191\n\t"
    "s_waitcnt vmcnt(6) lgkmcnt(4)\n\t"
    "v_smfmac_f32_16x16x64_bf16 v[116:119], v[120:123], v[92:99], v124\n\t"
    "ds_read_b64 v[92:93], v190 offset:64000\n\t"
    "ds_read_b64 v[94:95], v254 offset:64000\n\t"
    "ds_read_b64 v[96:97], v191 offset:64000\n\t"
    "ds_read_b64 v[98:99], v255 offset:64000\n\t"
    "s_add_u32 s90, s90, s95\n\t"
    "buffer_load_dwordx4 v[148:151], v127, %12, s90 offen offset:0 sc1\n\t"
    "v_lshrrev_b32 v252, 16, v236\n\t"
    "v_and_b32 v236, s92, v236\n\t"
    "v_lshrrev_b32 v253, 16, v237\n\t"
    "v_and_b32 v237, s92, v237\n\t"
    "s_waitcnt lgkmcnt(4)\n\t"
    "v_smfmac_f32_16x16x64_bf16 v[116:119], v[120:123], v[84:91], v124\n\t"
    "ds_read_b64 v[84:85], v236 offset:0\n\t"
    "ds_read_b64 v[86:87], v252 offset:0\n\t"
    "ds_read_b64 v[88:89], v237 offset:0\n\t"
    "ds_read_b64 v[90:91], v253 offset:0\n\t"
    "v_lshrrev_b32 v254, 16, v238\n\t"
    "v_and_b32 v238, s92, v238\n\t"
    "v_lshrrev_b32 v255, 16, v239\n\t"
    "v_and_b32 v239, s92, v239\n\t"
    "s_waitcnt vmcnt(6) lgkmcnt(4)\n\t"
    "v_smfmac_f32_16x16x64_bf16 v[116:119], v[120:123], v[92:99], v124\n\t"
    "ds_read_b64 v[92:93], v238 offset:0\n\t"
    "ds_read_b64 v[94:95], v254 offset:0\n\t"
    "ds_read_b64 v[96:97], v239 offset:0\n\t"
    "ds_read_b64 v[98:99], v255 offset:0\n\t"
    "s_add_u32 s90, s90, 0x8000\n\t"
    "buffer_load_dwordx4 v[152:155], v127, %12, s90 offen offset:0 sc1\n\t"
    "v_lshrrev_b32 v252, 16, v192\n\t"
    "v_and_b32 v192, s92, v192\n\t"
    "v_lshrrev_b32 v253, 16, v193\n\t"
    "v_cvt_pk_bf16_f32 v36, v116, v117\n\t"
    "v_cvt_pk_bf16_f32 v37, v118, v119\n\t"
    "v_and_b32 v119, s67, v39\n\t"
    "v_lshlrev_b32 v118, 16, v39\n\t"
    "v_and_b32 v117, s67, v38\n\t"
    "v_lshlrev_b32 v116, 16, v38\n\t"
    "v_and_b32 v193, s92, v193\n\t"
    "s_waitcnt vmcnt(4)\n\t"
    "s_mov_b64 exec, 0xffffffff\n\t"
    "buffer_store_dword v165, v125, %12, s96 offen offset:0 sc1\n\t"
    "s_mov_b64 exec, -1\n\t"
    "s_add_u32 s61, s61, 0x2000\n\t"
    "buffer_store_dwordx4 v[20:23], v127, %12, s61 offen sc1\n\t"
    "s_waitcnt lgkmcnt(4)\n\t"
    "v_smfmac_f32_16x16x64_bf16 v[116:119], v[120:123], v[84:91], v124\n\t"
    "ds_read_b64 v[84:85], v192 offset:64000\n\t"
    "ds_read_b64 v[86:87], v252 offset:64000\n\t"
    "ds_read_b64 v[88:89], v193 offset:64000\n\t"
    "ds_read_b64 v[90:91], v253 offset:64000\n\t"
    "v_lshrrev_b32 v254, 16, v194\n\t"
    "v_and_b32 v194, s92, v194\n\t"
    "v_lshrrev_b32 v255, 16, v195\n\t"
    "v_and_b32 v195, s92, v195\n\t"
    "s_waitcnt lgkmcnt(4)\n\t"
    "v_smfmac_f32_16x16x64_bf16 v[116:119], v[120:123], v[92:99], v124\n\t"
    "ds_read_b64 v[92:93], v194 offset:64000\n\t"
    "ds_read_b64 v[94:95], v254 offset:64000\n\t"
    "ds_read_b64 v[96:97], v195 offset:64000\n\t"
    "ds_read_b64 v[98:99], v255 offset:64000\n\t"
    "s_sub_u32 s90, s90, 0x8000\n\t"
    "buffer_load_dwordx4 v[240:243], v127, %12, s90 offen offset:1024 sc1\n\t"
    "v_lshrrev_b32 v252, 16, v196\n\t"
    "v_and_b32 v196, s92, v196\n\t"
    "v_lshrrev_b32 v253, 16, v197\n\t"
    "v_and_b32 v197, s92, v197\n\t"
    "s_waitcnt lgkmcnt(4)\n\t"
    "v_smfmac_f32_16x16x64_bf16 v[116:119], v[120:123], v[84:91], v124\n\t"
    "ds_read_b64 v[84:85], v196 offset:0\n\t"
    "ds_read_b64 v[86:87], v252 offset:0\n\t"
    "ds_read_b64 v[88:89], v197 offset:0\n\t"
    "ds_read_b64 v[90:91], v253 offset:0\n\t"
    "v_lshrrev_b32 v254, 16, v198\n\t"
    "v_and_b32 v198, s92, v198\n\t"
    "v_lshrrev_b32 v255, 16, v199\n\t"
    "v_and_b32 v199, s92, v199\n\t"
    "s_waitcnt lgkmcnt(4)\n\t"
    "v_smfmac_f32_16x16x64_bf16 v[116:119], v[120:123], v[92:99], v124\n\t"
    "ds_read_b64 v[92:93], v198 offset:0\n\t"
    "ds_read_b64 v[94:95], v254 offset:0\n\t"
    "ds_read_b64 v[96:97], v199 offset:0\n\t"
    "ds_read_b64 v[98:99], v255 offset:0\n\t"
    "s_add_u32 s90, s90, 0x8000\n\t"
    "buffer_load_dwordx4 v[208:211], v127, %12, s90 offen offset:1024 sc1\n\t"
    "v_lshrrev_b32 v252, 16, v200\n\t"
    "v_and_b32 v200, s92, v200\n\t"
    "v_lshrrev_b32 v253, 16, v201\n\t"
    "v_cvt_pk_bf16_f32 v38, v116, v117\n\t"
    "v_cvt_pk_bf16_f32 v39, v118, v119\n\t"
    "v_and_b32 v119, s67, v45\n\t"
    "v_lshlrev_b32 v118, 16, v45\n\t"
    "v_and_b32 v117, s67, v44\n\t"
    "v_lshlrev_b32 v116, 16, v44\n\t"
    "v_and_b32 v201, s92, v201\n\t"
    "s_waitcnt lgkmcnt(4)\n\t"
    "v_smfmac_f32_16x16x64_bf16 v[116:119], v[120:123], v[84:91], v124\n\t"
    "ds_read_b64 v[84:85], v200 offset:64000\n\t"
    "ds_read_b64 v[86:87], v252 offset:64000\n\t"
    "ds_read_b64 v[88:89], v201 offset:64000\n\t"
    "ds_read_b64 v[90:91], v253 offset:64000\n\t"
    "v_lshrrev_b32 v254, 16, v202\n\t"
    "v_and_b32 v202, s92, v202\n\t"
    "v_lshrrev_b32 v255, 16, v203\n\t"
    "v_and_b32 v203, s92, v203\n\t"
    "s_waitcnt vmcnt(7) lgkmcnt(4)\n\t"
    "v_smfmac_f32_16x16x64_bf16 v[116:119], v[120:123], v[92:99], v124\n\t"
    "ds_read_b64 v[92:93], v202 offset:64000\n\t"
    "ds_read_b64 v[94:95], v254 offset:64000\n\t"
    "ds_read_b64 v[96:97], v203 offset:64000\n\t"
    "ds_read_b64 v[98:99], v255 offset:64000\n\t"
    "s_add_u32 s91, s91, s95\n\t"
    "buffer_load_dwordx4 v[244:247], v127, %12, s91 offen offset:0 sc1\n\t"
    "v_lshrrev_b32 v252, 16, v204\n\t"
    "v_and_b32 v204, s92, v204\n\t"
    "v_lshrrev_b32 v253, 16, v205\n\t"
    "v_and_b32 v205, s92, v205\n\t"
    "s_waitcnt lgkmcnt(4)\n\t"
    "v_smfmac_f32_16x16x64_bf16 v[116:119], v[120:123], v[84:91], v124\n\t"
    "ds_read_b64 v[84:85], v204 offset:0\n\t"
    "ds_read_b64 v[86:87], v252 offset:0\n\t"
    "ds_read_b64 v[88:89], v205 offset:0\n\t"
    "ds_read_b64 v[90:91], v253 offset:0\n\t"
    "v_lshrrev_b32 v254, 16, v206\n\t"
    "v_and_b32 v206, s92, v206\n\t"
    "v_lshrrev_b32 v255, 16, v207\n\t"
    "v_and_b32 v207, s92, v207\n\t"
    "s_waitcnt vmcnt(7) lgkmcnt(4)\n\t"
    "v_smfmac_f32_16x16x64_bf16 v[116:119], v[120:123], v[92:99], v124\n\t"
    "ds_read_b64 v[92:93], v206 offset:0\n\t"
    "ds_read_b64 v[94:95], v254 offset:0\n\t"
    "ds_read_b64 v[96:97], v207 offset:0\n\t"
    "ds_read_b64 v[98:99], v255 offset:0\n\t"
    "s_add_u32 s91, s91, 0x8000\n\t"
    "buffer_load_dwordx4 v[248:251], v127, %12, s91 offen offset:0 sc1\n\t"
    "v_lshrrev_b32 v252, 16, v144\n\t"
    "v_and_b32 v144, s92, v144\n\t"
    "v_lshrrev_b32 v253, 16, v145\n\t"
    "v_cvt_pk_bf16_f32 v44, v116, v117\n\t"
    "v_cvt_pk_bf16_f32 v45, v118, v119\n\t"
    "v_and_b32 v119, s67, v47\n\t"
    "v_lshlrev_b32 v118, 16, v47\n\t"
    "v_and_b32 v117, s67, v46\n\t"
    "v_lshlrev_b32 v116, 16, v46\n\t"
    "v_and_b32 v145, s92, v145\n\t"
    "s_add_u32 s61, s61, 0x2000\n\t"
    "buffer_store_dwordx4 v[28:31], v127, %12, s61 offen sc1\n\t"
    "s_waitcnt lgkmcnt(4)\n\t"
    "v_smfmac_f32_16x16x64_bf16 v[116:119], v[120:123], v[84:91], v124\n\t"
    "ds_read_b64 v[84:85], v144 offset:64000\n\t"
    "ds_read_b64 v[86:87], v252 offset:64000\n\t"
    "ds_read_b64 v[88:89], v145 offset:64000\n\t"
    "ds_read_b64 v[90:91], v253 offset:64000\n\t"
    "v_lshrrev_b32 v254, 16, v146\n\t"
    "v_and_b32 v146, s92, v146\n\t"
    "v_lshrrev_b32 v255, 16, v147\n\t"
    "v_and_b32 v147, s92, v147\n\t"
    "s_waitcnt vmcnt(8) lgkmcnt(4)\n\t"
    "v_smfmac_f32_16x16x64_bf16 v[116:119], v[120:123], v[92:99], v124\n\t"
    "ds_read_b64 v[92:93], v146 offset:64000\n\t"
    "ds_read_b64 v[94:95], v254 offset:64000\n\t"
    "ds_read_b64 v[96:97], v147 offset:64000\n\t"
    "ds_read_b64 v[98:99], v255 offset:64000\n\t"
    "s_sub_u32 s91, s91, 0x8000\n\t"
    "buffer_load_dwordx4 v[212:215], v127, %12, s91 offen offset:1024 sc1\n\t"
    "v_lshrrev_b32 v252, 16, v148\n\t"
    "v_and_b32 v148, s92, v148\n\t"
    "v_lshrrev_b32 v253, 16, v149\n\t"
    "v_and_b32 v149, s92, v149\n\t"
    "s_waitcnt lgkmcnt(4)\n\t"
    "v_smfmac_f32_16x16x64_bf16 v[116:119], v[120:123], v[84:91], v124\n\t"
    "ds_read_b64 v[84:85], v148 offset:0\n\t"
    "ds_read_b64 v[86:87], v252 offset:0\n\t"
    "ds_read_b64 v[88:89], v149 offset:0\n\t"
    "ds_read_b64 v[90:91], v253 offset:0\n\t"
    "v_lshrrev_b32 v254, 16, v150\n\t"
    "v_and_b32 v150, s92, v150\n\t"
    "v_lshrrev_b32 v255, 16, v151\n\t"
    "v_and_b32 v151, s92, v151\n\t"
    "s_waitcnt vmcnt(8) lgkmcnt(4)\n\t"
    "v_smfmac_f32_16x16x64_bf16 v[116:119], v[120:123], v[92:99], v124\n\t"
    "ds_read_b64 v[92:93], v150 offset:0\n\t"
    "ds_read_b64 v[94:95], v254 offset:0\n\t"
    "ds_read_b64 v[96:97], v151 offset:0\n\t"
    "ds_read_b64 v[98:99], v255 offset:0\n\t"
    "s_add_u32 s91, s91, 0x8000\n\t"
    "buffer_load_dwordx4 v[216:219], v127, %12, s91 offen offset:1024 sc1\n\t"
    "v_lshrrev_b32 v252, 16, v152\n\t"
    "v_and_b32 v152, s92, v152\n\t"
    "v_lshrrev_b32 v253, 16, v153\n\t"
    "v_cvt_pk_bf16_f32 v46, v116, v117\n\t"
    "v_cvt_pk_bf16_f32 v47, v118, v119\n\t"
    "v_and_b32 v119, s67, v53\n\t"
    "v_lshlrev_b32 v118, 16, v53\n\t"
    "v_and_b32 v117, s67, v52\n\t"
    "v_lshlrev_b32 v116, 16, v52\n\t"
    "v_and_b32 v153, s92, v153\n\t"
    "s_waitcnt lgkmcnt(4)\n\t"
    "v_smfmac_f32_16x16x64_bf16 v[116:119], v[120:123], v[84:91], v124\n\t"
    "ds_read_b64 v[84:85], v152 offset:64000\n\t"
    "ds_read_b64 v[86:87], v252 offset:64000\n\t"
    "ds_read_b64 v[88:89], v153 offset:64000\n\t"
    "ds_read_b64 v[90:91], v253 offset:64000\n\t"
    "v_lshrrev_b32 v254, 16, v154\n\t"
    "v_and_b32 v154, s92, v154\n\t"
    "v_lshrrev_b32 v255, 16, v155\n\t"
    "v_and_b32 v155, s92, v155\n\t"
    "s_waitcnt vmcnt(6) lgkmcnt(4)\n\t"
    "v_smfmac_f32_16x16x64_bf16 v[116:119], v[120:123], v[92:99], v124\n\t"
    "ds_read_b64 v[92:93], v154 offset:64000\n\t"
    "ds_read_b64 v[94:95], v254 offset:64000\n\t"
    "ds_read_b64 v[96:97], v155 offset:64000\n\t"
    "ds_read_b64 v[98:99], v255 offset:64000\n\t"
    "s_sub_u32 s84, s84, s95\n\t"
    "buffer_load_dwordx4 v[220:223], v127, %12, s84 offen offset:0 sc1\n\t"
    "v_lshrrev_b32 v252, 16, v240\n\t"
    "v_and_b32 v240, s92, v240\n\t"
    "v_lshrrev_b32 v253, 16, v241\n\t"
    "v_and_b32 v241, s92, v241\n\t"
    "s_waitcnt lgkmcnt(4)\n\t"
    "v_smfmac_f32_16x16x64_bf16 v[116:119], v[120:123], v[84:91], v124\n\t"
    "ds_read_b64 v[84:85], v240 offset:0\n\t"
    "ds_read_b64 v[86:87], v252 offset:0\n\t"
    "ds_read_b64 v[88:89], v241 offset:0\n\t"
    "ds_read_b64 v[90:91], v253 offset:0\n\t"
    "v_lshrrev_b32 v254, 16, v242\n\t"
    "v_and_b32 v242, s92, v242\n\t"
    "v_lshrrev_b32 v255, 16, v243\n\t"
    "v_and_b32 v243, s92, v243\n\t"
    "s_cmp_lt_u32 %16, 4\n\t"
    "s_cbranch_scc1 L_tp195_%=\n\t"
    "s_setprio 0\n\t"
    "L_tp195_%=:\n\t"
    "s_waitcnt vmcnt(6) lgkmcnt(4)\n\t"
    "v_smfmac_f32_16x16x64_bf16 v[116:119], v[120:123], v[92:99], v124\n\t"
    "ds_read_b64 v[92:93], v242 offset:0\n\t"
    "ds_read_b64 v[94:95], v254 offset:0\n\t"
    "ds_read_b64 v[96:97], v243 offset:0\n\t"
    "ds_read_b64 v[98:99], v255 offset:0\n\t"
    "buffer_load_dwordx4 v[224:227], v127, %12, s84 offen offset:1024 sc1\n\t"
    "v_lshrrev_b32 v252, 16, v208\n\t"
    "v_and_b32 v208, s92, v208\n\t"
    "v_lshrrev_b32 v253, 16, v209\n\t"
    "v_cvt_pk_bf16_f32 v52, v116, v117\n\t"
    "v_cvt_pk_bf16_f32 v53, v118, v119\n\t"
    "v_and_b32 v119, s67, v55\n\t"
    "v_lshlrev_b32 v118, 16, v55\n\t"
    "v_and_b32 v117, s67, v54\n\t"
    "v_lshlrev_b32 v116, 16, v54\n\t"
    "v_and_b32 v209, s92, v209\n\t"
    "s_waitcnt vmcnt(4)\n\t"
    "s_mov_b64 exec, 0xffffffff\n\t"
    "buffer_store_dword v165, v125, %12, s96 offen offset:128 sc1\n\t"
    "s_mov_b64 exec, -1\n\t"
    "s_add_u32 s61, s61, 0x2000\n\t"
    "buffer_store_dwordx4 v[36:39], v127, %12, s61 offen sc1\n\t"
    "s_waitcnt lgkmcnt(4)\n\t"
    "v_smfmac_f32_16x16x64_bf16 v[116:119], v[120:123], v[84:91], v124\n\t"
    "ds_read_b64 v[84:85], v208 offset:64000\n\t"
    "ds_read_b64 v[86:87], v252 offset:64000\n\t"
    "ds_read_b64 v[88:89], v209 offset:64000\n\t"
    "ds_read_b64 v[90:91], v253 offset:64000\n\t"
    "v_lshrrev_b32 v254, 16, v210\n\t"
    "v_and_b32 v210, s92, v210\n\t"
    "v_lshrrev_b32 v255, 16, v211\n\t"
    "v_and_b32 v211, s92, v211\n\t"
    "s_waitcnt lgkmcnt(4)\n\t"
    "v_smfmac_f32_16x16x64_bf16 v[116:119], v[120:123], v[92:99], v124\n\t"
    "ds_read_b64 v[92:93], v210 offset:64000\n\t"
    "ds_read_b64 v[94:95], v254 offset:64000\n\t"
    "ds_read_b64 v[96:97], v211 offset:64000\n\t"
    "ds_read_b64 v[98:99], v255 offset:64000\n\t"
    "s_sub_u32 s85, s85, s95\n\t"
    "buffer_load_dwordx4 v[228:231], v127, %12, s85 offen offset:0 sc1\n\t"
    "v_lshrrev_b32 v252, 16, v244\n\t"
    "v_and_b32 v244, s92, v244\n\t"
    "v_lshrrev_b32 v253, 16, v245\n\t"
    "v_and_b32 v245, s92, v245\n\t"
    "s_waitcnt lgkmcnt(4)\n\t"
    "v_smfmac_f32_16x16x64_bf16 v[116:119], v[120:123], v[84:91], v124\n\t"
    "ds_read_b64 v[84:85], v244 offset:0\n\t"
    "ds_read_b64 v[86:87], v252 offset:0\n\t"
    "ds_read_b64 v[88:89], v245 offset:0\n\t"
    "ds_read_b64 v[90:91], v253 offset:0\n\t"
    "v_lshrrev_b32 v254, 16, v246\n\t"
    "v_and_b32 v246, s92, v246\n\t"
    "v_lshrrev_b32 v255, 16, v247\n\t"
    "v_and_b32 v247, s92, v247\n\t"
    "s_waitcnt lgkmcnt(4)\n\t"
    "v_smfmac_f32_16x16x64_bf16 v[116:119], v[120:123], v[92:99], v124\n\t"
    "ds_read_b64 v[92:93], v246 offset:0\n\t"
    "ds_read_b64 v[94:95], v254 offset:0\n\t"
    "ds_read_b64 v[96:97], v247 offset:0\n\t"
    "ds_read_b64 v[98:99], v255 offset:0\n\t"
    "buffer_load_dwordx4 v[232:235], v127, %12, s85 offen offset:1024 sc1\n\t"
    "v_lshrrev_b32 v252, 16, v248\n\t"
    "v_and_b32 v248, s92, v248\n\t"
    "v_lshrrev_b32 v253, 16, v249\n\t"
    "v_cvt_pk_bf16_f32 v54, v116, v117\n\t"
    "v_cvt_pk_bf16_f32 v55, v118, v119\n\t"
    "v_and_b32 v119, s67, v61\n\t"
    "v_lshlrev_b32 v118, 16, v61\n\t"
    "v_and_b32 v117, s67, v60\n\t"
    "v_lshlrev_b32 v116, 16, v60\n\t"
    "v_and_b32 v249, s92, v249\n\t"
    "s_waitcnt lgkmcnt(4)\n\t"
    "v_smfmac_f32_16x16x64_bf16 v[116:119], v[120:123], v[84:91], v124\n\t"
    "ds_read_b64 v[84:85], v248 offset:64000\n\t"
    "ds_read_b64 v[86:87], v252 offset:64000\n\t"
    "ds_read_b64 v[88:89], v249 offset:64000\n\t"
    "ds_read_b64 v[90:91], v253 offset:64000\n\t"
    "v_lshrrev_b32 v254, 16, v250\n\t"
    "v_and_b32 v250, s92, v250\n\t"
    "v_lshrrev_b32 v255, 16, v251\n\t"
    "v_and_b32 v251, s92, v251\n\t"
    "s_waitcnt vmcnt(7) lgkmcnt(4)\n\t"
    "v_smfmac_f32_16x16x64_bf16 v[116:119], v[120:123], v[92:99], v124\n\t"
    "ds_read_b64 v[92:93], v250 offset:64000\n\t"
    "ds_read_b64 v[94:95], v254 offset:64000\n\t"
    "ds_read_b64 v[96:97], v251 offset:64000\n\t"
    "ds_read_b64 v[98:99], v255 offset:64000\n\t"
    "s_sub_u32 s86, s86, s95\n\t"
    "buffer_load_dwordx4 v[168:171], v127, %12, s86 offen offset:0 sc1\n\t"
    "v_lshrrev_b32 v252, 16, v212\n\t"
    "v_and_b32 v212, s92, v212\n\t"
    "v_lshrrev_b32 v253, 16, v213\n\t"
    "v_and_b32 v213, s92, v213\n\t"
    "s_waitcnt lgkmcnt(4)\n\t"
    "v_smfmac_f32_16x16x64_bf16 v[116:119], v[120:123], v[84:91], v124\n\t"
    "ds_read_b64 v[84:85], v212 offset:0\n\t"
    "ds_read_b64 v[86:87], v252 offset:0\n\t"
    "ds_read_b64 v[88:89], v213 offset:0\n\t"
    "ds_read_b64 v[90:91], v253 offset:0\n\t"
    "v_lshrrev_b32 v254, 16, v214\n\t"
    "v_and_b32 v214, s92, v214\n\t"
    "v_lshrrev_b32 v255, 16, v215\n\t"
    "v_and_b32 v215, s92, v215\n\t"
    "s_waitcnt vmcnt(7) lgkmcnt(4)\n\t"
    "v_smfmac_f32_16x16x64_bf16 v[116:119], v[120:123], v[92:99], v124\n\t"
    "ds_read_b64 v[92:93], v214 offset:0\n\t"
    "ds_read_b64 v[94:95], v254 offset:0\n\t"
    "ds_read_b64 v[96:97], v215 offset:0\n\t"
    "ds_read_b64 v[98:99], v255 offset:0\n\t"
    "s_add_u32 s84, s84, 0x8000\n\t"
    "buffer_load_dwordx4 v[112:115], v127, %12, s84 offen offset:0 sc1\n\t"
    "v_lshrrev_b32 v252, 16, v216\n\t"
    "v_and_b32 v216, s92, v216\n\t"
    "v_lshrrev_b32 v253, 16, v217\n\t"
    "v_cvt_pk_bf16_f32 v60, v116, v117\n\t"
    "v_cvt_pk_bf16_f32 v61, v118, v119\n\t"
    "v_and_b32 v119, s67, v63\n\t"
    "v_lshlrev_b32 v118, 16, v63\n\t"
    "v_and_b32 v117, s67, v62\n\t"
    "v_lshlrev_b32 v116, 16, v62\n\t"
    "v_and_b32 v217, s92, v217\n\t"
    "s_add_u32 s61, s61, 0x2000\n\t"
    "buffer_store_dwordx4 v[44:47], v127, %12, s61 offen sc1\n\t"
    "s_waitcnt lgkmcnt(4)\n\t"
    "v_smfmac_f32_16x16x64_bf16 v[116:119], v[120:123], v[84:91], v124\n\t"
    "ds_read_b64 v[84:85], v216 offset:64000\n\t"
    "ds_read_b64 v[86:87], v252 offset:64000\n\t"
    "ds_read_b64 v[88:89], v217 offset:64000\n\t"
    "ds_read_b64 v[90:91], v253 offset:64000\n\t"
    "v_lshrrev_b32 v254, 16, v218\n\t"
    "v_and_b32 v218, s92, v218\n\t"
    "v_lshrrev_b32 v255, 16, v219\n\t"
    "v_and_b32 v219, s92, v219\n\t"
    "s_mov_b64 exec, 1\n\t"
    "buffer_load_dword v164, off, %12, s93 offset:0 sc1\n\t"
    "s_mov_b64 exec, -1\n\t"
    "s_waitcnt vmcnt(9) lgkmcnt(4)\n\t"
    "v_smfmac_f32_16x16x64_bf16 v[116:119], v[120:123], v[92:99], v124\n\t"
    "ds_read_b64 v[92:93], v218 offset:64000\n\t"
    "ds_read_b64 v[94:95], v254 offset:64000\n\t"
    "ds_read_b64 v[96:97], v219 offset:64000\n\t"
    "ds_read_b64 v[98:99], v255 offset:64000\n\t"
    "buffer_load_dwordx4 v[100:103], v127, %12, s86 offen offset:1024 sc1\n\t"
    "v_lshrrev_b32 v252, 16, v220\n\t"
    "v_and_b32 v220, s92, v220\n\t"
    "v_lshrrev_b32 v253, 16, v221\n\t"
    "v_and_b32 v221, s92, v221\n\t"
    "s_waitcnt lgkmcnt(4)\n\t"
    "v_smfmac_f32_16x16x64_bf16 v[116:119], v[120:123], v[84:91], v124\n\t"
    "ds_read_b64 v[84:85], v220 offset:0\n\t"
    "ds_read_b64 v[86:87], v252 offset:0\n\t"
    "ds_read_b64 v[88:89], v221 offset:0\n\t"
    "ds_read_b64 v[90:91], v253 offset:0\n\t"
    "v_lshrrev_b32 v254, 16, v222\n\t"
    "v_and_b32 v222, s92, v222\n\t"
    "v_lshrrev_b32 v255, 16, v223\n\t"
    "v_and_b32 v223, s92, v223\n\t"
    "s_cmp_lt_u32 %16, 4\n\t"
    "s_cbranch_scc1 L_tp207_%=\n\t"
    "s_setprio 1\n\t"
    "L_tp207_%=:\n\t"
    "s_waitcnt vmcnt(9) lgkmcnt(4)\n\t"
    "v_smfmac_f32_16x16x64_bf16 v[116:119], v[120:123], v[92:99], v124\n\t"
    "ds_read_b64 v[92:93], v222 offset:0\n\t"
    "ds_read_b64 v[94:95], v254 offset:0\n\t"
    "ds_read_b64 v[96:97], v223 offset:0\n\t"
    "ds_read_b64 v[98:99], v255 offset:0\n\t"
    "buffer_load_dwordx4 v[104:107], v127, %12, s84 offen offset:1024 sc1\n\t"
    "v_lshrrev_b32 v252, 16, v224\n\t"
    "v_and_b32 v224, s92, v224\n\t"
    "v_lshrrev_b32 v253, 16, v225\n\t"
    "v_cvt_pk_bf16_f32 v62, v116, v117\n\t"
    "v_cvt_pk_bf16_f32 v63, v118, v119\n\t"
    "v_mov_b64 v[172:173], s[70:71]\n\t"
    "v_mov_b64 v[174:175], s[72:73]\n\t"
    "v_and_b32 v225, s92, v225\n\t"
    "s_waitcnt lgkmcnt(4)\n\t"
    "v_smfmac_f32_16x16x64_bf16 v[172:175], v[120:123], v[84:91], v124\n\t"
    "ds_read_b64 v[84:85], v224 offset:0\n\t"
    "ds_read_b64 v[86:87], v252 offset:0\n\t"
    "ds_read_b64 v[88:89], v225 offset:0\n\t"
    "ds_read_b64 v[90:91], v253 offset:0\n\t"
    "v_lshrrev_b32 v254, 16, v226\n\t"
    "v_and_b32 v226, s92, v226\n\t"
    "v_lshrrev_b32 v255, 16, v227\n\t"
    "v_and_b32 v227, s92, v227\n\t"
    "s_waitcnt vmcnt(7) lgkmcnt(4)\n\t"
    "v_smfmac_f32_16x16x64_bf16 v[172:175], v[120:123], v[92:99], v124\n\t"
    "ds_read_b64 v[92:93], v226 offset:0\n\t"
    "ds_read_b64 v[94:95], v254 offset:0\n\t"
    "ds_read_b64 v[96:97], v227 offset:0\n\t"
    "ds_read_b64 v[98:99], v255 offset:0\n\t"
    "s_sub_u32 s87, s87, s95\n\t"
    "buffer_load_dwordx4 v[108:111], v127, %12, s87 offen offset:0 sc1\n\t"
    "v_lshrrev_b32 v252, 16, v228\n\t"
    "v_and_b32 v228, s92, v228\n\t"
    "v_lshrrev_b32 v253, 16, v229\n\t"
    "v_mov_b64 v[176:177], s[70:71]\n\t"
    "v_mov_b64 v[178:179], s[72:73]\n\t"
    "v_and_b32 v229, s92, v229\n\t"
    "s_waitcnt lgkmcnt(4)\n\t"
    "v_smfmac_f32_16x16x64_bf16 v[176:179], v[120:123], v[84:91], v124\n\t"
    "ds_read_b64 v[84:85], v228 offset:0\n\t"
    "ds_read_b64 v[86:87], v252 offset:0\n\t"
    "ds_read_b64 v[88:89], v229 offset:0\n\t"
    "ds_read_b64 v[90:91], v253 offset:0\n\t"
    "v_lshrrev_b32 v254, 16, v230\n\t"
    "v_and_b32 v230, s92, v230\n\t"
    "v_lshrrev_b32 v255, 16, v231\n\t"
    "v_and_b32 v231, s92, v231\n\t"
    "s_waitcnt vmcnt(3)\n\t"
    "v_readfirstlane_b32 s65, v164\n\t"
    "s_nop 0\n\t"
    "s_cmp_eq_u32 s65, 0\n\t"
    "s_cbranch_scc1 L_xw0_%=\n\t"
    "L_xwb0_%=:\n\t"
    "buffer_load_dwordx4 v[4:7], v127, %12, s94 offen sc1\n\t"
    "s_waitcnt lgkmcnt(4)\n\t"
    "v_smfmac_f32_16x16x64_bf16 v[176:179], v[120:123], v[92:99], v124\n\t"
    "ds_read_b64 v[92:93], v230 offset:0\n\t"
    "ds_read_b64 v[94:95], v254 offset:0\n\t"
    "ds_read_b64 v[96:97], v231 offset:0\n\t"
    "ds_read_b64 v[98:99], v255 offset:0\n\t"
    "s_add_u32 s85, s85, 0x8000\n\t"
    "buffer_load_dwordx4 v[200:203], v127, %12, s85 offen offset:0 sc1\n\t"
    "v_lshrrev_b32 v252, 16, v232\n\t"
    "v_and_b32 v232, s92, v232\n\t"
    "v_lshrrev_b32 v253, 16, v233\n\t"
    "v_mov_b64 v[180:181], s[70:71]\n\t"
    "v_mov_b64 v[182:183], s[72:73]\n\t"
    "v_and_b32 v233, s92, v233\n\t"
    "s_mov_b64 exec, 0xffffffff\n\t"
    "buffer_store_dword v165, v125, %12, s96 offen offset:256 sc1\n\t"
    "s_mov_b64 exec, -1\n\t"
    "s_add_u32 s61, s61, 0x2000\n\t"
    "buffer_store_dwordx4 v[52:55], v127, %12, s61 offen sc1\n\t"
    "s_waitcnt lgkmcnt(4)\n\t"
    "v_smfmac_f32_16x16x64_bf16 v[180:183], v[120:123], v[84:91], v124\n\t"
    "ds_read_b64 v[84:85], v232 offset:0\n\t"
    "ds_read_b64 v[86:87], v252 offset:0\n\t"
    "ds_read_b64 v[88:89], v233 offset:0\n\t"
    "ds_read_b64 v[90:91], v253 offset:0\n\t"
    "v_lshrrev_b32 v254, 16, v234\n\t"
    "v_and_b32 v234, s92, v234\n\t"
    "v_lshrrev_b32 v255, 16, v235\n\t"
    "v_and_b32 v235, s92, v235\n\t"
    "s_waitcnt lgkmcnt(4)\n\t"
    "v_smfmac_f32_16x16x64_bf16 v[180:183], v[120:123], v[92:99], v124\n\t"
    "ds_read_b64 v[92:93], v234 offset:0\n\t"
    "ds_read_b64 v[94:95], v254 offset:0\n\t"
    "ds_read_b64 v[96:97], v235 offset:0\n\t"
    "ds_read_b64 v[98:99], v255 offset:0\n\t"
    "buffer_load_dwordx4 v[204:207], v127, %12, s87 offen offset:1024 sc1\n\t"
    "v_lshrrev_b32 v252, 16, v168\n\t"
    "v_and_b32 v168, s92, v168\n\t"
    "v_lshrrev_b32 v253, 16, v169\n\t"
    "v_mov_b64 v[184:185], s[70:71]\n\t"
    "v_mov_b64 v[186:187], s[72:73]\n\t"
    "v_and_b32 v169, s92, v169\n\t"
    "s_waitcnt lgkmcnt(4)\n\t"
    "v_smfmac_f32_16x16x64_bf16 v[184:187], v[120:123], v[84:91], v124\n\t"
    "ds_read_b64 v[84:85], v168 offset:0\n\t"
    "ds_read_b64 v[86:87], v252 offset:0\n\t"
    "ds_read_b64 v[88:89], v169 offset:0\n\t"
    "ds_read_b64 v[90:91], v253 offset:0\n\t"
    "v_lshrrev_b32 v254, 16, v170\n\t"
    "v_and_b32 v170, s92, v170\n\t"
    "v_lshrrev_b32 v255, 16, v171\n\t"
    "v_and_b32 v171, s92, v171\n\t"
    "s_add_u32 s94, s94, 0x2000\n\t"
    "buffer_load_dwordx4 v[12:15], v127, %12, s94 offen sc1\n\t"
    "s_waitcnt lgkmcnt(4)\n\t"
    "v_smfmac_f32_16x16x64_bf16 v[184:187], v[120:123], v[92:99], v124\n\t"
    "ds_read_b64 v[92:93], v170 offset:0\n\t"
    "ds_read_b64 v[94:95], v254 offset:0\n\t"
    "ds_read_b64 v[96:97], v171 offset:0\n\t"
    "ds_read_b64 v[98:99], v255 offset:0\n\t"
    "buffer_load_dwordx4 v[144:147], v127, %12, s85 offen offset:1024 sc1\n\t"
    "v_lshrrev_b32 v252, 16, v112\n\t"
    "v_and_b32 v112, s92, v112\n\t"
    "v_lshrrev_b32 v253, 16, v113\n\t"
    "v_mov_b64 v[188:189], s[70:71]\n\t"
    "v_mov_b64 v[190:191], s[72:73]\n\t"
    "v_and_b32 v113, s92, v113\n\t"
    "s_waitcnt lgkmcnt(4)\n\t"
    "v_smfmac_f32_16x16x64_bf16 v[188:191], v[120:123], v[84:91], v124\n\t"
    "ds_read_b64 v[84:85], v112 offset:64000\n\t"
    "ds_read_b64 v[86:87], v252 offset:64000\n\t"
    "ds_read_b64 v[88:89], v113 offset:64000\n\t"
    "ds_read_b64 v[90:91], v253 offset:64000\n\t"
    "v_lshrrev_b32 v254, 16, v114\n\t"
    "v_and_b32 v114, s92, v114\n\t"
    "v_lshrrev_b32 v255, 16, v115\n\t"
    "v_and_b32 v115, s92, v115\n\t"
    "s_waitcnt vmcnt(9) lgkmcnt(4)\n\t"
    "v_smfmac_f32_16x16x64_bf16 v[188:191], v[120:123], v[92:99], v124\n\t"
    "ds_read_b64 v[92:93], v114 offset:64000\n\t"
    "ds_read_b64 v[94:95], v254 offset:64000\n\t"
    "ds_read_b64 v[96:97], v115 offset:64000\n\t"
    "ds_read_b64 v[98:99], v255 offset:64000\n\t"
    "s_sub_u32 s88, s88, s95\n\t"
    "buffer_load_dwordx4 v[148:151], v127, %12, s88 offen offset:0 sc1\n\t"
    "v_lshrrev_b32 v252, 16, v100\n\t"
    "v_and_b32 v100, s92, v100\n\t"
    "v_lshrrev_b32 v253, 16, v101\n\t"
    "v_and_b32 v101, s92, v101\n\t"
    "s_waitcnt lgkmcnt(4)\n\t"
    "v_smfmac_f32_16x16x64_bf16 v[172:175], v[120:123], v[84:91], v124\n\t"
    "ds_read_b64 v[84:85], v100 offset:0\n\t"
    "ds_read_b64 v[86:87], v252 offset:0\n\t"
    "ds_read_b64 v[88:89], v101 offset:0\n\t"
    "ds_read_b64 v[90:91], v253 offset:0\n\t"
    "v_lshrrev_b32 v254, 16, v102\n\t"
    "v_and_b32 v102, s92, v102\n\t"
    "v_lshrrev_b32 v255, 16, v103\n\t"
    "v_and_b32 v103, s92, v103\n\t"
    "s_waitcnt vmcnt(7)\n\t"
    "v_smfmac_f32_16x16x64_bf16 v[172:175], v[156:159], v[0:7], v124\n\t"
    "s_waitcnt lgkmcnt(4)\n\t"
    "v_smfmac_f32_16x16x64_bf16 v[172:175], v[120:123], v[92:99], v124\n\t"
    "ds_read_b64 v[92:93], v102 offset:0\n\t"
    "ds_read_b64 v[94:95], v254 offset:0\n\t"
    "ds_read_b64 v[96:97], v103 offset:0\n\t"
    "ds_read_b64 v[98:99], v255 offset:0\n\t"
    "s_add_u32 s86, s86, 0x8000\n\t"
    "buffer_load_dwordx4 v[152:155], v127, %12, s86 offen offset:0 sc1\n\t"
    "v_lshrrev_b32 v252, 16, v104\n\t"
    "v_and_b32 v104, s92, v104\n\t"
    "v_lshrrev_b32 v253, 16, v105\n\t"
    "v_mov_b64 v[192:193], s[70:71]\n\t"
    "v_mov_b64 v[194:195], s[72:73]\n\t"
    "v_and_b32 v105, s92, v105\n\t"
    "s_add_u32 s61, s61, 0x2000\n\t"
    "buffer_store_dwordx4 v[60:63], v127, %12, s61 offen sc1\n\t"
    "v_smfmac_f32_16x16x64_bf16 v[176:179], v[160:163], v[0:7], v124\n\t"
    "v_pk_mul_f32 v[64:65], v[172:173], v[172:173]\n\t"
    "v_pk_mul_f32 v[66:67], v[174:175], v[174:175]\n\t"
    "v_pk_fma_f32 v[64:65], v[64:65], v[82:83], v[80:81]\n\t"
    "v_pk_fma_f32 v[66:67], v[66:67], v[82:83], v[80:81]\n\t"
    "v_pk_mul_f32 v[64:65], v[64:65], v[172:173]\n\t"
    "s_waitcnt lgkmcnt(4)\n\t"
    "v_smfmac_f32_16x16x64_bf16 v[192:195], v[120:123], v[84:91], v124\n\t"
    "ds_read_b64 v[84:85], v104 offset:64000\n\t"
    "ds_read_b64 v[86:87], v252 offset:64000\n\t"
    "ds_read_b64 v[88:89], v105 offset:64000\n\t"
    "ds_read_b64 v[90:91], v253 offset:64000\n\t"
    "v_lshrrev_b32 v254, 16, v106\n\t"
    "v_and_b32 v106, s92, v106\n\t"
    "v_lshrrev_b32 v255, 16, v107\n\t"
    "v_and_b32 v107, s92, v107\n\t"
    "s_mov_b64 exec, 1\n\t"
    "buffer_load_dword v164, off, %12, s93 offset:128 sc1\n\t"
    "s_mov_b64 exec, -1\n\t"
    "v_pk_mul_f32 v[66:67], v[66:67], v[174:175]\n\t"
    "v_exp_f32 v68, v64\n\t"
    "v_exp_f32 v69, v65\n\t"
    "v_exp_f32 v70, v66\n\t"
    "v_exp_f32 v71, v67\n\t"
    "s_waitcnt lgkmcnt(4)\n\t"
    "v_smfmac_f32_16x16x64_bf16 v[192:195], v[120:123], v[92:99], v124\n\t"
    "ds_read_b64 v[92:93], v106 offset:64000\n\t"
    "ds_read_b64 v[94:95], v254 offset:64000\n\t"
    "ds_read_b64 v[96:97], v107 offset:64000\n\t"
    "ds_read_b64 v[98:99], v255 offset:64000\n\t"
    "buffer_load_dwordx4 v[244:247], v127, %12, s88 offen offset:1024 sc1\n\t"
    "v_lshrrev_b32 v252, 16, v108\n\t"
    "v_and_b32 v108, s92, v108\n\t"
    "v_lshrrev_b32 v253, 16, v109\n\t"
    "v_and_b32 v109, s92, v109\n\t"
    "v_pk_fma_f32 v[68:69], v[68:69], s[40:41], s[40:41]\n\t"
    "v_pk_fma_f32 v[70:71], v[70:71], s[42:43], s[42:43]\n\t"
    "v_rcp_f32 v68, v68\n\t"
    "v_rcp_f32 v69, v69\n\t"
    "v_rcp_f32 v70, v70\n\t"
    "s_waitcnt lgkmcnt(4)\n\t"
    "v_smfmac_f32_16x16x64_bf16 v[176:179], v[120:123], v[84:91], v124\n\t"
    "ds_read_b64 v[84:85], v108 offset:0\n\t"
    "ds_read_b64 v[86:87], v252 offset:0\n\t"
    "ds_read_b64 v[88:89], v109 offset:0\n\t"
    "ds_read_b64 v[90:91], v253 offset:0\n\t"
    "v_lshrrev_b32 v254, 16, v110\n\t"
    "v_and_b32 v110, s92, v110\n\t"
    "v_lshrrev_b32 v255, 16, v111\n\t"
    "v_and_b32 v111, s92, v111\n\t"
    "s_waitcnt vmcnt(6)\n\t"
    "v_smfmac_f32_16x16x64_bf16 v[180:183], v[156:159], v[8:15], v124\n\t"
    "v_rcp_f32 v71, v71\n\t"
    "v_pk_mul_f32 v[68:69], v[68:69], v[172:173]\n\t"
    "v_pk_fma_f32 v[68:69], v[70:71], v[174:175], v[68:69]\n\t"
    "v_add_f32 v236, v68, v69\n\t"
    "s_waitcnt lgkmcnt(4)\n\t"
    "v_smfmac_f32_16x16x64_bf16 v[176:179], v[120:123], v[92:99], v124\n\t"
    "ds_read_b64 v[92:93], v110 offset:0\n\t"
    "ds_read_b64 v[94:95], v254 offset:0\n\t"
    "ds_read_b64 v[96:97], v111 offset:0\n\t"
    "ds_read_b64 v[98:99], v255 offset:0\n\t"
    "buffer_load_dwordx4 v[248:251], v127, %12, s86 offen offset:1024 sc1\n\t"
    "v_lshrrev_b32 v252, 16, v200\n\t"
    "v_and_b32 v200, s92, v200\n\t"
    "v_lshrrev_b32 v253, 16, v201\n\t"
    "v_mov_b64 v[196:197], s[70:71]\n\t"
    "v_mov_b64 v[198:199], s[72:73]\n\t"
    "v_and_b32 v201, s92, v201\n\t"
    "v_smfmac_f32_16x16x64_bf16 v[184:187], v[160:163], v[8:15], v124\n\t"
    "v_pk_mul_f32 v[72:73], v[176:177], v[176:177]\n\t"
    "v_pk_mul_f32 v[74:75], v[178:179], v[178:179]\n\t"
    "v_pk_fma_f32 v[72:73], v[72:73], v[82:83], v[80:81]\n\t"
    "v_pk_fma_f32 v[74:75], v[74:75], v[82:83], v[80:81]\n\t"
    "v_pk_mul_f32 v[72:73], v[72:73], v[176:177]\n\t"
    "s_waitcnt lgkmcnt(4)\n\t"
    "v_smfmac_f32_16x16x64_bf16 v[196:199], v[120:123], v[84:91], v124\n\t"
    "ds_read_b64 v[84:85], v200 offset:64000\n\t"
    "ds_read_b64 v[86:87], v252 offset:64000\n\t"
    "ds_read_b64 v[88:89], v201 offset:64000\n\t"
    "ds_read_b64 v[90:91], v253 offset:64000\n\t"
    "v_lshrrev_b32 v254, 16, v202\n\t"
    "v_and_b32 v202, s92, v202\n\t"
    "v_lshrrev_b32 v255, 16, v203\n\t"
    "v_and_b32 v203, s92, v203\n\t"
    "v_pk_mul_f32 v[74:75], v[74:75], v[178:179]\n\t"
    "v_exp_f32 v76, v72\n\t"
    "v_exp_f32 v77, v73\n\t"
    "v_exp_f32 v78, v74\n\t"
    "v_exp_f32 v79, v75\n\t"
    "s_waitcnt lgkmcnt(4)\n\t"
    "v_smfmac_f32_16x16x64_bf16 v[196:199], v[120:123], v[92:99], v124\n\t"
    "ds_read_b64 v[92:93], v202 offset:64000\n\t"
    "ds_read_b64 v[94:95], v254 offset:64000\n\t"
    "ds_read_b64 v[96:97], v203 offset:64000\n\t"
    "ds_read_b64 v[98:99], v255 offset:64000\n\t"
    "s_sub_u32 s89, s89, s95\n\t"
    "buffer_load_dwordx4 v[212:215], v127, %12, s89 offen offset:0 sc1\n\t"
    "v_lshrrev_b32 v252, 16, v204\n\t"
    "v_and_b32 v204, s92, v204\n\t"
    "v_lshrrev_b32 v253, 16, v205\n\t"
    "v_and_b32 v205, s92, v205\n\t"
    "v_pk_fma_f32 v[76:77], v[76:77], s[40:41], s[40:41]\n\t"
    "v_pk_fma_f32 v[78:79], v[78:79], s[42:43], s[42:43]\n\t"
    "v_rcp_f32 v76, v76\n\t"
    "v_rcp_f32 v77, v77\n\t"
    "v_rcp_f32 v78, v78\n\t"
    "s_waitcnt lgkmcnt(4)\n\t"
    "v_smfmac_f32_16x16x64_bf16 v[180:183], v[120:123], v[84:91], v124\n\t"
    "ds_read_b64 v[84:85], v204 offset:0\n\t"
    "ds_read_b64 v[86:87], v252 offset:0\n\t"
    "ds_read_b64 v[88:89], v205 offset:0\n\t"
    "ds_read_b64 v[90:91], v253 offset:0\n\t"
    "v_lshrrev_b32 v254, 16, v206\n\t"
    "v_and_b32 v206, s92, v206\n\t"
    "v_lshrrev_b32 v255, 16, v207\n\t"
    "v_and_b32 v207, s92, v207\n\t"
    "s_waitcnt vmcnt(3)\n\t"
    "v_readfirstlane_b32 s65, v164\n\t"
    "s_nop 0\n\t"
    "s_cmp_eq_u32 s65, 0\n\t"
    "s_cbranch_scc1 L_xw1_%=\n\t"
    "L_xwb1_%=:\n\t"
    "s_add_u32 s94, s94, 0x2000\n\t"
    "buffer_load_dwordx4 v[20:23], v127, %12, s94 offen sc1\n\t"
    "v_rcp_f32 v79, v79\n\t"
    "v_pk_mul_f32 v[76:77], v[76:77], v[176:177]\n\t"
    "v_pk_fma_f32 v[76:77], v[78:79], v[178:179], v[76:77]\n\t"
    "v_add_f32 v237, v76, v77\n\t"
    "s_cmp_lt_u32 %16, 4\n\t"
    "s_cbranch_scc1 L_tp227_%=\n\t"
    "s_setprio 0\n\t"
    "L_tp227_%=:\n\t"
    "s_waitcnt lgkmcnt(4)\n\t"
    "v_smfmac_f32_16x16x64_bf16 v[180:183], v[120:123], v[92:99], v124\n\t"
    "ds_read_b64 v[92:93], v206 offset:0\n\t"
    "ds_read_b64 v[94:95], v254 offset:0\n\t"
    "ds_read_b64 v[96:97], v207 offset:0\n\t"
    "ds_read_b64 v[98:99], v255 offset:0\n\t"
    "s_add_u32 s87, s87, 0x8000\n\t"
    "buffer_load_dwordx4 v[216:219], v127, %12, s87 offen offset:0 sc1\n\t"
    "v_lshrrev_b32 v252, 16, v144\n\t"
    "v_and_b32 v144, s92, v144\n\t"
    "v_lshrrev_b32 v253, 16, v145\n\t"
    "v_mov_b64 v[200:201], s[70:71]\n\t"
    "v_mov_b64 v[202:203], s[72:73]\n\t"
    "v_and_b32 v145, s92, v145\n\t"
    "s_mov_b64 exec, 0xffffffff\n\t"
    "buffer_store_dword v165, v125, %12, s96 offen offset:384 sc1\n\t"
    "s_mov_b64 exec, -1\n\t"
    "v_pk_mul_f32 v[64:65], v[180:181], v[180:181]\n\t"
    "v_pk_mul_f32 v[66:67], v[182:183], v[182:183]\n\t"
    "v_pk_fma_f32 v[64:65], v[64:65], v[82:83], v[80:81]\n\t"
    "v_pk_fma_f32 v[66:67], v[66:67], v[82:83], v[80:81]\n\t"
    "v_pk_mul_f32 v[64:65], v[64:65], v[180:181]\n\t"
    "s_waitcnt lgkmcnt(4)\n\t"
    "v_smfmac_f32_16x16x64_bf16 v[200:203], v[120:123], v[84:91], v124\n\t"
    "ds_read_b64 v[84:85], v144 offset:64000\n\t"
    "ds_read_b64 v[86:87], v252 offset:64000\n\t"
    "ds_read_b64 v[88:89], v145 offset:64000\n\t"
    "ds_read_b64 v[90:91], v253 offset:64000\n\t"
    "v_lshrrev_b32 v254, 16, v146\n\t"
    "v_and_b32 v146, s92, v146\n\t"
    "v_lshrrev_b32 v255, 16, v147\n\t"
    "v_and_b32 v147, s92, v147\n\t"
    "v_pk_mul_f32 v[66:67], v[66:67], v[182:183]\n\t"
    "v_exp_f32 v68, v64\n\t"
    "v_exp_f32 v69, v65\n\t"
    "v_exp_f32 v70, v66\n\t"
    "v_exp_f32 v71, v67\n\t"
    "s_waitcnt lgkmcnt(4)\n\t"
    "v_smfmac_f32_16x16x64_bf16 v[200:203], v[120:123], v[92:99], v124\n\t"
    "ds_read_b64 v[92:93], v146 offset:64000\n\t"
    "ds_read_b64 v[94:95], v254 offset:64000\n\t"
    "ds_read_b64 v[96:97], v147 offset:64000\n\t"
    "ds_read_b64 v[98:99], v255 offset:64000\n\t"
    "buffer_load_dwordx4 v[220:223], v127, %12, s89 offen offset:1024 sc1\n\t"
    "v_lshrrev_b32 v252, 16, v148\n\t"
    "v_and_b32 v148, s92, v148\n\t"
    "v_lshrrev_b32 v253, 16, v149\n\t"
    "v_and_b32 v149, s92, v149\n\t"
    "v_pk_fma_f32 v[68:69], v[68:69], s[40:41], s[40:41]\n\t"
    "v_pk_fma_f32 v[70:71], v[70:71], s[42:43], s[42:43]\n\t"
    "v_rcp_f32 v68, v68\n\t"
    "v_rcp_f32 v69, v69\n\t"
    "v_rcp_f32 v70, v70\n\t"
    "s_waitcnt lgkmcnt(4)\n\t"
    "v_smfmac_f32_16x16x64_bf16 v[184:187], v[120:123], v[84:91], v124\n\t"
    "ds_read_b64 v[84:85], v148 offset:0\n\t"
    "ds_read_b64 v[86:87], v252 offset:0\n\t"
    "ds_read_b64 v[88:89], v149 offset:0\n\t"
    "ds_read_b64 v[90:91], v253 offset:0\n\t"
    "v_lshrrev_b32 v254, 16, v150\n\t"
    "v_and_b32 v150, s92, v150\n\t"
    "v_lshrrev_b32 v255, 16, v151\n\t"
    "v_and_b32 v151, s92, v151\n\t"
    "s_add_u32 s94, s94, 0x2000\n\t"
    "buffer_load_dwordx4 v[28:31], v127, %12, s94 offen sc1\n\t"
    "v_rcp_f32 v71, v71\n\t"
    "v_pk_mul_f32 v[68:69], v[68:69], v[180:181]\n\t"
    "v_pk_fma_f32 v[68:69], v[70:71], v[182:183], v[68:69]\n\t"
    "v_add_f32 v238, v68, v69\n\t"
    "s_waitcnt lgkmcnt(4)\n\t"
    "v_smfmac_f32_16x16x64_bf16 v[184:187], v[120:123], v[92:99], v124\n\t"
    "ds_read_b64 v[92:93], v150 offset:0\n\t"
    "ds_read_b64 v[94:95], v254 offset:0\n\t"
    "ds_read_b64 v[96:97], v151 offset:0\n\t"
    "ds_read_b64 v[98:99], v255 offset:0\n\t"
    "buffer_load_dwordx4 v[116:119], v127, %12, s87 offen offset:1024 sc1\n\t"
    "v_lshrrev_b32 v252, 16, v152\n\t"
    "v_and_b32 v152, s92, v152\n\t"
    "v_lshrrev_b32 v253, 16, v153\n\t"
    "v_mov_b64 v[204:205], s[70:71]\n\t"
    "v_mov_b64 v[206:207], s[72:73]\n\t"
    "v_and_b32 v153, s92, v153\n\t"
    "v_pk_mul_f32 v[72:73], v[184:185], v[184:185]\n\t"
    "v_pk_mul_f32 v[74:75], v[186:187], v[186:187]\n\t"
    "v_pk_fma_f32 v[72:73], v[72:73], v[82:83], v[80:81]\n\t"
    "v_pk_fma_f32 v[74:75], v[74:75], v[82:83], v[80:81]\n\t"
    "v_pk_mul_f32 v[72:73], v[72:73], v[184:185]\n\t"
    "s_waitcnt lgkmcnt(4)\n\t"
    "v_smfmac_f32_16x16x64_bf16 v[204:207], v[120:123], v[84:91], v124\n\t"
    "ds_read_b64 v[84:85], v152 offset:64000\n\t"
    "ds_read_b64 v[86:87], v252 offset:64000\n\t"
    "ds_read_b64 v[88:89], v153 offset:64000\n\t"
    "ds_read_b64 v[90:91], v253 offset:64000\n\t"
    "v_lshrrev_b32 v254, 16, v154\n\t"
    "v_and_b32 v154, s92, v154\n\t"
    "v_lshrrev_b32 v255, 16, v155\n\t"
    "v_and_b32 v155, s92, v155\n\t"
    "v_pk_mul_f32 v[74:75], v[74:75], v[186:187]\n\t"
    "v_exp_f32 v76, v72\n\t"
    "v_exp_f32 v77, v73\n\t"
    "v_exp_f32 v78, v74\n\t"
    "v_exp_f32 v79, v75\n\t"
    "s_waitcnt vmcnt(8) lgkmcnt(4)\n\t"
    "v_smfmac_f32_16x16x64_bf16 v[204:207], v[120:123], v[92:99], v124\n\t"
    "ds_read_b64 v[92:93], v154 offset:64000\n\t"
    "ds_read_b64 v[94:95], v254 offset:64000\n\t"
    "ds_read_b64 v[96:97], v155 offset:64000\n\t"
    "ds_read_b64 v[98:99], v255 offset:64000\n\t"
    "s_sub_u32 s90, s90, s95\n\t"
    "buffer_load_dwordx4 v[224:227], v127, %12, s90 offen offset:0 sc1\n\t"
    "v_lshrrev_b32 v252, 16, v244\n\t"
    "v_and_b32 v244, s92, v244\n\t"
    "v_lshrrev_b32 v253, 16, v245\n\t"
    "v_and_b32 v245, s92, v245\n\t"
    "v_pk_fma_f32 v[76:77], v[76:77], s[40:41], s[40:41]\n\t"
    "v_pk_fma_f32 v[78:79], v[78:79], s[42:43], s[42:43]\n\t"
    "v_rcp_f32 v76, v76\n\t"
    "v_rcp_f32 v77, v77\n\t"
    "v_rcp_f32 v78, v78\n\t"
    "s_waitcnt lgkmcnt(4)\n\t"
    "v_smfmac_f32_16x16x64_bf16 v[188:191], v[120:123], v[84:91], v124\n\t"
    "ds_read_b64 v[84:85], v244 offset:0\n\t"
    "ds_read_b64 v[86:87], v252 offset:0\n\t"
    "ds_read_b64 v[88:89], v245 offset:0\n\t"
    "ds_read_b64 v[90:91], v253 offset:0\n\t"
    "v_lshrrev_b32 v254, 16, v246\n\t"
    "v_and_b32 v246, s92, v246\n\t"
    "v_lshrrev_b32 v255, 16, v247\n\t"
    "v_and_b32 v247, s92, v247\n\t"
    "s_waitcnt vmcnt(6)\n\t"
    "v_smfmac_f32_16x16x64_bf16 v[188:191], v[156:159], v[16:23], v124\n\t"
    "v_rcp_f32 v79, v79\n\t"
    "v_pk_mul_f32 v[76:77], v[76:77], v[184:185]\n\t"
    "v_pk_fma_f32 v[76:77], v[78:79], v[186:187], v[76:77]\n\t"
    "v_add_f32 v239, v76, v77\n\t"
    "v_lshrrev_b32 v147, 4, v127\n\t"
    "v_add_u32 v148, 140288, v127\n\t"
    "s_lshl_b32 s65, %14, 12\n\t"
    "s_add_u32 s65, s65, 16777216\n\t"
    "buffer_load_dwordx4 v[0:3], v127, %12, s65 offen offset:0 sc1\n\t"
    "s_waitcnt lgkmcnt(4)\n\t"
    "v_smfmac_f32_16x16x64_bf16 v[188:191], v[120:123], v[92:99], v124\n\t"
    "ds_read_b64 v[92:93], v246 offset:0\n\t"
    "ds_read_b64 v[94:95], v254 offset:0\n\t"
    "ds_read_b64 v[96:97], v247 offset:0\n\t"
    "ds_read_b64 v[98:99], v255 offset:0\n\t"
    "s_add_u32 s88, s88, 0x8000\n\t"
    "buffer_load_dwordx4 v[228:231], v127, %12, s88 offen offset:0 sc1\n\t"
    "v_lshrrev_b32 v252, 16, v248\n\t"
    "v_and_b32 v248, s92, v248\n\t"
    "v_lshrrev_b32 v253, 16, v249\n\t"
    "v_mov_b64 v[208:209], s[70:71]\n\t"
    "v_mov_b64 v[210:211], s[72:73]\n\t"
    "v_and_b32 v249, s92, v249\n\t"
    "v_smfmac_f32_16x16x64_bf16 v[192:195], v[160:163], v[16:23], v124\n\t"
    "v_pk_mul_f32 v[64:65], v[188:189], v[188:189]\n\t"
    "v_pk_mul_f32 v[66:67], v[190:191], v[190:191]\n\t"
    "v_pk_fma_f32 v[64:65], v[64:65], v[82:83], v[80:81]\n\t"
    "v_pk_fma_f32 v[66:67], v[66:67], v[82:83], v[80:81]\n\t"
    "v_pk_mul_f32 v[64:65], v[64:65], v[188:189]\n\t"
    "s_lshl_b32 s65, %14, 12\n\t"
    "s_add_u32 s65, s65, 16777216\n\t"
    "buffer_load_dwordx4 v[4:7], v127, %12, s65 offen offset:1024 sc1\n\t"
    "s_waitcnt lgkmcnt(4)\n\t"
    "v_smfmac_f32_16x16x64_bf16 v[208:211], v[120:123], v[84:91], v124\n\t"
    "ds_read_b64 v[84:85], v248 offset:64000\n\t"
    "ds_read_b64 v[86:87], v252 offset:64000\n\t"
    "ds_read_b64 v[88:89], v249 offset:64000\n\t"
    "ds_read_b64 v[90:91], v253 offset:64000\n\t"
    "v_lshrrev_b32 v254, 16, v250\n\t"
    "v_and_b32 v250, s92, v250\n\t"
    "v_lshrrev_b32 v255, 16, v251\n\t"
    "v_and_b32 v251, s92, v251\n\t"
    "s_mov_b64 exec, 1\n\t"
    "buffer_load_dword v164, off, %12, s93 offset:256 sc1\n\t"
    "s_mov_b64 exec, -1\n\t"
    "v_pk_mul_f32 v[66:67], v[66:67], v[190:191]\n\t"
    "v_exp_f32 v68, v64\n\t"
    "v_exp_f32 v69, v65\n\t"
    "v_exp_f32 v70, v66\n\t"
    "v_exp_f32 v71, v67\n\t"
    "s_lshl_b32 s65, %14, 12\n\t"
    "s_add_u32 s65, s65, 16777216\n\t"
    "buffer_load_dwordx4 v[8:11], v127, %12, s65 offen offset:2048 sc1\n\t"
    "s_waitcnt lgkmcnt(4)\n\t"
    "v_smfmac_f32_16x16x64_bf16 v[208:211], v[120:123], v[92:99], v124\n\t"
    "ds_read_b64 v[92:93], v250 offset:64000\n\t"
    "ds_read_b64 v[94:95], v254 offset:64000\n\t"
    "ds_read_b64 v[96:97], v251 offset:64000\n\t"
    "ds_read_b64 v[98:99], v255 offset:64000\n\t"
    "buffer_load_dwordx4 v[232:235], v127, %12, s90 offen offset:1024 sc1\n\t"
    "v_lshrrev_b32 v252, 16, v212\n\t"
    "v_and_b32 v212, s92, v212\n\t"
    "v_lshrrev_b32 v253, 16, v213\n\t"
    "v_and_b32 v213, s92, v213\n\t"
    "v_pk_fma_f32 v[68:69], v[68:69], s[40:41], s[40:41]\n\t"
    "v_pk_fma_f32 v[70:71], v[70:71], s[42:43], s[42:43]\n\t"
    "v_rcp_f32 v68, v68\n\t"
    "v_rcp_f32 v69, v69\n\t"
    "v_rcp_f32 v70, v70\n\t"
    "s_lshl_b32 s65, %14, 12\n\t"
    "s_add_u32 s65, s65, 16777216\n\t"
    "buffer_load_dwordx4 v[12:15], v127, %12, s65 offen offset:3072 sc1\n\t"
    "s_waitcnt lgkmcnt(4)\n\t"
    "v_smfmac_f32_16x16x64_bf16 v[192:195], v[120:123], v[84:91], v124\n\t"
    "ds_read_b64 v[84:85], v212 offset:0\n\t"
    "ds_read_b64 v[86:87], v252 offset:0\n\t"
    "ds_read_b64 v[88:89], v213 offset:0\n\t"
    "ds_read_b64 v[90:91], v253 offset:0\n\t"
    "v_lshrrev_b32 v254, 16, v214\n\t"
    "v_and_b32 v214, s92, v214\n\t"
    "v_lshrrev_b32 v255, 16, v215\n\t"
    "v_and_b32 v215, s92, v215\n\t"
    "s_waitcnt vmcnt(9)\n\t"
    "v_smfmac_f32_16x16x64_bf16 v[196:199], v[156:159], v[24:31], v124\n\t"
    "v_rcp_f32 v71, v71\n\t"
    "v_pk_mul_f32 v[68:69], v[68:69], v[188:189]\n\t"
    "v_pk_fma_f32 v[68:69], v[70:71], v[190:191], v[68:69]\n\t"
    "v_add_f32 v240, v68, v69\n\t"
    "ds_read_b128 v[16:19], v148 offset:4096\n\t"
    "s_cmp_lt_u32 %16, 4\n\t"
    "s_cbranch_scc1 L_tp239_%=\n\t"
    "s_setprio 1\n\t"
    "L_tp239_%=:\n\t"
    "s_waitcnt lgkmcnt(5)\n\t"
    "v_smfmac_f32_16x16x64_bf16 v[192:195], v[120:123], v[92:99], v124\n\t"
    "ds_read_b64 v[92:93], v214 offset:0\n\t"
    "ds_read_b64 v[94:95], v254 offset:0\n\t"
    "ds_read_b64 v[96:97], v215 offset:0\n\t"
    "ds_read_b64 v[98:99], v255 offset:0\n\t"
    "buffer_load_dwordx4 v[168:171], v127, %12, s88 offen offset:1024 sc1\n\t"
    "v_lshrrev_b32 v252, 16, v216\n\t"
    "v_and_b32 v216, s92, v216\n\t"
    "v_lshrrev_b32 v253, 16, v217\n\t"
    "v_mov_b64 v[172:173], s[70:71]\n\t"
    "v_mov_b64 v[174:175], s[72:73]\n\t"
    "v_and_b32 v217, s92, v217\n\t"
    "v_smfmac_f32_16x16x64_bf16 v[200:203], v[160:163], v[24:31], v124\n\t"
    "v_pk_mul_f32 v[72:73], v[192:193], v[192:193]\n\t"
    "v_pk_mul_f32 v[74:75], v[194:195], v[194:195]\n\t"
    "v_pk_fma_f32 v[72:73], v[72:73], v[82:83], v[80:81]\n\t"
    "v_pk_fma_f32 v[74:75], v[74:75], v[82:83], v[80:81]\n\t"
    "v_pk_mul_f32 v[72:73], v[72:73], v[192:193]\n\t"
    "ds_read_b128 v[20:23], v148 offset:5120\n\t"
    "s_waitcnt lgkmcnt(6)\n\t"
    "v_smfmac_f32_16x16x64_bf16 v[172:175], v[120:123], v[84:91], v124\n\t"
    "ds_read_b64 v[84:85], v216 offset:64000\n\t"
    "ds_read_b64 v[86:87], v252 offset:64000\n\t"
    "ds_read_b64 v[88:89], v217 offset:64000\n\t"
    "ds_read_b64 v[90:91], v253 offset:64000\n\t"
    "v_lshrrev_b32 v254, 16, v218\n\t"
    "v_and_b32 v218, s92, v218\n\t"
    "v_lshrrev_b32 v255, 16, v219\n\t"
    "v_and_b32 v219, s92, v219\n\t"
    "v_pk_mul_f32 v[74:75], v[74:75], v[194:195]\n\t"
    "v_exp_f32 v76, v72\n\t"
    "v_exp_f32 v77, v73\n\t"
    "v_exp_f32 v78, v74\n\t"
    "v_exp_f32 v79, v75\n\t"
    "ds_read_b128 v[24:27], v148 offset:6144\n\t"
    "s_waitcnt lgkmcnt(6)\n\t"
    "v_smfmac_f32_16x16x64_bf16 v[172:175], v[120:123], v[92:99], v124\n\t"
    "ds_read_b64 v[92:93], v218 offset:64000\n\t"
    "ds_read_b64 v[94:95], v254 offset:64000\n\t"
    "ds_read_b64 v[96:97], v219 offset:64000\n\t"
    "ds_read_b64 v[98:99], v255 offset:64000\n\t"
    "s_sub_u32 s91, s91, s95\n\t"
    "buffer_load_dwordx4 v[112:115], v127, %12, s91 offen offset:0 sc1\n\t"
    "v_lshrrev_b32 v252, 16, v220\n\t"
    "v_and_b32 v220, s92, v220\n\t"
    "v_lshrrev_b32 v253, 16, v221\n\t"
    "v_and_b32 v221, s92, v221\n\t"
    "v_pk_fma_f32 v[76:77], v[76:77], s[40:41], s[40:41]\n\t"
    "v_pk_fma_f32 v[78:79], v[78:79], s[42:43], s[42:43]\n\t"
    "v_rcp_f32 v76, v76\n\t"
    "v_rcp_f32 v77, v77\n\t"
    "v_rcp_f32 v78, v78\n\t"
    "ds_read_b128 v[28:31], v148 offset:7168\n\t"
    "s_waitcnt lgkmcnt(6)\n\t"
    "v_smfmac_f32_16x16x64_bf16 v[196:199], v[120:123], v[84:91], v124\n\t"
    "ds_read_b64 v[84:85], v220 offset:0\n\t"
    "ds_read_b64 v[86:87], v252 offset:0\n\t"
    "ds_read_b64 v[88:89], v221 offset:0\n\t"
    "ds_read_b64 v[90:91], v253 offset:0\n\t"
    "v_lshrrev_b32 v254, 16, v222\n\t"
    "v_and_b32 v222, s92, v222\n\t"
    "v_lshrrev_b32 v255, 16, v223\n\t"
    "v_and_b32 v223, s92, v223\n\t"
    "s_waitcnt vmcnt(5)\n\t"
    "v_readfirstlane_b32 s65, v164\n\t"
    "s_nop 0\n\t"
    "s_cmp_eq_u32 s65, 0\n\t"
    "s_cbranch_scc1 L_xw2_%=\n\t"
    "L_xwb2_%=:\n\t"
    "s_add_u32 s94, s94, 0x2000\n\t"
    "buffer_load_dwordx4 v[36:39], v127, %12, s94 offen sc1\n\t"
    "v_rcp_f32 v79, v79\n\t"
    "v_pk_mul_f32 v[76:77], v[76:77], v[192:193]\n\t"
    "v_pk_fma_f32 v[76:77], v[78:79], v[194:195], v[76:77]\n\t"
    "v_add_f32 v241, v76, v77\n\t"
    "s_waitcnt lgkmcnt(5)\n\t"
    "v_smfmac_f32_16x16x64_bf16 v[196:199], v[120:123], v[92:99], v124\n\t"
    "ds_read_b64 v[92:93], v222 offset:0\n\t"
    "ds_read_b64 v[94:95], v254 offset:0\n\t"
    "ds_read_b64 v[96:97], v223 offset:0\n\t"
    "ds_read_b64 v[98:99], v255 offset:0\n\t"
    "s_add_u32 s89, s89, 0x8000\n\t"
    "buffer_load_dwordx4 v[100:103], v127, %12, s89 offen offset:0 sc1\n\t"
    "v_lshrrev_b32 v252, 16, v116\n\t"
    "v_and_b32 v116, s92, v116\n\t"
    "v_lshrrev_b32 v253, 16, v117\n\t"
    "v_mov_b64 v[176:177], s[70:71]\n\t"
    "v_mov_b64 v[178:179], s[72:73]\n\t"
    "v_and_b32 v117, s92, v117\n\t"
    "v_pk_mul_f32 v[64:65], v[196:197], v[196:197]\n\t"
    "v_pk_mul_f32 v[66:67], v[198:199], v[198:199]\n\t"
    "v_pk_fma_f32 v[64:65], v[64:65], v[82:83], v[80:81]\n\t"
    "v_pk_fma_f32 v[66:67], v[66:67], v[82:83], v[80:81]\n\t"
    "v_pk_mul_f32 v[64:65], v[64:65], v[196:197]\n\t"
    "s_waitcnt lgkmcnt(4)\n\t"
    "v_smfmac_f32_16x16x64_bf16 v[176:179], v[120:123], v[84:91], v124\n\t"
    "ds_read_b64 v[84:85], v116 offset:64000\n\t"
    "ds_read_b64 v[86:87], v252 offset:64000\n\t"
    "ds_read_b64 v[88:89], v117 offset:64000\n\t"
    "ds_read_b64 v[90:91], v253 offset:64000\n\t"
    "v_lshrrev_b32 v254, 16, v118\n\t"
    "v_and_b32 v118, s92, v118\n\t"
    "v_lshrrev_b32 v255, 16, v119\n\t"
    "v_and_b32 v119, s92, v119\n\t"
    "v_pk_mul_f32 v[66:67], v[66:67], v[198:199]\n\t"
    "v_exp_f32 v68, v64\n\t"
    "v_exp_f32 v69, v65\n\t"
    "v_exp_f32 v70, v66\n\t"
    "v_exp_f32 v71, v67\n\t"
    "s_waitcnt lgkmcnt(4)\n\t"
    "v_smfmac_f32_16x16x64_bf16 v[176:179], v[120:123], v[92:99], v124\n\t"
    "ds_read_b64 v[92:93], v118 offset:64000\n\t"
    "ds_read_b64 v[94:95], v254 offset:64000\n\t"
    "ds_read_b64 v[96:97], v119 offset:64000\n\t"
    "ds_read_b64 v[98:99], v255 offset:64000\n\t"
    "buffer_load_dwordx4 v[104:107], v127, %12, s91 offen offset:1024 sc1\n\t"
    "v_lshrrev_b32 v252, 16, v224\n\t"
    "v_and_b32 v224, s92, v224\n\t"
    "v_lshrrev_b32 v253, 16, v225\n\t"
    "v_and_b32 v225, s92, v225\n\t"
    "v_pk_fma_f32 v[68:69], v[68:69], s[40:41], s[40:41]\n\t"
    "v_pk_fma_f32 v[70:71], v[70:71], s[42:43], s[42:43]\n\t"
    "v_rcp_f32 v68, v68\n\t"
    "v_rcp_f32 v69, v69\n\t"
    "v_rcp_f32 v70, v70\n\t"
    "s_waitcnt lgkmcnt(4)\n\t"
    "v_smfmac_f32_16x16x64_bf16 v[200:203], v[120:123], v[84:91], v124\n\t"
    "ds_read_b64 v[84:85], v224 offset:0\n\t"
    "ds_read_b64 v[86:87], v252 offset:0\n\t"
    "ds_read_b64 v[88:89], v225 offset:0\n\t"
    "ds_read_b64 v[90:91], v253 offset:0\n\t"
    "v_lshrrev_b32 v254, 16, v226\n\t"
    "v_and_b32 v226, s92, v226\n\t"
    "v_lshrrev_b32 v255, 16, v227\n\t"
    "v_and_b32 v227, s92, v227\n\t"
    "s_add_u32 s94, s94, 0x2000\n\t"
    "buffer_load_dwordx4 v[44:47], v127, %12, s94 offen sc1\n\t"
    "v_rcp_f32 v71, v71\n\t"
    "v_pk_mul_f32 v[68:69], v[68:69], v[196:197]\n\t"
    "v_pk_fma_f32 v[68:69], v[70:71], v[198:199], v[68:69]\n\t"
    "v_add_f32 v242, v68, v69\n\t"
    "s_lshl_b32 s46, %14, 2\n\t"
    "s_add_u32 s46, s46, 48245760\n\t"
    "s_mov_b64 exec, 1\n\t"
    "buffer_load_dword v144, off, %12, s46 sc1\n\t"
    "s_mov_b64 exec, -1\n\t"
    "s_waitcnt lgkmcnt(4)\n\t"
    "v_smfmac_f32_16x16x64_bf16 v[200:203], v[120:123], v[92:99], v124\n\t"
    "ds_read_b64 v[92:93], v226 offset:0\n\t"
    "ds_read_b64 v[94:95], v254 offset:0\n\t"
    "ds_read_b64 v[96:97], v227 offset:0\n\t"
    "ds_read_b64 v[98:99], v255 offset:0\n\t"
    "buffer_load_dwordx4 v[108:111], v127, %12, s89 offen offset:1024 sc1\n\t"
    "v_lshrrev_b32 v252, 16, v228\n\t"
    "v_and_b32 v228, s92, v228\n\t"
    "v_lshrrev_b32 v253, 16, v229\n\t"
    "v_mov_b64 v[180:181], s[70:71]\n\t"
    "v_mov_b64 v[182:183], s[72:73]\n\t"
    "v_and_b32 v229, s92, v229\n\t"
    "v_pk_mul_f32 v[72:73], v[200:201], v[200:201]\n\t"
    "v_pk_mul_f32 v[74:75], v[202:203], v[202:203]\n\t"
    "v_pk_fma_f32 v[72:73], v[72:73], v[82:83], v[80:81]\n\t"
    "v_pk_fma_f32 v[74:75], v[74:75], v[82:83], v[80:81]\n\t"
    "v_pk_mul_f32 v[72:73], v[72:73], v[200:201]\n\t"
    "s_waitcnt lgkmcnt(4)\n\t"
    "v_smfmac_f32_16x16x64_bf16 v[180:183], v[120:123], v[84:91], v124\n\t"
    "ds_read_b64 v[84:85], v228 offset:64000\n\t"
    "ds_read_b64 v[86:87], v252 offset:64000\n\t"
    "ds_read_b64 v[88:89], v229 offset:64000\n\t"
    "ds_read_b64 v[90:91], v253 offset:64000\n\t"
    "v_lshrrev_b32 v254, 16, v230\n\t"
    "v_and_b32 v230, s92, v230\n\t"
    "v_lshrrev_b32 v255, 16, v231\n\t"
    "v_and_b32 v231, s92, v231\n\t"
    "v_pk_mul_f32 v[74:75], v[74:75], v[202:203]\n\t"
    "v_exp_f32 v76, v72\n\t"
    "v_exp_f32 v77, v73\n\t"
    "v_exp_f32 v78, v74\n\t"
    "v_exp_f32 v79, v75\n\t"
    "s_waitcnt vmcnt(9) lgkmcnt(4)\n\t"
    "v_smfmac_f32_16x16x64_bf16 v[180:183], v[120:123], v[92:99], v124\n\t"
    "ds_read_b64 v[92:93], v230 offset:64000\n\t"
    "ds_read_b64 v[94:95], v254 offset:64000\n\t"
    "ds_read_b64 v[96:97], v231 offset:64000\n\t"
    "ds_read_b64 v[98:99], v255 offset:64000\n\t"
    "s_add_u32 s90, s90, 0x8000\n\t"
    "buffer_load_dwordx4 v[248:251], v127, %12, s90 offen offset:0 sc1\n\t"
    "v_lshrrev_b32 v252, 16, v232\n\t"
    "v_and_b32 v232, s92, v232\n\t"
    "v_lshrrev_b32 v253, 16, v233\n\t"
    "v_and_b32 v233, s92, v233\n\t"
    "v_pk_fma_f32 v[76:77], v[76:77], s[40:41], s[40:41]\n\t"
    "v_pk_fma_f32 v[78:79], v[78:79], s[42:43], s[42:43]\n\t"
    "v_rcp_f32 v76, v76\n\t"
    "v_rcp_f32 v77, v77\n\t"
    "v_rcp_f32 v78, v78\n\t"
    "s_waitcnt lgkmcnt(4)\n\t"
    "v_smfmac_f32_16x16x64_bf16 v[204:207], v[120:123], v[84:91], v124\n\t"
    "ds_read_b64 v[84:85], v232 offset:0\n\t"
    "ds_read_b64 v[86:87], v252 offset:0\n\t"
    "ds_read_b64 v[88:89], v233 offset:0\n\t"
    "ds_read_b64 v[90:91], v253 offset:0\n\t"
    "v_lshrrev_b32 v254, 16, v234\n\t"
    "v_and_b32 v234, s92, v234\n\t"
    "v_lshrrev_b32 v255, 16, v235\n\t"
    "v_and_b32 v235, s92, v235\n\t"
    "s_mov_b64 exec, 1\n\t"
    "buffer_load_dword v164, off, %12, s93 offset:384 sc1\n\t"
    "s_mov_b64 exec, -1\n\t"
    "s_waitcnt vmcnt(7)\n\t"
    "v_smfmac_f32_16x16x64_bf16 v[204:207], v[156:159], v[32:39], v124\n\t"
    "v_rcp_f32 v79, v79\n\t"
    "v_pk_mul_f32 v[76:77], v[76:77], v[200:201]\n\t"
    "v_pk_fma_f32 v[76:77], v[78:79], v[202:203], v[76:77]\n\t"
    "v_add_f32 v243, v76, v77\n\t"
    "s_waitcnt lgkmcnt(4)\n\t"
    "v_smfmac_f32_16x16x64_bf16 v[204:207], v[120:123], v[92:99], v124\n\t"
    "ds_read_b64 v[92:93], v234 offset:0\n\t"
    "ds_read_b64 v[94:95], v254 offset:0\n\t"
    "ds_read_b64 v[96:97], v235 offset:0\n\t"
    "ds_read_b64 v[98:99], v255 offset:0\n\t"
    "buffer_load_dwordx4 v[212:215], v127, %12, s90 offen offset:1024 sc1\n\t"
    "v_lshrrev_b32 v252, 16, v168\n\t"
    "v_and_b32 v168, s92, v168\n\t"
    "v_lshrrev_b32 v253, 16, v169\n\t"
    "v_mov_b64 v[184:185], s[70:71]\n\t"
    "v_mov_b64 v[186:187], s[72:73]\n\t"
    "v_and_b32 v169, s92, v169\n\t"
    "v_smfmac_f32_16x16x64_bf16 v[208:211], v[160:163], v[32:39], v124\n\t"
    "v_pk_mul_f32 v[64:65], v[204:205], v[204:205]\n\t"
    "v_pk_mul_f32 v[66:67], v[206:207], v[206:207]\n\t"
    "v_pk_fma_f32 v[64:65], v[64:65], v[82:83], v[80:81]\n\t"
    "v_pk_fma_f32 v[66:67], v[66:67], v[82:83], v[80:81]\n\t"
    "v_pk_mul_f32 v[64:65], v[64:65], v[204:205]\n\t"
    "ds_read_b128 v[32:35], v148 offset:8192\n\t"
    "s_waitcnt lgkmcnt(5)\n\t"
    "v_smfmac_f32_16x16x64_bf16 v[184:187], v[120:123], v[84:91], v124\n\t"
    "ds_read_b64 v[84:85], v168 offset:64000\n\t"
    "ds_read_b64 v[86:87], v252 offset:64000\n\t"
    "ds_read_b64 v[88:89], v169 offset:64000\n\t"
    "ds_read_b64 v[90:91], v253 offset:64000\n\t"
    "v_lshrrev_b32 v254, 16, v170\n\t"
    "v_and_b32 v170, s92, v170\n\t"
    "v_lshrrev_b32 v255, 16, v171\n\t"
    "v_and_b32 v171, s92, v171\n\t"
    "v_pk_mul_f32 v[66:67], v[66:67], v[206:207]\n\t"
    "v_exp_f32 v68, v64\n\t"
    "v_exp_f32 v69, v65\n\t"
    "v_exp_f32 v70, v66\n\t"
    "v_exp_f32 v71, v67\n\t"
    "ds_read_b128 v[36:39], v148 offset:9216\n\t"
    "s_waitcnt lgkmcnt(6)\n\t"
    "v_smfmac_f32_16x16x64_bf16 v[184:187], v[120:123], v[92:99], v124\n\t"
    "ds_read_b64 v[92:93], v170 offset:64000\n\t"
    "ds_read_b64 v[94:95], v254 offset:64000\n\t"
    "ds_read_b64 v[96:97], v171 offset:64000\n\t"
    "ds_read_b64 v[98:99], v255 offset:64000\n\t"
    "s_add_u32 s91, s91, 0x8000\n\t"
    "buffer_load_dwordx4 v[216:219], v127, %12, s91 offen offset:0 sc1\n\t"
    "v_lshrrev_b32 v252, 16, v112\n\t"
    "v_and_b32 v112, s92, v112\n\t"
    "v_lshrrev_b32 v253, 16, v113\n\t"
    "v_and_b32 v113, s92, v113\n\t"
    "v_pk_fma_f32 v[68:69], v[68:69], s[40:41], s[40:41]\n\t"
    "v_pk_fma_f32 v[70:71], v[70:71], s[42:43], s[42:43]\n\t"
    "v_rcp_f32 v68, v68\n\t"
    "v_rcp_f32 v69, v69\n\t"
    "v_rcp_f32 v70, v70\n\t"
    "s_waitcnt lgkmcnt(5)\n\t"
    "v_smfmac_f32_16x16x64_bf16 v[208:211], v[120:123], v[84:91], v124\n\t"
    "ds_read_b64 v[84:85], v112 offset:0\n\t"
    "ds_read_b64 v[86:87], v252 offset:0\n\t"
    "ds_read_b64 v[88:89], v113 offset:0\n\t"
    "ds_read_b64 v[90:91], v253 offset:0\n\t"
    "v_lshrrev_b32 v254, 16, v114\n\t"
    "v_and_b32 v114, s92, v114\n\t"
    "v_lshrrev_b32 v255, 16, v115\n\t"
    "v_and_b32 v115, s92, v115\n\t"
    "s_waitcnt vmcnt(6)\n\t"
    "v_smfmac_f32_16x16x64_bf16 v[172:175], v[156:159], v[40:47], v124\n\t"
    "v_rcp_f32 v71, v71\n\t"
    "v_pk_mul_f32 v[68:69], v[68:69], v[204:205]\n\t"
    "v_pk_fma_f32 v[68:69], v[70:71], v[206:207], v[68:69]\n\t"
    "v_add_f32 v244, v68, v69\n\t"
    "s_waitcnt vmcnt(5)\n\t"
    "v_readfirstlane_b32 s38, v144\n\t"
    "s_nop 0\n\t"
    "s_cmp_eq_u32 s38, 0\n\t"
    "s_cbranch_scc1 L_rw_%=\n\t"
    "L_rok_%=:\n\t"
    "s_and_b32 s46, %16, 3\n\t"
    "s_mul_i32 s49, s46, 1040\n\t"
    "s_add_u32 s49, s49, 2064\n\t"
    "s_and_b32 s37, s38, 0xffff\n\t"
    "s_lshr_b32 s44, s38, 16\n\t"
    "s_sub_u32 s44, s44, s37\n\t"
    "s_lshl_b32 s37, s37, 2\n\t"
    "s_lshl_b32 s38, s44, 2\n\t"
    "s_mul_i32 s51, s46, s44\n\t"
    "s_add_u32 s48, s51, s44\n\t"
    "s_lshl_b32 s48, s48, 16\n\t"
    "s_add_u32 s48, s48, s51\n\t"
    "s_lshr_b32 s47, %16, 2\n\t"
    "s_add_u32 s48, s48, s47\n\t"
    "s_add_u32 s48, s48, 0x200\n\t"
    "s_mul_i32 s46, s37, 400\n\t"
    "s_add_u32 s96, %18, s46\n\t"
    "s_addc_u32 s97, %19, 0\n\t"
    "s_sub_u32 s98, 6553600, s46\n\t"
    "s_mov_b32 s99, 0x20000\n\t"
    "v_bfe_u32 v154, v147, 2, 2\n\t"
    "v_and_b32 v155, 3, v147\n\t"
    "v_mul_u32_u24 v154, 25, v154\n\t"
    "v_mad_u32_u24 v154, v155, 6, v154\n\t"
    "v_lshlrev_b32 v153, 2, v154\n\t"
    "s_and_b32 s53, s48, 0xff\n\t"
    "s_bfe_u32 s55, s48, 0x80008\n\t"
    "s_add_u32 s54, s53, s55\n\t"
    "s_add_u32 s55, s54, s55\n\t"
    "s_lshr_b32 s64, s48, 16\n\t"
    "s_mul_i32 s66, s53, 400\n\t"
    "buffer_load_dwordx3 v[128:130], v153, s[96:99], s66 offen\n\t"
    "buffer_load_dwordx4 v[132:135], v153, s[96:99], s66 offen offset:12\n\t"
    "s_mul_i32 s66, s54, 400\n\t"
    "buffer_load_dwordx3 v[136:138], v153, s[96:99], s66 offen\n\t"
    "buffer_load_dwordx4 v[140:143], v153, s[96:99], s66 offen offset:12\n\t"
    "v_cmp_eq_u32 s[62:63], 0, v155\n\t"
    "v_and_b32 v151, 48, v147\n\t"
    "v_or_b32 v155, 0x100000, v151\n\t"
    "v_cndmask_b32 v152, v155, v151, s[62:63]\n\t"
    "s_waitcnt lgkmcnt(4)\n\t"
    "v_smfmac_f32_16x16x64_bf16 v[208:211], v[120:123], v[92:99], v124\n\t"
    "ds_read_b64 v[92:93], v114 offset:0\n\t"
    "ds_read_b64 v[94:95], v254 offset:0\n\t"
    "ds_read_b64 v[96:97], v115 offset:0\n\t"
    "ds_read_b64 v[98:99], v255 offset:0\n\t"
    "buffer_load_dwordx4 v[220:223], v127, %12, s91 offen offset:1024 sc1\n\t"
    "v_lshrrev_b32 v252, 16, v100\n\t"
    "v_and_b32 v100, s92, v100\n\t"
    "v_lshrrev_b32 v253, 16, v101\n\t"
    "v_mov_b64 v[188:189], s[70:71]\n\t"
    "v_mov_b64 v[190:191], s[72:73]\n\t"
    "v_and_b32 v101, s92, v101\n\t"
    "v_smfmac_f32_16x16x64_bf16 v[176:179], v[160:163], v[40:47], v124\n\t"
    "v_pk_mul_f32 v[72:73], v[208:209], v[208:209]\n\t"
    "v_pk_mul_f32 v[74:75], v[210:211], v[210:211]\n\t"
    "v_pk_fma_f32 v[72:73], v[72:73], v[82:83], v[80:81]\n\t"
    "v_pk_fma_f32 v[74:75], v[74:75], v[82:83], v[80:81]\n\t"
    "v_pk_mul_f32 v[72:73], v[72:73], v[208:209]\n\t"
    "ds_read_b128 v[40:43], v148 offset:10240\n\t"
    "s_setprio 0\n\t"
    "s_waitcnt lgkmcnt(5)\n\t"
    "v_smfmac_f32_16x16x64_bf16 v[188:191], v[120:123], v[84:91], v124\n\t"
    "ds_read_b64 v[84:85], v100 offset:64000\n\t"
    "ds_read_b64 v[86:87], v252 offset:64000\n\t"
    "ds_read_b64 v[88:89], v101 offset:64000\n\t"
    "ds_read_b64 v[90:91], v253 offset:64000\n\t"
    "v_lshrrev_b32 v254, 16, v102\n\t"
    "v_and_b32 v102, s92, v102\n\t"
    "v_lshrrev_b32 v255, 16, v103\n\t"
    "v_and_b32 v103, s92, v103\n\t"
    "s_waitcnt vmcnt(7)\n\t"
    "v_readfirstlane_b32 s65, v164\n\t"
    "s_nop 0\n\t"
    "s_cmp_eq_u32 s65, 0\n\t"
    "s_cbranch_scc1 L_xw3_%=\n\t"
    "L_xwb3_%=:\n\t"
    "s_add_u32 s94, s94, 0x2000\n\t"
    "buffer_load_dwordx4 v[52:55], v127, %12, s94 offen sc1\n\t"
    "v_pk_mul_f32 v[74:75], v[74:75], v[210:211]\n\t"
    "v_exp_f32 v76, v72\n\t"
    "v_exp_f32 v77, v73\n\t"
    "v_exp_f32 v78, v74\n\t"
    "v_exp_f32 v79, v75\n\t"
    "ds_read_b128 v[44:47], v148 offset:11264\n\t"
    "s_waitcnt lgkmcnt(6)\n\t"
    "v_smfmac_f32_16x16x64_bf16 v[188:191], v[120:123], v[92:99], v124\n\t"
    "ds_read_b64 v[92:93], v102 offset:64000\n\t"
    "ds_read_b64 v[94:95], v254 offset:64000\n\t"
    "ds_read_b64 v[96:97], v103 offset:64000\n\t"
    "ds_read_b64 v[98:99], v255 offset:64000\n\t"
    "v_lshrrev_b32 v252, 16, v104\n\t"
    "v_and_b32 v104, s92, v104\n\t"
    "v_lshrrev_b32 v253, 16, v105\n\t"
    "v_and_b32 v105, s92, v105\n\t"
    "v_pk_fma_f32 v[76:77], v[76:77], s[40:41], s[40:41]\n\t"
    "v_pk_fma_f32 v[78:79], v[78:79], s[42:43], s[42:43]\n\t"
    "v_rcp_f32 v76, v76\n\t"
    "v_rcp_f32 v77, v77\n\t"
    "v_rcp_f32 v78, v78\n\t"
    "s_waitcnt lgkmcnt(5)\n\t"
    "v_smfmac_f32_16x16x64_bf16 v[172:175], v[120:123], v[84:91], v124\n\t"
    "ds_read_b64 v[84:85], v104 offset:0\n\t"
    "ds_read_b64 v[86:87], v252 offset:0\n\t"
    "ds_read_b64 v[88:89], v105 offset:0\n\t"
    "ds_read_b64 v[90:91], v253 offset:0\n\t"
    "v_lshrrev_b32 v254, 16, v106\n\t"
    "v_and_b32 v106, s92, v106\n\t"
    "v_lshrrev_b32 v255, 16, v107\n\t"
    "v_and_b32 v107, s92, v107\n\t"
    "v_rcp_f32 v79, v79\n\t"
    "v_pk_mul_f32 v[76:77], v[76:77], v[208:209]\n\t"
    "v_pk_fma_f32 v[76:77], v[78:79], v[210:211], v[76:77]\n\t"
    "v_add_f32 v245, v76, v77\n\t"
    "s_waitcnt lgkmcnt(4)\n\t"
    "v_smfmac_f32_16x16x64_bf16 v[172:175], v[120:123], v[92:99], v124\n\t"
    "ds_read_b64 v[92:93], v106 offset:0\n\t"
    "ds_read_b64 v[94:95], v254 offset:0\n\t"
    "ds_read_b64 v[96:97], v107 offset:0\n\t"
    "ds_read_b64 v[98:99], v255 offset:0\n\t"
    "v_lshrrev_b32 v252, 16, v108\n\t"
    "v_and_b32 v108, s92, v108\n\t"
    "v_lshrrev_b32 v253, 16, v109\n\t"
    "v_mov_b64 v[192:193], s[70:71]\n\t"
    "v_mov_b64 v[194:195], s[72:73]\n\t"
    "v_and_b32 v109, s92, v109\n\t"
    "v_pk_mul_f32 v[64:65], v[172:173], v[172:173]\n\t"
    "v_pk_mul_f32 v[66:67], v[174:175], v[174:175]\n\t"
    "v_pk_fma_f32 v[64:65], v[64:65], v[82:83], v[80:81]\n\t"
    "v_pk_fma_f32 v[66:67], v[66:67], v[82:83], v[80:81]\n\t"
    "v_pk_mul_f32 v[64:65], v[64:65], v[172:173]\n\t"
    "s_waitcnt lgkmcnt(4)\n\t"
    "v_smfmac_f32_16x16x64_bf16 v[192:195], v[120:123], v[84:91], v124\n\t"
    "ds_read_b64 v[84:85], v108 offset:64000\n\t"
    "ds_read_b64 v[86:87], v252 offset:64000\n\t"
    "ds_read_b64 v[88:89], v109 offset:64000\n\t"
    "ds_read_b64 v[90:91], v253 offset:64000\n\t"
    "v_lshrrev_b32 v254, 16, v110\n\t"
    "v_and_b32 v110, s92, v110\n\t"
    "v_lshrrev_b32 v255, 16, v111\n\t"
    "v_and_b32 v111, s92, v111\n\t"
    "s_add_u32 s94, s94, 0x2000\n\t"
    "buffer_load_dwordx4 v[60:63], v127, %12, s94 offen sc1\n\t"
    "v_pk_mul_f32 v[66:67], v[66:67], v[174:175]\n\t"
    "v_exp_f32 v68, v64\n\t"
    "v_exp_f32 v69, v65\n\t"
    "v_exp_f32 v70, v66\n\t"
    "v_exp_f32 v71, v67\n\t"
    "v_lshrrev_b32 v166, 7, v127\n\t"
    "v_and_b32 v167, 0x70, v127\n\t"
    "v_lshl_add_u32 v126, v166, 8, v167\n\t"
    "v_add_u32 v126, 128, v126\n\t"
    "s_bfe_u32 s46, %14, 0x30003\n\t"
    "s_min_u32 s46, s46, 6\n\t"
    "s_lshl_b32 s69, s46, 3\n\t"
    "s_add_u32 s69, s69, %16\n\t"
    "s_waitcnt lgkmcnt(4)\n\t"
    "v_smfmac_f32_16x16x64_bf16 v[192:195], v[120:123], v[92:99], v124\n\t"
    "ds_read_b64 v[92:93], v110 offset:64000\n\t"
    "ds_read_b64 v[94:95], v254 offset:64000\n\t"
    "ds_read_b64 v[96:97], v111 offset:64000\n\t"
    "ds_read_b64 v[98:99], v255 offset:64000\n\t"
    "v_lshrrev_b32 v252, 16, v248\n\t"
    "v_and_b32 v248, s92, v248\n\t"
    "v_lshrrev_b32 v253, 16, v249\n\t"
    "v_and_b32 v249, s92, v249\n\t"
    "v_pk_fma_f32 v[68:69], v[68:69], s[40:41], s[40:41]\n\t"
    "v_pk_fma_f32 v[70:71], v[70:71], s[42:43], s[42:43]\n\t"
    "v_rcp_f32 v68, v68\n\t"
    "v_rcp_f32 v69, v69\n\t"
    "v_rcp_f32 v70, v70\n\t"
    "s_barrier\n\t"
    "s_waitcnt lgkmcnt(4)\n\t"
    "v_smfmac_f32_16x16x64_bf16 v[176:179], v[120:123], v[84:91], v124\n\t"
    "ds_read_b64 v[84:85], v248 offset:64000\n\t"
    "ds_read_b64 v[86:87], v252 offset:64000\n\t"
    "ds_read_b64 v[88:89], v249 offset:64000\n\t"
    "ds_read_b64 v[90:91], v253 offset:64000\n\t"
    "v_lshrrev_b32 v254, 16, v250\n\t"
    "v_and_b32 v250, s92, v250\n\t"
    "v_lshrrev_b32 v255, 16, v251\n\t"
    "v_and_b32 v251, s92, v251\n\t"
    "v_rcp_f32 v71, v71\n\t"
    "v_pk_mul_f32 v[68:69], v[68:69], v[172:173]\n\t"
    "v_pk_fma_f32 v[68:69], v[70:71], v[174:175], v[68:69]\n\t"
    "v_add_f32 v246, v68, v69\n\t"
    "s_mul_i32 s44, s69, 1040\n\t"
    "s_add_u32 m0, s44, 0\n\t"
    "s_lshl_b32 s44, s69, 11\n\t"
    "buffer_load_dwordx4 v126, %15, s44 offen lds\n\t"
    "s_add_u32 s69, s69, 8\n\t"
    "s_sub_u32 s44, s69, 61\n\t"
    "s_cmp_ge_u32 s69, 61\n\t"
    "s_cselect_b32 s69, s44, s69\n\t"
    "s_waitcnt vmcnt(9) lgkmcnt(4)\n\t"
    "v_smfmac_f32_16x16x64_bf16 v[176:179], v[120:123], v[92:99], v124\n\t"
    "ds_read_b64 v[92:93], v250 offset:64000\n\t"
    "ds_read_b64 v[94:95], v254 offset:64000\n\t"
    "ds_read_b64 v[96:97], v251 offset:64000\n\t"
    "ds_read_b64 v[98:99], v255 offset:64000\n\t"
    "v_lshrrev_b32 v252, 16, v212\n\t"
    "v_and_b32 v212, s92, v212\n\t"
    "v_lshrrev_b32 v253, 16, v213\n\t"
    "v_and_b32 v213, s92, v213\n\t"
    "s_mul_i32 s44, s69, 1040\n\t"
    "s_add_u32 m0, s44, 0\n\t"
    "s_lshl_b32 s44, s69, 11\n\t"
    "buffer_load_dwordx4 v126, %15, s44 offen lds\n\t"
    "s_add_u32 s69, s69, 8\n\t"
    "s_sub_u32 s44, s69, 61\n\t"
    "s_cmp_ge_u32 s69, 61\n\t"
    "s_cselect_b32 s69, s44, s69\n\t"
    "v_lshlrev_b32 v146, 2, v147\n\t"
    "s_lshl_b32 s50, s37, 8\n\t"
    "s_add_u32 s50, s50, 26214400\n\t"
    "s_lshl_b32 s46, s53, 8\n\t"
    "s_add_u32 s46, s46, s50\n\t"
    "buffer_load_dword v144, v146, %12, s46 offen sc1\n\t"
    "buffer_load_dword v145, v146, %12, s46 offen offset:512 sc1\n\t"
    "v_mov_b32 v149, s49\n\t"
    "s_add_u32 s46, s51, 6\n\t"
    "v_mov_b32 v155, s46\n\t"
    "ds_write_b32 v149, v155\n\t"
    "s_mov_b32 s61, 0\n\t"
    "v_mov_b32 v149, 1024\n\t"
    "v_mov_b32 v150, 0\n\t"
    "ds_write_b32 v149, v150\n\t"
    "s_mov_b32 s45, 1\n\t"
    "s_waitcnt lgkmcnt(6)\n\t"
    "v_smfmac_f32_16x16x64_bf16 v[180:183], v[120:123], v[84:91], v124\n\t"
    "ds_read_b64 v[84:85], v212 offset:64000\n\t"
    "ds_read_b64 v[86:87], v252 offset:64000\n\t"
    "ds_read_b64 v[88:89], v213 offset:64000\n\t"
    "ds_read_b64 v[90:91], v253 offset:64000\n\t"
    "v_lshrrev_b32 v254, 16, v214\n\t"
    "v_and_b32 v214, s92, v214\n\t"
    "v_lshrrev_b32 v255, 16, v215\n\t"
    "v_and_b32 v215, s92, v215\n\t"
    "s_waitcnt vmcnt(5)\n\t"
    "v_smfmac_f32_16x16x64_bf16 v[180:183], v[156:159], v[48:55], v124\n\t"
    "v_pk_mul_f32 v[72:73], v[176:177], v[176:177]\n\t"
    "v_pk_mul_f32 v[74:75], v[178:179], v[178:179]\n\t"
    "v_pk_fma_f32 v[72:73], v[72:73], v[82:83], v[80:81]\n\t"
    "v_pk_fma_f32 v[74:75], v[74:75], v[82:83], v[80:81]\n\t"
    "v_pk_mul_f32 v[72:73], v[72:73], v[176:177]\n\t"
    "v_pk_mul_f32 v[74:75], v[74:75], v[178:179]\n\t"
    "v_exp_f32 v76, v72\n\t"
    "s_mul_i32 s44, s69, 1040\n\t"
    "s_add_u32 m0, s44, 0\n\t"
    "s_lshl_b32 s44, s69, 11\n\t"
    "buffer_load_dwordx4 v126, %15, s44 offen lds\n\t"
    "s_add_u32 s69, s69, 8\n\t"
    "s_sub_u32 s44, s69, 61\n\t"
    "s_cmp_ge_u32 s69, 61\n\t"
    "s_cselect_b32 s69, s44, s69\n\t"
    "s_waitcnt lgkmcnt(6)\n\t"
    "v_smfmac_f32_16x16x64_bf16 v[180:183], v[120:123], v[92:99], v124\n\t"
    "ds_read_b64 v[92:93], v214 offset:64000\n\t"
    "ds_read_b64 v[94:95], v254 offset:64000\n\t"
    "ds_read_b64 v[96:97], v215 offset:64000\n\t"
    "ds_read_b64 v[98:99], v255 offset:64000\n\t"
    "v_lshrrev_b32 v252, 16, v216\n\t"
    "v_and_b32 v216, s92, v216\n\t"
    "v_lshrrev_b32 v253, 16, v217\n\t"
    "v_and_b32 v217, s92, v217\n\t"
    "v_smfmac_f32_16x16x64_bf16 v[184:187], v[160:163], v[48:55], v124\n\t"
    "v_exp_f32 v77, v73\n\t"
    "v_exp_f32 v78, v74\n\t"
    "v_exp_f32 v79, v75\n\t"
    "v_pk_fma_f32 v[76:77], v[76:77], s[40:41], s[40:41]\n\t"
    "v_pk_fma_f32 v[78:79], v[78:79], s[42:43], s[42:43]\n\t"
    "v_rcp_f32 v76, v76\n\t"
    "v_rcp_f32 v77, v77\n\t"
    "s_mul_i32 s44, s69, 1040\n\t"
    "s_add_u32 m0, s44, 0\n\t"
    "s_lshl_b32 s44, s69, 11\n\t"
    "buffer_load_dwordx4 v126, %15, s44 offen lds\n\t"
    "s_add_u32 s69, s69, 8\n\t"
    "s_sub_u32 s44, s69, 61\n\t"
    "s_cmp_ge_u32 s69, 61\n\t"
    "s_cselect_b32 s69, s44, s69\n\t"
    "ds_read_b128 v[48:51], v148 offset:12288\n\t"
    "s_waitcnt lgkmcnt(5)\n\t"
    "v_smfmac_f32_16x16x64_bf16 v[184:187], v[120:123], v[84:91], v124\n\t"
    "ds_read_b64 v[84:85], v216 offset:64000\n\t"
    "ds_read_b64 v[86:87], v252 offset:64000\n\t"
    "ds_read_b64 v[88:89], v217 offset:64000\n\t"
    "ds_read_b64 v[90:91], v253 offset:64000\n\t"
    "v_lshrrev_b32 v254, 16, v218\n\t"
    "v_and_b32 v218, s92, v218\n\t"
    "v_lshrrev_b32 v255, 16, v219\n\t"
    "v_and_b32 v219, s92, v219\n\t"
    "v_rcp_f32 v78, v78\n\t"
    "v_rcp_f32 v79, v79\n\t"
    "v_pk_mul_f32 v[76:77], v[76:77], v[176:177]\n\t"
    "v_pk_fma_f32 v[76:77], v[78:79], v[178:179], v[76:77]\n\t"
    "v_add_f32 v247, v76, v77\n\t"
    "v_pk_mul_f32 v[64:65], v[180:181], v[180:181]\n\t"
    "v_pk_mul_f32 v[66:67], v[182:183], v[182:183]\n\t"
    "s_mul_i32 s44, s69, 1040\n\t"
    "s_add_u32 m0, s44, 0\n\t"
    "s_lshl_b32 s44, s69, 11\n\t"
    "buffer_load_dwordx4 v126, %15, s44 offen lds\n\t"
    "s_add_u32 s69, s69, 8\n\t"
    "s_sub_u32 s44, s69, 61\n\t"
    "s_cmp_ge_u32 s69, 61\n\t"
    "s_cselect_b32 s69, s44, s69\n\t"
    "ds_read_b128 v[52:55], v148 offset:13312\n\t"
    "s_waitcnt lgkmcnt(6)\n\t"
    "v_smfmac_f32_16x16x64_bf16 v[184:187], v[120:123], v[92:99], v124\n\t"
    "ds_read_b64 v[92:93], v218 offset:64000\n\t"
    "ds_read_b64 v[94:95], v254 offset:64000\n\t"
    "ds_read_b64 v[96:97], v219 offset:64000\n\t"
    "ds_read_b64 v[98:99], v255 offset:64000\n\t"
    "v_lshrrev_b32 v252, 16, v220\n\t"
    "v_and_b32 v220, s92, v220\n\t"
    "v_lshrrev_b32 v253, 16, v221\n\t"
    "v_and_b32 v221, s92, v221\n\t"
    "v_pk_fma_f32 v[64:65], v[64:65], v[82:83], v[80:81]\n\t"
    "v_pk_fma_f32 v[66:67], v[66:67], v[82:83], v[80:81]\n\t"
    "v_pk_mul_f32 v[64:65], v[64:65], v[180:181]\n\t"
    "v_pk_mul_f32 v[66:67], v[66:67], v[182:183]\n\t"
    "v_exp_f32 v68, v64\n\t"
    "v_exp_f32 v69, v65\n\t"
    "v_exp_f32 v70, v66\n\t"
    "s_mul_i32 s44, s69, 1040\n\t"
    "s_add_u32 m0, s44, 0\n\t"
    "s_lshl_b32 s44, s69, 11\n\t"
    "buffer_load_dwordx4 v126, %15, s44 offen lds\n\t"
    "s_add_u32 s69, s69, 8\n\t"
    "s_sub_u32 s44, s69, 61\n\t"
    "s_cmp_ge_u32 s69, 61\n\t"
    "s_cselect_b32 s69, s44, s69\n\t"
    "s_waitcnt lgkmcnt(5)\n\t"
    "v_smfmac_f32_16x16x64_bf16 v[188:191], v[120:123], v[84:91], v124\n\t"
    "ds_read_b64 v[84:85], v220 offset:64000\n\t"
    "ds_read_b64 v[86:87], v252 offset:64000\n\t"
    "ds_read_b64 v[88:89], v221 offset:64000\n\t"
    "ds_read_b64 v[90:91], v253 offset:64000\n\t"
    "v_lshrrev_b32 v254, 16, v222\n\t"
    "v_and_b32 v222, s92, v222\n\t"
    "v_lshrrev_b32 v255, 16, v223\n\t"
    "v_and_b32 v223, s92, v223\n\t"
    "s_waitcnt vmcnt(8)\n\t"
    "v_smfmac_f32_16x16x64_bf16 v[188:191], v[156:159], v[56:63], v124\n\t"
    "v_exp_f32 v71, v67\n\t"
    "v_pk_fma_f32 v[68:69], v[68:69], s[40:41], s[40:41]\n\t"
    "v_pk_fma_f32 v[70:71], v[70:71], s[42:43], s[42:43]\n\t"
    "v_rcp_f32 v68, v68\n\t"
    "v_rcp_f32 v69, v69\n\t"
    "v_rcp_f32 v70, v70\n\t"
    "v_rcp_f32 v71, v71\n\t"
    "s_mul_i32 s44, s69, 1040\n\t"
    "s_add_u32 m0, s44, 0\n\t"
    "s_lshl_b32 s44, s69, 11\n\t"
    "buffer_load_dwordx4 v126, %15, s44 offen lds\n\t"
    "s_add_u32 s69, s69, 8\n\t"
    "s_sub_u32 s44, s69, 61\n\t"
    "s_cmp_ge_u32 s69, 61\n\t"
    "s_cselect_b32 s69, s44, s69\n\t"
    "s_waitcnt lgkmcnt(4)\n\t"
    "v_smfmac_f32_16x16x64_bf16 v[188:191], v[120:123], v[92:99], v124\n\t"
    "ds_read_b64 v[92:93], v222 offset:64000\n\t"
    "ds_read_b64 v[94:95], v254 offset:64000\n\t"
    "ds_read_b64 v[96:97], v223 offset:64000\n\t"
    "ds_read_b64 v[98:99], v255 offset:64000\n\t"
    "v_smfmac_f32_16x16x64_bf16 v[192:195], v[160:163], v[56:63], v124\n\t"
    "v_pk_mul_f32 v[68:69], v[68:69], v[180:181]\n\t"
    "v_pk_fma_f32 v[68:69], v[70:71], v[182:183], v[68:69]\n\t"
    "v_add_f32 v248, v68, v69\n\t"
    "v_pk_mul_f32 v[72:73], v[184:185], v[184:185]\n\t"
    "v_pk_mul_f32 v[74:75], v[186:187], v[186:187]\n\t"
    "v_pk_fma_f32 v[72:73], v[72:73], v[82:83], v[80:81]\n\t"
    "v_pk_fma_f32 v[74:75], v[74:75], v[82:83], v[80:81]\n\t"
    "s_mul_i32 s44, s69, 1040\n\t"
    "s_add_u32 m0, s44, 0\n\t"
    "s_lshl_b32 s44, s69, 11\n\t"
    "buffer_load_dwordx4 v126, %15, s44 offen lds\n\t"
    "s_add_u32 s69, s69, 8\n\t"
    "s_sub_u32 s44, s69, 61\n\t"
    "s_cmp_ge_u32 s69, 61\n\t"
    "s_cselect_b32 s69, s44, s69\n\t"
    "ds_read_b128 v[56:59], v148 offset:14336\n\t"
    "s_waitcnt lgkmcnt(5)\n\t"
    "v_smfmac_f32_16x16x64_bf16 v[192:195], v[120:123], v[84:91], v124\n\t"
    "v_pk_mul_f32 v[72:73], v[72:73], v[184:185]\n\t"
    "v_pk_mul_f32 v[74:75], v[74:75], v[186:187]\n\t"
    "v_exp_f32 v76, v72\n\t"
    "v_exp_f32 v77, v73\n\t"
    "v_exp_f32 v78, v74\n\t"
    "v_exp_f32 v79, v75\n\t"
    "v_pk_fma_f32 v[76:77], v[76:77], s[40:41], s[40:41]\n\t"
    "ds_read_b128 v[60:63], v148 offset:15360\n\t"
    "s_waitcnt lgkmcnt(2)\n\t"
    "v_smfmac_f32_16x16x64_bf16 v[192:195], v[120:123], v[92:99], v124\n\t"
    "v_pk_fma_f32 v[78:79], v[78:79], s[42:43], s[42:43]\n\t"
    "v_rcp_f32 v76, v76\n\t"
    "v_rcp_f32 v77, v77\n\t"
    "v_rcp_f32 v78, v78\n\t"
    "v_rcp_f32 v79, v79\n\t"
    "v_pk_mul_f32 v[76:77], v[76:77], v[184:185]\n\t"
    "v_pk_fma_f32 v[76:77], v[78:79], v[186:187], v[76:77]\n\t"
    "s_barrier\n\t"
    "v_add_u32 v126, 124928, v126\n\t"
    "s_bfe_u32 s46, %14, 0x30003\n\t"
    "s_min_u32 s46, s46, 6\n\t"
    "s_lshl_b32 s69, s46, 3\n\t"
    "s_add_u32 s69, s69, %16\n\t"
    "s_mul_i32 s44, s69, 1040\n\t"
    "s_add_u32 m0, s44, 63440\n\t"
    "s_lshl_b32 s44, s69, 11\n\t"
    "buffer_load_dwordx4 v126, %15, s44 offen lds\n\t"
    "s_add_u32 s69, s69, 8\n\t"
    "s_sub_u32 s44, s69, 64\n\t"
    "s_cmp_ge_u32 s69, 64\n\t"
    "s_cselect_b32 s69, s44, s69\n\t"
    "s_mul_i32 s44, s69, 1040\n\t"
    "s_add_u32 m0, s44, 63440\n\t"
    "s_lshl_b32 s44, s69, 11\n\t"
    "buffer_load_dwordx4 v126, %15, s44 offen lds\n\t"
    "s_add_u32 s69, s69, 8\n\t"
    "s_sub_u32 s44, s69, 64\n\t"
    "s_cmp_ge_u32 s69, 64\n\t"
    "s_cselect_b32 s69, s44, s69\n\t"
    "s_mul_i32 s44, s69, 1040\n\t"
    "s_add_u32 m0, s44, 63440\n\t"
    "s_lshl_b32 s44, s69, 11\n\t"
    "buffer_load_dwordx4 v126, %15, s44 offen lds\n\t"
    "s_add_u32 s69, s69, 8\n\t"
    "s_sub_u32 s44, s69, 64\n\t"
    "s_cmp_ge_u32 s69, 64\n\t"
    "s_cselect_b32 s69, s44, s69\n\t"
    "s_mul_i32 s44, s69, 1040\n\t"
    "s_add_u32 m0, s44, 63440\n\t"
    "s_lshl_b32 s44, s69, 11\n\t"
    "buffer_load_dwordx4 v126, %15, s44 offen lds\n\t"
    "s_add_u32 s69, s69, 8\n\t"
    "s_sub_u32 s44, s69, 64\n\t"
    "s_cmp_ge_u32 s69, 64\n\t"
    "s_cselect_b32 s69, s44, s69\n\t"
    "ds_read_b128 v[84:87], v148 offset:21504\n\t"
    "ds_read_b128 v[88:91], v148 offset:22528\n\t"
    "v_add_f32 v249, v76, v77\n\t"
    "s_mul_i32 s44, s69, 1040\n\t"
    "s_add_u32 m0, s44, 63440\n\t"
    "s_lshl_b32 s44, s69, 11\n\t"
    "buffer_load_dwordx4 v126, %15, s44 offen lds\n\t"
    "s_add_u32 s69, s69, 8\n\t"
    "s_sub_u32 s44, s69, 64\n\t"
    "s_cmp_ge_u32 s69, 64\n\t"
    "s_cselect_b32 s69, s44, s69\n\t"
    "s_mul_i32 s44, s69, 1040\n\t"
    "s_add_u32 m0, s44, 63440\n\t"
    "s_lshl_b32 s44, s69, 11\n\t"
    "buffer_load_dwordx4 v126, %15, s44 offen lds\n\t"
    "s_add_u32 s69, s69, 8\n\t"
    "s_sub_u32 s44, s69, 64\n\t"
    "s_cmp_ge_u32 s69, 64\n\t"
    "s_cselect_b32 s69, s44, s69\n\t"
    "v_pk_mul_f32 v[64:65], v[188:189], v[188:189]\n\t"
    "v_pk_mul_f32 v[66:67], v[190:191], v[190:191]\n\t"
    "v_pk_fma_f32 v[64:65], v[64:65], v[82:83], v[80:81]\n\t"
    "v_pk_fma_f32 v[66:67], v[66:67], v[82:83], v[80:81]\n\t"
    "v_pk_mul_f32 v[64:65], v[64:65], v[188:189]\n\t"
    "v_pk_mul_f32 v[66:67], v[66:67], v[190:191]\n\t"
    "v_exp_f32 v68, v64\n\t"
    "v_exp_f32 v69, v65\n\t"
    "v_exp_f32 v70, v66\n\t"
    "v_exp_f32 v71, v67\n\t"
    "v_pk_fma_f32 v[68:69], v[68:69], s[40:41], s[40:41]\n\t"
    "v_pk_fma_f32 v[70:71], v[70:71], s[42:43], s[42:43]\n\t"
    "v_rcp_f32 v68, v68\n\t"
    "v_rcp_f32 v69, v69\n\t"
    "v_rcp_f32 v70, v70\n\t"
    "v_rcp_f32 v71, v71\n\t"
    "v_pk_mul_f32 v[68:69], v[68:69], v[188:189]\n\t"
    "v_pk_fma_f32 v[68:69], v[70:71], v[190:191], v[68:69]\n\t"
    "v_add_f32 v250, v68, v69\n\t"
    "s_mul_i32 s44, s69, 1040\n\t"
    "s_add_u32 m0, s44, 63440\n\t"
    "s_lshl_b32 s44, s69, 11\n\t"
    "buffer_load_dwordx4 v126, %15, s44 offen lds\n\t"
    "s_add_u32 s69, s69, 8\n\t"
    "s_sub_u32 s44, s69, 64\n\t"
    "s_cmp_ge_u32 s69, 64\n\t"
    "s_cselect_b32 s69, s44, s69\n\t"
    "s_mul_i32 s44, s69, 1040\n\t"
    "s_add_u32 m0, s44, 63440\n\t"
    "s_lshl_b32 s44, s69, 11\n\t"
    "buffer_load_dwordx4 v126, %15, s44 offen lds\n\t"
    "s_add_u32 s69, s69, 8\n\t"
    "s_sub_u32 s44, s69, 64\n\t"
    "s_cmp_ge_u32 s69, 64\n\t"
    "s_cselect_b32 s69, s44, s69\n\t"
    "v_pk_mul_f32 v[72:73], v[192:193], v[192:193]\n\t"
    "v_pk_mul_f32 v[74:75], v[194:195], v[194:195]\n\t"
    "v_pk_fma_f32 v[72:73], v[72:73], v[82:83], v[80:81]\n\t"
    "v_pk_fma_f32 v[74:75], v[74:75], v[82:83], v[80:81]\n\t"
    "v_pk_mul_f32 v[72:73], v[72:73], v[192:193]\n\t"
    "v_pk_mul_f32 v[74:75], v[74:75], v[194:195]\n\t"
    "v_exp_f32 v76, v72\n\t"
    "v_exp_f32 v77, v73\n\t"
    "v_exp_f32 v78, v74\n\t"
    "v_exp_f32 v79, v75\n\t"
    "v_pk_fma_f32 v[76:77], v[76:77], s[40:41], s[40:41]\n\t"
    "v_pk_fma_f32 v[78:79], v[78:79], s[42:43], s[42:43]\n\t"
    "v_rcp_f32 v76, v76\n\t"
    "v_rcp_f32 v77, v77\n\t"
    "v_rcp_f32 v78, v78\n\t"
    "v_rcp_f32 v79, v79\n\t"
    "v_pk_mul_f32 v[76:77], v[76:77], v[192:193]\n\t"
    "v_pk_fma_f32 v[76:77], v[78:79], v[194:195], v[76:77]\n\t"
    "v_add_f32 v251, v76, v77\n\t"
    "ds_read_b128 v[64:67], v148 offset:16384\n\t"
    "ds_read_b128 v[68:71], v148 offset:17408\n\t"
    "ds_read_b128 v[72:75], v148 offset:18432\n\t"
    "ds_read_b128 v[76:79], v148 offset:19456\n\t"
    "ds_read_b128 v[80:83], v148 offset:20480\n\t"
    "s_waitcnt vmcnt(0) lgkmcnt(0)\n\t"
    "s_mov_b32 s68, 130\n\t"
    "v_mad_u32_u24 v128, v128, s68, v152\n\t"
    "v_and_b32 v128, -16, v128\n\t"
    "v_mad_u32_u24 v129, v129, s68, v151\n\t"
    "v_and_b32 v129, -16, v129\n\t"
    "v_mad_u32_u24 v130, v130, s68, v151\n\t"
    "v_and_b32 v130, -16, v130\n\t"
    "v_mad_u32_u24 v132, v132, s68, v151\n\t"
    "v_and_b32 v132, -16, v132\n\t"
    "v_mad_u32_u24 v133, v133, s68, v151\n\t"
    "v_and_b32 v133, -16, v133\n\t"
    "v_mad_u32_u24 v134, v134, s68, v151\n\t"
    "v_and_b32 v134, -16, v134\n\t"
    "v_mad_u32_u24 v135, v135, s68, v151\n\t"
    "v_and_b32 v135, -16, v135\n\t"
    "v_lshrrev_b32 v122, 4, v147\n\t"
    "v_and_b32 v155, 15, v147\n\t"
    "v_lshlrev_b32 v155, 4, v155\n\t"
    "v_lshl_add_u32 v122, v122, 16, v155\n\t"
    "v_mov_b32 v150, 1\n\t"
    "v_mov_b32 v123, 0\n\t"
    "v_lshlrev_b32 v149, 20, v147\n\t"
    "v_add_u32 v149, s49, v149\n\t"
    "s_mov_b32 s52, s53\n\t"
    "s_cmp_ge_u32 %16, 4\n\t"
    "s_cbranch_scc1 L_st4_%=\n\t"
    "s_bfe_u32 s65, s84, 0xc0002\n\t"
    "s_add_u32 s65, s65, s36\n\t"
    "buffer_store_dword v236, v125, %12, s65 offen offset:0 sc1\n\t"
    "buffer_store_dword v237, v125, %12, s65 offen offset:256 sc1\n\t"
    "s_bfe_u32 s65, s85, 0xc0002\n\t"
    "s_add_u32 s65, s65, s36\n\t"
    "buffer_store_dword v238, v125, %12, s65 offen offset:0 sc1\n\t"
    "buffer_store_dword v239, v125, %12, s65 offen offset:256 sc1\n\t"
    "s_bfe_u32 s65, s86, 0xc0002\n\t"
    "s_add_u32 s65, s65, s36\n\t"
    "buffer_store_dword v240, v125, %12, s65 offen offset:0 sc1\n\t"
    "buffer_store_dword v241, v125, %12, s65 offen offset:256 sc1\n\t"
    "s_bfe_u32 s65, s87, 0xc0002\n\t"
    "s_add_u32 s65, s65, s36\n\t"
    "buffer_store_dword v242, v125, %12, s65 offen offset:0 sc1\n\t"
    "buffer_store_dword v243, v125, %12, s65 offen offset:256 sc1\n\t"
    "s_bfe_u32 s65, s88, 0xc0002\n\t"
    "s_add_u32 s65, s65, s36\n\t"
    "buffer_store_dword v244, v125, %12, s65 offen offset:0 sc1\n\t"
    "buffer_store_dword v245, v125, %12, s65 offen offset:256 sc1\n\t"
    "s_bfe_u32 s65, s89, 0xc0002\n\t"
    "s_add_u32 s65, s65, s36\n\t"
    "buffer_store_dword v246, v125, %12, s65 offen offset:0 sc1\n\t"
    "buffer_store_dword v247, v125, %12, s65 offen offset:256 sc1\n\t"
    "s_bfe_u32 s65, s90, 0xc0002\n\t"
    "s_add_u32 s65, s65, s36\n\t"
    "buffer_store_dword v248, v125, %12, s65 offen offset:0 sc1\n\t"
    "buffer_store_dword v249, v125, %12, s65 offen offset:256 sc1\n\t"
    "s_bfe_u32 s65, s91, 0xc0002\n\t"
    "s_add_u32 s65, s65, s36\n\t"
    "buffer_store_dword v250, v125, %12, s65 offen offset:0 sc1\n\t"
    "buffer_store_dword v251, v125, %12, s65 offen offset:256 sc1\n\t"
    "s_sub_u32 s61, s64, 3\n\t"
    "L_st4_%=:\n\t"
    "s_barrier\n\t"
    "s_mov_b32 s68, 130\n\t"
    "ds_read_b128 v[156:159], v128 offset:0\n\t"
    "ds_read_b128 v[160:163], v128 offset:64\n\t"
    "ds_read_b128 v[164:167], v129 offset:0\n\t"
    "ds_read_b128 v[168:171], v129 offset:64\n\t"
    "ds_read_b128 v[172:175], v130 offset:0\n\t"
    "ds_read_b128 v[176:179], v130 offset:64\n\t"
    "ds_read_b128 v[180:183], v132 offset:0\n\t"
    "ds_read_b128 v[184:187], v132 offset:64\n\t"
    "ds_read_b128 v[188:191], v133 offset:0\n\t"
    "ds_read_b128 v[192:195], v133 offset:64\n\t"
    "ds_read_b128 v[196:199], v134 offset:0\n\t"
    "ds_read_b128 v[200:203], v134 offset:64\n\t"
    "ds_read_b128 v[204:207], v135 offset:0\n\t"
    "ds_read_b128 v[208:211], v135 offset:64\n\t"
    "s_setprio 1\n\t"
    "L_pnn_%=:\n\t"
    "s_mul_i32 s69, s52, 272\n\t"
    "s_add_u32 s69, s69, 130048\n\t"
    "s_mul_i32 s66, s55, 400\n\t"
    "s_waitcnt lgkmcnt(8)\n\t"
    "v_mfma_f32_16x16x32_bf16 v[92:95], v[156:159], v[156:159], 0\n\t"
    "v_add_u32 v147, s69, v146\n\t"
    "v_fmac_f32 v121, v108, v72\n\t"
    "ds_add_rtn_u32 v148, v149, v150\n\t"
    "v_mfma_f32_16x16x32_bf16 v[92:95], v[160:163], v[160:163], v[92:95]\n\t"
    "v_fmac_f32 v120, v109, v73\n\t"
    "v_fmac_f32 v121, v110, v74\n\t"
    "buffer_load_dwordx3 v[128:130], v153, s[96:99], s66 offen\n\t"
    "v_mfma_f32_16x16x32_bf16 v[96:99], v[164:167], v[164:167], 0\n\t"
    "v_fmac_f32 v120, v111, v75\n\t"
    "v_fmac_f32 v121, v112, v76\n\t"
    "v_mfma_f32_16x16x32_bf16 v[96:99], v[168:171], v[168:171], v[96:99]\n\t"
    "v_fmac_f32 v120, v113, v77\n\t"
    "v_fmac_f32 v121, v114, v78\n\t"
    "buffer_load_dwordx4 v[132:135], v153, s[96:99], s66 offen offset:12\n\t"
    "v_mfma_f32_16x16x32_bf16 v[100:103], v[164:167], v[172:175], 0\n\t"
    "v_fmac_f32 v120, v115, v79\n\t"
    "v_fmac_f32 v121, v116, v80\n\t"
    "v_mfma_f32_16x16x32_bf16 v[100:103], v[168:171], v[176:179], v[100:103]\n\t"
    "v_fmac_f32 v120, v117, v81\n\t"
    "v_fmac_f32 v121, v118, v82\n\t"
    "s_waitcnt vmcnt(3)\n\t"
    "s_cmp_ge_u32 s55, s61\n\t"
    "s_cbranch_scc1 L_req0_%=\n\t"
    "L_back0_%=:\n\t"
    "v_mfma_f32_16x16x32_bf16 v[104:107], v[172:175], v[172:175], 0\n\t"
    "v_mad_u32_u24 v136, v136, s68, v152\n\t"
    "v_and_b32 v136, -16, v136\n\t"
    "v_mfma_f32_16x16x32_bf16 v[104:107], v[176:179], v[176:179], v[104:107]\n\t"
    "v_fmac_f32 v120, v119, v83\n\t"
    "v_add_f32 v154, v120, v121\n\t"
    "ds_write_b32 v147, v154\n\t"
    "v_mfma_f32_16x16x32_bf16 v[108:111], v[156:159], v[164:167], 0\n\t"
    "v_fma_f32 v120, v92, v84, v144\n\t"
    "v_mul_f32 v121, v96, v0\n\t"
    "v_mfma_f32_16x16x32_bf16 v[108:111], v[160:163], v[168:171], v[108:111]\n\t"
    "v_fmac_f32 v120, v97, v1\n\t"
    "v_fmac_f32 v121, v98, v2\n\t"
    "s_waitcnt lgkmcnt(8)\n\t"
    "v_mfma_f32_16x16x32_bf16 v[112:115], v[164:167], v[180:183], 0\n\t"
    "v_fmac_f32 v120, v99, v3\n\t"
    "v_fmac_f32 v121, v100, v4\n\t"
    "v_mfma_f32_16x16x32_bf16 v[112:115], v[168:171], v[184:187], v[112:115]\n\t"
    "v_fmac_f32 v120, v101, v5\n\t"
    "v_fmac_f32 v121, v102, v6\n\t"
    "v_mfma_f32_16x16x32_bf16 v[116:119], v[172:175], v[180:183], 0\n\t"
    "v_fmac_f32 v120, v103, v7\n\t"
    "v_fmac_f32 v121, v104, v8\n\t"
    "v_mfma_f32_16x16x32_bf16 v[116:119], v[176:179], v[184:187], v[116:119]\n\t"
    "v_fmac_f32 v120, v105, v9\n\t"
    "v_fmac_f32 v121, v106, v10\n\t"
    "ds_read_b128 v[212:215], v136 offset:0\n\t"
    "ds_read_b128 v[216:219], v136 offset:64\n\t"
    "v_mfma_f32_16x16x32_bf16 v[92:95], v[180:183], v[180:183], 0\n\t"
    "v_fmac_f32 v120, v107, v11\n\t"
    "v_mad_u32_u24 v137, v137, s68, v151\n\t"
    "v_mfma_f32_16x16x32_bf16 v[92:95], v[184:187], v[184:187], v[92:95]\n\t"
    "v_and_b32 v137, -16, v137\n\t"
    "v_fmac_f32 v121, v108, v85\n\t"
    "v_mfma_f32_16x16x32_bf16 v[96:99], v[156:159], v[172:175], 0\n\t"
    "v_fmac_f32 v120, v112, v12\n\t"
    "v_fmac_f32 v121, v113, v13\n\t"
    "v_mfma_f32_16x16x32_bf16 v[96:99], v[160:163], v[176:179], v[96:99]\n\t"
    "v_fmac_f32 v120, v114, v14\n\t"
    "v_fmac_f32 v121, v115, v15\n\t"
    "s_lshl_b32 s51, s55, 8\n\t"
    "s_add_u32 s51, s51, s50\n\t"
    "s_waitcnt lgkmcnt(8)\n\t"
    "v_mfma_f32_16x16x32_bf16 v[100:103], v[164:167], v[188:191], 0\n\t"
    "v_mad_u32_u24 v138, v138, s68, v151\n\t"
    "v_and_b32 v138, -16, v138\n\t"
    "v_mfma_f32_16x16x32_bf16 v[100:103], v[168:171], v[192:195], v[100:103]\n\t"
    "v_fmac_f32 v120, v116, v16\n\t"
    "v_fmac_f32 v121, v117, v17\n\t"
    "buffer_load_dword v144, v146, %12, s51 offen sc1\n\t"
    "v_mfma_f32_16x16x32_bf16 v[104:107], v[172:175], v[188:191], 0\n\t"
    "v_fmac_f32 v120, v118, v18\n\t"
    "v_fmac_f32 v121, v119, v19\n\t"
    "v_mfma_f32_16x16x32_bf16 v[104:107], v[176:179], v[192:195], v[104:107]\n\t"
    "v_fmac_f32 v120, v92, v20\n\t"
    "v_fmac_f32 v121, v93, v21\n\t"
    "s_waitcnt lgkmcnt(6)\n\t"
    "v_mfma_f32_16x16x32_bf16 v[108:111], v[164:167], v[196:199], 0\n\t"
    "v_fmac_f32 v120, v94, v22\n\t"
    "v_fmac_f32 v121, v95, v23\n\t"
    "v_mfma_f32_16x16x32_bf16 v[108:111], v[168:171], v[200:203], v[108:111]\n\t"
    "v_fmac_f32 v120, v96, v86\n\t"
    "v_mad_u32_u24 v140, v140, s68, v151\n\t"
    "v_mfma_f32_16x16x32_bf16 v[112:115], v[156:159], v[180:183], 0\n\t"
    "v_fmac_f32 v121, v100, v24\n\t"
    "v_fmac_f32 v120, v101, v25\n\t"
    "v_mfma_f32_16x16x32_bf16 v[112:115], v[160:163], v[184:187], v[112:115]\n\t"
    "v_fmac_f32 v121, v102, v26\n\t"
    "v_fmac_f32 v120, v103, v27\n\t"
    "s_waitcnt lgkmcnt(3)\n\t"
    "v_mfma_f32_16x16x32_bf16 v[116:119], v[164:167], v[204:207], 0\n\t"
    "v_readfirstlane_b32 s60, v148\n\t"
    "v_and_b32 v140, -16, v140\n\t"
    "v_mfma_f32_16x16x32_bf16 v[116:119], v[168:171], v[208:211], v[116:119]\n\t"
    "v_fmac_f32 v121, v104, v28\n\t"
    "v_fmac_f32 v120, v105, v29\n\t"
    "ds_read_b128 v[164:167], v137 offset:0\n\t"
    "ds_read_b128 v[168:171], v137 offset:64\n\t"
    "v_mfma_f32_16x16x32_bf16 v[92:95], v[172:175], v[196:199], 0\n\t"
    "v_fmac_f32 v121, v106, v30\n\t"
    "v_fmac_f32 v120, v107, v31\n\t"
    "v_mfma_f32_16x16x32_bf16 v[92:95], v[176:179], v[200:203], v[92:95]\n\t"
    "v_fmac_f32 v121, v108, v32\n\t"
    "v_fmac_f32 v120, v109, v33\n\t"
    "v_mfma_f32_16x16x32_bf16 v[96:99], v[172:175], v[204:207], 0\n\t"
    "v_fmac_f32 v121, v110, v34\n\t"
    "v_fmac_f32 v120, v111, v35\n\t"
    "v_mfma_f32_16x16x32_bf16 v[96:99], v[176:179], v[208:211], v[96:99]\n\t"
    "v_fmac_f32 v121, v112, v87\n\t"
    "v_mad_u32_u24 v141, v141, s68, v151\n\t"
    "ds_read_b128 v[172:175], v138 offset:0\n\t"
    "ds_read_b128 v[176:179], v138 offset:64\n\t"
    "v_mfma_f32_16x16x32_bf16 v[100:103], v[156:159], v[188:191], 0\n\t"
    "v_fmac_f32 v120, v116, v36\n\t"
    "v_fmac_f32 v121, v117, v37\n\t"
    "v_mfma_f32_16x16x32_bf16 v[100:103], v[160:163], v[192:195], v[100:103]\n\t"
    "v_fmac_f32 v120, v118, v38\n\t"
    "v_fmac_f32 v121, v119, v39\n\t"
    "v_mfma_f32_16x16x32_bf16 v[104:107], v[180:183], v[188:191], 0\n\t"
    "v_and_b32 v141, -16, v141\n\t"
    "v_fmac_f32 v120, v92, v40\n\t"
    "v_mfma_f32_16x16x32_bf16 v[104:107], v[184:187], v[192:195], v[104:107]\n\t"
    "v_fmac_f32 v121, v93, v41\n\t"
    "v_fmac_f32 v120, v94, v42\n\t"
    "v_mfma_f32_16x16x32_bf16 v[108:111], v[180:183], v[196:199], 0\n\t"
    "v_fmac_f32 v121, v95, v43\n\t"
    "v_fmac_f32 v120, v96, v44\n\t"
    "v_mfma_f32_16x16x32_bf16 v[108:111], v[184:187], v[200:203], v[108:111]\n\t"
    "v_fmac_f32 v121, v97, v45\n\t"
    "v_fmac_f32 v120, v98, v46\n\t"
    "v_mfma_f32_16x16x32_bf16 v[112:115], v[180:183], v[204:207], 0\n\t"
    "v_fmac_f32 v121, v99, v47\n\t"
    "v_fmac_f32 v120, v100, v88\n\t"
    "v_mfma_f32_16x16x32_bf16 v[112:115], v[184:187], v[208:211], v[112:115]\n\t"
    "v_mad_u32_u24 v142, v142, s68, v151\n\t"
    "v_and_b32 v142, -16, v142\n\t"
    "ds_read_b128 v[180:183], v140 offset:0\n\t"
    "ds_read_b128 v[184:187], v140 offset:64\n\t"
    "s_cmp_eq_u32 s45, 1\n\t"
    "s_cbranch_scc1 L_sig_%=\n\t"
    "L_sigb_%=:\n\t"
    "v_mfma_f32_16x16x32_bf16 v[116:119], v[156:159], v[196:199], 0\n\t"
    "v_fmac_f32 v121, v104, v48\n\t"
    "v_fmac_f32 v120, v105, v49\n\t"
    "v_mfma_f32_16x16x32_bf16 v[116:119], v[160:163], v[200:203], v[116:119]\n\t"
    "v_fmac_f32 v121, v106, v50\n\t"
    "v_fmac_f32 v120, v107, v51\n\t"
    "v_mfma_f32_16x16x32_bf16 v[92:95], v[188:191], v[188:191], 0\n\t"
    "v_mad_u32_u24 v143, v143, s68, v151\n\t"
    "v_and_b32 v143, -16, v143\n\t"
    "v_mfma_f32_16x16x32_bf16 v[92:95], v[192:195], v[192:195], v[92:95]\n\t"
    "v_fmac_f32 v121, v108, v52\n\t"
    "v_fmac_f32 v120, v109, v53\n\t"
    "v_mfma_f32_16x16x32_bf16 v[96:99], v[188:191], v[196:199], 0\n\t"
    "v_fmac_f32 v121, v110, v54\n\t"
    "v_fmac_f32 v120, v111, v55\n\t"
    "v_mfma_f32_16x16x32_bf16 v[96:99], v[192:195], v[200:203], v[96:99]\n\t"
    "v_fmac_f32 v121, v112, v56\n\t"
    "v_fmac_f32 v120, v113, v57\n\t"
    "v_mfma_f32_16x16x32_bf16 v[100:103], v[188:191], v[204:207], 0\n\t"
    "v_fmac_f32 v121, v114, v58\n\t"
    "v_fmac_f32 v120, v115, v59\n\t"
    "v_mfma_f32_16x16x32_bf16 v[100:103], v[192:195], v[208:211], v[100:103]\n\t"
    "v_fmac_f32 v121, v116, v89\n\t"
    "ds_read_b128 v[188:191], v141 offset:0\n\t"
    "ds_read_b128 v[192:195], v141 offset:64\n\t"
    "v_mfma_f32_16x16x32_bf16 v[104:107], v[156:159], v[204:207], 0\n\t"
    "v_fmac_f32 v120, v92, v60\n\t"
    "v_fmac_f32 v121, v93, v61\n\t"
    "v_mfma_f32_16x16x32_bf16 v[104:107], v[160:163], v[208:211], v[104:107]\n\t"
    "v_fmac_f32 v120, v94, v62\n\t"
    "v_fmac_f32 v121, v95, v63\n\t"
    "v_mfma_f32_16x16x32_bf16 v[108:111], v[196:199], v[196:199], 0\n\t"
    "v_fmac_f32 v120, v96, v64\n\t"
    "v_fmac_f32 v121, v97, v65\n\t"
    "v_mfma_f32_16x16x32_bf16 v[108:111], v[200:203], v[200:203], v[108:111]\n\t"
    "v_fmac_f32 v120, v98, v66\n\t"
    "v_fmac_f32 v121, v99, v67\n\t"
    "v_mfma_f32_16x16x32_bf16 v[112:115], v[196:199], v[204:207], 0\n\t"
    "v_fmac_f32 v120, v100, v68\n\t"
    "v_fmac_f32 v121, v101, v69\n\t"
    "v_mfma_f32_16x16x32_bf16 v[112:115], v[200:203], v[208:211], v[112:115]\n\t"
    "v_fmac_f32 v120, v102, v70\n\t"
    "v_fmac_f32 v121, v103, v71\n\t"
    "ds_read_b128 v[196:199], v142 offset:0\n\t"
    "ds_read_b128 v[200:203], v142 offset:64\n\t"
    "v_mfma_f32_16x16x32_bf16 v[116:119], v[204:207], v[204:207], 0\n\t"
    "v_fmac_f32 v120, v104, v90\n\t"
    "v_mfma_f32_16x16x32_bf16 v[116:119], v[208:211], v[208:211], v[116:119]\n\t"
    "ds_read_b128 v[204:207], v143 offset:0\n\t"
    "ds_read_b128 v[208:211], v143 offset:64\n\t"
    "s_mov_b32 s52, s53\n\t"
    "s_mov_b32 s53, s54\n\t"
    "s_mov_b32 s54, s55\n\t"
    "s_mov_b32 s55, s60\n\t"
    "s_cmp_lt_u32 s54, s64\n\t"
    "s_cbranch_scc1 L_pr0_%=\n\t"
    "s_setprio 0\n\t"
    "L_pr0_%=:\n\t"
    "s_cmp_lt_u32 s53, s64\n\t"
    "s_cbranch_scc0 L_done_%=\n\t"
    "s_mul_i32 s69, s52, 272\n\t"
    "s_add_u32 s69, s69, 130048\n\t"
    "s_mul_i32 s66, s55, 400\n\t"
    "s_waitcnt lgkmcnt(8)\n\t"
    "v_mfma_f32_16x16x32_bf16 v[92:95], v[212:215], v[212:215], 0\n\t"
    "v_add_u32 v147, s69, v146\n\t"
    "v_fmac_f32 v121, v108, v72\n\t"
    "ds_add_rtn_u32 v148, v149, v150\n\t"
    "v_mfma_f32_16x16x32_bf16 v[92:95], v[216:219], v[216:219], v[92:95]\n\t"
    "v_fmac_f32 v120, v109, v73\n\t"
    "v_fmac_f32 v121, v110, v74\n\t"
    "buffer_load_dwordx3 v[136:138], v153, s[96:99], s66 offen\n\t"
    "v_mfma_f32_16x16x32_bf16 v[96:99], v[164:167], v[164:167], 0\n\t"
    "v_fmac_f32 v120, v111, v75\n\t"
    "v_fmac_f32 v121, v112, v76\n\t"
    "v_mfma_f32_16x16x32_bf16 v[96:99], v[168:171], v[168:171], v[96:99]\n\t"
    "v_fmac_f32 v120, v113, v77\n\t"
    "v_fmac_f32 v121, v114, v78\n\t"
    "buffer_load_dwordx4 v[140:143], v153, s[96:99], s66 offen offset:12\n\t"
    "v_mfma_f32_16x16x32_bf16 v[100:103], v[164:167], v[172:175], 0\n\t"
    "v_fmac_f32 v120, v115, v79\n\t"
    "v_fmac_f32 v121, v116, v80\n\t"
    "v_mfma_f32_16x16x32_bf16 v[100:103], v[168:171], v[176:179], v[100:103]\n\t"
    "v_fmac_f32 v120, v117, v81\n\t"
    "v_fmac_f32 v121, v118, v82\n\t"
    "s_waitcnt vmcnt(3)\n\t"
    "s_cmp_ge_u32 s55, s61\n\t"
    "s_cbranch_scc1 L_req1_%=\n\t"
    "L_back1_%=:\n\t"
    "v_mfma_f32_16x16x32_bf16 v[104:107], v[172:175], v[172:175], 0\n\t"
    "v_mad_u32_u24 v128, v128, s68, v152\n\t"
    "v_and_b32 v128, -16, v128\n\t"
    "v_mfma_f32_16x16x32_bf16 v[104:107], v[176:179], v[176:179], v[104:107]\n\t"
    "v_fmac_f32 v120, v119, v83\n\t"
    "v_add_f32 v154, v120, v121\n\t"
    "ds_write_b32 v147, v154\n\t"
    "v_mfma_f32_16x16x32_bf16 v[108:111], v[212:215], v[164:167], 0\n\t"
    "v_fma_f32 v120, v92, v84, v145\n\t"
    "v_mul_f32 v121, v96, v0\n\t"
    "v_mfma_f32_16x16x32_bf16 v[108:111], v[216:219], v[168:171], v[108:111]\n\t"
    "v_fmac_f32 v120, v97, v1\n\t"
    "v_fmac_f32 v121, v98, v2\n\t"
    "s_waitcnt lgkmcnt(8)\n\t"
    "v_mfma_f32_16x16x32_bf16 v[112:115], v[164:167], v[180:183], 0\n\t"
    "v_fmac_f32 v120, v99, v3\n\t"
    "v_fmac_f32 v121, v100, v4\n\t"
    "v_mfma_f32_16x16x32_bf16 v[112:115], v[168:171], v[184:187], v[112:115]\n\t"
    "v_fmac_f32 v120, v101, v5\n\t"
    "v_fmac_f32 v121, v102, v6\n\t"
    "v_mfma_f32_16x16x32_bf16 v[116:119], v[172:175], v[180:183], 0\n\t"
    "v_fmac_f32 v120, v103, v7\n\t"
    "v_fmac_f32 v121, v104, v8\n\t"
    "v_mfma_f32_16x16x32_bf16 v[116:119], v[176:179], v[184:187], v[116:119]\n\t"
    "v_fmac_f32 v120, v105, v9\n\t"
    "v_fmac_f32 v121, v106, v10\n\t"
    "ds_read_b128 v[156:159], v128 offset:0\n\t"
    "ds_read_b128 v[160:163], v128 offset:64\n\t"
    "v_mfma_f32_16x16x32_bf16 v[92:95], v[180:183], v[180:183], 0\n\t"
    "v_fmac_f32 v120, v107, v11\n\t"
    "v_mad_u32_u24 v129, v129, s68, v151\n\t"
    "v_mfma_f32_16x16x32_bf16 v[92:95], v[184:187], v[184:187], v[92:95]\n\t"
    "v_and_b32 v129, -16, v129\n\t"
    "v_fmac_f32 v121, v108, v85\n\t"
    "v_mfma_f32_16x16x32_bf16 v[96:99], v[212:215], v[172:175], 0\n\t"
    "v_fmac_f32 v120, v112, v12\n\t"
    "v_fmac_f32 v121, v113, v13\n\t"
    "v_mfma_f32_16x16x32_bf16 v[96:99], v[216:219], v[176:179], v[96:99]\n\t"
    "v_fmac_f32 v120, v114, v14\n\t"
    "v_fmac_f32 v121, v115, v15\n\t"
    "s_lshl_b32 s51, s55, 8\n\t"
    "s_add_u32 s51, s51, s50\n\t"
    "s_waitcnt lgkmcnt(8)\n\t"
    "v_mfma_f32_16x16x32_bf16 v[100:103], v[164:167], v[188:191], 0\n\t"
    "v_mad_u32_u24 v130, v130, s68, v151\n\t"
    "v_and_b32 v130, -16, v130\n\t"
    "v_mfma_f32_16x16x32_bf16 v[100:103], v[168:171], v[192:195], v[100:103]\n\t"
    "v_fmac_f32 v120, v116, v16\n\t"
    "v_fmac_f32 v121, v117, v17\n\t"
    "buffer_load_dword v145, v146, %12, s51 offen sc1\n\t"
    "v_mfma_f32_16x16x32_bf16 v[104:107], v[172:175], v[188:191], 0\n\t"
    "v_fmac_f32 v120, v118, v18\n\t"
    "v_fmac_f32 v121, v119, v19\n\t"
    "v_mfma_f32_16x16x32_bf16 v[104:107], v[176:179], v[192:195], v[104:107]\n\t"
    "v_fmac_f32 v120, v92, v20\n\t"
    "v_fmac_f32 v121, v93, v21\n\t"
    "s_waitcnt lgkmcnt(6)\n\t"
    "v_mfma_f32_16x16x32_bf16 v[108:111], v[164:167], v[196:199], 0\n\t"
    "v_fmac_f32 v120, v94, v22\n\t"
    "v_fmac_f32 v121, v95, v23\n\t"
    "v_mfma_f32_16x16x32_bf16 v[108:111], v[168:171], v[200:203], v[108:111]\n\t"
    "v_fmac_f32 v120, v96, v86\n\t"
    "v_mad_u32_u24 v132, v132, s68, v151\n\t"
    "v_mfma_f32_16x16x32_bf16 v[112:115], v[212:215], v[180:183], 0\n\t"
    "v_fmac_f32 v121, v100, v24\n\t"
    "v_fmac_f32 v120, v101, v25\n\t"
    "v_mfma_f32_16x16x32_bf16 v[112:115], v[216:219], v[184:187], v[112:115]\n\t"
    "v_fmac_f32 v121, v102, v26\n\t"
    "v_fmac_f32 v120, v103, v27\n\t"
    "s_waitcnt lgkmcnt(3)\n\t"
    "v_mfma_f32_16x16x32_bf16 v[116:119], v[164:167], v[204:207], 0\n\t"
    "v_readfirstlane_b32 s60, v148\n\t"
    "v_and_b32 v132, -16, v132\n\t"
    "v_mfma_f32_16x16x32_bf16 v[116:119], v[168:171], v[208:211], v[116:119]\n\t"
    "v_fmac_f32 v121, v104, v28\n\t"
    "v_fmac_f32 v120, v105, v29\n\t"
    "ds_read_b128 v[164:167], v129 offset:0\n\t"
    "ds_read_b128 v[168:171], v129 offset:64\n\t"
    "v_mfma_f32_16x16x32_bf16 v[92:95], v[172:175], v[196:199], 0\n\t"
    "v_fmac_f32 v121, v106, v30\n\t"
    "v_fmac_f32 v120, v107, v31\n\t"
    "v_mfma_f32_16x16x32_bf16 v[92:95], v[176:179], v[200:203], v[92:95]\n\t"
    "v_fmac_f32 v121, v108, v32\n\t"
    "v_fmac_f32 v120, v109, v33\n\t"
    "v_mfma_f32_16x16x32_bf16 v[96:99], v[172:175], v[204:207], 0\n\t"
    "v_fmac_f32 v121, v110, v34\n\t"
    "v_fmac_f32 v120, v111, v35\n\t"
    "v_mfma_f32_16x16x32_bf16 v[96:99], v[176:179], v[208:211], v[96:99]\n\t"
    "v_fmac_f32 v121, v112, v87\n\t"
    "v_mad_u32_u24 v133, v133, s68, v151\n\t"
    "ds_read_b128 v[172:175], v130 offset:0\n\t"
    "ds_read_b128 v[176:179], v130 offset:64\n\t"
    "v_mfma_f32_16x16x32_bf16 v[100:103], v[212:215], v[188:191], 0\n\t"
    "v_fmac_f32 v120, v116, v36\n\t"
    "v_fmac_f32 v121, v117, v37\n\t"
    "v_mfma_f32_16x16x32_bf16 v[100:103], v[216:219], v[192:195], v[100:103]\n\t"
    "v_fmac_f32 v120, v118, v38\n\t"
    "v_fmac_f32 v121, v119, v39\n\t"
    "v_mfma_f32_16x16x32_bf16 v[104:107], v[180:183], v[188:191], 0\n\t"
    "v_and_b32 v133, -16, v133\n\t"
    "v_fmac_f32 v120, v92, v40\n\t"
    "v_mfma_f32_16x16x32_bf16 v[104:107], v[184:187], v[192:195], v[104:107]\n\t"
    "v_fmac_f32 v121, v93, v41\n\t"
    "v_fmac_f32 v120, v94, v42\n\t"
    "v_mfma_f32_16x16x32_bf16 v[108:111], v[180:183], v[196:199], 0\n\t"
    "v_fmac_f32 v121, v95, v43\n\t"
    "v_fmac_f32 v120, v96, v44\n\t"
    "v_mfma_f32_16x16x32_bf16 v[108:111], v[184:187], v[200:203], v[108:111]\n\t"
    "v_fmac_f32 v121, v97, v45\n\t"
    "v_fmac_f32 v120, v98, v46\n\t"
    "v_mfma_f32_16x16x32_bf16 v[112:115], v[180:183], v[204:207], 0\n\t"
    "v_fmac_f32 v121, v99, v47\n\t"
    "v_fmac_f32 v120, v100, v88\n\t"
    "v_mfma_f32_16x16x32_bf16 v[112:115], v[184:187], v[208:211], v[112:115]\n\t"
    "v_mad_u32_u24 v134, v134, s68, v151\n\t"
    "v_and_b32 v134, -16, v134\n\t"
    "ds_read_b128 v[180:183], v132 offset:0\n\t"
    "ds_read_b128 v[184:187], v132 offset:64\n\t"
    "v_mfma_f32_16x16x32_bf16 v[116:119], v[212:215], v[196:199], 0\n\t"
    "v_fmac_f32 v121, v104, v48\n\t"
    "v_fmac_f32 v120, v105, v49\n\t"
    "v_mfma_f32_16x16x32_bf16 v[116:119], v[216:219], v[200:203], v[116:119]\n\t"
    "v_fmac_f32 v121, v106, v50\n\t"
    "v_fmac_f32 v120, v107, v51\n\t"
    "v_mfma_f32_16x16x32_bf16 v[92:95], v[188:191], v[188:191], 0\n\t"
    "v_mad_u32_u24 v135, v135, s68, v151\n\t"
    "v_and_b32 v135, -16, v135\n\t"
    "v_mfma_f32_16x16x32_bf16 v[92:95], v[192:195], v[192:195], v[92:95]\n\t"
    "v_fmac_f32 v121, v108, v52\n\t"
    "v_fmac_f32 v120, v109, v53\n\t"
    "v_mfma_f32_16x16x32_bf16 v[96:99], v[188:191], v[196:199], 0\n\t"
    "v_fmac_f32 v121, v110, v54\n\t"
    "v_fmac_f32 v120, v111, v55\n\t"
    "v_mfma_f32_16x16x32_bf16 v[96:99], v[192:195], v[200:203], v[96:99]\n\t"
    "v_fmac_f32 v121, v112, v56\n\t"
    "v_fmac_f32 v120, v113, v57\n\t"
    "v_mfma_f32_16x16x32_bf16 v[100:103], v[188:191], v[204:207], 0\n\t"
    "v_fmac_f32 v121, v114, v58\n\t"
    "v_fmac_f32 v120, v115, v59\n\t"
    "v_mfma_f32_16x16x32_bf16 v[100:103], v[192:195], v[208:211], v[100:103]\n\t"
    "v_fmac_f32 v121, v116, v89\n\t"
    "ds_read_b128 v[188:191], v133 offset:0\n\t"
    "ds_read_b128 v[192:195], v133 offset:64\n\t"
    "v_mfma_f32_16x16x32_bf16 v[104:107], v[212:215], v[204:207], 0\n\t"
    "v_fmac_f32 v120, v92, v60\n\t"
    "v_fmac_f32 v121, v93, v61\n\t"
    "v_mfma_f32_16x16x32_bf16 v[104:107], v[216:219], v[208:211], v[104:107]\n\t"
    "v_fmac_f32 v120, v94, v62\n\t"
    "v_fmac_f32 v121, v95, v63\n\t"
    "v_mfma_f32_16x16x32_bf16 v[108:111], v[196:199], v[196:199], 0\n\t"
    "v_fmac_f32 v120, v96, v64\n\t"
    "v_fmac_f32 v121, v97, v65\n\t"
    "v_mfma_f32_16x16x32_bf16 v[108:111], v[200:203], v[200:203], v[108:111]\n\t"
    "v_fmac_f32 v120, v98, v66\n\t"
    "v_fmac_f32 v121, v99, v67\n\t"
    "v_mfma_f32_16x16x32_bf16 v[112:115], v[196:199], v[204:207], 0\n\t"
    "v_fmac_f32 v120, v100, v68\n\t"
    "v_fmac_f32 v121, v101, v69\n\t"
    "v_mfma_f32_16x16x32_bf16 v[112:115], v[200:203], v[208:211], v[112:115]\n\t"
    "v_fmac_f32 v120, v102, v70\n\t"
    "v_fmac_f32 v121, v103, v71\n\t"
    "ds_read_b128 v[196:199], v134 offset:0\n\t"
    "ds_read_b128 v[200:203], v134 offset:64\n\t"
    "v_mfma_f32_16x16x32_bf16 v[116:119], v[204:207], v[204:207], 0\n\t"
    "v_fmac_f32 v120, v104, v90\n\t"
    "v_mfma_f32_16x16x32_bf16 v[116:119], v[208:211], v[208:211], v[116:119]\n\t"
    "ds_read_b128 v[204:207], v135 offset:0\n\t"
    "ds_read_b128 v[208:211], v135 offset:64\n\t"
    "s_mov_b32 s52, s53\n\t"
    "s_mov_b32 s53, s54\n\t"
    "s_mov_b32 s54, s55\n\t"
    "s_mov_b32 s55, s60\n\t"
    "s_cmp_lt_u32 s54, s64\n\t"
    "s_cbranch_scc1 L_pr1_%=\n\t"
    "s_setprio 0\n\t"
    "L_pr1_%=:\n\t"
    "s_cmp_lt_u32 s53, s64\n\t"
    "s_cbranch_scc1 L_pnn_%=\n\t"
    "L_done_%=:\n\t"
    "s_mul_i32 s69, s52, 272\n\t"
    "s_add_u32 s69, s69, 130048\n\t"
    "v_add_u32 v147, s69, v146\n\t"
    "v_fmac_f32 v121, v108, v72\n\t"
    "v_fmac_f32 v120, v109, v73\n\t"
    "v_fmac_f32 v121, v110, v74\n\t"
    "v_fmac_f32 v120, v111, v75\n\t"
    "v_fmac_f32 v121, v112, v76\n\t"
    "v_fmac_f32 v120, v113, v77\n\t"
    "v_fmac_f32 v121, v114, v78\n\t"
    "v_fmac_f32 v120, v115, v79\n\t"
    "v_fmac_f32 v121, v116, v80\n\t"
    "v_fmac_f32 v120, v117, v81\n\t"
    "v_fmac_f32 v121, v118, v82\n\t"
    "v_fmac_f32 v120, v119, v83\n\t"
    "v_add_f32 v154, v120, v121\n\t"
    "ds_write_b32 v147, v154\n\t"
    "v_lshrrev_b32 v147, 2, v146\n\t"
    "s_mov_b64 s[92:93], %17\n\t"
    "s_and_b32 s93, s93, 0xffff\n\t"
    "s_mov_b32 s94, 32768\n\t"
    "s_mov_b32 s95, 0x20000\n\t"
    "s_lshl_b32 s44, s37, 1\n\t"
    "v_and_b32 v155, 15, v147\n\t"
    "v_lshl_add_u32 v155, %16, 4, v155\n\t"
    "v_lshrrev_b32 v154, 4, v147\n\t"
    "v_mul_u32_u24 v122, 272, v155\n\t"
    "v_lshl_add_u32 v122, v154, 6, v122\n\t"
    "v_add_u32 v122, 130048, v122\n\t"
    "v_mul_u32_u24 v154, 2688, v154\n\t"
    "v_lshl_add_u32 v154, v155, 2, v154\n\t"
    "v_add_u32 v154, 152896, v154\n\t"
    "v_cmp_le_u32 vcc, 48, v147\n\t"
    "v_cmp_gt_u32 s[46:47], s38, v155\n\t"
    "v_lshlrev_b32 v155, 1, v155\n\t"
    "s_and_b64 vcc, vcc, s[46:47]\n\t"
    "s_waitcnt lgkmcnt(0)\n\t"
    "s_barrier\n\t"
    "s_cbranch_vccz L_fin_%=\n\t"
    "ds_read_b128 v[92:95], v122 offset:0\n\t"
    "ds_read_b128 v[96:99], v122 offset:16\n\t"
    "ds_read_b128 v[100:103], v122 offset:32\n\t"
    "ds_read_b128 v[104:107], v122 offset:48\n\t"
    "ds_read2_b32 v[108:109], v154 offset0:0 offset1:84\n\t"
    "ds_read2_b32 v[110:111], v154 offset0:168 offset1:252\n\t"
    "v_add_u32 v154, 1344, v154\n\t"
    "ds_read2_b32 v[112:113], v154 offset0:0 offset1:84\n\t"
    "ds_read2_b32 v[114:115], v154 offset0:168 offset1:252\n\t"
    "s_waitcnt lgkmcnt(6)\n\t"
    "v_add_f32 v92, v92, v96\n\t"
    "v_add_f32 v93, v93, v97\n\t"
    "v_add_f32 v94, v94, v98\n\t"
    "v_add_f32 v95, v95, v99\n\t"
    "s_waitcnt lgkmcnt(5)\n\t"
    "v_add_f32 v92, v92, v100\n\t"
    "v_add_f32 v93, v93, v101\n\t"
    "v_add_f32 v94, v94, v102\n\t"
    "v_add_f32 v95, v95, v103\n\t"
    "s_waitcnt lgkmcnt(4)\n\t"
    "v_add_f32 v92, v92, v104\n\t"
    "v_add_f32 v93, v93, v105\n\t"
    "v_add_f32 v94, v94, v106\n\t"
    "v_add_f32 v95, v95, v107\n\t"
    "s_waitcnt lgkmcnt(2)\n\t"
    "v_add_f32 v92, v92, v108\n\t"
    "v_add_f32 v93, v93, v109\n\t"
    "v_add_f32 v94, v94, v110\n\t"
    "v_add_f32 v95, v95, v111\n\t"
    "s_waitcnt lgkmcnt(0)\n\t"
    "v_add_f32 v92, v92, v112\n\t"
    "v_add_f32 v93, v93, v113\n\t"
    "v_add_f32 v94, v94, v114\n\t"
    "v_add_f32 v95, v95, v115\n\t"
    "v_add_f32 v92, v92, v93\n\t"
    "v_add_f32 v94, v94, v95\n\t"
    "v_add_f32 v92, v92, v94\n\t"
    "v_mov_b32 v93, v92\n\t"
    "s_nop 1\n\t"
    "v_permlane16_swap_b32 v93, v92\n\t"
    "s_nop 1\n\t"
    "v_add_f32 v92, v92, v93\n\t"
    "v_mov_b32 v93, v92\n\t"
    "s_nop 1\n\t"
    "v_permlane32_swap_b32 v93, v92\n\t"
    "s_nop 1\n\t"
    "v_add_f32 v92, v92, v93\n\t"
    "v_add_f32 v92, s39, v92\n\t"
    "v_cvt_pk_bf16_f32 v92, v92, v92\n\t"
    "s_mov_b64 exec, vcc\n\t"
    "buffer_store_short v92, v155, s[92:95], s44 offen\n\t"
    "s_mov_b64 exec, -1\n\t"
    "s_branch L_fin_%=\n\t"
    "L_req0_%=:\n\t"
    "s_cmp_lg_u32 s61, 0\n\t"
    "s_cbranch_scc1 L_req0b_%=\n\t"
    "s_bfe_u32 s65, s84, 0xc0002\n\t"
    "s_add_u32 s65, s65, s36\n\t"
    "buffer_store_dword v236, v125, %12, s65 offen offset:0 sc1\n\t"
    "buffer_store_dword v237, v125, %12, s65 offen offset:256 sc1\n\t"
    "s_bfe_u32 s65, s85, 0xc0002\n\t"
    "s_add_u32 s65, s65, s36\n\t"
    "buffer_store_dword v238, v125, %12, s65 offen offset:0 sc1\n\t"
    "buffer_store_dword v239, v125, %12, s65 offen offset:256 sc1\n\t"
    "s_bfe_u32 s65, s86, 0xc0002\n\t"
    "s_add_u32 s65, s65, s36\n\t"
    "buffer_store_dword v240, v125, %12, s65 offen offset:0 sc1\n\t"
    "buffer_store_dword v241, v125, %12, s65 offen offset:256 sc1\n\t"
    "s_bfe_u32 s65, s87, 0xc0002\n\t"
    "s_add_u32 s65, s65, s36\n\t"
    "buffer_store_dword v242, v125, %12, s65 offen offset:0 sc1\n\t"
    "buffer_store_dword v243, v125, %12, s65 offen offset:256 sc1\n\t"
    "s_bfe_u32 s65, s88, 0xc0002\n\t"
    "s_add_u32 s65, s65, s36\n\t"
    "buffer_store_dword v244, v125, %12, s65 offen offset:0 sc1\n\t"
    "buffer_store_dword v245, v125, %12, s65 offen offset:256 sc1\n\t"
    "s_bfe_u32 s65, s89, 0xc0002\n\t"
    "s_add_u32 s65, s65, s36\n\t"
    "buffer_store_dword v246, v125, %12, s65 offen offset:0 sc1\n\t"
    "buffer_store_dword v247, v125, %12, s65 offen offset:256 sc1\n\t"
    "s_bfe_u32 s65, s90, 0xc0002\n\t"
    "s_add_u32 s65, s65, s36\n\t"
    "buffer_store_dword v248, v125, %12, s65 offen offset:0 sc1\n\t"
    "buffer_store_dword v249, v125, %12, s65 offen offset:256 sc1\n\t"
    "s_bfe_u32 s65, s91, 0xc0002\n\t"
    "s_add_u32 s65, s65, s36\n\t"
    "buffer_store_dword v250, v125, %12, s65 offen offset:0 sc1\n\t"
    "buffer_store_dword v251, v125, %12, s65 offen offset:256 sc1\n\t"
    "s_sub_u32 s61, s64, 3\n\t"
    "s_branch L_back0_%=\n\t"
    "L_req0b_%=:\n\t"
    "s_cmp_lg_u32 s61, 1\n\t"
    "s_cbranch_scc1 L_req0c_%=\n\t"
    "v_lshrrev_b32 v154, 16, v122\n\t"
    "v_lshl_add_u32 v154, %16, 2, v154\n\t"
    "v_mul_u32_u24 v154, 336, v154\n\t"
    "v_and_b32 v122, 0xf0, v122\n\t"
    "v_cmp_gt_u32 vcc, 80, v122\n\t"
    "v_add3_u32 v122, v122, v154, s45\n\t"
    "s_mov_b32 s61, -1\n\t"
    "v_add_f32 v124, v220, v224\n\t"
    "v_add_f32 v154, v228, v232\n\t"
    "v_add_f32 v124, v124, v154\n\t"
    "v_add_f32 v125, v221, v225\n\t"
    "v_add_f32 v154, v229, v233\n\t"
    "v_add_f32 v125, v125, v154\n\t"
    "v_add_f32 v126, v222, v226\n\t"
    "v_add_f32 v154, v230, v234\n\t"
    "v_add_f32 v126, v126, v154\n\t"
    "v_add_f32 v127, v223, v227\n\t"
    "v_add_f32 v154, v231, v235\n\t"
    "v_add_f32 v127, v127, v154\n\t"
    "ds_write_b128 v122, v[124:127] offset:0\n\t"
    "s_cmp_le_u32 s38, 64\n\t"
    "s_cbranch_scc1 L_back0_%=\n\t"
    "v_add_f32 v124, v236, v240\n\t"
    "v_add_f32 v154, v244, v248\n\t"
    "v_add_f32 v124, v124, v154\n\t"
    "v_add_f32 v125, v237, v241\n\t"
    "v_add_f32 v154, v245, v249\n\t"
    "v_add_f32 v125, v125, v154\n\t"
    "v_add_f32 v126, v238, v242\n\t"
    "v_add_f32 v154, v246, v250\n\t"
    "v_add_f32 v126, v126, v154\n\t"
    "v_add_f32 v127, v239, v243\n\t"
    "v_add_f32 v154, v247, v251\n\t"
    "v_add_f32 v127, v127, v154\n\t"
    "s_and_saveexec_b64 s[46:47], vcc\n\t"
    "ds_write_b128 v122, v[124:127] offset:256\n\t"
    "s_mov_b64 exec, s[46:47]\n\t"
    "s_branch L_back0_%=\n\t"
    "L_req0c_%=:\n\t"
    "s_lshl_b32 s46, %14, 7\n\t"
    "s_add_u32 s46, s46, 48246784\n\t"
    "s_mov_b32 s47, 1048576\n\t"
    "s_mov_b64 exec, 1\n\t"
    "s_cmp_ge_u32 s55, s64\n\t"
    "s_cbranch_scc1 L_pq0_%=\n\t"
    "buffer_load_dword v123, off, %12, s46 sc1\n\t"
    "s_mov_b64 exec, -1\n\t"
    "s_branch L_back0_%=\n\t"
    "L_pq0_%=:\n\t"
    "v_cmp_eq_u32 vcc, 0, v123\n\t"
    "s_cbranch_vccz L_in0_%=\n\t"
    "L_poll0_%=:\n\t"
    "buffer_load_dword v155, off, %12, s46 sc1\n\t"
    "s_sub_u32 s47, s47, 1\n\t"
    "s_cmp_eq_u32 s47, 0\n\t"
    "s_cbranch_scc1 L_in0_%=\n\t"
    "s_waitcnt vmcnt(0)\n\t"
    "v_cmp_eq_u32 vcc, 0, v155\n\t"
    "s_cbranch_vccz L_in0_%=\n\t"
    "s_sleep 8\n\t"
    "s_branch L_poll0_%=\n\t"
    "L_in0_%=:\n\t"
    "s_mov_b64 exec, -1\n\t"
    "s_lshl_b32 s46, s37, 2\n\t"
    "s_lshl_b32 s47, %16, 20\n\t"
    "s_add_u32 s46, s46, s47\n\t"
    "s_add_u32 s46, s46, 17825792\n\t"
    "v_and_b32 v155, 0x80, v122\n\t"
    "v_lshl_add_u32 v155, v155, 23, v122\n\t"
    "buffer_load_dwordx4 v[220:223], v122, %12, s46 offen sc1\n\t"
    "s_add_u32 s46, s46, 0x40000\n\t"
    "buffer_load_dwordx4 v[224:227], v122, %12, s46 offen sc1\n\t"
    "s_add_u32 s46, s46, 0x40000\n\t"
    "buffer_load_dwordx4 v[228:231], v122, %12, s46 offen sc1\n\t"
    "s_add_u32 s46, s46, 0x40000\n\t"
    "buffer_load_dwordx4 v[232:235], v122, %12, s46 offen sc1\n\t"
    "s_add_u32 s46, s46, 0x40000\n\t"
    "s_mov_b32 s61, 1\n\t"
    "s_cmp_le_u32 s38, 64\n\t"
    "s_cbranch_scc1 L_back0_%=\n\t"
    "s_sub_u32 s46, s46, 0x40000\n\t"
    "buffer_load_dwordx4 v[248:251], v155, %12, s46 offen offset:256 sc1\n\t"
    "s_sub_u32 s46, s46, 0x40000\n\t"
    "buffer_load_dwordx4 v[244:247], v155, %12, s46 offen offset:256 sc1\n\t"
    "s_sub_u32 s46, s46, 0x40000\n\t"
    "buffer_load_dwordx4 v[240:243], v155, %12, s46 offen offset:256 sc1\n\t"
    "s_sub_u32 s46, s46, 0x40000\n\t"
    "buffer_load_dwordx4 v[236:239], v155, %12, s46 offen offset:256 sc1\n\t"
    "s_branch L_back0_%=\n\t"
    "L_req1_%=:\n\t"
    "s_cmp_lg_u32 s61, 1\n\t"
    "s_cbranch_scc1 L_req1c_%=\n\t"
    "v_lshrrev_b32 v154, 16, v122\n\t"
    "v_lshl_add_u32 v154, %16, 2, v154\n\t"
    "v_mul_u32_u24 v154, 336, v154\n\t"
    "v_and_b32 v122, 0xf0, v122\n\t"
    "v_cmp_gt_u32 vcc, 80, v122\n\t"
    "v_add3_u32 v122, v122, v154, s45\n\t"
    "s_mov_b32 s61, -1\n\t"
    "v_add_f32 v124, v220, v224\n\t"
    "v_add_f32 v154, v228, v232\n\t"
    "v_add_f32 v124, v124, v154\n\t"
    "v_add_f32 v125, v221, v225\n\t"
    "v_add_f32 v154, v229, v233\n\t"
    "v_add_f32 v125, v125, v154\n\t"
    "v_add_f32 v126, v222, v226\n\t"
    "v_add_f32 v154, v230, v234\n\t"
    "v_add_f32 v126, v126, v154\n\t"
    "v_add_f32 v127, v223, v227\n\t"
    "v_add_f32 v154, v231, v235\n\t"
    "v_add_f32 v127, v127, v154\n\t"
    "ds_write_b128 v122, v[124:127] offset:0\n\t"
    "s_cmp_le_u32 s38, 64\n\t"
    "s_cbranch_scc1 L_back1_%=\n\t"
    "v_add_f32 v124, v236, v240\n\t"
    "v_add_f32 v154, v244, v248\n\t"
    "v_add_f32 v124, v124, v154\n\t"
    "v_add_f32 v125, v237, v241\n\t"
    "v_add_f32 v154, v245, v249\n\t"
    "v_add_f32 v125, v125, v154\n\t"
    "v_add_f32 v126, v238, v242\n\t"
    "v_add_f32 v154, v246, v250\n\t"
    "v_add_f32 v126, v126, v154\n\t"
    "v_add_f32 v127, v239, v243\n\t"
    "v_add_f32 v154, v247, v251\n\t"
    "v_add_f32 v127, v127, v154\n\t"
    "s_and_saveexec_b64 s[46:47], vcc\n\t"
    "ds_write_b128 v122, v[124:127] offset:256\n\t"
    "s_mov_b64 exec, s[46:47]\n\t"
    "s_branch L_back1_%=\n\t"
    "L_req1c_%=:\n\t"
    "s_lshl_b32 s46, %14, 7\n\t"
    "s_add_u32 s46, s46, 48246784\n\t"
    "s_mov_b32 s47, 1048576\n\t"
    "s_mov_b64 exec, 1\n\t"
    "s_cmp_ge_u32 s55, s64\n\t"
    "s_cbranch_scc1 L_pq1_%=\n\t"
    "buffer_load_dword v123, off, %12, s46 sc1\n\t"
    "s_mov_b64 exec, -1\n\t"
    "s_branch L_back1_%=\n\t"
    "L_pq1_%=:\n\t"
    "v_cmp_eq_u32 vcc, 0, v123\n\t"
    "s_cbranch_vccz L_in1_%=\n\t"
    "L_poll1_%=:\n\t"
    "buffer_load_dword v155, off, %12, s46 sc1\n\t"
    "s_sub_u32 s47, s47, 1\n\t"
    "s_cmp_eq_u32 s47, 0\n\t"
    "s_cbranch_scc1 L_in1_%=\n\t"
    "s_waitcnt vmcnt(0)\n\t"
    "v_cmp_eq_u32 vcc, 0, v155\n\t"
    "s_cbranch_vccz L_in1_%=\n\t"
    "s_sleep 8\n\t"
    "s_branch L_poll1_%=\n\t"
    "L_in1_%=:\n\t"
    "s_mov_b64 exec, -1\n\t"
    "s_lshl_b32 s46, s37, 2\n\t"
    "s_lshl_b32 s47, %16, 20\n\t"
    "s_add_u32 s46, s46, s47\n\t"
    "s_add_u32 s46, s46, 17825792\n\t"
    "v_and_b32 v155, 0x80, v122\n\t"
    "v_lshl_add_u32 v155, v155, 23, v122\n\t"
    "buffer_load_dwordx4 v[220:223], v122, %12, s46 offen sc1\n\t"
    "s_add_u32 s46, s46, 0x40000\n\t"
    "buffer_load_dwordx4 v[224:227], v122, %12, s46 offen sc1\n\t"
    "s_add_u32 s46, s46, 0x40000\n\t"
    "buffer_load_dwordx4 v[228:231], v122, %12, s46 offen sc1\n\t"
    "s_add_u32 s46, s46, 0x40000\n\t"
    "buffer_load_dwordx4 v[232:235], v122, %12, s46 offen sc1\n\t"
    "s_add_u32 s46, s46, 0x40000\n\t"
    "s_mov_b32 s61, 1\n\t"
    "s_cmp_le_u32 s38, 64\n\t"
    "s_cbranch_scc1 L_back1_%=\n\t"
    "s_sub_u32 s46, s46, 0x40000\n\t"
    "buffer_load_dwordx4 v[248:251], v155, %12, s46 offen offset:256 sc1\n\t"
    "s_sub_u32 s46, s46, 0x40000\n\t"
    "buffer_load_dwordx4 v[244:247], v155, %12, s46 offen offset:256 sc1\n\t"
    "s_sub_u32 s46, s46, 0x40000\n\t"
    "buffer_load_dwordx4 v[240:243], v155, %12, s46 offen offset:256 sc1\n\t"
    "s_sub_u32 s46, s46, 0x40000\n\t"
    "buffer_load_dwordx4 v[236:239], v155, %12, s46 offen offset:256 sc1\n\t"
    "s_branch L_back1_%=\n\t"
    "L_sig_%=:\n\t"
    "v_subrev_u32 v155, s49, v149\n\t"
    "v_add_u32 v155, 1024, v155\n\t"
    "s_waitcnt vmcnt(0)\n\t"
    "ds_add_rtn_u32 v148, v155, v150\n\t"
    "s_mov_b32 s45, 152896\n\t"
    "s_mov_b32 s46, 48234496\n\t"
    "v_mov_b32 v155, 1\n\t"
    "s_waitcnt lgkmcnt(0)\n\t"
    "v_readfirstlane_b32 s47, v148\n\t"
    "s_nop 0\n\t"
    "s_cmp_lg_u32 s47, 7\n\t"
    "s_cbranch_scc1 L_sigb_%=\n\t"
    "s_mov_b64 exec, 1\n\t"
    "buffer_atomic_add v155, off, %12, s46 sc0 sc1\n\t"
    "s_mov_b64 exec, -1\n\t"
    "s_waitcnt vmcnt(0)\n\t"
    "v_readfirstlane_b32 s47, v155\n\t"
    "v_lshlrev_b32 v155, 5, v146\n\t"
    "s_cmp_lg_u32 s47, 255\n\t"
    "s_cbranch_scc1 L_sigb_%=\n\t"
    "s_mov_b32 s46, 48246784\n\t"
    "buffer_store_dword v150, v155, %12, s46 offen sc1\n\t"
    "s_add_u32 s46, s46, 0x2000\n\t"
    "buffer_store_dword v150, v155, %12, s46 offen sc1\n\t"
    "s_add_u32 s46, s46, 0x2000\n\t"
    "buffer_store_dword v150, v155, %12, s46 offen sc1\n\t"
    "s_add_u32 s46, s46, 0x2000\n\t"
    "buffer_store_dword v150, v155, %12, s46 offen sc1\n\t"
    "s_add_u32 s46, s46, 0x2000\n\t"
    "s_branch L_sigb_%=\n\t"
    "L_fin_%=:\n\t"
    "s_branch L_pend_%=\n\t"
    "L_pub_%=:\n\t"
    "s_memrealtime s[46:47]\n\t"
    "s_lshl_b32 s49, %14, 2\n\t"
    "s_add_u32 s49, s49, 48244736\n\t"
    "s_waitcnt lgkmcnt(0)\n\t"
    "s_sub_u32 s47, s46, s44\n\t"
    "s_mul_i32 s47, s47, 320\n\t"
    "s_lshr_b32 s47, s47, 8\n\t"
    "s_add_u32 s46, s46, s47\n\t"
    "s_or_b32 s46, s46, 1\n\t"
    "v_mov_b32 v83, s46\n\t"
    "s_mov_b64 exec, 1\n\t"
    "buffer_store_dword v83, off, %12, s49 sc1\n\t"
    "s_mov_b64 exec, -1\n\t"
    "s_branch L_pubb_%=\n\t"
    "L_part0_%=:\n\t"
    "s_mov_b32 s47, 0\n\t"
    "s_call_b64 s[98:99], L_bld_%=\n\t"
    "s_branch L_partb0_%=\n\t"
    "L_part1_%=:\n\t"
    "s_mov_b32 s47, 1048576\n\t"
    "s_waitcnt vmcnt(8)\n\t"
    "s_call_b64 s[98:99], L_bchk_%=\n\t"
    "s_branch L_partb1_%=\n\t"
    "L_bchk_%=:\n\t"
    "v_min_u32 v132, v128, v129\n\t"
    "v_min3_u32 v132, v132, v130, v131\n\t"
    "v_cmp_eq_u32 vcc, 0, v132\n\t"
    "s_cbranch_vccz L_bok_%=\n\t"
    "L_bld_%=:\n\t"
    "s_mov_b32 s46, 48244736\n\t"
    "buffer_load_dwordx4 v[128:131], v127, %12, s46 offen sc1\n\t"
    "s_cmp_eq_u32 s47, 0\n\t"
    "s_cbranch_scc1 L_bret_%=\n\t"
    "s_sub_u32 s47, s47, 1\n\t"
    "s_sleep 16\n\t"
    "s_waitcnt vmcnt(0)\n\t"
    "s_branch L_bchk_%=\n\t"
    "L_bok_%=:\n\t"
    "v_readfirstlane_b32 s46, v128\n\t"
    "v_mov_b32 v138, -4096\n\t"
    "v_mov_b32 v139, 4096\n\t"
    "v_subrev_u32 v128, s46, v128\n\t"
    "v_subrev_u32 v129, s46, v129\n\t"
    "v_subrev_u32 v130, s46, v130\n\t"
    "v_subrev_u32 v131, s46, v131\n\t"
    "v_writelane_b32 v128, 50, 0\n\t"
    "v_add3_u32 v132, v128, v129, v130\n\t"
    "v_add_u32 v132, v132, v131\n\t"
    "s_nop 1\n\t"
    "v_add_u32_dpp v132, v132, v132 row_shr:1 row_mask:0xf bank_mask:0xf\n\t"
    "s_nop 1\n\t"
    "v_add_u32_dpp v132, v132, v132 row_shr:2 row_mask:0xf bank_mask:0xf\n\t"
    "s_nop 1\n\t"
    "v_add_u32_dpp v132, v132, v132 row_shr:4 row_mask:0xf bank_mask:0xf\n\t"
    "s_nop 1\n\t"
    "v_add_u32_dpp v132, v132, v132 row_shr:8 row_mask:0xf bank_mask:0xf\n\t"
    "s_nop 1\n\t"
    "v_add_u32_dpp v132, v132, v132 row_bcast:15 row_mask:0xa bank_mask:0xf\n\t"
    "s_nop 1\n\t"
    "v_add_u32_dpp v132, v132, v132 row_bcast:31 row_mask:0xc bank_mask:0xf\n\t"
    "s_nop 0\n\t"
    "v_readlane_b32 s46, v132, 63\n\t"
    "s_movk_i32 s47, 20\n\t"
    "v_mov_b32 v133, -8192\n\t"
    "v_mov_b32 v134, 5120\n\t"
    "s_ashr_i32 s46, s46, 8\n\t"
    "v_sub_u32 v128, s46, v128\n\t"
    "v_med3_i32 v128, v128, v138, v139\n\t"
    "v_mul_i32_i24 v128, s47, v128\n\t"
    "v_med3_i32 v128, v128, v133, v134\n\t"
    "v_and_b32 v140, 0x3ff, v128\n\t"
    "v_ashrrev_i32 v128, 10, v128\n\t"
    "v_sub_u32 v129, s46, v129\n\t"
    "v_med3_i32 v129, v129, v138, v139\n\t"
    "v_mul_i32_i24 v129, s47, v129\n\t"
    "v_med3_i32 v129, v129, v133, v134\n\t"
    "v_and_b32 v141, 0x3ff, v129\n\t"
    "v_ashrrev_i32 v129, 10, v129\n\t"
    "v_sub_u32 v130, s46, v130\n\t"
    "v_med3_i32 v130, v130, v138, v139\n\t"
    "v_mul_i32_i24 v130, s47, v130\n\t"
    "v_med3_i32 v130, v130, v133, v134\n\t"
    "v_and_b32 v142, 0x3ff, v130\n\t"
    "v_ashrrev_i32 v130, 10, v130\n\t"
    "v_sub_u32 v131, s46, v131\n\t"
    "v_med3_i32 v131, v131, v138, v139\n\t"
    "v_mul_i32_i24 v131, s47, v131\n\t"
    "v_med3_i32 v131, v131, v133, v134\n\t"
    "v_and_b32 v143, 0x3ff, v131\n\t"
    "v_ashrrev_i32 v131, 10, v131\n\t"
    "v_add3_u32 v132, v128, v129, v130\n\t"
    "v_add_u32 v132, v132, v131\n\t"
    "s_nop 1\n\t"
    "v_add_u32_dpp v132, v132, v132 row_shr:1 row_mask:0xf bank_mask:0xf\n\t"
    "s_nop 1\n\t"
    "v_add_u32_dpp v132, v132, v132 row_shr:2 row_mask:0xf bank_mask:0xf\n\t"
    "s_nop 1\n\t"
    "v_add_u32_dpp v132, v132, v132 row_shr:4 row_mask:0xf bank_mask:0xf\n\t"
    "s_nop 1\n\t"
    "v_add_u32_dpp v132, v132, v132 row_shr:8 row_mask:0xf bank_mask:0xf\n\t"
    "s_nop 1\n\t"
    "v_add_u32_dpp v132, v132, v132 row_bcast:15 row_mask:0xa bank_mask:0xf\n\t"
    "s_nop 1\n\t"
    "v_add_u32_dpp v132, v132, v132 row_bcast:31 row_mask:0xc bank_mask:0xf\n\t"
    "s_nop 0\n\t"
    "v_readlane_b32 s46, v132, 63\n\t"
    "s_movk_i32 s47, 0x400\n\t"
    "s_sub_u32 s48, s47, 512\n\t"
    "s_mov_b32 s49, s46\n\t"
    "v_cmp_le_u32 vcc, s48, v140\n\t"
    "s_bcnt1_i32_b64 s44, vcc\n\t"
    "s_add_u32 s49, s49, s44\n\t"
    "v_cmp_le_u32 vcc, s48, v141\n\t"
    "s_bcnt1_i32_b64 s44, vcc\n\t"
    "s_add_u32 s49, s49, s44\n\t"
    "v_cmp_le_u32 vcc, s48, v142\n\t"
    "s_bcnt1_i32_b64 s44, vcc\n\t"
    "s_add_u32 s49, s49, s44\n\t"
    "v_cmp_le_u32 vcc, s48, v143\n\t"
    "s_bcnt1_i32_b64 s44, vcc\n\t"
    "s_add_u32 s49, s49, s44\n\t"
    "s_cmp_lt_i32 s49, 0\n\t"
    "s_cselect_b32 s47, s48, s47\n\t"
    "s_sub_u32 s48, s47, 256\n\t"
    "s_mov_b32 s49, s46\n\t"
    "v_cmp_le_u32 vcc, s48, v140\n\t"
    "s_bcnt1_i32_b64 s44, vcc\n\t"
    "s_add_u32 s49, s49, s44\n\t"
    "v_cmp_le_u32 vcc, s48, v141\n\t"
    "s_bcnt1_i32_b64 s44, vcc\n\t"
    "s_add_u32 s49, s49, s44\n\t"
    "v_cmp_le_u32 vcc, s48, v142\n\t"
    "s_bcnt1_i32_b64 s44, vcc\n\t"
    "s_add_u32 s49, s49, s44\n\t"
    "v_cmp_le_u32 vcc, s48, v143\n\t"
    "s_bcnt1_i32_b64 s44, vcc\n\t"
    "s_add_u32 s49, s49, s44\n\t"
    "s_cmp_lt_i32 s49, 0\n\t"
    "s_cselect_b32 s47, s48, s47\n\t"
    "s_sub_u32 s48, s47, 128\n\t"
    "s_mov_b32 s49, s46\n\t"
    "v_cmp_le_u32 vcc, s48, v140\n\t"
    "s_bcnt1_i32_b64 s44, vcc\n\t"
    "s_add_u32 s49, s49, s44\n\t"
    "v_cmp_le_u32 vcc, s48, v141\n\t"
    "s_bcnt1_i32_b64 s44, vcc\n\t"
    "s_add_u32 s49, s49, s44\n\t"
    "v_cmp_le_u32 vcc, s48, v142\n\t"
    "s_bcnt1_i32_b64 s44, vcc\n\t"
    "s_add_u32 s49, s49, s44\n\t"
    "v_cmp_le_u32 vcc, s48, v143\n\t"
    "s_bcnt1_i32_b64 s44, vcc\n\t"
    "s_add_u32 s49, s49, s44\n\t"
    "s_cmp_lt_i32 s49, 0\n\t"
    "s_cselect_b32 s47, s48, s47\n\t"
    "s_sub_u32 s48, s47, 64\n\t"
    "s_mov_b32 s49, s46\n\t"
    "v_cmp_le_u32 vcc, s48, v140\n\t"
    "s_bcnt1_i32_b64 s44, vcc\n\t"
    "s_add_u32 s49, s49, s44\n\t"
    "v_cmp_le_u32 vcc, s48, v141\n\t"
    "s_bcnt1_i32_b64 s44, vcc\n\t"
    "s_add_u32 s49, s49, s44\n\t"
    "v_cmp_le_u32 vcc, s48, v142\n\t"
    "s_bcnt1_i32_b64 s44, vcc\n\t"
    "s_add_u32 s49, s49, s44\n\t"
    "v_cmp_le_u32 vcc, s48, v143\n\t"
    "s_bcnt1_i32_b64 s44, vcc\n\t"
    "s_add_u32 s49, s49, s44\n\t"
    "s_cmp_lt_i32 s49, 0\n\t"
    "s_cselect_b32 s47, s48, s47\n\t"
    "s_sub_u32 s48, s47, 32\n\t"
    "s_mov_b32 s49, s46\n\t"
    "v_cmp_le_u32 vcc, s48, v140\n\t"
    "s_bcnt1_i32_b64 s44, vcc\n\t"
    "s_add_u32 s49, s49, s44\n\t"
    "v_cmp_le_u32 vcc, s48, v141\n\t"
    "s_bcnt1_i32_b64 s44, vcc\n\t"
    "s_add_u32 s49, s49, s44\n\t"
    "v_cmp_le_u32 vcc, s48, v142\n\t"
    "s_bcnt1_i32_b64 s44, vcc\n\t"
    "s_add_u32 s49, s49, s44\n\t"
    "v_cmp_le_u32 vcc, s48, v143\n\t"
    "s_bcnt1_i32_b64 s44, vcc\n\t"
    "s_add_u32 s49, s49, s44\n\t"
    "s_cmp_lt_i32 s49, 0\n\t"
    "s_cselect_b32 s47, s48, s47\n\t"
    "s_sub_u32 s47, s47, 32\n\t"
    "s_max_u32 s47, s47, 1\n\t"
    "v_cmp_le_u32 vcc, s47, v140\n\t"
    "s_nop 1\n\t"
    "v_addc_co_u32 v128, vcc, 0, v128, vcc\n\t"
    "v_cmp_le_u32 vcc, s47, v141\n\t"
    "s_nop 1\n\t"
    "v_addc_co_u32 v129, vcc, 0, v129, vcc\n\t"
    "v_cmp_le_u32 vcc, s47, v142\n\t"
    "s_nop 1\n\t"
    "v_addc_co_u32 v130, vcc, 0, v130, vcc\n\t"
    "v_cmp_le_u32 vcc, s47, v143\n\t"
    "s_nop 1\n\t"
    "v_addc_co_u32 v131, vcc, 0, v131, vcc\n\t"
    "v_add3_u32 v132, v128, v129, v130\n\t"
    "v_add_u32 v132, v132, v131\n\t"
    "s_nop 1\n\t"
    "v_add_u32_dpp v132, v132, v132 row_shr:1 row_mask:0xf bank_mask:0xf\n\t"
    "s_nop 1\n\t"
    "v_add_u32_dpp v132, v132, v132 row_shr:2 row_mask:0xf bank_mask:0xf\n\t"
    "s_nop 1\n\t"
    "v_add_u32_dpp v132, v132, v132 row_shr:4 row_mask:0xf bank_mask:0xf\n\t"
    "s_nop 1\n\t"
    "v_add_u32_dpp v132, v132, v132 row_shr:8 row_mask:0xf bank_mask:0xf\n\t"
    "s_nop 1\n\t"
    "v_add_u32_dpp v132, v132, v132 row_bcast:15 row_mask:0xa bank_mask:0xf\n\t"
    "s_nop 1\n\t"
    "v_add_u32_dpp v132, v132, v132 row_bcast:31 row_mask:0xc bank_mask:0xf\n\t"
    "s_nop 0\n\t"
    "v_readlane_b32 s46, v132, 63\n\t"
    "v_lshrrev_b32 v135, 2, v127\n\t"
    "s_cmp_le_u32 s46, 256\n\t"
    "s_cselect_b32 s47, -1, 0\n\t"
    "v_add_u32 v133, 4, v135\n\t"
    "v_mul_u32_u24 v134, s46, v133\n\t"
    "v_lshrrev_b32 v134, 8, v134\n\t"
    "v_sub_u32 v134, v132, v134\n\t"
    "v_and_b32 v134, s47, v134\n\t"
    "v_lshl_add_u32 v136, v133, 4, v134\n\t"
    "v_sub_u32 v132, v132, v131\n\t"
    "v_add_u32 v133, 3, v135\n\t"
    "v_mul_u32_u24 v134, s46, v133\n\t"
    "v_lshrrev_b32 v134, 8, v134\n\t"
    "v_sub_u32 v134, v132, v134\n\t"
    "v_and_b32 v134, s47, v134\n\t"
    "v_lshl_add_u32 v137, v133, 4, v134\n\t"
    "v_lshl_or_b32 v143, v136, 16, v137\n\t"
    "v_sub_u32 v132, v132, v130\n\t"
    "v_add_u32 v133, 2, v135\n\t"
    "v_mul_u32_u24 v134, s46, v133\n\t"
    "v_lshrrev_b32 v134, 8, v134\n\t"
    "v_sub_u32 v134, v132, v134\n\t"
    "v_and_b32 v134, s47, v134\n\t"
    "v_lshl_add_u32 v136, v133, 4, v134\n\t"
    "v_lshl_or_b32 v142, v137, 16, v136\n\t"
    "v_sub_u32 v132, v132, v129\n\t"
    "v_add_u32 v133, 1, v135\n\t"
    "v_mul_u32_u24 v134, s46, v133\n\t"
    "v_lshrrev_b32 v134, 8, v134\n\t"
    "v_sub_u32 v134, v132, v134\n\t"
    "v_and_b32 v134, s47, v134\n\t"
    "v_lshl_add_u32 v137, v133, 4, v134\n\t"
    "v_lshl_or_b32 v141, v136, 16, v137\n\t"
    "v_sub_u32 v132, v132, v128\n\t"
    "v_add_u32 v133, 0, v135\n\t"
    "v_mul_u32_u24 v134, s46, v133\n\t"
    "v_lshrrev_b32 v134, 8, v134\n\t"
    "v_sub_u32 v134, v132, v134\n\t"
    "v_and_b32 v134, s47, v134\n\t"
    "v_lshl_add_u32 v136, v133, 4, v134\n\t"
    "v_lshl_or_b32 v140, v137, 16, v136\n\t"
    "s_mov_b32 s46, 48245760\n\t"
    "buffer_store_dwordx4 v[140:143], v127, %12, s46 offen sc1\n\t"
    "s_mov_b32 s97, 1\n\t"
    "L_bret_%=:\n\t"
    "s_setpc_b64 s[98:99]\n\t"
    "L_rw_%=:\n\t"
    "s_mov_b32 s47, 1048576\n\t"
    "L_rw2_%=:\n\t"
    "s_sleep 16\n\t"
    "s_lshl_b32 s46, %14, 2\n\t"
    "s_add_u32 s46, s46, 48245760\n\t"
    "s_mov_b64 exec, 1\n\t"
    "buffer_load_dword v144, off, %12, s46 sc1\n\t"
    "s_mov_b64 exec, -1\n\t"
    "s_waitcnt vmcnt(0)\n\t"
    "v_readfirstlane_b32 s38, v144\n\t"
    "s_sub_u32 s47, s47, 1\n\t"
    "s_cmp_eq_u32 s47, 0\n\t"
    "s_cselect_b32 s38, 0x100000, s38\n\t"
    "s_cmp_eq_u32 s38, 0\n\t"
    "s_cbranch_scc1 L_rw2_%=\n\t"
    "s_branch L_rok_%=\n\t"
    "L_xw0_%=:\n\t"
    "s_mov_b32 s68, 1048576\n\t"
    "L_xv0_%=:\n\t"
    "s_sleep 4\n\t"
    "s_mov_b64 exec, 1\n\t"
    "buffer_load_dword v164, off, %12, s93 offset:0 sc1\n\t"
    "s_mov_b64 exec, -1\n\t"
    "s_sub_u32 s68, s68, 1\n\t"
    "s_cmp_eq_u32 s68, 0\n\t"
    "s_cbranch_scc1 L_xwb0_%=\n\t"
    "s_waitcnt vmcnt(0)\n\t"
    "v_readfirstlane_b32 s65, v164\n\t"
    "s_nop 0\n\t"
    "s_cmp_eq_u32 s65, 0\n\t"
    "s_cbranch_scc1 L_xv0_%=\n\t"
    "s_branch L_xwb0_%=\n\t"
    "L_xw1_%=:\n\t"
    "s_mov_b32 s68, 1048576\n\t"
    "L_xv1_%=:\n\t"
    "s_sleep 4\n\t"
    "s_mov_b64 exec, 1\n\t"
    "buffer_load_dword v164, off, %12, s93 offset:128 sc1\n\t"
    "s_mov_b64 exec, -1\n\t"
    "s_sub_u32 s68, s68, 1\n\t"
    "s_cmp_eq_u32 s68, 0\n\t"
    "s_cbranch_scc1 L_xwb1_%=\n\t"
    "s_waitcnt vmcnt(0)\n\t"
    "v_readfirstlane_b32 s65, v164\n\t"
    "s_nop 0\n\t"
    "s_cmp_eq_u32 s65, 0\n\t"
    "s_cbranch_scc1 L_xv1_%=\n\t"
    "s_branch L_xwb1_%=\n\t"
    "L_xw2_%=:\n\t"
    "s_mov_b32 s68, 1048576\n\t"
    "L_xv2_%=:\n\t"
    "s_sleep 4\n\t"
    "s_mov_b64 exec, 1\n\t"
    "buffer_load_dword v164, off, %12, s93 offset:256 sc1\n\t"
    "s_mov_b64 exec, -1\n\t"
    "s_sub_u32 s68, s68, 1\n\t"
    "s_cmp_eq_u32 s68, 0\n\t"
    "s_cbranch_scc1 L_xwb2_%=\n\t"
    "s_waitcnt vmcnt(0)\n\t"
    "v_readfirstlane_b32 s65, v164\n\t"
    "s_nop 0\n\t"
    "s_cmp_eq_u32 s65, 0\n\t"
    "s_cbranch_scc1 L_xv2_%=\n\t"
    "s_branch L_xwb2_%=\n\t"
    "L_xw3_%=:\n\t"
    "s_mov_b32 s68, 1048576\n\t"
    "L_xv3_%=:\n\t"
    "s_sleep 4\n\t"
    "s_mov_b64 exec, 1\n\t"
    "buffer_load_dword v164, off, %12, s93 offset:384 sc1\n\t"
    "s_mov_b64 exec, -1\n\t"
    "s_sub_u32 s68, s68, 1\n\t"
    "s_cmp_eq_u32 s68, 0\n\t"
    "s_cbranch_scc1 L_xwb3_%=\n\t"
    "s_waitcnt vmcnt(0)\n\t"
    "v_readfirstlane_b32 s65, v164\n\t"
    "s_nop 0\n\t"
    "s_cmp_eq_u32 s65, 0\n\t"
    "s_cbranch_scc1 L_xv3_%=\n\t"
    "s_branch L_xwb3_%=\n\t"
    "L_mp2_%=:\n\t"
    "v_lshrrev_b32 v57, 4, v127\n\t"
    "v_add_u32 v57, 32, v57\n\t"
    "s_and_b32 s65, %14, 7\n\t"
    "s_add_u32 s65, s65, 24\n\t"
    "v_writelane_b32 v57, s65, 16\n\t"
    "v_mov_b32 v58, 16\n\t"
    "v_lshlrev_b32 v57, 7, v57\n\t"
    "v_writelane_b32 v58, 32, 16\n\t"
    "s_mov_b32 s65, 48234496\n\t"
    "v_mov_b32 v56, 32\n\t"
    "s_mov_b32 s66, 1048576\n\t"
    "s_mov_b32 s65, 48234496\n\t"
    "s_mov_b64 exec, 0x1ffff\n\t"
    "buffer_load_dword v56, v57, %12, s65 offen sc1\n\t"
    "s_mov_b64 exec, -1\n\t"
    "s_branch L_mp2b_%=\n\t"
    "L_mp3_%=:\n\t"
    "v_cmp_gt_u32 vcc, v58, v56\n\t"
    "s_cbranch_vccz L_mp3b_%=\n\t"
    "s_sub_u32 s66, s66, 1\n\t"
    "s_cmp_eq_u32 s66, 0\n\t"
    "s_cbranch_scc1 L_mp3b_%=\n\t"
    "s_sleep 8\n\t"
    "s_mov_b32 s65, 48234496\n\t"
    "s_mov_b64 exec, 0x1ffff\n\t"
    "buffer_load_dword v56, v57, %12, s65 offen sc1\n\t"
    "s_mov_b64 exec, -1\n\t"
    "s_waitcnt vmcnt(0)\n\t"
    "s_branch L_mp3_%=\n\t"
    "L_pend_%=:\n\t"
    ".Ldcp_end:\n\t"
    :: "s"(s_idx), "s"(p_wp), "s"(p_w1), "n"(0), "n"(0), "n"(0), "s"(pb1), "s"(pw2), "s"(pb2), "s"(b2s),
       "n"(0),
       "s"((unsigned long long)ws & 0xFFFFFFFFFFFFull), "s"(s_ws), "n"(0), "s"(bid), "s"(s_tab), "s"(w), "s"(p_out),
       "s"((unsigned)(unsigned long long)idx), "s"((unsigned)((unsigned long long)idx >> 32) & 0xFFFFu)
    : "memory", "vcc", "scc", "m0", "s36", "s37", "s38", "s39", "s40", "s41", "s42", "s43", "s44", "s45", "s46", "s47", "s48", "s49", "s50", "s51", "s52", "s53", "s54", "s55", "s56", "s57", "s58", "s59", "s60", "s61", "s62", "s63", "s64", "s65", "s66", "s67", "s68", "s69", "s70", "s71", "s72", "s73", "s74", "s75", "s76", "s77", "s78", "s79", "s80", "s81", "s82", "s83", "s84", "s85", "s86", "s87", "s88", "s89", "s90", "s91", "s92", "s93", "s94", "s95", "s96", "s97", "s98", "s99", "v0", "v1", "v2", "v3", "v4", "v5", "v6", "v7", "v8", "v9", "v10", "v11", "v12", "v13", "v14", "v15", "v16", "v17", "v18", "v19", "v20", "v21", "v22", "v23", "v24", "v25", "v26", "v27", "v28", "v29", "v30", "v31", "v32", "v33", "v34", "v35", "v36", "v37", "v38", "v39", "v40", "v41", "v42", "v43", "v44", "v45", "v46", "v47", "v48", "v49", "v50", "v51", "v52", "v53", "v54", "v55", "v56", "v57", "v58", "v59", "v60", "v61", "v62", "v63", "v64", "v65", "v66", "v67", "v68", "v69", "v70", "v71", "v72", "v73", "v74", "v75", "v76", "v77", "v78", "v79", "v80", "v81", "v82", "v83", "v84", "v85", "v86", "v87", "v88", "v89", "v90", "v91", "v92", "v93", "v94", "v95", "v96", "v97", "v98", "v99", "v100", "v101", "v102", "v103", "v104", "v105", "v106", "v107", "v108", "v109", "v110", "v111", "v112", "v113", "v114", "v115", "v116", "v117", "v118", "v119", "v120", "v121", "v122", "v123", "v124", "v125", "v126", "v127", "v128", "v129", "v130", "v131", "v132", "v133", "v134", "v135", "v136", "v137", "v138", "v139", "v140", "v141", "v142", "v143", "v144", "v145", "v146", "v147", "v148", "v149", "v150", "v151", "v152", "v153", "v154", "v155", "v156", "v157", "v158", "v159", "v160", "v161", "v162", "v163", "v164", "v165", "v166", "v167", "v168", "v169", "v170", "v171", "v172", "v173", "v174", "v175", "v176", "v177", "v178", "v179", "v180", "v181", "v182", "v183", "v184", "v185", "v186", "v187", "v188", "v189", "v190", "v191", "v192", "v193", "v194", "v195", "v196", "v197", "v198", "v199", "v200", "v201", "v202", "v203", "v204", "v205", "v206", "v207", "v208", "v209", "v210", "v211", "v212", "v213", "v214", "v215", "v216", "v217", "v218", "v219", "v220", "v221", "v222", "v223", "v224", "v225", "v226", "v227", "v228", "v229", "v230", "v231", "v232", "v233", "v234", "v235", "v236", "v237", "v238", "v239", "v240", "v241", "v242", "v243", "v244", "v245", "v246", "v247", "v248", "v249", "v250", "v251", "v252", "v253", "v254", "v255");
}

extern "C" void kernel_launch(void* const* d_in, const int* in_sizes, int n_in, void* d_out, int out_size, void* d_ws,
                              size_t ws_size, hipStream_t stream) {
  const int* idx = (const int*)d_in[0];
  const unsigned short* table = (const unsigned short*)d_in[1];
  const unsigned short* w_p = (const unsigned short*)d_in[2];
  const unsigned short* w1 = (const unsigned short*)d_in[3];
  const unsigned short* b1 = (const unsigned short*)d_in[4];
  const unsigned short* w2 = (const unsigned short*)d_in[5];
  const unsigned short* b2 = (const unsigned short*)d_in[6];
  char* ws = (char*)d_ws;
  dcp_all<<<dim3(256), dim3(512), 0, stream>>>(idx, table, w_p, w1, b1, w2, b2, ws, (__hip_bfloat16*)d_out);
}
```

```cpp
#include <hip/hip_runtime.h>
#include <hip/hip_bf16.h>
#include <stdint.h>

typedef __attribute__((ext_vector_type(2))) unsigned u32x2;

constexpr int BATCH = 16384, NF = 100, DIM = 128, VOC = 1000, HID = 512;
constexpr size_t NWIN = 13;
constexpr size_t WS_IDX = 0;
constexpr size_t IDX_STREAM = 212992;
constexpr size_t WS_W1F = 3407872;
constexpr size_t W1F_SLAB = 102400;
constexpr size_t WS_TF = 16515072;
constexpr size_t WS_WT = 16777216;
constexpr size_t WS_PART = 17825792;
constexpr size_t WS_P0 = 27262976;
constexpr size_t WS_XCH = 31457280;
constexpr size_t XCH_PITCH = 69632;
constexpr size_t WS_CNT = 49283072;
constexpr size_t WS_CH = 49291264;
constexpr size_t WS_RP = 49292288;
constexpr size_t WS_TS = 49293312;
constexpr size_t WS_RNG = 49294336;
constexpr size_t WS_RDY = 49295360;
constexpr size_t WS_DBG = 49328128;
constexpr size_t WS_END = 49721344;

__device__ __forceinline__ __amdgpu_buffer_rsrc_t mk_srd(const void* p, unsigned bytes) {
  return __builtin_amdgcn_make_buffer_rsrc((void*)p, (short)0, (int)bytes, 0x00020000);
}

__global__ __launch_bounds__(512, 2) void dcp_all(const int* __restrict__ idx, const unsigned short* __restrict__ table,
                                                  const unsigned short* __restrict__ w_p, const unsigned short* __restrict__ w1,
                                                  const unsigned short* __restrict__ b1, const unsigned short* __restrict__ w2,
                                                  const unsigned short* __restrict__ b2, char* __restrict__ ws,
                                                  __hip_bfloat16* __restrict__ out) {
  __shared__ __attribute__((aligned(1024))) char smem[163840];
  const int w = __builtin_amdgcn_readfirstlane(threadIdx.x >> 6) + (int)(uintptr_t)smem;
  const int bid = blockIdx.x;
  asm volatile(
    "s_getpc_b64 s[36:37]\n\t"
    ".Ldcp_pc:\n\t"
    "s_bfe_u32 s38, %0, 0x40003\n\t"
    "s_lshl_b32 s38, s38, 13\n\t"
    "v_and_b32 v122, 63, %1\n\t"
    "v_lshlrev_b32 v122, 7, v122\n\t"
    "v_add_u32 v122, s38, v122\n\t"
    "v_min_u32 v122, .Ldcp_end-.Ldcp_pc-4, v122\n\t"
    "s_cmp_lg_u32 %2, 3\n\t"
    "s_cbranch_scc1 L_nopf_%=\n\t"
    "global_load_dword v123, v122, s[36:37]\n\t"
    "L_nopf_%=:\n\t"
    :: "s"(bid), "v"(threadIdx.x), "s"(w) : "memory", "scc", "s36", "s37", "s38", "s74", "s75", "v122", "v123");
  const int xcd = bid & 7, loc = bid >> 3;
  const int cg = xcd * 16 + (loc >> 1);
  const __amdgpu_buffer_rsrc_t s_idx = mk_srd(idx + (size_t)bid * 64 * NF, (unsigned)((BATCH - bid * 64) * NF * 4));
  const __amdgpu_buffer_rsrc_t s_tab = mk_srd(table, VOC * DIM * 2);
  const __amdgpu_buffer_rsrc_t s_ws = mk_srd(ws, (unsigned)WS_END);
  const unsigned short* pb1 = b1 + 4 * cg;
  const unsigned short* pw2 = w2 + 4 * cg;
  const unsigned long long pb2 = (unsigned long long)b2 & ~3ull;
  const unsigned b2s = ((unsigned)(unsigned long long)b2 & 2u) * 8u;
  const unsigned long long p_wp = (unsigned long long)w_p & 0xFFFFFFFFFFFFull, p_w1 = (unsigned long long)w1 & 0xFFFFFFFFFFFFull;
  const unsigned long long p_out = (unsigned long long)out & 0xFFFFFFFFFFFFull;
  asm volatile(
    "v_mbcnt_lo_u32_b32 v147, -1, 0\n\t"
    "v_mbcnt_hi_u32_b32 v147, -1, v147\n\t"
    "s_mov_b64 s[92:93], %1\n\t"
    "s_mov_b32 s94, 20000\n\t"
    "s_mov_b32 s95, 0x20000\n\t"
    "s_lshr_b32 s46, %14, 3\n\t"
    "s_lshl_b32 s51, s46, 2\n\t"
    "v_lshlrev_b32 v122, 4, v147\n\t"
    "s_cmp_ge_u32 %16, 2\n\t"
    "s_cbranch_scc1 L_aimg_%=\n\t"
    "s_lshl_b32 s44, %16, 10\n\t"
    "s_add_u32 m0, s44, 140288\n\t"
    "s_nop 0\n\t"
    "buffer_load_dwordx4 v122, s[92:95], s44 offen lds\n\t"
    "s_add_u32 s44, s44, 0x800\n\t"
    "s_add_u32 m0, s44, 140288\n\t"
    "s_nop 0\n\t"
    "buffer_load_dwordx4 v122, s[92:95], s44 offen lds\n\t"
    "s_add_u32 s44, s44, 0x800\n\t"
    "s_add_u32 m0, s44, 140288\n\t"
    "s_nop 0\n\t"
    "buffer_load_dwordx4 v122, s[92:95], s44 offen lds\n\t"
    "s_add_u32 s44, s44, 0x800\n\t"
    "s_add_u32 m0, s44, 140288\n\t"
    "s_nop 0\n\t"
    "buffer_load_dwordx4 v122, s[92:95], s44 offen lds\n\t"
    "s_add_u32 s44, s44, 0x800\n\t"
    "s_add_u32 m0, s44, 140288\n\t"
    "s_nop 0\n\t"
    "buffer_load_dwordx4 v122, s[92:95], s44 offen lds\n\t"
    "s_add_u32 s44, s44, 0x800\n\t"
    "s_add_u32 m0, s44, 140288\n\t"
    "s_nop 0\n\t"
    "buffer_load_dwordx4 v122, s[92:95], s44 offen lds\n\t"
    "s_add_u32 s44, s44, 0x800\n\t"
    "s_add_u32 m0, s44, 140288\n\t"
    "s_nop 0\n\t"
    "buffer_load_dwordx4 v122, s[92:95], s44 offen lds\n\t"
    "s_add_u32 s44, s44, 0x800\n\t"
    "s_add_u32 m0, s44, 140288\n\t"
    "s_nop 0\n\t"
    "buffer_load_dwordx4 v122, s[92:95], s44 offen lds\n\t"
    "s_add_u32 s44, s44, 0x800\n\t"
    "s_add_u32 m0, s44, 140288\n\t"
    "s_nop 0\n\t"
    "buffer_load_dwordx4 v122, s[92:95], s44 offen lds\n\t"
    "s_add_u32 s44, s44, 0x800\n\t"
    "s_add_u32 m0, s44, 140288\n\t"
    "s_nop 0\n\t"
    "buffer_load_dwordx4 v122, s[92:95], s44 offen lds\n\t"
    "s_add_u32 s44, s44, 0x800\n\t"
    "s_mov_b32 s98, 13107200\n\t"
    "s_mov_b32 s99, 0x20000\n\t"
    "s_cmp_eq_u32 %16, 0\n\t"
    "s_cbranch_scc1 L_aside_%=\n\t"
    "s_branch L_ahelp_%=\n\t"
    "L_aimg_%=:\n\t"
    "s_cmp_lg_u32 %16, 2\n\t"
    "s_cbranch_scc1 L_anoinit_%=\n\t"
    "v_mov_b32 v0, 0\n\t"
    "v_mov_b32 v1, 0\n\t"
    "v_mov_b32 v2, 0\n\t"
    "v_mov_b32 v3, 0\n\t"
    "v_lshlrev_b32 v8, 4, v147\n\t"
    "v_add_u32 v8, 130048, v8\n\t"
    "v_mov_b32 v9, 1024\n\t"
    "ds_write_b128 v9, v[0:3]\n\t"
    "ds_write_b128 v8, v[0:3] offset:0\n\t"
    "ds_write_b128 v8, v[0:3] offset:1024\n\t"
    "ds_write_b128 v8, v[0:3] offset:2048\n\t"
    "ds_write_b32 v9, v0 offset:2084\n\t"
    "ds_write_b32 v9, v0 offset:2088\n\t"
    "ds_write_b32 v9, v0 offset:1044\n\t"
    "ds_write_b32 v9, v0 offset:1052\n\t"
    "v_mov_b32 v4, 3\n\t"
    "ds_write_b32 v9, v4 offset:1040\n\t"
    "v_mov_b32 v5, 16\n\t"
    "ds_write_b32 v9, v5 offset:2080\n\t"
    "v_mov_b32 v6, 33\n\t"
    "ds_write_b32 v9, v6 offset:3120\n\t"
    "v_mov_b32 v7, 52\n\t"
    "ds_write_b32 v9, v7 offset:4160\n\t"
    "L_anoinit_%=:\n\t"
    "s_and_b32 s46, %16, 3\n\t"
    "s_mul_i32 s49, s46, 1040\n\t"
    "s_add_u32 s49, s49, 2064\n\t"
    "s_lshl_b32 s46, s46, 3\n\t"
    "s_lshr_b32 s47, 1076763402, s46\n\t"
    "s_and_b32 s47, s47, 0xff\n\t"
    "s_lshl_b32 s48, s47, 16\n\t"
    "s_lshr_b32 s47, 773523968, s46\n\t"
    "s_and_b32 s47, s47, 0xff\n\t"
    "s_add_u32 s48, s48, s47\n\t"
    "s_lshr_b32 s47, %16, 2\n\t"
    "s_add_u32 s47, s47, 0x200\n\t"
    "s_cmp_eq_u32 s46, 0\n\t"
    "s_cselect_b32 s47, 0x100, s47\n\t"
    "s_add_u32 s48, s48, s47\n\t"
    "v_lshrrev_b32 v126, 3, v147\n\t"
    "v_and_b32 v154, 7, v147\n\t"
    "v_lshlrev_b32 v126, 8, v126\n\t"
    "v_lshl_add_u32 v126, v154, 4, v126\n\t"
    "v_lshlrev_b32 v117, 20, v147\n\t"
    "v_add_u32 v117, 1024, v117\n\t"
    "v_mov_b32 v118, 1\n\t"
    "s_and_b32 s46, %16, 6\n\t"
    "s_cmp_eq_u32 s46, 4\n\t"
    "s_cbranch_scc1 L_adma_%=\n\t"
    "s_lshr_b32 s61, %16, 2\n\t"
    "s_lshl_b32 s61, s61, 1\n\t"
    "s_and_b32 s46, %16, 1\n\t"
    "s_add_u32 s61, s61, s46\n\t"
    "s_add_u32 s46, s61, 84\n\t"
    "s_add_u32 s69, s51, s46\n\t"
    "s_sub_u32 s44, s69, 125\n\t"
    "s_cmp_ge_u32 s69, 125\n\t"
    "s_cselect_b32 s69, s44, s69\n\t"
    "s_mul_i32 m0, s69, 1040\n\t"
    "s_lshl_b32 s44, s69, 11\n\t"
    "buffer_load_dwordx4 v126, %15, s44 offen lds\n\t"
    "s_add_u32 s69, s69, 4\n\t"
    "s_sub_u32 s44, s69, 125\n\t"
    "s_cmp_ge_u32 s69, 125\n\t"
    "s_cselect_b32 s69, s44, s69\n\t"
    "s_mul_i32 m0, s69, 1040\n\t"
    "s_lshl_b32 s44, s69, 11\n\t"
    "buffer_load_dwordx4 v126, %15, s44 offen lds\n\t"
    "s_add_u32 s69, s69, 4\n\t"
    "s_sub_u32 s44, s69, 125\n\t"
    "s_cmp_ge_u32 s69, 125\n\t"
    "s_cselect_b32 s69, s44, s69\n\t"
    "s_waitcnt lgkmcnt(0)\n\t"
    "s_barrier\n\t"
    "s_load_dwordx2 s[72:73], %6, 0\n\t"
    "s_load_dwordx2 s[88:89], %7, 0\n\t"
    "s_load_dword s91, %8, 0\n\t"
    "s_mul_i32 m0, s69, 1040\n\t"
    "s_lshl_b32 s44, s69, 11\n\t"
    "buffer_load_dwordx4 v126, %15, s44 offen lds\n\t"
    "s_add_u32 s69, s69, 4\n\t"
    "s_sub_u32 s44, s69, 125\n\t"
    "s_cmp_ge_u32 s69, 125\n\t"
    "s_cselect_b32 s69, s44, s69\n\t"
    "s_mul_i32 m0, s69, 1040\n\t"
    "s_lshl_b32 s44, s69, 11\n\t"
    "buffer_load_dwordx4 v126, %15, s44 offen lds\n\t"
    "s_add_u32 s69, s69, 4\n\t"
    "s_sub_u32 s44, s69, 125\n\t"
    "s_cmp_ge_u32 s69, 125\n\t"
    "s_cselect_b32 s69, s44, s69\n\t"
    "s_mul_i32 m0, s69, 1040\n\t"
    "s_lshl_b32 s44, s69, 11\n\t"
    "buffer_load_dwordx4 v126, %15, s44 offen lds\n\t"
    "s_add_u32 s69, s69, 4\n\t"
    "s_sub_u32 s44, s69, 125\n\t"
    "s_cmp_ge_u32 s69, 125\n\t"
    "s_cselect_b32 s69, s44, s69\n\t"
    "s_mul_i32 m0, s69, 1040\n\t"
    "s_lshl_b32 s44, s69, 11\n\t"
    "buffer_load_dwordx4 v126, %15, s44 offen lds\n\t"
    "s_add_u32 s69, s69, 4\n\t"
    "s_sub_u32 s44, s69, 125\n\t"
    "s_cmp_ge_u32 s69, 125\n\t"
    "s_cselect_b32 s69, s44, s69\n\t"
    "v_bfe_u32 v154, v147, 2, 2\n\t"
    "v_and_b32 v155, 3, v147\n\t"
    "v_mul_u32_u24 v154, 25, v154\n\t"
    "v_mad_u32_u24 v154, v155, 6, v154\n\t"
    "v_lshlrev_b32 v153, 2, v154\n\t"
    "s_and_b32 s53, s48, 0xff\n\t"
    "s_bfe_u32 s55, s48, 0x80008\n\t"
    "s_add_u32 s54, s53, s55\n\t"
    "s_add_u32 s55, s54, s55\n\t"
    "s_lshr_b32 s64, s48, 16\n\t"
    "v_cmp_eq_u32 s[62:63], 0, v155\n\t"
    "v_and_b32 v151, 48, v147\n\t"
    "v_or_b32 v155, 0x100000, v151\n\t"
    "v_cndmask_b32 v152, v155, v151, s[62:63]\n\t"
    "v_lshrrev_b32 v94, 4, v147\n\t"
    "v_mul_u32_u24 v92, 0x1388, v94\n\t"
    "v_lshl_add_u32 v92, v154, 1, v92\n\t"
    "v_add_u32 v92, 140288, v92\n\t"
    "v_mul_u32_u24 v93, 200, v154\n\t"
    "v_mad_u32_u24 v93, v94, 50, v93\n\t"
    "v_add_u32 v93, 140288, v93\n\t"
    "v_add_u32 v95, 140288, v122\n\t"
    "v_mov_b32 v0, 0\n\t"
    "v_mov_b32 v28, 0\n\t"
    "v_mov_b32 v1, 0\n\t"
    "v_mov_b32 v29, 0\n\t"
    "v_mov_b32 v2, 0\n\t"
    "v_mov_b32 v30, 0\n\t"
    "v_mov_b32 v3, 0\n\t"
    "v_mov_b32 v31, 0\n\t"
    "v_mov_b32 v4, 0\n\t"
    "v_mov_b32 v32, 0\n\t"
    "v_mov_b32 v5, 0\n\t"
    "v_mov_b32 v33, 0\n\t"
    "v_mov_b32 v6, 0\n\t"
    "v_mov_b32 v34, 0\n\t"
    "v_mov_b32 v7, 0\n\t"
    "v_mov_b32 v35, 0\n\t"
    "v_mov_b32 v8, 0\n\t"
    "v_mov_b32 v36, 0\n\t"
    "v_mov_b32 v9, 0\n\t"
    "v_mov_b32 v37, 0\n\t"
    "v_mov_b32 v10, 0\n\t"
    "v_mov_b32 v38, 0\n\t"
    "v_mov_b32 v11, 0\n\t"
    "v_mov_b32 v39, 0\n\t"
    "v_mov_b32 v12, 0\n\t"
    "v_mov_b32 v40, 0\n\t"
    "v_mov_b32 v13, 0\n\t"
    "v_mov_b32 v41, 0\n\t"
    "v_mov_b32 v14, 0\n\t"
    "v_mov_b32 v42, 0\n\t"
    "v_mov_b32 v15, 0\n\t"
    "v_mov_b32 v43, 0\n\t"
    "v_mov_b32 v16, 0\n\t"
    "v_mov_b32 v44, 0\n\t"
    "v_mov_b32 v17, 0\n\t"
    "v_mov_b32 v45, 0\n\t"
    "v_mov_b32 v18, 0\n\t"
    "v_mov_b32 v46, 0\n\t"
    "v_mov_b32 v19, 0\n\t"
    "v_mov_b32 v47, 0\n\t"
    "v_mov_b32 v20, 0\n\t"
    "v_mov_b32 v48, 0\n\t"
    "v_mov_b32 v21, 0\n\t"
    "v_mov_b32 v49, 0\n\t"
    "v_mov_b32 v22, 0\n\t"
    "v_mov_b32 v50, 0\n\t"
    "v_mov_b32 v23, 0\n\t"
    "v_mov_b32 v51, 0\n\t"
    "s_cmp_eq_u32 s61, 1\n\t"
    "s_cbranch_scc1 L_afold1_%=\n\t"
    "s_cmp_eq_u32 s61, 2\n\t"
    "s_cbranch_scc1 L_afold2_%=\n\t"
    "s_cmp_eq_u32 s61, 3\n\t"
    "s_cbranch_scc1 L_afold3_%=\n\t"
    "L_afold0_%=:\n\t"
    "L_asp0_%=:\n\t"
    "ds_read_b32 v119, v117 offset:1044\n\t"
    "s_waitcnt lgkmcnt(0)\n\t"
    "v_readfirstlane_b32 s46, v119\n\t"
    "s_nop 0\n\t"
    "s_cmp_ge_u32 s46, 2\n\t"
    "s_cbranch_scc1 L_ago0_%=\n\t"
    "s_sleep 1\n\t"
    "s_branch L_asp0_%=\n\t"
    "L_ago0_%=:\n\t"
    "ds_read_u16_d16_hi v0, v92 offset:208\n\t"
    "ds_read_u16_d16_hi v28, v93 offset:802\n\t"
    "ds_read_u16_d16_hi v1, v92 offset:1408\n\t"
    "ds_read_u16_d16_hi v29, v93 offset:814\n\t"
    "ds_read_u16_d16_hi v2, v92 offset:2608\n\t"
    "ds_read_u16_d16_hi v30, v93 offset:826\n\t"
    "ds_read_u16_d16_hi v3, v92 offset:3808\n\t"
    "ds_read_u16_d16_hi v31, v93 offset:838\n\t"
    "ds_read_u16_d16_hi v4, v92 offset:410\n\t"
    "ds_read_u16_d16_hi v32, v93 offset:1004\n\t"
    "ds_read_u16_d16_hi v5, v92 offset:1610\n\t"
    "ds_read_u16_d16_hi v33, v93 offset:1016\n\t"
    "ds_read_u16_d16_hi v6, v92 offset:2810\n\t"
    "ds_read_u16_d16_hi v34, v93 offset:1028\n\t"
    "ds_read_u16_d16_hi v7, v92 offset:4010\n\t"
    "ds_read_u16_d16_hi v35, v93 offset:1040\n\t"
    "ds_read_u16_d16_hi v8, v92 offset:612\n\t"
    "ds_read_u16_d16_hi v36, v93 offset:1206\n\t"
    "ds_read_u16_d16_hi v9, v92 offset:1812\n\t"
    "ds_read_u16_d16_hi v37, v93 offset:1218\n\t"
    "ds_read_u16_d16_hi v10, v92 offset:3012\n\t"
    "ds_read_u16_d16_hi v38, v93 offset:1230\n\t"
    "ds_read_u16_d16_hi v11, v92 offset:4212\n\t"
    "ds_read_u16_d16_hi v39, v93 offset:1242\n\t"
    "ds_read_u16_d16_hi v12, v92 offset:0\n\t"
    "ds_read_u16_d16_hi v13, v92 offset:2\n\t"
    "ds_read_u16_d16_hi v41, v93 offset:200\n\t"
    "ds_read_u16_d16_hi v14, v92 offset:4\n\t"
    "ds_read_u16_d16_hi v42, v93 offset:400\n\t"
    "ds_read_u16_d16_hi v15, v92 offset:6\n\t"
    "ds_read_u16_d16_hi v43, v93 offset:600\n\t"
    "ds_read_u16_d16_hi v16, v92 offset:8\n\t"
    "ds_read_u16_d16_hi v44, v93 offset:800\n\t"
    "ds_read_u16_d16_hi v17, v92 offset:10\n\t"
    "ds_read_u16_d16_hi v45, v93 offset:1000\n\t"
    "ds_read_u16_d16_hi v18, v92 offset:12\n\t"
    "ds_read_u16_d16_hi v46, v93 offset:1200\n\t"
    "ds_add_u32 v117, v118 offset:0\n\t"
    "s_waitcnt lgkmcnt(15)\n\t"
    "v_add_f32 v0, v0, v28\n\t"
    "v_add_f32 v1, v1, v29\n\t"
    "v_add_f32 v2, v2, v30\n\t"
    "v_add_f32 v3, v3, v31\n\t"
    "v_add_f32 v4, v4, v32\n\t"
    "v_add_f32 v5, v5, v33\n\t"
    "v_add_f32 v6, v6, v34\n\t"
    "v_add_f32 v7, v7, v35\n\t"
    "s_waitcnt lgkmcnt(14)\n\t"
    "v_add_f32 v8, v8, v36\n\t"
    "v_add_f32 v9, v9, v37\n\t"
    "v_add_f32 v10, v10, v38\n\t"
    "v_add_f32 v11, v11, v39\n\t"
    "s_waitcnt lgkmcnt(7)\n\t"
    "v_cndmask_b32 v12, 0, v12, s[62:63]\n\t"
    "v_add_f32 v13, v13, v41\n\t"
    "v_add_f32 v14, v14, v42\n\t"
    "v_add_f32 v15, v15, v43\n\t"
    "s_waitcnt lgkmcnt(1)\n\t"
    "v_add_f32 v16, v16, v44\n\t"
    "v_add_f32 v17, v17, v45\n\t"
    "v_add_f32 v18, v18, v46\n\t"
    "s_call_b64 s[98:99], L_setup_%=\n\t"
    "L_asp1_%=:\n\t"
    "ds_read_b32 v119, v117 offset:0\n\t"
    "s_waitcnt lgkmcnt(0)\n\t"
    "v_readfirstlane_b32 s46, v119\n\t"
    "s_nop 0\n\t"
    "s_cmp_ge_u32 s46, 4\n\t"
    "s_cbranch_scc1 L_ago1_%=\n\t"
    "s_sleep 1\n\t"
    "s_branch L_asp1_%=\n\t"
    "L_ago1_%=:\n\t"
    "ds_write_b128 v95, v[0:3] offset:6144\n\t"
    "ds_write_b128 v95, v[4:7] offset:10240\n\t"
    "ds_write_b128 v95, v[8:11] offset:14336\n\t"
    "ds_write_b128 v95, v[12:15] offset:21504\n\t"
    "ds_write_b128 v95, v[16:19] offset:22528\n\t"
    "ds_add_u32 v117, v118 offset:4\n\t"
    "s_branch L_ab0_%=\n\t"
    "L_afold1_%=:\n\t"
    "L_asp2_%=:\n\t"
    "ds_read_b32 v119, v117 offset:1044\n\t"
    "s_waitcnt lgkmcnt(0)\n\t"
    "v_readfirstlane_b32 s46, v119\n\t"
    "s_nop 0\n\t"
    "s_cmp_ge_u32 s46, 2\n\t"
    "s_cbranch_scc1 L_ago2_%=\n\t"
    "s_sleep 1\n\t"
    "s_branch L_asp2_%=\n\t"
    "L_ago2_%=:\n\t"
    "ds_read_u16_d16_hi v0, v92 offset:204\n\t"
    "ds_read_u16_d16_hi v28, v93 offset:402\n\t"
    "ds_read_u16_d16_hi v1, v92 offset:1404\n\t"
    "ds_read_u16_d16_hi v29, v93 offset:414\n\t"
    "ds_read_u16_d16_hi v2, v92 offset:2604\n\t"
    "ds_read_u16_d16_hi v30, v93 offset:426\n\t"
    "ds_read_u16_d16_hi v3, v92 offset:3804\n\t"
    "ds_read_u16_d16_hi v31, v93 offset:438\n\t"
    "ds_read_u16_d16_hi v4, v92 offset:408\n\t"
    "ds_read_u16_d16_hi v32, v93 offset:804\n\t"
    "ds_read_u16_d16_hi v5, v92 offset:1608\n\t"
    "ds_read_u16_d16_hi v33, v93 offset:816\n\t"
    "ds_read_u16_d16_hi v6, v92 offset:2808\n\t"
    "ds_read_u16_d16_hi v34, v93 offset:828\n\t"
    "ds_read_u16_d16_hi v7, v92 offset:4008\n\t"
    "ds_read_u16_d16_hi v35, v93 offset:840\n\t"
    "ds_read_u16_d16_hi v8, v92 offset:412\n\t"
    "ds_read_u16_d16_hi v36, v93 offset:1204\n\t"
    "ds_read_u16_d16_hi v9, v92 offset:1612\n\t"
    "ds_read_u16_d16_hi v37, v93 offset:1216\n\t"
    "ds_read_u16_d16_hi v10, v92 offset:2812\n\t"
    "ds_read_u16_d16_hi v38, v93 offset:1228\n\t"
    "ds_read_u16_d16_hi v11, v92 offset:4012\n\t"
    "ds_read_u16_d16_hi v39, v93 offset:1240\n\t"
    "ds_read_u16_d16_hi v12, v92 offset:810\n\t"
    "ds_read_u16_d16_hi v40, v93 offset:1008\n\t"
    "ds_read_u16_d16_hi v13, v92 offset:2010\n\t"
    "ds_read_u16_d16_hi v41, v93 offset:1020\n\t"
    "ds_read_u16_d16_hi v14, v92 offset:3210\n\t"
    "ds_read_u16_d16_hi v42, v93 offset:1032\n\t"
    "ds_read_u16_d16_hi v15, v92 offset:4410\n\t"
    "ds_read_u16_d16_hi v43, v93 offset:1044\n\t"
    "ds_read_u16_d16_hi v16, v92 offset:202\n\t"
    "ds_read_u16_d16_hi v17, v92 offset:1402\n\t"
    "ds_read_u16_d16_hi v18, v92 offset:2602\n\t"
    "ds_read_u16_d16_hi v19, v92 offset:3802\n\t"
    "ds_read_u16_d16_hi v20, v92 offset:808\n\t"
    "ds_read_u16_d16_hi v21, v92 offset:2008\n\t"
    "ds_read_u16_d16_hi v22, v92 offset:3208\n\t"
    "ds_read_u16_d16_hi v23, v92 offset:4408\n\t"
    "ds_add_u32 v117, v118 offset:0\n\t"
    "s_waitcnt lgkmcnt(15)\n\t"
    "v_add_f32 v0, v0, v28\n\t"
    "v_add_f32 v1, v1, v29\n\t"
    "v_add_f32 v2, v2, v30\n\t"
    "v_add_f32 v3, v3, v31\n\t"
    "v_add_f32 v4, v4, v32\n\t"
    "v_add_f32 v5, v5, v33\n\t"
    "v_add_f32 v6, v6, v34\n\t"
    "v_add_f32 v7, v7, v35\n\t"
    "v_add_f32 v8, v8, v36\n\t"
    "v_add_f32 v9, v9, v37\n\t"
    "v_add_f32 v10, v10, v38\n\t"
    "v_add_f32 v11, v11, v39\n\t"
    "s_waitcnt lgkmcnt(9)\n\t"
    "v_add_f32 v12, v12, v40\n\t"
    "v_add_f32 v13, v13, v41\n\t"
    "v_add_f32 v14, v14, v42\n\t"
    "v_add_f32 v15, v15, v43\n\t"
    "s_waitcnt lgkmcnt(5)\n\t"
    "s_waitcnt lgkmcnt(1)\n\t"
    "s_call_b64 s[98:99], L_setup_%=\n\t"
    "L_asp3_%=:\n\t"
    "ds_read_b32 v119, v117 offset:0\n\t"
    "s_waitcnt lgkmcnt(0)\n\t"
    "v_readfirstlane_b32 s46, v119\n\t"
    "s_nop 0\n\t"
    "s_cmp_ge_u32 s46, 4\n\t"
    "s_cbranch_scc1 L_ago3_%=\n\t"
    "s_sleep 1\n\t"
    "s_branch L_asp3_%=\n\t"
    "L_ago3_%=:\n\t"
    "ds_write_b128 v95, v[0:3] offset:1024\n\t"
    "ds_write_b128 v95, v[4:7] offset:7168\n\t"
    "ds_write_b128 v95, v[8:11] offset:11264\n\t"
    "ds_write_b128 v95, v[12:15] offset:16384\n\t"
    "ds_write_b128 v95, v[16:19] offset:0\n\t"
    "ds_write_b128 v95, v[20:23] offset:15360\n\t"
    "ds_add_u32 v117, v118 offset:4\n\t"
    "s_branch L_ab0_%=\n\t"
    "L_afold2_%=:\n\t"
    "s_call_b64 s[98:99], L_setup_%=\n\t"
    "L_asp4_%=:\n\t"
    "ds_read_b32 v119, v117 offset:1044\n\t"
    "s_waitcnt lgkmcnt(0)\n\t"
    "v_readfirstlane_b32 s46, v119\n\t"
    "s_nop 0\n\t"
    "s_cmp_ge_u32 s46, 2\n\t"
    "s_cbranch_scc1 L_ago4_%=\n\t"
    "s_sleep 1\n\t"
    "s_branch L_asp4_%=\n\t"
    "L_ago4_%=:\n\t"
    "ds_read_u16_d16_hi v0, v92 offset:206\n\t"
    "ds_read_u16_d16_hi v28, v93 offset:602\n\t"
    "ds_read_u16_d16_hi v1, v92 offset:1406\n\t"
    "ds_read_u16_d16_hi v29, v93 offset:614\n\t"
    "ds_read_u16_d16_hi v2, v92 offset:2606\n\t"
    "ds_read_u16_d16_hi v30, v93 offset:626\n\t"
    "ds_read_u16_d16_hi v3, v92 offset:3806\n\t"
    "ds_read_u16_d16_hi v31, v93 offset:638\n\t"
    "ds_read_u16_d16_hi v4, v92 offset:210\n\t"
    "ds_read_u16_d16_hi v32, v93 offset:1002\n\t"
    "ds_read_u16_d16_hi v5, v92 offset:1410\n\t"
    "ds_read_u16_d16_hi v33, v93 offset:1014\n\t"
    "ds_read_u16_d16_hi v6, v92 offset:2610\n\t"
    "ds_read_u16_d16_hi v34, v93 offset:1026\n\t"
    "ds_read_u16_d16_hi v7, v92 offset:3810\n\t"
    "ds_read_u16_d16_hi v35, v93 offset:1038\n\t"
    "ds_read_u16_d16_hi v8, v92 offset:608\n\t"
    "ds_read_u16_d16_hi v36, v93 offset:806\n\t"
    "ds_read_u16_d16_hi v9, v92 offset:1808\n\t"
    "ds_read_u16_d16_hi v37, v93 offset:818\n\t"
    "ds_read_u16_d16_hi v10, v92 offset:3008\n\t"
    "ds_read_u16_d16_hi v38, v93 offset:830\n\t"
    "ds_read_u16_d16_hi v11, v92 offset:4208\n\t"
    "ds_read_u16_d16_hi v39, v93 offset:842\n\t"
    "ds_read_u16_d16_hi v12, v92 offset:812\n\t"
    "ds_read_u16_d16_hi v40, v93 offset:1208\n\t"
    "ds_read_u16_d16_hi v13, v92 offset:2012\n\t"
    "ds_read_u16_d16_hi v41, v93 offset:1220\n\t"
    "ds_read_u16_d16_hi v14, v92 offset:3212\n\t"
    "ds_read_u16_d16_hi v42, v93 offset:1232\n\t"
    "ds_read_u16_d16_hi v15, v92 offset:4412\n\t"
    "ds_read_u16_d16_hi v43, v93 offset:1244\n\t"
    "ds_read_u16_d16_hi v16, v92 offset:404\n\t"
    "ds_read_u16_d16_hi v17, v92 offset:1604\n\t"
    "ds_read_u16_d16_hi v18, v92 offset:2804\n\t"
    "ds_read_u16_d16_hi v19, v92 offset:4004\n\t"
    "ds_read_u16_d16_hi v20, v92 offset:1010\n\t"
    "ds_read_u16_d16_hi v21, v92 offset:2210\n\t"
    "ds_read_u16_d16_hi v22, v92 offset:3410\n\t"
    "ds_read_u16_d16_hi v23, v92 offset:4610\n\t"
    "ds_add_u32 v117, v118 offset:0\n\t"
    "s_waitcnt lgkmcnt(15)\n\t"
    "v_add_f32 v0, v0, v28\n\t"
    "v_add_f32 v1, v1, v29\n\t"
    "v_add_f32 v2, v2, v30\n\t"
    "v_add_f32 v3, v3, v31\n\t"
    "v_add_f32 v4, v4, v32\n\t"
    "v_add_f32 v5, v5, v33\n\t"
    "v_add_f32 v6, v6, v34\n\t"
    "v_add_f32 v7, v7, v35\n\t"
    "v_add_f32 v8, v8, v36\n\t"
    "v_add_f32 v9, v9, v37\n\t"
    "v_add_f32 v10, v10, v38\n\t"
    "v_add_f32 v11, v11, v39\n\t"
    "s_waitcnt lgkmcnt(9)\n\t"
    "v_add_f32 v12, v12, v40\n\t"
    "v_add_f32 v13, v13, v41\n\t"
    "v_add_f32 v14, v14, v42\n\t"
    "v_add_f32 v15, v15, v43\n\t"
    "s_waitcnt lgkmcnt(5)\n\t"
    "s_waitcnt lgkmcnt(1)\n\t"
    "L_asp5_%=:\n\t"
    "ds_read_b32 v119, v117 offset:0\n\t"
    "s_waitcnt lgkmcnt(0)\n\t"
    "v_readfirstlane_b32 s46, v119\n\t"
    "s_nop 0\n\t"
    "s_cmp_ge_u32 s46, 4\n\t"
    "s_cbranch_scc1 L_ago5_%=\n\t"
    "s_sleep 1\n\t"
    "s_branch L_asp5_%=\n\t"
    "L_ago5_%=:\n\t"
    "ds_write_b128 v95, v[0:3] offset:3072\n\t"
    "ds_write_b128 v95, v[4:7] offset:8192\n\t"
    "ds_write_b128 v95, v[8:11] offset:12288\n\t"
    "ds_write_b128 v95, v[12:15] offset:17408\n\t"
    "ds_write_b128 v95, v[16:19] offset:2048\n\t"
    "ds_write_b128 v95, v[20:23] offset:18432\n\t"
    "ds_add_u32 v117, v118 offset:4\n\t"
    "s_branch L_ab0_%=\n\t"
    "L_afold3_%=:\n\t"
    "s_call_b64 s[98:99], L_setup_%=\n\t"
    "L_asp6_%=:\n\t"
    "ds_read_b32 v119, v117 offset:1044\n\t"
    "s_waitcnt lgkmcnt(0)\n\t"
    "v_readfirstlane_b32 s46, v119\n\t"
    "s_nop 0\n\t"
    "s_cmp_ge_u32 s46, 2\n\t"
    "s_cbranch_scc1 L_ago6_%=\n\t"
    "s_sleep 1\n\t"
    "s_branch L_asp6_%=\n\t"
    "L_ago6_%=:\n\t"
    "ds_read_u16_d16_hi v0, v92 offset:406\n\t"
    "ds_read_u16_d16_hi v28, v93 offset:604\n\t"
    "ds_read_u16_d16_hi v1, v92 offset:1606\n\t"
    "ds_read_u16_d16_hi v29, v93 offset:616\n\t"
    "ds_read_u16_d16_hi v2, v92 offset:2806\n\t"
    "ds_read_u16_d16_hi v30, v93 offset:628\n\t"
    "ds_read_u16_d16_hi v3, v92 offset:4006\n\t"
    "ds_read_u16_d16_hi v31, v93 offset:640\n\t"
    "ds_read_u16_d16_hi v4, v92 offset:212\n\t"
    "ds_read_u16_d16_hi v32, v93 offset:1202\n\t"
    "ds_read_u16_d16_hi v5, v92 offset:1412\n\t"
    "ds_read_u16_d16_hi v33, v93 offset:1214\n\t"
    "ds_read_u16_d16_hi v6, v92 offset:2612\n\t"
    "ds_read_u16_d16_hi v34, v93 offset:1226\n\t"
    "ds_read_u16_d16_hi v7, v92 offset:3812\n\t"
    "ds_read_u16_d16_hi v35, v93 offset:1238\n\t"
    "ds_read_u16_d16_hi v8, v92 offset:610\n\t"
    "ds_read_u16_d16_hi v36, v93 offset:1006\n\t"
    "ds_read_u16_d16_hi v9, v92 offset:1810\n\t"
    "ds_read_u16_d16_hi v37, v93 offset:1018\n\t"
    "ds_read_u16_d16_hi v10, v92 offset:3010\n\t"
    "ds_read_u16_d16_hi v38, v93 offset:1030\n\t"
    "ds_read_u16_d16_hi v11, v92 offset:4210\n\t"
    "ds_read_u16_d16_hi v39, v93 offset:1042\n\t"
    "ds_read_u16_d16_hi v12, v92 offset:1012\n\t"
    "ds_read_u16_d16_hi v40, v93 offset:1210\n\t"
    "ds_read_u16_d16_hi v13, v92 offset:2212\n\t"
    "ds_read_u16_d16_hi v41, v93 offset:1222\n\t"
    "ds_read_u16_d16_hi v14, v92 offset:3412\n\t"
    "ds_read_u16_d16_hi v42, v93 offset:1234\n\t"
    "ds_read_u16_d16_hi v15, v92 offset:4612\n\t"
    "ds_read_u16_d16_hi v43, v93 offset:1246\n\t"
    "ds_read_u16_d16_hi v16, v92 offset:606\n\t"
    "ds_read_u16_d16_hi v17, v92 offset:1806\n\t"
    "ds_read_u16_d16_hi v18, v92 offset:3006\n\t"
    "ds_read_u16_d16_hi v19, v92 offset:4206\n\t"
    "ds_read_u16_d16_hi v20, v92 offset:1212\n\t"
    "ds_read_u16_d16_hi v21, v92 offset:2412\n\t"
    "ds_read_u16_d16_hi v22, v92 offset:3612\n\t"
    "ds_read_u16_d16_hi v23, v92 offset:4812\n\t"
    "ds_add_u32 v117, v118 offset:0\n\t"
    "s_waitcnt lgkmcnt(15)\n\t"
    "v_add_f32 v0, v0, v28\n\t"
    "v_add_f32 v1, v1, v29\n\t"
    "v_add_f32 v2, v2, v30\n\t"
    "v_add_f32 v3, v3, v31\n\t"
    "v_add_f32 v4, v4, v32\n\t"
    "v_add_f32 v5, v5, v33\n\t"
    "v_add_f32 v6, v6, v34\n\t"
    "v_add_f32 v7, v7, v35\n\t"
    "v_add_f32 v8, v8, v36\n\t"
    "v_add_f32 v9, v9, v37\n\t"
    "v_add_f32 v10, v10, v38\n\t"
    "v_add_f32 v11, v11, v39\n\t"
    "s_waitcnt lgkmcnt(9)\n\t"
    "v_add_f32 v12, v12, v40\n\t"
    "v_add_f32 v13, v13, v41\n\t"
    "v_add_f32 v14, v14, v42\n\t"
    "v_add_f32 v15, v15, v43\n\t"
    "s_waitcnt lgkmcnt(5)\n\t"
    "s_waitcnt lgkmcnt(1)\n\t"
    "L_asp7_%=:\n\t"
    "ds_read_b32 v119, v117 offset:0\n\t"
    "s_waitcnt lgkmcnt(0)\n\t"
    "v_readfirstlane_b32 s46, v119\n\t"
    "s_nop 0\n\t"
    "s_cmp_ge_u32 s46, 4\n\t"
    "s_cbranch_scc1 L_ago7_%=\n\t"
    "s_sleep 1\n\t"
    "s_branch L_asp7_%=\n\t"
    "L_ago7_%=:\n\t"
    "ds_write_b128 v95, v[0:3] offset:4096\n\t"
    "ds_write_b128 v95, v[4:7] offset:9216\n\t"
    "ds_write_b128 v95, v[8:11] offset:13312\n\t"
    "ds_write_b128 v95, v[12:15] offset:19456\n\t"
    "ds_write_b128 v95, v[16:19] offset:5120\n\t"
    "ds_write_b128 v95, v[20:23] offset:20480\n\t"
    "ds_add_u32 v117, v118 offset:4\n\t"
    "L_ab0_%=:\n\t"
    "s_mul_i32 m0, s69, 1040\n\t"
    "s_lshl_b32 s44, s69, 11\n\t"
    "buffer_load_dwordx4 v126, %15, s44 offen lds\n\t"
    "s_add_u32 s69, s69, 4\n\t"
    "s_sub_u32 s44, s69, 125\n\t"
    "s_cmp_ge_u32 s69, 125\n\t"
    "s_cselect_b32 s69, s44, s69\n\t"
    "s_mul_i32 m0, s69, 1040\n\t"
    "s_lshl_b32 s44, s69, 11\n\t"
    "buffer_load_dwordx4 v126, %15, s44 offen lds\n\t"
    "s_add_u32 s69, s69, 4\n\t"
    "s_sub_u32 s44, s69, 125\n\t"
    "s_cmp_ge_u32 s69, 125\n\t"
    "s_cselect_b32 s69, s44, s69\n\t"
    "s_mul_i32 m0, s69, 1040\n\t"
    "s_lshl_b32 s44, s69, 11\n\t"
    "buffer_load_dwordx4 v126, %15, s44 offen lds\n\t"
    "s_add_u32 s69, s69, 4\n\t"
    "s_sub_u32 s44, s69, 125\n\t"
    "s_cmp_ge_u32 s69, 125\n\t"
    "s_cselect_b32 s69, s44, s69\n\t"
    "s_mul_i32 m0, s69, 1040\n\t"
    "s_lshl_b32 s44, s69, 11\n\t"
    "buffer_load_dwordx4 v126, %15, s44 offen lds\n\t"
    "s_add_u32 s69, s69, 4\n\t"
    "s_sub_u32 s44, s69, 125\n\t"
    "s_cmp_ge_u32 s69, 125\n\t"
    "s_cselect_b32 s69, s44, s69\n\t"
    "s_mul_i32 m0, s69, 1040\n\t"
    "s_lshl_b32 s44, s69, 11\n\t"
    "buffer_load_dwordx4 v126, %15, s44 offen lds\n\t"
    "s_add_u32 s69, s69, 4\n\t"
    "s_sub_u32 s44, s69, 125\n\t"
    "s_cmp_ge_u32 s69, 125\n\t"
    "s_cselect_b32 s69, s44, s69\n\t"
    "s_branch L_ab_%=\n\t"
    "L_adma_%=:\n\t"
    "s_sub_u32 s46, %16, 4\n\t"
    "s_add_u32 s69, s51, s46\n\t"
    "s_sub_u32 s44, s69, 125\n\t"
    "s_cmp_ge_u32 s69, 125\n\t"
    "s_cselect_b32 s69, s44, s69\n\t"
    "s_mul_i32 m0, s69, 1040\n\t"
    "s_lshl_b32 s44, s69, 11\n\t"
    "buffer_load_dwordx4 v126, %15, s44 offen lds\n\t"
    "s_add_u32 s69, s69, 2\n\t"
    "s_sub_u32 s44, s69, 125\n\t"
    "s_cmp_ge_u32 s69, 125\n\t"
    "s_cselect_b32 s69, s44, s69\n\t"
    "s_mul_i32 m0, s69, 1040\n\t"
    "s_lshl_b32 s44, s69, 11\n\t"
    "buffer_load_dwordx4 v126, %15, s44 offen lds\n\t"
    "s_add_u32 s69, s69, 2\n\t"
    "s_sub_u32 s44, s69, 125\n\t"
    "s_cmp_ge_u32 s69, 125\n\t"
    "s_cselect_b32 s69, s44, s69\n\t"
    "s_barrier\n\t"
    "s_load_dwordx2 s[72:73], %6, 0\n\t"
    "s_load_dwordx2 s[88:89], %7, 0\n\t"
    "s_load_dword s91, %8, 0\n\t"
    "s_mul_i32 m0, s69, 1040\n\t"
    "s_lshl_b32 s44, s69, 11\n\t"
    "buffer_load_dwordx4 v126, %15, s44 offen lds\n\t"
    "s_add_u32 s69, s69, 2\n\t"
    "s_sub_u32 s44, s69, 125\n\t"
    "s_cmp_ge_u32 s69, 125\n\t"
    "s_cselect_b32 s69, s44, s69\n\t"
    "s_mul_i32 m0, s69, 1040\n\t"
    "s_lshl_b32 s44, s69, 11\n\t"
    "buffer_load_dwordx4 v126, %15, s44 offen lds\n\t"
    "s_add_u32 s69, s69, 2\n\t"
    "s_sub_u32 s44, s69, 125\n\t"
    "s_cmp_ge_u32 s69, 125\n\t"
    "s_cselect_b32 s69, s44, s69\n\t"
    "s_mul_i32 m0, s69, 1040\n\t"
    "s_lshl_b32 s44, s69, 11\n\t"
    "buffer_load_dwordx4 v126, %15, s44 offen lds\n\t"
    "s_add_u32 s69, s69, 2\n\t"
    "s_sub_u32 s44, s69, 125\n\t"
    "s_cmp_ge_u32 s69, 125\n\t"
    "s_cselect_b32 s69, s44, s69\n\t"
    "s_mul_i32 m0, s69, 1040\n\t"
    "s_lshl_b32 s44, s69, 11\n\t"
    "buffer_load_dwordx4 v126, %15, s44 offen lds\n\t"
    "s_add_u32 s69, s69, 2\n\t"
    "s_sub_u32 s44, s69, 125\n\t"
    "s_cmp_ge_u32 s69, 125\n\t"
    "s_cselect_b32 s69, s44, s69\n\t"
    "s_mul_i32 m0, s69, 1040\n\t"
    "s_lshl_b32 s44, s69, 11\n\t"
    "buffer_load_dwordx4 v126, %15, s44 offen lds\n\t"
    "s_add_u32 s69, s69, 2\n\t"
    "s_sub_u32 s44, s69, 125\n\t"
    "s_cmp_ge_u32 s69, 125\n\t"
    "s_cselect_b32 s69, s44, s69\n\t"
    "s_mul_i32 m0, s69, 1040\n\t"
    "s_lshl_b32 s44, s69, 11\n\t"
    "buffer_load_dwordx4 v126, %15, s44 offen lds\n\t"
    "s_add_u32 s69, s69, 2\n\t"
    "s_sub_u32 s44, s69, 125\n\t"
    "s_cmp_ge_u32 s69, 125\n\t"
    "s_cselect_b32 s69, s44, s69\n\t"
    "s_mul_i32 m0, s69, 1040\n\t"
    "s_lshl_b32 s44, s69, 11\n\t"
    "buffer_load_dwordx4 v126, %15, s44 offen lds\n\t"
    "s_add_u32 s69, s69, 2\n\t"
    "s_sub_u32 s44, s69, 125\n\t"
    "s_cmp_ge_u32 s69, 125\n\t"
    "s_cselect_b32 s69, s44, s69\n\t"
    "s_mul_i32 m0, s69, 1040\n\t"
    "s_lshl_b32 s44, s69, 11\n\t"
    "buffer_load_dwordx4 v126, %15, s44 offen lds\n\t"
    "s_add_u32 s69, s69, 2\n\t"
    "s_sub_u32 s44, s69, 125\n\t"
    "s_cmp_ge_u32 s69, 125\n\t"
    "s_cselect_b32 s69, s44, s69\n\t"
    "s_mul_i32 m0, s69, 1040\n\t"
    "s_lshl_b32 s44, s69, 11\n\t"
    "buffer_load_dwordx4 v126, %15, s44 offen lds\n\t"
    "s_add_u32 s69, s69, 2\n\t"
    "s_sub_u32 s44, s69, 125\n\t"
    "s_cmp_ge_u32 s69, 125\n\t"
    "s_cselect_b32 s69, s44, s69\n\t"
    "s_mul_i32 m0, s69, 1040\n\t"
    "s_lshl_b32 s44, s69, 11\n\t"
    "buffer_load_dwordx4 v126, %15, s44 offen lds\n\t"
    "s_add_u32 s69, s69, 2\n\t"
    "s_sub_u32 s44, s69, 125\n\t"
    "s_cmp_ge_u32 s69, 125\n\t"
    "s_cselect_b32 s69, s44, s69\n\t"
    "v_bfe_u32 v154, v147, 2, 2\n\t"
    "v_and_b32 v155, 3, v147\n\t"
    "v_mul_u32_u24 v154, 25, v154\n\t"
    "v_mad_u32_u24 v154, v155, 6, v154\n\t"
    "v_lshlrev_b32 v153, 2, v154\n\t"
    "s_and_b32 s53, s48, 0xff\n\t"
    "s_bfe_u32 s55, s48, 0x80008\n\t"
    "s_add_u32 s54, s53, s55\n\t"
    "s_add_u32 s55, s54, s55\n\t"
    "s_lshr_b32 s64, s48, 16\n\t"
    "v_cmp_eq_u32 s[62:63], 0, v155\n\t"
    "v_and_b32 v151, 48, v147\n\t"
    "v_or_b32 v155, 0x100000, v151\n\t"
    "v_cndmask_b32 v152, v155, v151, s[62:63]\n\t"
    "v_add_u32 v95, 140288, v122\n\t"
    "s_mul_i32 m0, s69, 1040\n\t"
    "s_lshl_b32 s44, s69, 11\n\t"
    "buffer_load_dwordx4 v126, %15, s44 offen lds\n\t"
    "s_add_u32 s69, s69, 2\n\t"
    "s_sub_u32 s44, s69, 125\n\t"
    "s_cmp_ge_u32 s69, 125\n\t"
    "s_cselect_b32 s69, s44, s69\n\t"
    "s_mul_i32 m0, s69, 1040\n\t"
    "s_lshl_b32 s44, s69, 11\n\t"
    "buffer_load_dwordx4 v126, %15, s44 offen lds\n\t"
    "s_add_u32 s69, s69, 2\n\t"
    "s_sub_u32 s44, s69, 125\n\t"
    "s_cmp_ge_u32 s69, 125\n\t"
    "s_cselect_b32 s69, s44, s69\n\t"
    "s_mul_i32 m0, s69, 1040\n\t"
    "s_lshl_b32 s44, s69, 11\n\t"
    "buffer_load_dwordx4 v126, %15, s44 offen lds\n\t"
    "s_add_u32 s69, s69, 2\n\t"
    "s_sub_u32 s44, s69, 125\n\t"
    "s_cmp_ge_u32 s69, 125\n\t"
    "s_cselect_b32 s69, s44, s69\n\t"
    "s_mul_i32 m0, s69, 1040\n\t"
    "s_lshl_b32 s44, s69, 11\n\t"
    "buffer_load_dwordx4 v126, %15, s44 offen lds\n\t"
    "s_add_u32 s69, s69, 2\n\t"
    "s_sub_u32 s44, s69, 125\n\t"
    "s_cmp_ge_u32 s69, 125\n\t"
    "s_cselect_b32 s69, s44, s69\n\t"
    "s_mul_i32 m0, s69, 1040\n\t"
    "s_lshl_b32 s44, s69, 11\n\t"
    "buffer_load_dwordx4 v126, %15, s44 offen lds\n\t"
    "s_add_u32 s69, s69, 2\n\t"
    "s_sub_u32 s44, s69, 125\n\t"
    "s_cmp_ge_u32 s69, 125\n\t"
    "s_cselect_b32 s69, s44, s69\n\t"
    "s_mul_i32 m0, s69, 1040\n\t"
    "s_lshl_b32 s44, s69, 11\n\t"
    "buffer_load_dwordx4 v126, %15, s44 offen lds\n\t"
    "s_add_u32 s69, s69, 2\n\t"
    "s_sub_u32 s44, s69, 125\n\t"
    "s_cmp_ge_u32 s69, 125\n\t"
    "s_cselect_b32 s69, s44, s69\n\t"
    "s_mul_i32 m0, s69, 1040\n\t"
    "s_lshl_b32 s44, s69, 11\n\t"
    "buffer_load_dwordx4 v126, %15, s44 offen lds\n\t"
    "s_add_u32 s69, s69, 2\n\t"
    "s_sub_u32 s44, s69, 125\n\t"
    "s_cmp_ge_u32 s69, 125\n\t"
    "s_cselect_b32 s69, s44, s69\n\t"
    "s_mul_i32 m0, s69, 1040\n\t"
    "s_lshl_b32 s44, s69, 11\n\t"
    "buffer_load_dwordx4 v126, %15, s44 offen lds\n\t"
    "s_add_u32 s69, s69, 2\n\t"
    "s_sub_u32 s44, s69, 125\n\t"
    "s_cmp_ge_u32 s69, 125\n\t"
    "s_cselect_b32 s69, s44, s69\n\t"
    "s_mul_i32 m0, s69, 1040\n\t"
    "s_lshl_b32 s44, s69, 11\n\t"
    "buffer_load_dwordx4 v126, %15, s44 offen lds\n\t"
    "s_add_u32 s69, s69, 2\n\t"
    "s_sub_u32 s44, s69, 125\n\t"
    "s_cmp_ge_u32 s69, 125\n\t"
    "s_cselect_b32 s69, s44, s69\n\t"
    "s_mul_i32 m0, s69, 1040\n\t"
    "s_lshl_b32 s44, s69, 11\n\t"
    "buffer_load_dwordx4 v126, %15, s44 offen lds\n\t"
    "s_add_u32 s69, s69, 2\n\t"
    "s_sub_u32 s44, s69, 125\n\t"
    "s_cmp_ge_u32 s69, 125\n\t"
    "s_cselect_b32 s69, s44, s69\n\t"
    "s_mul_i32 m0, s69, 1040\n\t"
    "s_lshl_b32 s44, s69, 11\n\t"
    "buffer_load_dwordx4 v126, %15, s44 offen lds\n\t"
    "s_add_u32 s69, s69, 2\n\t"
    "s_sub_u32 s44, s69, 125\n\t"
    "s_cmp_ge_u32 s69, 125\n\t"
    "s_cselect_b32 s69, s44, s69\n\t"
    "s_mul_i32 m0, s69, 1040\n\t"
    "s_lshl_b32 s44, s69, 11\n\t"
    "buffer_load_dwordx4 v126, %15, s44 offen lds\n\t"
    "s_add_u32 s69, s69, 2\n\t"
    "s_sub_u32 s44, s69, 125\n\t"
    "s_cmp_ge_u32 s69, 125\n\t"
    "s_cselect_b32 s69, s44, s69\n\t"
    "s_mul_i32 m0, s69, 1040\n\t"
    "s_lshl_b32 s44, s69, 11\n\t"
    "buffer_load_dwordx4 v126, %15, s44 offen lds\n\t"
    "s_add_u32 s69, s69, 2\n\t"
    "s_sub_u32 s44, s69, 125\n\t"
    "s_cmp_ge_u32 s69, 125\n\t"
    "s_cselect_b32 s69, s44, s69\n\t"
    "s_mul_i32 m0, s69, 1040\n\t"
    "s_lshl_b32 s44, s69, 11\n\t"
    "buffer_load_dwordx4 v126, %15, s44 offen lds\n\t"
    "s_add_u32 s69, s69, 2\n\t"
    "s_sub_u32 s44, s69, 125\n\t"
    "s_cmp_ge_u32 s69, 125\n\t"
    "s_cselect_b32 s69, s44, s69\n\t"
    "s_mul_i32 m0, s69, 1040\n\t"
    "s_lshl_b32 s44, s69, 11\n\t"
    "buffer_load_dwordx4 v126, %15, s44 offen lds\n\t"
    "s_add_u32 s69, s69, 2\n\t"
    "s_sub_u32 s44, s69, 125\n\t"
    "s_cmp_ge_u32 s69, 125\n\t"
    "s_cselect_b32 s69, s44, s69\n\t"
    "s_mul_i32 m0, s69, 1040\n\t"
    "s_lshl_b32 s44, s69, 11\n\t"
    "buffer_load_dwordx4 v126, %15, s44 offen lds\n\t"
    "s_add_u32 s69, s69, 2\n\t"
    "s_sub_u32 s44, s69, 125\n\t"
    "s_cmp_ge_u32 s69, 125\n\t"
    "s_cselect_b32 s69, s44, s69\n\t"
    "s_mul_i32 m0, s69, 1040\n\t"
    "s_lshl_b32 s44, s69, 11\n\t"
    "buffer_load_dwordx4 v126, %15, s44 offen lds\n\t"
    "s_add_u32 s69, s69, 2\n\t"
    "s_sub_u32 s44, s69, 125\n\t"
    "s_cmp_ge_u32 s69, 125\n\t"
    "s_cselect_b32 s69, s44, s69\n\t"
    "s_mul_i32 m0, s69, 1040\n\t"
    "s_lshl_b32 s44, s69, 11\n\t"
    "buffer_load_dwordx4 v126, %15, s44 offen lds\n\t"
    "s_add_u32 s69, s69, 2\n\t"
    "s_sub_u32 s44, s69, 125\n\t"
    "s_cmp_ge_u32 s69, 125\n\t"
    "s_cselect_b32 s69, s44, s69\n\t"
    "s_mul_i32 m0, s69, 1040\n\t"
    "s_lshl_b32 s44, s69, 11\n\t"
    "buffer_load_dwordx4 v126, %15, s44 offen lds\n\t"
    "s_add_u32 s69, s69, 2\n\t"
    "s_sub_u32 s44, s69, 125\n\t"
    "s_cmp_ge_u32 s69, 125\n\t"
    "s_cselect_b32 s69, s44, s69\n\t"
    "s_mul_i32 m0, s69, 1040\n\t"
    "s_lshl_b32 s44, s69, 11\n\t"
    "buffer_load_dwordx4 v126, %15, s44 offen lds\n\t"
    "s_add_u32 s69, s69, 2\n\t"
    "s_sub_u32 s44, s69, 125\n\t"
    "s_cmp_ge_u32 s69, 125\n\t"
    "s_cselect_b32 s69, s44, s69\n\t"
    "s_mul_i32 m0, s69, 1040\n\t"
    "s_lshl_b32 s44, s69, 11\n\t"
    "buffer_load_dwordx4 v126, %15, s44 offen lds\n\t"
    "s_add_u32 s69, s69, 2\n\t"
    "s_sub_u32 s44, s69, 125\n\t"
    "s_cmp_ge_u32 s69, 125\n\t"
    "s_cselect_b32 s69, s44, s69\n\t"
    "s_mul_i32 m0, s69, 1040\n\t"
    "s_lshl_b32 s44, s69, 11\n\t"
    "buffer_load_dwordx4 v126, %15, s44 offen lds\n\t"
    "s_add_u32 s69, s69, 2\n\t"
    "s_sub_u32 s44, s69, 125\n\t"
    "s_cmp_ge_u32 s69, 125\n\t"
    "s_cselect_b32 s69, s44, s69\n\t"
    "s_mul_i32 m0, s69, 1040\n\t"
    "s_lshl_b32 s44, s69, 11\n\t"
    "buffer_load_dwordx4 v126, %15, s44 offen lds\n\t"
    "s_add_u32 s69, s69, 2\n\t"
    "s_sub_u32 s44, s69, 125\n\t"
    "s_cmp_ge_u32 s69, 125\n\t"
    "s_cselect_b32 s69, s44, s69\n\t"
    "s_mul_i32 m0, s69, 1040\n\t"
    "s_lshl_b32 s44, s69, 11\n\t"
    "buffer_load_dwordx4 v126, %15, s44 offen lds\n\t"
    "s_add_u32 s69, s69, 2\n\t"
    "s_sub_u32 s44, s69, 125\n\t"
    "s_cmp_ge_u32 s69, 125\n\t"
    "s_cselect_b32 s69, s44, s69\n\t"
    "s_mul_i32 m0, s69, 1040\n\t"
    "s_lshl_b32 s44, s69, 11\n\t"
    "buffer_load_dwordx4 v126, %15, s44 offen lds\n\t"
    "s_add_u32 s69, s69, 2\n\t"
    "s_sub_u32 s44, s69, 125\n\t"
    "s_cmp_ge_u32 s69, 125\n\t"
    "s_cselect_b32 s69, s44, s69\n\t"
    "s_mul_i32 m0, s69, 1040\n\t"
    "s_lshl_b32 s44, s69, 11\n\t"
    "buffer_load_dwordx4 v126, %15, s44 offen lds\n\t"
    "s_add_u32 s69, s69, 2\n\t"
    "s_sub_u32 s44, s69, 125\n\t"
    "s_cmp_ge_u32 s69, 125\n\t"
    "s_cselect_b32 s69, s44, s69\n\t"
    "s_mul_i32 m0, s69, 1040\n\t"
    "s_lshl_b32 s44, s69, 11\n\t"
    "buffer_load_dwordx4 v126, %15, s44 offen lds\n\t"
    "s_add_u32 s69, s69, 2\n\t"
    "s_sub_u32 s44, s69, 125\n\t"
    "s_cmp_ge_u32 s69, 125\n\t"
    "s_cselect_b32 s69, s44, s69\n\t"
    "s_mul_i32 m0, s69, 1040\n\t"
    "s_lshl_b32 s44, s69, 11\n\t"
    "buffer_load_dwordx4 v126, %15, s44 offen lds\n\t"
    "s_add_u32 s69, s69, 2\n\t"
    "s_sub_u32 s44, s69, 125\n\t"
    "s_cmp_ge_u32 s69, 125\n\t"
    "s_cselect_b32 s69, s44, s69\n\t"
    "s_mul_i32 m0, s69, 1040\n\t"
    "s_lshl_b32 s44, s69, 11\n\t"
    "buffer_load_dwordx4 v126, %15, s44 offen lds\n\t"
    "s_add_u32 s69, s69, 2\n\t"
    "s_sub_u32 s44, s69, 125\n\t"
    "s_cmp_ge_u32 s69, 125\n\t"
    "s_cselect_b32 s69, s44, s69\n\t"
    "s_mul_i32 m0, s69, 1040\n\t"
    "s_lshl_b32 s44, s69, 11\n\t"
    "buffer_load_dwordx4 v126, %15, s44 offen lds\n\t"
    "s_add_u32 s69, s69, 2\n\t"
    "s_sub_u32 s44, s69, 125\n\t"
    "s_cmp_ge_u32 s69, 125\n\t"
    "s_cselect_b32 s69, s44, s69\n\t"
    "s_call_b64 s[98:99], L_setup_%=\n\t"
    "L_ab_%=:\n\t"
    "s_mul_i32 s66, s53, 400\n\t"
    "buffer_load_dwordx3 v[128:130], v153, %0, s66 offen\n\t"
    "buffer_load_dwordx4 v[132:135], v153, %0, s66 offen offset:12\n\t"
    "s_mul_i32 s66, s54, 400\n\t"
    "buffer_load_dwordx3 v[136:138], v153, %0, s66 offen\n\t"
    "buffer_load_dwordx4 v[140:143], v153, %0, s66 offen offset:12\n\t"
    "L_asp8_%=:\n\t"
    "ds_read_b32 v119, v117 offset:4\n\t"
    "s_waitcnt lgkmcnt(0)\n\t"
    "v_readfirstlane_b32 s46, v119\n\t"
    "s_nop 0\n\t"
    "s_cmp_ge_u32 s46, 4\n\t"
    "s_cbranch_scc1 L_ago8_%=\n\t"
    "s_sleep 1\n\t"
    "s_branch L_asp8_%=\n\t"
    "L_ago8_%=:\n\t"
    "ds_read_b128 v[0:3], v95 offset:0\n\t"
    "ds_read_b128 v[4:7], v95 offset:1024\n\t"
    "ds_read_b128 v[8:11], v95 offset:2048\n\t"
    "ds_read_b128 v[12:15], v95 offset:3072\n\t"
    "ds_read_b128 v[16:19], v95 offset:4096\n\t"
    "ds_read_b128 v[20:23], v95 offset:5120\n\t"
    "ds_read_b128 v[24:27], v95 offset:6144\n\t"
    "ds_read_b128 v[28:31], v95 offset:7168\n\t"
    "ds_read_b128 v[32:35], v95 offset:8192\n\t"
    "ds_read_b128 v[36:39], v95 offset:9216\n\t"
    "ds_read_b128 v[40:43], v95 offset:10240\n\t"
    "ds_read_b128 v[44:47], v95 offset:11264\n\t"
    "ds_read_b128 v[48:51], v95 offset:12288\n\t"
    "ds_read_b128 v[52:55], v95 offset:13312\n\t"
    "ds_read_b128 v[56:59], v95 offset:14336\n\t"
    "ds_read_b128 v[60:63], v95 offset:15360\n\t"
    "ds_read_b128 v[64:67], v95 offset:16384\n\t"
    "ds_read_b128 v[68:71], v95 offset:17408\n\t"
    "ds_read_b128 v[72:75], v95 offset:18432\n\t"
    "ds_read_b128 v[76:79], v95 offset:19456\n\t"
    "ds_read_b128 v[80:83], v95 offset:20480\n\t"
    "ds_read_b128 v[84:87], v95 offset:21504\n\t"
    "ds_read_b128 v[88:91], v95 offset:22528\n\t"
    "s_waitcnt vmcnt(4)\n\t"
    "ds_add_u32 v117, v118 offset:8\n\t"
    "s_waitcnt vmcnt(0)\n\t"
    "s_branch L_awd_%=\n\t"
    "s_waitcnt vmcnt(4)\n\t"
    "ds_add_u32 v117, v118 offset:8\n\t"
    "s_branch L_await_%=\n\t"
    "L_setup_%=:\n\t"
    "v_mbcnt_lo_u32_b32 v100, -1, 0\n\t"
    "v_mbcnt_hi_u32_b32 v100, -1, v100\n\t"
    "s_lshl_b32 s61, %16, 15\n\t"
    "s_add_u32 s61, s61, 16515072\n\t"
    "v_lshlrev_b32 v127, 4, v100\n\t"
    "s_lshr_b32 s37, %14, 4\n\t"
    "s_and_b32 s39, %14, 7\n\t"
    "v_and_b32 v101, 15, v100\n\t"
    "s_lshl_b32 s39, s39, 4\n\t"
    "s_add_u32 s39, s39, s37\n\t"
    "s_mul_i32 s65, %16, 125\n\t"
    "v_add_u32 v155, s65, v101\n\t"
    "s_mul_i32 s63, s39, 102400\n\t"
    "s_add_u32 s63, s63, 3407872\n\t"
    "v_lshrrev_b32 v102, 2, v101\n\t"
    "s_and_b32 s37, s37, 7\n\t"
    "s_lshl_b32 s37, s37, 11\n\t"
    "v_lshrrev_b32 v103, 3, v100\n\t"
    "s_mul_i32 s36, s39, 65536\n\t"
    "s_bfe_u32 s65, %14, 0x10003\n\t"
    "v_and_b32 v103, 2, v103\n\t"
    "s_lshl_b32 s38, s65, 15\n\t"
    "s_add_u32 s36, s36, s38\n\t"
    "v_and_b32 v104, 1, v100\n\t"
    "s_lshl_b32 s38, %16, 12\n\t"
    "s_add_u32 s36, s36, s38\n\t"
    "v_lshlrev_b32 v104, 4, v104\n\t"
    "s_add_u32 s36, s36, 17825792\n\t"
    "v_mov_b32 v101, 0x3f80\n\t"
    "v_lshlrev_b32 v104, v104, v101\n\t"
    "v_cmp_eq_u32 vcc, v102, v103\n\t"
    "s_waitcnt lgkmcnt(0)\n\t"
    "s_lshr_b32 s39, s91, %9\n\t"
    "s_lshl_b32 s39, s39, 16\n\t"
    "v_cndmask_b32 v120, 0, v104, vcc\n\t"
    "v_cndmask_b32 v121, 0, v104, vcc\n\t"
    "v_add_u32 v103, 1, v103\n\t"
    "v_cmp_eq_u32 vcc, v102, v103\n\t"
    "s_lshl_b32 s40, s88, 16\n\t"
    "s_and_b32 s41, s88, 0xffff0000\n\t"
    "v_cndmask_b32 v122, 0, v104, vcc\n\t"
    "v_cndmask_b32 v123, 0, v104, vcc\n\t"
    "v_and_b32 v101, 2, v100\n\t"
    "v_mov_b32 v102, 0x44444444\n\t"
    "v_mov_b32 v103, 0xeeeeeeee\n\t"
    "v_cmp_eq_u32 vcc, 0, v101\n\t"
    "s_lshl_b32 s42, s89, 16\n\t"
    "s_and_b32 s43, s89, 0xffff0000\n\t"
    "v_cndmask_b32 v124, v103, v102, vcc\n\t"
    "v_lshrrev_b32 v101, 4, v100\n\t"
    "v_mul_u32_u24 v101, 8000, v101\n\t"
    "v_lshl_add_u32 v125, v155, 3, v101\n\t"
    "v_add_u32 v125, 64000, v125\n\t"
    "s_lshl_b32 s70, s72, 16\n\t"
    "s_and_b32 s71, s72, 0xffff0000\n\t"
    "s_lshl_b32 s72, s73, 16\n\t"
    "s_and_b32 s73, s73, 0xffff0000\n\t"
    "s_mov_b32 s67, 0xffff0000\n\t"
    "s_and_b32 s65, %14, 8\n\t"
    "s_add_u32 s38, s65, %16\n\t"
    "s_mul_i32 s38, s38, 212992\n\t"
    "s_add_u32 s38, s38, 196608\n\t"
    "s_add_u32 s84, s37, s38\n\t"
    "s_add_u32 s37, s37, 0x800\n\t"
    "s_and_b32 s37, s37, 0x3800\n\t"
    "s_add_u32 s85, s37, s38\n\t"
    "s_add_u32 s37, s37, 0x800\n\t"
    "s_and_b32 s37, s37, 0x3800\n\t"
    "s_add_u32 s86, s37, s38\n\t"
    "s_add_u32 s37, s37, 0x800\n\t"
    "s_and_b32 s37, s37, 0x3800\n\t"
    "s_add_u32 s87, s37, s38\n\t"
    "s_add_u32 s37, s37, 0x800\n\t"
    "s_and_b32 s37, s37, 0x3800\n\t"
    "s_add_u32 s88, s37, s38\n\t"
    "s_add_u32 s37, s37, 0x800\n\t"
    "s_and_b32 s37, s37, 0x3800\n\t"
    "s_add_u32 s89, s37, s38\n\t"
    "s_add_u32 s37, s37, 0x800\n\t"
    "s_and_b32 s37, s37, 0x3800\n\t"
    "s_add_u32 s90, s37, s38\n\t"
    "s_add_u32 s37, s37, 0x800\n\t"
    "s_and_b32 s37, s37, 0x3800\n\t"
    "s_add_u32 s91, s37, s38\n\t"
    "s_mul_i32 s94, s65, 425984\n\t"
    "s_sub_u32 s94, s94, 1703936\n\t"
    "s_sub_u32 s95, 0x8000, s94\n\t"
    "s_lshl_b32 s97, s65, 11\n\t"
    "s_sub_u32 s97, s97, 229376\n\t"
    "s_mov_b32 s92, 64000\n\t"
    "s_mov_b32 s93, -64000\n\t"
    "s_mov_b32 s96, 0x2000\n\t"
    "s_mov_b32 s62, 2\n\t"
    "v_rcp_f32 v101, s40\n\t"
    "v_rcp_f32 v102, s41\n\t"
    "v_rcp_f32 v103, s42\n\t"
    "v_rcp_f32 v104, s43\n\t"
    "v_mov_b32 v100, 0x7f7fffff\n\t"
    "v_med3_f32 v101, v101, -v100, v100\n\t"
    "v_med3_f32 v102, v102, -v100, v100\n\t"
    "v_med3_f32 v103, v103, -v100, v100\n\t"
    "v_med3_f32 v104, v104, -v100, v100\n\t"
    "v_readfirstlane_b32 s40, v101\n\t"
    "v_readfirstlane_b32 s41, v102\n\t"
    "v_readfirstlane_b32 s42, v103\n\t"
    "v_readfirstlane_b32 s43, v104\n\t"
    "s_setpc_b64 s[98:99]\n\t"
    "L_aside_%=:\n\t"
    "s_setprio 3\n\t"
    "v_cmp_gt_u32 vcc, 48, v147\n\t"
    "v_mov_b32 v133, 0x40000000\n\t"
    "v_lshrrev_b32 v134, 3, v147\n\t"
    "v_bfe_u32 v135, v147, 2, 1\n\t"
    "v_and_b32 v130, 3, v147\n\t"
    "v_lshl_add_u32 v132, v135, 5, v130\n\t"
    "v_mul_u32_u24 v64, 400, v132\n\t"
    "v_lshl_add_u32 v64, v134, 5, v64\n\t"
    "s_movk_i32 s50, 0\n\t"
    "buffer_load_dwordx4 v[0:3], v64, %0, s50 offen offset:16\n\t"
    "buffer_load_dwordx4 v[4:7], v64, %0, s50 offen offset:32\n\t"
    "v_lshlrev_b32 v66, 14, v134\n\t"
    "v_lshl_add_u32 v66, v132, 4, v66\n\t"
    "v_lshlrev_b32 v68, 8, v134\n\t"
    "v_lshl_add_u32 v68, v135, 2, v68\n\t"
    "v_add_u32 v68, 130048, v68\n\t"
    "v_cndmask_b32 v68, v133, v68, vcc\n\t"
    "v_cndmask_b32 v66, v133, v66, vcc\n\t"
    "v_lshrrev_b32 v134, 2, v147\n\t"
    "v_bfe_u32 v135, v147, 1, 1\n\t"
    "v_and_b32 v130, 1, v147\n\t"
    "v_lshl_add_u32 v132, v135, 5, v130\n\t"
    "v_mul_u32_u24 v65, 400, v132\n\t"
    "v_lshl_add_u32 v65, v134, 5, v65\n\t"
    "v_lshlrev_b32 v67, 14, v134\n\t"
    "v_lshl_add_u32 v67, v132, 4, v67\n\t"
    "v_lshlrev_b32 v69, 8, v134\n\t"
    "v_lshl_add_u32 v69, v135, 2, v69\n\t"
    "v_add_u32 v69, 130048, v69\n\t"
    "v_cndmask_b32 v69, v133, v69, vcc\n\t"
    "v_cndmask_b32 v67, v133, v67, vcc\n\t"
    "s_barrier\n\t"
    "s_load_dwordx2 s[72:73], %6, 0\n\t"
    "s_load_dwordx2 s[88:89], %7, 0\n\t"
    "s_load_dword s91, %8, 0\n\t"
    "v_lshlrev_b32 v128, 4, v147\n\t"
    "v_add_u32 v128, 133120, v128\n\t"
    "v_and_b32 v130, 1, v147\n\t"
    "v_cndmask_b32 v128, v133, v128, vcc\n\t"
    "v_mul_u32_u24 v127, 7, v130\n\t"
    "v_cmp_eq_u32 vcc, 1, v130\n\t"
    "v_mov_b32 v126, 0x07060100\n\t"
    "v_mov_b32 v133, 0x05040100\n\t"
    "v_mov_b32 v136, 1\n\t"
    "v_cndmask_b32 v126, v126, v133, vcc\n\t"
    "s_mov_b32 s36, 0\n\t"
    "s_mov_b32 s37, 8000\n\t"
    "s_mov_b32 s38, 16000\n\t"
    "s_mov_b32 s39, 24000\n\t"
    "s_mov_b32 s40, 32000\n\t"
    "s_mov_b32 s41, 40000\n\t"
    "s_mov_b32 s42, 48000\n\t"
    "s_mov_b32 s43, 56000\n\t"
    "s_movk_i32 s44, 0xf8\n\t"
    "s_mov_b32 s45, 0x01010000\n\t"
    "s_mov_b32 s46, 0x020c000c\n\t"
    "s_lshr_b32 s49, %14, 4\n\t"
    "s_mul_i32 s49, s49, 212992\n\t"
    "s_and_b32 s50, %14, 15\n\t"
    "s_lshl_b32 s50, s50, 10\n\t"
    "s_add_u32 s49, s49, s50\n\t"
    "v_lshrrev_b32 v134, 4, v147\n\t"
    "v_bfe_u32 v135, v147, 2, 2\n\t"
    "v_and_b32 v130, 3, v147\n\t"
    "v_lshlrev_b32 v137, 17, v134\n\t"
    "v_lshl_add_u32 v137, v135, 10, v137\n\t"
    "v_lshl_add_u32 v137, v130, 4, v137\n\t"
    "v_add_u32 v138, 0x1000, v137\n\t"
    "v_mul_u32_u24 v141, 0x210, v134\n\t"
    "v_lshl_add_u32 v141, v135, 6, v141\n\t"
    "v_lshl_add_u32 v141, v130, 4, v141\n\t"
    "v_add_u32 v141, 134656, v141\n\t"
    "v_mul_u32_u24 v142, 0x210, v130\n\t"
    "v_lshl_add_u32 v142, v135, 6, v142\n\t"
    "v_lshl_add_u32 v142, v134, 3, v142\n\t"
    "v_add_u32 v142, 134656, v142\n\t"
    "s_mov_b32 s50, 102400\n\t"
    "v_mul_u32_u24 v139, s50, v134\n\t"
    "v_and_b32 v132, 15, v147\n\t"
    "v_lshl_add_u32 v139, v132, 4, v139\n\t"
    "v_add_u32 v140, 409600, v139\n\t"
    "s_lshr_b32 s50, %14, 4\n\t"
    "s_lshl_b32 s47, s50, 13\n\t"
    "s_lshl_b32 s48, s50, 8\n\t"
    "s_and_b32 s50, %14, 7\n\t"
    "s_lshl_b32 s51, s50, 7\n\t"
    "s_add_u32 s47, s47, s51\n\t"
    "s_mul_i32 s51, s50, 1638400\n\t"
    "s_add_u32 s48, s48, s51\n\t"
    "s_bfe_u32 s50, %14, 0x10003\n\t"
    "s_lshl_b32 s51, s50, 19\n\t"
    "s_add_u32 s47, s47, s51\n\t"
    "s_lshl_b32 s51, s50, 12\n\t"
    "s_add_u32 s48, s48, s51\n\t"
    "s_add_u32 s48, s48, 3407872\n\t"
    "s_waitcnt vmcnt(2)\n\t"
    "v_lshlrev_b32 v134, 20, v147\n\t"
    "v_add_u32 v134, 2068, v134\n\t"
    "ds_add_u32 v134, v136\n\t"
    "v_mov_b32 v110, 65664\n\t"
    "v_mov_b32 v111, 131136\n\t"
    "v_mov_b32 v112, 262176\n\t"
    "v_mov_b32 v113, 524304\n\t"
    "v_mov_b32 v114, 1048584\n\t"
    "v_mov_b32 v115, 2097156\n\t"
    "v_mov_b32 v116, 4194306\n\t"
    "v_mov_b32 v117, 8388609\n\t"
    "s_waitcnt vmcnt(0)\n\t"
    "v_lshl_add_u32 v76, v0, 3, s36\n\t"
    "v_lshl_add_u32 v77, v1, 3, s37\n\t"
    "v_lshl_add_u32 v78, v2, 3, s38\n\t"
    "v_lshl_add_u32 v79, v3, 3, s39\n\t"
    "v_lshl_add_u32 v80, v4, 3, s40\n\t"
    "v_lshl_add_u32 v81, v5, 3, s41\n\t"
    "v_lshl_add_u32 v82, v6, 3, s42\n\t"
    "v_lshl_add_u32 v83, v7, 3, s43\n\t"
    "v_and_or_b32 v92, v76, s44, v68\n\t"
    "ds_or_rtn_b32 v118, v92, v110\n\t"
    "v_and_or_b32 v93, v77, s44, v68\n\t"
    "ds_or_rtn_b32 v119, v93, v111\n\t"
    "v_and_or_b32 v94, v78, s44, v68\n\t"
    "ds_or_rtn_b32 v120, v94, v112\n\t"
    "v_and_or_b32 v95, v79, s44, v68\n\t"
    "ds_or_rtn_b32 v121, v95, v113\n\t"
    "v_and_or_b32 v96, v80, s44, v68\n\t"
    "ds_or_rtn_b32 v122, v96, v114\n\t"
    "v_and_or_b32 v97, v81, s44, v68\n\t"
    "ds_or_rtn_b32 v123, v97, v115\n\t"
    "v_and_or_b32 v98, v82, s44, v68\n\t"
    "ds_or_rtn_b32 v124, v98, v116\n\t"
    "v_and_or_b32 v99, v83, s44, v68\n\t"
    "ds_or_rtn_b32 v125, v99, v117\n\t"
    "v_lshl_or_b32 v164, v77, 16, v76\n\t"
    "v_lshl_or_b32 v165, v79, 16, v78\n\t"
    "v_lshl_or_b32 v166, v81, 16, v80\n\t"
    "v_lshl_or_b32 v167, v83, 16, v82\n\t"
    "s_movk_i32 s50, 0\n\t"
    "buffer_load_dwordx4 v[8:11], v64, %0, s50 offen offset:1616\n\t"
    "buffer_load_dwordx4 v[12:15], v64, %0, s50 offen offset:1632\n\t"
    "s_movk_i32 s50, 3200\n\t"
    "buffer_load_dwordx4 v[16:19], v64, %0, s50 offen offset:16\n\t"
    "buffer_load_dwordx4 v[20:23], v64, %0, s50 offen offset:32\n\t"
    "s_waitcnt vmcnt(2)\n\t"
    "v_lshl_add_u32 v84, v8, 3, s36\n\t"
    "v_lshl_add_u32 v85, v9, 3, s37\n\t"
    "v_lshl_add_u32 v86, v10, 3, s38\n\t"
    "v_lshl_add_u32 v87, v11, 3, s39\n\t"
    "v_lshl_add_u32 v88, v12, 3, s40\n\t"
    "v_lshl_add_u32 v89, v13, 3, s41\n\t"
    "v_lshl_add_u32 v90, v14, 3, s42\n\t"
    "v_lshl_add_u32 v91, v15, 3, s43\n\t"
    "s_waitcnt lgkmcnt(4)\n\t"
    "v_and_b32 v118, v118, v110\n\t"
    "v_perm_b32 v118, v118, v118, s46\n\t"
    "ds_or_b32 v92, v118\n\t"
    "v_and_b32 v119, v119, v111\n\t"
    "v_perm_b32 v119, v119, v119, s46\n\t"
    "ds_or_b32 v93, v119\n\t"
    "v_and_b32 v120, v120, v112\n\t"
    "v_perm_b32 v120, v120, v120, s46\n\t"
    "ds_or_b32 v94, v120\n\t"
    "v_and_b32 v121, v121, v113\n\t"
    "v_perm_b32 v121, v121, v121, s46\n\t"
    "ds_or_b32 v95, v121\n\t"
    "s_waitcnt lgkmcnt(4)\n\t"
    "v_and_b32 v122, v122, v114\n\t"
    "v_perm_b32 v122, v122, v122, s46\n\t"
    "ds_or_b32 v96, v122\n\t"
    "v_and_b32 v123, v123, v115\n\t"
    "v_perm_b32 v123, v123, v123, s46\n\t"
    "ds_or_b32 v97, v123\n\t"
    "v_and_b32 v124, v124, v116\n\t"
    "v_perm_b32 v124, v124, v124, s46\n\t"
    "ds_or_b32 v98, v124\n\t"
    "v_and_b32 v125, v125, v117\n\t"
    "v_perm_b32 v125, v125, v125, s46\n\t"
    "ds_or_b32 v99, v125\n\t"
    "buffer_store_dwordx4 v[164:167], v66, %12, s49 offen offset:0 sc1\n\t"
    "v_and_or_b32 v100, v84, s44, v68\n\t"
    "ds_read_b32 v100, v100\n\t"
    "v_and_or_b32 v101, v85, s44, v68\n\t"
    "ds_read_b32 v101, v101\n\t"
    "v_and_or_b32 v102, v86, s44, v68\n\t"
    "ds_read_b32 v102, v102\n\t"
    "v_and_or_b32 v103, v87, s44, v68\n\t"
    "ds_read_b32 v103, v103\n\t"
    "v_and_or_b32 v104, v88, s44, v68\n\t"
    "ds_read_b32 v104, v104\n\t"
    "v_and_or_b32 v105, v89, s44, v68\n\t"
    "ds_read_b32 v105, v105\n\t"
    "v_and_or_b32 v106, v90, s44, v68\n\t"
    "ds_read_b32 v106, v106\n\t"
    "v_and_or_b32 v107, v91, s44, v68\n\t"
    "ds_read_b32 v107, v107\n\t"
    "s_waitcnt vmcnt(1)\n\t"
    "v_lshl_add_u32 v76, v16, 3, s36\n\t"
    "v_lshl_add_u32 v77, v17, 3, s37\n\t"
    "v_lshl_add_u32 v78, v18, 3, s38\n\t"
    "v_lshl_add_u32 v79, v19, 3, s39\n\t"
    "v_lshl_add_u32 v80, v20, 3, s40\n\t"
    "v_lshl_add_u32 v81, v21, 3, s41\n\t"
    "v_lshl_add_u32 v82, v22, 3, s42\n\t"
    "v_lshl_add_u32 v83, v23, 3, s43\n\t"
    "s_movk_i32 s50, 3200\n\t"
    "buffer_load_dwordx4 v[24:27], v64, %0, s50 offen offset:1616\n\t"
    "buffer_load_dwordx4 v[28:31], v64, %0, s50 offen offset:1632\n\t"
    "s_movk_i32 s50, 6400\n\t"
    "buffer_load_dwordx4 v[32:35], v64, %0, s50 offen offset:16\n\t"
    "buffer_load_dwordx4 v[36:39], v64, %0, s50 offen offset:32\n\t"
    "v_mov_b32 v129, 0xffff0000\n\t"
    "s_waitcnt lgkmcnt(4)\n\t"
    "v_perm_b32 v100, v100, v84, v126\n\t"
    "v_perm_b32 v101, v101, v85, v126\n\t"
    "v_perm_b32 v102, v102, v86, v126\n\t"
    "v_perm_b32 v103, v103, v87, v126\n\t"
    "s_waitcnt lgkmcnt(0)\n\t"
    "v_perm_b32 v104, v104, v88, v126\n\t"
    "v_perm_b32 v105, v105, v89, v126\n\t"
    "v_perm_b32 v106, v106, v90, v126\n\t"
    "v_perm_b32 v107, v107, v91, v126\n\t"
    "v_max_u32 v108, v100, v101\n\t"
    "v_min_u32 v101, v100, v101\n\t"
    "v_max_u32 v100, v102, v103\n\t"
    "v_min_u32 v103, v102, v103\n\t"
    "v_max_u32 v102, v104, v105\n\t"
    "v_min_u32 v105, v104, v105\n\t"
    "v_max_u32 v104, v106, v107\n\t"
    "v_min_u32 v107, v106, v107\n\t"
    "v_max_u32 v106, v108, v100\n\t"
    "v_min_u32 v100, v108, v100\n\t"
    "v_max_u32 v108, v101, v103\n\t"
    "v_min_u32 v103, v101, v103\n\t"
    "v_max_u32 v101, v102, v104\n\t"
    "v_min_u32 v104, v102, v104\n\t"
    "v_max_u32 v102, v105, v107\n\t"
    "v_min_u32 v107, v105, v107\n\t"
    "v_bfi_b32 v130, v106, 0, v129\n\t"
    "v_cmp_eq_u32 vcc, 0, v130\n\t"
    "v_and_or_b32 v92, v106, s44, v68\n\t"
    "s_nop 0\n\t"
    "v_cndmask_b32 v130, v130, v129, vcc\n\t"
    "v_ffbl_b32 v131, v130\n\t"
    "v_and_b32 v134, 7, v131\n\t"
    "v_lshlrev_b32 v134, v134, s45\n\t"
    "v_xor_b32 v129, v129, v134\n\t"
    "v_xor_b32 v132, v131, v127\n\t"
    "v_bfi_b32 v130, v101, 0, v129\n\t"
    "v_cmp_eq_u32 vcc, 0, v130\n\t"
    "v_and_or_b32 v93, v101, s44, v68\n\t"
    "v_xor_b32 v133, 23, v132\n\t"
    "v_lshlrev_b32 v110, v132, v136\n\t"
    "v_and_b32 v132, 7, v132\n\t"
    "v_lshl_or_b32 v110, v136, v133, v110\n\t"
    "v_lshl_add_u32 v132, v132, 1, v128\n\t"
    "ds_or_rtn_b32 v118, v92, v110\n\t"
    "ds_write_b16 v132, v106\n\t"
    "v_cndmask_b32 v130, v130, v129, vcc\n\t"
    "v_ffbl_b32 v131, v130\n\t"
    "v_and_b32 v134, 7, v131\n\t"
    "v_lshlrev_b32 v134, v134, s45\n\t"
    "v_xor_b32 v129, v129, v134\n\t"
    "v_xor_b32 v132, v131, v127\n\t"
    "v_bfi_b32 v130, v108, 0, v129\n\t"
    "v_cmp_eq_u32 vcc, 0, v130\n\t"
    "v_and_or_b32 v94, v108, s44, v68\n\t"
    "v_xor_b32 v133, 23, v132\n\t"
    "v_lshlrev_b32 v111, v132, v136\n\t"
    "v_and_b32 v132, 7, v132\n\t"
    "v_lshl_or_b32 v111, v136, v133, v111\n\t"
    "v_lshl_add_u32 v132, v132, 1, v128\n\t"
    "ds_or_rtn_b32 v119, v93, v111\n\t"
    "ds_write_b16 v132, v101\n\t"
    "v_cndmask_b32 v130, v130, v129, vcc\n\t"
    "v_ffbl_b32 v131, v130\n\t"
    "v_and_b32 v134, 7, v131\n\t"
    "v_lshlrev_b32 v134, v134, s45\n\t"
    "v_xor_b32 v129, v129, v134\n\t"
    "v_xor_b32 v132, v131, v127\n\t"
    "v_bfi_b32 v130, v100, 0, v129\n\t"
    "v_cmp_eq_u32 vcc, 0, v130\n\t"
    "v_and_or_b32 v95, v100, s44, v68\n\t"
    "v_xor_b32 v133, 23, v132\n\t"
    "v_lshlrev_b32 v112, v132, v136\n\t"
    "v_and_b32 v132, 7, v132\n\t"
    "v_lshl_or_b32 v112, v136, v133, v112\n\t"
    "v_lshl_add_u32 v132, v132, 1, v128\n\t"
    "ds_or_rtn_b32 v120, v94, v112\n\t"
    "ds_write_b16 v132, v108\n\t"
    "v_cndmask_b32 v130, v130, v129, vcc\n\t"
    "v_ffbl_b32 v131, v130\n\t"
    "v_and_b32 v134, 7, v131\n\t"
    "v_lshlrev_b32 v134, v134, s45\n\t"
    "v_xor_b32 v129, v129, v134\n\t"
    "v_xor_b32 v132, v131, v127\n\t"
    "v_bfi_b32 v130, v102, 0, v129\n\t"
    "v_cmp_eq_u32 vcc, 0, v130\n\t"
    "v_and_or_b32 v96, v102, s44, v68\n\t"
    "v_xor_b32 v133, 23, v132\n\t"
    "v_lshlrev_b32 v113, v132, v136\n\t"
    "v_and_b32 v132, 7, v132\n\t"
    "v_lshl_or_b32 v113, v136, v133, v113\n\t"
    "v_lshl_add_u32 v132, v132, 1, v128\n\t"
    "ds_or_rtn_b32 v121, v95, v113\n\t"
    "ds_write_b16 v132, v100\n\t"
    "v_cndmask_b32 v130, v130, v129, vcc\n\t"
    "v_ffbl_b32 v131, v130\n\t"
    "v_and_b32 v134, 7, v131\n\t"
    "v_lshlrev_b32 v134, v134, s45\n\t"
    "v_xor_b32 v129, v129, v134\n\t"
    "v_xor_b32 v132, v131, v127\n\t"
    "s_waitcnt lgkmcnt(7)\n\t"
    "v_and_b32 v118, v118, v110\n\t"
    "v_perm_b32 v118, v118, v118, s46\n\t"
    "ds_or_b32 v92, v118\n\t"
    "v_bfi_b32 v130, v104, 0, v129\n\t"
    "v_cmp_eq_u32 vcc, 0, v130\n\t"
    "v_and_or_b32 v97, v104, s44, v68\n\t"
    "v_xor_b32 v133, 23, v132\n\t"
    "v_lshlrev_b32 v114, v132, v136\n\t"
    "v_and_b32 v132, 7, v132\n\t"
    "v_lshl_or_b32 v114, v136, v133, v114\n\t"
    "v_lshl_add_u32 v132, v132, 1, v128\n\t"
    "ds_or_rtn_b32 v122, v96, v114\n\t"
    "ds_write_b16 v132, v102\n\t"
    "v_cndmask_b32 v130, v130, v129, vcc\n\t"
    "v_ffbl_b32 v131, v130\n\t"
    "v_and_b32 v134, 7, v131\n\t"
    "v_lshlrev_b32 v134, v134, s45\n\t"
    "v_xor_b32 v129, v129, v134\n\t"
    "v_xor_b32 v132, v131, v127\n\t"
    "s_waitcnt lgkmcnt(8)\n\t"
    "v_and_b32 v119, v119, v111\n\t"
    "v_perm_b32 v119, v119, v119, s46\n\t"
    "ds_or_b32 v93, v119\n\t"
    "v_bfi_b32 v130, v103, 0, v129\n\t"
    "v_cmp_eq_u32 vcc, 0, v130\n\t"
    "v_and_or_b32 v98, v103, s44, v68\n\t"
    "v_xor_b32 v133, 23, v132\n\t"
    "v_lshlrev_b32 v115, v132, v136\n\t"
    "v_and_b32 v132, 7, v132\n\t"
    "v_lshl_or_b32 v115, v136, v133, v115\n\t"
    "v_lshl_add_u32 v132, v132, 1, v128\n\t"
    "ds_or_rtn_b32 v123, v97, v115\n\t"
    "ds_write_b16 v132, v104\n\t"
    "v_cndmask_b32 v130, v130, v129, vcc\n\t"
    "v_ffbl_b32 v131, v130\n\t"
    "v_and_b32 v134, 7, v131\n\t"
    "v_lshlrev_b32 v134, v134, s45\n\t"
    "v_xor_b32 v129, v129, v134\n\t"
    "v_xor_b32 v132, v131, v127\n\t"
    "s_waitcnt lgkmcnt(9)\n\t"
    "v_and_b32 v120, v120, v112\n\t"
    "v_perm_b32 v120, v120, v120, s46\n\t"
    "ds_or_b32 v94, v120\n\t"
    "v_bfi_b32 v130, v107, 0, v129\n\t"
    "v_cmp_eq_u32 vcc, 0, v130\n\t"
    "v_and_or_b32 v99, v107, s44, v68\n\t"
    "v_xor_b32 v133, 23, v132\n\t"
    "v_lshlrev_b32 v116, v132, v136\n\t"
    "v_and_b32 v132, 7, v132\n\t"
    "v_lshl_or_b32 v116, v136, v133, v116\n\t"
    "v_lshl_add_u32 v132, v132, 1, v128\n\t"
    "ds_or_rtn_b32 v124, v98, v116\n\t"
    "ds_write_b16 v132, v103\n\t"
    "v_cndmask_b32 v130, v130, v129, vcc\n\t"
    "v_ffbl_b32 v131, v130\n\t"
    "v_xor_b32 v132, v131, v127\n\t"
    "s_waitcnt lgkmcnt(10)\n\t"
    "v_and_b32 v121, v121, v113\n\t"
    "v_perm_b32 v121, v121, v121, s46\n\t"
    "ds_or_b32 v95, v121\n\t"
    "v_xor_b32 v133, 23, v132\n\t"
    "v_lshlrev_b32 v117, v132, v136\n\t"
    "v_and_b32 v132, 7, v132\n\t"
    "v_lshl_or_b32 v117, v136, v133, v117\n\t"
    "v_lshl_add_u32 v132, v132, 1, v128\n\t"
    "ds_or_rtn_b32 v125, v99, v117\n\t"
    "ds_write_b16 v132, v107\n\t"
    "s_waitcnt lgkmcnt(10)\n\t"
    "v_and_b32 v122, v122, v114\n\t"
    "v_perm_b32 v122, v122, v122, s46\n\t"
    "ds_or_b32 v96, v122\n\t"
    "ds_read_b128 v[72:75], v128\n\t"
    "v_and_or_b32 v100, v76, s44, v68\n\t"
    "ds_read_b32 v100, v100\n\t"
    "v_and_or_b32 v101, v77, s44, v68\n\t"
    "ds_read_b32 v101, v101\n\t"
    "v_and_or_b32 v102, v78, s44, v68\n\t"
    "ds_read_b32 v102, v102\n\t"
    "v_and_or_b32 v103, v79, s44, v68\n\t"
    "ds_read_b32 v103, v103\n\t"
    "v_and_or_b32 v104, v80, s44, v68\n\t"
    "ds_read_b32 v104, v104\n\t"
    "v_and_or_b32 v105, v81, s44, v68\n\t"
    "ds_read_b32 v105, v105\n\t"
    "v_and_or_b32 v106, v82, s44, v68\n\t"
    "ds_read_b32 v106, v106\n\t"
    "v_and_or_b32 v107, v83, s44, v68\n\t"
    "ds_read_b32 v107, v107\n\t"
    "s_waitcnt vmcnt(2)\n\t"
    "v_lshl_add_u32 v84, v24, 3, s36\n\t"
    "v_lshl_add_u32 v85, v25, 3, s37\n\t"
    "v_lshl_add_u32 v86, v26, 3, s38\n\t"
    "v_lshl_add_u32 v87, v27, 3, s39\n\t"
    "v_lshl_add_u32 v88, v28, 3, s40\n\t"
    "v_lshl_add_u32 v89, v29, 3, s41\n\t"
    "v_lshl_add_u32 v90, v30, 3, s42\n\t"
    "v_lshl_add_u32 v91, v31, 3, s43\n\t"
    "s_movk_i32 s50, 6400\n\t"
    "buffer_load_dwordx4 v[40:43], v65, %0, s50 offen offset:1616\n\t"
    "buffer_load_dwordx4 v[44:47], v65, %0, s50 offen offset:1632\n\t"
    "s_movk_i32 s50, 6400\n\t"
    "buffer_load_dwordx4 v[48:51], v65, %0, s50 offen offset:2416\n\t"
    "buffer_load_dwordx4 v[52:55], v65, %0, s50 offen offset:2432\n\t"
    "s_movk_i32 s50, 9600\n\t"
    "buffer_load_dwordx4 v[56:59], v65, %0, s50 offen offset:16\n\t"
    "buffer_load_dwordx4 v[60:63], v65, %0, s50 offen offset:32\n\t"
    "s_movk_i32 s50, 9600\n\t"
    "buffer_load_dwordx4 v[0:3], v65, %0, s50 offen offset:816\n\t"
    "buffer_load_dwordx4 v[4:7], v65, %0, s50 offen offset:832\n\t"
    "s_movk_i32 s50, 9600\n\t"
    "buffer_load_dwordx4 v[8:11], v65, %0, s50 offen offset:1616\n\t"
    "buffer_load_dwordx4 v[12:15], v65, %0, s50 offen offset:1632\n\t"
    "v_mov_b32 v134, 1032\n\t"
    "L_asvsp8_%=:\n\t"
    "ds_read_b32 v130, v134\n\t"
    "s_waitcnt lgkmcnt(0)\n\t"
    "v_readfirstlane_b32 s50, v130\n\t"
    "s_nop 0\n\t"
    "s_cmp_lt_u32 s50, 6\n\t"
    "s_cbranch_scc1 L_asvsp8_%=\n\t"
    "s_mov_b64 s[96:97], %2\n\t"
    "s_and_b32 s97, s97, 0xffff\n\t"
    "buffer_load_dwordx4 v[156:159], v137, s[96:99], s47 offen offset:0\n\t"
    "buffer_load_dwordx4 v[160:163], v138, s[96:99], s47 offen offset:0\n\t"
    "buffer_load_dwordx4 v[164:167], v137, s[96:99], s47 offen offset:64\n\t"
    "buffer_load_dwordx4 v[168:171], v138, s[96:99], s47 offen offset:64\n\t"
    "s_add_u32 s47, s47, 0x100000\n\t"
    "buffer_load_dwordx4 v[172:175], v137, s[96:99], s47 offen offset:0\n\t"
    "buffer_load_dwordx4 v[176:179], v138, s[96:99], s47 offen offset:0\n\t"
    "buffer_load_dwordx4 v[180:183], v137, s[96:99], s47 offen offset:64\n\t"
    "buffer_load_dwordx4 v[184:187], v138, s[96:99], s47 offen offset:64\n\t"
    "s_add_u32 s47, s47, 0x100000\n\t"
    "buffer_load_dwordx4 v[188:191], v137, s[96:99], s47 offen offset:0\n\t"
    "buffer_load_dwordx4 v[192:195], v138, s[96:99], s47 offen offset:0\n\t"
    "buffer_load_dwordx4 v[196:199], v137, s[96:99], s47 offen offset:64\n\t"
    "buffer_load_dwordx4 v[200:203], v138, s[96:99], s47 offen offset:64\n\t"
    "s_add_u32 s47, s47, 0x100000\n\t"
    "v_and_b32 v123, v123, v115\n\t"
    "v_perm_b32 v123, v123, v123, s46\n\t"
    "ds_or_b32 v97, v123\n\t"
    "v_and_b32 v124, v124, v116\n\t"
    "v_perm_b32 v124, v124, v124, s46\n\t"
    "ds_or_b32 v98, v124\n\t"
    "v_and_b32 v125, v125, v117\n\t"
    "v_perm_b32 v125, v125, v125, s46\n\t"
    "ds_or_b32 v99, v125\n\t"
    "buffer_store_dwordx4 v[72:75], v66, %12, s49 offen offset:64 sc1\n\t"
    "v_mov_b32 v129, 0xffff0000\n\t"
    "v_perm_b32 v100, v100, v76, v126\n\t"
    "v_perm_b32 v101, v101, v77, v126\n\t"
    "v_perm_b32 v102, v102, v78, v126\n\t"
    "v_perm_b32 v103, v103, v79, v126\n\t"
    "v_perm_b32 v104, v104, v80, v126\n\t"
    "v_perm_b32 v105, v105, v81, v126\n\t"
    "v_perm_b32 v106, v106, v82, v126\n\t"
    "v_perm_b32 v107, v107, v83, v126\n\t"
    "v_max_u32 v108, v100, v101\n\t"
    "v_min_u32 v101, v100, v101\n\t"
    "v_max_u32 v100, v102, v103\n\t"
    "v_min_u32 v103, v102, v103\n\t"
    "v_max_u32 v102, v104, v105\n\t"
    "v_min_u32 v105, v104, v105\n\t"
    "v_max_u32 v104, v106, v107\n\t"
    "v_min_u32 v107, v106, v107\n\t"
    "v_max_u32 v106, v108, v100\n\t"
    "v_min_u32 v100, v108, v100\n\t"
    "v_max_u32 v108, v101, v103\n\t"
    "v_min_u32 v103, v101, v103\n\t"
    "v_max_u32 v101, v102, v104\n\t"
    "v_min_u32 v104, v102, v104\n\t"
    "v_max_u32 v102, v105, v107\n\t"
    "v_min_u32 v107, v105, v107\n\t"
    "v_bfi_b32 v130, v106, 0, v129\n\t"
    "v_cmp_eq_u32 vcc, 0, v130\n\t"
    "v_and_or_b32 v92, v106, s44, v68\n\t"
    "s_nop 0\n\t"
    "v_cndmask_b32 v130, v130, v129, vcc\n\t"
    "v_ffbl_b32 v131, v130\n\t"
    "v_and_b32 v134, 7, v131\n\t"
    "v_lshlrev_b32 v134, v134, s45\n\t"
    "v_xor_b32 v129, v129, v134\n\t"
    "v_xor_b32 v132, v131, v127\n\t"
    "v_bfi_b32 v130, v101, 0, v129\n\t"
    "v_cmp_eq_u32 vcc, 0, v130\n\t"
    "v_and_or_b32 v93, v101, s44, v68\n\t"
    "v_xor_b32 v133, 23, v132\n\t"
    "v_lshlrev_b32 v110, v132, v136\n\t"
    "v_and_b32 v132, 7, v132\n\t"
    "v_lshl_or_b32 v110, v136, v133, v110\n\t"
    "v_lshl_add_u32 v132, v132, 1, v128\n\t"
    "ds_or_rtn_b32 v118, v92, v110\n\t"
    "ds_write_b16 v132, v106\n\t"
    "v_cndmask_b32 v130, v130, v129, vcc\n\t"
    "v_ffbl_b32 v131, v130\n\t"
    "v_and_b32 v134, 7, v131\n\t"
    "v_lshlrev_b32 v134, v134, s45\n\t"
    "v_xor_b32 v129, v129, v134\n\t"
    "v_xor_b32 v132, v131, v127\n\t"
    "v_bfi_b32 v130, v108, 0, v129\n\t"
    "v_cmp_eq_u32 vcc, 0, v130\n\t"
    "v_and_or_b32 v94, v108, s44, v68\n\t"
    "v_xor_b32 v133, 23, v132\n\t"
    "v_lshlrev_b32 v111, v132, v136\n\t"
    "v_and_b32 v132, 7, v132\n\t"
    "v_lshl_or_b32 v111, v136, v133, v111\n\t"
    "v_lshl_add_u32 v132, v132, 1, v128\n\t"
    "ds_or_rtn_b32 v119, v93, v111\n\t"
    "ds_write_b16 v132, v101\n\t"
    "v_cndmask_b32 v130, v130, v129, vcc\n\t"
    "v_ffbl_b32 v131, v130\n\t"
    "v_and_b32 v134, 7, v131\n\t"
    "v_lshlrev_b32 v134, v134, s45\n\t"
    "v_xor_b32 v129, v129, v134\n\t"
    "v_xor_b32 v132, v131, v127\n\t"
    "v_bfi_b32 v130, v100, 0, v129\n\t"
    "v_cmp_eq_u32 vcc, 0, v130\n\t"
    "v_and_or_b32 v95, v100, s44, v68\n\t"
    "v_xor_b32 v133, 23, v132\n\t"
    "v_lshlrev_b32 v112, v132, v136\n\t"
    "v_and_b32 v132, 7, v132\n\t"
    "v_lshl_or_b32 v112, v136, v133, v112\n\t"
    "v_lshl_add_u32 v132, v132, 1, v128\n\t"
    "ds_or_rtn_b32 v120, v94, v112\n\t"
    "ds_write_b16 v132, v108\n\t"
    "v_cndmask_b32 v130, v130, v129, vcc\n\t"
    "v_ffbl_b32 v131, v130\n\t"
    "v_and_b32 v134, 7, v131\n\t"
    "v_lshlrev_b32 v134, v134, s45\n\t"
    "v_xor_b32 v129, v129, v134\n\t"
    "v_xor_b32 v132, v131, v127\n\t"
    "v_bfi_b32 v130, v102, 0, v129\n\t"
    "v_cmp_eq_u32 vcc, 0, v130\n\t"
    "v_and_or_b32 v96, v102, s44, v68\n\t"
    "v_xor_b32 v133, 23, v132\n\t"
    "v_lshlrev_b32 v113, v132, v136\n\t"
    "v_and_b32 v132, 7, v132\n\t"
    "v_lshl_or_b32 v113, v136, v133, v113\n\t"
    "v_lshl_add_u32 v132, v132, 1, v128\n\t"
    "ds_or_rtn_b32 v121, v95, v113\n\t"
    "ds_write_b16 v132, v100\n\t"
    "v_cndmask_b32 v130, v130, v129, vcc\n\t"
    "v_ffbl_b32 v131, v130\n\t"
    "v_and_b32 v134, 7, v131\n\t"
    "v_lshlrev_b32 v134, v134, s45\n\t"
    "v_xor_b32 v129, v129, v134\n\t"
    "v_xor_b32 v132, v131, v127\n\t"
    "s_waitcnt lgkmcnt(7)\n\t"
    "v_and_b32 v118, v118, v110\n\t"
    "v_perm_b32 v118, v118, v118, s46\n\t"
    "ds_or_b32 v92, v118\n\t"
    "v_bfi_b32 v130, v104, 0, v129\n\t"
    "v_cmp_eq_u32 vcc, 0, v130\n\t"
    "v_and_or_b32 v97, v104, s44, v68\n\t"
    "v_xor_b32 v133, 23, v132\n\t"
    "v_lshlrev_b32 v114, v132, v136\n\t"
    "v_and_b32 v132, 7, v132\n\t"
    "v_lshl_or_b32 v114, v136, v133, v114\n\t"
    "v_lshl_add_u32 v132, v132, 1, v128\n\t"
    "ds_or_rtn_b32 v122, v96, v114\n\t"
    "ds_write_b16 v132, v102\n\t"
    "v_cndmask_b32 v130, v130, v129, vcc\n\t"
    "v_ffbl_b32 v131, v130\n\t"
    "v_and_b32 v134, 7, v131\n\t"
    "v_lshlrev_b32 v134, v134, s45\n\t"
    "v_xor_b32 v129, v129, v134\n\t"
    "v_xor_b32 v132, v131, v127\n\t"
    "s_waitcnt lgkmcnt(8)\n\t"
    "v_and_b32 v119, v119, v111\n\t"
    "v_perm_b32 v119, v119, v119, s46\n\t"
    "ds_or_b32 v93, v119\n\t"
    "v_bfi_b32 v130, v103, 0, v129\n\t"
    "v_cmp_eq_u32 vcc, 0, v130\n\t"
    "v_and_or_b32 v98, v103, s44, v68\n\t"
    "v_xor_b32 v133, 23, v132\n\t"
    "v_lshlrev_b32 v115, v132, v136\n\t"
    "v_and_b32 v132, 7, v132\n\t"
    "v_lshl_or_b32 v115, v136, v133, v115\n\t"
    "v_lshl_add_u32 v132, v132, 1, v128\n\t"
    "ds_or_rtn_b32 v123, v97, v115\n\t"
    "ds_write_b16 v132, v104\n\t"
    "v_cndmask_b32 v130, v130, v129, vcc\n\t"
    "v_ffbl_b32 v131, v130\n\t"
    "v_and_b32 v134, 7, v131\n\t"
    "v_lshlrev_b32 v134, v134, s45\n\t"
    "v_xor_b32 v129, v129, v134\n\t"
    "v_xor_b32 v132, v131, v127\n\t"
    "s_waitcnt lgkmcnt(9)\n\t"
    "v_and_b32 v120, v120, v112\n\t"
    "v_perm_b32 v120, v120, v120, s46\n\t"
    "ds_or_b32 v94, v120\n\t"
    "v_bfi_b32 v130, v107, 0, v129\n\t"
    "v_cmp_eq_u32 vcc, 0, v130\n\t"
    "v_and_or_b32 v99, v107, s44, v68\n\t"
    "v_xor_b32 v133, 23, v132\n\t"
    "v_lshlrev_b32 v116, v132, v136\n\t"
    "v_and_b32 v132, 7, v132\n\t"
    "v_lshl_or_b32 v116, v136, v133, v116\n\t"
    "v_lshl_add_u32 v132, v132, 1, v128\n\t"
    "ds_or_rtn_b32 v124, v98, v116\n\t"
    "ds_write_b16 v132, v103\n\t"
    "v_cndmask_b32 v130, v130, v129, vcc\n\t"
    "v_ffbl_b32 v131, v130\n\t"
    "v_xor_b32 v132, v131, v127\n\t"
    "s_waitcnt lgkmcnt(10)\n\t"
    "v_and_b32 v121, v121, v113\n\t"
    "v_perm_b32 v121, v121, v121, s46\n\t"
    "ds_or_b32 v95, v121\n\t"
    "v_xor_b32 v133, 23, v132\n\t"
    "v_lshlrev_b32 v117, v132, v136\n\t"
    "v_and_b32 v132, 7, v132\n\t"
    "v_lshl_or_b32 v117, v136, v133, v117\n\t"
    "v_lshl_add_u32 v132, v132, 1, v128\n\t"
    "ds_or_rtn_b32 v125, v99, v117\n\t"
    "ds_write_b16 v132, v107\n\t"
    "s_waitcnt lgkmcnt(10)\n\t"
    "v_and_b32 v122, v122, v114\n\t"
    "v_perm_b32 v122, v122, v122, s46\n\t"
    "ds_or_b32 v96, v122\n\t"
    "ds_read_b128 v[72:75], v128\n\t"
    "v_and_or_b32 v100, v84, s44, v68\n\t"
    "ds_read_b32 v100, v100\n\t"
    "v_and_or_b32 v101, v85, s44, v68\n\t"
    "ds_read_b32 v101, v101\n\t"
    "v_and_or_b32 v102, v86, s44, v68\n\t"
    "ds_read_b32 v102, v102\n\t"
    "v_and_or_b32 v103, v87, s44, v68\n\t"
    "ds_read_b32 v103, v103\n\t"
    "v_and_or_b32 v104, v88, s44, v68\n\t"
    "ds_read_b32 v104, v104\n\t"
    "v_and_or_b32 v105, v89, s44, v68\n\t"
    "ds_read_b32 v105, v105\n\t"
    "v_and_or_b32 v106, v90, s44, v68\n\t"
    "ds_read_b32 v106, v106\n\t"
    "v_and_or_b32 v107, v91, s44, v68\n\t"
    "ds_read_b32 v107, v107\n\t"
    "s_waitcnt vmcnt(23)\n\t"
    "v_lshl_add_u32 v76, v32, 3, s36\n\t"
    "v_lshl_add_u32 v77, v33, 3, s37\n\t"
    "v_lshl_add_u32 v78, v34, 3, s38\n\t"
    "v_lshl_add_u32 v79, v35, 3, s39\n\t"
    "v_lshl_add_u32 v80, v36, 3, s40\n\t"
    "v_lshl_add_u32 v81, v37, 3, s41\n\t"
    "v_lshl_add_u32 v82, v38, 3, s42\n\t"
    "v_lshl_add_u32 v83, v39, 3, s43\n\t"
    "s_movk_i32 s50, 9600\n\t"
    "buffer_load_dwordx4 v[16:19], v65, %0, s50 offen offset:2416\n\t"
    "buffer_load_dwordx4 v[20:23], v65, %0, s50 offen offset:2432\n\t"
    "buffer_load_dwordx4 v[204:207], v137, s[96:99], s47 offen offset:0\n\t"
    "buffer_load_dwordx4 v[208:211], v138, s[96:99], s47 offen offset:0\n\t"
    "buffer_load_dwordx4 v[212:215], v137, s[96:99], s47 offen offset:64\n\t"
    "buffer_load_dwordx4 v[216:219], v138, s[96:99], s47 offen offset:64\n\t"
    "s_add_u32 s47, s47, 0x100000\n\t"
    "buffer_load_dwordx4 v[220:223], v137, s[96:99], s47 offen offset:0\n\t"
    "buffer_load_dwordx4 v[224:227], v138, s[96:99], s47 offen offset:0\n\t"
    "buffer_load_dwordx4 v[228:231], v137, s[96:99], s47 offen offset:64\n\t"
    "buffer_load_dwordx4 v[232:235], v138, s[96:99], s47 offen offset:64\n\t"
    "s_add_u32 s47, s47, 0x100000\n\t"
    "buffer_load_dwordx4 v[236:239], v137, s[96:99], s47 offen offset:0\n\t"
    "buffer_load_dwordx4 v[240:243], v138, s[96:99], s47 offen offset:0\n\t"
    "buffer_load_dwordx4 v[244:247], v137, s[96:99], s47 offen offset:64\n\t"
    "buffer_load_dwordx4 v[248:251], v138, s[96:99], s47 offen offset:64\n\t"
    "s_add_u32 s47, s47, 0x100000\n\t"
    "s_waitcnt lgkmcnt(11)\n\t"
    "v_and_b32 v123, v123, v115\n\t"
    "v_perm_b32 v123, v123, v123, s46\n\t"
    "ds_or_b32 v97, v123\n\t"
    "v_and_b32 v124, v124, v116\n\t"
    "v_perm_b32 v124, v124, v124, s46\n\t"
    "ds_or_b32 v98, v124\n\t"
    "v_and_b32 v125, v125, v117\n\t"
    "v_perm_b32 v125, v125, v125, s46\n\t"
    "ds_or_b32 v99, v125\n\t"
    "s_waitcnt lgkmcnt(11)\n\t"
    "buffer_store_dwordx4 v[72:75], v66, %12, s49 offen offset:128 sc1\n\t"
    "v_mov_b32 v129, 0xffff0000\n\t"
    "s_waitcnt lgkmcnt(7)\n\t"
    "v_perm_b32 v100, v100, v84, v126\n\t"
    "v_perm_b32 v101, v101, v85, v126\n\t"
    "v_perm_b32 v102, v102, v86, v126\n\t"
    "v_perm_b32 v103, v103, v87, v126\n\t"
    "s_waitcnt lgkmcnt(3)\n\t"
    "v_perm_b32 v104, v104, v88, v126\n\t"
    "v_perm_b32 v105, v105, v89, v126\n\t"
    "v_perm_b32 v106, v106, v90, v126\n\t"
    "v_perm_b32 v107, v107, v91, v126\n\t"
    "v_max_u32 v108, v100, v101\n\t"
    "v_min_u32 v101, v100, v101\n\t"
    "v_max_u32 v100, v102, v103\n\t"
    "v_min_u32 v103, v102, v103\n\t"
    "v_max_u32 v102, v104, v105\n\t"
    "v_min_u32 v105, v104, v105\n\t"
    "v_max_u32 v104, v106, v107\n\t"
    "v_min_u32 v107, v106, v107\n\t"
    "v_max_u32 v106, v108, v100\n\t"
    "v_min_u32 v100, v108, v100\n\t"
    "v_max_u32 v108, v101, v103\n\t"
    "v_min_u32 v103, v101, v103\n\t"
    "v_max_u32 v101, v102, v104\n\t"
    "v_min_u32 v104, v102, v104\n\t"
    "v_max_u32 v102, v105, v107\n\t"
    "v_min_u32 v107, v105, v107\n\t"
    "v_bfi_b32 v130, v106, 0, v129\n\t"
    "v_cmp_eq_u32 vcc, 0, v130\n\t"
    "v_and_or_b32 v92, v106, s44, v68\n\t"
    "s_nop 0\n\t"
    "v_cndmask_b32 v130, v130, v129, vcc\n\t"
    "v_ffbl_b32 v131, v130\n\t"
    "v_and_b32 v134, 7, v131\n\t"
    "v_lshlrev_b32 v134, v134, s45\n\t"
    "v_xor_b32 v129, v129, v134\n\t"
    "v_xor_b32 v132, v131, v127\n\t"
    "v_bfi_b32 v130, v101, 0, v129\n\t"
    "v_cmp_eq_u32 vcc, 0, v130\n\t"
    "v_and_or_b32 v93, v101, s44, v68\n\t"
    "v_xor_b32 v133, 23, v132\n\t"
    "v_lshlrev_b32 v110, v132, v136\n\t"
    "v_and_b32 v132, 7, v132\n\t"
    "v_lshl_or_b32 v110, v136, v133, v110\n\t"
    "v_lshl_add_u32 v132, v132, 1, v128\n\t"
    "ds_or_rtn_b32 v118, v92, v110\n\t"
    "ds_write_b16 v132, v106\n\t"
    "v_cndmask_b32 v130, v130, v129, vcc\n\t"
    "v_ffbl_b32 v131, v130\n\t"
    "v_and_b32 v134, 7, v131\n\t"
    "v_lshlrev_b32 v134, v134, s45\n\t"
    "v_xor_b32 v129, v129, v134\n\t"
    "v_xor_b32 v132, v131, v127\n\t"
    "v_bfi_b32 v130, v108, 0, v129\n\t"
    "v_cmp_eq_u32 vcc, 0, v130\n\t"
    "v_and_or_b32 v94, v108, s44, v68\n\t"
    "v_xor_b32 v133, 23, v132\n\t"
    "v_lshlrev_b32 v111, v132, v136\n\t"
    "v_and_b32 v132, 7, v132\n\t"
    "v_lshl_or_b32 v111, v136, v133, v111\n\t"
    "v_lshl_add_u32 v132, v132, 1, v128\n\t"
    "ds_or_rtn_b32 v119, v93, v111\n\t"
    "ds_write_b16 v132, v101\n\t"
    "v_cndmask_b32 v130, v130, v129, vcc\n\t"
    "v_ffbl_b32 v131, v130\n\t"
    "v_and_b32 v134, 7, v131\n\t"
    "v_lshlrev_b32 v134, v134, s45\n\t"
    "v_xor_b32 v129, v129, v134\n\t"
    "v_xor_b32 v132, v131, v127\n\t"
    "v_bfi_b32 v130, v100, 0, v129\n\t"
    "v_cmp_eq_u32 vcc, 0, v130\n\t"
    "v_and_or_b32 v95, v100, s44, v68\n\t"
    "v_xor_b32 v133, 23, v132\n\t"
    "v_lshlrev_b32 v112, v132, v136\n\t"
    "v_and_b32 v132, 7, v132\n\t"
    "v_lshl_or_b32 v112, v136, v133, v112\n\t"
    "v_lshl_add_u32 v132, v132, 1, v128\n\t"
    "ds_or_rtn_b32 v120, v94, v112\n\t"
    "ds_write_b16 v132, v108\n\t"
    "v_cndmask_b32 v130, v130, v129, vcc\n\t"
    "v_ffbl_b32 v131, v130\n\t"
    "v_and_b32 v134, 7, v131\n\t"
    "v_lshlrev_b32 v134, v134, s45\n\t"
    "v_xor_b32 v129, v129, v134\n\t"
    "v_xor_b32 v132, v131, v127\n\t"
    "v_bfi_b32 v130, v102, 0, v129\n\t"
    "v_cmp_eq_u32 vcc, 0, v130\n\t"
    "v_and_or_b32 v96, v102, s44, v68\n\t"
    "v_xor_b32 v133, 23, v132\n\t"
    "v_lshlrev_b32 v113, v132, v136\n\t"
    "v_and_b32 v132, 7, v132\n\t"
    "v_lshl_or_b32 v113, v136, v133, v113\n\t"
    "v_lshl_add_u32 v132, v132, 1, v128\n\t"
    "ds_or_rtn_b32 v121, v95, v113\n\t"
    "ds_write_b16 v132, v100\n\t"
    "v_cndmask_b32 v130, v130, v129, vcc\n\t"
    "v_ffbl_b32 v131, v130\n\t"
    "v_and_b32 v134, 7, v131\n\t"
    "v_lshlrev_b32 v134, v134, s45\n\t"
    "v_xor_b32 v129, v129, v134\n\t"
    "v_xor_b32 v132, v131, v127\n\t"
    "s_waitcnt lgkmcnt(7)\n\t"
    "v_and_b32 v118, v118, v110\n\t"
    "v_perm_b32 v118, v118, v118, s46\n\t"
    "ds_or_b32 v92, v118\n\t"
    "v_bfi_b32 v130, v104, 0, v129\n\t"
    "v_cmp_eq_u32 vcc, 0, v130\n\t"
    "v_and_or_b32 v97, v104, s44, v68\n\t"
    "v_xor_b32 v133, 23, v132\n\t"
    "v_lshlrev_b32 v114, v132, v136\n\t"
    "v_and_b32 v132, 7, v132\n\t"
    "v_lshl_or_b32 v114, v136, v133, v114\n\t"
    "v_lshl_add_u32 v132, v132, 1, v128\n\t"
    "ds_or_rtn_b32 v122, v96, v114\n\t"
    "ds_write_b16 v132, v102\n\t"
    "v_cndmask_b32 v130, v130, v129, vcc\n\t"
    "v_ffbl_b32 v131, v130\n\t"
    "v_and_b32 v134, 7, v131\n\t"
    "v_lshlrev_b32 v134, v134, s45\n\t"
    "v_xor_b32 v129, v129, v134\n\t"
    "v_xor_b32 v132, v131, v127\n\t"
    "s_waitcnt lgkmcnt(8)\n\t"
    "v_and_b32 v119, v119, v111\n\t"
    "v_perm_b32 v119, v119, v119, s46\n\t"
    "ds_or_b32 v93, v119\n\t"
    "v_bfi_b32 v130, v103, 0, v129\n\t"
    "v_cmp_eq_u32 vcc, 0, v130\n\t"
    "v_and_or_b32 v98, v103, s44, v68\n\t"
    "v_xor_b32 v133, 23, v132\n\t"
    "v_lshlrev_b32 v115, v132, v136\n\t"
    "v_and_b32 v132, 7, v132\n\t"
    "v_lshl_or_b32 v115, v136, v133, v115\n\t"
    "v_lshl_add_u32 v132, v132, 1, v128\n\t"
    "ds_or_rtn_b32 v123, v97, v115\n\t"
    "ds_write_b16 v132, v104\n\t"
    "v_cndmask_b32 v130, v130, v129, vcc\n\t"
    "v_ffbl_b32 v131, v130\n\t"
    "v_and_b32 v134, 7, v131\n\t"
    "v_lshlrev_b32 v134, v134, s45\n\t"
    "v_xor_b32 v129, v129, v134\n\t"
    "v_xor_b32 v132, v131, v127\n\t"
    "s_waitcnt lgkmcnt(9)\n\t"
    "v_and_b32 v120, v120, v112\n\t"
    "v_perm_b32 v120, v120, v120, s46\n\t"
    "ds_or_b32 v94, v120\n\t"
    "v_bfi_b32 v130, v107, 0, v129\n\t"
    "v_cmp_eq_u32 vcc, 0, v130\n\t"
    "v_and_or_b32 v99, v107, s44, v68\n\t"
    "v_xor_b32 v133, 23, v132\n\t"
    "v_lshlrev_b32 v116, v132, v136\n\t"
    "v_and_b32 v132, 7, v132\n\t"
    "v_lshl_or_b32 v116, v136, v133, v116\n\t"
    "v_lshl_add_u32 v132, v132, 1, v128\n\t"
    "ds_or_rtn_b32 v124, v98, v116\n\t"
    "ds_write_b16 v132, v103\n\t"
    "v_cndmask_b32 v130, v130, v129, vcc\n\t"
    "v_ffbl_b32 v131, v130\n\t"
    "v_xor_b32 v132, v131, v127\n\t"
    "s_waitcnt lgkmcnt(10)\n\t"
    "v_and_b32 v121, v121, v113\n\t"
    "v_perm_b32 v121, v121, v121, s46\n\t"
    "ds_or_b32 v95, v121\n\t"
    "v_xor_b32 v133, 23, v132\n\t"
    "v_lshlrev_b32 v117, v132, v136\n\t"
    "v_and_b32 v132, 7, v132\n\t"
    "v_lshl_or_b32 v117, v136, v133, v117\n\t"
    "v_lshl_add_u32 v132, v132, 1, v128\n\t"
    "ds_or_rtn_b32 v125, v99, v117\n\t"
    "ds_write_b16 v132, v107\n\t"
    "s_waitcnt lgkmcnt(10)\n\t"
    "v_and_b32 v122, v122, v114\n\t"
    "v_perm_b32 v122, v122, v122, s46\n\t"
    "ds_or_b32 v96, v122\n\t"
    "ds_read_b128 v[72:75], v128\n\t"
    "v_and_or_b32 v100, v76, s44, v68\n\t"
    "ds_read_b32 v100, v100\n\t"
    "v_and_or_b32 v101, v77, s44, v68\n\t"
    "ds_read_b32 v101, v101\n\t"
    "v_and_or_b32 v102, v78, s44, v68\n\t"
    "ds_read_b32 v102, v102\n\t"
    "v_and_or_b32 v103, v79, s44, v68\n\t"
    "ds_read_b32 v103, v103\n\t"
    "v_and_or_b32 v104, v80, s44, v68\n\t"
    "ds_read_b32 v104, v104\n\t"
    "v_and_or_b32 v105, v81, s44, v68\n\t"
    "ds_read_b32 v105, v105\n\t"
    "v_and_or_b32 v106, v82, s44, v68\n\t"
    "ds_read_b32 v106, v106\n\t"
    "v_and_or_b32 v107, v83, s44, v68\n\t"
    "ds_read_b32 v107, v107\n\t"
    "s_waitcnt vmcnt(36)\n\t"
    "v_lshl_add_u32 v84, v40, 3, s36\n\t"
    "v_lshl_add_u32 v85, v41, 3, s37\n\t"
    "v_lshl_add_u32 v86, v42, 3, s38\n\t"
    "v_lshl_add_u32 v87, v43, 3, s39\n\t"
    "v_lshl_add_u32 v88, v44, 3, s40\n\t"
    "v_lshl_add_u32 v89, v45, 3, s41\n\t"
    "v_lshl_add_u32 v90, v46, 3, s42\n\t"
    "v_lshl_add_u32 v91, v47, 3, s43\n\t"
    "buffer_load_dwordx4 v[24:27], v137, s[96:99], s47 offen offset:0\n\t"
    "buffer_load_dwordx4 v[28:31], v138, s[96:99], s47 offen offset:0\n\t"
    "buffer_load_dwordx4 v[32:35], v137, s[96:99], s47 offen offset:64\n\t"
    "buffer_load_dwordx4 v[36:39], v138, s[96:99], s47 offen offset:64\n\t"
    "s_add_u32 s47, s47, 0x100000\n\t"
    "buffer_load_dwordx4 v[40:43], v137, s[96:99], s47 offen offset:0\n\t"
    "buffer_load_dwordx4 v[44:47], v138, s[96:99], s47 offen offset:0\n\t"
    "s_waitcnt lgkmcnt(11)\n\t"
    "v_and_b32 v123, v123, v115\n\t"
    "v_perm_b32 v123, v123, v123, s46\n\t"
    "ds_or_b32 v97, v123\n\t"
    "v_and_b32 v124, v124, v116\n\t"
    "v_perm_b32 v124, v124, v124, s46\n\t"
    "ds_or_b32 v98, v124\n\t"
    "v_and_b32 v125, v125, v117\n\t"
    "v_perm_b32 v125, v125, v125, s46\n\t"
    "ds_or_b32 v99, v125\n\t"
    "s_waitcnt lgkmcnt(11)\n\t"
    "buffer_store_dwordx4 v[72:75], v66, %12, s49 offen offset:192 sc1\n\t"
    "v_mov_b32 v129, 0xffff0000\n\t"
    "s_waitcnt lgkmcnt(7)\n\t"
    "v_perm_b32 v100, v100, v76, v126\n\t"
    "v_perm_b32 v101, v101, v77, v126\n\t"
    "v_perm_b32 v102, v102, v78, v126\n\t"
    "v_perm_b32 v103, v103, v79, v126\n\t"
    "s_waitcnt lgkmcnt(3)\n\t"
    "v_perm_b32 v104, v104, v80, v126\n\t"
    "v_perm_b32 v105, v105, v81, v126\n\t"
    "v_perm_b32 v106, v106, v82, v126\n\t"
    "v_perm_b32 v107, v107, v83, v126\n\t"
    "s_waitcnt vmcnt(33)\n\t"
    "ds_write_b128 v141, v[156:159] offset:0\n\t"
    "ds_write_b128 v141, v[160:163] offset:256\n\t"
    "ds_read_b64_tr_b16 v[156:157], v142 offset:0\n\t"
    "ds_read_b64_tr_b16 v[158:159], v142 offset:256\n\t"
    "ds_read_b64_tr_b16 v[160:161], v142 offset:32\n\t"
    "ds_read_b64_tr_b16 v[162:163], v142 offset:288\n\t"
    "s_waitcnt vmcnt(31)\n\t"
    "ds_write_b128 v141, v[164:167] offset:2112\n\t"
    "ds_write_b128 v141, v[168:171] offset:2368\n\t"
    "ds_read_b64_tr_b16 v[164:165], v142 offset:2112\n\t"
    "ds_read_b64_tr_b16 v[166:167], v142 offset:2368\n\t"
    "ds_read_b64_tr_b16 v[168:169], v142 offset:2144\n\t"
    "ds_read_b64_tr_b16 v[170:171], v142 offset:2400\n\t"
    "v_max_u32 v108, v100, v101\n\t"
    "v_min_u32 v101, v100, v101\n\t"
    "v_max_u32 v100, v102, v103\n\t"
    "v_min_u32 v103, v102, v103\n\t"
    "v_max_u32 v102, v104, v105\n\t"
    "v_min_u32 v105, v104, v105\n\t"
    "v_max_u32 v104, v106, v107\n\t"
    "v_min_u32 v107, v106, v107\n\t"
    "v_max_u32 v106, v108, v100\n\t"
    "v_min_u32 v100, v108, v100\n\t"
    "v_max_u32 v108, v101, v103\n\t"
    "v_min_u32 v103, v101, v103\n\t"
    "v_max_u32 v101, v102, v104\n\t"
    "v_min_u32 v104, v102, v104\n\t"
    "v_max_u32 v102, v105, v107\n\t"
    "v_min_u32 v107, v105, v107\n\t"
    "v_bfi_b32 v130, v106, 0, v129\n\t"
    "v_cmp_eq_u32 vcc, 0, v130\n\t"
    "v_and_or_b32 v92, v106, s44, v68\n\t"
    "s_nop 0\n\t"
    "v_cndmask_b32 v130, v130, v129, vcc\n\t"
    "v_ffbl_b32 v131, v130\n\t"
    "v_and_b32 v134, 7, v131\n\t"
    "v_lshlrev_b32 v134, v134, s45\n\t"
    "v_xor_b32 v129, v129, v134\n\t"
    "v_xor_b32 v132, v131, v127\n\t"
    "v_bfi_b32 v130, v101, 0, v129\n\t"
    "v_cmp_eq_u32 vcc, 0, v130\n\t"
    "v_and_or_b32 v93, v101, s44, v68\n\t"
    "v_xor_b32 v133, 23, v132\n\t"
    "v_lshlrev_b32 v110, v132, v136\n\t"
    "v_and_b32 v132, 7, v132\n\t"
    "v_lshl_or_b32 v110, v136, v133, v110\n\t"
    "v_lshl_add_u32 v132, v132, 1, v128\n\t"
    "ds_or_rtn_b32 v118, v92, v110\n\t"
    "ds_write_b16 v132, v106\n\t"
    "v_cndmask_b32 v130, v130, v129, vcc\n\t"
    "v_ffbl_b32 v131, v130\n\t"
    "v_and_b32 v134, 7, v131\n\t"
    "v_lshlrev_b32 v134, v134, s45\n\t"
    "v_xor_b32 v129, v129, v134\n\t"
    "v_xor_b32 v132, v131, v127\n\t"
    "v_bfi_b32 v130, v108, 0, v129\n\t"
    "v_cmp_eq_u32 vcc, 0, v130\n\t"
    "v_and_or_b32 v94, v108, s44, v68\n\t"
    "v_xor_b32 v133, 23, v132\n\t"
    "v_lshlrev_b32 v111, v132, v136\n\t"
    "v_and_b32 v132, 7, v132\n\t"
    "v_lshl_or_b32 v111, v136, v133, v111\n\t"
    "v_lshl_add_u32 v132, v132, 1, v128\n\t"
    "ds_or_rtn_b32 v119, v93, v111\n\t"
    "ds_write_b16 v132, v101\n\t"
    "v_cndmask_b32 v130, v130, v129, vcc\n\t"
    "v_ffbl_b32 v131, v130\n\t"
    "v_and_b32 v134, 7, v131\n\t"
    "v_lshlrev_b32 v134, v134, s45\n\t"
    "v_xor_b32 v129, v129, v134\n\t"
    "v_xor_b32 v132, v131, v127\n\t"
    "s_waitcnt lgkmcnt(10)\n\t"
    "buffer_store_dwordx4 v[156:159], v139, %12, s48 offen sc1\n\t"
    "buffer_store_dwordx4 v[160:163], v140, %12, s48 offen sc1\n\t"
    "s_add_u32 s48, s48, 819200\n\t"
    "s_waitcnt lgkmcnt(4)\n\t"
    "buffer_store_dwordx4 v[164:167], v139, %12, s48 offen sc1\n\t"
    "buffer_store_dwordx4 v[168:171], v140, %12, s48 offen sc1\n\t"
    "s_add_u32 s48, s48, -811008\n\t"
    "s_waitcnt vmcnt(33)\n\t"
    "ds_write_b128 v141, v[172:175] offset:0\n\t"
    "ds_write_b128 v141, v[176:179] offset:256\n\t"
    "ds_read_b64_tr_b16 v[172:173], v142 offset:0\n\t"
    "ds_read_b64_tr_b16 v[174:175], v142 offset:256\n\t"
    "ds_read_b64_tr_b16 v[176:177], v142 offset:32\n\t"
    "ds_read_b64_tr_b16 v[178:179], v142 offset:288\n\t"
    "s_waitcnt vmcnt(31)\n\t"
    "ds_write_b128 v141, v[180:183] offset:2112\n\t"
    "ds_write_b128 v141, v[184:187] offset:2368\n\t"
    "ds_read_b64_tr_b16 v[180:181], v142 offset:2112\n\t"
    "ds_read_b64_tr_b16 v[182:183], v142 offset:2368\n\t"
    "ds_read_b64_tr_b16 v[184:185], v142 offset:2144\n\t"
    "ds_read_b64_tr_b16 v[186:187], v142 offset:2400\n\t"
    "v_bfi_b32 v130, v100, 0, v129\n\t"
    "v_cmp_eq_u32 vcc, 0, v130\n\t"
    "v_and_or_b32 v95, v100, s44, v68\n\t"
    "v_xor_b32 v133, 23, v132\n\t"
    "v_lshlrev_b32 v112, v132, v136\n\t"
    "v_and_b32 v132, 7, v132\n\t"
    "v_lshl_or_b32 v112, v136, v133, v112\n\t"
    "v_lshl_add_u32 v132, v132, 1, v128\n\t"
    "ds_or_rtn_b32 v120, v94, v112\n\t"
    "ds_write_b16 v132, v108\n\t"
    "v_cndmask_b32 v130, v130, v129, vcc\n\t"
    "v_ffbl_b32 v131, v130\n\t"
    "v_and_b32 v134, 7, v131\n\t"
    "v_lshlrev_b32 v134, v134, s45\n\t"
    "v_xor_b32 v129, v129, v134\n\t"
    "v_xor_b32 v132, v131, v127\n\t"
    "v_bfi_b32 v130, v102, 0, v129\n\t"
    "v_cmp_eq_u32 vcc, 0, v130\n\t"
    "v_and_or_b32 v96, v102, s44, v68\n\t"
    "v_xor_b32 v133, 23, v132\n\t"
    "v_lshlrev_b32 v113, v132, v136\n\t"
    "v_and_b32 v132, 7, v132\n\t"
    "v_lshl_or_b32 v113, v136, v133, v113\n\t"
    "v_lshl_add_u32 v132, v132, 1, v128\n\t"
    "ds_or_rtn_b32 v121, v95, v113\n\t"
    "ds_write_b16 v132, v100\n\t"
    "v_cndmask_b32 v130, v130, v129, vcc\n\t"
    "v_ffbl_b32 v131, v130\n\t"
    "v_and_b32 v134, 7, v131\n\t"
    "v_lshlrev_b32 v134, v134, s45\n\t"
    "v_xor_b32 v129, v129, v134\n\t"
    "v_xor_b32 v132, v131, v127\n\t"
    "s_waitcnt lgkmcnt(15)\n\t"
    "v_and_b32 v118, v118, v110\n\t"
    "v_perm_b32 v118, v118, v118, s46\n\t"
    "ds_or_b32 v92, v118\n\t"
    "s_lshl_b32 s52, %14, 2\n\t"
    "s_add_u32 s52, s52, 49291264\n\t"
    "buffer_load_dword v71, off, %12, s52 offset:1024 sc1\n\t"
    "v_bfi_b32 v130, v104, 0, v129\n\t"
    "v_cmp_eq_u32 vcc, 0, v130\n\t"
    "v_and_or_b32 v97, v104, s44, v68\n\t"
    "v_xor_b32 v133, 23, v132\n\t"
    "v_lshlrev_b32 v114, v132, v136\n\t"
    "v_and_b32 v132, 7, v132\n\t"
    "v_lshl_or_b32 v114, v136, v133, v114\n\t"
    "v_lshl_add_u32 v132, v132, 1, v128\n\t"
    "ds_or_rtn_b32 v122, v96, v114\n\t"
    "ds_write_b16 v132, v102\n\t"
    "v_cndmask_b32 v130, v130, v129, vcc\n\t"
    "v_ffbl_b32 v131, v130\n\t"
    "v_and_b32 v134, 7, v131\n\t"
    "v_lshlrev_b32 v134, v134, s45\n\t"
    "v_xor_b32 v129, v129, v134\n\t"
    "v_xor_b32 v132, v131, v127\n\t"
    "v_and_b32 v119, v119, v111\n\t"
    "v_perm_b32 v119, v119, v119, s46\n\t"
    "ds_or_b32 v93, v119\n\t"
    "s_waitcnt lgkmcnt(14)\n\t"
    "buffer_store_dwordx4 v[172:175], v139, %12, s48 offen sc1\n\t"
    "buffer_store_dwordx4 v[176:179], v140, %12, s48 offen sc1\n\t"
    "s_add_u32 s48, s48, 819200\n\t"
    "s_waitcnt lgkmcnt(8)\n\t"
    "buffer_store_dwordx4 v[180:183], v139, %12, s48 offen sc1\n\t"
    "buffer_store_dwordx4 v[184:187], v140, %12, s48 offen sc1\n\t"
    "s_add_u32 s48, s48, -811008\n\t"
    "s_waitcnt vmcnt(34)\n\t"
    "ds_write_b128 v141, v[188:191] offset:0\n\t"
    "ds_write_b128 v141, v[192:195] offset:256\n\t"
    "ds_read_b64_tr_b16 v[188:189], v142 offset:0\n\t"
    "ds_read_b64_tr_b16 v[190:191], v142 offset:256\n\t"
    "ds_read_b64_tr_b16 v[192:193], v142 offset:32\n\t"
    "ds_read_b64_tr_b16 v[194:195], v142 offset:288\n\t"
    "s_waitcnt vmcnt(32)\n\t"
    "ds_write_b128 v141, v[196:199] offset:2112\n\t"
    "ds_write_b128 v141, v[200:203] offset:2368\n\t"
    "ds_read_b64_tr_b16 v[196:197], v142 offset:2112\n\t"
    "ds_read_b64_tr_b16 v[198:199], v142 offset:2368\n\t"
    "ds_read_b64_tr_b16 v[200:201], v142 offset:2144\n\t"
    "ds_read_b64_tr_b16 v[202:203], v142 offset:2400\n\t"
    "v_bfi_b32 v130, v103, 0, v129\n\t"
    "v_cmp_eq_u32 vcc, 0, v130\n\t"
    "v_and_or_b32 v98, v103, s44, v68\n\t"
    "v_xor_b32 v133, 23, v132\n\t"
    "v_lshlrev_b32 v115, v132, v136\n\t"
    "v_and_b32 v132, 7, v132\n\t"
    "v_lshl_or_b32 v115, v136, v133, v115\n\t"
    "v_lshl_add_u32 v132, v132, 1, v128\n\t"
    "ds_or_rtn_b32 v123, v97, v115\n\t"
    "ds_write_b16 v132, v104\n\t"
    "v_cndmask_b32 v130, v130, v129, vcc\n\t"
    "v_ffbl_b32 v131, v130\n\t"
    "v_and_b32 v134, 7, v131\n\t"
    "v_lshlrev_b32 v134, v134, s45\n\t"
    "v_xor_b32 v129, v129, v134\n\t"
    "v_xor_b32 v132, v131, v127\n\t"
    "s_waitcnt lgkmcnt(15)\n\t"
    "v_and_b32 v120, v120, v112\n\t"
    "v_perm_b32 v120, v120, v120, s46\n\t"
    "ds_or_b32 v94, v120\n\t"
    "v_bfi_b32 v130, v107, 0, v129\n\t"
    "v_cmp_eq_u32 vcc, 0, v130\n\t"
    "v_and_or_b32 v99, v107, s44, v68\n\t"
    "v_xor_b32 v133, 23, v132\n\t"
    "v_lshlrev_b32 v116, v132, v136\n\t"
    "v_and_b32 v132, 7, v132\n\t"
    "v_lshl_or_b32 v116, v136, v133, v116\n\t"
    "v_lshl_add_u32 v132, v132, 1, v128\n\t"
    "ds_or_rtn_b32 v124, v98, v116\n\t"
    "ds_write_b16 v132, v103\n\t"
    "v_cndmask_b32 v130, v130, v129, vcc\n\t"
    "v_ffbl_b32 v131, v130\n\t"
    "v_xor_b32 v132, v131, v127\n\t"
    "v_and_b32 v121, v121, v113\n\t"
    "v_perm_b32 v121, v121, v121, s46\n\t"
    "ds_or_b32 v95, v121\n\t"
    "v_xor_b32 v133, 23, v132\n\t"
    "v_lshlrev_b32 v117, v132, v136\n\t"
    "v_and_b32 v132, 7, v132\n\t"
    "v_lshl_or_b32 v117, v136, v133, v117\n\t"
    "v_lshl_add_u32 v132, v132, 1, v128\n\t"
    "ds_or_rtn_b32 v125, v99, v117\n\t"
    "ds_write_b16 v132, v107\n\t"
    "v_and_b32 v122, v122, v114\n\t"
    "v_perm_b32 v122, v122, v122, s46\n\t"
    "ds_or_b32 v96, v122\n\t"
    "ds_read_b128 v[72:75], v128\n\t"
    "s_waitcnt lgkmcnt(15)\n\t"
    "buffer_store_dwordx4 v[188:191], v139, %12, s48 offen sc1\n\t"
    "buffer_store_dwordx4 v[192:195], v140, %12, s48 offen sc1\n\t"
    "s_add_u32 s48, s48, 819200\n\t"
    "s_waitcnt lgkmcnt(10)\n\t"
    "buffer_store_dwordx4 v[196:199], v139, %12, s48 offen sc1\n\t"
    "buffer_store_dwordx4 v[200:203], v140, %12, s48 offen sc1\n\t"
    "s_add_u32 s48, s48, -811008\n\t"
    "v_mov_b32 v134, 1036\n\t"
    "L_asvsp12_%=:\n\t"
    "ds_read_b32 v130, v134\n\t"
    "s_waitcnt lgkmcnt(0)\n\t"
    "v_readfirstlane_b32 s50, v130\n\t"
    "s_nop 0\n\t"
    "s_cmp_lt_u32 s50, 1\n\t"
    "s_cbranch_scc1 L_asvsp12_%=\n\t"
    "v_and_or_b32 v100, v84, s44, v69\n\t"
    "ds_read_b32 v100, v100\n\t"
    "v_and_or_b32 v101, v85, s44, v69\n\t"
    "ds_read_b32 v101, v101\n\t"
    "v_and_or_b32 v102, v86, s44, v69\n\t"
    "ds_read_b32 v102, v102\n\t"
    "v_and_or_b32 v103, v87, s44, v69\n\t"
    "ds_read_b32 v103, v103\n\t"
    "v_and_or_b32 v104, v88, s44, v69\n\t"
    "ds_read_b32 v104, v104\n\t"
    "v_and_or_b32 v105, v89, s44, v69\n\t"
    "ds_read_b32 v105, v105\n\t"
    "v_and_or_b32 v106, v90, s44, v69\n\t"
    "ds_read_b32 v106, v106\n\t"
    "v_and_or_b32 v107, v91, s44, v69\n\t"
    "ds_read_b32 v107, v107\n\t"
    "v_lshl_add_u32 v76, v48, 3, s36\n\t"
    "v_lshl_add_u32 v77, v49, 3, s37\n\t"
    "v_lshl_add_u32 v78, v50, 3, s38\n\t"
    "v_lshl_add_u32 v79, v51, 3, s39\n\t"
    "v_lshl_add_u32 v80, v52, 3, s40\n\t"
    "v_lshl_add_u32 v81, v53, 3, s41\n\t"
    "v_lshl_add_u32 v82, v54, 3, s42\n\t"
    "v_lshl_add_u32 v83, v55, 3, s43\n\t"
    "v_mov_b32 v134, 2076\n\t"
    "L_asvsp1052_%=:\n\t"
    "ds_read_b32 v130, v134\n\t"
    "s_waitcnt lgkmcnt(0)\n\t"
    "v_readfirstlane_b32 s50, v130\n\t"
    "s_nop 0\n\t"
    "s_cmp_lt_u32 s50, 1\n\t"
    "s_cbranch_scc1 L_asvsp1052_%=\n\t"
    "v_mov_b32 v70, 2072\n\t"
    "ds_read_b32 v70, v70\n\t"
    "s_and_b32 s50, %14, 7\n\t"
    "s_lshr_b32 s51, %14, 5\n\t"
    "s_add_u32 s50, s50, 8\n\t"
    "s_lshl_b32 s50, s50, 7\n\t"
    "s_lshl_b32 s51, s51, 7\n\t"
    "v_mov_b32 v132, s51\n\t"
    "v_writelane_b32 v132, s50, 0\n\t"
    "s_waitcnt vmcnt(8)\n\t"
    "s_waitcnt vmcnt(0)\n\t"
    "s_waitcnt lgkmcnt(0)\n\t"
    "v_cmp_ne_u32 vcc, v71, v70\n\t"
    "s_cbranch_vccz L_ahsok_%=\n\t"
    "s_mov_b32 s51, 1048576\n\t"
    "L_ahs_%=:\n\t"
    "buffer_load_dword v71, off, %12, s52 offset:1024 sc1\n\t"
    "s_sub_u32 s51, s51, 1\n\t"
    "s_waitcnt vmcnt(0)\n\t"
    "s_cmp_eq_u32 s51, 0\n\t"
    "s_cbranch_scc1 L_ahsok_%=\n\t"
    "v_cmp_ne_u32 vcc, v71, v70\n\t"
    "s_cbranch_vccnz L_ahs_%=\n\t"
    "L_ahsok_%=:\n\t"
    "s_mov_b32 s50, 49284096\n\t"
    "s_mov_b64 exec, 3\n\t"
    "buffer_atomic_add v136, v132, %12, s50 offen sc1\n\t"
    "s_mov_b64 exec, -1\n\t"
    "buffer_load_dwordx4 v[156:159], v137, s[96:99], s47 offen offset:64\n\t"
    "buffer_load_dwordx4 v[160:163], v138, s[96:99], s47 offen offset:64\n\t"
    "s_add_u32 s47, s47, 0x100000\n\t"
    "buffer_load_dwordx4 v[164:167], v137, s[96:99], s47 offen offset:0\n\t"
    "buffer_load_dwordx4 v[168:171], v138, s[96:99], s47 offen offset:0\n\t"
    "buffer_load_dwordx4 v[172:175], v137, s[96:99], s47 offen offset:64\n\t"
    "buffer_load_dwordx4 v[176:179], v138, s[96:99], s47 offen offset:64\n\t"
    "s_add_u32 s47, s47, 0x100000\n\t"
    "buffer_load_dwordx4 v[180:183], v137, s[96:99], s47 offen offset:0\n\t"
    "buffer_load_dwordx4 v[184:187], v138, s[96:99], s47 offen offset:0\n\t"
    "buffer_load_dwordx4 v[188:191], v137, s[96:99], s47 offen offset:64\n\t"
    "buffer_load_dwordx4 v[192:195], v138, s[96:99], s47 offen offset:64\n\t"
    "s_add_u32 s47, s47, 0x100000\n\t"
    "buffer_load_dwordx4 v[196:199], v137, s[96:99], s47 offen offset:0\n\t"
    "buffer_load_dwordx4 v[200:203], v138, s[96:99], s47 offen offset:0\n\t"
    "v_and_b32 v123, v123, v115\n\t"
    "v_perm_b32 v123, v123, v123, s46\n\t"
    "ds_or_b32 v97, v123\n\t"
    "v_and_b32 v124, v124, v116\n\t"
    "v_perm_b32 v124, v124, v124, s46\n\t"
    "ds_or_b32 v98, v124\n\t"
    "v_and_b32 v125, v125, v117\n\t"
    "v_perm_b32 v125, v125, v125, s46\n\t"
    "ds_or_b32 v99, v125\n\t"
    "buffer_store_dwordx4 v[72:75], v66, %12, s49 offen offset:256 sc1\n\t"
    "v_mov_b32 v129, 0xffff0000\n\t"
    "v_perm_b32 v100, v100, v84, v126\n\t"
    "v_perm_b32 v101, v101, v85, v126\n\t"
    "v_perm_b32 v102, v102, v86, v126\n\t"
    "v_perm_b32 v103, v103, v87, v126\n\t"
    "v_perm_b32 v104, v104, v88, v126\n\t"
    "v_perm_b32 v105, v105, v89, v126\n\t"
    "v_perm_b32 v106, v106, v90, v126\n\t"
    "v_perm_b32 v107, v107, v91, v126\n\t"
    "ds_write_b128 v141, v[204:207] offset:0\n\t"
    "ds_write_b128 v141, v[208:211] offset:256\n\t"
    "ds_read_b64_tr_b16 v[204:205], v142 offset:0\n\t"
    "ds_read_b64_tr_b16 v[206:207], v142 offset:256\n\t"
    "ds_read_b64_tr_b16 v[208:209], v142 offset:32\n\t"
    "ds_read_b64_tr_b16 v[210:211], v142 offset:288\n\t"
    "ds_write_b128 v141, v[212:215] offset:2112\n\t"
    "ds_write_b128 v141, v[216:219] offset:2368\n\t"
    "ds_read_b64_tr_b16 v[212:213], v142 offset:2112\n\t"
    "ds_read_b64_tr_b16 v[214:215], v142 offset:2368\n\t"
    "ds_read_b64_tr_b16 v[216:217], v142 offset:2144\n\t"
    "ds_read_b64_tr_b16 v[218:219], v142 offset:2400\n\t"
    "v_max_u32 v108, v100, v101\n\t"
    "v_min_u32 v101, v100, v101\n\t"
    "v_max_u32 v100, v102, v103\n\t"
    "v_min_u32 v103, v102, v103\n\t"
    "v_max_u32 v102, v104, v105\n\t"
    "v_min_u32 v105, v104, v105\n\t"
    "v_max_u32 v104, v106, v107\n\t"
    "v_min_u32 v107, v106, v107\n\t"
    "v_max_u32 v106, v108, v100\n\t"
    "v_min_u32 v100, v108, v100\n\t"
    "v_max_u32 v108, v101, v103\n\t"
    "v_min_u32 v103, v101, v103\n\t"
    "v_max_u32 v101, v102, v104\n\t"
    "v_min_u32 v104, v102, v104\n\t"
    "v_max_u32 v102, v105, v107\n\t"
    "v_min_u32 v107, v105, v107\n\t"
    "v_bfi_b32 v130, v106, 0, v129\n\t"
    "v_cmp_eq_u32 vcc, 0, v130\n\t"
    "v_and_or_b32 v92, v106, s44, v69\n\t"
    "s_nop 0\n\t"
    "v_cndmask_b32 v130, v130, v129, vcc\n\t"
    "v_ffbl_b32 v131, v130\n\t"
    "v_and_b32 v134, 7, v131\n\t"
    "v_lshlrev_b32 v134, v134, s45\n\t"
    "v_xor_b32 v129, v129, v134\n\t"
    "v_xor_b32 v132, v131, v127\n\t"
    "v_bfi_b32 v130, v101, 0, v129\n\t"
    "v_cmp_eq_u32 vcc, 0, v130\n\t"
    "v_and_or_b32 v93, v101, s44, v69\n\t"
    "v_xor_b32 v133, 23, v132\n\t"
    "v_lshlrev_b32 v110, v132, v136\n\t"
    "v_and_b32 v132, 7, v132\n\t"
    "v_lshl_or_b32 v110, v136, v133, v110\n\t"
    "v_lshl_add_u32 v132, v132, 1, v128\n\t"
    "ds_or_rtn_b32 v118, v92, v110\n\t"
    "ds_write_b16 v132, v106\n\t"
    "v_cndmask_b32 v130, v130, v129, vcc\n\t"
    "v_ffbl_b32 v131, v130\n\t"
    "v_and_b32 v134, 7, v131\n\t"
    "v_lshlrev_b32 v134, v134, s45\n\t"
    "v_xor_b32 v129, v129, v134\n\t"
    "v_xor_b32 v132, v131, v127\n\t"
    "v_bfi_b32 v130, v108, 0, v129\n\t"
    "v_cmp_eq_u32 vcc, 0, v130\n\t"
    "v_and_or_b32 v94, v108, s44, v69\n\t"
    "v_xor_b32 v133, 23, v132\n\t"
    "v_lshlrev_b32 v111, v132, v136\n\t"
    "v_and_b32 v132, 7, v132\n\t"
    "v_lshl_or_b32 v111, v136, v133, v111\n\t"
    "v_lshl_add_u32 v132, v132, 1, v128\n\t"
    "ds_or_rtn_b32 v119, v93, v111\n\t"
    "ds_write_b16 v132, v101\n\t"
    "v_cndmask_b32 v130, v130, v129, vcc\n\t"
    "v_ffbl_b32 v131, v130\n\t"
    "v_and_b32 v134, 7, v131\n\t"
    "v_lshlrev_b32 v134, v134, s45\n\t"
    "v_xor_b32 v129, v129, v134\n\t"
    "v_xor_b32 v132, v131, v127\n\t"
    "s_waitcnt lgkmcnt(10)\n\t"
    "buffer_store_dwordx4 v[204:207], v139, %12, s48 offen sc1\n\t"
    "buffer_store_dwordx4 v[208:211], v140, %12, s48 offen sc1\n\t"
    "s_add_u32 s48, s48, 819200\n\t"
    "s_waitcnt lgkmcnt(4)\n\t"
    "buffer_store_dwordx4 v[212:215], v139, %12, s48 offen sc1\n\t"
    "buffer_store_dwordx4 v[216:219], v140, %12, s48 offen sc1\n\t"
    "s_add_u32 s48, s48, -811008\n\t"
    "ds_write_b128 v141, v[220:223] offset:0\n\t"
    "ds_write_b128 v141, v[224:227] offset:256\n\t"
    "ds_read_b64_tr_b16 v[220:221], v142 offset:0\n\t"
    "ds_read_b64_tr_b16 v[222:223], v142 offset:256\n\t"
    "ds_read_b64_tr_b16 v[224:225], v142 offset:32\n\t"
    "ds_read_b64_tr_b16 v[226:227], v142 offset:288\n\t"
    "ds_write_b128 v141, v[228:231] offset:2112\n\t"
    "ds_write_b128 v141, v[232:235] offset:2368\n\t"
    "ds_read_b64_tr_b16 v[228:229], v142 offset:2112\n\t"
    "ds_read_b64_tr_b16 v[230:231], v142 offset:2368\n\t"
    "ds_read_b64_tr_b16 v[232:233], v142 offset:2144\n\t"
    "ds_read_b64_tr_b16 v[234:235], v142 offset:2400\n\t"
    "v_bfi_b32 v130, v100, 0, v129\n\t"
    "v_cmp_eq_u32 vcc, 0, v130\n\t"
    "v_and_or_b32 v95, v100, s44, v69\n\t"
    "v_xor_b32 v133, 23, v132\n\t"
    "v_lshlrev_b32 v112, v132, v136\n\t"
    "v_and_b32 v132, 7, v132\n\t"
    "v_lshl_or_b32 v112, v136, v133, v112\n\t"
    "v_lshl_add_u32 v132, v132, 1, v128\n\t"
    "ds_or_rtn_b32 v120, v94, v112\n\t"
    "ds_write_b16 v132, v108\n\t"
    "v_cndmask_b32 v130, v130, v129, vcc\n\t"
    "v_ffbl_b32 v131, v130\n\t"
    "v_and_b32 v134, 7, v131\n\t"
    "v_lshlrev_b32 v134, v134, s45\n\t"
    "v_xor_b32 v129, v129, v134\n\t"
    "v_xor_b32 v132, v131, v127\n\t"
    "v_bfi_b32 v130, v102, 0, v129\n\t"
    "v_cmp_eq_u32 vcc, 0, v130\n\t"
    "v_and_or_b32 v96, v102, s44, v69\n\t"
    "v_xor_b32 v133, 23, v132\n\t"
    "v_lshlrev_b32 v113, v132, v136\n\t"
    "v_and_b32 v132, 7, v132\n\t"
    "v_lshl_or_b32 v113, v136, v133, v113\n\t"
    "v_lshl_add_u32 v132, v132, 1, v128\n\t"
    "ds_or_rtn_b32 v121, v95, v113\n\t"
    "ds_write_b16 v132, v100\n\t"
    "v_cndmask_b32 v130, v130, v129, vcc\n\t"
    "v_ffbl_b32 v131, v130\n\t"
    "v_and_b32 v134, 7, v131\n\t"
    "v_lshlrev_b32 v134, v134, s45\n\t"
    "v_xor_b32 v129, v129, v134\n\t"
    "v_xor_b32 v132, v131, v127\n\t"
    "s_waitcnt lgkmcnt(15)\n\t"
    "v_and_b32 v118, v118, v110\n\t"
    "v_perm_b32 v118, v118, v118, s46\n\t"
    "ds_or_b32 v92, v118\n\t"
    "v_bfi_b32 v130, v104, 0, v129\n\t"
    "v_cmp_eq_u32 vcc, 0, v130\n\t"
    "v_and_or_b32 v97, v104, s44, v69\n\t"
    "v_xor_b32 v133, 23, v132\n\t"
    "v_lshlrev_b32 v114, v132, v136\n\t"
    "v_and_b32 v132, 7, v132\n\t"
    "v_lshl_or_b32 v114, v136, v133, v114\n\t"
    "v_lshl_add_u32 v132, v132, 1, v128\n\t"
    "ds_or_rtn_b32 v122, v96, v114\n\t"
    "ds_write_b16 v132, v102\n\t"
    "v_cndmask_b32 v130, v130, v129, vcc\n\t"
    "v_ffbl_b32 v131, v130\n\t"
    "v_and_b32 v134, 7, v131\n\t"
    "v_lshlrev_b32 v134, v134, s45\n\t"
    "v_xor_b32 v129, v129, v134\n\t"
    "v_xor_b32 v132, v131, v127\n\t"
    "v_and_b32 v119, v119, v111\n\t"
    "v_perm_b32 v119, v119, v119, s46\n\t"
    "ds_or_b32 v93, v119\n\t"
    "s_waitcnt lgkmcnt(14)\n\t"
    "buffer_store_dwordx4 v[220:223], v139, %12, s48 offen sc1\n\t"
    "buffer_store_dwordx4 v[224:227], v140, %12, s48 offen sc1\n\t"
    "s_add_u32 s48, s48, 819200\n\t"
    "s_waitcnt lgkmcnt(8)\n\t"
    "buffer_store_dwordx4 v[228:231], v139, %12, s48 offen sc1\n\t"
    "buffer_store_dwordx4 v[232:235], v140, %12, s48 offen sc1\n\t"
    "s_add_u32 s48, s48, -811008\n\t"
    "ds_write_b128 v141, v[236:239] offset:0\n\t"
    "ds_write_b128 v141, v[240:243] offset:256\n\t"
    "ds_read_b64_tr_b16 v[236:237], v142 offset:0\n\t"
    "ds_read_b64_tr_b16 v[238:239], v142 offset:256\n\t"
    "ds_read_b64_tr_b16 v[240:241], v142 offset:32\n\t"
    "ds_read_b64_tr_b16 v[242:243], v142 offset:288\n\t"
    "ds_write_b128 v141, v[244:247] offset:2112\n\t"
    "ds_write_b128 v141, v[248:251] offset:2368\n\t"
    "ds_read_b64_tr_b16 v[244:245], v142 offset:2112\n\t"
    "ds_read_b64_tr_b16 v[246:247], v142 offset:2368\n\t"
    "ds_read_b64_tr_b16 v[248:249], v142 offset:2144\n\t"
    "ds_read_b64_tr_b16 v[250:251], v142 offset:2400\n\t"
    "v_bfi_b32 v130, v103, 0, v129\n\t"
    "v_cmp_eq_u32 vcc, 0, v130\n\t"
    "v_and_or_b32 v98, v103, s44, v69\n\t"
    "v_xor_b32 v133, 23, v132\n\t"
    "v_lshlrev_b32 v115, v132, v136\n\t"
    "v_and_b32 v132, 7, v132\n\t"
    "v_lshl_or_b32 v115, v136, v133, v115\n\t"
    "v_lshl_add_u32 v132, v132, 1, v128\n\t"
    "ds_or_rtn_b32 v123, v97, v115\n\t"
    "ds_write_b16 v132, v104\n\t"
    "v_cndmask_b32 v130, v130, v129, vcc\n\t"
    "v_ffbl_b32 v131, v130\n\t"
    "v_and_b32 v134, 7, v131\n\t"
    "v_lshlrev_b32 v134, v134, s45\n\t"
    "v_xor_b32 v129, v129, v134\n\t"
    "v_xor_b32 v132, v131, v127\n\t"
    "s_waitcnt lgkmcnt(15)\n\t"
    "v_and_b32 v120, v120, v112\n\t"
    "v_perm_b32 v120, v120, v120, s46\n\t"
    "ds_or_b32 v94, v120\n\t"
    "v_bfi_b32 v130, v107, 0, v129\n\t"
    "v_cmp_eq_u32 vcc, 0, v130\n\t"
    "v_and_or_b32 v99, v107, s44, v69\n\t"
    "v_xor_b32 v133, 23, v132\n\t"
    "v_lshlrev_b32 v116, v132, v136\n\t"
    "v_and_b32 v132, 7, v132\n\t"
    "v_lshl_or_b32 v116, v136, v133, v116\n\t"
    "v_lshl_add_u32 v132, v132, 1, v128\n\t"
    "ds_or_rtn_b32 v124, v98, v116\n\t"
    "ds_write_b16 v132, v103\n\t"
    "v_cndmask_b32 v130, v130, v129, vcc\n\t"
    "v_ffbl_b32 v131, v130\n\t"
    "v_xor_b32 v132, v131, v127\n\t"
    "v_and_b32 v121, v121, v113\n\t"
    "v_perm_b32 v121, v121, v121, s46\n\t"
    "ds_or_b32 v95, v121\n\t"
    "v_xor_b32 v133, 23, v132\n\t"
    "v_lshlrev_b32 v117, v132, v136\n\t"
    "v_and_b32 v132, 7, v132\n\t"
    "v_lshl_or_b32 v117, v136, v133, v117\n\t"
    "v_lshl_add_u32 v132, v132, 1, v128\n\t"
    "ds_or_rtn_b32 v125, v99, v117\n\t"
    "ds_write_b16 v132, v107\n\t"
    "v_and_b32 v122, v122, v114\n\t"
    "v_perm_b32 v122, v122, v122, s46\n\t"
    "ds_or_b32 v96, v122\n\t"
    "ds_read_b128 v[72:75], v128\n\t"
    "v_and_or_b32 v100, v76, s44, v69\n\t"
    "ds_read_b32 v100, v100\n\t"
    "v_and_or_b32 v101, v77, s44, v69\n\t"
    "ds_read_b32 v101, v101\n\t"
    "v_and_or_b32 v102, v78, s44, v69\n\t"
    "ds_read_b32 v102, v102\n\t"
    "v_and_or_b32 v103, v79, s44, v69\n\t"
    "ds_read_b32 v103, v103\n\t"
    "v_and_or_b32 v104, v80, s44, v69\n\t"
    "ds_read_b32 v104, v104\n\t"
    "v_and_or_b32 v105, v81, s44, v69\n\t"
    "ds_read_b32 v105, v105\n\t"
    "v_and_or_b32 v106, v82, s44, v69\n\t"
    "ds_read_b32 v106, v106\n\t"
    "v_and_or_b32 v107, v83, s44, v69\n\t"
    "ds_read_b32 v107, v107\n\t"
    "v_lshl_add_u32 v84, v56, 3, s36\n\t"
    "v_lshl_add_u32 v85, v57, 3, s37\n\t"
    "v_lshl_add_u32 v86, v58, 3, s38\n\t"
    "v_lshl_add_u32 v87, v59, 3, s39\n\t"
    "v_lshl_add_u32 v88, v60, 3, s40\n\t"
    "v_lshl_add_u32 v89, v61, 3, s41\n\t"
    "v_lshl_add_u32 v90, v62, 3, s42\n\t"
    "v_lshl_add_u32 v91, v63, 3, s43\n\t"
    "s_waitcnt lgkmcnt(15)\n\t"
    "buffer_store_dwordx4 v[236:239], v139, %12, s48 offen sc1\n\t"
    "buffer_store_dwordx4 v[240:243], v140, %12, s48 offen sc1\n\t"
    "s_add_u32 s48, s48, 819200\n\t"
    "buffer_store_dwordx4 v[244:247], v139, %12, s48 offen sc1\n\t"
    "buffer_store_dwordx4 v[248:251], v140, %12, s48 offen sc1\n\t"
    "s_add_u32 s48, s48, -811008\n\t"
    "buffer_load_dwordx4 v[48:51], v137, s[96:99], s47 offen offset:64\n\t"
    "buffer_load_dwordx4 v[52:55], v138, s[96:99], s47 offen offset:64\n\t"
    "s_add_u32 s47, s47, 0x100000\n\t"
    "buffer_load_dwordx4 v[204:207], v137, s[96:99], s47 offen offset:0\n\t"
    "buffer_load_dwordx4 v[208:211], v138, s[96:99], s47 offen offset:0\n\t"
    "buffer_load_dwordx4 v[212:215], v137, s[96:99], s47 offen offset:64\n\t"
    "buffer_load_dwordx4 v[216:219], v138, s[96:99], s47 offen offset:64\n\t"
    "s_add_u32 s47, s47, 0x100000\n\t"
    "buffer_load_dwordx4 v[220:223], v137, s[96:99], s47 offen offset:0\n\t"
    "buffer_load_dwordx4 v[224:227], v138, s[96:99], s47 offen offset:0\n\t"
    "buffer_load_dwordx4 v[228:231], v137, s[96:99], s47 offen offset:64\n\t"
    "buffer_load_dwordx4 v[232:235], v138, s[96:99], s47 offen offset:64\n\t"
    "s_add_u32 s47, s47, 0x100000\n\t"
    "s_waitcnt lgkmcnt(11)\n\t"
    "v_and_b32 v123, v123, v115\n\t"
    "v_perm_b32 v123, v123, v123, s46\n\t"
    "ds_or_b32 v97, v123\n\t"
    "v_and_b32 v124, v124, v116\n\t"
    "v_perm_b32 v124, v124, v124, s46\n\t"
    "ds_or_b32 v98, v124\n\t"
    "v_and_b32 v125, v125, v117\n\t"
    "v_perm_b32 v125, v125, v125, s46\n\t"
    "ds_or_b32 v99, v125\n\t"
    "s_waitcnt lgkmcnt(11)\n\t"
    "buffer_store_dwordx4 v[72:75], v67, %12, s49 offen offset:320 sc1\n\t"
    "v_mov_b32 v129, 0xffff0000\n\t"
    "s_waitcnt lgkmcnt(7)\n\t"
    "v_perm_b32 v100, v100, v76, v126\n\t"
    "v_perm_b32 v101, v101, v77, v126\n\t"
    "v_perm_b32 v102, v102, v78, v126\n\t"
    "v_perm_b32 v103, v103, v79, v126\n\t"
    "s_waitcnt lgkmcnt(3)\n\t"
    "v_perm_b32 v104, v104, v80, v126\n\t"
    "v_perm_b32 v105, v105, v81, v126\n\t"
    "v_perm_b32 v106, v106, v82, v126\n\t"
    "v_perm_b32 v107, v107, v83, v126\n\t"
    "ds_write_b128 v141, v[24:27] offset:0\n\t"
    "ds_write_b128 v141, v[28:31] offset:256\n\t"
    "ds_read_b64_tr_b16 v[24:25], v142 offset:0\n\t"
    "ds_read_b64_tr_b16 v[26:27], v142 offset:256\n\t"
    "ds_read_b64_tr_b16 v[28:29], v142 offset:32\n\t"
    "ds_read_b64_tr_b16 v[30:31], v142 offset:288\n\t"
    "ds_write_b128 v141, v[32:35] offset:2112\n\t"
    "ds_write_b128 v141, v[36:39] offset:2368\n\t"
    "ds_read_b64_tr_b16 v[32:33], v142 offset:2112\n\t"
    "ds_read_b64_tr_b16 v[34:35], v142 offset:2368\n\t"
    "ds_read_b64_tr_b16 v[36:37], v142 offset:2144\n\t"
    "ds_read_b64_tr_b16 v[38:39], v142 offset:2400\n\t"
    "v_max_u32 v108, v100, v101\n\t"
    "v_min_u32 v101, v100, v101\n\t"
    "v_max_u32 v100, v102, v103\n\t"
    "v_min_u32 v103, v102, v103\n\t"
    "v_max_u32 v102, v104, v105\n\t"
    "v_min_u32 v105, v104, v105\n\t"
    "v_max_u32 v104, v106, v107\n\t"
    "v_min_u32 v107, v106, v107\n\t"
    "v_max_u32 v106, v108, v100\n\t"
    "v_min_u32 v100, v108, v100\n\t"
    "v_max_u32 v108, v101, v103\n\t"
    "v_min_u32 v103, v101, v103\n\t"
    "v_max_u32 v101, v102, v104\n\t"
    "v_min_u32 v104, v102, v104\n\t"
    "v_max_u32 v102, v105, v107\n\t"
    "v_min_u32 v107, v105, v107\n\t"
    "v_bfi_b32 v130, v106, 0, v129\n\t"
    "v_cmp_eq_u32 vcc, 0, v130\n\t"
    "v_and_or_b32 v92, v106, s44, v69\n\t"
    "s_nop 0\n\t"
    "v_cndmask_b32 v130, v130, v129, vcc\n\t"
    "v_ffbl_b32 v131, v130\n\t"
    "v_and_b32 v134, 7, v131\n\t"
    "v_lshlrev_b32 v134, v134, s45\n\t"
    "v_xor_b32 v129, v129, v134\n\t"
    "v_xor_b32 v132, v131, v127\n\t"
    "v_bfi_b32 v130, v101, 0, v129\n\t"
    "v_cmp_eq_u32 vcc, 0, v130\n\t"
    "v_and_or_b32 v93, v101, s44, v69\n\t"
    "v_xor_b32 v133, 23, v132\n\t"
    "v_lshlrev_b32 v110, v132, v136\n\t"
    "v_and_b32 v132, 7, v132\n\t"
    "v_lshl_or_b32 v110, v136, v133, v110\n\t"
    "v_lshl_add_u32 v132, v132, 1, v128\n\t"
    "ds_or_rtn_b32 v118, v92, v110\n\t"
    "ds_write_b16 v132, v106\n\t"
    "v_cndmask_b32 v130, v130, v129, vcc\n\t"
    "v_ffbl_b32 v131, v130\n\t"
    "v_and_b32 v134, 7, v131\n\t"
    "v_lshlrev_b32 v134, v134, s45\n\t"
    "v_xor_b32 v129, v129, v134\n\t"
    "v_xor_b32 v132, v131, v127\n\t"
    "v_bfi_b32 v130, v108, 0, v129\n\t"
    "v_cmp_eq_u32 vcc, 0, v130\n\t"
    "v_and_or_b32 v94, v108, s44, v69\n\t"
    "v_xor_b32 v133, 23, v132\n\t"
    "v_lshlrev_b32 v111, v132, v136\n\t"
    "v_and_b32 v132, 7, v132\n\t"
    "v_lshl_or_b32 v111, v136, v133, v111\n\t"
    "v_lshl_add_u32 v132, v132, 1, v128\n\t"
    "ds_or_rtn_b32 v119, v93, v111\n\t"
    "ds_write_b16 v132, v101\n\t"
    "v_cndmask_b32 v130, v130, v129, vcc\n\t"
    "v_ffbl_b32 v131, v130\n\t"
    "v_and_b32 v134, 7, v131\n\t"
    "v_lshlrev_b32 v134, v134, s45\n\t"
    "v_xor_b32 v129, v129, v134\n\t"
    "v_xor_b32 v132, v131, v127\n\t"
    "s_waitcnt lgkmcnt(10)\n\t"
    "buffer_store_dwordx4 v[24:27], v139, %12, s48 offen sc1\n\t"
    "buffer_store_dwordx4 v[28:31], v140, %12, s48 offen sc1\n\t"
    "s_add_u32 s48, s48, 819200\n\t"
    "s_waitcnt lgkmcnt(4)\n\t"
    "buffer_store_dwordx4 v[32:35], v139, %12, s48 offen sc1\n\t"
    "buffer_store_dwordx4 v[36:39], v140, %12, s48 offen sc1\n\t"
    "s_add_u32 s48, s48, -811008\n\t"
    "ds_write_b128 v141, v[40:43] offset:0\n\t"
    "ds_write_b128 v141, v[44:47] offset:256\n\t"
    "ds_read_b64_tr_b16 v[40:41], v142 offset:0\n\t"
    "ds_read_b64_tr_b16 v[42:43], v142 offset:256\n\t"
    "ds_read_b64_tr_b16 v[44:45], v142 offset:32\n\t"
    "ds_read_b64_tr_b16 v[46:47], v142 offset:288\n\t"
    "v_bfi_b32 v130, v100, 0, v129\n\t"
    "v_cmp_eq_u32 vcc, 0, v130\n\t"
    "v_and_or_b32 v95, v100, s44, v69\n\t"
    "v_xor_b32 v133, 23, v132\n\t"
    "v_lshlrev_b32 v112, v132, v136\n\t"
    "v_and_b32 v132, 7, v132\n\t"
    "v_lshl_or_b32 v112, v136, v133, v112\n\t"
    "v_lshl_add_u32 v132, v132, 1, v128\n\t"
    "ds_or_rtn_b32 v120, v94, v112\n\t"
    "ds_write_b16 v132, v108\n\t"
    "v_cndmask_b32 v130, v130, v129, vcc\n\t"
    "v_ffbl_b32 v131, v130\n\t"
    "v_and_b32 v134, 7, v131\n\t"
    "v_lshlrev_b32 v134, v134, s45\n\t"
    "v_xor_b32 v129, v129, v134\n\t"
    "v_xor_b32 v132, v131, v127\n\t"
    "v_bfi_b32 v130, v102, 0, v129\n\t"
    "v_cmp_eq_u32 vcc, 0, v130\n\t"
    "v_and_or_b32 v96, v102, s44, v69\n\t"
    "v_xor_b32 v133, 23, v132\n\t"
    "v_lshlrev_b32 v113, v132, v136\n\t"
    "v_and_b32 v132, 7, v132\n\t"
    "v_lshl_or_b32 v113, v136, v133, v113\n\t"
    "v_lshl_add_u32 v132, v132, 1, v128\n\t"
    "ds_or_rtn_b32 v121, v95, v113\n\t"
    "ds_write_b16 v132, v100\n\t"
    "v_cndmask_b32 v130, v130, v129, vcc\n\t"
    "v_ffbl_b32 v131, v130\n\t"
    "v_and_b32 v134, 7, v131\n\t"
    "v_lshlrev_b32 v134, v134, s45\n\t"
    "v_xor_b32 v129, v129, v134\n\t"
    "v_xor_b32 v132, v131, v127\n\t"
    "s_waitcnt lgkmcnt(13)\n\t"
    "v_and_b32 v118, v118, v110\n\t"
    "v_perm_b32 v118, v118, v118, s46\n\t"
    "ds_or_b32 v92, v118\n\t"
    "v_bfi_b32 v130, v104, 0, v129\n\t"
    "v_cmp_eq_u32 vcc, 0, v130\n\t"
    "v_and_or_b32 v97, v104, s44, v69\n\t"
    "v_xor_b32 v133, 23, v132\n\t"
    "v_lshlrev_b32 v114, v132, v136\n\t"
    "v_and_b32 v132, 7, v132\n\t"
    "v_lshl_or_b32 v114, v136, v133, v114\n\t"
    "v_lshl_add_u32 v132, v132, 1, v128\n\t"
    "ds_or_rtn_b32 v122, v96, v114\n\t"
    "ds_write_b16 v132, v102\n\t"
    "v_cndmask_b32 v130, v130, v129, vcc\n\t"
    "v_ffbl_b32 v131, v130\n\t"
    "v_and_b32 v134, 7, v131\n\t"
    "v_lshlrev_b32 v134, v134, s45\n\t"
    "v_xor_b32 v129, v129, v134\n\t"
    "v_xor_b32 v132, v131, v127\n\t"
    "s_waitcnt lgkmcnt(14)\n\t"
    "v_and_b32 v119, v119, v111\n\t"
    "v_perm_b32 v119, v119, v119, s46\n\t"
    "ds_or_b32 v93, v119\n\t"
    "s_waitcnt lgkmcnt(8)\n\t"
    "buffer_store_dwordx4 v[40:43], v139, %12, s48 offen sc1\n\t"
    "buffer_store_dwordx4 v[44:47], v140, %12, s48 offen sc1\n\t"
    "s_add_u32 s48, s48, 819200\n\t"
    "v_bfi_b32 v130, v103, 0, v129\n\t"
    "v_cmp_eq_u32 vcc, 0, v130\n\t"
    "v_and_or_b32 v98, v103, s44, v69\n\t"
    "v_xor_b32 v133, 23, v132\n\t"
    "v_lshlrev_b32 v115, v132, v136\n\t"
    "v_and_b32 v132, 7, v132\n\t"
    "v_lshl_or_b32 v115, v136, v133, v115\n\t"
    "v_lshl_add_u32 v132, v132, 1, v128\n\t"
    "ds_or_rtn_b32 v123, v97, v115\n\t"
    "ds_write_b16 v132, v104\n\t"
    "v_cndmask_b32 v130, v130, v129, vcc\n\t"
    "v_ffbl_b32 v131, v130\n\t"
    "v_and_b32 v134, 7, v131\n\t"
    "v_lshlrev_b32 v134, v134, s45\n\t"
    "v_xor_b32 v129, v129, v134\n\t"
    "v_xor_b32 v132, v131, v127\n\t"
    "s_waitcnt lgkmcnt(9)\n\t"
    "v_and_b32 v120, v120, v112\n\t"
    "v_perm_b32 v120, v120, v120, s46\n\t"
    "ds_or_b32 v94, v120\n\t"
    "v_bfi_b32 v130, v107, 0, v129\n\t"
    "v_cmp_eq_u32 vcc, 0, v130\n\t"
    "v_and_or_b32 v99, v107, s44, v69\n\t"
    "v_xor_b32 v133, 23, v132\n\t"
    "v_lshlrev_b32 v116, v132, v136\n\t"
    "v_and_b32 v132, 7, v132\n\t"
    "v_lshl_or_b32 v116, v136, v133, v116\n\t"
    "v_lshl_add_u32 v132, v132, 1, v128\n\t"
    "ds_or_rtn_b32 v124, v98, v116\n\t"
    "ds_write_b16 v132, v103\n\t"
    "v_cndmask_b32 v130, v130, v129, vcc\n\t"
    "v_ffbl_b32 v131, v130\n\t"
    "v_xor_b32 v132, v131, v127\n\t"
    "s_waitcnt lgkmcnt(10)\n\t"
    "v_and_b32 v121, v121, v113\n\t"
    "v_perm_b32 v121, v121, v121, s46\n\t"
    "ds_or_b32 v95, v121\n\t"
    "v_xor_b32 v133, 23, v132\n\t"
    "v_lshlrev_b32 v117, v132, v136\n\t"
    "v_and_b32 v132, 7, v132\n\t"
    "v_lshl_or_b32 v117, v136, v133, v117\n\t"
    "v_lshl_add_u32 v132, v132, 1, v128\n\t"
    "ds_or_rtn_b32 v125, v99, v117\n\t"
    "ds_write_b16 v132, v107\n\t"
    "s_waitcnt lgkmcnt(10)\n\t"
    "v_and_b32 v122, v122, v114\n\t"
    "v_perm_b32 v122, v122, v122, s46\n\t"
    "ds_or_b32 v96, v122\n\t"
    "ds_read_b128 v[72:75], v128\n\t"
    "v_and_or_b32 v100, v84, s44, v69\n\t"
    "ds_read_b32 v100, v100\n\t"
    "v_and_or_b32 v101, v85, s44, v69\n\t"
    "ds_read_b32 v101, v101\n\t"
    "v_and_or_b32 v102, v86, s44, v69\n\t"
    "ds_read_b32 v102, v102\n\t"
    "v_and_or_b32 v103, v87, s44, v69\n\t"
    "ds_read_b32 v103, v103\n\t"
    "v_and_or_b32 v104, v88, s44, v69\n\t"
    "ds_read_b32 v104, v104\n\t"
    "v_and_or_b32 v105, v89, s44, v69\n\t"
    "ds_read_b32 v105, v105\n\t"
    "v_and_or_b32 v106, v90, s44, v69\n\t"
    "ds_read_b32 v106, v106\n\t"
    "v_and_or_b32 v107, v91, s44, v69\n\t"
    "ds_read_b32 v107, v107\n\t"
    "v_lshl_add_u32 v76, v0, 3, s36\n\t"
    "v_lshl_add_u32 v77, v1, 3, s37\n\t"
    "v_lshl_add_u32 v78, v2, 3, s38\n\t"
    "v_lshl_add_u32 v79, v3, 3, s39\n\t"
    "v_lshl_add_u32 v80, v4, 3, s40\n\t"
    "v_lshl_add_u32 v81, v5, 3, s41\n\t"
    "v_lshl_add_u32 v82, v6, 3, s42\n\t"
    "v_lshl_add_u32 v83, v7, 3, s43\n\t"
    "v_mbcnt_lo_u32_b32 v70, -1, 0\n\t"
    "v_mbcnt_hi_u32_b32 v70, -1, v70\n\t"
    "v_add_u32 v70, 8, v70\n\t"
    "s_and_b32 s50, %14, 7\n\t"
    "s_add_u32 s50, s50, 16\n\t"
    "v_writelane_b32 v70, s50, 8\n\t"
    "v_mov_b32 v148, 32\n\t"
    "v_lshlrev_b32 v70, 7, v70\n\t"
    "s_mov_b32 s52, 49283072\n\t"
    "s_mov_b64 exec, 0x1ff\n\t"
    "buffer_load_dword v71, v70, %12, s52 offen sc1\n\t"
    "s_mov_b64 exec, -1\n\t"
    "s_mov_b32 s54, 0x40000000\n\t"
    "s_mov_b32 s55, 0\n\t"
    "s_waitcnt lgkmcnt(11)\n\t"
    "v_and_b32 v123, v123, v115\n\t"
    "v_perm_b32 v123, v123, v123, s46\n\t"
    "ds_or_b32 v97, v123\n\t"
    "v_and_b32 v124, v124, v116\n\t"
    "v_perm_b32 v124, v124, v124, s46\n\t"
    "ds_or_b32 v98, v124\n\t"
    "v_and_b32 v125, v125, v117\n\t"
    "v_perm_b32 v125, v125, v125, s46\n\t"
    "ds_or_b32 v99, v125\n\t"
    "s_waitcnt lgkmcnt(11)\n\t"
    "buffer_store_dwordx4 v[72:75], v67, %12, s49 offen offset:352 sc1\n\t"
    "v_mov_b32 v129, 0xffff0000\n\t"
    "s_waitcnt lgkmcnt(7)\n\t"
    "v_perm_b32 v100, v100, v84, v126\n\t"
    "v_perm_b32 v101, v101, v85, v126\n\t"
    "v_perm_b32 v102, v102, v86, v126\n\t"
    "v_perm_b32 v103, v103, v87, v126\n\t"
    "s_waitcnt lgkmcnt(3)\n\t"
    "v_perm_b32 v104, v104, v88, v126\n\t"
    "v_perm_b32 v105, v105, v89, v126\n\t"
    "v_perm_b32 v106, v106, v90, v126\n\t"
    "v_perm_b32 v107, v107, v91, v126\n\t"
    "s_waitcnt vmcnt(42)\n\t"
    "ds_write_b128 v141, v[156:159] offset:2112\n\t"
    "ds_write_b128 v141, v[160:163] offset:2368\n\t"
    "ds_read_b64_tr_b16 v[156:157], v142 offset:2112\n\t"
    "ds_read_b64_tr_b16 v[158:159], v142 offset:2368\n\t"
    "ds_read_b64_tr_b16 v[160:161], v142 offset:2144\n\t"
    "ds_read_b64_tr_b16 v[162:163], v142 offset:2400\n\t"
    "s_waitcnt vmcnt(40)\n\t"
    "ds_write_b128 v141, v[164:167] offset:0\n\t"
    "ds_write_b128 v141, v[168:171] offset:256\n\t"
    "ds_read_b64_tr_b16 v[164:165], v142 offset:0\n\t"
    "ds_read_b64_tr_b16 v[166:167], v142 offset:256\n\t"
    "ds_read_b64_tr_b16 v[168:169], v142 offset:32\n\t"
    "ds_read_b64_tr_b16 v[170:171], v142 offset:288\n\t"
    "v_max_u32 v108, v100, v101\n\t"
    "v_min_u32 v101, v100, v101\n\t"
    "v_max_u32 v100, v102, v103\n\t"
    "v_min_u32 v103, v102, v103\n\t"
    "v_max_u32 v102, v104, v105\n\t"
    "v_min_u32 v105, v104, v105\n\t"
    "v_max_u32 v104, v106, v107\n\t"
    "v_min_u32 v107, v106, v107\n\t"
    "v_max_u32 v106, v108, v100\n\t"
    "v_min_u32 v100, v108, v100\n\t"
    "v_max_u32 v108, v101, v103\n\t"
    "v_min_u32 v103, v101, v103\n\t"
    "v_max_u32 v101, v102, v104\n\t"
    "v_min_u32 v104, v102, v104\n\t"
    "v_max_u32 v102, v105, v107\n\t"
    "v_min_u32 v107, v105, v107\n\t"
    "v_bfi_b32 v130, v106, 0, v129\n\t"
    "v_cmp_eq_u32 vcc, 0, v130\n\t"
    "v_and_or_b32 v92, v106, s44, v69\n\t"
    "s_nop 0\n\t"
    "v_cndmask_b32 v130, v130, v129, vcc\n\t"
    "v_ffbl_b32 v131, v130\n\t"
    "v_and_b32 v134, 7, v131\n\t"
    "v_lshlrev_b32 v134, v134, s45\n\t"
    "v_xor_b32 v129, v129, v134\n\t"
    "v_xor_b32 v132, v131, v127\n\t"
    "v_bfi_b32 v130, v101, 0, v129\n\t"
    "v_cmp_eq_u32 vcc, 0, v130\n\t"
    "v_and_or_b32 v93, v101, s44, v69\n\t"
    "v_xor_b32 v133, 23, v132\n\t"
    "v_lshlrev_b32 v110, v132, v136\n\t"
    "v_and_b32 v132, 7, v132\n\t"
    "v_lshl_or_b32 v110, v136, v133, v110\n\t"
    "v_lshl_add_u32 v132, v132, 1, v128\n\t"
    "ds_or_b32 v92, v110\n\t"
    "ds_write_b16 v132, v106\n\t"
    "v_cndmask_b32 v130, v130, v129, vcc\n\t"
    "v_ffbl_b32 v131, v130\n\t"
    "v_and_b32 v134, 7, v131\n\t"
    "v_lshlrev_b32 v134, v134, s45\n\t"
    "v_xor_b32 v129, v129, v134\n\t"
    "v_xor_b32 v132, v131, v127\n\t"
    "v_bfi_b32 v130, v108, 0, v129\n\t"
    "v_cmp_eq_u32 vcc, 0, v130\n\t"
    "v_and_or_b32 v94, v108, s44, v69\n\t"
    "v_xor_b32 v133, 23, v132\n\t"
    "v_lshlrev_b32 v111, v132, v136\n\t"
    "v_and_b32 v132, 7, v132\n\t"
    "v_lshl_or_b32 v111, v136, v133, v111\n\t"
    "v_lshl_add_u32 v132, v132, 1, v128\n\t"
    "ds_or_b32 v93, v111\n\t"
    "ds_write_b16 v132, v101\n\t"
    "v_cndmask_b32 v130, v130, v129, vcc\n\t"
    "v_ffbl_b32 v131, v130\n\t"
    "v_and_b32 v134, 7, v131\n\t"
    "v_lshlrev_b32 v134, v134, s45\n\t"
    "v_xor_b32 v129, v129, v134\n\t"
    "v_xor_b32 v132, v131, v127\n\t"
    "s_waitcnt lgkmcnt(10)\n\t"
    "buffer_store_dwordx4 v[156:159], v139, %12, s48 offen sc1\n\t"
    "buffer_store_dwordx4 v[160:163], v140, %12, s48 offen sc1\n\t"
    "s_add_u32 s48, s48, -811008\n\t"
    "s_waitcnt lgkmcnt(4)\n\t"
    "buffer_store_dwordx4 v[164:167], v139, %12, s48 offen sc1\n\t"
    "buffer_store_dwordx4 v[168:171], v140, %12, s48 offen sc1\n\t"
    "s_add_u32 s48, s48, 819200\n\t"
    "s_waitcnt vmcnt(42)\n\t"
    "ds_write_b128 v141, v[172:175] offset:2112\n\t"
    "ds_write_b128 v141, v[176:179] offset:2368\n\t"
    "ds_read_b64_tr_b16 v[172:173], v142 offset:2112\n\t"
    "ds_read_b64_tr_b16 v[174:175], v142 offset:2368\n\t"
    "ds_read_b64_tr_b16 v[176:177], v142 offset:2144\n\t"
    "ds_read_b64_tr_b16 v[178:179], v142 offset:2400\n\t"
    "s_waitcnt vmcnt(40)\n\t"
    "ds_write_b128 v141, v[180:183] offset:0\n\t"
    "ds_write_b128 v141, v[184:187] offset:256\n\t"
    "ds_read_b64_tr_b16 v[180:181], v142 offset:0\n\t"
    "ds_read_b64_tr_b16 v[182:183], v142 offset:256\n\t"
    "ds_read_b64_tr_b16 v[184:185], v142 offset:32\n\t"
    "ds_read_b64_tr_b16 v[186:187], v142 offset:288\n\t"
    "v_bfi_b32 v130, v100, 0, v129\n\t"
    "v_cmp_eq_u32 vcc, 0, v130\n\t"
    "v_and_or_b32 v95, v100, s44, v69\n\t"
    "v_xor_b32 v133, 23, v132\n\t"
    "v_lshlrev_b32 v112, v132, v136\n\t"
    "v_and_b32 v132, 7, v132\n\t"
    "v_lshl_or_b32 v112, v136, v133, v112\n\t"
    "v_lshl_add_u32 v132, v132, 1, v128\n\t"
    "ds_or_b32 v94, v112\n\t"
    "ds_write_b16 v132, v108\n\t"
    "v_cndmask_b32 v130, v130, v129, vcc\n\t"
    "v_ffbl_b32 v131, v130\n\t"
    "v_and_b32 v134, 7, v131\n\t"
    "v_lshlrev_b32 v134, v134, s45\n\t"
    "v_xor_b32 v129, v129, v134\n\t"
    "v_xor_b32 v132, v131, v127\n\t"
    "v_bfi_b32 v130, v102, 0, v129\n\t"
    "v_cmp_eq_u32 vcc, 0, v130\n\t"
    "v_and_or_b32 v96, v102, s44, v69\n\t"
    "v_xor_b32 v133, 23, v132\n\t"
    "v_lshlrev_b32 v113, v132, v136\n\t"
    "v_and_b32 v132, 7, v132\n\t"
    "v_lshl_or_b32 v113, v136, v133, v113\n\t"
    "v_lshl_add_u32 v132, v132, 1, v128\n\t"
    "ds_or_b32 v95, v113\n\t"
    "ds_write_b16 v132, v100\n\t"
    "v_cndmask_b32 v130, v130, v129, vcc\n\t"
    "v_ffbl_b32 v131, v130\n\t"
    "v_and_b32 v134, 7, v131\n\t"
    "v_lshlrev_b32 v134, v134, s45\n\t"
    "v_xor_b32 v129, v129, v134\n\t"
    "v_xor_b32 v132, v131, v127\n\t"
    "s_waitcnt vmcnt(5)\n\t"
    "s_call_b64 s[54:55], L_af1w_%=\n\t"
    "s_mov_b64 s[54:55], 0\n\t"
    "L_af1n15_%=:\n\t"
    "v_bfi_b32 v130, v104, 0, v129\n\t"
    "v_cmp_eq_u32 vcc, 0, v130\n\t"
    "v_and_or_b32 v97, v104, s44, v69\n\t"
    "v_xor_b32 v133, 23, v132\n\t"
    "v_lshlrev_b32 v114, v132, v136\n\t"
    "v_and_b32 v132, 7, v132\n\t"
    "v_lshl_or_b32 v114, v136, v133, v114\n\t"
    "v_lshl_add_u32 v132, v132, 1, v128\n\t"
    "ds_or_b32 v96, v114\n\t"
    "ds_write_b16 v132, v102\n\t"
    "v_cndmask_b32 v130, v130, v129, vcc\n\t"
    "v_ffbl_b32 v131, v130\n\t"
    "v_and_b32 v134, 7, v131\n\t"
    "v_lshlrev_b32 v134, v134, s45\n\t"
    "v_xor_b32 v129, v129, v134\n\t"
    "v_xor_b32 v132, v131, v127\n\t"
    "s_waitcnt lgkmcnt(12)\n\t"
    "buffer_store_dwordx4 v[172:175], v139, %12, s48 offen sc1\n\t"
    "buffer_store_dwordx4 v[176:179], v140, %12, s48 offen sc1\n\t"
    "s_add_u32 s48, s48, -811008\n\t"
    "s_waitcnt lgkmcnt(6)\n\t"
    "buffer_store_dwordx4 v[180:183], v139, %12, s48 offen sc1\n\t"
    "buffer_store_dwordx4 v[184:187], v140, %12, s48 offen sc1\n\t"
    "s_add_u32 s48, s48, 819200\n\t"
    "ds_write_b128 v141, v[188:191] offset:2112\n\t"
    "ds_write_b128 v141, v[192:195] offset:2368\n\t"
    "ds_read_b64_tr_b16 v[188:189], v142 offset:2112\n\t"
    "ds_read_b64_tr_b16 v[190:191], v142 offset:2368\n\t"
    "ds_read_b64_tr_b16 v[192:193], v142 offset:2144\n\t"
    "ds_read_b64_tr_b16 v[194:195], v142 offset:2400\n\t"
    "ds_write_b128 v141, v[196:199] offset:0\n\t"
    "ds_write_b128 v141, v[200:203] offset:256\n\t"
    "ds_read_b64_tr_b16 v[196:197], v142 offset:0\n\t"
    "ds_read_b64_tr_b16 v[198:199], v142 offset:256\n\t"
    "ds_read_b64_tr_b16 v[200:201], v142 offset:32\n\t"
    "ds_read_b64_tr_b16 v[202:203], v142 offset:288\n\t"
    "v_bfi_b32 v130, v103, 0, v129\n\t"
    "v_cmp_eq_u32 vcc, 0, v130\n\t"
    "v_and_or_b32 v98, v103, s44, v69\n\t"
    "v_xor_b32 v133, 23, v132\n\t"
    "v_lshlrev_b32 v115, v132, v136\n\t"
    "v_and_b32 v132, 7, v132\n\t"
    "v_lshl_or_b32 v115, v136, v133, v115\n\t"
    "v_lshl_add_u32 v132, v132, 1, v128\n\t"
    "ds_or_b32 v97, v115\n\t"
    "ds_write_b16 v132, v104\n\t"
    "v_cndmask_b32 v130, v130, v129, vcc\n\t"
    "v_ffbl_b32 v131, v130\n\t"
    "v_and_b32 v134, 7, v131\n\t"
    "v_lshlrev_b32 v134, v134, s45\n\t"
    "v_xor_b32 v129, v129, v134\n\t"
    "v_xor_b32 v132, v131, v127\n\t"
    "v_bfi_b32 v130, v107, 0, v129\n\t"
    "v_cmp_eq_u32 vcc, 0, v130\n\t"
    "v_and_or_b32 v99, v107, s44, v69\n\t"
    "v_xor_b32 v133, 23, v132\n\t"
    "v_lshlrev_b32 v116, v132, v136\n\t"
    "v_and_b32 v132, 7, v132\n\t"
    "v_lshl_or_b32 v116, v136, v133, v116\n\t"
    "v_lshl_add_u32 v132, v132, 1, v128\n\t"
    "ds_or_b32 v98, v116\n\t"
    "ds_write_b16 v132, v103\n\t"
    "v_cndmask_b32 v130, v130, v129, vcc\n\t"
    "v_ffbl_b32 v131, v130\n\t"
    "v_xor_b32 v132, v131, v127\n\t"
    "v_xor_b32 v133, 23, v132\n\t"
    "v_lshlrev_b32 v117, v132, v136\n\t"
    "v_and_b32 v132, 7, v132\n\t"
    "v_lshl_or_b32 v117, v136, v133, v117\n\t"
    "v_lshl_add_u32 v132, v132, 1, v128\n\t"
    "ds_or_b32 v99, v117\n\t"
    "ds_write_b16 v132, v107\n\t"
    "ds_read_b128 v[72:75], v128\n\t"
    "v_and_or_b32 v100, v76, s44, v69\n\t"
    "ds_read_b32 v100, v100\n\t"
    "v_and_or_b32 v101, v77, s44, v69\n\t"
    "ds_read_b32 v101, v101\n\t"
    "v_and_or_b32 v102, v78, s44, v69\n\t"
    "ds_read_b32 v102, v102\n\t"
    "v_and_or_b32 v103, v79, s44, v69\n\t"
    "ds_read_b32 v103, v103\n\t"
    "v_and_or_b32 v104, v80, s44, v69\n\t"
    "ds_read_b32 v104, v104\n\t"
    "v_and_or_b32 v105, v81, s44, v69\n\t"
    "ds_read_b32 v105, v105\n\t"
    "v_and_or_b32 v106, v82, s44, v69\n\t"
    "ds_read_b32 v106, v106\n\t"
    "v_and_or_b32 v107, v83, s44, v69\n\t"
    "ds_read_b32 v107, v107\n\t"
    "v_lshl_add_u32 v84, v8, 3, s36\n\t"
    "v_lshl_add_u32 v85, v9, 3, s37\n\t"
    "v_lshl_add_u32 v86, v10, 3, s38\n\t"
    "v_lshl_add_u32 v87, v11, 3, s39\n\t"
    "v_lshl_add_u32 v88, v12, 3, s40\n\t"
    "v_lshl_add_u32 v89, v13, 3, s41\n\t"
    "v_lshl_add_u32 v90, v14, 3, s42\n\t"
    "v_lshl_add_u32 v91, v15, 3, s43\n\t"
    "s_waitcnt lgkmcnt(15)\n\t"
    "buffer_store_dwordx4 v[188:191], v139, %12, s48 offen sc1\n\t"
    "buffer_store_dwordx4 v[192:195], v140, %12, s48 offen sc1\n\t"
    "s_add_u32 s48, s48, -811008\n\t"
    "buffer_store_dwordx4 v[196:199], v139, %12, s48 offen sc1\n\t"
    "buffer_store_dwordx4 v[200:203], v140, %12, s48 offen sc1\n\t"
    "s_add_u32 s48, s48, 819200\n\t"
    "s_waitcnt lgkmcnt(8)\n\t"
    "buffer_store_dwordx4 v[72:75], v67, %12, s49 offen offset:384 sc1\n\t"
    "v_mov_b32 v129, 0xffff0000\n\t"
    "s_waitcnt lgkmcnt(4)\n\t"
    "v_perm_b32 v100, v100, v76, v126\n\t"
    "v_perm_b32 v101, v101, v77, v126\n\t"
    "v_perm_b32 v102, v102, v78, v126\n\t"
    "v_perm_b32 v103, v103, v79, v126\n\t"
    "s_waitcnt lgkmcnt(0)\n\t"
    "v_perm_b32 v104, v104, v80, v126\n\t"
    "v_perm_b32 v105, v105, v81, v126\n\t"
    "v_perm_b32 v106, v106, v82, v126\n\t"
    "v_perm_b32 v107, v107, v83, v126\n\t"
    "ds_write_b128 v141, v[48:51] offset:2112\n\t"
    "ds_write_b128 v141, v[52:55] offset:2368\n\t"
    "ds_read_b64_tr_b16 v[48:49], v142 offset:2112\n\t"
    "ds_read_b64_tr_b16 v[50:51], v142 offset:2368\n\t"
    "ds_read_b64_tr_b16 v[52:53], v142 offset:2144\n\t"
    "ds_read_b64_tr_b16 v[54:55], v142 offset:2400\n\t"
    "ds_write_b128 v141, v[204:207] offset:0\n\t"
    "ds_write_b128 v141, v[208:211] offset:256\n\t"
    "ds_read_b64_tr_b16 v[204:205], v142 offset:0\n\t"
    "ds_read_b64_tr_b16 v[206:207], v142 offset:256\n\t"
    "ds_read_b64_tr_b16 v[208:209], v142 offset:32\n\t"
    "ds_read_b64_tr_b16 v[210:211], v142 offset:288\n\t"
    "v_max_u32 v108, v100, v101\n\t"
    "v_min_u32 v101, v100, v101\n\t"
    "v_max_u32 v100, v102, v103\n\t"
    "v_min_u32 v103, v102, v103\n\t"
    "v_max_u32 v102, v104, v105\n\t"
    "v_min_u32 v105, v104, v105\n\t"
    "v_max_u32 v104, v106, v107\n\t"
    "v_min_u32 v107, v106, v107\n\t"
    "v_max_u32 v106, v108, v100\n\t"
    "v_min_u32 v100, v108, v100\n\t"
    "v_max_u32 v108, v101, v103\n\t"
    "v_min_u32 v103, v101, v103\n\t"
    "v_max_u32 v101, v102, v104\n\t"
    "v_min_u32 v104, v102, v104\n\t"
    "v_max_u32 v102, v105, v107\n\t"
    "v_min_u32 v107, v105, v107\n\t"
    "v_bfi_b32 v130, v106, 0, v129\n\t"
    "v_cmp_eq_u32 vcc, 0, v130\n\t"
    "v_and_or_b32 v92, v106, s44, v69\n\t"
    "s_nop 0\n\t"
    "v_cndmask_b32 v130, v130, v129, vcc\n\t"
    "v_ffbl_b32 v131, v130\n\t"
    "v_and_b32 v134, 7, v131\n\t"
    "v_lshlrev_b32 v134, v134, s45\n\t"
    "v_xor_b32 v129, v129, v134\n\t"
    "v_xor_b32 v132, v131, v127\n\t"
    "v_bfi_b32 v130, v101, 0, v129\n\t"
    "v_cmp_eq_u32 vcc, 0, v130\n\t"
    "v_and_or_b32 v93, v101, s44, v69\n\t"
    "v_xor_b32 v133, 23, v132\n\t"
    "v_lshlrev_b32 v110, v132, v136\n\t"
    "v_and_b32 v132, 7, v132\n\t"
    "v_lshl_or_b32 v110, v136, v133, v110\n\t"
    "v_lshl_add_u32 v132, v132, 1, v128\n\t"
    "ds_or_b32 v92, v110\n\t"
    "ds_write_b16 v132, v106\n\t"
    "v_cndmask_b32 v130, v130, v129, vcc\n\t"
    "v_ffbl_b32 v131, v130\n\t"
    "v_and_b32 v134, 7, v131\n\t"
    "v_lshlrev_b32 v134, v134, s45\n\t"
    "v_xor_b32 v129, v129, v134\n\t"
    "v_xor_b32 v132, v131, v127\n\t"
    "v_bfi_b32 v130, v108, 0, v129\n\t"
    "v_cmp_eq_u32 vcc, 0, v130\n\t"
    "v_and_or_b32 v94, v108, s44, v69\n\t"
    "v_xor_b32 v133, 23, v132\n\t"
    "v_lshlrev_b32 v111, v132, v136\n\t"
    "v_and_b32 v132, 7, v132\n\t"
    "v_lshl_or_b32 v111, v136, v133, v111\n\t"
    "v_lshl_add_u32 v132, v132, 1, v128\n\t"
    "ds_or_b32 v93, v111\n\t"
    "ds_write_b16 v132, v101\n\t"
    "v_cndmask_b32 v130, v130, v129, vcc\n\t"
    "v_ffbl_b32 v131, v130\n\t"
    "v_and_b32 v134, 7, v131\n\t"
    "v_lshlrev_b32 v134, v134, s45\n\t"
    "v_xor_b32 v129, v129, v134\n\t"
    "v_xor_b32 v132, v131, v127\n\t"
    "s_waitcnt lgkmcnt(10)\n\t"
    "buffer_store_dwordx4 v[48:51], v139, %12, s48 offen sc1\n\t"
    "buffer_store_dwordx4 v[52:55], v140, %12, s48 offen sc1\n\t"
    "s_add_u32 s48, s48, -811008\n\t"
    "s_waitcnt lgkmcnt(4)\n\t"
    "buffer_store_dwordx4 v[204:207], v139, %12, s48 offen sc1\n\t"
    "buffer_store_dwordx4 v[208:211], v140, %12, s48 offen sc1\n\t"
    "s_add_u32 s48, s48, 819200\n\t"
    "ds_write_b128 v141, v[212:215] offset:2112\n\t"
    "ds_write_b128 v141, v[216:219] offset:2368\n\t"
    "ds_read_b64_tr_b16 v[212:213], v142 offset:2112\n\t"
    "ds_read_b64_tr_b16 v[214:215], v142 offset:2368\n\t"
    "ds_read_b64_tr_b16 v[216:217], v142 offset:2144\n\t"
    "ds_read_b64_tr_b16 v[218:219], v142 offset:2400\n\t"
    "ds_write_b128 v141, v[220:223] offset:0\n\t"
    "ds_write_b128 v141, v[224:227] offset:256\n\t"
    "ds_read_b64_tr_b16 v[220:221], v142 offset:0\n\t"
    "ds_read_b64_tr_b16 v[222:223], v142 offset:256\n\t"
    "ds_read_b64_tr_b16 v[224:225], v142 offset:32\n\t"
    "ds_read_b64_tr_b16 v[226:227], v142 offset:288\n\t"
    "v_bfi_b32 v130, v100, 0, v129\n\t"
    "v_cmp_eq_u32 vcc, 0, v130\n\t"
    "v_and_or_b32 v95, v100, s44, v69\n\t"
    "v_xor_b32 v133, 23, v132\n\t"
    "v_lshlrev_b32 v112, v132, v136\n\t"
    "v_and_b32 v132, 7, v132\n\t"
    "v_lshl_or_b32 v112, v136, v133, v112\n\t"
    "v_lshl_add_u32 v132, v132, 1, v128\n\t"
    "ds_or_b32 v94, v112\n\t"
    "ds_write_b16 v132, v108\n\t"
    "v_cndmask_b32 v130, v130, v129, vcc\n\t"
    "v_ffbl_b32 v131, v130\n\t"
    "v_and_b32 v134, 7, v131\n\t"
    "v_lshlrev_b32 v134, v134, s45\n\t"
    "v_xor_b32 v129, v129, v134\n\t"
    "v_xor_b32 v132, v131, v127\n\t"
    "v_bfi_b32 v130, v102, 0, v129\n\t"
    "v_cmp_eq_u32 vcc, 0, v130\n\t"
    "v_and_or_b32 v96, v102, s44, v69\n\t"
    "v_xor_b32 v133, 23, v132\n\t"
    "v_lshlrev_b32 v113, v132, v136\n\t"
    "v_and_b32 v132, 7, v132\n\t"
    "v_lshl_or_b32 v113, v136, v133, v113\n\t"
    "v_lshl_add_u32 v132, v132, 1, v128\n\t"
    "ds_or_b32 v95, v113\n\t"
    "ds_write_b16 v132, v100\n\t"
    "v_cndmask_b32 v130, v130, v129, vcc\n\t"
    "v_ffbl_b32 v131, v130\n\t"
    "v_and_b32 v134, 7, v131\n\t"
    "v_lshlrev_b32 v134, v134, s45\n\t"
    "v_xor_b32 v129, v129, v134\n\t"
    "v_xor_b32 v132, v131, v127\n\t"
    "v_bfi_b32 v130, v104, 0, v129\n\t"
    "v_cmp_eq_u32 vcc, 0, v130\n\t"
    "v_and_or_b32 v97, v104, s44, v69\n\t"
    "v_xor_b32 v133, 23, v132\n\t"
    "v_lshlrev_b32 v114, v132, v136\n\t"
    "v_and_b32 v132, 7, v132\n\t"
    "v_lshl_or_b32 v114, v136, v133, v114\n\t"
    "v_lshl_add_u32 v132, v132, 1, v128\n\t"
    "ds_or_b32 v96, v114\n\t"
    "ds_write_b16 v132, v102\n\t"
    "v_cndmask_b32 v130, v130, v129, vcc\n\t"
    "v_ffbl_b32 v131, v130\n\t"
    "v_and_b32 v134, 7, v131\n\t"
    "v_lshlrev_b32 v134, v134, s45\n\t"
    "v_xor_b32 v129, v129, v134\n\t"
    "v_xor_b32 v132, v131, v127\n\t"
    "s_waitcnt lgkmcnt(12)\n\t"
    "buffer_store_dwordx4 v[212:215], v139, %12, s48 offen sc1\n\t"
    "buffer_store_dwordx4 v[216:219], v140, %12, s48 offen sc1\n\t"
    "s_add_u32 s48, s48, -811008\n\t"
    "s_waitcnt lgkmcnt(6)\n\t"
    "s_bitcmp1_b32 %14, 3\n\t"
    "s_cselect_b32 s48, 0x40000000, s48\n\t"
    "buffer_store_dwordx4 v[220:223], v139, %12, s48 offen sc1\n\t"
    "buffer_store_dwordx4 v[224:227], v140, %12, s48 offen sc1\n\t"
    "s_add_u32 s48, s48, 819200\n\t"
    "ds_write_b128 v141, v[228:231] offset:2112\n\t"
    "ds_write_b128 v141, v[232:235] offset:2368\n\t"
    "ds_read_b64_tr_b16 v[228:229], v142 offset:2112\n\t"
    "ds_read_b64_tr_b16 v[230:231], v142 offset:2368\n\t"
    "ds_read_b64_tr_b16 v[232:233], v142 offset:2144\n\t"
    "ds_read_b64_tr_b16 v[234:235], v142 offset:2400\n\t"
    "v_bfi_b32 v130, v103, 0, v129\n\t"
    "v_cmp_eq_u32 vcc, 0, v130\n\t"
    "v_and_or_b32 v98, v103, s44, v69\n\t"
    "v_xor_b32 v133, 23, v132\n\t"
    "v_lshlrev_b32 v115, v132, v136\n\t"
    "v_and_b32 v132, 7, v132\n\t"
    "v_lshl_or_b32 v115, v136, v133, v115\n\t"
    "v_lshl_add_u32 v132, v132, 1, v128\n\t"
    "ds_or_b32 v97, v115\n\t"
    "ds_write_b16 v132, v104\n\t"
    "v_cndmask_b32 v130, v130, v129, vcc\n\t"
    "v_ffbl_b32 v131, v130\n\t"
    "v_and_b32 v134, 7, v131\n\t"
    "v_lshlrev_b32 v134, v134, s45\n\t"
    "v_xor_b32 v129, v129, v134\n\t"
    "v_xor_b32 v132, v131, v127\n\t"
    "v_bfi_b32 v130, v107, 0, v129\n\t"
    "v_cmp_eq_u32 vcc, 0, v130\n\t"
    "v_and_or_b32 v99, v107, s44, v69\n\t"
    "v_xor_b32 v133, 23, v132\n\t"
    "v_lshlrev_b32 v116, v132, v136\n\t"
    "v_and_b32 v132, 7, v132\n\t"
    "v_lshl_or_b32 v116, v136, v133, v116\n\t"
    "v_lshl_add_u32 v132, v132, 1, v128\n\t"
    "ds_or_b32 v98, v116\n\t"
    "ds_write_b16 v132, v103\n\t"
    "v_cndmask_b32 v130, v130, v129, vcc\n\t"
    "v_ffbl_b32 v131, v130\n\t"
    "v_xor_b32 v132, v131, v127\n\t"
    "v_xor_b32 v133, 23, v132\n\t"
    "v_lshlrev_b32 v117, v132, v136\n\t"
    "v_and_b32 v132, 7, v132\n\t"
    "v_lshl_or_b32 v117, v136, v133, v117\n\t"
    "v_lshl_add_u32 v132, v132, 1, v128\n\t"
    "ds_or_b32 v99, v117\n\t"
    "ds_write_b16 v132, v107\n\t"
    "ds_read_b128 v[72:75], v128\n\t"
    "v_and_or_b32 v100, v84, s44, v69\n\t"
    "ds_read_b32 v100, v100\n\t"
    "v_and_or_b32 v101, v85, s44, v69\n\t"
    "ds_read_b32 v101, v101\n\t"
    "v_and_or_b32 v102, v86, s44, v69\n\t"
    "ds_read_b32 v102, v102\n\t"
    "v_and_or_b32 v103, v87, s44, v69\n\t"
    "ds_read_b32 v103, v103\n\t"
    "v_and_or_b32 v104, v88, s44, v69\n\t"
    "ds_read_b32 v104, v104\n\t"
    "v_and_or_b32 v105, v89, s44, v69\n\t"
    "ds_read_b32 v105, v105\n\t"
    "v_and_or_b32 v106, v90, s44, v69\n\t"
    "ds_read_b32 v106, v106\n\t"
    "v_and_or_b32 v107, v91, s44, v69\n\t"
    "ds_read_b32 v107, v107\n\t"
    "v_lshl_add_u32 v76, v16, 3, s36\n\t"
    "v_lshl_add_u32 v77, v17, 3, s37\n\t"
    "v_lshl_add_u32 v78, v18, 3, s38\n\t"
    "v_lshl_add_u32 v79, v19, 3, s39\n\t"
    "v_lshl_add_u32 v80, v20, 3, s40\n\t"
    "v_lshl_add_u32 v81, v21, 3, s41\n\t"
    "v_lshl_add_u32 v82, v22, 3, s42\n\t"
    "v_lshl_add_u32 v83, v23, 3, s43\n\t"
    "s_waitcnt lgkmcnt(15)\n\t"
    "buffer_store_dwordx4 v[228:231], v139, %12, s48 offen sc1\n\t"
    "buffer_store_dwordx4 v[232:235], v140, %12, s48 offen sc1\n\t"
    "s_add_u32 s48, s48, -811008\n\t"
    "s_waitcnt lgkmcnt(8)\n\t"
    "buffer_store_dwordx4 v[72:75], v67, %12, s49 offen offset:416 sc1\n\t"
    "v_mov_b32 v129, 0xffff0000\n\t"
    "s_waitcnt lgkmcnt(4)\n\t"
    "v_perm_b32 v100, v100, v84, v126\n\t"
    "v_perm_b32 v101, v101, v85, v126\n\t"
    "v_perm_b32 v102, v102, v86, v126\n\t"
    "v_perm_b32 v103, v103, v87, v126\n\t"
    "s_waitcnt lgkmcnt(0)\n\t"
    "v_perm_b32 v104, v104, v88, v126\n\t"
    "v_perm_b32 v105, v105, v89, v126\n\t"
    "v_perm_b32 v106, v106, v90, v126\n\t"
    "v_perm_b32 v107, v107, v91, v126\n\t"
    "v_max_u32 v108, v100, v101\n\t"
    "v_min_u32 v101, v100, v101\n\t"
    "v_max_u32 v100, v102, v103\n\t"
    "v_min_u32 v103, v102, v103\n\t"
    "v_max_u32 v102, v104, v105\n\t"
    "v_min_u32 v105, v104, v105\n\t"
    "v_max_u32 v104, v106, v107\n\t"
    "v_min_u32 v107, v106, v107\n\t"
    "v_max_u32 v106, v108, v100\n\t"
    "v_min_u32 v100, v108, v100\n\t"
    "v_max_u32 v108, v101, v103\n\t"
    "v_min_u32 v103, v101, v103\n\t"
    "v_max_u32 v101, v102, v104\n\t"
    "v_min_u32 v104, v102, v104\n\t"
    "v_max_u32 v102, v105, v107\n\t"
    "v_min_u32 v107, v105, v107\n\t"
    "v_bfi_b32 v130, v106, 0, v129\n\t"
    "v_cmp_eq_u32 vcc, 0, v130\n\t"
    "v_and_or_b32 v92, v106, s44, v69\n\t"
    "s_nop 0\n\t"
    "v_cndmask_b32 v130, v130, v129, vcc\n\t"
    "v_ffbl_b32 v131, v130\n\t"
    "v_and_b32 v134, 7, v131\n\t"
    "v_lshlrev_b32 v134, v134, s45\n\t"
    "v_xor_b32 v129, v129, v134\n\t"
    "v_xor_b32 v132, v131, v127\n\t"
    "v_bfi_b32 v130, v101, 0, v129\n\t"
    "v_cmp_eq_u32 vcc, 0, v130\n\t"
    "v_and_or_b32 v93, v101, s44, v69\n\t"
    "v_xor_b32 v133, 23, v132\n\t"
    "v_lshlrev_b32 v110, v132, v136\n\t"
    "v_and_b32 v132, 7, v132\n\t"
    "v_lshl_or_b32 v110, v136, v133, v110\n\t"
    "v_lshl_add_u32 v132, v132, 1, v128\n\t"
    "ds_or_b32 v92, v110\n\t"
    "ds_write_b16 v132, v106\n\t"
    "v_cndmask_b32 v130, v130, v129, vcc\n\t"
    "v_ffbl_b32 v131, v130\n\t"
    "v_and_b32 v134, 7, v131\n\t"
    "v_lshlrev_b32 v134, v134, s45\n\t"
    "v_xor_b32 v129, v129, v134\n\t"
    "v_xor_b32 v132, v131, v127\n\t"
    "v_bfi_b32 v130, v108, 0, v129\n\t"
    "v_cmp_eq_u32 vcc, 0, v130\n\t"
    "v_and_or_b32 v94, v108, s44, v69\n\t"
    "v_xor_b32 v133, 23, v132\n\t"
    "v_lshlrev_b32 v111, v132, v136\n\t"
    "v_and_b32 v132, 7, v132\n\t"
    "v_lshl_or_b32 v111, v136, v133, v111\n\t"
    "v_lshl_add_u32 v132, v132, 1, v128\n\t"
    "ds_or_b32 v93, v111\n\t"
    "ds_write_b16 v132, v101\n\t"
    "v_cndmask_b32 v130, v130, v129, vcc\n\t"
    "v_ffbl_b32 v131, v130\n\t"
    "v_and_b32 v134, 7, v131\n\t"
    "v_lshlrev_b32 v134, v134, s45\n\t"
    "v_xor_b32 v129, v129, v134\n\t"
    "v_xor_b32 v132, v131, v127\n\t"
    "v_bfi_b32 v130, v100, 0, v129\n\t"
    "v_cmp_eq_u32 vcc, 0, v130\n\t"
    "v_and_or_b32 v95, v100, s44, v69\n\t"
    "v_xor_b32 v133, 23, v132\n\t"
    "v_lshlrev_b32 v112, v132, v136\n\t"
    "v_and_b32 v132, 7, v132\n\t"
    "v_lshl_or_b32 v112, v136, v133, v112\n\t"
    "v_lshl_add_u32 v132, v132, 1, v128\n\t"
    "ds_or_b32 v94, v112\n\t"
    "ds_write_b16 v132, v108\n\t"
    "v_cndmask_b32 v130, v130, v129, vcc\n\t"
    "v_ffbl_b32 v131, v130\n\t"
    "v_and_b32 v134, 7, v131\n\t"
    "v_lshlrev_b32 v134, v134, s45\n\t"
    "v_xor_b32 v129, v129, v134\n\t"
    "v_xor_b32 v132, v131, v127\n\t"
    "v_bfi_b32 v130, v102, 0, v129\n\t"
    "v_cmp_eq_u32 vcc, 0, v130\n\t"
    "v_and_or_b32 v96, v102, s44, v69\n\t"
    "v_xor_b32 v133, 23, v132\n\t"
    "v_lshlrev_b32 v113, v132, v136\n\t"
    "v_and_b32 v132, 7, v132\n\t"
    "v_lshl_or_b32 v113, v136, v133, v113\n\t"
    "v_lshl_add_u32 v132, v132, 1, v128\n\t"
    "ds_or_b32 v95, v113\n\t"
    "ds_write_b16 v132, v100\n\t"
    "v_cndmask_b32 v130, v130, v129, vcc\n\t"
    "v_ffbl_b32 v131, v130\n\t"
    "v_and_b32 v134, 7, v131\n\t"
    "v_lshlrev_b32 v134, v134, s45\n\t"
    "v_xor_b32 v129, v129, v134\n\t"
    "v_xor_b32 v132, v131, v127\n\t"
    "v_bfi_b32 v130, v104, 0, v129\n\t"
    "v_cmp_eq_u32 vcc, 0, v130\n\t"
    "v_and_or_b32 v97, v104, s44, v69\n\t"
    "v_xor_b32 v133, 23, v132\n\t"
    "v_lshlrev_b32 v114, v132, v136\n\t"
    "v_and_b32 v132, 7, v132\n\t"
    "v_lshl_or_b32 v114, v136, v133, v114\n\t"
    "v_lshl_add_u32 v132, v132, 1, v128\n\t"
    "ds_or_b32 v96, v114\n\t"
    "ds_write_b16 v132, v102\n\t"
    "v_cndmask_b32 v130, v130, v129, vcc\n\t"
    "v_ffbl_b32 v131, v130\n\t"
    "v_and_b32 v134, 7, v131\n\t"
    "v_lshlrev_b32 v134, v134, s45\n\t"
    "v_xor_b32 v129, v129, v134\n\t"
    "v_xor_b32 v132, v131, v127\n\t"
    "v_bfi_b32 v130, v103, 0, v129\n\t"
    "v_cmp_eq_u32 vcc, 0, v130\n\t"
    "v_and_or_b32 v98, v103, s44, v69\n\t"
    "v_xor_b32 v133, 23, v132\n\t"
    "v_lshlrev_b32 v115, v132, v136\n\t"
    "v_and_b32 v132, 7, v132\n\t"
    "v_lshl_or_b32 v115, v136, v133, v115\n\t"
    "v_lshl_add_u32 v132, v132, 1, v128\n\t"
    "ds_or_b32 v97, v115\n\t"
    "ds_write_b16 v132, v104\n\t"
    "v_cndmask_b32 v130, v130, v129, vcc\n\t"
    "v_ffbl_b32 v131, v130\n\t"
    "v_and_b32 v134, 7, v131\n\t"
    "v_lshlrev_b32 v134, v134, s45\n\t"
    "v_xor_b32 v129, v129, v134\n\t"
    "v_xor_b32 v132, v131, v127\n\t"
    "v_bfi_b32 v130, v107, 0, v129\n\t"
    "v_cmp_eq_u32 vcc, 0, v130\n\t"
    "v_and_or_b32 v99, v107, s44, v69\n\t"
    "v_xor_b32 v133, 23, v132\n\t"
    "v_lshlrev_b32 v116, v132, v136\n\t"
    "v_and_b32 v132, 7, v132\n\t"
    "v_lshl_or_b32 v116, v136, v133, v116\n\t"
    "v_lshl_add_u32 v132, v132, 1, v128\n\t"
    "ds_or_b32 v98, v116\n\t"
    "ds_write_b16 v132, v103\n\t"
    "v_cndmask_b32 v130, v130, v129, vcc\n\t"
    "v_ffbl_b32 v131, v130\n\t"
    "v_xor_b32 v132, v131, v127\n\t"
    "v_xor_b32 v133, 23, v132\n\t"
    "v_lshlrev_b32 v117, v132, v136\n\t"
    "v_and_b32 v132, 7, v132\n\t"
    "v_lshl_or_b32 v117, v136, v133, v117\n\t"
    "v_lshl_add_u32 v132, v132, 1, v128\n\t"
    "ds_or_b32 v99, v117\n\t"
    "ds_write_b16 v132, v107\n\t"
    "ds_read_b128 v[72:75], v128\n\t"
    "v_and_or_b32 v100, v76, s44, v69\n\t"
    "ds_read_b32 v100, v100\n\t"
    "v_and_or_b32 v101, v77, s44, v69\n\t"
    "ds_read_b32 v101, v101\n\t"
    "v_and_or_b32 v102, v78, s44, v69\n\t"
    "ds_read_b32 v102, v102\n\t"
    "v_and_or_b32 v103, v79, s44, v69\n\t"
    "ds_read_b32 v103, v103\n\t"
    "v_and_or_b32 v104, v80, s44, v69\n\t"
    "ds_read_b32 v104, v104\n\t"
    "v_and_or_b32 v105, v81, s44, v69\n\t"
    "ds_read_b32 v105, v105\n\t"
    "v_and_or_b32 v106, v82, s44, v69\n\t"
    "ds_read_b32 v106, v106\n\t"
    "v_and_or_b32 v107, v83, s44, v69\n\t"
    "ds_read_b32 v107, v107\n\t"
    "s_waitcnt lgkmcnt(8)\n\t"
    "buffer_store_dwordx4 v[72:75], v67, %12, s49 offen offset:448 sc1\n\t"
    "v_mov_b32 v129, 0xffff0000\n\t"
    "s_waitcnt lgkmcnt(4)\n\t"
    "v_perm_b32 v100, v100, v76, v126\n\t"
    "v_perm_b32 v101, v101, v77, v126\n\t"
    "v_perm_b32 v102, v102, v78, v126\n\t"
    "v_perm_b32 v103, v103, v79, v126\n\t"
    "s_waitcnt lgkmcnt(0)\n\t"
    "v_perm_b32 v104, v104, v80, v126\n\t"
    "v_perm_b32 v105, v105, v81, v126\n\t"
    "v_perm_b32 v106, v106, v82, v126\n\t"
    "v_perm_b32 v107, v107, v83, v126\n\t"
    "v_max_u32 v108, v100, v101\n\t"
    "v_min_u32 v101, v100, v101\n\t"
    "v_max_u32 v100, v102, v103\n\t"
    "v_min_u32 v103, v102, v103\n\t"
    "v_max_u32 v102, v104, v105\n\t"
    "v_min_u32 v105, v104, v105\n\t"
    "v_max_u32 v104, v106, v107\n\t"
    "v_min_u32 v107, v106, v107\n\t"
    "v_max_u32 v106, v108, v100\n\t"
    "v_min_u32 v100, v108, v100\n\t"
    "v_max_u32 v108, v101, v103\n\t"
    "v_min_u32 v103, v101, v103\n\t"
    "v_max_u32 v101, v102, v104\n\t"
    "v_min_u32 v104, v102, v104\n\t"
    "v_max_u32 v102, v105, v107\n\t"
    "v_min_u32 v107, v105, v107\n\t"
    "v_bfi_b32 v130, v106, 0, v129\n\t"
    "v_cmp_eq_u32 vcc, 0, v130\n\t"
    "v_and_or_b32 v92, v106, s44, v69\n\t"
    "s_nop 0\n\t"
    "v_cndmask_b32 v130, v130, v129, vcc\n\t"
    "v_ffbl_b32 v131, v130\n\t"
    "v_and_b32 v134, 7, v131\n\t"
    "v_lshlrev_b32 v134, v134, s45\n\t"
    "v_xor_b32 v129, v129, v134\n\t"
    "v_xor_b32 v132, v131, v127\n\t"
    "v_bfi_b32 v130, v101, 0, v129\n\t"
    "v_cmp_eq_u32 vcc, 0, v130\n\t"
    "v_and_b32 v132, 7, v132\n\t"
    "v_lshl_add_u32 v132, v132, 1, v128\n\t"
    "ds_write_b16 v132, v106\n\t"
    "v_cndmask_b32 v130, v130, v129, vcc\n\t"
    "v_ffbl_b32 v131, v130\n\t"
    "v_and_b32 v134, 7, v131\n\t"
    "v_lshlrev_b32 v134, v134, s45\n\t"
    "v_xor_b32 v129, v129, v134\n\t"
    "v_xor_b32 v132, v131, v127\n\t"
    "v_bfi_b32 v130, v108, 0, v129\n\t"
    "v_cmp_eq_u32 vcc, 0, v130\n\t"
    "v_and_b32 v132, 7, v132\n\t"
    "v_lshl_add_u32 v132, v132, 1, v128\n\t"
    "ds_write_b16 v132, v101\n\t"
    "v_cndmask_b32 v130, v130, v129, vcc\n\t"
    "v_ffbl_b32 v131, v130\n\t"
    "v_and_b32 v134, 7, v131\n\t"
    "v_lshlrev_b32 v134, v134, s45\n\t"
    "v_xor_b32 v129, v129, v134\n\t"
    "v_xor_b32 v132, v131, v127\n\t"
    "v_bfi_b32 v130, v100, 0, v129\n\t"
    "v_cmp_eq_u32 vcc, 0, v130\n\t"
    "v_and_b32 v132, 7, v132\n\t"
    "v_lshl_add_u32 v132, v132, 1, v128\n\t"
    "ds_write_b16 v132, v108\n\t"
    "v_cndmask_b32 v130, v130, v129, vcc\n\t"
    "v_ffbl_b32 v131, v130\n\t"
    "v_and_b32 v134, 7, v131\n\t"
    "v_lshlrev_b32 v134, v134, s45\n\t"
    "v_xor_b32 v129, v129, v134\n\t"
    "v_xor_b32 v132, v131, v127\n\t"
    "v_bfi_b32 v130, v102, 0, v129\n\t"
    "v_cmp_eq_u32 vcc, 0, v130\n\t"
    "v_and_b32 v132, 7, v132\n\t"
    "v_lshl_add_u32 v132, v132, 1, v128\n\t"
    "ds_write_b16 v132, v100\n\t"
    "v_cndmask_b32 v130, v130, v129, vcc\n\t"
    "v_ffbl_b32 v131, v130\n\t"
    "v_and_b32 v134, 7, v131\n\t"
    "v_lshlrev_b32 v134, v134, s45\n\t"
    "v_xor_b32 v129, v129, v134\n\t"
    "v_xor_b32 v132, v131, v127\n\t"
    "v_bfi_b32 v130, v104, 0, v129\n\t"
    "v_cmp_eq_u32 vcc, 0, v130\n\t"
    "v_and_b32 v132, 7, v132\n\t"
    "v_lshl_add_u32 v132, v132, 1, v128\n\t"
    "ds_write_b16 v132, v102\n\t"
    "v_cndmask_b32 v130, v130, v129, vcc\n\t"
    "v_ffbl_b32 v131, v130\n\t"
    "v_and_b32 v134, 7, v131\n\t"
    "v_lshlrev_b32 v134, v134, s45\n\t"
    "v_xor_b32 v129, v129, v134\n\t"
    "v_xor_b32 v132, v131, v127\n\t"
    "v_bfi_b32 v130, v103, 0, v129\n\t"
    "v_cmp_eq_u32 vcc, 0, v130\n\t"
    "v_and_b32 v132, 7, v132\n\t"
    "v_lshl_add_u32 v132, v132, 1, v128\n\t"
    "ds_write_b16 v132, v104\n\t"
    "v_cndmask_b32 v130, v130, v129, vcc\n\t"
    "v_ffbl_b32 v131, v130\n\t"
    "v_and_b32 v134, 7, v131\n\t"
    "v_lshlrev_b32 v134, v134, s45\n\t"
    "v_xor_b32 v129, v129, v134\n\t"
    "v_xor_b32 v132, v131, v127\n\t"
    "v_bfi_b32 v130, v107, 0, v129\n\t"
    "v_cmp_eq_u32 vcc, 0, v130\n\t"
    "v_and_b32 v132, 7, v132\n\t"
    "v_lshl_add_u32 v132, v132, 1, v128\n\t"
    "ds_write_b16 v132, v103\n\t"
    "v_cndmask_b32 v130, v130, v129, vcc\n\t"
    "v_ffbl_b32 v131, v130\n\t"
    "v_xor_b32 v132, v131, v127\n\t"
    "v_and_b32 v132, 7, v132\n\t"
    "v_lshl_add_u32 v132, v132, 1, v128\n\t"
    "ds_write_b16 v132, v107\n\t"
    "ds_read_b128 v[72:75], v128\n\t"
    "s_waitcnt lgkmcnt(0)\n\t"
    "buffer_store_dwordx4 v[72:75], v67, %12, s49 offen offset:480 sc1\n\t"
    "s_setprio 0\n\t"
    "v_mov_b32 v134, 1036\n\t"
    "L_asvsp12d_%=:\n\t"
    "ds_read_b32 v130, v134\n\t"
    "s_waitcnt lgkmcnt(0)\n\t"
    "v_readfirstlane_b32 s50, v130\n\t"
    "s_nop 0\n\t"
    "s_cmp_lt_u32 s50, 4\n\t"
    "s_cbranch_scc1 L_asvsp12d_%=\n\t"
    "s_call_b64 s[98:99], L_setup_%=\n\t"
    "v_lshrrev_b32 v105, 4, v127\n\t"
    "v_and_b32 v105, 48, v105\n\t"
    "s_movk_i32 s65, 130\n\t"
    "v_mad_u32_u24 v104, v155, s65, v105\n\t"
    "v_add_u32 v105, 14170, v104\n\t"
    "v_and_b32 v104, -16, v104\n\t"
    "v_and_b32 v105, -16, v105\n\t"
    "ds_read_b128 v[164:167], v104 offset:0\n\t"
    "ds_read_b128 v[168:171], v104 offset:64\n\t"
    "ds_read_b128 v[172:175], v104 offset:2080\n\t"
    "ds_read_b128 v[176:179], v104 offset:2144\n\t"
    "ds_read_b128 v[180:183], v104 offset:4160\n\t"
    "ds_read_b128 v[184:187], v104 offset:4224\n\t"
    "ds_read_b128 v[188:191], v104 offset:6240\n\t"
    "ds_read_b128 v[192:195], v104 offset:6304\n\t"
    "ds_read_b128 v[196:199], v104 offset:8320\n\t"
    "ds_read_b128 v[200:203], v104 offset:8384\n\t"
    "ds_read_b128 v[204:207], v104 offset:10400\n\t"
    "ds_read_b128 v[208:211], v104 offset:10464\n\t"
    "s_cmp_lt_u32 s62, 2\n\t"
    "s_cbranch_scc1 L_mel0_%=\n\t"
    "buffer_load_dwordx4 v[220:223], v127, %12, s61 offen offset:2048 sc1\n\t"
    "buffer_load_dwordx4 v[224:227], v127, %12, s61 offen offset:3072 sc1\n\t"
    "s_add_u32 s61, s61, 0x1000\n\t"
    "buffer_load_dwordx4 v[228:231], v127, %12, s61 offen offset:2048 sc1\n\t"
    "buffer_load_dwordx4 v[232:235], v127, %12, s61 offen offset:3072 sc1\n\t"
    "s_add_u32 s61, s61, 0x1000\n\t"
    "buffer_load_dwordx4 v[236:239], v127, %12, s61 offen offset:2048 sc1\n\t"
    "buffer_load_dwordx4 v[240:243], v127, %12, s61 offen offset:3072 sc1\n\t"
    "s_add_u32 s61, s61, 0x1000\n\t"
    "buffer_load_dwordx4 v[244:247], v127, %12, s61 offen offset:2048 sc1\n\t"
    "buffer_load_dwordx4 v[248:251], v127, %12, s61 offen offset:3072 sc1\n\t"
    "s_add_u32 s61, s61, 0x1000\n\t"
    "buffer_load_dwordx4 v[252:255], v127, %12, s61 offen offset:2048 sc1\n\t"
    "s_mov_b32 s62, 1\n\t"
    "L_mel0_%=:\n\t"
    "buffer_load_dwordx4 v[0:3], v127, %12, s63 offen offset:0 sc1\n\t"
    "buffer_load_dwordx4 v[4:7], v127, %12, s63 offen offset:1024 sc1\n\t"
    "buffer_load_dwordx4 v[8:11], v127, %12, s63 offen offset:2048 sc1\n\t"
    "buffer_load_dwordx4 v[12:15], v127, %12, s63 offen offset:3072 sc1\n\t"
    "s_add_u32 s63, s63, 0x1000\n\t"
    "s_mov_b32 s62, 0\n\t"
    "s_and_b32 s65, %14, 8\n\t"
    "s_lshl_b32 s65, s65, 10\n\t"
    "s_add_u32 s65, s65, s63\n\t"
    "s_mov_b32 m0, 136192\n\t"
    "s_add_u32 s65, s65, 0\n\t"
    "buffer_load_dwordx4 v127, %12, s65 offen offset:0 sc1 lds\n\t"
    "buffer_load_dwordx4 v127, %12, s65 offen offset:1024 sc1 lds\n\t"
    "buffer_load_dwordx4 v127, %12, s65 offen offset:2048 sc1 lds\n\t"
    "buffer_load_dwordx4 v127, %12, s65 offen offset:3072 sc1 lds\n\t"
    "s_mov_b32 m0, 140288\n\t"
    "s_add_u32 s65, s65, 4096\n\t"
    "buffer_load_dwordx4 v127, %12, s65 offen offset:0 sc1 lds\n\t"
    "buffer_load_dwordx4 v127, %12, s65 offen offset:1024 sc1 lds\n\t"
    "buffer_load_dwordx4 v127, %12, s65 offen offset:2048 sc1 lds\n\t"
    "buffer_load_dwordx4 v127, %12, s65 offen offset:3072 sc1 lds\n\t"
    "s_waitcnt vmcnt(21)\n\t"
    "s_and_b32 s50, %14, 7\n\t"
    "s_lshr_b32 s51, %14, 4\n\t"
    "s_add_u32 s51, s51, 8\n\t"
    "s_lshl_b32 s50, s50, 7\n\t"
    "s_lshl_b32 s51, s51, 7\n\t"
    "v_mov_b32 v132, s51\n\t"
    "v_writelane_b32 v132, s50, 0\n\t"
    "s_mov_b32 s50, 49286144\n\t"
    "s_mov_b64 exec, 3\n\t"
    "buffer_atomic_add v136, v132, %12, s50 offen sc1\n\t"
    "s_mov_b64 exec, -1\n\t"
    "s_branch L_af1x_%=\n\t"
    "L_af1w_%=:\n\t"
    "s_mov_b32 s51, 1048576\n\t"
    "s_mov_b64 exec, 0x1ff\n\t"
    "L_af1_%=:\n\t"
    "v_cmp_gt_u32 vcc, v148, v71\n\t"
    "s_cbranch_vccz L_af1ok_%=\n\t"
    "s_sleep 8\n\t"
    "buffer_load_dword v71, v70, %12, s52 offen sc1\n\t"
    "s_sub_u32 s51, s51, 1\n\t"
    "s_waitcnt vmcnt(0)\n\t"
    "s_cmp_lg_u32 s51, 0\n\t"
    "s_cbranch_scc1 L_af1_%=\n\t"
    "L_af1ok_%=:\n\t"
    "s_mov_b64 exec, -1\n\t"
    "v_mov_b32 v134, 3112\n\t"
    "ds_write_b32 v134, v136\n\t"
    "v_mbcnt_lo_u32_b32 v70, -1, 0\n\t"
    "v_mbcnt_hi_u32_b32 v70, -1, v70\n\t"
    "s_and_b32 s50, %14, 7\n\t"
    "s_lshl_b32 s50, s50, 4\n\t"
    "s_lshr_b32 s51, %14, 4\n\t"
    "s_add_u32 s50, s50, s51\n\t"
    "s_mul_i32 s50, s50, 102400\n\t"
    "s_add_u32 s50, s50, 3407872\n\t"
    "v_lshlrev_b32 v148, 7, v70\n\t"
    "buffer_load_dword v71, v148, %12, s50 offen sc1\n\t"
    "s_and_b32 s51, %14, 8\n\t"
    "s_lshl_b32 s51, s51, 10\n\t"
    "s_add_u32 s50, s50, s51\n\t"
    "s_add_u32 s50, s50, 0x1000\n\t"
    "v_lshrrev_b32 v149, 4, v70\n\t"
    "v_lshl_add_u32 v149, v149, 11, v148\n\t"
    "buffer_load_dword v71, v148, %12, s50 offen sc1\n\t"
    "s_bfe_u32 s50, %14, 0x40003\n\t"
    "s_lshl_b32 s50, s50, 14\n\t"
    "s_add_u32 s50, s50, 16517120\n\t"
    "v_lshrrev_b32 v150, 3, v70\n\t"
    "v_lshl_add_u32 v150, v150, 10, v148\n\t"
    "buffer_load_dword v71, v149, %12, s50 offen sc1\n\t"
    "s_and_b32 s50, %14, 8\n\t"
    "s_bfe_u32 s51, %14, 0x30004\n\t"
    "s_add_u32 s50, s50, s51\n\t"
    "s_mul_i32 s50, s50, 212992\n\t"
    "s_add_u32 s50, s50, 196608\n\t"
    "buffer_load_dword v71, v150, %12, s50 offen sc1\n\t"
    "s_setpc_b64 s[54:55]\n\t"
    "L_af1x_%=:\n\t"
    "s_mov_b32 s69, 0x40000000\n\t"
    "s_branch L_aend2_%=\n\t"
    "L_ahelp_%=:\n\t"
    "s_setprio 3\n\t"
    "v_cmp_gt_u32 vcc, 48, v147\n\t"
    "v_mov_b32 v133, 0x40000000\n\t"
    "v_lshrrev_b32 v134, 3, v147\n\t"
    "v_add_u32 v134, 6, v134\n\t"
    "v_bfe_u32 v135, v147, 2, 1\n\t"
    "v_and_b32 v130, 3, v147\n\t"
    "v_lshl_add_u32 v132, v135, 5, v130\n\t"
    "v_mul_u32_u24 v64, 400, v132\n\t"
    "v_lshl_add_u32 v64, v134, 5, v64\n\t"
    "s_movk_i32 s50, 0\n\t"
    "buffer_load_dwordx4 v[0:3], v64, %0, s50 offen offset:16\n\t"
    "buffer_load_dwordx4 v[4:7], v64, %0, s50 offen offset:32\n\t"
    "v_lshlrev_b32 v66, 14, v134\n\t"
    "v_lshl_add_u32 v66, v132, 4, v66\n\t"
    "v_lshlrev_b32 v68, 8, v134\n\t"
    "v_lshl_add_u32 v68, v135, 2, v68\n\t"
    "v_add_u32 v68, 130048, v68\n\t"
    "v_cndmask_b32 v68, v133, v68, vcc\n\t"
    "v_cndmask_b32 v66, v133, v66, vcc\n\t"
    "s_barrier\n\t"
    "s_load_dwordx2 s[72:73], %6, 0\n\t"
    "s_load_dwordx2 s[88:89], %7, 0\n\t"
    "s_load_dword s91, %8, 0\n\t"
    "v_lshlrev_b32 v128, 4, v147\n\t"
    "v_add_u32 v128, 133888, v128\n\t"
    "v_and_b32 v130, 1, v147\n\t"
    "v_cndmask_b32 v128, v133, v128, vcc\n\t"
    "v_mul_u32_u24 v127, 7, v130\n\t"
    "v_cmp_eq_u32 vcc, 1, v130\n\t"
    "v_mov_b32 v126, 0x07060100\n\t"
    "v_mov_b32 v133, 0x05040100\n\t"
    "v_mov_b32 v136, 1\n\t"
    "v_cndmask_b32 v126, v126, v133, vcc\n\t"
    "s_mov_b32 s36, 0\n\t"
    "s_mov_b32 s37, 8000\n\t"
    "s_mov_b32 s38, 16000\n\t"
    "s_mov_b32 s39, 24000\n\t"
    "s_mov_b32 s40, 32000\n\t"
    "s_mov_b32 s41, 40000\n\t"
    "s_mov_b32 s42, 48000\n\t"
    "s_mov_b32 s43, 56000\n\t"
    "s_movk_i32 s44, 0xf8\n\t"
    "s_mov_b32 s45, 0x01010000\n\t"
    "s_mov_b32 s46, 0x020c000c\n\t"
    "s_lshr_b32 s49, %14, 4\n\t"
    "s_mul_i32 s49, s49, 212992\n\t"
    "s_and_b32 s50, %14, 15\n\t"
    "s_lshl_b32 s50, s50, 10\n\t"
    "s_add_u32 s49, s49, s50\n\t"
    "s_lshl_b32 s52, %14, 2\n\t"
    "s_add_u32 s52, s52, 49291264\n\t"
    "buffer_load_dword v70, off, %12, s52 offset:1024 sc1\n\t"
    "s_cmp_eq_u32 %14, 0\n\t"
    "s_cselect_b32 s47, 49291264, 0x40000000\n\t"
    "s_cselect_b32 s48, 49283072, 0x40000000\n\t"
    "v_lshlrev_b32 v152, 4, v147\n\t"
    "v_lshlrev_b32 v132, 7, v147\n\t"
    "v_mov_b32 v71, 0\n\t"
    "buffer_store_dword v71, v132, %12, s48 offen sc1\n\t"
    "v_mov_b32 v148, 0\n\t"
    "v_mov_b32 v149, 0\n\t"
    "v_mov_b32 v150, 0\n\t"
    "v_mov_b32 v151, 0\n\t"
    "buffer_store_dwordx4 v[148:151], v152, %12, s47 offen offset:2048 sc1\n\t"
    "buffer_store_dwordx4 v[148:151], v152, %12, s47 offen offset:3072 sc1\n\t"
    "s_add_u32 s50, s48, 0x1000\n\t"
    "s_waitcnt vmcnt(6)\n\t"
    "v_lshlrev_b32 v134, 20, v147\n\t"
    "v_add_u32 v134, 2068, v134\n\t"
    "ds_add_u32 v134, v136\n\t"
    "v_mov_b32 v110, 65664\n\t"
    "v_mov_b32 v111, 131136\n\t"
    "v_mov_b32 v112, 262176\n\t"
    "v_mov_b32 v113, 524304\n\t"
    "v_mov_b32 v114, 1048584\n\t"
    "v_mov_b32 v115, 2097156\n\t"
    "v_mov_b32 v116, 4194306\n\t"
    "v_mov_b32 v117, 8388609\n\t"
    "s_waitcnt vmcnt(4)\n\t"
    "v_lshl_add_u32 v76, v0, 3, s36\n\t"
    "v_lshl_add_u32 v77, v1, 3, s37\n\t"
    "v_lshl_add_u32 v78, v2, 3, s38\n\t"
    "v_lshl_add_u32 v79, v3, 3, s39\n\t"
    "v_lshl_add_u32 v80, v4, 3, s40\n\t"
    "v_lshl_add_u32 v81, v5, 3, s41\n\t"
    "v_lshl_add_u32 v82, v6, 3, s42\n\t"
    "v_lshl_add_u32 v83, v7, 3, s43\n\t"
    "v_and_or_b32 v92, v76, s44, v68\n\t"
    "ds_or_rtn_b32 v118, v92, v110\n\t"
    "v_and_or_b32 v93, v77, s44, v68\n\t"
    "ds_or_rtn_b32 v119, v93, v111\n\t"
    "v_and_or_b32 v94, v78, s44, v68\n\t"
    "ds_or_rtn_b32 v120, v94, v112\n\t"
    "v_and_or_b32 v95, v79, s44, v68\n\t"
    "ds_or_rtn_b32 v121, v95, v113\n\t"
    "v_and_or_b32 v96, v80, s44, v68\n\t"
    "ds_or_rtn_b32 v122, v96, v114\n\t"
    "v_and_or_b32 v97, v81, s44, v68\n\t"
    "ds_or_rtn_b32 v123, v97, v115\n\t"
    "v_and_or_b32 v98, v82, s44, v68\n\t"
    "ds_or_rtn_b32 v124, v98, v116\n\t"
    "v_and_or_b32 v99, v83, s44, v68\n\t"
    "ds_or_rtn_b32 v125, v99, v117\n\t"
    "v_lshl_or_b32 v164, v77, 16, v76\n\t"
    "v_lshl_or_b32 v165, v79, 16, v78\n\t"
    "v_lshl_or_b32 v166, v81, 16, v80\n\t"
    "v_lshl_or_b32 v167, v83, 16, v82\n\t"
    "s_movk_i32 s50, 0\n\t"
    "buffer_load_dwordx4 v[8:11], v64, %0, s50 offen offset:1616\n\t"
    "buffer_load_dwordx4 v[12:15], v64, %0, s50 offen offset:1632\n\t"
    "s_movk_i32 s50, 3200\n\t"
    "buffer_load_dwordx4 v[16:19], v64, %0, s50 offen offset:16\n\t"
    "buffer_load_dwordx4 v[20:23], v64, %0, s50 offen offset:32\n\t"
    "s_waitcnt vmcnt(7)\n\t"
    "v_add_u32 v70, 1, v70\n\t"
    "buffer_store_dword v70, off, %12, s52 sc1\n\t"
    "v_mov_b32 v71, 2072\n\t"
    "ds_write_b32 v71, v70\n\t"
    "s_waitcnt vmcnt(3)\n\t"
    "v_lshl_add_u32 v84, v8, 3, s36\n\t"
    "v_lshl_add_u32 v85, v9, 3, s37\n\t"
    "v_lshl_add_u32 v86, v10, 3, s38\n\t"
    "v_lshl_add_u32 v87, v11, 3, s39\n\t"
    "v_lshl_add_u32 v88, v12, 3, s40\n\t"
    "v_lshl_add_u32 v89, v13, 3, s41\n\t"
    "v_lshl_add_u32 v90, v14, 3, s42\n\t"
    "v_lshl_add_u32 v91, v15, 3, s43\n\t"
    "s_waitcnt lgkmcnt(5)\n\t"
    "v_and_b32 v118, v118, v110\n\t"
    "v_perm_b32 v118, v118, v118, s46\n\t"
    "ds_or_b32 v92, v118\n\t"
    "v_and_b32 v119, v119, v111\n\t"
    "v_perm_b32 v119, v119, v119, s46\n\t"
    "ds_or_b32 v93, v119\n\t"
    "v_and_b32 v120, v120, v112\n\t"
    "v_perm_b32 v120, v120, v120, s46\n\t"
    "ds_or_b32 v94, v120\n\t"
    "v_and_b32 v121, v121, v113\n\t"
    "v_perm_b32 v121, v121, v121, s46\n\t"
    "ds_or_b32 v95, v121\n\t"
    "s_waitcnt lgkmcnt(5)\n\t"
    "v_and_b32 v122, v122, v114\n\t"
    "v_perm_b32 v122, v122, v122, s46\n\t"
    "ds_or_b32 v96, v122\n\t"
    "v_and_b32 v123, v123, v115\n\t"
    "v_perm_b32 v123, v123, v123, s46\n\t"
    "ds_or_b32 v97, v123\n\t"
    "v_and_b32 v124, v124, v116\n\t"
    "v_perm_b32 v124, v124, v124, s46\n\t"
    "ds_or_b32 v98, v124\n\t"
    "v_and_b32 v125, v125, v117\n\t"
    "v_perm_b32 v125, v125, v125, s46\n\t"
    "ds_or_b32 v99, v125\n\t"
    "buffer_store_dwordx4 v[164:167], v66, %12, s49 offen offset:0 sc1\n\t"
    "v_and_or_b32 v100, v84, s44, v68\n\t"
    "ds_read_b32 v100, v100\n\t"
    "v_and_or_b32 v101, v85, s44, v68\n\t"
    "ds_read_b32 v101, v101\n\t"
    "v_and_or_b32 v102, v86, s44, v68\n\t"
    "ds_read_b32 v102, v102\n\t"
    "v_and_or_b32 v103, v87, s44, v68\n\t"
    "ds_read_b32 v103, v103\n\t"
    "v_and_or_b32 v104, v88, s44, v68\n\t"
    "ds_read_b32 v104, v104\n\t"
    "v_and_or_b32 v105, v89, s44, v68\n\t"
    "ds_read_b32 v105, v105\n\t"
    "v_and_or_b32 v106, v90, s44, v68\n\t"
    "ds_read_b32 v106, v106\n\t"
    "v_and_or_b32 v107, v91, s44, v68\n\t"
    "ds_read_b32 v107, v107\n\t"
    "s_waitcnt vmcnt(2)\n\t"
    "v_lshl_add_u32 v76, v16, 3, s36\n\t"
    "v_lshl_add_u32 v77, v17, 3, s37\n\t"
    "v_lshl_add_u32 v78, v18, 3, s38\n\t"
    "v_lshl_add_u32 v79, v19, 3, s39\n\t"
    "v_lshl_add_u32 v80, v20, 3, s40\n\t"
    "v_lshl_add_u32 v81, v21, 3, s41\n\t"
    "v_lshl_add_u32 v82, v22, 3, s42\n\t"
    "v_lshl_add_u32 v83, v23, 3, s43\n\t"
    "s_movk_i32 s50, 3200\n\t"
    "buffer_load_dwordx4 v[24:27], v64, %0, s50 offen offset:1616\n\t"
    "buffer_load_dwordx4 v[28:31], v64, %0, s50 offen offset:1632\n\t"
    "s_movk_i32 s50, 6400\n\t"
    "buffer_load_dwordx4 v[32:35], v64, %0, s50 offen offset:16\n\t"
    "buffer_load_dwordx4 v[36:39], v64, %0, s50 offen offset:32\n\t"
    "s_cmp_lg_u32 %14, 0\n\t"
    "s_cbranch_scc1 L_ahe184_%=\n\t"
    "buffer_load_dwordx4 v[148:151], v152, %12, s47 offen sc1\n\t"
    "L_ahe184_%=:\n\t"
    "v_mov_b32 v129, 0xffff0000\n\t"
    "s_waitcnt lgkmcnt(4)\n\t"
    "v_perm_b32 v100, v100, v84, v126\n\t"
    "v_perm_b32 v101, v101, v85, v126\n\t"
    "v_perm_b32 v102, v102, v86, v126\n\t"
    "v_perm_b32 v103, v103, v87, v126\n\t"
    "s_waitcnt lgkmcnt(0)\n\t"
    "v_perm_b32 v104, v104, v88, v126\n\t"
    "v_perm_b32 v105, v105, v89, v126\n\t"
    "v_perm_b32 v106, v106, v90, v126\n\t"
    "v_perm_b32 v107, v107, v91, v126\n\t"
    "v_max_u32 v108, v100, v101\n\t"
    "v_min_u32 v101, v100, v101\n\t"
    "v_max_u32 v100, v102, v103\n\t"
    "v_min_u32 v103, v102, v103\n\t"
    "v_max_u32 v102, v104, v105\n\t"
    "v_min_u32 v105, v104, v105\n\t"
    "v_max_u32 v104, v106, v107\n\t"
    "v_min_u32 v107, v106, v107\n\t"
    "v_max_u32 v106, v108, v100\n\t"
    "v_min_u32 v100, v108, v100\n\t"
    "v_max_u32 v108, v101, v103\n\t"
    "v_min_u32 v103, v101, v103\n\t"
    "v_max_u32 v101, v102, v104\n\t"
    "v_min_u32 v104, v102, v104\n\t"
    "v_max_u32 v102, v105, v107\n\t"
    "v_min_u32 v107, v105, v107\n\t"
    "v_bfi_b32 v130, v106, 0, v129\n\t"
    "v_cmp_eq_u32 vcc, 0, v130\n\t"
    "v_and_or_b32 v92, v106, s44, v68\n\t"
    "s_nop 0\n\t"
    "v_cndmask_b32 v130, v130, v129, vcc\n\t"
    "v_ffbl_b32 v131, v130\n\t"
    "v_and_b32 v134, 7, v131\n\t"
    "v_lshlrev_b32 v134, v134, s45\n\t"
    "v_xor_b32 v129, v129, v134\n\t"
    "v_xor_b32 v132, v131, v127\n\t"
    "v_bfi_b32 v130, v101, 0, v129\n\t"
    "v_cmp_eq_u32 vcc, 0, v130\n\t"
    "v_and_or_b32 v93, v101, s44, v68\n\t"
    "v_xor_b32 v133, 23, v132\n\t"
    "v_lshlrev_b32 v110, v132, v136\n\t"
    "v_and_b32 v132, 7, v132\n\t"
    "v_lshl_or_b32 v110, v136, v133, v110\n\t"
    "v_lshl_add_u32 v132, v132, 1, v128\n\t"
    "ds_or_rtn_b32 v118, v92, v110\n\t"
    "ds_write_b16 v132, v106\n\t"
    "v_cndmask_b32 v130, v130, v129, vcc\n\t"
    "v_ffbl_b32 v131, v130\n\t"
    "v_and_b32 v134, 7, v131\n\t"
    "v_lshlrev_b32 v134, v134, s45\n\t"
    "v_xor_b32 v129, v129, v134\n\t"
    "v_xor_b32 v132, v131, v127\n\t"
    "v_bfi_b32 v130, v108, 0, v129\n\t"
    "v_cmp_eq_u32 vcc, 0, v130\n\t"
    "v_and_or_b32 v94, v108, s44, v68\n\t"
    "v_xor_b32 v133, 23, v132\n\t"
    "v_lshlrev_b32 v111, v132, v136\n\t"
    "v_and_b32 v132, 7, v132\n\t"
    "v_lshl_or_b32 v111, v136, v133, v111\n\t"
    "v_lshl_add_u32 v132, v132, 1, v128\n\t"
    "ds_or_rtn_b32 v119, v93, v111\n\t"
    "ds_write_b16 v132, v101\n\t"
    "v_cndmask_b32 v130, v130, v129, vcc\n\t"
    "v_ffbl_b32 v131, v130\n\t"
    "v_and_b32 v134, 7, v131\n\t"
    "v_lshlrev_b32 v134, v134, s45\n\t"
    "v_xor_b32 v129, v129, v134\n\t"
    "v_xor_b32 v132, v131, v127\n\t"
    "v_bfi_b32 v130, v100, 0, v129\n\t"
    "v_cmp_eq_u32 vcc, 0, v130\n\t"
    "v_and_or_b32 v95, v100, s44, v68\n\t"
    "v_xor_b32 v133, 23, v132\n\t"
    "v_lshlrev_b32 v112, v132, v136\n\t"
    "v_and_b32 v132, 7, v132\n\t"
    "v_lshl_or_b32 v112, v136, v133, v112\n\t"
    "v_lshl_add_u32 v132, v132, 1, v128\n\t"
    "ds_or_rtn_b32 v120, v94, v112\n\t"
    "ds_write_b16 v132, v108\n\t"
    "v_cndmask_b32 v130, v130, v129, vcc\n\t"
    "v_ffbl_b32 v131, v130\n\t"
    "v_and_b32 v134, 7, v131\n\t"
    "v_lshlrev_b32 v134, v134, s45\n\t"
    "v_xor_b32 v129, v129, v134\n\t"
    "v_xor_b32 v132, v131, v127\n\t"
    "v_bfi_b32 v130, v102, 0, v129\n\t"
    "v_cmp_eq_u32 vcc, 0, v130\n\t"
    "v_and_or_b32 v96, v102, s44, v68\n\t"
    "v_xor_b32 v133, 23, v132\n\t"
    "v_lshlrev_b32 v113, v132, v136\n\t"
    "v_and_b32 v132, 7, v132\n\t"
    "v_lshl_or_b32 v113, v136, v133, v113\n\t"
    "v_lshl_add_u32 v132, v132, 1, v128\n\t"
    "ds_or_rtn_b32 v121, v95, v113\n\t"
    "ds_write_b16 v132, v100\n\t"
    "v_cndmask_b32 v130, v130, v129, vcc\n\t"
    "v_ffbl_b32 v131, v130\n\t"
    "v_and_b32 v134, 7, v131\n\t"
    "v_lshlrev_b32 v134, v134, s45\n\t"
    "v_xor_b32 v129, v129, v134\n\t"
    "v_xor_b32 v132, v131, v127\n\t"
    "s_waitcnt lgkmcnt(7)\n\t"
    "v_and_b32 v118, v118, v110\n\t"
    "v_perm_b32 v118, v118, v118, s46\n\t"
    "ds_or_b32 v92, v118\n\t"
    "v_bfi_b32 v130, v104, 0, v129\n\t"
    "v_cmp_eq_u32 vcc, 0, v130\n\t"
    "v_and_or_b32 v97, v104, s44, v68\n\t"
    "v_xor_b32 v133, 23, v132\n\t"
    "v_lshlrev_b32 v114, v132, v136\n\t"
    "v_and_b32 v132, 7, v132\n\t"
    "v_lshl_or_b32 v114, v136, v133, v114\n\t"
    "v_lshl_add_u32 v132, v132, 1, v128\n\t"
    "ds_or_rtn_b32 v122, v96, v114\n\t"
    "ds_write_b16 v132, v102\n\t"
    "v_cndmask_b32 v130, v130, v129, vcc\n\t"
    "v_ffbl_b32 v131, v130\n\t"
    "v_and_b32 v134, 7, v131\n\t"
    "v_lshlrev_b32 v134, v134, s45\n\t"
    "v_xor_b32 v129, v129, v134\n\t"
    "v_xor_b32 v132, v131, v127\n\t"
    "s_waitcnt lgkmcnt(8)\n\t"
    "v_and_b32 v119, v119, v111\n\t"
    "v_perm_b32 v119, v119, v119, s46\n\t"
    "ds_or_b32 v93, v119\n\t"
    "v_bfi_b32 v130, v103, 0, v129\n\t"
    "v_cmp_eq_u32 vcc, 0, v130\n\t"
    "v_and_or_b32 v98, v103, s44, v68\n\t"
    "v_xor_b32 v133, 23, v132\n\t"
    "v_lshlrev_b32 v115, v132, v136\n\t"
    "v_and_b32 v132, 7, v132\n\t"
    "v_lshl_or_b32 v115, v136, v133, v115\n\t"
    "v_lshl_add_u32 v132, v132, 1, v128\n\t"
    "ds_or_rtn_b32 v123, v97, v115\n\t"
    "ds_write_b16 v132, v104\n\t"
    "v_cndmask_b32 v130, v130, v129, vcc\n\t"
    "v_ffbl_b32 v131, v130\n\t"
    "v_and_b32 v134, 7, v131\n\t"
    "v_lshlrev_b32 v134, v134, s45\n\t"
    "v_xor_b32 v129, v129, v134\n\t"
    "v_xor_b32 v132, v131, v127\n\t"
    "s_waitcnt lgkmcnt(9)\n\t"
    "v_and_b32 v120, v120, v112\n\t"
    "v_perm_b32 v120, v120, v120, s46\n\t"
    "ds_or_b32 v94, v120\n\t"
    "v_bfi_b32 v130, v107, 0, v129\n\t"
    "v_cmp_eq_u32 vcc, 0, v130\n\t"
    "v_and_or_b32 v99, v107, s44, v68\n\t"
    "v_xor_b32 v133, 23, v132\n\t"
    "v_lshlrev_b32 v116, v132, v136\n\t"
    "v_and_b32 v132, 7, v132\n\t"
    "v_lshl_or_b32 v116, v136, v133, v116\n\t"
    "v_lshl_add_u32 v132, v132, 1, v128\n\t"
    "ds_or_rtn_b32 v124, v98, v116\n\t"
    "ds_write_b16 v132, v103\n\t"
    "v_cndmask_b32 v130, v130, v129, vcc\n\t"
    "v_ffbl_b32 v131, v130\n\t"
    "v_xor_b32 v132, v131, v127\n\t"
    "s_waitcnt lgkmcnt(10)\n\t"
    "v_and_b32 v121, v121, v113\n\t"
    "v_perm_b32 v121, v121, v121, s46\n\t"
    "ds_or_b32 v95, v121\n\t"
    "v_xor_b32 v133, 23, v132\n\t"
    "v_lshlrev_b32 v117, v132, v136\n\t"
    "v_and_b32 v132, 7, v132\n\t"
    "v_lshl_or_b32 v117, v136, v133, v117\n\t"
    "v_lshl_add_u32 v132, v132, 1, v128\n\t"
    "ds_or_rtn_b32 v125, v99, v117\n\t"
    "ds_write_b16 v132, v107\n\t"
    "s_waitcnt lgkmcnt(10)\n\t"
    "v_and_b32 v122, v122, v114\n\t"
    "v_perm_b32 v122, v122, v122, s46\n\t"
    "ds_or_b32 v96, v122\n\t"
    "ds_read_b128 v[72:75], v128\n\t"
    "v_and_or_b32 v100, v76, s44, v68\n\t"
    "ds_read_b32 v100, v100\n\t"
    "v_and_or_b32 v101, v77, s44, v68\n\t"
    "ds_read_b32 v101, v101\n\t"
    "v_and_or_b32 v102, v78, s44, v68\n\t"
    "ds_read_b32 v102, v102\n\t"
    "v_and_or_b32 v103, v79, s44, v68\n\t"
    "ds_read_b32 v103, v103\n\t"
    "v_and_or_b32 v104, v80, s44, v68\n\t"
    "ds_read_b32 v104, v104\n\t"
    "v_and_or_b32 v105, v81, s44, v68\n\t"
    "ds_read_b32 v105, v105\n\t"
    "v_and_or_b32 v106, v82, s44, v68\n\t"
    "ds_read_b32 v106, v106\n\t"
    "v_and_or_b32 v107, v83, s44, v68\n\t"
    "ds_read_b32 v107, v107\n\t"
    "s_cmp_lg_u32 %14, 0\n\t"
    "s_cbranch_scc1 L_ahe378_%=\n\t"
    "s_waitcnt vmcnt(0)\n\t"
    "buffer_store_dwordx4 v[148:151], v152, %12, s47 offen offset:1024 sc1\n\t"
    "L_ahe378_%=:\n\t"
    "s_waitcnt vmcnt(2)\n\t"
    "v_lshl_add_u32 v84, v24, 3, s36\n\t"
    "v_lshl_add_u32 v85, v25, 3, s37\n\t"
    "v_lshl_add_u32 v86, v26, 3, s38\n\t"
    "v_lshl_add_u32 v87, v27, 3, s39\n\t"
    "v_lshl_add_u32 v88, v28, 3, s40\n\t"
    "v_lshl_add_u32 v89, v29, 3, s41\n\t"
    "v_lshl_add_u32 v90, v30, 3, s42\n\t"
    "v_lshl_add_u32 v91, v31, 3, s43\n\t"
    "v_mul_u32_u24 v58, 400, v147\n\t"
    "buffer_load_dwordx4 v[48:51], v58, %0, 0 offen\n\t"
    "s_lshr_b32 s50, %14, 5\n\t"
    "s_mul_i32 s50, s50, 125\n\t"
    "s_bfe_u32 s51, %14, 0x30002\n\t"
    "s_lshl_b32 s51, s51, 4\n\t"
    "s_min_u32 s51, s51, 109\n\t"
    "s_add_u32 s50, s50, s51\n\t"
    "s_lshl_b32 s50, s50, 8\n\t"
    "s_and_b32 s51, %14, 3\n\t"
    "s_lshl_b32 s51, s51, 6\n\t"
    "s_add_u32 s50, s50, s51\n\t"
    "s_bitcmp1_b32 %14, 1\n\t"
    "s_cselect_b32 s50, s50, 0x40000000\n\t"
    "v_and_b32 v59, 48, v147\n\t"
    "v_and_b32 v60, 15, v147\n\t"
    "v_lshl_add_u32 v59, v60, 8, v59\n\t"
    "buffer_load_dwordx4 v[52:55], v59, %15, s50 offen\n\t"
    "s_cmp_lg_u32 %14, 0\n\t"
    "s_cbranch_scc1 L_ahe410_%=\n\t"
    "buffer_load_dwordx4 v[148:151], v152, %12, s47 offen sc1\n\t"
    "L_ahe410_%=:\n\t"
    "s_waitcnt lgkmcnt(11)\n\t"
    "v_and_b32 v123, v123, v115\n\t"
    "v_perm_b32 v123, v123, v123, s46\n\t"
    "ds_or_b32 v97, v123\n\t"
    "v_and_b32 v124, v124, v116\n\t"
    "v_perm_b32 v124, v124, v124, s46\n\t"
    "ds_or_b32 v98, v124\n\t"
    "v_and_b32 v125, v125, v117\n\t"
    "v_perm_b32 v125, v125, v125, s46\n\t"
    "ds_or_b32 v99, v125\n\t"
    "s_waitcnt lgkmcnt(11)\n\t"
    "buffer_store_dwordx4 v[72:75], v66, %12, s49 offen offset:64 sc1\n\t"
    "v_mov_b32 v129, 0xffff0000\n\t"
    "s_waitcnt lgkmcnt(7)\n\t"
    "v_perm_b32 v100, v100, v76, v126\n\t"
    "v_perm_b32 v101, v101, v77, v126\n\t"
    "v_perm_b32 v102, v102, v78, v126\n\t"
    "v_perm_b32 v103, v103, v79, v126\n\t"
    "s_waitcnt lgkmcnt(3)\n\t"
    "v_perm_b32 v104, v104, v80, v126\n\t"
    "v_perm_b32 v105, v105, v81, v126\n\t"
    "v_perm_b32 v106, v106, v82, v126\n\t"
    "v_perm_b32 v107, v107, v83, v126\n\t"
    "v_max_u32 v108, v100, v101\n\t"
    "v_min_u32 v101, v100, v101\n\t"
    "v_max_u32 v100, v102, v103\n\t"
    "v_min_u32 v103, v102, v103\n\t"
    "v_max_u32 v102, v104, v105\n\t"
    "v_min_u32 v105, v104, v105\n\t"
    "v_max_u32 v104, v106, v107\n\t"
    "v_min_u32 v107, v106, v107\n\t"
    "v_max_u32 v106, v108, v100\n\t"
    "v_min_u32 v100, v108, v100\n\t"
    "v_max_u32 v108, v101, v103\n\t"
    "v_min_u32 v103, v101, v103\n\t"
    "v_max_u32 v101, v102, v104\n\t"
    "v_min_u32 v104, v102, v104\n\t"
    "v_max_u32 v102, v105, v107\n\t"
    "v_min_u32 v107, v105, v107\n\t"
    "v_bfi_b32 v130, v106, 0, v129\n\t"
    "v_cmp_eq_u32 vcc, 0, v130\n\t"
    "v_and_or_b32 v92, v106, s44, v68\n\t"
    "s_nop 0\n\t"
    "v_cndmask_b32 v130, v130, v129, vcc\n\t"
    "v_ffbl_b32 v131, v130\n\t"
    "v_and_b32 v134, 7, v131\n\t"
    "v_lshlrev_b32 v134, v134, s45\n\t"
    "v_xor_b32 v129, v129, v134\n\t"
    "v_xor_b32 v132, v131, v127\n\t"
    "v_bfi_b32 v130, v101, 0, v129\n\t"
    "v_cmp_eq_u32 vcc, 0, v130\n\t"
    "v_and_or_b32 v93, v101, s44, v68\n\t"
    "v_xor_b32 v133, 23, v132\n\t"
    "v_lshlrev_b32 v110, v132, v136\n\t"
    "v_and_b32 v132, 7, v132\n\t"
    "v_lshl_or_b32 v110, v136, v133, v110\n\t"
    "v_lshl_add_u32 v132, v132, 1, v128\n\t"
    "ds_or_rtn_b32 v118, v92, v110\n\t"
    "ds_write_b16 v132, v106\n\t"
    "v_cndmask_b32 v130, v130, v129, vcc\n\t"
    "v_ffbl_b32 v131, v130\n\t"
    "v_and_b32 v134, 7, v131\n\t"
    "v_lshlrev_b32 v134, v134, s45\n\t"
    "v_xor_b32 v129, v129, v134\n\t"
    "v_xor_b32 v132, v131, v127\n\t"
    "v_bfi_b32 v130, v108, 0, v129\n\t"
    "v_cmp_eq_u32 vcc, 0, v130\n\t"
    "v_and_or_b32 v94, v108, s44, v68\n\t"
    "v_xor_b32 v133, 23, v132\n\t"
    "v_lshlrev_b32 v111, v132, v136\n\t"
    "v_and_b32 v132, 7, v132\n\t"
    "v_lshl_or_b32 v111, v136, v133, v111\n\t"
    "v_lshl_add_u32 v132, v132, 1, v128\n\t"
    "ds_or_rtn_b32 v119, v93, v111\n\t"
    "ds_write_b16 v132, v101\n\t"
    "v_cndmask_b32 v130, v130, v129, vcc\n\t"
    "v_ffbl_b32 v131, v130\n\t"
    "v_and_b32 v134, 7, v131\n\t"
    "v_lshlrev_b32 v134, v134, s45\n\t"
    "v_xor_b32 v129, v129, v134\n\t"
    "v_xor_b32 v132, v131, v127\n\t"
    "v_bfi_b32 v130, v100, 0, v129\n\t"
    "v_cmp_eq_u32 vcc, 0, v130\n\t"
    "v_and_or_b32 v95, v100, s44, v68\n\t"
    "v_xor_b32 v133, 23, v132\n\t"
    "v_lshlrev_b32 v112, v132, v136\n\t"
    "v_and_b32 v132, 7, v132\n\t"
    "v_lshl_or_b32 v112, v136, v133, v112\n\t"
    "v_lshl_add_u32 v132, v132, 1, v128\n\t"
    "ds_or_rtn_b32 v120, v94, v112\n\t"
    "ds_write_b16 v132, v108\n\t"
    "v_cndmask_b32 v130, v130, v129, vcc\n\t"
    "v_ffbl_b32 v131, v130\n\t"
    "v_and_b32 v134, 7, v131\n\t"
    "v_lshlrev_b32 v134, v134, s45\n\t"
    "v_xor_b32 v129, v129, v134\n\t"
    "v_xor_b32 v132, v131, v127\n\t"
    "v_bfi_b32 v130, v102, 0, v129\n\t"
    "v_cmp_eq_u32 vcc, 0, v130\n\t"
    "v_and_or_b32 v96, v102, s44, v68\n\t"
    "v_xor_b32 v133, 23, v132\n\t"
    "v_lshlrev_b32 v113, v132, v136\n\t"
    "v_and_b32 v132, 7, v132\n\t"
    "v_lshl_or_b32 v113, v136, v133, v113\n\t"
    "v_lshl_add_u32 v132, v132, 1, v128\n\t"
    "ds_or_rtn_b32 v121, v95, v113\n\t"
    "ds_write_b16 v132, v100\n\t"
    "v_cndmask_b32 v130, v130, v129, vcc\n\t"
    "v_ffbl_b32 v131, v130\n\t"
    "v_and_b32 v134, 7, v131\n\t"
    "v_lshlrev_b32 v134, v134, s45\n\t"
    "v_xor_b32 v129, v129, v134\n\t"
    "v_xor_b32 v132, v131, v127\n\t"
    "s_waitcnt lgkmcnt(7)\n\t"
    "v_and_b32 v118, v118, v110\n\t"
    "v_perm_b32 v118, v118, v118, s46\n\t"
    "ds_or_b32 v92, v118\n\t"
    "v_bfi_b32 v130, v104, 0, v129\n\t"
    "v_cmp_eq_u32 vcc, 0, v130\n\t"
    "v_and_or_b32 v97, v104, s44, v68\n\t"
    "v_xor_b32 v133, 23, v132\n\t"
    "v_lshlrev_b32 v114, v132, v136\n\t"
    "v_and_b32 v132, 7, v132\n\t"
    "v_lshl_or_b32 v114, v136, v133, v114\n\t"
    "v_lshl_add_u32 v132, v132, 1, v128\n\t"
    "ds_or_rtn_b32 v122, v96, v114\n\t"
    "ds_write_b16 v132, v102\n\t"
    "v_cndmask_b32 v130, v130, v129, vcc\n\t"
    "v_ffbl_b32 v131, v130\n\t"
    "v_and_b32 v134, 7, v131\n\t"
    "v_lshlrev_b32 v134, v134, s45\n\t"
    "v_xor_b32 v129, v129, v134\n\t"
    "v_xor_b32 v132, v131, v127\n\t"
    "s_waitcnt lgkmcnt(8)\n\t"
    "v_and_b32 v119, v119, v111\n\t"
    "v_perm_b32 v119, v119, v119, s46\n\t"
    "ds_or_b32 v93, v119\n\t"
    "v_bfi_b32 v130, v103, 0, v129\n\t"
    "v_cmp_eq_u32 vcc, 0, v130\n\t"
    "v_and_or_b32 v98, v103, s44, v68\n\t"
    "v_xor_b32 v133, 23, v132\n\t"
    "v_lshlrev_b32 v115, v132, v136\n\t"
    "v_and_b32 v132, 7, v132\n\t"
    "v_lshl_or_b32 v115, v136, v133, v115\n\t"
    "v_lshl_add_u32 v132, v132, 1, v128\n\t"
    "ds_or_rtn_b32 v123, v97, v115\n\t"
    "ds_write_b16 v132, v104\n\t"
    "v_cndmask_b32 v130, v130, v129, vcc\n\t"
    "v_ffbl_b32 v131, v130\n\t"
    "v_and_b32 v134, 7, v131\n\t"
    "v_lshlrev_b32 v134, v134, s45\n\t"
    "v_xor_b32 v129, v129, v134\n\t"
    "v_xor_b32 v132, v131, v127\n\t"
    "s_waitcnt lgkmcnt(9)\n\t"
    "v_and_b32 v120, v120, v112\n\t"
    "v_perm_b32 v120, v120, v120, s46\n\t"
    "ds_or_b32 v94, v120\n\t"
    "v_bfi_b32 v130, v107, 0, v129\n\t"
    "v_cmp_eq_u32 vcc, 0, v130\n\t"
    "v_and_or_b32 v99, v107, s44, v68\n\t"
    "v_xor_b32 v133, 23, v132\n\t"
    "v_lshlrev_b32 v116, v132, v136\n\t"
    "v_and_b32 v132, 7, v132\n\t"
    "v_lshl_or_b32 v116, v136, v133, v116\n\t"
    "v_lshl_add_u32 v132, v132, 1, v128\n\t"
    "ds_or_rtn_b32 v124, v98, v116\n\t"
    "ds_write_b16 v132, v103\n\t"
    "v_cndmask_b32 v130, v130, v129, vcc\n\t"
    "v_ffbl_b32 v131, v130\n\t"
    "v_xor_b32 v132, v131, v127\n\t"
    "s_waitcnt lgkmcnt(10)\n\t"
    "v_and_b32 v121, v121, v113\n\t"
    "v_perm_b32 v121, v121, v121, s46\n\t"
    "ds_or_b32 v95, v121\n\t"
    "v_xor_b32 v133, 23, v132\n\t"
    "v_lshlrev_b32 v117, v132, v136\n\t"
    "v_and_b32 v132, 7, v132\n\t"
    "v_lshl_or_b32 v117, v136, v133, v117\n\t"
    "v_lshl_add_u32 v132, v132, 1, v128\n\t"
    "ds_or_rtn_b32 v125, v99, v117\n\t"
    "ds_write_b16 v132, v107\n\t"
    "s_waitcnt lgkmcnt(10)\n\t"
    "v_and_b32 v122, v122, v114\n\t"
    "v_perm_b32 v122, v122, v122, s46\n\t"
    "ds_or_b32 v96, v122\n\t"
    "ds_read_b128 v[72:75], v128\n\t"
    "v_and_or_b32 v100, v84, s44, v68\n\t"
    "ds_read_b32 v100, v100\n\t"
    "v_and_or_b32 v101, v85, s44, v68\n\t"
    "ds_read_b32 v101, v101\n\t"
    "v_and_or_b32 v102, v86, s44, v68\n\t"
    "ds_read_b32 v102, v102\n\t"
    "v_and_or_b32 v103, v87, s44, v68\n\t"
    "ds_read_b32 v103, v103\n\t"
    "v_and_or_b32 v104, v88, s44, v68\n\t"
    "ds_read_b32 v104, v104\n\t"
    "v_and_or_b32 v105, v89, s44, v68\n\t"
    "ds_read_b32 v105, v105\n\t"
    "v_and_or_b32 v106, v90, s44, v68\n\t"
    "ds_read_b32 v106, v106\n\t"
    "v_and_or_b32 v107, v91, s44, v68\n\t"
    "ds_read_b32 v107, v107\n\t"
    "s_cmp_lg_u32 %14, 0\n\t"
    "s_cbranch_scc1 L_ahe616_%=\n\t"
    "s_waitcnt vmcnt(0)\n\t"
    "buffer_store_dwordx4 v[148:151], v152, %12, s47 offen offset:1024 sc1\n\t"
    "L_ahe616_%=:\n\t"
    "s_waitcnt vmcnt(3)\n\t"
    "v_lshl_add_u32 v76, v32, 3, s36\n\t"
    "v_lshl_add_u32 v77, v33, 3, s37\n\t"
    "v_lshl_add_u32 v78, v34, 3, s38\n\t"
    "v_lshl_add_u32 v79, v35, 3, s39\n\t"
    "v_lshl_add_u32 v80, v36, 3, s40\n\t"
    "v_lshl_add_u32 v81, v37, 3, s41\n\t"
    "v_lshl_add_u32 v82, v38, 3, s42\n\t"
    "v_lshl_add_u32 v83, v39, 3, s43\n\t"
    "s_waitcnt vmcnt(1)\n\t"
    "v_lshl_add_u32 v48, v48, 3, s36\n\t"
    "v_lshl_add_u32 v49, v49, 3, s37\n\t"
    "v_lshl_add_u32 v50, v50, 3, s38\n\t"
    "v_lshl_add_u32 v51, v51, 3, s39\n\t"
    "v_lshl_or_b32 v56, v49, 16, v48\n\t"
    "v_lshl_or_b32 v57, v51, 16, v50\n\t"
    "s_and_b32 s51, %14, 1\n\t"
    "s_mul_i32 s51, s51, 1016\n\t"
    "s_sub_u32 s50, s49, s51\n\t"
    "s_add_u32 s50, s50, 196608\n\t"
    "buffer_store_dwordx2 v[56:57], v152, %12, s50 offen sc1\n\t"
    "s_lshl_b32 s50, %14, 10\n\t"
    "s_add_u32 s50, s50, 16515072\n\t"
    "s_bitcmp1_b32 %14, 1\n\t"
    "s_cselect_b32 s50, s50, 0x40000000\n\t"
    "buffer_store_dwordx4 v[52:55], v152, %12, s50 offen sc1\n\t"
    "s_mul_i32 s50, %14, 69632\n\t"
    "s_add_u32 s50, s50, 31522816\n\t"
    "v_mov_b32 v60, 0\n\t"
    "v_lshlrev_b32 v61, 3, v152\n\t"
    "s_mov_b64 exec, 0xffffffff\n\t"
    "buffer_store_dword v60, v61, %12, s50 offen sc1\n\t"
    "s_lshl_b32 s50, %14, 7\n\t"
    "s_add_u32 s50, s50, 49295360\n\t"
    "v_lshrrev_b32 v62, 2, v152\n\t"
    "buffer_store_dword v60, v62, %12, s50 offen sc1\n\t"
    "s_mov_b64 exec, -1\n\t"
    "s_cmp_lg_u32 %14, 0\n\t"
    "s_cbranch_scc1 L_ahe658_%=\n\t"
    "buffer_load_dwordx4 v[148:151], v152, %12, s47 offen sc1\n\t"
    "L_ahe658_%=:\n\t"
    "s_waitcnt lgkmcnt(11)\n\t"
    "v_and_b32 v123, v123, v115\n\t"
    "v_perm_b32 v123, v123, v123, s46\n\t"
    "ds_or_b32 v97, v123\n\t"
    "v_and_b32 v124, v124, v116\n\t"
    "v_perm_b32 v124, v124, v124, s46\n\t"
    "ds_or_b32 v98, v124\n\t"
    "v_and_b32 v125, v125, v117\n\t"
    "v_perm_b32 v125, v125, v125, s46\n\t"
    "ds_or_b32 v99, v125\n\t"
    "s_waitcnt lgkmcnt(11)\n\t"
    "buffer_store_dwordx4 v[72:75], v66, %12, s49 offen offset:128 sc1\n\t"
    "v_mov_b32 v129, 0xffff0000\n\t"
    "s_waitcnt lgkmcnt(7)\n\t"
    "v_perm_b32 v100, v100, v84, v126\n\t"
    "v_perm_b32 v101, v101, v85, v126\n\t"
    "v_perm_b32 v102, v102, v86, v126\n\t"
    "v_perm_b32 v103, v103, v87, v126\n\t"
    "s_waitcnt lgkmcnt(3)\n\t"
    "v_perm_b32 v104, v104, v88, v126\n\t"
    "v_perm_b32 v105, v105, v89, v126\n\t"
    "v_perm_b32 v106, v106, v90, v126\n\t"
    "v_perm_b32 v107, v107, v91, v126\n\t"
    "v_max_u32 v108, v100, v101\n\t"
    "v_min_u32 v101, v100, v101\n\t"
    "v_max_u32 v100, v102, v103\n\t"
    "v_min_u32 v103, v102, v103\n\t"
    "v_max_u32 v102, v104, v105\n\t"
    "v_min_u32 v105, v104, v105\n\t"
    "v_max_u32 v104, v106, v107\n\t"
    "v_min_u32 v107, v106, v107\n\t"
    "v_max_u32 v106, v108, v100\n\t"
    "v_min_u32 v100, v108, v100\n\t"
    "v_max_u32 v108, v101, v103\n\t"
    "v_min_u32 v103, v101, v103\n\t"
    "v_max_u32 v101, v102, v104\n\t"
    "v_min_u32 v104, v102, v104\n\t"
    "v_max_u32 v102, v105, v107\n\t"
    "v_min_u32 v107, v105, v107\n\t"
    "v_bfi_b32 v130, v106, 0, v129\n\t"
    "v_cmp_eq_u32 vcc, 0, v130\n\t"
    "v_and_or_b32 v92, v106, s44, v68\n\t"
    "s_nop 0\n\t"
    "v_cndmask_b32 v130, v130, v129, vcc\n\t"
    "v_ffbl_b32 v131, v130\n\t"
    "v_and_b32 v134, 7, v131\n\t"
    "v_lshlrev_b32 v134, v134, s45\n\t"
    "v_xor_b32 v129, v129, v134\n\t"
    "v_xor_b32 v132, v131, v127\n\t"
    "v_bfi_b32 v130, v101, 0, v129\n\t"
    "v_cmp_eq_u32 vcc, 0, v130\n\t"
    "v_and_or_b32 v93, v101, s44, v68\n\t"
    "v_xor_b32 v133, 23, v132\n\t"
    "v_lshlrev_b32 v110, v132, v136\n\t"
    "v_and_b32 v132, 7, v132\n\t"
    "v_lshl_or_b32 v110, v136, v133, v110\n\t"
    "v_lshl_add_u32 v132, v132, 1, v128\n\t"
    "ds_or_rtn_b32 v118, v92, v110\n\t"
    "ds_write_b16 v132, v106\n\t"
    "v_cndmask_b32 v130, v130, v129, vcc\n\t"
    "v_ffbl_b32 v131, v130\n\t"
    "v_and_b32 v134, 7, v131\n\t"
    "v_lshlrev_b32 v134, v134, s45\n\t"
    "v_xor_b32 v129, v129, v134\n\t"
    "v_xor_b32 v132, v131, v127\n\t"
    "v_bfi_b32 v130, v108, 0, v129\n\t"
    "v_cmp_eq_u32 vcc, 0, v130\n\t"
    "v_and_or_b32 v94, v108, s44, v68\n\t"
    "v_xor_b32 v133, 23, v132\n\t"
    "v_lshlrev_b32 v111, v132, v136\n\t"
    "v_and_b32 v132, 7, v132\n\t"
    "v_lshl_or_b32 v111, v136, v133, v111\n\t"
    "v_lshl_add_u32 v132, v132, 1, v128\n\t"
    "ds_or_rtn_b32 v119, v93, v111\n\t"
    "ds_write_b16 v132, v101\n\t"
    "v_cndmask_b32 v130, v130, v129, vcc\n\t"
    "v_ffbl_b32 v131, v130\n\t"
    "v_and_b32 v134, 7, v131\n\t"
    "v_lshlrev_b32 v134, v134, s45\n\t"
    "v_xor_b32 v129, v129, v134\n\t"
    "v_xor_b32 v132, v131, v127\n\t"
    "v_bfi_b32 v130, v100, 0, v129\n\t"
    "v_cmp_eq_u32 vcc, 0, v130\n\t"
    "v_and_or_b32 v95, v100, s44, v68\n\t"
    "v_xor_b32 v133, 23, v132\n\t"
    "v_lshlrev_b32 v112, v132, v136\n\t"
    "v_and_b32 v132, 7, v132\n\t"
    "v_lshl_or_b32 v112, v136, v133, v112\n\t"
    "v_lshl_add_u32 v132, v132, 1, v128\n\t"
    "ds_or_rtn_b32 v120, v94, v112\n\t"
    "ds_write_b16 v132, v108\n\t"
    "v_cndmask_b32 v130, v130, v129, vcc\n\t"
    "v_ffbl_b32 v131, v130\n\t"
    "v_and_b32 v134, 7, v131\n\t"
    "v_lshlrev_b32 v134, v134, s45\n\t"
    "v_xor_b32 v129, v129, v134\n\t"
    "v_xor_b32 v132, v131, v127\n\t"
    "v_bfi_b32 v130, v102, 0, v129\n\t"
    "v_cmp_eq_u32 vcc, 0, v130\n\t"
    "v_and_or_b32 v96, v102, s44, v68\n\t"
    "v_xor_b32 v133, 23, v132\n\t"
    "v_lshlrev_b32 v113, v132, v136\n\t"
    "v_and_b32 v132, 7, v132\n\t"
    "v_lshl_or_b32 v113, v136, v133, v113\n\t"
    "v_lshl_add_u32 v132, v132, 1, v128\n\t"
    "ds_or_rtn_b32 v121, v95, v113\n\t"
    "ds_write_b16 v132, v100\n\t"
    "v_cndmask_b32 v130, v130, v129, vcc\n\t"
    "v_ffbl_b32 v131, v130\n\t"
    "v_and_b32 v134, 7, v131\n\t"
    "v_lshlrev_b32 v134, v134, s45\n\t"
    "v_xor_b32 v129, v129, v134\n\t"
    "v_xor_b32 v132, v131, v127\n\t"
    "s_waitcnt lgkmcnt(7)\n\t"
    "v_and_b32 v118, v118, v110\n\t"
    "v_perm_b32 v118, v118, v118, s46\n\t"
    "ds_or_b32 v92, v118\n\t"
    "v_bfi_b32 v130, v104, 0, v129\n\t"
    "v_cmp_eq_u32 vcc, 0, v130\n\t"
    "v_and_or_b32 v97, v104, s44, v68\n\t"
    "v_xor_b32 v133, 23, v132\n\t"
    "v_lshlrev_b32 v114, v132, v136\n\t"
    "v_and_b32 v132, 7, v132\n\t"
    "v_lshl_or_b32 v114, v136, v133, v114\n\t"
    "v_lshl_add_u32 v132, v132, 1, v128\n\t"
    "ds_or_rtn_b32 v122, v96, v114\n\t"
    "ds_write_b16 v132, v102\n\t"
    "v_cndmask_b32 v130, v130, v129, vcc\n\t"
    "v_ffbl_b32 v131, v130\n\t"
    "v_and_b32 v134, 7, v131\n\t"
    "v_lshlrev_b32 v134, v134, s45\n\t"
    "v_xor_b32 v129, v129, v134\n\t"
    "v_xor_b32 v132, v131, v127\n\t"
    "s_waitcnt lgkmcnt(8)\n\t"
    "v_and_b32 v119, v119, v111\n\t"
    "v_perm_b32 v119, v119, v119, s46\n\t"
    "ds_or_b32 v93, v119\n\t"
    "v_bfi_b32 v130, v103, 0, v129\n\t"
    "v_cmp_eq_u32 vcc, 0, v130\n\t"
    "v_and_or_b32 v98, v103, s44, v68\n\t"
    "v_xor_b32 v133, 23, v132\n\t"
    "v_lshlrev_b32 v115, v132, v136\n\t"
    "v_and_b32 v132, 7, v132\n\t"
    "v_lshl_or_b32 v115, v136, v133, v115\n\t"
    "v_lshl_add_u32 v132, v132, 1, v128\n\t"
    "ds_or_rtn_b32 v123, v97, v115\n\t"
    "ds_write_b16 v132, v104\n\t"
    "v_cndmask_b32 v130, v130, v129, vcc\n\t"
    "v_ffbl_b32 v131, v130\n\t"
    "v_and_b32 v134, 7, v131\n\t"
    "v_lshlrev_b32 v134, v134, s45\n\t"
    "v_xor_b32 v129, v129, v134\n\t"
    "v_xor_b32 v132, v131, v127\n\t"
    "s_waitcnt lgkmcnt(9)\n\t"
    "v_and_b32 v120, v120, v112\n\t"
    "v_perm_b32 v120, v120, v120, s46\n\t"
    "ds_or_b32 v94, v120\n\t"
    "v_bfi_b32 v130, v107, 0, v129\n\t"
    "v_cmp_eq_u32 vcc, 0, v130\n\t"
    "v_and_or_b32 v99, v107, s44, v68\n\t"
    "v_xor_b32 v133, 23, v132\n\t"
    "v_lshlrev_b32 v116, v132, v136\n\t"
    "v_and_b32 v132, 7, v132\n\t"
    "v_lshl_or_b32 v116, v136, v133, v116\n\t"
    "v_lshl_add_u32 v132, v132, 1, v128\n\t"
    "ds_or_rtn_b32 v124, v98, v116\n\t"
    "ds_write_b16 v132, v103\n\t"
    "v_cndmask_b32 v130, v130, v129, vcc\n\t"
    "v_ffbl_b32 v131, v130\n\t"
    "v_xor_b32 v132, v131, v127\n\t"
    "s_waitcnt lgkmcnt(10)\n\t"
    "v_and_b32 v121, v121, v113\n\t"
    "v_perm_b32 v121, v121, v121, s46\n\t"
    "ds_or_b32 v95, v121\n\t"
    "v_xor_b32 v133, 23, v132\n\t"
    "v_lshlrev_b32 v117, v132, v136\n\t"
    "v_and_b32 v132, 7, v132\n\t"
    "v_lshl_or_b32 v117, v136, v133, v117\n\t"
    "v_lshl_add_u32 v132, v132, 1, v128\n\t"
    "ds_or_rtn_b32 v125, v99, v117\n\t"
    "ds_write_b16 v132, v107\n\t"
    "s_waitcnt lgkmcnt(10)\n\t"
    "v_and_b32 v122, v122, v114\n\t"
    "v_perm_b32 v122, v122, v122, s46\n\t"
    "ds_or_b32 v96, v122\n\t"
    "ds_read_b128 v[72:75], v128\n\t"
    "v_and_or_b32 v100, v76, s44, v68\n\t"
    "ds_read_b32 v100, v100\n\t"
    "v_and_or_b32 v101, v77, s44, v68\n\t"
    "ds_read_b32 v101, v101\n\t"
    "v_and_or_b32 v102, v78, s44, v68\n\t"
    "ds_read_b32 v102, v102\n\t"
    "v_and_or_b32 v103, v79, s44, v68\n\t"
    "ds_read_b32 v103, v103\n\t"
    "v_and_or_b32 v104, v80, s44, v68\n\t"
    "ds_read_b32 v104, v104\n\t"
    "v_and_or_b32 v105, v81, s44, v68\n\t"
    "ds_read_b32 v105, v105\n\t"
    "v_and_or_b32 v106, v82, s44, v68\n\t"
    "ds_read_b32 v106, v106\n\t"
    "v_and_or_b32 v107, v83, s44, v68\n\t"
    "ds_read_b32 v107, v107\n\t"
    "s_cmp_lg_u32 %14, 0\n\t"
    "s_cbranch_scc1 L_ahe864_%=\n\t"
    "s_waitcnt vmcnt(0)\n\t"
    "buffer_store_dwordx4 v[148:151], v152, %12, s47 offen offset:1024 sc1\n\t"
    "L_ahe864_%=:\n\t"
    "s_cmp_lg_u32 %14, 0\n\t"
    "s_cbranch_scc1 L_ahe869_%=\n\t"
    "buffer_load_dwordx4 v[148:151], v152, %12, s47 offen sc1\n\t"
    "L_ahe869_%=:\n\t"
    "s_waitcnt lgkmcnt(11)\n\t"
    "v_and_b32 v123, v123, v115\n\t"
    "v_perm_b32 v123, v123, v123, s46\n\t"
    "ds_or_b32 v97, v123\n\t"
    "v_and_b32 v124, v124, v116\n\t"
    "v_perm_b32 v124, v124, v124, s46\n\t"
    "ds_or_b32 v98, v124\n\t"
    "v_and_b32 v125, v125, v117\n\t"
    "v_perm_b32 v125, v125, v125, s46\n\t"
    "ds_or_b32 v99, v125\n\t"
    "s_waitcnt lgkmcnt(11)\n\t"
    "buffer_store_dwordx4 v[72:75], v66, %12, s49 offen offset:192 sc1\n\t"
    "v_mov_b32 v129, 0xffff0000\n\t"
    "s_waitcnt lgkmcnt(7)\n\t"
    "v_perm_b32 v100, v100, v76, v126\n\t"
    "v_perm_b32 v101, v101, v77, v126\n\t"
    "v_perm_b32 v102, v102, v78, v126\n\t"
    "v_perm_b32 v103, v103, v79, v126\n\t"
    "s_waitcnt lgkmcnt(3)\n\t"
    "v_perm_b32 v104, v104, v80, v126\n\t"
    "v_perm_b32 v105, v105, v81, v126\n\t"
    "v_perm_b32 v106, v106, v82, v126\n\t"
    "v_perm_b32 v107, v107, v83, v126\n\t"
    "v_max_u32 v108, v100, v101\n\t"
    "v_min_u32 v101, v100, v101\n\t"
    "v_max_u32 v100, v102, v103\n\t"
    "v_min_u32 v103, v102, v103\n\t"
    "v_max_u32 v102, v104, v105\n\t"
    "v_min_u32 v105, v104, v105\n\t"
    "v_max_u32 v104, v106, v107\n\t"
    "v_min_u32 v107, v106, v107\n\t"
    "v_max_u32 v106, v108, v100\n\t"
    "v_min_u32 v100, v108, v100\n\t"
    "v_max_u32 v108, v101, v103\n\t"
    "v_min_u32 v103, v101, v103\n\t"
    "v_max_u32 v101, v102, v104\n\t"
    "v_min_u32 v104, v102, v104\n\t"
    "v_max_u32 v102, v105, v107\n\t"
    "v_min_u32 v107, v105, v107\n\t"
    "v_bfi_b32 v130, v106, 0, v129\n\t"
    "v_cmp_eq_u32 vcc, 0, v130\n\t"
    "v_and_or_b32 v92, v106, s44, v68\n\t"
    "s_nop 0\n\t"
    "v_cndmask_b32 v130, v130, v129, vcc\n\t"
    "v_ffbl_b32 v131, v130\n\t"
    "v_and_b32 v134, 7, v131\n\t"
    "v_lshlrev_b32 v134, v134, s45\n\t"
    "v_xor_b32 v129, v129, v134\n\t"
    "v_xor_b32 v132, v131, v127\n\t"
    "v_bfi_b32 v130, v101, 0, v129\n\t"
    "v_cmp_eq_u32 vcc, 0, v130\n\t"
    "v_and_or_b32 v93, v101, s44, v68\n\t"
    "v_xor_b32 v133, 23, v132\n\t"
    "v_lshlrev_b32 v110, v132, v136\n\t"
    "v_and_b32 v132, 7, v132\n\t"
    "v_lshl_or_b32 v110, v136, v133, v110\n\t"
    "v_lshl_add_u32 v132, v132, 1, v128\n\t"
    "ds_or_rtn_b32 v118, v92, v110\n\t"
    "ds_write_b16 v132, v106\n\t"
    "v_cndmask_b32 v130, v130, v129, vcc\n\t"
    "v_ffbl_b32 v131, v130\n\t"
    "v_and_b32 v134, 7, v131\n\t"
    "v_lshlrev_b32 v134, v134, s45\n\t"
    "v_xor_b32 v129, v129, v134\n\t"
    "v_xor_b32 v132, v131, v127\n\t"
    "v_bfi_b32 v130, v108, 0, v129\n\t"
    "v_cmp_eq_u32 vcc, 0, v130\n\t"
    "v_and_or_b32 v94, v108, s44, v68\n\t"
    "v_xor_b32 v133, 23, v132\n\t"
    "v_lshlrev_b32 v111, v132, v136\n\t"
    "v_and_b32 v132, 7, v132\n\t"
    "v_lshl_or_b32 v111, v136, v133, v111\n\t"
    "v_lshl_add_u32 v132, v132, 1, v128\n\t"
    "ds_or_rtn_b32 v119, v93, v111\n\t"
    "ds_write_b16 v132, v101\n\t"
    "v_cndmask_b32 v130, v130, v129, vcc\n\t"
    "v_ffbl_b32 v131, v130\n\t"
    "v_and_b32 v134, 7, v131\n\t"
    "v_lshlrev_b32 v134, v134, s45\n\t"
    "v_xor_b32 v129, v129, v134\n\t"
    "v_xor_b32 v132, v131, v127\n\t"
    "v_bfi_b32 v130, v100, 0, v129\n\t"
    "v_cmp_eq_u32 vcc, 0, v130\n\t"
    "v_and_or_b32 v95, v100, s44, v68\n\t"
    "v_xor_b32 v133, 23, v132\n\t"
    "v_lshlrev_b32 v112, v132, v136\n\t"
    "v_and_b32 v132, 7, v132\n\t"
    "v_lshl_or_b32 v112, v136, v133, v112\n\t"
    "v_lshl_add_u32 v132, v132, 1, v128\n\t"
    "ds_or_rtn_b32 v120, v94, v112\n\t"
    "ds_write_b16 v132, v108\n\t"
    "v_cndmask_b32 v130, v130, v129, vcc\n\t"
    "v_ffbl_b32 v131, v130\n\t"
    "v_and_b32 v134, 7, v131\n\t"
    "v_lshlrev_b32 v134, v134, s45\n\t"
    "v_xor_b32 v129, v129, v134\n\t"
    "v_xor_b32 v132, v131, v127\n\t"
    "v_bfi_b32 v130, v102, 0, v129\n\t"
    "v_cmp_eq_u32 vcc, 0, v130\n\t"
    "v_and_or_b32 v96, v102, s44, v68\n\t"
    "v_xor_b32 v133, 23, v132\n\t"
    "v_lshlrev_b32 v113, v132, v136\n\t"
    "v_and_b32 v132, 7, v132\n\t"
    "v_lshl_or_b32 v113, v136, v133, v113\n\t"
    "v_lshl_add_u32 v132, v132, 1, v128\n\t"
    "ds_or_rtn_b32 v121, v95, v113\n\t"
    "ds_write_b16 v132, v100\n\t"
    "v_cndmask_b32 v130, v130, v129, vcc\n\t"
    "v_ffbl_b32 v131, v130\n\t"
    "v_and_b32 v134, 7, v131\n\t"
    "v_lshlrev_b32 v134, v134, s45\n\t"
    "v_xor_b32 v129, v129, v134\n\t"
    "v_xor_b32 v132, v131, v127\n\t"
    "s_waitcnt lgkmcnt(7)\n\t"
    "v_and_b32 v118, v118, v110\n\t"
    "v_perm_b32 v118, v118, v118, s46\n\t"
    "ds_or_b32 v92, v118\n\t"
    "s_waitcnt vmcnt(2)\n\t"
    "ds_write_b32 v71, v136 offset:4\n\t"
    "v_bfi_b32 v130, v104, 0, v129\n\t"
    "v_cmp_eq_u32 vcc, 0, v130\n\t"
    "v_and_or_b32 v97, v104, s44, v68\n\t"
    "v_xor_b32 v133, 23, v132\n\t"
    "v_lshlrev_b32 v114, v132, v136\n\t"
    "v_and_b32 v132, 7, v132\n\t"
    "v_lshl_or_b32 v114, v136, v133, v114\n\t"
    "v_lshl_add_u32 v132, v132, 1, v128\n\t"
    "ds_or_rtn_b32 v122, v96, v114\n\t"
    "ds_write_b16 v132, v102\n\t"
    "v_cndmask_b32 v130, v130, v129, vcc\n\t"
    "v_ffbl_b32 v131, v130\n\t"
    "v_and_b32 v134, 7, v131\n\t"
    "v_lshlrev_b32 v134, v134, s45\n\t"
    "v_xor_b32 v129, v129, v134\n\t"
    "v_xor_b32 v132, v131, v127\n\t"
    "s_waitcnt lgkmcnt(9)\n\t"
    "v_and_b32 v119, v119, v111\n\t"
    "v_perm_b32 v119, v119, v119, s46\n\t"
    "ds_or_b32 v93, v119\n\t"
    "v_bfi_b32 v130, v103, 0, v129\n\t"
    "v_cmp_eq_u32 vcc, 0, v130\n\t"
    "v_and_or_b32 v98, v103, s44, v68\n\t"
    "v_xor_b32 v133, 23, v132\n\t"
    "v_lshlrev_b32 v115, v132, v136\n\t"
    "v_and_b32 v132, 7, v132\n\t"
    "v_lshl_or_b32 v115, v136, v133, v115\n\t"
    "v_lshl_add_u32 v132, v132, 1, v128\n\t"
    "ds_or_rtn_b32 v123, v97, v115\n\t"
    "ds_write_b16 v132, v104\n\t"
    "v_cndmask_b32 v130, v130, v129, vcc\n\t"
    "v_ffbl_b32 v131, v130\n\t"
    "v_and_b32 v134, 7, v131\n\t"
    "v_lshlrev_b32 v134, v134, s45\n\t"
    "v_xor_b32 v129, v129, v134\n\t"
    "v_xor_b32 v132, v131, v127\n\t"
    "s_waitcnt lgkmcnt(10)\n\t"
    "v_and_b32 v120, v120, v112\n\t"
    "v_perm_b32 v120, v120, v120, s46\n\t"
    "ds_or_b32 v94, v120\n\t"
    "v_bfi_b32 v130, v107, 0, v129\n\t"
    "v_cmp_eq_u32 vcc, 0, v130\n\t"
    "v_and_or_b32 v99, v107, s44, v68\n\t"
    "v_xor_b32 v133, 23, v132\n\t"
    "v_lshlrev_b32 v116, v132, v136\n\t"
    "v_and_b32 v132, 7, v132\n\t"
    "v_lshl_or_b32 v116, v136, v133, v116\n\t"
    "v_lshl_add_u32 v132, v132, 1, v128\n\t"
    "ds_or_rtn_b32 v124, v98, v116\n\t"
    "ds_write_b16 v132, v103\n\t"
    "v_cndmask_b32 v130, v130, v129, vcc\n\t"
    "v_ffbl_b32 v131, v130\n\t"
    "v_xor_b32 v132, v131, v127\n\t"
    "s_waitcnt lgkmcnt(11)\n\t"
    "v_and_b32 v121, v121, v113\n\t"
    "v_perm_b32 v121, v121, v121, s46\n\t"
    "ds_or_b32 v95, v121\n\t"
    "v_xor_b32 v133, 23, v132\n\t"
    "v_lshlrev_b32 v117, v132, v136\n\t"
    "v_and_b32 v132, 7, v132\n\t"
    "v_lshl_or_b32 v117, v136, v133, v117\n\t"
    "v_lshl_add_u32 v132, v132, 1, v128\n\t"
    "ds_or_rtn_b32 v125, v99, v117\n\t"
    "ds_write_b16 v132, v107\n\t"
    "s_waitcnt lgkmcnt(10)\n\t"
    "v_and_b32 v122, v122, v114\n\t"
    "v_perm_b32 v122, v122, v122, s46\n\t"
    "ds_or_b32 v96, v122\n\t"
    "ds_read_b128 v[72:75], v128\n\t"
    "s_waitcnt lgkmcnt(3)\n\t"
    "v_and_b32 v123, v123, v115\n\t"
    "v_perm_b32 v123, v123, v123, s46\n\t"
    "ds_or_b32 v97, v123\n\t"
    "v_and_b32 v124, v124, v116\n\t"
    "v_perm_b32 v124, v124, v124, s46\n\t"
    "ds_or_b32 v98, v124\n\t"
    "v_and_b32 v125, v125, v117\n\t"
    "v_perm_b32 v125, v125, v125, s46\n\t"
    "ds_or_b32 v99, v125\n\t"
    "s_waitcnt lgkmcnt(3)\n\t"
    "buffer_store_dwordx4 v[72:75], v66, %12, s49 offen offset:256 sc1\n\t"
    "v_mov_b32 v134, 1036\n\t"
    "ds_write_b32 v134, v136\n\t"
    "s_cmp_lg_u32 %14, 0\n\t"
    "s_cbranch_scc1 L_ahsend_%=\n\t"
    "s_waitcnt vmcnt(0)\n\t"
    "buffer_store_dwordx4 v[148:151], v152, %12, s47 offen offset:1024 sc1\n\t"
    "s_setprio 0\n\t"
    "v_and_b32 v132, 0x70, v152\n\t"
    "v_lshlrev_b32 v132, 3, v132\n\t"
    "s_mov_b32 s51, 1048576\n\t"
    "L_ahs2_%=:\n\t"
    "s_sleep 8\n\t"
    "buffer_load_dword v71, v132, %12, s48 offen offset:1024 sc1\n\t"
    "buffer_load_dwordx4 v[148:151], v152, %12, s47 offen sc1\n\t"
    "s_sub_u32 s51, s51, 1\n\t"
    "s_cmp_eq_u32 s51, 0\n\t"
    "s_cbranch_scc1 L_ahsend_%=\n\t"
    "s_waitcnt vmcnt(0)\n\t"
    "buffer_store_dwordx4 v[148:151], v152, %12, s47 offen offset:1024 sc1\n\t"
    "v_cmp_gt_u32 vcc, 32, v71\n\t"
    "s_cbranch_vccnz L_ahs2_%=\n\t"
    "L_ahsend_%=:\n\t"
    "s_setprio 0\n\t"
    "s_and_b32 s46, %16, 3\n\t"
    "s_mul_i32 s49, s46, 1040\n\t"
    "s_add_u32 s49, s49, 2064\n\t"
    "s_lshl_b32 s46, s46, 3\n\t"
    "s_lshr_b32 s47, 1076763402, s46\n\t"
    "s_and_b32 s47, s47, 0xff\n\t"
    "s_lshl_b32 s48, s47, 16\n\t"
    "s_lshr_b32 s47, 773523968, s46\n\t"
    "s_and_b32 s47, s47, 0xff\n\t"
    "s_add_u32 s48, s48, s47\n\t"
    "s_lshr_b32 s47, %16, 2\n\t"
    "s_add_u32 s47, s47, 0x200\n\t"
    "s_cmp_eq_u32 s46, 0\n\t"
    "s_cselect_b32 s47, 0x100, s47\n\t"
    "s_add_u32 s48, s48, s47\n\t"
    "v_bfe_u32 v154, v147, 2, 2\n\t"
    "v_and_b32 v155, 3, v147\n\t"
    "v_mul_u32_u24 v154, 25, v154\n\t"
    "v_mad_u32_u24 v154, v155, 6, v154\n\t"
    "v_lshlrev_b32 v153, 2, v154\n\t"
    "s_and_b32 s53, s48, 0xff\n\t"
    "s_bfe_u32 s55, s48, 0x80008\n\t"
    "s_add_u32 s54, s53, s55\n\t"
    "s_add_u32 s55, s54, s55\n\t"
    "s_lshr_b32 s64, s48, 16\n\t"
    "s_mul_i32 s66, s53, 400\n\t"
    "buffer_load_dwordx3 v[128:130], v153, %0, s66 offen\n\t"
    "buffer_load_dwordx4 v[132:135], v153, %0, s66 offen offset:12\n\t"
    "s_mul_i32 s66, s54, 400\n\t"
    "buffer_load_dwordx3 v[136:138], v153, %0, s66 offen\n\t"
    "buffer_load_dwordx4 v[140:143], v153, %0, s66 offen offset:12\n\t"
    "v_cmp_eq_u32 s[62:63], 0, v155\n\t"
    "v_and_b32 v151, 48, v147\n\t"
    "v_or_b32 v155, 0x100000, v151\n\t"
    "v_cndmask_b32 v152, v155, v151, s[62:63]\n\t"
    "v_lshlrev_b32 v117, 20, v147\n\t"
    "v_add_u32 v117, 1024, v117\n\t"
    "v_mov_b32 v118, 1\n\t"
    "v_lshlrev_b32 v95, 4, v147\n\t"
    "v_add_u32 v95, 140288, v95\n\t"
    "s_call_b64 s[98:99], L_setup_%=\n\t"
    "L_await_%=:\n\t"
    "L_asp9_%=:\n\t"
    "ds_read_b32 v118, v117 offset:8\n\t"
    "ds_read_b32 v119, v117 offset:4\n\t"
    "s_waitcnt lgkmcnt(0)\n\t"
    "v_readfirstlane_b32 s46, v119\n\t"
    "s_nop 0\n\t"
    "s_cmp_ge_u32 s46, 4\n\t"
    "s_cbranch_scc1 L_ago9_%=\n\t"
    "s_sleep 1\n\t"
    "s_branch L_asp9_%=\n\t"
    "L_ago9_%=:\n\t"
    "v_readfirstlane_b32 s47, v118\n\t"
    "ds_read_b128 v[0:3], v95 offset:0\n\t"
    "ds_read_b128 v[4:7], v95 offset:1024\n\t"
    "ds_read_b128 v[8:11], v95 offset:2048\n\t"
    "ds_read_b128 v[12:15], v95 offset:3072\n\t"
    "ds_read_b128 v[16:19], v95 offset:4096\n\t"
    "ds_read_b128 v[20:23], v95 offset:5120\n\t"
    "ds_read_b128 v[24:27], v95 offset:6144\n\t"
    "ds_read_b128 v[28:31], v95 offset:7168\n\t"
    "ds_read_b128 v[32:35], v95 offset:8192\n\t"
    "ds_read_b128 v[36:39], v95 offset:9216\n\t"
    "ds_read_b128 v[40:43], v95 offset:10240\n\t"
    "ds_read_b128 v[44:47], v95 offset:11264\n\t"
    "ds_read_b128 v[48:51], v95 offset:12288\n\t"
    "ds_read_b128 v[52:55], v95 offset:13312\n\t"
    "ds_read_b128 v[56:59], v95 offset:14336\n\t"
    "ds_read_b128 v[60:63], v95 offset:15360\n\t"
    "ds_read_b128 v[64:67], v95 offset:16384\n\t"
    "ds_read_b128 v[68:71], v95 offset:17408\n\t"
    "ds_read_b128 v[72:75], v95 offset:18432\n\t"
    "ds_read_b128 v[76:79], v95 offset:19456\n\t"
    "ds_read_b128 v[80:83], v95 offset:20480\n\t"
    "ds_read_b128 v[84:87], v95 offset:21504\n\t"
    "ds_read_b128 v[88:91], v95 offset:22528\n\t"
    "s_waitcnt vmcnt(0)\n\t"
    "s_cmp_lg_u32 %16, 1\n\t"
    "s_cbranch_scc1 L_anh3_%=\n\t"
    "v_mov_b32 v119, 4\n\t"
    "ds_write_b32 v117, v119 offset:12\n\t"
    "s_lshl_b32 s65, %14, 12\n\t"
    "s_add_u32 s65, s65, 16777216\n\t"
    "s_waitcnt lgkmcnt(15)\n\t"
    "buffer_store_dwordx4 v[0:3], v127, %12, s65 offen offset:0 sc1\n\t"
    "buffer_store_dwordx4 v[4:7], v127, %12, s65 offen offset:1024 sc1\n\t"
    "buffer_store_dwordx4 v[8:11], v127, %12, s65 offen offset:2048 sc1\n\t"
    "buffer_store_dwordx4 v[12:15], v127, %12, s65 offen offset:3072 sc1\n\t"
    "L_anh3_%=:\n\t"
    "s_cmp_ge_u32 s47, 6\n\t"
    "s_cbranch_scc1 L_adok_%=\n\t"
    "L_awd_%=:\n\t"
    "L_asp10_%=:\n\t"
    "ds_read_b32 v119, v117 offset:8\n\t"
    "s_waitcnt lgkmcnt(0)\n\t"
    "v_readfirstlane_b32 s46, v119\n\t"
    "s_nop 0\n\t"
    "s_cmp_ge_u32 s46, 6\n\t"
    "s_cbranch_scc1 L_ago10_%=\n\t"
    "s_sleep 1\n\t"
    "s_branch L_asp10_%=\n\t"
    "L_ago10_%=:\n\t"
    "L_adok_%=:\n\t"
    "L_amain_%=:\n\t"
    "s_mov_b32 s68, 130\n\t"
    "v_mad_u32_u24 v128, v128, s68, v152\n\t"
    "v_and_b32 v128, -16, v128\n\t"
    "ds_read_b128 v[156:159], v128 offset:0\n\t"
    "ds_read_b128 v[160:163], v128 offset:64\n\t"
    "v_mad_u32_u24 v129, v129, s68, v151\n\t"
    "v_and_b32 v129, -16, v129\n\t"
    "ds_read_b128 v[164:167], v129 offset:0\n\t"
    "ds_read_b128 v[168:171], v129 offset:64\n\t"
    "v_mad_u32_u24 v130, v130, s68, v151\n\t"
    "v_and_b32 v130, -16, v130\n\t"
    "ds_read_b128 v[172:175], v130 offset:0\n\t"
    "ds_read_b128 v[176:179], v130 offset:64\n\t"
    "s_mul_i32 s66, s55, 400\n\t"
    "buffer_load_dwordx3 v[128:130], v153, %0, s66 offen\n\t"
    "v_mad_u32_u24 v132, v132, s68, v151\n\t"
    "v_and_b32 v132, -16, v132\n\t"
    "ds_read_b128 v[180:183], v132 offset:0\n\t"
    "ds_read_b128 v[184:187], v132 offset:64\n\t"
    "v_mad_u32_u24 v133, v133, s68, v151\n\t"
    "v_and_b32 v133, -16, v133\n\t"
    "ds_read_b128 v[188:191], v133 offset:0\n\t"
    "ds_read_b128 v[192:195], v133 offset:64\n\t"
    "v_mad_u32_u24 v134, v134, s68, v151\n\t"
    "v_and_b32 v134, -16, v134\n\t"
    "ds_read_b128 v[196:199], v134 offset:0\n\t"
    "ds_read_b128 v[200:203], v134 offset:64\n\t"
    "v_mad_u32_u24 v135, v135, s68, v151\n\t"
    "v_and_b32 v135, -16, v135\n\t"
    "ds_read_b128 v[204:207], v135 offset:0\n\t"
    "ds_read_b128 v[208:211], v135 offset:64\n\t"
    "v_mov_b32 v150, 1\n\t"
    "v_lshlrev_b32 v149, 20, v147\n\t"
    "v_add_u32 v149, s49, v149\n\t"
    "v_lshlrev_b32 v146, 2, v147\n\t"
    "s_mov_b32 s52, s53\n\t"
    "s_lshl_b32 s50, %14, 14\n\t"
    "s_add_u32 s50, s50, 27262976\n\t"
    "L_apnn_%=:\n\t"
    "s_mul_i32 s69, s52, 256\n\t"
    "s_add_u32 s69, s69, s50\n\t"
    "s_waitcnt lgkmcnt(8)\n\t"
    "v_mfma_f32_16x16x32_bf16 v[92:95], v[156:159], v[156:159], 0\n\t"
    "v_fmac_f32 v145, v108, v72\n\t"
    "v_fmac_f32 v144, v109, v73\n\t"
    "ds_add_rtn_u32 v148, v149, v150\n\t"
    "v_mfma_f32_16x16x32_bf16 v[92:95], v[160:163], v[160:163], v[92:95]\n\t"
    "v_fmac_f32 v145, v110, v74\n\t"
    "v_fmac_f32 v144, v111, v75\n\t"
    "buffer_load_dwordx4 v[132:135], v153, %0, s66 offen offset:12\n\t"
    "v_mfma_f32_16x16x32_bf16 v[96:99], v[164:167], v[164:167], 0\n\t"
    "v_fmac_f32 v145, v112, v76\n\t"
    "v_fmac_f32 v144, v113, v77\n\t"
    "v_mfma_f32_16x16x32_bf16 v[96:99], v[168:171], v[168:171], v[96:99]\n\t"
    "v_fmac_f32 v145, v114, v78\n\t"
    "v_fmac_f32 v144, v115, v79\n\t"
    "v_mfma_f32_16x16x32_bf16 v[100:103], v[164:167], v[172:175], 0\n\t"
    "v_fmac_f32 v145, v116, v80\n\t"
    "v_fmac_f32 v144, v117, v81\n\t"
    "v_mfma_f32_16x16x32_bf16 v[100:103], v[168:171], v[176:179], v[100:103]\n\t"
    "v_fmac_f32 v145, v118, v82\n\t"
    "v_fmac_f32 v144, v119, v83\n\t"
    "s_waitcnt vmcnt(4)\n\t"
    "s_cmp_ge_u32 s55, s64\n\t"
    "s_cbranch_scc1 L_ahk00_%=\n\t"
    "L_ahb00_%=:\n\t"
    "v_mfma_f32_16x16x32_bf16 v[104:107], v[172:175], v[172:175], 0\n\t"
    "v_mad_u32_u24 v136, v136, s68, v152\n\t"
    "v_and_b32 v136, -16, v136\n\t"
    "v_mfma_f32_16x16x32_bf16 v[104:107], v[176:179], v[176:179], v[104:107]\n\t"
    "v_add_f32 v154, v144, v145\n\t"
    "buffer_store_dword v154, v146, %12, s69 offen sc1\n\t"
    "v_mul_f32 v144, v92, v84\n\t"
    "v_mfma_f32_16x16x32_bf16 v[108:111], v[156:159], v[164:167], 0\n\t"
    "v_mul_f32 v145, v96, v0\n\t"
    "v_fmac_f32 v144, v97, v1\n\t"
    "v_mfma_f32_16x16x32_bf16 v[108:111], v[160:163], v[168:171], v[108:111]\n\t"
    "v_fmac_f32 v145, v98, v2\n\t"
    "v_fmac_f32 v144, v99, v3\n\t"
    "s_waitcnt lgkmcnt(7)\n\t"
    "v_mfma_f32_16x16x32_bf16 v[112:115], v[164:167], v[180:183], 0\n\t"
    "v_fmac_f32 v145, v100, v4\n\t"
    "v_fmac_f32 v144, v101, v5\n\t"
    "v_mfma_f32_16x16x32_bf16 v[112:115], v[168:171], v[184:187], v[112:115]\n\t"
    "v_fmac_f32 v145, v102, v6\n\t"
    "v_fmac_f32 v144, v103, v7\n\t"
    "v_mfma_f32_16x16x32_bf16 v[116:119], v[172:175], v[180:183], 0\n\t"
    "v_fmac_f32 v145, v104, v8\n\t"
    "v_fmac_f32 v144, v105, v9\n\t"
    "v_mfma_f32_16x16x32_bf16 v[116:119], v[176:179], v[184:187], v[116:119]\n\t"
    "v_fmac_f32 v145, v106, v10\n\t"
    "v_fmac_f32 v144, v107, v11\n\t"
    "ds_read_b128 v[212:215], v136 offset:0\n\t"
    "ds_read_b128 v[216:219], v136 offset:64\n\t"
    "v_mfma_f32_16x16x32_bf16 v[92:95], v[180:183], v[180:183], 0\n\t"
    "v_mad_u32_u24 v137, v137, s68, v151\n\t"
    "v_and_b32 v137, -16, v137\n\t"
    "v_mfma_f32_16x16x32_bf16 v[92:95], v[184:187], v[184:187], v[92:95]\n\t"
    "v_fmac_f32 v145, v108, v85\n\t"
    "v_mad_u32_u24 v138, v138, s68, v151\n\t"
    "v_mfma_f32_16x16x32_bf16 v[96:99], v[156:159], v[172:175], 0\n\t"
    "v_fmac_f32 v144, v112, v12\n\t"
    "v_fmac_f32 v145, v113, v13\n\t"
    "v_mfma_f32_16x16x32_bf16 v[96:99], v[160:163], v[176:179], v[96:99]\n\t"
    "v_fmac_f32 v144, v114, v14\n\t"
    "v_fmac_f32 v145, v115, v15\n\t"
    "s_waitcnt lgkmcnt(7)\n\t"
    "v_mfma_f32_16x16x32_bf16 v[100:103], v[164:167], v[188:191], 0\n\t"
    "v_and_b32 v138, -16, v138\n\t"
    "v_fmac_f32 v144, v116, v16\n\t"
    "v_mfma_f32_16x16x32_bf16 v[100:103], v[168:171], v[192:195], v[100:103]\n\t"
    "v_fmac_f32 v145, v117, v17\n\t"
    "v_fmac_f32 v144, v118, v18\n\t"
    "v_mfma_f32_16x16x32_bf16 v[104:107], v[172:175], v[188:191], 0\n\t"
    "v_fmac_f32 v145, v119, v19\n\t"
    "v_fmac_f32 v144, v92, v20\n\t"
    "v_mfma_f32_16x16x32_bf16 v[104:107], v[176:179], v[192:195], v[104:107]\n\t"
    "v_fmac_f32 v145, v93, v21\n\t"
    "v_fmac_f32 v144, v94, v22\n\t"
    "s_waitcnt lgkmcnt(5)\n\t"
    "v_mfma_f32_16x16x32_bf16 v[108:111], v[164:167], v[196:199], 0\n\t"
    "v_fmac_f32 v145, v95, v23\n\t"
    "v_fmac_f32 v144, v96, v86\n\t"
    "v_mfma_f32_16x16x32_bf16 v[108:111], v[168:171], v[200:203], v[108:111]\n\t"
    "v_mfma_f32_16x16x32_bf16 v[112:115], v[156:159], v[180:183], 0\n\t"
    "v_fmac_f32 v145, v100, v24\n\t"
    "v_fmac_f32 v144, v101, v25\n\t"
    "v_mfma_f32_16x16x32_bf16 v[112:115], v[160:163], v[184:187], v[112:115]\n\t"
    "v_fmac_f32 v145, v102, v26\n\t"
    "v_fmac_f32 v144, v103, v27\n\t"
    "s_waitcnt lgkmcnt(2)\n\t"
    "v_mfma_f32_16x16x32_bf16 v[116:119], v[164:167], v[204:207], 0\n\t"
    "v_readfirstlane_b32 s60, v148\n\t"
    "v_fmac_f32 v145, v104, v28\n\t"
    "v_mfma_f32_16x16x32_bf16 v[116:119], v[168:171], v[208:211], v[116:119]\n\t"
    "v_fmac_f32 v144, v105, v29\n\t"
    "v_fmac_f32 v145, v106, v30\n\t"
    "ds_read_b128 v[164:167], v137 offset:0\n\t"
    "ds_read_b128 v[168:171], v137 offset:64\n\t"
    "v_mfma_f32_16x16x32_bf16 v[92:95], v[172:175], v[196:199], 0\n\t"
    "v_fmac_f32 v144, v107, v31\n\t"
    "v_fmac_f32 v145, v108, v32\n\t"
    "v_mfma_f32_16x16x32_bf16 v[92:95], v[176:179], v[200:203], v[92:95]\n\t"
    "v_fmac_f32 v144, v109, v33\n\t"
    "v_fmac_f32 v145, v110, v34\n\t"
    "s_waitcnt vmcnt(4)\n\t"
    "s_cmp_ge_u32 s55, s64\n\t"
    "s_cbranch_scc1 L_ahk10_%=\n\t"
    "L_ahb10_%=:\n\t"
    "v_mfma_f32_16x16x32_bf16 v[96:99], v[172:175], v[204:207], 0\n\t"
    "v_mad_u32_u24 v140, v140, s68, v151\n\t"
    "v_and_b32 v140, -16, v140\n\t"
    "v_mfma_f32_16x16x32_bf16 v[96:99], v[176:179], v[208:211], v[96:99]\n\t"
    "v_fmac_f32 v144, v111, v35\n\t"
    "v_fmac_f32 v145, v112, v87\n\t"
    "ds_read_b128 v[172:175], v138 offset:0\n\t"
    "ds_read_b128 v[176:179], v138 offset:64\n\t"
    "s_mul_i32 s66, s60, 400\n\t"
    "v_mfma_f32_16x16x32_bf16 v[100:103], v[156:159], v[188:191], 0\n\t"
    "v_fmac_f32 v144, v116, v36\n\t"
    "v_fmac_f32 v145, v117, v37\n\t"
    "v_mfma_f32_16x16x32_bf16 v[100:103], v[160:163], v[192:195], v[100:103]\n\t"
    "v_fmac_f32 v144, v118, v38\n\t"
    "v_fmac_f32 v145, v119, v39\n\t"
    "buffer_load_dwordx3 v[136:138], v153, %0, s66 offen\n\t"
    "v_mfma_f32_16x16x32_bf16 v[104:107], v[180:183], v[188:191], 0\n\t"
    "v_mad_u32_u24 v141, v141, s68, v151\n\t"
    "v_and_b32 v141, -16, v141\n\t"
    "v_mfma_f32_16x16x32_bf16 v[104:107], v[184:187], v[192:195], v[104:107]\n\t"
    "v_fmac_f32 v144, v92, v40\n\t"
    "v_fmac_f32 v145, v93, v41\n\t"
    "v_mfma_f32_16x16x32_bf16 v[108:111], v[180:183], v[196:199], 0\n\t"
    "v_fmac_f32 v144, v94, v42\n\t"
    "v_fmac_f32 v145, v95, v43\n\t"
    "v_mfma_f32_16x16x32_bf16 v[108:111], v[184:187], v[200:203], v[108:111]\n\t"
    "v_fmac_f32 v144, v96, v44\n\t"
    "v_fmac_f32 v145, v97, v45\n\t"
    "v_mfma_f32_16x16x32_bf16 v[112:115], v[180:183], v[204:207], 0\n\t"
    "v_fmac_f32 v144, v98, v46\n\t"
    "v_fmac_f32 v145, v99, v47\n\t"
    "v_mfma_f32_16x16x32_bf16 v[112:115], v[184:187], v[208:211], v[112:115]\n\t"
    "v_fmac_f32 v144, v100, v88\n\t"
    "v_mad_u32_u24 v142, v142, s68, v151\n\t"
    "ds_read_b128 v[180:183], v140 offset:0\n\t"
    "ds_read_b128 v[184:187], v140 offset:64\n\t"
    "v_mfma_f32_16x16x32_bf16 v[116:119], v[156:159], v[196:199], 0\n\t"
    "v_and_b32 v142, -16, v142\n\t"
    "v_fmac_f32 v145, v104, v48\n\t"
    "v_mfma_f32_16x16x32_bf16 v[116:119], v[160:163], v[200:203], v[116:119]\n\t"
    "v_fmac_f32 v144, v105, v49\n\t"
    "v_fmac_f32 v145, v106, v50\n\t"
    "v_mfma_f32_16x16x32_bf16 v[92:95], v[188:191], v[188:191], 0\n\t"
    "v_fmac_f32 v144, v107, v51\n\t"
    "v_mad_u32_u24 v143, v143, s68, v151\n\t"
    "v_mfma_f32_16x16x32_bf16 v[92:95], v[192:195], v[192:195], v[92:95]\n\t"
    "v_and_b32 v143, -16, v143\n\t"
    "v_fmac_f32 v145, v108, v52\n\t"
    "v_mfma_f32_16x16x32_bf16 v[96:99], v[188:191], v[196:199], 0\n\t"
    "v_fmac_f32 v144, v109, v53\n\t"
    "v_fmac_f32 v145, v110, v54\n\t"
    "v_mfma_f32_16x16x32_bf16 v[96:99], v[192:195], v[200:203], v[96:99]\n\t"
    "v_fmac_f32 v144, v111, v55\n\t"
    "v_fmac_f32 v145, v112, v56\n\t"
    "v_mfma_f32_16x16x32_bf16 v[100:103], v[188:191], v[204:207], 0\n\t"
    "v_fmac_f32 v144, v113, v57\n\t"
    "v_fmac_f32 v145, v114, v58\n\t"
    "v_mfma_f32_16x16x32_bf16 v[100:103], v[192:195], v[208:211], v[100:103]\n\t"
    "v_fmac_f32 v144, v115, v59\n\t"
    "v_fmac_f32 v145, v116, v89\n\t"
    "ds_read_b128 v[188:191], v141 offset:0\n\t"
    "ds_read_b128 v[192:195], v141 offset:64\n\t"
    "v_mfma_f32_16x16x32_bf16 v[104:107], v[156:159], v[204:207], 0\n\t"
    "v_fmac_f32 v144, v92, v60\n\t"
    "v_fmac_f32 v145, v93, v61\n\t"
    "v_mfma_f32_16x16x32_bf16 v[104:107], v[160:163], v[208:211], v[104:107]\n\t"
    "v_fmac_f32 v144, v94, v62\n\t"
    "v_fmac_f32 v145, v95, v63\n\t"
    "v_mfma_f32_16x16x32_bf16 v[108:111], v[196:199], v[196:199], 0\n\t"
    "v_fmac_f32 v144, v96, v64\n\t"
    "v_fmac_f32 v145, v97, v65\n\t"
    "v_mfma_f32_16x16x32_bf16 v[108:111], v[200:203], v[200:203], v[108:111]\n\t"
    "v_fmac_f32 v144, v98, v66\n\t"
    "v_fmac_f32 v145, v99, v67\n\t"
    "v_mfma_f32_16x16x32_bf16 v[112:115], v[196:199], v[204:207], 0\n\t"
    "v_fmac_f32 v144, v100, v68\n\t"
    "v_fmac_f32 v145, v101, v69\n\t"
    "v_mfma_f32_16x16x32_bf16 v[112:115], v[200:203], v[208:211], v[112:115]\n\t"
    "v_fmac_f32 v144, v102, v70\n\t"
    "v_fmac_f32 v145, v103, v71\n\t"
    "ds_read_b128 v[196:199], v142 offset:0\n\t"
    "ds_read_b128 v[200:203], v142 offset:64\n\t"
    "v_mfma_f32_16x16x32_bf16 v[116:119], v[204:207], v[204:207], 0\n\t"
    "v_fmac_f32 v144, v104, v90\n\t"
    "v_mfma_f32_16x16x32_bf16 v[116:119], v[208:211], v[208:211], v[116:119]\n\t"
    "ds_read_b128 v[204:207], v143 offset:0\n\t"
    "ds_read_b128 v[208:211], v143 offset:64\n\t"
    "s_mov_b32 s52, s53\n\t"
    "s_mov_b32 s53, s54\n\t"
    "s_mov_b32 s54, s55\n\t"
    "s_mov_b32 s55, s60\n\t"
    "s_cmp_lt_u32 s53, s64\n\t"
    "s_cbranch_scc0 L_adone_%=\n\t"
    "s_mul_i32 s69, s52, 256\n\t"
    "s_add_u32 s69, s69, s50\n\t"
    "s_waitcnt lgkmcnt(8)\n\t"
    "v_mfma_f32_16x16x32_bf16 v[92:95], v[212:215], v[212:215], 0\n\t"
    "v_fmac_f32 v145, v108, v72\n\t"
    "v_fmac_f32 v144, v109, v73\n\t"
    "ds_add_rtn_u32 v148, v149, v150\n\t"
    "v_mfma_f32_16x16x32_bf16 v[92:95], v[216:219], v[216:219], v[92:95]\n\t"
    "v_fmac_f32 v145, v110, v74\n\t"
    "v_fmac_f32 v144, v111, v75\n\t"
    "buffer_load_dwordx4 v[140:143], v153, %0, s66 offen offset:12\n\t"
    "v_mfma_f32_16x16x32_bf16 v[96:99], v[164:167], v[164:167], 0\n\t"
    "v_fmac_f32 v145, v112, v76\n\t"
    "v_fmac_f32 v144, v113, v77\n\t"
    "v_mfma_f32_16x16x32_bf16 v[96:99], v[168:171], v[168:171], v[96:99]\n\t"
    "v_fmac_f32 v145, v114, v78\n\t"
    "v_fmac_f32 v144, v115, v79\n\t"
    "v_mfma_f32_16x16x32_bf16 v[100:103], v[164:167], v[172:175], 0\n\t"
    "v_fmac_f32 v145, v116, v80\n\t"
    "v_fmac_f32 v144, v117, v81\n\t"
    "v_mfma_f32_16x16x32_bf16 v[100:103], v[168:171], v[176:179], v[100:103]\n\t"
    "v_fmac_f32 v145, v118, v82\n\t"
    "v_fmac_f32 v144, v119, v83\n\t"
    "s_waitcnt vmcnt(4)\n\t"
    "s_cmp_ge_u32 s55, s64\n\t"
    "s_cbranch_scc1 L_ahk01_%=\n\t"
    "L_ahb01_%=:\n\t"
    "v_mfma_f32_16x16x32_bf16 v[104:107], v[172:175], v[172:175], 0\n\t"
    "v_mad_u32_u24 v128, v128, s68, v152\n\t"
    "v_and_b32 v128, -16, v128\n\t"
    "v_mfma_f32_16x16x32_bf16 v[104:107], v[176:179], v[176:179], v[104:107]\n\t"
    "v_add_f32 v154, v144, v145\n\t"
    "buffer_store_dword v154, v146, %12, s69 offen sc1\n\t"
    "v_mul_f32 v144, v92, v84\n\t"
    "v_mfma_f32_16x16x32_bf16 v[108:111], v[212:215], v[164:167], 0\n\t"
    "v_mul_f32 v145, v96, v0\n\t"
    "v_fmac_f32 v144, v97, v1\n\t"
    "v_mfma_f32_16x16x32_bf16 v[108:111], v[216:219], v[168:171], v[108:111]\n\t"
    "v_fmac_f32 v145, v98, v2\n\t"
    "v_fmac_f32 v144, v99, v3\n\t"
    "s_waitcnt lgkmcnt(7)\n\t"
    "v_mfma_f32_16x16x32_bf16 v[112:115], v[164:167], v[180:183], 0\n\t"
    "v_fmac_f32 v145, v100, v4\n\t"
    "v_fmac_f32 v144, v101, v5\n\t"
    "v_mfma_f32_16x16x32_bf16 v[112:115], v[168:171], v[184:187], v[112:115]\n\t"
    "v_fmac_f32 v145, v102, v6\n\t"
    "v_fmac_f32 v144, v103, v7\n\t"
    "v_mfma_f32_16x16x32_bf16 v[116:119], v[172:175], v[180:183], 0\n\t"
    "v_fmac_f32 v145, v104, v8\n\t"
    "v_fmac_f32 v144, v105, v9\n\t"
    "v_mfma_f32_16x16x32_bf16 v[116:119], v[176:179], v[184:187], v[116:119]\n\t"
    "v_fmac_f32 v145, v106, v10\n\t"
    "v_fmac_f32 v144, v107, v11\n\t"
    "ds_read_b128 v[156:159], v128 offset:0\n\t"
    "ds_read_b128 v[160:163], v128 offset:64\n\t"
    "v_mfma_f32_16x16x32_bf16 v[92:95], v[180:183], v[180:183], 0\n\t"
    "v_mad_u32_u24 v129, v129, s68, v151\n\t"
    "v_and_b32 v129, -16, v129\n\t"
    "v_mfma_f32_16x16x32_bf16 v[92:95], v[184:187], v[184:187], v[92:95]\n\t"
    "v_fmac_f32 v145, v108, v85\n\t"
    "v_mad_u32_u24 v130, v130, s68, v151\n\t"
    "v_mfma_f32_16x16x32_bf16 v[96:99], v[212:215], v[172:175], 0\n\t"
    "v_fmac_f32 v144, v112, v12\n\t"
    "v_fmac_f32 v145, v113, v13\n\t"
    "v_mfma_f32_16x16x32_bf16 v[96:99], v[216:219], v[176:179], v[96:99]\n\t"
    "v_fmac_f32 v144, v114, v14\n\t"
    "v_fmac_f32 v145, v115, v15\n\t"
    "s_waitcnt lgkmcnt(7)\n\t"
    "v_mfma_f32_16x16x32_bf16 v[100:103], v[164:167], v[188:191], 0\n\t"
    "v_and_b32 v130, -16, v130\n\t"
    "v_fmac_f32 v144, v116, v16\n\t"
    "v_mfma_f32_16x16x32_bf16 v[100:103], v[168:171], v[192:195], v[100:103]\n\t"
    "v_fmac_f32 v145, v117, v17\n\t"
    "v_fmac_f32 v144, v118, v18\n\t"
    "v_mfma_f32_16x16x32_bf16 v[104:107], v[172:175], v[188:191], 0\n\t"
    "v_fmac_f32 v145, v119, v19\n\t"
    "v_fmac_f32 v144, v92, v20\n\t"
    "v_mfma_f32_16x16x32_bf16 v[104:107], v[176:179], v[192:195], v[104:107]\n\t"
    "v_fmac_f32 v145, v93, v21\n\t"
    "v_fmac_f32 v144, v94, v22\n\t"
    "s_waitcnt lgkmcnt(5)\n\t"
    "v_mfma_f32_16x16x32_bf16 v[108:111], v[164:167], v[196:199], 0\n\t"
    "v_fmac_f32 v145, v95, v23\n\t"
    "v_fmac_f32 v144, v96, v86\n\t"
    "v_mfma_f32_16x16x32_bf16 v[108:111], v[168:171], v[200:203], v[108:111]\n\t"
    "v_mfma_f32_16x16x32_bf16 v[112:115], v[212:215], v[180:183], 0\n\t"
    "v_fmac_f32 v145, v100, v24\n\t"
    "v_fmac_f32 v144, v101, v25\n\t"
    "v_mfma_f32_16x16x32_bf16 v[112:115], v[216:219], v[184:187], v[112:115]\n\t"
    "v_fmac_f32 v145, v102, v26\n\t"
    "v_fmac_f32 v144, v103, v27\n\t"
    "s_waitcnt lgkmcnt(2)\n\t"
    "v_mfma_f32_16x16x32_bf16 v[116:119], v[164:167], v[204:207], 0\n\t"
    "v_readfirstlane_b32 s60, v148\n\t"
    "v_fmac_f32 v145, v104, v28\n\t"
    "v_mfma_f32_16x16x32_bf16 v[116:119], v[168:171], v[208:211], v[116:119]\n\t"
    "v_fmac_f32 v144, v105, v29\n\t"
    "v_fmac_f32 v145, v106, v30\n\t"
    "ds_read_b128 v[164:167], v129 offset:0\n\t"
    "ds_read_b128 v[168:171], v129 offset:64\n\t"
    "v_mfma_f32_16x16x32_bf16 v[92:95], v[172:175], v[196:199], 0\n\t"
    "v_fmac_f32 v144, v107, v31\n\t"
    "v_fmac_f32 v145, v108, v32\n\t"
    "v_mfma_f32_16x16x32_bf16 v[92:95], v[176:179], v[200:203], v[92:95]\n\t"
    "v_fmac_f32 v144, v109, v33\n\t"
    "v_fmac_f32 v145, v110, v34\n\t"
    "s_waitcnt vmcnt(4)\n\t"
    "s_cmp_ge_u32 s55, s64\n\t"
    "s_cbranch_scc1 L_ahk11_%=\n\t"
    "L_ahb11_%=:\n\t"
    "v_mfma_f32_16x16x32_bf16 v[96:99], v[172:175], v[204:207], 0\n\t"
    "v_mad_u32_u24 v132, v132, s68, v151\n\t"
    "v_and_b32 v132, -16, v132\n\t"
    "v_mfma_f32_16x16x32_bf16 v[96:99], v[176:179], v[208:211], v[96:99]\n\t"
    "v_fmac_f32 v144, v111, v35\n\t"
    "v_fmac_f32 v145, v112, v87\n\t"
    "ds_read_b128 v[172:175], v130 offset:0\n\t"
    "ds_read_b128 v[176:179], v130 offset:64\n\t"
    "s_mul_i32 s66, s60, 400\n\t"
    "v_mfma_f32_16x16x32_bf16 v[100:103], v[212:215], v[188:191], 0\n\t"
    "v_fmac_f32 v144, v116, v36\n\t"
    "v_fmac_f32 v145, v117, v37\n\t"
    "v_mfma_f32_16x16x32_bf16 v[100:103], v[216:219], v[192:195], v[100:103]\n\t"
    "v_fmac_f32 v144, v118, v38\n\t"
    "v_fmac_f32 v145, v119, v39\n\t"
    "buffer_load_dwordx3 v[128:130], v153, %0, s66 offen\n\t"
    "v_mfma_f32_16x16x32_bf16 v[104:107], v[180:183], v[188:191], 0\n\t"
    "v_mad_u32_u24 v133, v133, s68, v151\n\t"
    "v_and_b32 v133, -16, v133\n\t"
    "v_mfma_f32_16x16x32_bf16 v[104:107], v[184:187], v[192:195], v[104:107]\n\t"
    "v_fmac_f32 v144, v92, v40\n\t"
    "v_fmac_f32 v145, v93, v41\n\t"
    "v_mfma_f32_16x16x32_bf16 v[108:111], v[180:183], v[196:199], 0\n\t"
    "v_fmac_f32 v144, v94, v42\n\t"
    "v_fmac_f32 v145, v95, v43\n\t"
    "v_mfma_f32_16x16x32_bf16 v[108:111], v[184:187], v[200:203], v[108:111]\n\t"
    "v_fmac_f32 v144, v96, v44\n\t"
    "v_fmac_f32 v145, v97, v45\n\t"
    "v_mfma_f32_16x16x32_bf16 v[112:115], v[180:183], v[204:207], 0\n\t"
    "v_fmac_f32 v144, v98, v46\n\t"
    "v_fmac_f32 v145, v99, v47\n\t"
    "v_mfma_f32_16x16x32_bf16 v[112:115], v[184:187], v[208:211], v[112:115]\n\t"
    "v_fmac_f32 v144, v100, v88\n\t"
    "v_mad_u32_u24 v134, v134, s68, v151\n\t"
    "ds_read_b128 v[180:183], v132 offset:0\n\t"
    "ds_read_b128 v[184:187], v132 offset:64\n\t"
    "v_mfma_f32_16x16x32_bf16 v[116:119], v[212:215], v[196:199], 0\n\t"
    "v_and_b32 v134, -16, v134\n\t"
    "v_fmac_f32 v145, v104, v48\n\t"
    "v_mfma_f32_16x16x32_bf16 v[116:119], v[216:219], v[200:203], v[116:119]\n\t"
    "v_fmac_f32 v144, v105, v49\n\t"
    "v_fmac_f32 v145, v106, v50\n\t"
    "v_mfma_f32_16x16x32_bf16 v[92:95], v[188:191], v[188:191], 0\n\t"
    "v_fmac_f32 v144, v107, v51\n\t"
    "v_mad_u32_u24 v135, v135, s68, v151\n\t"
    "v_mfma_f32_16x16x32_bf16 v[92:95], v[192:195], v[192:195], v[92:95]\n\t"
    "v_and_b32 v135, -16, v135\n\t"
    "v_fmac_f32 v145, v108, v52\n\t"
    "v_mfma_f32_16x16x32_bf16 v[96:99], v[188:191], v[196:199], 0\n\t"
    "v_fmac_f32 v144, v109, v53\n\t"
    "v_fmac_f32 v145, v110, v54\n\t"
    "v_mfma_f32_16x16x32_bf16 v[96:99], v[192:195], v[200:203], v[96:99]\n\t"
    "v_fmac_f32 v144, v111, v55\n\t"
    "v_fmac_f32 v145, v112, v56\n\t"
    "v_mfma_f32_16x16x32_bf16 v[100:103], v[188:191], v[204:207], 0\n\t"
    "v_fmac_f32 v144, v113, v57\n\t"
    "v_fmac_f32 v145, v114, v58\n\t"
    "v_mfma_f32_16x16x32_bf16 v[100:103], v[192:195], v[208:211], v[100:103]\n\t"
    "v_fmac_f32 v144, v115, v59\n\t"
    "v_fmac_f32 v145, v116, v89\n\t"
    "ds_read_b128 v[188:191], v133 offset:0\n\t"
    "ds_read_b128 v[192:195], v133 offset:64\n\t"
    "v_mfma_f32_16x16x32_bf16 v[104:107], v[212:215], v[204:207], 0\n\t"
    "v_fmac_f32 v144, v92, v60\n\t"
    "v_fmac_f32 v145, v93, v61\n\t"
    "v_mfma_f32_16x16x32_bf16 v[104:107], v[216:219], v[208:211], v[104:107]\n\t"
    "v_fmac_f32 v144, v94, v62\n\t"
    "v_fmac_f32 v145, v95, v63\n\t"
    "v_mfma_f32_16x16x32_bf16 v[108:111], v[196:199], v[196:199], 0\n\t"
    "v_fmac_f32 v144, v96, v64\n\t"
    "v_fmac_f32 v145, v97, v65\n\t"
    "v_mfma_f32_16x16x32_bf16 v[108:111], v[200:203], v[200:203], v[108:111]\n\t"
    "v_fmac_f32 v144, v98, v66\n\t"
    "v_fmac_f32 v145, v99, v67\n\t"
    "v_mfma_f32_16x16x32_bf16 v[112:115], v[196:199], v[204:207], 0\n\t"
    "v_fmac_f32 v144, v100, v68\n\t"
    "v_fmac_f32 v145, v101, v69\n\t"
    "v_mfma_f32_16x16x32_bf16 v[112:115], v[200:203], v[208:211], v[112:115]\n\t"
    "v_fmac_f32 v144, v102, v70\n\t"
    "v_fmac_f32 v145, v103, v71\n\t"
    "ds_read_b128 v[196:199], v134 offset:0\n\t"
    "ds_read_b128 v[200:203], v134 offset:64\n\t"
    "v_mfma_f32_16x16x32_bf16 v[116:119], v[204:207], v[204:207], 0\n\t"
    "v_fmac_f32 v144, v104, v90\n\t"
    "v_mfma_f32_16x16x32_bf16 v[116:119], v[208:211], v[208:211], v[116:119]\n\t"
    "ds_read_b128 v[204:207], v135 offset:0\n\t"
    "ds_read_b128 v[208:211], v135 offset:64\n\t"
    "s_mov_b32 s52, s53\n\t"
    "s_mov_b32 s53, s54\n\t"
    "s_mov_b32 s54, s55\n\t"
    "s_mov_b32 s55, s60\n\t"
    "s_cmp_lt_u32 s53, s64\n\t"
    "s_cbranch_scc1 L_apnn_%=\n\t"
    "L_adone_%=:\n\t"
    "s_mul_i32 s69, s52, 256\n\t"
    "s_add_u32 s69, s69, s50\n\t"
    "v_fmac_f32 v145, v108, v72\n\t"
    "v_fmac_f32 v144, v109, v73\n\t"
    "v_fmac_f32 v145, v110, v74\n\t"
    "v_fmac_f32 v144, v111, v75\n\t"
    "v_fmac_f32 v145, v112, v76\n\t"
    "v_fmac_f32 v144, v113, v77\n\t"
    "v_fmac_f32 v145, v114, v78\n\t"
    "v_fmac_f32 v144, v115, v79\n\t"
    "v_fmac_f32 v145, v116, v80\n\t"
    "v_fmac_f32 v144, v117, v81\n\t"
    "v_fmac_f32 v145, v118, v82\n\t"
    "v_fmac_f32 v144, v119, v83\n\t"
    "v_add_f32 v40, v144, v145\n\t"
    "L_aend_%=:\n\t"
    "s_branch L_aend2_%=\n\t"
    "L_ahk00_%=:\n\t"
    "v_mov_b32 v147, 3112\n\t"
    "s_cmp_lt_u32 s54, s64\n\t"
    "ds_read_b32 v147, v147\n\t"
    "s_cbranch_scc1 L_ahb00_%=\n\t"
    "s_waitcnt vmcnt(3)\n\t"
    "v_add_u32 v137, 0, v155\n\t"
    "v_add_u32 v138, 16, v155\n\t"
    "v_add_u32 v140, 32, v155\n\t"
    "v_add_u32 v141, 48, v155\n\t"
    "v_add_u32 v142, 64, v155\n\t"
    "v_add_u32 v143, 80, v155\n\t"
    "s_branch L_ahb00_%=\n\t"
    "L_ahk10_%=:\n\t"
    "s_waitcnt lgkmcnt(4)\n\t"
    "v_readfirstlane_b32 s65, v147\n\t"
    "s_nop 0\n\t"
    "s_cmp_eq_u32 s65, 0\n\t"
    "s_cbranch_scc1 L_ahb10_%=\n\t"
    "s_cmp_lt_u32 s62, 2\n\t"
    "s_cbranch_scc1 L_mel1_%=\n\t"
    "buffer_load_dwordx4 v[220:223], v127, %12, s61 offen offset:2048 sc1\n\t"
    "buffer_load_dwordx4 v[224:227], v127, %12, s61 offen offset:3072 sc1\n\t"
    "s_add_u32 s61, s61, 0x1000\n\t"
    "buffer_load_dwordx4 v[228:231], v127, %12, s61 offen offset:2048 sc1\n\t"
    "buffer_load_dwordx4 v[232:235], v127, %12, s61 offen offset:3072 sc1\n\t"
    "s_add_u32 s61, s61, 0x1000\n\t"
    "buffer_load_dwordx4 v[236:239], v127, %12, s61 offen offset:2048 sc1\n\t"
    "buffer_load_dwordx4 v[240:243], v127, %12, s61 offen offset:3072 sc1\n\t"
    "s_add_u32 s61, s61, 0x1000\n\t"
    "buffer_load_dwordx4 v[244:247], v127, %12, s61 offen offset:2048 sc1\n\t"
    "buffer_load_dwordx4 v[248:251], v127, %12, s61 offen offset:3072 sc1\n\t"
    "s_add_u32 s61, s61, 0x1000\n\t"
    "buffer_load_dwordx4 v[252:255], v127, %12, s61 offen offset:2048 sc1\n\t"
    "s_mov_b32 s62, 1\n\t"
    "L_mel1_%=:\n\t"
    "s_cmp_lt_u32 s54, s64\n\t"
    "s_cbranch_scc1 L_ahb10_%=\n\t"
    "buffer_load_dwordx4 v[0:3], v127, %12, s63 offen offset:0 sc1\n\t"
    "buffer_load_dwordx4 v[4:7], v127, %12, s63 offen offset:1024 sc1\n\t"
    "buffer_load_dwordx4 v[8:11], v127, %12, s63 offen offset:2048 sc1\n\t"
    "buffer_load_dwordx4 v[12:15], v127, %12, s63 offen offset:3072 sc1\n\t"
    "s_add_u32 s63, s63, 0x1000\n\t"
    "s_mov_b32 s62, 0\n\t"
    "s_branch L_ahb10_%=\n\t"
    "L_ahk01_%=:\n\t"
    "v_mov_b32 v147, 3112\n\t"
    "s_cmp_lt_u32 s54, s64\n\t"
    "ds_read_b32 v147, v147\n\t"
    "s_cbranch_scc1 L_ahb01_%=\n\t"
    "s_waitcnt vmcnt(3)\n\t"
    "v_add_u32 v129, 0, v155\n\t"
    "v_add_u32 v130, 16, v155\n\t"
    "v_add_u32 v132, 32, v155\n\t"
    "v_add_u32 v133, 48, v155\n\t"
    "v_add_u32 v134, 64, v155\n\t"
    "v_add_u32 v135, 80, v155\n\t"
    "s_branch L_ahb01_%=\n\t"
    "L_ahk11_%=:\n\t"
    "s_waitcnt lgkmcnt(4)\n\t"
    "v_readfirstlane_b32 s65, v147\n\t"
    "s_nop 0\n\t"
    "s_cmp_eq_u32 s65, 0\n\t"
    "s_cbranch_scc1 L_ahb11_%=\n\t"
    "s_cmp_lt_u32 s62, 2\n\t"
    "s_cbranch_scc1 L_mel3_%=\n\t"
    "buffer_load_dwordx4 v[220:223], v127, %12, s61 offen offset:2048 sc1\n\t"
    "buffer_load_dwordx4 v[224:227], v127, %12, s61 offen offset:3072 sc1\n\t"
    "s_add_u32 s61, s61, 0x1000\n\t"
    "buffer_load_dwordx4 v[228:231], v127, %12, s61 offen offset:2048 sc1\n\t"
    "buffer_load_dwordx4 v[232:235], v127, %12, s61 offen offset:3072 sc1\n\t"
    "s_add_u32 s61, s61, 0x1000\n\t"
    "buffer_load_dwordx4 v[236:239], v127, %12, s61 offen offset:2048 sc1\n\t"
    "buffer_load_dwordx4 v[240:243], v127, %12, s61 offen offset:3072 sc1\n\t"
    "s_add_u32 s61, s61, 0x1000\n\t"
    "buffer_load_dwordx4 v[244:247], v127, %12, s61 offen offset:2048 sc1\n\t"
    "buffer_load_dwordx4 v[248:251], v127, %12, s61 offen offset:3072 sc1\n\t"
    "s_add_u32 s61, s61, 0x1000\n\t"
    "buffer_load_dwordx4 v[252:255], v127, %12, s61 offen offset:2048 sc1\n\t"
    "s_mov_b32 s62, 1\n\t"
    "L_mel3_%=:\n\t"
    "s_cmp_lt_u32 s54, s64\n\t"
    "s_cbranch_scc1 L_ahb11_%=\n\t"
    "buffer_load_dwordx4 v[0:3], v127, %12, s63 offen offset:0 sc1\n\t"
    "buffer_load_dwordx4 v[4:7], v127, %12, s63 offen offset:1024 sc1\n\t"
    "buffer_load_dwordx4 v[8:11], v127, %12, s63 offen offset:2048 sc1\n\t"
    "buffer_load_dwordx4 v[12:15], v127, %12, s63 offen offset:3072 sc1\n\t"
    "s_add_u32 s63, s63, 0x1000\n\t"
    "s_mov_b32 s62, 0\n\t"
    "s_branch L_ahb11_%=\n\t"
    "L_aend2_%=:\n\t"
    "s_setprio 0\n\t"
    "v_lshlrev_b32 v113, 16, v127\n\t"
    "v_add_u32 v113, 3108, v113\n\t"
    "s_cmp_eq_u32 s62, 0\n\t"
    "s_cbranch_scc1 L_mpf_%=\n\t"
    "L_ml0_%=:\n\t"
    "ds_read_b32 v114, v113 offset:4\n\t"
    "s_waitcnt lgkmcnt(0)\n\t"
    "v_readfirstlane_b32 s65, v114\n\t"
    "s_nop 0\n\t"
    "s_cmp_lg_u32 s65, 0\n\t"
    "s_cbranch_scc1 L_mm0_%=\n\t"
    "s_sleep 2\n\t"
    "s_branch L_ml0_%=\n\t"
    "L_mm0_%=:\n\t"
    "s_cmp_lt_u32 s62, 2\n\t"
    "s_cbranch_scc1 L_mel5_%=\n\t"
    "buffer_load_dwordx4 v[220:223], v127, %12, s61 offen offset:2048 sc1\n\t"
    "buffer_load_dwordx4 v[224:227], v127, %12, s61 offen offset:3072 sc1\n\t"
    "s_add_u32 s61, s61, 0x1000\n\t"
    "buffer_load_dwordx4 v[228:231], v127, %12, s61 offen offset:2048 sc1\n\t"
    "buffer_load_dwordx4 v[232:235], v127, %12, s61 offen offset:3072 sc1\n\t"
    "s_add_u32 s61, s61, 0x1000\n\t"
    "buffer_load_dwordx4 v[236:239], v127, %12, s61 offen offset:2048 sc1\n\t"
    "buffer_load_dwordx4 v[240:243], v127, %12, s61 offen offset:3072 sc1\n\t"
    "s_add_u32 s61, s61, 0x1000\n\t"
    "buffer_load_dwordx4 v[244:247], v127, %12, s61 offen offset:2048 sc1\n\t"
    "buffer_load_dwordx4 v[248:251], v127, %12, s61 offen offset:3072 sc1\n\t"
    "s_add_u32 s61, s61, 0x1000\n\t"
    "buffer_load_dwordx4 v[252:255], v127, %12, s61 offen offset:2048 sc1\n\t"
    "s_mov_b32 s62, 1\n\t"
    "L_mel5_%=:\n\t"
    "buffer_load_dwordx4 v[0:3], v127, %12, s63 offen offset:0 sc1\n\t"
    "buffer_load_dwordx4 v[4:7], v127, %12, s63 offen offset:1024 sc1\n\t"
    "buffer_load_dwordx4 v[8:11], v127, %12, s63 offen offset:2048 sc1\n\t"
    "buffer_load_dwordx4 v[12:15], v127, %12, s63 offen offset:3072 sc1\n\t"
    "s_add_u32 s63, s63, 0x1000\n\t"
    "s_mov_b32 s62, 0\n\t"
    "L_mpf_%=:\n\t"
    "v_lshrrev_b32 v105, 4, v127\n\t"
    "v_and_b32 v105, 48, v105\n\t"
    "s_movk_i32 s65, 130\n\t"
    "v_mad_u32_u24 v104, v155, s65, v105\n\t"
    "v_add_u32 v105, 14170, v104\n\t"
    "v_and_b32 v104, -16, v104\n\t"
    "v_and_b32 v105, -16, v105\n\t"
    "buffer_load_dwordx4 v[128:131], v127, %12, s61 offen offset:3072 sc1\n\t"
    "s_add_u32 s61, s61, 0x1000\n\t"
    "buffer_load_dwordx4 v[132:135], v127, %12, s61 offen offset:2048 sc1\n\t"
    "ds_read_b128 v[156:159], v104 offset:12480\n\t"
    "ds_read_b128 v[160:163], v104 offset:12544\n\t"
    "buffer_load_dwordx4 v[136:139], v127, %12, s61 offen offset:3072 sc1\n\t"
    "s_add_u32 s61, s61, 0x1000\n\t"
    "buffer_load_dwordx4 v[140:143], v127, %12, s61 offen offset:2048 sc1\n\t"
    "ds_read_b128 v[212:215], v105 offset:0\n\t"
    "ds_read_b128 v[216:219], v105 offset:64\n\t"
    "buffer_load_dwordx4 v[144:147], v127, %12, s61 offen offset:3072 sc1\n\t"
    "s_add_u32 s61, s61, 0x1000\n\t"
    "buffer_load_dwordx4 v[148:151], v127, %12, s61 offen offset:2048 sc1\n\t"
    "buffer_load_dwordx4 v[152:155], v127, %12, s61 offen offset:3072 sc1\n\t"
    "s_add_u32 s61, s61, 0x1000\n\t"
    "v_mov_b32 v114, 1\n\t"
    "ds_add_u32 v113, v114\n\t"
    "s_waitcnt lgkmcnt(5)\n\t"
    "s_waitcnt vmcnt(7)\n\t"
    "v_mfma_f32_16x16x32_bf16 v[16:19], v[0:3], v[164:167], 0\n\t"
    "v_mfma_f32_16x16x32_bf16 v[16:19], v[4:7], v[168:171], v[16:19]\n\t"
    "v_mfma_f32_16x16x32_bf16 v[16:19], v[8:11], v[220:223], v[16:19]\n\t"
    "v_mfma_f32_16x16x32_bf16 v[16:19], v[12:15], v[224:227], v[16:19]\n\t"
    "v_mfma_f32_16x16x32_bf16 v[20:23], v[0:3], v[172:175], 0\n\t"
    "v_mfma_f32_16x16x32_bf16 v[20:23], v[4:7], v[176:179], v[20:23]\n\t"
    "v_mfma_f32_16x16x32_bf16 v[20:23], v[8:11], v[228:231], v[20:23]\n\t"
    "v_mfma_f32_16x16x32_bf16 v[20:23], v[12:15], v[232:235], v[20:23]\n\t"
    "v_mfma_f32_16x16x32_bf16 v[80:83], v[0:3], v[180:183], 0\n\t"
    "v_mfma_f32_16x16x32_bf16 v[80:83], v[4:7], v[184:187], v[80:83]\n\t"
    "v_mfma_f32_16x16x32_bf16 v[80:83], v[8:11], v[236:239], v[80:83]\n\t"
    "v_mfma_f32_16x16x32_bf16 v[80:83], v[12:15], v[240:243], v[80:83]\n\t"
    "v_cvt_pk_bf16_f32 v24, v16, v17\n\t"
    "v_cvt_pk_bf16_f32 v25, v18, v19\n\t"
    "v_mfma_f32_16x16x32_bf16 v[16:19], v[0:3], v[188:191], 0\n\t"
    "v_mfma_f32_16x16x32_bf16 v[16:19], v[4:7], v[192:195], v[16:19]\n\t"
    "v_mfma_f32_16x16x32_bf16 v[16:19], v[8:11], v[244:247], v[16:19]\n\t"
    "v_mfma_f32_16x16x32_bf16 v[16:19], v[12:15], v[248:251], v[16:19]\n\t"
    "v_cvt_pk_bf16_f32 v26, v20, v21\n\t"
    "v_cvt_pk_bf16_f32 v27, v22, v23\n\t"
    "s_waitcnt vmcnt(6)\n\t"
    "v_mfma_f32_16x16x32_bf16 v[20:23], v[0:3], v[196:199], 0\n\t"
    "v_mfma_f32_16x16x32_bf16 v[20:23], v[4:7], v[200:203], v[20:23]\n\t"
    "v_mfma_f32_16x16x32_bf16 v[20:23], v[8:11], v[252:255], v[20:23]\n\t"
    "v_mfma_f32_16x16x32_bf16 v[20:23], v[12:15], v[128:131], v[20:23]\n\t"
    "v_cvt_pk_bf16_f32 v28, v80, v81\n\t"
    "v_cvt_pk_bf16_f32 v29, v82, v83\n\t"
    "s_waitcnt lgkmcnt(0)\n\t"
    "s_waitcnt vmcnt(4)\n\t"
    "v_mfma_f32_16x16x32_bf16 v[80:83], v[0:3], v[204:207], 0\n\t"
    "v_mfma_f32_16x16x32_bf16 v[80:83], v[4:7], v[208:211], v[80:83]\n\t"
    "v_mfma_f32_16x16x32_bf16 v[80:83], v[8:11], v[132:135], v[80:83]\n\t"
    "v_mfma_f32_16x16x32_bf16 v[80:83], v[12:15], v[136:139], v[80:83]\n\t"
    "v_cvt_pk_bf16_f32 v30, v16, v17\n\t"
    "v_cvt_pk_bf16_f32 v31, v18, v19\n\t"
    "s_waitcnt vmcnt(2)\n\t"
    "v_mfma_f32_16x16x32_bf16 v[16:19], v[0:3], v[156:159], 0\n\t"
    "buffer_load_dwordx4 v[100:103], v127, %12, s84 offen offset:0 sc1\n\t"
    "s_add_u32 s84, s84, s97\n\t"
    "v_mfma_f32_16x16x32_bf16 v[16:19], v[4:7], v[160:163], v[16:19]\n\t"
    "buffer_load_dwordx4 v[104:107], v127, %12, s85 offen offset:0 sc1\n\t"
    "s_add_u32 s85, s85, s97\n\t"
    "v_mfma_f32_16x16x32_bf16 v[16:19], v[8:11], v[140:143], v[16:19]\n\t"
    "v_mfma_f32_16x16x32_bf16 v[16:19], v[12:15], v[144:147], v[16:19]\n\t"
    "v_cvt_pk_bf16_f32 v32, v20, v21\n\t"
    "v_cvt_pk_bf16_f32 v33, v22, v23\n\t"
    "s_waitcnt vmcnt(2)\n\t"
    "v_mfma_f32_16x16x32_bf16 v[20:23], v[0:3], v[212:215], 0\n\t"
    "buffer_load_dwordx4 v[108:111], v127, %12, s86 offen offset:0 sc1\n\t"
    "s_add_u32 s86, s86, s97\n\t"
    "v_mfma_f32_16x16x32_bf16 v[20:23], v[4:7], v[216:219], v[20:23]\n\t"
    "buffer_load_dwordx4 v[112:115], v127, %12, s87 offen offset:0 sc1\n\t"
    "s_add_u32 s87, s87, s97\n\t"
    "v_mfma_f32_16x16x32_bf16 v[20:23], v[8:11], v[148:151], v[20:23]\n\t"
    "v_mfma_f32_16x16x32_bf16 v[20:23], v[12:15], v[152:155], v[20:23]\n\t"
    "v_cvt_pk_bf16_f32 v34, v80, v81\n\t"
    "v_cvt_pk_bf16_f32 v35, v82, v83\n\t"
    "v_cvt_pk_bf16_f32 v36, v16, v17\n\t"
    "v_cvt_pk_bf16_f32 v37, v18, v19\n\t"
    "s_nop 3\n\t"
    "v_cvt_pk_bf16_f32 v38, v20, v21\n\t"
    "v_cvt_pk_bf16_f32 v39, v22, v23\n\t"
    "v_lshrrev_b32 v41, 2, v127\n\t"
    "s_and_b32 s65, %14, 8\n\t"
    "s_lshl_b32 s65, s65, 10\n\t"
    "s_add_u32 s63, s63, s65\n\t"
    "buffer_store_dword v40, v41, %12, s69 offen sc1\n\t"
    "v_mov_b32 v82, 3108\n\t"
    "s_cmp_eq_u32 %16, 0\n\t"
    "s_cselect_b32 s47, 0x1ff, 0\n\t"
    "L_my_%=:\n\t"
    "ds_read_b32 v81, v82\n\t"
    "s_waitcnt lgkmcnt(0)\n\t"
    "v_cmp_gt_u32 vcc, 8, v81\n\t"
    "s_cbranch_vccz L_mz_%=\n\t"
    "s_sleep 2\n\t"
    "s_branch L_my_%=\n\t"
    "L_mz_%=:\n\t"
    "ds_write_b64 v125, v[24:25] offset:0\n\t"
    "v_mov_b64 v[0:1], 0\n\t"
    "ds_write_b64 v125, v[26:27] offset:128\n\t"
    "v_mov_b64 v[2:3], 0\n\t"
    "ds_write_b64 v125, v[28:29] offset:256\n\t"
    "v_mov_b64 v[4:5], 0\n\t"
    "ds_write_b64 v125, v[30:31] offset:384\n\t"
    "v_mov_b64 v[6:7], 0\n\t"
    "ds_write_b64 v125, v[32:33] offset:512\n\t"
    "ds_write_b64 v125, v[34:35] offset:640\n\t"
    "ds_write_b64 v125, v[36:37] offset:768\n\t"
    "ds_write_b64 v125, v[38:39] offset:872\n\t"
    "v_add_u32 v125, s93, v125\n\t"
    "s_sub_u32 s93, 0, s93\n\t"
    "s_waitcnt lgkmcnt(0)\n\t"
    "s_barrier\n\t"
    "s_memrealtime s[44:45]\n\t"
    "s_waitcnt lgkmcnt(0)\n\t"
    "s_waitcnt vmcnt(4)\n\t"
    "v_add_u32_sdwa v126, s92, v100 dst_sel:DWORD dst_unused:UNUSED_PAD src0_sel:DWORD src1_sel:WORD_1\n\t"
    "v_add_u32_sdwa v100, s92, v100 dst_sel:DWORD dst_unused:UNUSED_PAD src0_sel:DWORD src1_sel:WORD_0\n\t"
    "ds_read_b64 v[84:85], v100 offset:0\n\t"
    "ds_read_b64 v[86:87], v126 offset:0\n\t"
    "v_add_u32_sdwa v90, s92, v101 dst_sel:DWORD dst_unused:UNUSED_PAD src0_sel:DWORD src1_sel:WORD_1\n\t"
    "v_add_u32_sdwa v101, s92, v101 dst_sel:DWORD dst_unused:UNUSED_PAD src0_sel:DWORD src1_sel:WORD_0\n\t"
    "ds_read_b64 v[88:89], v101 offset:0\n\t"
    "ds_read_b64 v[90:91], v90 offset:0\n\t"
    "v_add_u32 v63, s96, v127\n\t"
    "s_add_u32 s96, s96, 0x1000\n\t"
    "s_and_b32 s96, s96, 0x3000\n\t"
    "v_add_u32 v63, 128000, v63\n\t"
    "ds_read_b128 v[48:51], v63 offset:0\n\t"
    "ds_read_b128 v[52:55], v63 offset:1024\n\t"
    "ds_read_b128 v[56:59], v63 offset:2048\n\t"
    "ds_read_b128 v[60:63], v63 offset:3072\n\t"
    "v_add_u32_sdwa v126, s92, v102 dst_sel:DWORD dst_unused:UNUSED_PAD src0_sel:DWORD src1_sel:WORD_1\n\t"
    "v_add_u32_sdwa v102, s92, v102 dst_sel:DWORD dst_unused:UNUSED_PAD src0_sel:DWORD src1_sel:WORD_0\n\t"
    "ds_read_b64 v[92:93], v102 offset:0\n\t"
    "ds_read_b64 v[94:95], v126 offset:0\n\t"
    "v_add_u32_sdwa v98, s92, v103 dst_sel:DWORD dst_unused:UNUSED_PAD src0_sel:DWORD src1_sel:WORD_1\n\t"
    "v_add_u32_sdwa v103, s92, v103 dst_sel:DWORD dst_unused:UNUSED_PAD src0_sel:DWORD src1_sel:WORD_0\n\t"
    "ds_read_b64 v[96:97], v103 offset:0\n\t"
    "ds_read_b64 v[98:99], v98 offset:0\n\t"
    "buffer_load_dwordx4 v[100:103], v127, %12, s88 offen offset:0 sc1\n\t"
    "s_add_u32 s88, s88, s97\n\t"
    "s_waitcnt vmcnt(4)\n\t"
    "v_add_u32_sdwa v126, s92, v104 dst_sel:DWORD dst_unused:UNUSED_PAD src0_sel:DWORD src1_sel:WORD_1\n\t"
    "v_add_u32_sdwa v104, s92, v104 dst_sel:DWORD dst_unused:UNUSED_PAD src0_sel:DWORD src1_sel:WORD_0\n\t"
    "v_mov_b64 v[8:9], 0\n\t"
    "v_mov_b64 v[10:11], 0\n\t"
    "v_mov_b64 v[12:13], 0\n\t"
    "v_mov_b64 v[14:15], 0\n\t"
    "v_mov_b64 v[16:17], 0\n\t"
    "v_mov_b64 v[18:19], 0\n\t"
    "s_waitcnt lgkmcnt(8)\n\t"
    "v_smfmac_f32_16x16x64_bf16 v[0:3], v[120:123], v[84:91], v124\n\t"
    "ds_read_b64 v[84:85], v104 offset:0\n\t"
    "s_waitcnt lgkmcnt(5)\n\t"
    "v_mfma_f32_16x16x32_bf16 v[116:119], v[48:51], v[164:167], 0\n\t"
    "ds_read_b64 v[86:87], v126 offset:0\n\t"
    "v_mfma_f32_16x16x32_bf16 v[116:119], v[52:55], v[168:171], v[116:119]\n\t"
    "v_add_u32_sdwa v90, s92, v105 dst_sel:DWORD dst_unused:UNUSED_PAD src0_sel:DWORD src1_sel:WORD_1\n\t"
    "v_add_u32_sdwa v105, s92, v105 dst_sel:DWORD dst_unused:UNUSED_PAD src0_sel:DWORD src1_sel:WORD_0\n\t"
    "ds_read_b64 v[88:89], v105 offset:0\n\t"
    "v_mfma_f32_16x16x32_bf16 v[116:119], v[56:59], v[220:223], v[116:119]\n\t"
    "ds_read_b64 v[90:91], v90 offset:0\n\t"
    "v_mfma_f32_16x16x32_bf16 v[116:119], v[60:63], v[224:227], v[116:119]\n\t"
    "v_add_u32_sdwa v126, s92, v106 dst_sel:DWORD dst_unused:UNUSED_PAD src0_sel:DWORD src1_sel:WORD_1\n\t"
    "v_add_u32_sdwa v106, s92, v106 dst_sel:DWORD dst_unused:UNUSED_PAD src0_sel:DWORD src1_sel:WORD_0\n\t"
    "v_mfma_f32_16x16x32_bf16 v[80:83], v[48:51], v[172:175], 0\n\t"
    "v_mov_b64 v[20:21], 0\n\t"
    "v_mov_b64 v[22:23], 0\n\t"
    "v_mov_b64 v[24:25], 0\n\t"
    "v_mov_b64 v[26:27], 0\n\t"
    "v_mov_b64 v[28:29], 0\n\t"
    "v_mov_b64 v[30:31], 0\n\t"
    "s_waitcnt vmcnt(3) lgkmcnt(4)\n\t"
    "v_smfmac_f32_16x16x64_bf16 v[4:7], v[120:123], v[92:99], v124\n\t"
    "ds_read_b64 v[92:93], v106 offset:0\n\t"
    "v_mfma_f32_16x16x32_bf16 v[80:83], v[52:55], v[176:179], v[80:83]\n\t"
    "ds_read_b64 v[94:95], v126 offset:0\n\t"
    "v_mfma_f32_16x16x32_bf16 v[80:83], v[56:59], v[228:231], v[80:83]\n\t"
    "v_add_u32_sdwa v98, s92, v107 dst_sel:DWORD dst_unused:UNUSED_PAD src0_sel:DWORD src1_sel:WORD_1\n\t"
    "v_add_u32_sdwa v107, s92, v107 dst_sel:DWORD dst_unused:UNUSED_PAD src0_sel:DWORD src1_sel:WORD_0\n\t"
    "ds_read_b64 v[96:97], v107 offset:0\n\t"
    "v_mfma_f32_16x16x32_bf16 v[80:83], v[60:63], v[232:235], v[80:83]\n\t"
    "ds_read_b64 v[98:99], v98 offset:0\n\t"
    "v_cvt_pk_bf16_f32 v116, v116, v117\n\t"
    "v_cvt_pk_bf16_f32 v117, v118, v119\n\t"
    "ds_write_b64 v125, v[116:117] offset:0\n\t"
    "v_mfma_f32_16x16x32_bf16 v[116:119], v[48:51], v[180:183], 0\n\t"
    "buffer_load_dwordx4 v[104:107], v127, %12, s89 offen offset:0 sc1\n\t"
    "s_add_u32 s89, s89, s97\n\t"
    "v_add_u32_sdwa v126, s92, v108 dst_sel:DWORD dst_unused:UNUSED_PAD src0_sel:DWORD src1_sel:WORD_1\n\t"
    "v_add_u32_sdwa v108, s92, v108 dst_sel:DWORD dst_unused:UNUSED_PAD src0_sel:DWORD src1_sel:WORD_0\n\t"
    "v_mfma_f32_16x16x32_bf16 v[116:119], v[52:55], v[184:187], v[116:119]\n\t"
    "v_mov_b64 v[32:33], 0\n\t"
    "v_mov_b64 v[34:35], 0\n\t"
    "v_mov_b64 v[36:37], 0\n\t"
    "v_mov_b64 v[38:39], 0\n\t"
    "v_mov_b64 v[40:41], 0\n\t"
    "v_mov_b64 v[42:43], 0\n\t"
    "v_mov_b64 v[44:45], 0\n\t"
    "v_mov_b64 v[46:47], 0\n\t"
    "s_waitcnt lgkmcnt(5)\n\t"
    "v_smfmac_f32_16x16x64_bf16 v[8:11], v[120:123], v[84:91], v124\n\t"
    "ds_read_b64 v[84:85], v108 offset:0\n\t"
    "v_mfma_f32_16x16x32_bf16 v[116:119], v[56:59], v[236:239], v[116:119]\n\t"
    "ds_read_b64 v[86:87], v126 offset:0\n\t"
    "v_mfma_f32_16x16x32_bf16 v[116:119], v[60:63], v[240:243], v[116:119]\n\t"
    "v_add_u32_sdwa v90, s92, v109 dst_sel:DWORD dst_unused:UNUSED_PAD src0_sel:DWORD src1_sel:WORD_1\n\t"
    "v_add_u32_sdwa v109, s92, v109 dst_sel:DWORD dst_unused:UNUSED_PAD src0_sel:DWORD src1_sel:WORD_0\n\t"
    "ds_read_b64 v[88:89], v109 offset:0\n\t"
    "v_cvt_pk_bf16_f32 v80, v80, v81\n\t"
    "v_cvt_pk_bf16_f32 v81, v82, v83\n\t"
    "ds_write_b64 v125, v[80:81] offset:128\n\t"
    "v_mfma_f32_16x16x32_bf16 v[80:83], v[48:51], v[188:191], 0\n\t"
    "ds_read_b64 v[90:91], v90 offset:0\n\t"
    "v_mfma_f32_16x16x32_bf16 v[80:83], v[52:55], v[192:195], v[80:83]\n\t"
    "v_add_u32_sdwa v126, s92, v110 dst_sel:DWORD dst_unused:UNUSED_PAD src0_sel:DWORD src1_sel:WORD_1\n\t"
    "v_add_u32_sdwa v110, s92, v110 dst_sel:DWORD dst_unused:UNUSED_PAD src0_sel:DWORD src1_sel:WORD_0\n\t"
    "v_mfma_f32_16x16x32_bf16 v[80:83], v[56:59], v[244:247], v[80:83]\n\t"
    "s_waitcnt vmcnt(3) lgkmcnt(6)\n\t"
    "v_smfmac_f32_16x16x64_bf16 v[12:15], v[120:123], v[92:99], v124\n\t"
    "ds_read_b64 v[92:93], v110 offset:0\n\t"
    "v_mfma_f32_16x16x32_bf16 v[80:83], v[60:63], v[248:251], v[80:83]\n\t"
    "ds_read_b64 v[94:95], v126 offset:0\n\t"
    "v_cvt_pk_bf16_f32 v116, v116, v117\n\t"
    "v_cvt_pk_bf16_f32 v117, v118, v119\n\t"
    "ds_write_b64 v125, v[116:117] offset:256\n\t"
    "v_mfma_f32_16x16x32_bf16 v[116:119], v[48:51], v[196:199], 0\n\t"
    "v_add_u32_sdwa v98, s92, v111 dst_sel:DWORD dst_unused:UNUSED_PAD src0_sel:DWORD src1_sel:WORD_1\n\t"
    "v_add_u32_sdwa v111, s92, v111 dst_sel:DWORD dst_unused:UNUSED_PAD src0_sel:DWORD src1_sel:WORD_0\n\t"
    "ds_read_b64 v[96:97], v111 offset:0\n\t"
    "v_mfma_f32_16x16x32_bf16 v[116:119], v[52:55], v[200:203], v[116:119]\n\t"
    "ds_read_b64 v[98:99], v98 offset:0\n\t"
    "v_mfma_f32_16x16x32_bf16 v[116:119], v[56:59], v[252:255], v[116:119]\n\t"
    "buffer_load_dwordx4 v[108:111], v127, %12, s90 offen offset:0 sc1\n\t"
    "s_add_u32 s90, s90, s97\n\t"
    "v_add_u32_sdwa v126, s92, v112 dst_sel:DWORD dst_unused:UNUSED_PAD src0_sel:DWORD src1_sel:WORD_1\n\t"
    "v_add_u32_sdwa v112, s92, v112 dst_sel:DWORD dst_unused:UNUSED_PAD src0_sel:DWORD src1_sel:WORD_0\n\t"
    "v_mfma_f32_16x16x32_bf16 v[116:119], v[60:63], v[128:131], v[116:119]\n\t"
    "v_cvt_pk_bf16_f32 v0, v0, v1\n\t"
    "v_cvt_pk_bf16_f32 v1, v2, v3\n\t"
    "v_cvt_pk_bf16_f32 v80, v80, v81\n\t"
    "v_cvt_pk_bf16_f32 v81, v82, v83\n\t"
    "ds_write_b64 v125, v[80:81] offset:384\n\t"
    "v_add_u32 v79, s96, v127\n\t"
    "s_add_u32 s96, s96, 0x1000\n\t"
    "s_and_b32 s96, s96, 0x3000\n\t"
    "v_add_u32 v79, 128000, v79\n\t"
    "ds_read_b128 v[64:67], v79 offset:0\n\t"
    "ds_read_b128 v[68:71], v79 offset:1024\n\t"
    "ds_read_b128 v[72:75], v79 offset:2048\n\t"
    "ds_read_b128 v[76:79], v79 offset:3072\n\t"
    "s_waitcnt lgkmcnt(10)\n\t"
    "v_smfmac_f32_16x16x64_bf16 v[16:19], v[120:123], v[84:91], v124\n\t"
    "ds_read_b64 v[84:85], v112 offset:0\n\t"
    "v_mfma_f32_16x16x32_bf16 v[80:83], v[48:51], v[204:207], 0\n\t"
    "ds_read_b64 v[86:87], v126 offset:0\n\t"
    "v_mfma_f32_16x16x32_bf16 v[80:83], v[52:55], v[208:211], v[80:83]\n\t"
    "v_add_u32_sdwa v90, s92, v113 dst_sel:DWORD dst_unused:UNUSED_PAD src0_sel:DWORD src1_sel:WORD_1\n\t"
    "v_add_u32_sdwa v113, s92, v113 dst_sel:DWORD dst_unused:UNUSED_PAD src0_sel:DWORD src1_sel:WORD_0\n\t"
    "ds_read_b64 v[88:89], v113 offset:0\n\t"
    "v_mfma_f32_16x16x32_bf16 v[80:83], v[56:59], v[132:135], v[80:83]\n\t"
    "ds_read_b64 v[90:91], v90 offset:0\n\t"
    "v_mfma_f32_16x16x32_bf16 v[80:83], v[60:63], v[136:139], v[80:83]\n\t"
    "v_add_u32_sdwa v126, s92, v114 dst_sel:DWORD dst_unused:UNUSED_PAD src0_sel:DWORD src1_sel:WORD_1\n\t"
    "v_add_u32_sdwa v114, s92, v114 dst_sel:DWORD dst_unused:UNUSED_PAD src0_sel:DWORD src1_sel:WORD_0\n\t"
    "v_cvt_pk_bf16_f32 v116, v116, v117\n\t"
    "v_cvt_pk_bf16_f32 v117, v118, v119\n\t"
    "ds_write_b64 v125, v[116:117] offset:512\n\t"
    "v_mfma_f32_16x16x32_bf16 v[116:119], v[48:51], v[156:159], 0\n\t"
    "v_cvt_pk_bf16_f32 v2, v4, v5\n\t"
    "v_cvt_pk_bf16_f32 v3, v6, v7\n\t"
    "v_mov_b64 v[4:5], 0\n\t"
    "v_mov_b64 v[6:7], 0\n\t"
    "s_waitcnt vmcnt(2) lgkmcnt(10)\n\t"
    "v_smfmac_f32_16x16x64_bf16 v[20:23], v[120:123], v[92:99], v124\n\t"
    "ds_read_b64 v[92:93], v114 offset:0\n\t"
    "v_mfma_f32_16x16x32_bf16 v[116:119], v[52:55], v[160:163], v[116:119]\n\t"
    "ds_read_b64 v[94:95], v126 offset:0\n\t"
    "v_mfma_f32_16x16x32_bf16 v[116:119], v[56:59], v[140:143], v[116:119]\n\t"
    "v_add_u32_sdwa v98, s92, v115 dst_sel:DWORD dst_unused:UNUSED_PAD src0_sel:DWORD src1_sel:WORD_1\n\t"
    "v_add_u32_sdwa v115, s92, v115 dst_sel:DWORD dst_unused:UNUSED_PAD src0_sel:DWORD src1_sel:WORD_0\n\t"
    "ds_read_b64 v[96:97], v115 offset:0\n\t"
    "v_mfma_f32_16x16x32_bf16 v[116:119], v[60:63], v[144:147], v[116:119]\n\t"
    "ds_read_b64 v[98:99], v98 offset:0\n\t"
    "v_cvt_pk_bf16_f32 v80, v80, v81\n\t"
    "v_cvt_pk_bf16_f32 v81, v82, v83\n\t"
    "ds_write_b64 v125, v[80:81] offset:640\n\t"
    "v_mfma_f32_16x16x32_bf16 v[80:83], v[48:51], v[212:215], 0\n\t"
    "buffer_load_dwordx4 v[112:115], v127, %12, s91 offen offset:0 sc1\n\t"
    "s_add_u32 s91, s91, s97\n\t"
    "v_add_u32_sdwa v126, s92, v100 dst_sel:DWORD dst_unused:UNUSED_PAD src0_sel:DWORD src1_sel:WORD_1\n\t"
    "v_add_u32_sdwa v100, s92, v100 dst_sel:DWORD dst_unused:UNUSED_PAD src0_sel:DWORD src1_sel:WORD_0\n\t"
    "v_mfma_f32_16x16x32_bf16 v[80:83], v[52:55], v[216:219], v[80:83]\n\t"
    "v_cvt_pk_bf16_f32 v8, v8, v9\n\t"
    "v_cvt_pk_bf16_f32 v9, v10, v11\n\t"
    "s_waitcnt lgkmcnt(6)\n\t"
    "v_smfmac_f32_16x16x64_bf16 v[24:27], v[120:123], v[84:91], v124\n\t"
    "ds_read_b64 v[84:85], v100 offset:0\n\t"
    "v_mfma_f32_16x16x32_bf16 v[80:83], v[56:59], v[148:151], v[80:83]\n\t"
    "ds_read_b64 v[86:87], v126 offset:0\n\t"
    "v_mfma_f32_16x16x32_bf16 v[80:83], v[60:63], v[152:155], v[80:83]\n\t"
    "v_add_u32_sdwa v90, s92, v101 dst_sel:DWORD dst_unused:UNUSED_PAD src0_sel:DWORD src1_sel:WORD_1\n\t"
    "v_add_u32_sdwa v101, s92, v101 dst_sel:DWORD dst_unused:UNUSED_PAD src0_sel:DWORD src1_sel:WORD_0\n\t"
    "ds_read_b64 v[88:89], v101 offset:0\n\t"
    "v_cvt_pk_bf16_f32 v116, v116, v117\n\t"
    "v_cvt_pk_bf16_f32 v117, v118, v119\n\t"
    "ds_write_b64 v125, v[116:117] offset:768\n\t"
    "v_mfma_f32_16x16x32_bf16 v[116:119], v[64:67], v[164:167], 0\n\t"
    "ds_read_b64 v[90:91], v90 offset:0\n\t"
    "v_mfma_f32_16x16x32_bf16 v[116:119], v[68:71], v[168:171], v[116:119]\n\t"
    "v_add_u32_sdwa v126, s92, v102 dst_sel:DWORD dst_unused:UNUSED_PAD src0_sel:DWORD src1_sel:WORD_1\n\t"
    "v_add_u32_sdwa v102, s92, v102 dst_sel:DWORD dst_unused:UNUSED_PAD src0_sel:DWORD src1_sel:WORD_0\n\t"
    "v_mfma_f32_16x16x32_bf16 v[116:119], v[72:75], v[220:223], v[116:119]\n\t"
    "v_cvt_pk_bf16_f32 v10, v12, v13\n\t"
    "v_cvt_pk_bf16_f32 v11, v14, v15\n\t"
    "v_mov_b64 v[12:13], 0\n\t"
    "v_mov_b64 v[14:15], 0\n\t"
    "s_cmp_lg_u32 s47, 0\n\t"
    "s_cbranch_scc1 L_mp2_%=\n\t"
    "L_mp2b_%=:\n\t"
    "s_waitcnt vmcnt(2) lgkmcnt(6)\n\t"
    "v_smfmac_f32_16x16x64_bf16 v[28:31], v[120:123], v[92:99], v124\n\t"
    "ds_read_b64 v[92:93], v102 offset:0\n\t"
    "v_mfma_f32_16x16x32_bf16 v[116:119], v[76:79], v[224:227], v[116:119]\n\t"
    "ds_read_b64 v[94:95], v126 offset:0\n\t"
    "v_cvt_pk_bf16_f32 v80, v80, v81\n\t"
    "v_cvt_pk_bf16_f32 v81, v82, v83\n\t"
    "ds_write_b64 v125, v[80:81] offset:872\n\t"
    "v_mfma_f32_16x16x32_bf16 v[80:83], v[64:67], v[172:175], 0\n\t"
    "v_add_u32_sdwa v98, s92, v103 dst_sel:DWORD dst_unused:UNUSED_PAD src0_sel:DWORD src1_sel:WORD_1\n\t"
    "v_add_u32_sdwa v103, s92, v103 dst_sel:DWORD dst_unused:UNUSED_PAD src0_sel:DWORD src1_sel:WORD_0\n\t"
    "ds_read_b64 v[96:97], v103 offset:0\n\t"
    "v_mfma_f32_16x16x32_bf16 v[80:83], v[68:71], v[176:179], v[80:83]\n\t"
    "ds_read_b64 v[98:99], v98 offset:0\n\t"
    "v_mfma_f32_16x16x32_bf16 v[80:83], v[72:75], v[228:231], v[80:83]\n\t"
    "v_add_u32_sdwa v126, s92, v104 dst_sel:DWORD dst_unused:UNUSED_PAD src0_sel:DWORD src1_sel:WORD_1\n\t"
    "v_add_u32_sdwa v104, s92, v104 dst_sel:DWORD dst_unused:UNUSED_PAD src0_sel:DWORD src1_sel:WORD_0\n\t"
    "v_mfma_f32_16x16x32_bf16 v[80:83], v[76:79], v[232:235], v[80:83]\n\t"
    "v_cvt_pk_bf16_f32 v16, v16, v17\n\t"
    "v_cvt_pk_bf16_f32 v17, v18, v19\n\t"
    "v_cvt_pk_bf16_f32 v116, v116, v117\n\t"
    "v_cvt_pk_bf16_f32 v117, v118, v119\n\t"
    "ds_write_b64 v125, v[116:117] offset:32000\n\t"
    "s_waitcnt lgkmcnt(6)\n\t"
    "v_smfmac_f32_16x16x64_bf16 v[32:35], v[120:123], v[84:91], v124\n\t"
    "ds_read_b64 v[84:85], v104 offset:0\n\t"
    "v_mfma_f32_16x16x32_bf16 v[116:119], v[64:67], v[180:183], 0\n\t"
    "ds_read_b64 v[86:87], v126 offset:0\n\t"
    "v_mfma_f32_16x16x32_bf16 v[116:119], v[68:71], v[184:187], v[116:119]\n\t"
    "v_add_u32_sdwa v90, s92, v105 dst_sel:DWORD dst_unused:UNUSED_PAD src0_sel:DWORD src1_sel:WORD_1\n\t"
    "v_add_u32_sdwa v105, s92, v105 dst_sel:DWORD dst_unused:UNUSED_PAD src0_sel:DWORD src1_sel:WORD_0\n\t"
    "ds_read_b64 v[88:89], v105 offset:0\n\t"
    "v_mfma_f32_16x16x32_bf16 v[116:119], v[72:75], v[236:239], v[116:119]\n\t"
    "ds_read_b64 v[90:91], v90 offset:0\n\t"
    "v_mfma_f32_16x16x32_bf16 v[116:119], v[76:79], v[240:243], v[116:119]\n\t"
    "v_add_u32_sdwa v126, s92, v106 dst_sel:DWORD dst_unused:UNUSED_PAD src0_sel:DWORD src1_sel:WORD_1\n\t"
    "v_add_u32_sdwa v106, s92, v106 dst_sel:DWORD dst_unused:UNUSED_PAD src0_sel:DWORD src1_sel:WORD_0\n\t"
    "v_cvt_pk_bf16_f32 v80, v80, v81\n\t"
    "v_cvt_pk_bf16_f32 v81, v82, v83\n\t"
    "ds_write_b64 v125, v[80:81] offset:32128\n\t"
    "v_mfma_f32_16x16x32_bf16 v[80:83], v[64:67], v[188:191], 0\n\t"
    "v_cvt_pk_bf16_f32 v18, v20, v21\n\t"
    "v_cvt_pk_bf16_f32 v19, v22, v23\n\t"
    "v_mov_b64 v[20:21], 0\n\t"
    "v_mov_b64 v[22:23], 0\n\t"
    "s_waitcnt vmcnt(0)\n\t"
    "s_cmp_lg_u32 s47, 0\n\t"
    "s_cbranch_scc1 L_mp3_%=\n\t"
    "L_mp3b_%=:\n\t"
    "s_barrier\n\t"
    "s_add_u32 s84, s84, s95\n\t"
    "buffer_load_dwordx4 v[100:103], v127, %12, s84 offen offset:0 sc1\n\t"
    "s_lshl_b32 s50, %16, 10\n\t"
    "s_add_u32 s63, s63, s50\n\t"
    "s_add_u32 s50, s50, 0x2000\n\t"
    "s_xor_b32 s50, s50, 0x2000\n\t"
    "s_add_u32 m0, s50, 128000\n\t"
    "s_add_u32 s63, s63, 0x4000\n\t"
    "buffer_load_dwordx4 v127, %12, s63 offen sc1 lds\n\t"
    "s_cmp_lt_u32 %16, 4\n\t"
    "s_cbranch_scc1 L_sp1_%=\n\t"
    "s_setprio 1\n\t"
    "L_sp1_%=:\n\t"
    "s_waitcnt lgkmcnt(6)\n\t"
    "v_smfmac_f32_16x16x64_bf16 v[36:39], v[120:123], v[92:99], v124\n\t"
    "ds_read_b64 v[92:93], v106 offset:0\n\t"
    "v_mfma_f32_16x16x32_bf16 v[80:83], v[68:71], v[192:195], v[80:83]\n\t"
    "ds_read_b64 v[94:95], v126 offset:0\n\t"
    "v_mfma_f32_16x16x32_bf16 v[80:83], v[72:75], v[244:247], v[80:83]\n\t"
    "v_add_u32_sdwa v98, s92, v107 dst_sel:DWORD dst_unused:UNUSED_PAD src0_sel:DWORD src1_sel:WORD_1\n\t"
    "v_add_u32_sdwa v107, s92, v107 dst_sel:DWORD dst_unused:UNUSED_PAD src0_sel:DWORD src1_sel:WORD_0\n\t"
    "ds_read_b64 v[96:97], v107 offset:0\n\t"
    "v_mfma_f32_16x16x32_bf16 v[80:83], v[76:79], v[248:251], v[80:83]\n\t"
    "ds_read_b64 v[98:99], v98 offset:0\n\t"
    "v_cvt_pk_bf16_f32 v116, v116, v117\n\t"
    "v_cvt_pk_bf16_f32 v117, v118, v119\n\t"
    "ds_write_b64 v125, v[116:117] offset:32256\n\t"
    "v_mfma_f32_16x16x32_bf16 v[116:119], v[64:67], v[196:199], 0\n\t"
    "buffer_load_dwordx4 v[104:107], v127, %12, s84 offen offset:1024 sc1\n\t"
    "v_add_u32_sdwa v126, s92, v108 dst_sel:DWORD dst_unused:UNUSED_PAD src0_sel:DWORD src1_sel:WORD_1\n\t"
    "v_add_u32_sdwa v108, s92, v108 dst_sel:DWORD dst_unused:UNUSED_PAD src0_sel:DWORD src1_sel:WORD_0\n\t"
    "v_mfma_f32_16x16x32_bf16 v[116:119], v[68:71], v[200:203], v[116:119]\n\t"
    "v_cvt_pk_bf16_f32 v24, v24, v25\n\t"
    "v_cvt_pk_bf16_f32 v25, v26, v27\n\t"
    "v_mov_b64 v[48:49], 0\n\t"
    "v_mov_b64 v[50:51], 0\n\t"
    "v_mov_b64 v[52:53], 0\n\t"
    "v_mov_b64 v[54:55], 0\n\t"
    "s_waitcnt lgkmcnt(6)\n\t"
    "v_smfmac_f32_16x16x64_bf16 v[40:43], v[120:123], v[84:91], v124\n\t"
    "ds_read_b64 v[84:85], v108 offset:0\n\t"
    "v_mfma_f32_16x16x32_bf16 v[116:119], v[72:75], v[252:255], v[116:119]\n\t"
    "ds_read_b64 v[86:87], v126 offset:0\n\t"
    "v_mfma_f32_16x16x32_bf16 v[116:119], v[76:79], v[128:131], v[116:119]\n\t"
    "v_add_u32_sdwa v90, s92, v109 dst_sel:DWORD dst_unused:UNUSED_PAD src0_sel:DWORD src1_sel:WORD_1\n\t"
    "v_add_u32_sdwa v109, s92, v109 dst_sel:DWORD dst_unused:UNUSED_PAD src0_sel:DWORD src1_sel:WORD_0\n\t"
    "ds_read_b64 v[88:89], v109 offset:0\n\t"
    "v_cvt_pk_bf16_f32 v80, v80, v81\n\t"
    "v_cvt_pk_bf16_f32 v81, v82, v83\n\t"
    "ds_write_b64 v125, v[80:81] offset:32384\n\t"
    "v_mfma_f32_16x16x32_bf16 v[80:83], v[64:67], v[204:207], 0\n\t"
    "ds_read_b64 v[90:91], v90 offset:0\n\t"
    "v_mfma_f32_16x16x32_bf16 v[80:83], v[68:71], v[208:211], v[80:83]\n\t"
    "v_add_u32_sdwa v126, s92, v110 dst_sel:DWORD dst_unused:UNUSED_PAD src0_sel:DWORD src1_sel:WORD_1\n\t"
    "v_add_u32_sdwa v110, s92, v110 dst_sel:DWORD dst_unused:UNUSED_PAD src0_sel:DWORD src1_sel:WORD_0\n\t"
    "v_mfma_f32_16x16x32_bf16 v[80:83], v[72:75], v[132:135], v[80:83]\n\t"
    "v_cvt_pk_bf16_f32 v26, v28, v29\n\t"
    "v_cvt_pk_bf16_f32 v27, v30, v31\n\t"
    "v_mov_b64 v[28:29], 0\n\t"
    "v_mov_b64 v[30:31], 0\n\t"
    "s_waitcnt lgkmcnt(6)\n\t"
    "v_smfmac_f32_16x16x64_bf16 v[44:47], v[120:123], v[92:99], v124\n\t"
    "ds_read_b64 v[92:93], v110 offset:0\n\t"
    "v_mfma_f32_16x16x32_bf16 v[80:83], v[76:79], v[136:139], v[80:83]\n\t"
    "ds_read_b64 v[94:95], v126 offset:0\n\t"
    "v_cvt_pk_bf16_f32 v116, v116, v117\n\t"
    "v_cvt_pk_bf16_f32 v117, v118, v119\n\t"
    "ds_write_b64 v125, v[116:117] offset:32512\n\t"
    "v_mfma_f32_16x16x32_bf16 v[116:119], v[64:67], v[156:159], 0\n\t"
    "v_add_u32_sdwa v98, s92, v111 dst_sel:DWORD dst_unused:UNUSED_PAD src0_sel:DWORD src1_sel:WORD_1\n\t"
    "v_add_u32_sdwa v111, s92, v111 dst_sel:DWORD dst_unused:UNUSED_PAD src0_sel:DWORD src1_sel:WORD_0\n\t"
    "ds_read_b64 v[96:97], v111 offset:0\n\t"
    "v_mfma_f32_16x16x32_bf16 v[116:119], v[68:71], v[160:163], v[116:119]\n\t"
    "ds_read_b64 v[98:99], v98 offset:0\n\t"
    "v_mfma_f32_16x16x32_bf16 v[116:119], v[72:75], v[140:143], v[116:119]\n\t"
    "s_add_u32 s85, s85, s95\n\t"
    "buffer_load_dwordx4 v[108:111], v127, %12, s85 offen offset:0 sc1\n\t"
    "v_add_u32_sdwa v126, s92, v112 dst_sel:DWORD dst_unused:UNUSED_PAD src0_sel:DWORD src1_sel:WORD_1\n\t"
    "v_add_u32_sdwa v112, s92, v112 dst_sel:DWORD dst_unused:UNUSED_PAD src0_sel:DWORD src1_sel:WORD_0\n\t"
    "v_mfma_f32_16x16x32_bf16 v[116:119], v[76:79], v[144:147], v[116:119]\n\t"
    "v_cvt_pk_bf16_f32 v32, v32, v33\n\t"
    "v_cvt_pk_bf16_f32 v33, v34, v35\n\t"
    "v_cvt_pk_bf16_f32 v80, v80, v81\n\t"
    "v_cvt_pk_bf16_f32 v81, v82, v83\n\t"
    "ds_write_b64 v125, v[80:81] offset:32640\n\t"
    "v_mov_b64 v[56:57], 0\n\t"
    "v_mov_b64 v[58:59], 0\n\t"
    "v_mov_b64 v[60:61], 0\n\t"
    "v_mov_b64 v[62:63], 0\n\t"
    "s_waitcnt lgkmcnt(6)\n\t"
    "v_smfmac_f32_16x16x64_bf16 v[48:51], v[120:123], v[84:91], v124\n\t"
    "ds_read_b64 v[84:85], v112 offset:0\n\t"
    "v_mfma_f32_16x16x32_bf16 v[80:83], v[64:67], v[212:215], 0\n\t"
    "ds_read_b64 v[86:87], v126 offset:0\n\t"
    "v_mfma_f32_16x16x32_bf16 v[80:83], v[68:71], v[216:219], v[80:83]\n\t"
    "v_add_u32_sdwa v90, s92, v113 dst_sel:DWORD dst_unused:UNUSED_PAD src0_sel:DWORD src1_sel:WORD_1\n\t"
    "v_add_u32_sdwa v113, s92, v113 dst_sel:DWORD dst_unused:UNUSED_PAD src0_sel:DWORD src1_sel:WORD_0\n\t"
    "ds_read_b64 v[88:89], v113 offset:0\n\t"
    "v_mfma_f32_16x16x32_bf16 v[80:83], v[72:75], v[148:151], v[80:83]\n\t"
    "ds_read_b64 v[90:91], v90 offset:0\n\t"
    "v_mfma_f32_16x16x32_bf16 v[80:83], v[76:79], v[152:155], v[80:83]\n\t"
    "v_add_u32_sdwa v126, s92, v114 dst_sel:DWORD dst_unused:UNUSED_PAD src0_sel:DWORD src1_sel:WORD_1\n\t"
    "v_add_u32_sdwa v114, s92, v114 dst_sel:DWORD dst_unused:UNUSED_PAD src0_sel:DWORD src1_sel:WORD_0\n\t"
    "v_cvt_pk_bf16_f32 v116, v116, v117\n\t"
    "v_cvt_pk_bf16_f32 v117, v118, v119\n\t"
    "ds_write_b64 v125, v[116:117] offset:32768\n\t"
    "s_nop 2\n\t"
    "v_cvt_pk_bf16_f32 v80, v80, v81\n\t"
    "v_cvt_pk_bf16_f32 v81, v82, v83\n\t"
    "ds_write_b64 v125, v[80:81] offset:32872\n\t"
    "v_cvt_pk_bf16_f32 v34, v36, v37\n\t"
    "v_cvt_pk_bf16_f32 v35, v38, v39\n\t"
    "v_mov_b64 v[36:37], 0\n\t"
    "v_mov_b64 v[38:39], 0\n\t"
    "v_cvt_pk_bf16_f32 v40, v40, v41\n\t"
    "v_cvt_pk_bf16_f32 v41, v42, v43\n\t"
    "v_cvt_pk_bf16_f32 v42, v44, v45\n\t"
    "v_cvt_pk_bf16_f32 v43, v46, v47\n\t"
    "v_mov_b64 v[44:45], 0\n\t"
    "v_mov_b64 v[46:47], 0\n\t"
    "s_waitcnt vmcnt(3) lgkmcnt(7)\n\t"
    "v_smfmac_f32_16x16x64_bf16 v[52:55], v[120:123], v[92:99], v124\n\t"
    "ds_read_b64 v[92:93], v114 offset:0\n\t"
    "ds_read_b64 v[94:95], v126 offset:0\n\t"
    "v_add_u32_sdwa v98, s92, v115 dst_sel:DWORD dst_unused:UNUSED_PAD src0_sel:DWORD src1_sel:WORD_1\n\t"
    "v_add_u32_sdwa v115, s92, v115 dst_sel:DWORD dst_unused:UNUSED_PAD src0_sel:DWORD src1_sel:WORD_0\n\t"
    "ds_read_b64 v[96:97], v115 offset:0\n\t"
    "ds_read_b64 v[98:99], v98 offset:0\n\t"
    "buffer_load_dwordx4 v[112:115], v127, %12, s85 offen offset:1024 sc1\n\t"
    "s_sub_u32 s92, 64000, s92\n\t"
    "v_add_u32_sdwa v126, s92, v100 dst_sel:DWORD dst_unused:UNUSED_PAD src0_sel:DWORD src1_sel:WORD_1\n\t"
    "v_add_u32_sdwa v100, s92, v100 dst_sel:DWORD dst_unused:UNUSED_PAD src0_sel:DWORD src1_sel:WORD_0\n\t"
    "v_cvt_pk_bf16_f32 v48, v48, v49\n\t"
    "v_cvt_pk_bf16_f32 v49, v50, v51\n\t"
    "s_waitcnt lgkmcnt(4)\n\t"
    "s_waitcnt lgkmcnt(0)\n\t"
    "s_waitcnt vmcnt(3)\n\t"
    "s_barrier\n\t"
    "v_add_u32 v125, s93, v125\n\t"
    "s_sub_u32 s93, 0, s93\n\t"
    "v_smfmac_f32_16x16x64_bf16 v[56:59], v[120:123], v[84:91], v124\n\t"
    "ds_read_b64 v[84:85], v100 offset:0\n\t"
    "ds_read_b64 v[86:87], v126 offset:0\n\t"
    "v_add_u32_sdwa v90, s92, v101 dst_sel:DWORD dst_unused:UNUSED_PAD src0_sel:DWORD src1_sel:WORD_1\n\t"
    "v_add_u32_sdwa v101, s92, v101 dst_sel:DWORD dst_unused:UNUSED_PAD src0_sel:DWORD src1_sel:WORD_0\n\t"
    "ds_read_b64 v[88:89], v101 offset:0\n\t"
    "ds_read_b64 v[90:91], v90 offset:0\n\t"
    "v_add_u32_sdwa v126, s92, v102 dst_sel:DWORD dst_unused:UNUSED_PAD src0_sel:DWORD src1_sel:WORD_1\n\t"
    "v_add_u32_sdwa v102, s92, v102 dst_sel:DWORD dst_unused:UNUSED_PAD src0_sel:DWORD src1_sel:WORD_0\n\t"
    "v_cvt_pk_bf16_f32 v50, v52, v53\n\t"
    "v_cvt_pk_bf16_f32 v51, v54, v55\n\t"
    "v_mov_b64 v[52:53], 0\n\t"
    "v_mov_b64 v[54:55], 0\n\t"
    "s_waitcnt vmcnt(2)\n\t"
    "v_smfmac_f32_16x16x64_bf16 v[60:63], v[120:123], v[92:99], v124\n\t"
    "ds_read_b64 v[92:93], v102 offset:0\n\t"
    "ds_read_b64 v[94:95], v126 offset:0\n\t"
    "v_add_u32_sdwa v98, s92, v103 dst_sel:DWORD dst_unused:UNUSED_PAD src0_sel:DWORD src1_sel:WORD_1\n\t"
    "v_add_u32_sdwa v103, s92, v103 dst_sel:DWORD dst_unused:UNUSED_PAD src0_sel:DWORD src1_sel:WORD_0\n\t"
    "ds_read_b64 v[96:97], v103 offset:0\n\t"
    "ds_read_b64 v[98:99], v98 offset:0\n\t"
    "s_add_u32 s86, s86, s95\n\t"
    "buffer_load_dwordx4 v[100:103], v127, %12, s86 offen offset:0 sc1\n\t"
    "v_add_u32_sdwa v126, s92, v104 dst_sel:DWORD dst_unused:UNUSED_PAD src0_sel:DWORD src1_sel:WORD_1\n\t"
    "v_add_u32_sdwa v104, s92, v104 dst_sel:DWORD dst_unused:UNUSED_PAD src0_sel:DWORD src1_sel:WORD_0\n\t"
    "v_cvt_pk_bf16_f32 v56, v56, v57\n\t"
    "v_cvt_pk_bf16_f32 v57, v58, v59\n\t"
    "v_add_u32 v79, s96, v127\n\t"
    "s_add_u32 s96, s96, 0x1000\n\t"
    "s_and_b32 s96, s96, 0x3000\n\t"
    "v_add_u32 v79, 128000, v79\n\t"
    "ds_read_b128 v[64:67], v79 offset:0\n\t"
    "ds_read_b128 v[68:71], v79 offset:1024\n\t"
    "ds_read_b128 v[72:75], v79 offset:2048\n\t"
    "ds_read_b128 v[76:79], v79 offset:3072\n\t"
    "v_mov_b64 v[116:117], 0\n\t"
    "v_mov_b64 v[118:119], 0\n\t"
    "v_cvt_pk_bf16_f32 v58, v60, v61\n\t"
    "v_cvt_pk_bf16_f32 v59, v62, v63\n\t"
    "v_mov_b64 v[60:61], 0\n\t"
    "v_mov_b64 v[62:63], 0\n\t"
    "s_cmp_eq_u32 %16, 0\n\t"
    "s_cselect_b32 s48, 2, -1\n\t"
    "s_mov_b32 s66, 4\n\t"
    "s_branch L_loop_%=\n\t"
    ".p2align 9\n\t"
    "s_nop 0\n\t"
    "s_nop 0\n\t"
    "s_nop 0\n\t"
    "s_nop 0\n\t"
    "s_nop 0\n\t"
    "s_nop 0\n\t"
    "s_nop 0\n\t"
    "s_nop 0\n\t"
    "s_nop 0\n\t"
    "s_nop 0\n\t"
    "s_nop 0\n\t"
    "s_nop 0\n\t"
    "s_nop 0\n\t"
    "s_nop 0\n\t"
    "s_nop 0\n\t"
    "s_nop 0\n\t"
    "s_nop 0\n\t"
    "s_nop 0\n\t"
    "s_nop 0\n\t"
    "s_nop 0\n\t"
    "s_nop 0\n\t"
    "s_nop 0\n\t"
    "s_nop 0\n\t"
    "s_nop 0\n\t"
    "s_nop 0\n\t"
    "s_nop 0\n\t"
    "s_nop 0\n\t"
    "s_nop 0\n\t"
    "s_nop 0\n\t"
    "s_nop 0\n\t"
    "s_nop 0\n\t"
    "s_nop 0\n\t"
    "s_nop 0\n\t"
    "L_loop_%=:\n\t"
    "s_waitcnt lgkmcnt(8)\n\t"
    "v_smfmac_f32_16x16x64_bf16 v[116:119], v[120:123], v[84:91], v124\n\t"
    "ds_read_b64 v[84:85], v104 offset:0\n\t"
    "ds_read_b64 v[86:87], v126 offset:0\n\t"
    "v_add_u32_sdwa v90, s92, v105 dst_sel:DWORD dst_unused:UNUSED_PAD src0_sel:DWORD src1_sel:WORD_1\n\t"
    "v_add_u32_sdwa v105, s92, v105 dst_sel:DWORD dst_unused:UNUSED_PAD src0_sel:DWORD src1_sel:WORD_0\n\t"
    "ds_read_b64 v[88:89], v105 offset:0\n\t"
    "ds_read_b64 v[90:91], v90 offset:0\n\t"
    "v_add_u32_sdwa v126, s92, v106 dst_sel:DWORD dst_unused:UNUSED_PAD src0_sel:DWORD src1_sel:WORD_1\n\t"
    "v_add_u32_sdwa v106, s92, v106 dst_sel:DWORD dst_unused:UNUSED_PAD src0_sel:DWORD src1_sel:WORD_0\n\t"
    "s_waitcnt vmcnt(2) lgkmcnt(8)\n\t"
    "v_smfmac_f32_16x16x64_bf16 v[116:119], v[120:123], v[92:99], v124\n\t"
    "ds_read_b64 v[92:93], v106 offset:0\n\t"
    "ds_read_b64 v[94:95], v126 offset:0\n\t"
    "v_add_u32_sdwa v98, s92, v107 dst_sel:DWORD dst_unused:UNUSED_PAD src0_sel:DWORD src1_sel:WORD_1\n\t"
    "v_add_u32_sdwa v107, s92, v107 dst_sel:DWORD dst_unused:UNUSED_PAD src0_sel:DWORD src1_sel:WORD_0\n\t"
    "ds_read_b64 v[96:97], v107 offset:0\n\t"
    "ds_read_b64 v[98:99], v98 offset:0\n\t"
    "buffer_load_dwordx4 v[104:107], v127, %12, s86 offen offset:1024 sc1\n\t"
    "v_add_u32_sdwa v126, s92, v108 dst_sel:DWORD dst_unused:UNUSED_PAD src0_sel:DWORD src1_sel:WORD_1\n\t"
    "v_cvt_pk_bf16_f32 v4, v116, v117\n\t"
    "v_cvt_pk_bf16_f32 v5, v118, v119\n\t"
    "v_and_b32 v119, s67, v7\n\t"
    "v_lshlrev_b32 v118, 16, v7\n\t"
    "v_and_b32 v117, s67, v6\n\t"
    "v_lshlrev_b32 v116, 16, v6\n\t"
    "v_add_u32_sdwa v108, s92, v108 dst_sel:DWORD dst_unused:UNUSED_PAD src0_sel:DWORD src1_sel:WORD_0\n\t"
    "s_waitcnt lgkmcnt(4)\n\t"
    "v_smfmac_f32_16x16x64_bf16 v[116:119], v[120:123], v[84:91], v124\n\t"
    "ds_read_b64 v[84:85], v108 offset:0\n\t"
    "v_mfma_f32_16x16x32_bf16 v[80:83], v[64:67], v[164:167], 0\n\t"
    "ds_read_b64 v[86:87], v126 offset:0\n\t"
    "v_mfma_f32_16x16x32_bf16 v[80:83], v[68:71], v[168:171], v[80:83]\n\t"
    "v_add_u32_sdwa v90, s92, v109 dst_sel:DWORD dst_unused:UNUSED_PAD src0_sel:DWORD src1_sel:WORD_1\n\t"
    "v_add_u32_sdwa v109, s92, v109 dst_sel:DWORD dst_unused:UNUSED_PAD src0_sel:DWORD src1_sel:WORD_0\n\t"
    "ds_read_b64 v[88:89], v109 offset:0\n\t"
    "v_mfma_f32_16x16x32_bf16 v[80:83], v[72:75], v[220:223], v[80:83]\n\t"
    "ds_read_b64 v[90:91], v90 offset:0\n\t"
    "v_mfma_f32_16x16x32_bf16 v[80:83], v[76:79], v[224:227], v[80:83]\n\t"
    "v_add_u32_sdwa v126, s92, v110 dst_sel:DWORD dst_unused:UNUSED_PAD src0_sel:DWORD src1_sel:WORD_1\n\t"
    "v_add_u32_sdwa v110, s92, v110 dst_sel:DWORD dst_unused:UNUSED_PAD src0_sel:DWORD src1_sel:WORD_0\n\t"
    "s_waitcnt vmcnt(2) lgkmcnt(4)\n\t"
    "v_smfmac_f32_16x16x64_bf16 v[116:119], v[120:123], v[92:99], v124\n\t"
    "ds_read_b64 v[92:93], v110 offset:0\n\t"
    "ds_read_b64 v[94:95], v126 offset:0\n\t"
    "v_add_u32_sdwa v98, s92, v111 dst_sel:DWORD dst_unused:UNUSED_PAD src0_sel:DWORD src1_sel:WORD_1\n\t"
    "v_add_u32_sdwa v111, s92, v111 dst_sel:DWORD dst_unused:UNUSED_PAD src0_sel:DWORD src1_sel:WORD_0\n\t"
    "ds_read_b64 v[96:97], v111 offset:0\n\t"
    "ds_read_b64 v[98:99], v98 offset:0\n\t"
    "s_add_u32 s87, s87, s95\n\t"
    "buffer_load_dwordx4 v[108:111], v127, %12, s87 offen offset:0 sc1\n\t"
    "v_add_u32_sdwa v126, s92, v112 dst_sel:DWORD dst_unused:UNUSED_PAD src0_sel:DWORD src1_sel:WORD_1\n\t"
    "v_cvt_pk_bf16_f32 v6, v116, v117\n\t"
    "v_cvt_pk_bf16_f32 v7, v118, v119\n\t"
    "v_and_b32 v119, s67, v13\n\t"
    "v_lshlrev_b32 v118, 16, v13\n\t"
    "v_and_b32 v117, s67, v12\n\t"
    "v_lshlrev_b32 v116, 16, v12\n\t"
    "v_add_u32_sdwa v112, s92, v112 dst_sel:DWORD dst_unused:UNUSED_PAD src0_sel:DWORD src1_sel:WORD_0\n\t"
    "s_waitcnt lgkmcnt(4)\n\t"
    "v_smfmac_f32_16x16x64_bf16 v[116:119], v[120:123], v[84:91], v124\n\t"
    "ds_read_b64 v[84:85], v112 offset:0\n\t"
    "ds_read_b64 v[86:87], v126 offset:0\n\t"
    "v_cvt_pk_bf16_f32 v80, v80, v81\n\t"
    "v_cvt_pk_bf16_f32 v81, v82, v83\n\t"
    "ds_write_b64 v125, v[80:81] offset:0\n\t"
    "v_mfma_f32_16x16x32_bf16 v[80:83], v[64:67], v[172:175], 0\n\t"
    "v_add_u32_sdwa v90, s92, v113 dst_sel:DWORD dst_unused:UNUSED_PAD src0_sel:DWORD src1_sel:WORD_1\n\t"
    "v_add_u32_sdwa v113, s92, v113 dst_sel:DWORD dst_unused:UNUSED_PAD src0_sel:DWORD src1_sel:WORD_0\n\t"
    "ds_read_b64 v[88:89], v113 offset:0\n\t"
    "v_mfma_f32_16x16x32_bf16 v[80:83], v[68:71], v[176:179], v[80:83]\n\t"
    "ds_read_b64 v[90:91], v90 offset:0\n\t"
    "v_mfma_f32_16x16x32_bf16 v[80:83], v[72:75], v[228:231], v[80:83]\n\t"
    "v_mfma_f32_16x16x32_bf16 v[80:83], v[76:79], v[232:235], v[80:83]\n\t"
    "v_add_u32_sdwa v126, s92, v114 dst_sel:DWORD dst_unused:UNUSED_PAD src0_sel:DWORD src1_sel:WORD_1\n\t"
    "v_add_u32_sdwa v114, s92, v114 dst_sel:DWORD dst_unused:UNUSED_PAD src0_sel:DWORD src1_sel:WORD_0\n\t"
    "s_waitcnt vmcnt(2) lgkmcnt(5)\n\t"
    "v_smfmac_f32_16x16x64_bf16 v[116:119], v[120:123], v[92:99], v124\n\t"
    "ds_read_b64 v[92:93], v114 offset:0\n\t"
    "ds_read_b64 v[94:95], v126 offset:0\n\t"
    "v_add_u32_sdwa v98, s92, v115 dst_sel:DWORD dst_unused:UNUSED_PAD src0_sel:DWORD src1_sel:WORD_1\n\t"
    "v_add_u32_sdwa v115, s92, v115 dst_sel:DWORD dst_unused:UNUSED_PAD src0_sel:DWORD src1_sel:WORD_0\n\t"
    "ds_read_b64 v[96:97], v115 offset:0\n\t"
    "ds_read_b64 v[98:99], v98 offset:0\n\t"
    "buffer_load_dwordx4 v[112:115], v127, %12, s87 offen offset:1024 sc1\n\t"
    "v_add_u32_sdwa v126, s92, v100 dst_sel:DWORD dst_unused:UNUSED_PAD src0_sel:DWORD src1_sel:WORD_1\n\t"
    "v_cvt_pk_bf16_f32 v12, v116, v117\n\t"
    "v_cvt_pk_bf16_f32 v13, v118, v119\n\t"
    "v_and_b32 v119, s67, v15\n\t"
    "v_lshlrev_b32 v118, 16, v15\n\t"
    "v_and_b32 v117, s67, v14\n\t"
    "v_lshlrev_b32 v116, 16, v14\n\t"
    "v_add_u32_sdwa v100, s92, v100 dst_sel:DWORD dst_unused:UNUSED_PAD src0_sel:DWORD src1_sel:WORD_0\n\t"
    "s_waitcnt lgkmcnt(4)\n\t"
    "v_smfmac_f32_16x16x64_bf16 v[116:119], v[120:123], v[84:91], v124\n\t"
    "ds_read_b64 v[84:85], v100 offset:0\n\t"
    "ds_read_b64 v[86:87], v126 offset:0\n\t"
    "v_cvt_pk_bf16_f32 v80, v80, v81\n\t"
    "v_cvt_pk_bf16_f32 v81, v82, v83\n\t"
    "ds_write_b64 v125, v[80:81] offset:128\n\t"
    "v_mfma_f32_16x16x32_bf16 v[80:83], v[64:67], v[180:183], 0\n\t"
    "v_add_u32_sdwa v90, s92, v101 dst_sel:DWORD dst_unused:UNUSED_PAD src0_sel:DWORD src1_sel:WORD_1\n\t"
    "v_add_u32_sdwa v101, s92, v101 dst_sel:DWORD dst_unused:UNUSED_PAD src0_sel:DWORD src1_sel:WORD_0\n\t"
    "ds_read_b64 v[88:89], v101 offset:0\n\t"
    "v_mfma_f32_16x16x32_bf16 v[80:83], v[68:71], v[184:187], v[80:83]\n\t"
    "ds_read_b64 v[90:91], v90 offset:0\n\t"
    "v_mfma_f32_16x16x32_bf16 v[80:83], v[72:75], v[236:239], v[80:83]\n\t"
    "v_mfma_f32_16x16x32_bf16 v[80:83], v[76:79], v[240:243], v[80:83]\n\t"
    "v_add_u32_sdwa v126, s92, v102 dst_sel:DWORD dst_unused:UNUSED_PAD src0_sel:DWORD src1_sel:WORD_1\n\t"
    "v_add_u32_sdwa v102, s92, v102 dst_sel:DWORD dst_unused:UNUSED_PAD src0_sel:DWORD src1_sel:WORD_0\n\t"
    "s_waitcnt vmcnt(2) lgkmcnt(5)\n\t"
    "v_smfmac_f32_16x16x64_bf16 v[116:119], v[120:123], v[92:99], v124\n\t"
    "ds_read_b64 v[92:93], v102 offset:0\n\t"
    "ds_read_b64 v[94:95], v126 offset:0\n\t"
    "v_add_u32_sdwa v98, s92, v103 dst_sel:DWORD dst_unused:UNUSED_PAD src0_sel:DWORD src1_sel:WORD_1\n\t"
    "v_add_u32_sdwa v103, s92, v103 dst_sel:DWORD dst_unused:UNUSED_PAD src0_sel:DWORD src1_sel:WORD_0\n\t"
    "ds_read_b64 v[96:97], v103 offset:0\n\t"
    "ds_read_b64 v[98:99], v98 offset:0\n\t"
    "s_add_u32 s88, s88, s95\n\t"
    "buffer_load_dwordx4 v[100:103], v127, %12, s88 offen offset:0 sc1\n\t"
    "v_add_u32_sdwa v126, s92, v104 dst_sel:DWORD dst_unused:UNUSED_PAD src0_sel:DWORD src1_sel:WORD_1\n\t"
    "v_cvt_pk_bf16_f32 v14, v116, v117\n\t"
    "v_cvt_pk_bf16_f32 v15, v118, v119\n\t"
    "v_and_b32 v119, s67, v21\n\t"
    "v_lshlrev_b32 v118, 16, v21\n\t"
    "v_and_b32 v117, s67, v20\n\t"
    "v_lshlrev_b32 v116, 16, v20\n\t"
    "v_add_u32_sdwa v104, s92, v104 dst_sel:DWORD dst_unused:UNUSED_PAD src0_sel:DWORD src1_sel:WORD_0\n\t"
    "s_waitcnt lgkmcnt(4)\n\t"
    "v_smfmac_f32_16x16x64_bf16 v[116:119], v[120:123], v[84:91], v124\n\t"
    "ds_read_b64 v[84:85], v104 offset:0\n\t"
    "ds_read_b64 v[86:87], v126 offset:0\n\t"
    "v_cvt_pk_bf16_f32 v80, v80, v81\n\t"
    "v_cvt_pk_bf16_f32 v81, v82, v83\n\t"
    "ds_write_b64 v125, v[80:81] offset:256\n\t"
    "v_add_u32_sdwa v90, s92, v105 dst_sel:DWORD dst_unused:UNUSED_PAD src0_sel:DWORD src1_sel:WORD_1\n\t"
    "v_add_u32_sdwa v105, s92, v105 dst_sel:DWORD dst_unused:UNUSED_PAD src0_sel:DWORD src1_sel:WORD_0\n\t"
    "ds_read_b64 v[88:89], v105 offset:0\n\t"
    "ds_read_b64 v[90:91], v90 offset:0\n\t"
    "v_add_u32_sdwa v126, s92, v106 dst_sel:DWORD dst_unused:UNUSED_PAD src0_sel:DWORD src1_sel:WORD_1\n\t"
    "v_add_u32_sdwa v106, s92, v106 dst_sel:DWORD dst_unused:UNUSED_PAD src0_sel:DWORD src1_sel:WORD_0\n\t"
    "s_waitcnt vmcnt(2) lgkmcnt(5)\n\t"
    "v_smfmac_f32_16x16x64_bf16 v[116:119], v[120:123], v[92:99], v124\n\t"
    "ds_read_b64 v[92:93], v106 offset:0\n\t"
    "ds_read_b64 v[94:95], v126 offset:0\n\t"
    "v_add_u32_sdwa v98, s92, v107 dst_sel:DWORD dst_unused:UNUSED_PAD src0_sel:DWORD src1_sel:WORD_1\n\t"
    "v_add_u32_sdwa v107, s92, v107 dst_sel:DWORD dst_unused:UNUSED_PAD src0_sel:DWORD src1_sel:WORD_0\n\t"
    "ds_read_b64 v[96:97], v107 offset:0\n\t"
    "ds_read_b64 v[98:99], v98 offset:0\n\t"
    "buffer_load_dwordx4 v[104:107], v127, %12, s88 offen offset:1024 sc1\n\t"
    "v_add_u32_sdwa v126, s92, v108 dst_sel:DWORD dst_unused:UNUSED_PAD src0_sel:DWORD src1_sel:WORD_1\n\t"
    "v_cvt_pk_bf16_f32 v20, v116, v117\n\t"
    "v_cvt_pk_bf16_f32 v21, v118, v119\n\t"
    "v_and_b32 v119, s67, v23\n\t"
    "v_lshlrev_b32 v118, 16, v23\n\t"
    "v_and_b32 v117, s67, v22\n\t"
    "v_lshlrev_b32 v116, 16, v22\n\t"
    "v_add_u32_sdwa v108, s92, v108 dst_sel:DWORD dst_unused:UNUSED_PAD src0_sel:DWORD src1_sel:WORD_0\n\t"
    "s_waitcnt lgkmcnt(4)\n\t"
    "v_smfmac_f32_16x16x64_bf16 v[116:119], v[120:123], v[84:91], v124\n\t"
    "ds_read_b64 v[84:85], v108 offset:0\n\t"
    "v_mfma_f32_16x16x32_bf16 v[80:83], v[64:67], v[188:191], 0\n\t"
    "ds_read_b64 v[86:87], v126 offset:0\n\t"
    "v_mfma_f32_16x16x32_bf16 v[80:83], v[68:71], v[192:195], v[80:83]\n\t"
    "v_add_u32_sdwa v90, s92, v109 dst_sel:DWORD dst_unused:UNUSED_PAD src0_sel:DWORD src1_sel:WORD_1\n\t"
    "v_add_u32_sdwa v109, s92, v109 dst_sel:DWORD dst_unused:UNUSED_PAD src0_sel:DWORD src1_sel:WORD_0\n\t"
    "ds_read_b64 v[88:89], v109 offset:0\n\t"
    "v_mfma_f32_16x16x32_bf16 v[80:83], v[72:75], v[244:247], v[80:83]\n\t"
    "ds_read_b64 v[90:91], v90 offset:0\n\t"
    "v_mfma_f32_16x16x32_bf16 v[80:83], v[76:79], v[248:251], v[80:83]\n\t"
    "v_add_u32_sdwa v126, s92, v110 dst_sel:DWORD dst_unused:UNUSED_PAD src0_sel:DWORD src1_sel:WORD_1\n\t"
    "v_add_u32_sdwa v110, s92, v110 dst_sel:DWORD dst_unused:UNUSED_PAD src0_sel:DWORD src1_sel:WORD_0\n\t"
    "s_waitcnt vmcnt(2) lgkmcnt(4)\n\t"
    "v_smfmac_f32_16x16x64_bf16 v[116:119], v[120:123], v[92:99], v124\n\t"
    "ds_read_b64 v[92:93], v110 offset:0\n\t"
    "ds_read_b64 v[94:95], v126 offset:0\n\t"
    "v_add_u32_sdwa v98, s92, v111 dst_sel:DWORD dst_unused:UNUSED_PAD src0_sel:DWORD src1_sel:WORD_1\n\t"
    "v_add_u32_sdwa v111, s92, v111 dst_sel:DWORD dst_unused:UNUSED_PAD src0_sel:DWORD src1_sel:WORD_0\n\t"
    "ds_read_b64 v[96:97], v111 offset:0\n\t"
    "ds_read_b64 v[98:99], v98 offset:0\n\t"
    "s_add_u32 s89, s89, s95\n\t"
    "buffer_load_dwordx4 v[108:111], v127, %12, s89 offen offset:0 sc1\n\t"
    "v_add_u32_sdwa v126, s92, v112 dst_sel:DWORD dst_unused:UNUSED_PAD src0_sel:DWORD src1_sel:WORD_1\n\t"
    "v_cvt_pk_bf16_f32 v22, v116, v117\n\t"
    "v_cvt_pk_bf16_f32 v23, v118, v119\n\t"
    "v_and_b32 v119, s67, v29\n\t"
    "v_lshlrev_b32 v118, 16, v29\n\t"
    "v_and_b32 v117, s67, v28\n\t"
    "v_lshlrev_b32 v116, 16, v28\n\t"
    "v_add_u32_sdwa v112, s92, v112 dst_sel:DWORD dst_unused:UNUSED_PAD src0_sel:DWORD src1_sel:WORD_0\n\t"
    "s_waitcnt lgkmcnt(4)\n\t"
    "v_smfmac_f32_16x16x64_bf16 v[116:119], v[120:123], v[84:91], v124\n\t"
    "ds_read_b64 v[84:85], v112 offset:0\n\t"
    "ds_read_b64 v[86:87], v126 offset:0\n\t"
    "v_cvt_pk_bf16_f32 v80, v80, v81\n\t"
    "v_cvt_pk_bf16_f32 v81, v82, v83\n\t"
    "ds_write_b64 v125, v[80:81] offset:384\n\t"
    "v_add_u32_sdwa v90, s92, v113 dst_sel:DWORD dst_unused:UNUSED_PAD src0_sel:DWORD src1_sel:WORD_1\n\t"
    "v_add_u32_sdwa v113, s92, v113 dst_sel:DWORD dst_unused:UNUSED_PAD src0_sel:DWORD src1_sel:WORD_0\n\t"
    "ds_read_b64 v[88:89], v113 offset:0\n\t"
    "ds_read_b64 v[90:91], v90 offset:0\n\t"
    "v_add_u32_sdwa v126, s92, v114 dst_sel:DWORD dst_unused:UNUSED_PAD src0_sel:DWORD src1_sel:WORD_1\n\t"
    "v_add_u32_sdwa v114, s92, v114 dst_sel:DWORD dst_unused:UNUSED_PAD src0_sel:DWORD src1_sel:WORD_0\n\t"
    "s_waitcnt vmcnt(2) lgkmcnt(5)\n\t"
    "v_smfmac_f32_16x16x64_bf16 v[116:119], v[120:123], v[92:99], v124\n\t"
    "ds_read_b64 v[92:93], v114 offset:0\n\t"
    "ds_read_b64 v[94:95], v126 offset:0\n\t"
    "v_add_u32_sdwa v98, s92, v115 dst_sel:DWORD dst_unused:UNUSED_PAD src0_sel:DWORD src1_sel:WORD_1\n\t"
    "v_add_u32_sdwa v115, s92, v115 dst_sel:DWORD dst_unused:UNUSED_PAD src0_sel:DWORD src1_sel:WORD_0\n\t"
    "ds_read_b64 v[96:97], v115 offset:0\n\t"
    "ds_read_b64 v[98:99], v98 offset:0\n\t"
    "buffer_load_dwordx4 v[112:115], v127, %12, s89 offen offset:1024 sc1\n\t"
    "v_add_u32_sdwa v126, s92, v100 dst_sel:DWORD dst_unused:UNUSED_PAD src0_sel:DWORD src1_sel:WORD_1\n\t"
    "v_cvt_pk_bf16_f32 v28, v116, v117\n\t"
    "v_cvt_pk_bf16_f32 v29, v118, v119\n\t"
    "v_and_b32 v119, s67, v31\n\t"
    "v_lshlrev_b32 v118, 16, v31\n\t"
    "v_and_b32 v117, s67, v30\n\t"
    "v_lshlrev_b32 v116, 16, v30\n\t"
    "v_add_u32_sdwa v100, s92, v100 dst_sel:DWORD dst_unused:UNUSED_PAD src0_sel:DWORD src1_sel:WORD_0\n\t"
    "s_waitcnt lgkmcnt(4)\n\t"
    "v_smfmac_f32_16x16x64_bf16 v[116:119], v[120:123], v[84:91], v124\n\t"
    "ds_read_b64 v[84:85], v100 offset:0\n\t"
    "v_mfma_f32_16x16x32_bf16 v[80:83], v[64:67], v[196:199], 0\n\t"
    "ds_read_b64 v[86:87], v126 offset:0\n\t"
    "v_mfma_f32_16x16x32_bf16 v[80:83], v[68:71], v[200:203], v[80:83]\n\t"
    "v_add_u32_sdwa v90, s92, v101 dst_sel:DWORD dst_unused:UNUSED_PAD src0_sel:DWORD src1_sel:WORD_1\n\t"
    "v_add_u32_sdwa v101, s92, v101 dst_sel:DWORD dst_unused:UNUSED_PAD src0_sel:DWORD src1_sel:WORD_0\n\t"
    "ds_read_b64 v[88:89], v101 offset:0\n\t"
    "v_mfma_f32_16x16x32_bf16 v[80:83], v[72:75], v[252:255], v[80:83]\n\t"
    "ds_read_b64 v[90:91], v90 offset:0\n\t"
    "v_mfma_f32_16x16x32_bf16 v[80:83], v[76:79], v[128:131], v[80:83]\n\t"
    "v_add_u32_sdwa v126, s92, v102 dst_sel:DWORD dst_unused:UNUSED_PAD src0_sel:DWORD src1_sel:WORD_1\n\t"
    "v_add_u32_sdwa v102, s92, v102 dst_sel:DWORD dst_unused:UNUSED_PAD src0_sel:DWORD src1_sel:WORD_0\n\t"
    "s_xor_b32 s50, s50, 0x2000\n\t"
    "s_add_u32 m0, s50, 128000\n\t"
    "s_add_u32 s63, s63, 0x4000\n\t"
    "buffer_load_dwordx4 v127, %12, s63 offen sc1 lds\n\t"
    "s_waitcnt vmcnt(3) lgkmcnt(4)\n\t"
    "v_smfmac_f32_16x16x64_bf16 v[116:119], v[120:123], v[92:99], v124\n\t"
    "ds_read_b64 v[92:93], v102 offset:0\n\t"
    "ds_read_b64 v[94:95], v126 offset:0\n\t"
    "v_add_u32_sdwa v98, s92, v103 dst_sel:DWORD dst_unused:UNUSED_PAD src0_sel:DWORD src1_sel:WORD_1\n\t"
    "v_add_u32_sdwa v103, s92, v103 dst_sel:DWORD dst_unused:UNUSED_PAD src0_sel:DWORD src1_sel:WORD_0\n\t"
    "ds_read_b64 v[96:97], v103 offset:0\n\t"
    "ds_read_b64 v[98:99], v98 offset:0\n\t"
    "s_add_u32 s90, s90, s95\n\t"
    "buffer_load_dwordx4 v[100:103], v127, %12, s90 offen offset:0 sc1\n\t"
    "v_add_u32_sdwa v126, s92, v104 dst_sel:DWORD dst_unused:UNUSED_PAD src0_sel:DWORD src1_sel:WORD_1\n\t"
    "v_cvt_pk_bf16_f32 v30, v116, v117\n\t"
    "v_cvt_pk_bf16_f32 v31, v118, v119\n\t"
    "v_and_b32 v119, s67, v37\n\t"
    "v_lshlrev_b32 v118, 16, v37\n\t"
    "v_and_b32 v117, s67, v36\n\t"
    "v_lshlrev_b32 v116, 16, v36\n\t"
    "v_add_u32_sdwa v104, s92, v104 dst_sel:DWORD dst_unused:UNUSED_PAD src0_sel:DWORD src1_sel:WORD_0\n\t"
    "s_waitcnt lgkmcnt(4)\n\t"
    "v_smfmac_f32_16x16x64_bf16 v[116:119], v[120:123], v[84:91], v124\n\t"
    "ds_read_b64 v[84:85], v104 offset:0\n\t"
    "ds_read_b64 v[86:87], v126 offset:0\n\t"
    "v_cvt_pk_bf16_f32 v80, v80, v81\n\t"
    "v_cvt_pk_bf16_f32 v81, v82, v83\n\t"
    "ds_write_b64 v125, v[80:81] offset:512\n\t"
    "v_add_u32_sdwa v90, s92, v105 dst_sel:DWORD dst_unused:UNUSED_PAD src0_sel:DWORD src1_sel:WORD_1\n\t"
    "v_add_u32_sdwa v105, s92, v105 dst_sel:DWORD dst_unused:UNUSED_PAD src0_sel:DWORD src1_sel:WORD_0\n\t"
    "ds_read_b64 v[88:89], v105 offset:0\n\t"
    "ds_read_b64 v[90:91], v90 offset:0\n\t"
    "v_add_u32_sdwa v126, s92, v106 dst_sel:DWORD dst_unused:UNUSED_PAD src0_sel:DWORD src1_sel:WORD_1\n\t"
    "v_add_u32_sdwa v106, s92, v106 dst_sel:DWORD dst_unused:UNUSED_PAD src0_sel:DWORD src1_sel:WORD_0\n\t"
    "s_waitcnt vmcnt(3) lgkmcnt(5)\n\t"
    "v_smfmac_f32_16x16x64_bf16 v[116:119], v[120:123], v[92:99], v124\n\t"
    "ds_read_b64 v[92:93], v106 offset:0\n\t"
    "ds_read_b64 v[94:95], v126 offset:0\n\t"
    "v_add_u32_sdwa v98, s92, v107 dst_sel:DWORD dst_unused:UNUSED_PAD src0_sel:DWORD src1_sel:WORD_1\n\t"
    "v_add_u32_sdwa v107, s92, v107 dst_sel:DWORD dst_unused:UNUSED_PAD src0_sel:DWORD src1_sel:WORD_0\n\t"
    "ds_read_b64 v[96:97], v107 offset:0\n\t"
    "ds_read_b64 v[98:99], v98 offset:0\n\t"
    "buffer_load_dwordx4 v[104:107], v127, %12, s90 offen offset:1024 sc1\n\t"
    "v_add_u32_sdwa v126, s92, v108 dst_sel:DWORD dst_unused:UNUSED_PAD src0_sel:DWORD src1_sel:WORD_1\n\t"
    "v_cvt_pk_bf16_f32 v36, v116, v117\n\t"
    "v_cvt_pk_bf16_f32 v37, v118, v119\n\t"
    "v_and_b32 v119, s67, v39\n\t"
    "v_lshlrev_b32 v118, 16, v39\n\t"
    "v_and_b32 v117, s67, v38\n\t"
    "v_lshlrev_b32 v116, 16, v38\n\t"
    "v_add_u32_sdwa v108, s92, v108 dst_sel:DWORD dst_unused:UNUSED_PAD src0_sel:DWORD src1_sel:WORD_0\n\t"
    "s_waitcnt lgkmcnt(4)\n\t"
    "v_smfmac_f32_16x16x64_bf16 v[116:119], v[120:123], v[84:91], v124\n\t"
    "ds_read_b64 v[84:85], v108 offset:0\n\t"
    "v_mfma_f32_16x16x32_bf16 v[80:83], v[64:67], v[204:207], 0\n\t"
    "ds_read_b64 v[86:87], v126 offset:0\n\t"
    "v_mfma_f32_16x16x32_bf16 v[80:83], v[68:71], v[208:211], v[80:83]\n\t"
    "v_add_u32_sdwa v90, s92, v109 dst_sel:DWORD dst_unused:UNUSED_PAD src0_sel:DWORD src1_sel:WORD_1\n\t"
    "v_add_u32_sdwa v109, s92, v109 dst_sel:DWORD dst_unused:UNUSED_PAD src0_sel:DWORD src1_sel:WORD_0\n\t"
    "ds_read_b64 v[88:89], v109 offset:0\n\t"
    "v_mfma_f32_16x16x32_bf16 v[80:83], v[72:75], v[132:135], v[80:83]\n\t"
    "ds_read_b64 v[90:91], v90 offset:0\n\t"
    "v_mfma_f32_16x16x32_bf16 v[80:83], v[76:79], v[136:139], v[80:83]\n\t"
    "v_add_u32_sdwa v126, s92, v110 dst_sel:DWORD dst_unused:UNUSED_PAD src0_sel:DWORD src1_sel:WORD_1\n\t"
    "v_add_u32_sdwa v110, s92, v110 dst_sel:DWORD dst_unused:UNUSED_PAD src0_sel:DWORD src1_sel:WORD_0\n\t"
    "s_cmp_lt_u32 %16, 4\n\t"
    "s_cbranch_scc1 L_tp19_%=\n\t"
    "s_setprio 0\n\t"
    "L_tp19_%=:\n\t"
    "s_waitcnt vmcnt(3) lgkmcnt(4)\n\t"
    "v_smfmac_f32_16x16x64_bf16 v[116:119], v[120:123], v[92:99], v124\n\t"
    "ds_read_b64 v[92:93], v110 offset:0\n\t"
    "ds_read_b64 v[94:95], v126 offset:0\n\t"
    "v_add_u32_sdwa v98, s92, v111 dst_sel:DWORD dst_unused:UNUSED_PAD src0_sel:DWORD src1_sel:WORD_1\n\t"
    "v_add_u32_sdwa v111, s92, v111 dst_sel:DWORD dst_unused:UNUSED_PAD src0_sel:DWORD src1_sel:WORD_0\n\t"
    "ds_read_b64 v[96:97], v111 offset:0\n\t"
    "ds_read_b64 v[98:99], v98 offset:0\n\t"
    "s_add_u32 s91, s91, s95\n\t"
    "buffer_load_dwordx4 v[108:111], v127, %12, s91 offen offset:0 sc1\n\t"
    "v_add_u32_sdwa v126, s92, v112 dst_sel:DWORD dst_unused:UNUSED_PAD src0_sel:DWORD src1_sel:WORD_1\n\t"
    "v_cvt_pk_bf16_f32 v38, v116, v117\n\t"
    "v_cvt_pk_bf16_f32 v39, v118, v119\n\t"
    "v_and_b32 v119, s67, v45\n\t"
    "v_lshlrev_b32 v118, 16, v45\n\t"
    "v_and_b32 v117, s67, v44\n\t"
    "v_lshlrev_b32 v116, 16, v44\n\t"
    "v_add_u32_sdwa v112, s92, v112 dst_sel:DWORD dst_unused:UNUSED_PAD src0_sel:DWORD src1_sel:WORD_0\n\t"
    "s_waitcnt lgkmcnt(4)\n\t"
    "v_smfmac_f32_16x16x64_bf16 v[116:119], v[120:123], v[84:91], v124\n\t"
    "ds_read_b64 v[84:85], v112 offset:0\n\t"
    "ds_read_b64 v[86:87], v126 offset:0\n\t"
    "v_cvt_pk_bf16_f32 v80, v80, v81\n\t"
    "v_cvt_pk_bf16_f32 v81, v82, v83\n\t"
    "ds_write_b64 v125, v[80:81] offset:640\n\t"
    "v_add_u32_sdwa v90, s92, v113 dst_sel:DWORD dst_unused:UNUSED_PAD src0_sel:DWORD src1_sel:WORD_1\n\t"
    "v_add_u32_sdwa v113, s92, v113 dst_sel:DWORD dst_unused:UNUSED_PAD src0_sel:DWORD src1_sel:WORD_0\n\t"
    "ds_read_b64 v[88:89], v113 offset:0\n\t"
    "ds_read_b64 v[90:91], v90 offset:0\n\t"
    "v_add_u32_sdwa v126, s92, v114 dst_sel:DWORD dst_unused:UNUSED_PAD src0_sel:DWORD src1_sel:WORD_1\n\t"
    "v_add_u32_sdwa v114, s92, v114 dst_sel:DWORD dst_unused:UNUSED_PAD src0_sel:DWORD src1_sel:WORD_0\n\t"
    "s_waitcnt vmcnt(2) lgkmcnt(5)\n\t"
    "v_smfmac_f32_16x16x64_bf16 v[116:119], v[120:123], v[92:99], v124\n\t"
    "ds_read_b64 v[92:93], v114 offset:0\n\t"
    "ds_read_b64 v[94:95], v126 offset:0\n\t"
    "v_add_u32_sdwa v98, s92, v115 dst_sel:DWORD dst_unused:UNUSED_PAD src0_sel:DWORD src1_sel:WORD_1\n\t"
    "v_add_u32_sdwa v115, s92, v115 dst_sel:DWORD dst_unused:UNUSED_PAD src0_sel:DWORD src1_sel:WORD_0\n\t"
    "ds_read_b64 v[96:97], v115 offset:0\n\t"
    "ds_read_b64 v[98:99], v98 offset:0\n\t"
    "buffer_load_dwordx4 v[112:115], v127, %12, s91 offen offset:1024 sc1\n\t"
    "v_add_u32_sdwa v126, s92, v100 dst_sel:DWORD dst_unused:UNUSED_PAD src0_sel:DWORD src1_sel:WORD_1\n\t"
    "v_cvt_pk_bf16_f32 v44, v116, v117\n\t"
    "v_cvt_pk_bf16_f32 v45, v118, v119\n\t"
    "v_and_b32 v119, s67, v47\n\t"
    "v_lshlrev_b32 v118, 16, v47\n\t"
    "v_and_b32 v117, s67, v46\n\t"
    "v_lshlrev_b32 v116, 16, v46\n\t"
    "v_add_u32_sdwa v100, s92, v100 dst_sel:DWORD dst_unused:UNUSED_PAD src0_sel:DWORD src1_sel:WORD_0\n\t"
    "s_waitcnt lgkmcnt(4)\n\t"
    "v_smfmac_f32_16x16x64_bf16 v[116:119], v[120:123], v[84:91], v124\n\t"
    "ds_read_b64 v[84:85], v100 offset:0\n\t"
    "v_mfma_f32_16x16x32_bf16 v[80:83], v[64:67], v[156:159], 0\n\t"
    "ds_read_b64 v[86:87], v126 offset:0\n\t"
    "v_mfma_f32_16x16x32_bf16 v[80:83], v[68:71], v[160:163], v[80:83]\n\t"
    "v_add_u32_sdwa v90, s92, v101 dst_sel:DWORD dst_unused:UNUSED_PAD src0_sel:DWORD src1_sel:WORD_1\n\t"
    "v_add_u32_sdwa v101, s92, v101 dst_sel:DWORD dst_unused:UNUSED_PAD src0_sel:DWORD src1_sel:WORD_0\n\t"
    "ds_read_b64 v[88:89], v101 offset:0\n\t"
    "v_mfma_f32_16x16x32_bf16 v[80:83], v[72:75], v[140:143], v[80:83]\n\t"
    "ds_read_b64 v[90:91], v90 offset:0\n\t"
    "v_mfma_f32_16x16x32_bf16 v[80:83], v[76:79], v[144:147], v[80:83]\n\t"
    "v_add_u32_sdwa v126, s92, v102 dst_sel:DWORD dst_unused:UNUSED_PAD src0_sel:DWORD src1_sel:WORD_1\n\t"
    "v_add_u32_sdwa v102, s92, v102 dst_sel:DWORD dst_unused:UNUSED_PAD src0_sel:DWORD src1_sel:WORD_0\n\t"
    "s_waitcnt vmcnt(2) lgkmcnt(4)\n\t"
    "v_smfmac_f32_16x16x64_bf16 v[116:119], v[120:123], v[92:99], v124\n\t"
    "ds_read_b64 v[92:93], v102 offset:0\n\t"
    "ds_read_b64 v[94:95], v126 offset:0\n\t"
    "v_add_u32_sdwa v98, s92, v103 dst_sel:DWORD dst_unused:UNUSED_PAD src0_sel:DWORD src1_sel:WORD_1\n\t"
    "v_add_u32_sdwa v103, s92, v103 dst_sel:DWORD dst_unused:UNUSED_PAD src0_sel:DWORD src1_sel:WORD_0\n\t"
    "ds_read_b64 v[96:97], v103 offset:0\n\t"
    "ds_read_b64 v[98:99], v98 offset:0\n\t"
    "s_add_u32 s84, s84, s94\n\t"
    "buffer_load_dwordx4 v[100:103], v127, %12, s84 offen offset:0 sc1\n\t"
    "v_add_u32_sdwa v126, s92, v104 dst_sel:DWORD dst_unused:UNUSED_PAD src0_sel:DWORD src1_sel:WORD_1\n\t"
    "v_cvt_pk_bf16_f32 v46, v116, v117\n\t"
    "v_cvt_pk_bf16_f32 v47, v118, v119\n\t"
    "v_and_b32 v119, s67, v53\n\t"
    "v_lshlrev_b32 v118, 16, v53\n\t"
    "v_and_b32 v117, s67, v52\n\t"
    "v_lshlrev_b32 v116, 16, v52\n\t"
    "v_add_u32_sdwa v104, s92, v104 dst_sel:DWORD dst_unused:UNUSED_PAD src0_sel:DWORD src1_sel:WORD_0\n\t"
    "s_waitcnt lgkmcnt(4)\n\t"
    "v_smfmac_f32_16x16x64_bf16 v[116:119], v[120:123], v[84:91], v124\n\t"
    "ds_read_b64 v[84:85], v104 offset:0\n\t"
    "ds_read_b64 v[86:87], v126 offset:0\n\t"
    "v_cvt_pk_bf16_f32 v80, v80, v81\n\t"
    "v_cvt_pk_bf16_f32 v81, v82, v83\n\t"
    "ds_write_b64 v125, v[80:81] offset:768\n\t"
    "v_add_u32_sdwa v90, s92, v105 dst_sel:DWORD dst_unused:UNUSED_PAD src0_sel:DWORD src1_sel:WORD_1\n\t"
    "v_add_u32_sdwa v105, s92, v105 dst_sel:DWORD dst_unused:UNUSED_PAD src0_sel:DWORD src1_sel:WORD_0\n\t"
    "ds_read_b64 v[88:89], v105 offset:0\n\t"
    "ds_read_b64 v[90:91], v90 offset:0\n\t"
    "v_add_u32_sdwa v126, s92, v106 dst_sel:DWORD dst_unused:UNUSED_PAD src0_sel:DWORD src1_sel:WORD_1\n\t"
    "v_add_u32_sdwa v106, s92, v106 dst_sel:DWORD dst_unused:UNUSED_PAD src0_sel:DWORD src1_sel:WORD_0\n\t"
    "s_waitcnt vmcnt(2) lgkmcnt(5)\n\t"
    "v_smfmac_f32_16x16x64_bf16 v[116:119], v[120:123], v[92:99], v124\n\t"
    "ds_read_b64 v[92:93], v106 offset:0\n\t"
    "ds_read_b64 v[94:95], v126 offset:0\n\t"
    "v_add_u32_sdwa v98, s92, v107 dst_sel:DWORD dst_unused:UNUSED_PAD src0_sel:DWORD src1_sel:WORD_1\n\t"
    "v_add_u32_sdwa v107, s92, v107 dst_sel:DWORD dst_unused:UNUSED_PAD src0_sel:DWORD src1_sel:WORD_0\n\t"
    "ds_read_b64 v[96:97], v107 offset:0\n\t"
    "ds_read_b64 v[98:99], v98 offset:0\n\t"
    "buffer_load_dwordx4 v[104:107], v127, %12, s84 offen offset:1024 sc1\n\t"
    "v_add_u32_sdwa v126, s92, v108 dst_sel:DWORD dst_unused:UNUSED_PAD src0_sel:DWORD src1_sel:WORD_1\n\t"
    "v_cvt_pk_bf16_f32 v52, v116, v117\n\t"
    "v_cvt_pk_bf16_f32 v53, v118, v119\n\t"
    "v_and_b32 v119, s67, v55\n\t"
    "v_lshlrev_b32 v118, 16, v55\n\t"
    "v_and_b32 v117, s67, v54\n\t"
    "v_lshlrev_b32 v116, 16, v54\n\t"
    "v_add_u32_sdwa v108, s92, v108 dst_sel:DWORD dst_unused:UNUSED_PAD src0_sel:DWORD src1_sel:WORD_0\n\t"
    "s_waitcnt lgkmcnt(4)\n\t"
    "v_smfmac_f32_16x16x64_bf16 v[116:119], v[120:123], v[84:91], v124\n\t"
    "ds_read_b64 v[84:85], v108 offset:0\n\t"
    "v_mfma_f32_16x16x32_bf16 v[80:83], v[64:67], v[212:215], 0\n\t"
    "ds_read_b64 v[86:87], v126 offset:0\n\t"
    "v_mfma_f32_16x16x32_bf16 v[80:83], v[68:71], v[216:219], v[80:83]\n\t"
    "v_add_u32_sdwa v90, s92, v109 dst_sel:DWORD dst_unused:UNUSED_PAD src0_sel:DWORD src1_sel:WORD_1\n\t"
    "v_add_u32_sdwa v109, s92, v109 dst_sel:DWORD dst_unused:UNUSED_PAD src0_sel:DWORD src1_sel:WORD_0\n\t"
    "ds_read_b64 v[88:89], v109 offset:0\n\t"
    "v_mfma_f32_16x16x32_bf16 v[80:83], v[72:75], v[148:151], v[80:83]\n\t"
    "ds_read_b64 v[90:91], v90 offset:0\n\t"
    "v_mfma_f32_16x16x32_bf16 v[80:83], v[76:79], v[152:155], v[80:83]\n\t"
    "v_add_u32 v79, s96, v127\n\t"
    "s_add_u32 s96, s96, 0x1000\n\t"
    "s_and_b32 s96, s96, 0x3000\n\t"
    "v_add_u32 v79, 128000, v79\n\t"
    "ds_read_b128 v[64:67], v79 offset:0\n\t"
    "ds_read_b128 v[68:71], v79 offset:1024\n\t"
    "ds_read_b128 v[72:75], v79 offset:2048\n\t"
    "ds_read_b128 v[76:79], v79 offset:3072\n\t"
    "v_add_u32_sdwa v126, s92, v110 dst_sel:DWORD dst_unused:UNUSED_PAD src0_sel:DWORD src1_sel:WORD_1\n\t"
    "v_add_u32_sdwa v110, s92, v110 dst_sel:DWORD dst_unused:UNUSED_PAD src0_sel:DWORD src1_sel:WORD_0\n\t"
    "s_waitcnt vmcnt(2) lgkmcnt(8)\n\t"
    "v_smfmac_f32_16x16x64_bf16 v[116:119], v[120:123], v[92:99], v124\n\t"
    "ds_read_b64 v[92:93], v110 offset:0\n\t"
    "ds_read_b64 v[94:95], v126 offset:0\n\t"
    "v_add_u32_sdwa v98, s92, v111 dst_sel:DWORD dst_unused:UNUSED_PAD src0_sel:DWORD src1_sel:WORD_1\n\t"
    "v_add_u32_sdwa v111, s92, v111 dst_sel:DWORD dst_unused:UNUSED_PAD src0_sel:DWORD src1_sel:WORD_0\n\t"
    "ds_read_b64 v[96:97], v111 offset:0\n\t"
    "ds_read_b64 v[98:99], v98 offset:0\n\t"
    "s_add_u32 s85, s85, s94\n\t"
    "buffer_load_dwordx4 v[108:111], v127, %12, s85 offen offset:0 sc1\n\t"
    "v_add_u32_sdwa v126, s92, v112 dst_sel:DWORD dst_unused:UNUSED_PAD src0_sel:DWORD src1_sel:WORD_1\n\t"
    "v_cvt_pk_bf16_f32 v54, v116, v117\n\t"
    "v_cvt_pk_bf16_f32 v55, v118, v119\n\t"
    "v_and_b32 v119, s67, v61\n\t"
    "v_lshlrev_b32 v118, 16, v61\n\t"
    "v_and_b32 v117, s67, v60\n\t"
    "v_lshlrev_b32 v116, 16, v60\n\t"
    "v_add_u32_sdwa v112, s92, v112 dst_sel:DWORD dst_unused:UNUSED_PAD src0_sel:DWORD src1_sel:WORD_0\n\t"
    "s_waitcnt lgkmcnt(8)\n\t"
    "v_smfmac_f32_16x16x64_bf16 v[116:119], v[120:123], v[84:91], v124\n\t"
    "ds_read_b64 v[84:85], v112 offset:0\n\t"
    "ds_read_b64 v[86:87], v126 offset:0\n\t"
    "v_cvt_pk_bf16_f32 v80, v80, v81\n\t"
    "v_cvt_pk_bf16_f32 v81, v82, v83\n\t"
    "ds_write_b64 v125, v[80:81] offset:872\n\t"
    "v_add_u32_sdwa v90, s92, v113 dst_sel:DWORD dst_unused:UNUSED_PAD src0_sel:DWORD src1_sel:WORD_1\n\t"
    "v_add_u32_sdwa v113, s92, v113 dst_sel:DWORD dst_unused:UNUSED_PAD src0_sel:DWORD src1_sel:WORD_0\n\t"
    "ds_read_b64 v[88:89], v113 offset:0\n\t"
    "ds_read_b64 v[90:91], v90 offset:0\n\t"
    "v_add_u32_sdwa v126, s92, v114 dst_sel:DWORD dst_unused:UNUSED_PAD src0_sel:DWORD src1_sel:WORD_1\n\t"
    "v_add_u32_sdwa v114, s92, v114 dst_sel:DWORD dst_unused:UNUSED_PAD src0_sel:DWORD src1_sel:WORD_0\n\t"
    "s_waitcnt vmcnt(2) lgkmcnt(5)\n\t"
    "v_smfmac_f32_16x16x64_bf16 v[116:119], v[120:123], v[92:99], v124\n\t"
    "ds_read_b64 v[92:93], v114 offset:0\n\t"
    "ds_read_b64 v[94:95], v126 offset:0\n\t"
    "v_add_u32_sdwa v98, s92, v115 dst_sel:DWORD dst_unused:UNUSED_PAD src0_sel:DWORD src1_sel:WORD_1\n\t"
    "v_add_u32_sdwa v115, s92, v115 dst_sel:DWORD dst_unused:UNUSED_PAD src0_sel:DWORD src1_sel:WORD_0\n\t"
    "ds_read_b64 v[96:97], v115 offset:0\n\t"
    "ds_read_b64 v[98:99], v98 offset:0\n\t"
    "buffer_load_dwordx4 v[112:115], v127, %12, s85 offen offset:1024 sc1\n\t"
    "v_add_u32_sdwa v126, s92, v100 dst_sel:DWORD dst_unused:UNUSED_PAD src0_sel:DWORD src1_sel:WORD_1\n\t"
    "v_cvt_pk_bf16_f32 v60, v116, v117\n\t"
    "v_cvt_pk_bf16_f32 v61, v118, v119\n\t"
    "v_and_b32 v119, s67, v63\n\t"
    "v_lshlrev_b32 v118, 16, v63\n\t"
    "v_and_b32 v117, s67, v62\n\t"
    "v_lshlrev_b32 v116, 16, v62\n\t"
    "v_add_u32_sdwa v100, s92, v100 dst_sel:DWORD dst_unused:UNUSED_PAD src0_sel:DWORD src1_sel:WORD_0\n\t"
    "s_waitcnt lgkmcnt(4)\n\t"
    "v_smfmac_f32_16x16x64_bf16 v[116:119], v[120:123], v[84:91], v124\n\t"
    "ds_read_b64 v[84:85], v100 offset:0\n\t"
    "ds_read_b64 v[86:87], v126 offset:0\n\t"
    "v_add_u32_sdwa v90, s92, v101 dst_sel:DWORD dst_unused:UNUSED_PAD src0_sel:DWORD src1_sel:WORD_1\n\t"
    "v_add_u32_sdwa v101, s92, v101 dst_sel:DWORD dst_unused:UNUSED_PAD src0_sel:DWORD src1_sel:WORD_0\n\t"
    "ds_read_b64 v[88:89], v101 offset:0\n\t"
    "ds_read_b64 v[90:91], v90 offset:0\n\t"
    "v_add_u32_sdwa v126, s92, v102 dst_sel:DWORD dst_unused:UNUSED_PAD src0_sel:DWORD src1_sel:WORD_1\n\t"
    "v_add_u32_sdwa v102, s92, v102 dst_sel:DWORD dst_unused:UNUSED_PAD src0_sel:DWORD src1_sel:WORD_0\n\t"
    "s_cmp_lt_u32 %16, 4\n\t"
    "s_cbranch_scc1 L_tp31_%=\n\t"
    "s_setprio 1\n\t"
    "L_tp31_%=:\n\t"
    "s_waitcnt vmcnt(2) lgkmcnt(4)\n\t"
    "v_smfmac_f32_16x16x64_bf16 v[116:119], v[120:123], v[92:99], v124\n\t"
    "ds_read_b64 v[92:93], v102 offset:0\n\t"
    "ds_read_b64 v[94:95], v126 offset:0\n\t"
    "v_add_u32_sdwa v98, s92, v103 dst_sel:DWORD dst_unused:UNUSED_PAD src0_sel:DWORD src1_sel:WORD_1\n\t"
    "v_add_u32_sdwa v103, s92, v103 dst_sel:DWORD dst_unused:UNUSED_PAD src0_sel:DWORD src1_sel:WORD_0\n\t"
    "ds_read_b64 v[96:97], v103 offset:0\n\t"
    "ds_read_b64 v[98:99], v98 offset:0\n\t"
    "s_add_u32 s86, s86, s94\n\t"
    "buffer_load_dwordx4 v[100:103], v127, %12, s86 offen offset:0 sc1\n\t"
    "v_add_u32_sdwa v126, s92, v104 dst_sel:DWORD dst_unused:UNUSED_PAD src0_sel:DWORD src1_sel:WORD_1\n\t"
    "v_cvt_pk_bf16_f32 v62, v116, v117\n\t"
    "v_cvt_pk_bf16_f32 v63, v118, v119\n\t"
    "v_and_b32 v119, s67, v1\n\t"
    "v_lshlrev_b32 v118, 16, v1\n\t"
    "v_and_b32 v117, s67, v0\n\t"
    "v_lshlrev_b32 v116, 16, v0\n\t"
    "v_add_u32_sdwa v104, s92, v104 dst_sel:DWORD dst_unused:UNUSED_PAD src0_sel:DWORD src1_sel:WORD_0\n\t"
    "s_waitcnt lgkmcnt(4)\n\t"
    "v_smfmac_f32_16x16x64_bf16 v[116:119], v[120:123], v[84:91], v124\n\t"
    "ds_read_b64 v[84:85], v104 offset:0\n\t"
    "v_mfma_f32_16x16x32_bf16 v[80:83], v[64:67], v[164:167], 0\n\t"
    "ds_read_b64 v[86:87], v126 offset:0\n\t"
    "v_mfma_f32_16x16x32_bf16 v[80:83], v[68:71], v[168:171], v[80:83]\n\t"
    "v_add_u32_sdwa v90, s92, v105 dst_sel:DWORD dst_unused:UNUSED_PAD src0_sel:DWORD src1_sel:WORD_1\n\t"
    "v_add_u32_sdwa v105, s92, v105 dst_sel:DWORD dst_unused:UNUSED_PAD src0_sel:DWORD src1_sel:WORD_0\n\t"
    "ds_read_b64 v[88:89], v105 offset:0\n\t"
    "v_mfma_f32_16x16x32_bf16 v[80:83], v[72:75], v[220:223], v[80:83]\n\t"
    "ds_read_b64 v[90:91], v90 offset:0\n\t"
    "v_mfma_f32_16x16x32_bf16 v[80:83], v[76:79], v[224:227], v[80:83]\n\t"
    "v_add_u32_sdwa v126, s92, v106 dst_sel:DWORD dst_unused:UNUSED_PAD src0_sel:DWORD src1_sel:WORD_1\n\t"
    "v_add_u32_sdwa v106, s92, v106 dst_sel:DWORD dst_unused:UNUSED_PAD src0_sel:DWORD src1_sel:WORD_0\n\t"
    "s_waitcnt vmcnt(2) lgkmcnt(4)\n\t"
    "v_smfmac_f32_16x16x64_bf16 v[116:119], v[120:123], v[92:99], v124\n\t"
    "ds_read_b64 v[92:93], v106 offset:0\n\t"
    "ds_read_b64 v[94:95], v126 offset:0\n\t"
    "v_add_u32_sdwa v98, s92, v107 dst_sel:DWORD dst_unused:UNUSED_PAD src0_sel:DWORD src1_sel:WORD_1\n\t"
    "v_add_u32_sdwa v107, s92, v107 dst_sel:DWORD dst_unused:UNUSED_PAD src0_sel:DWORD src1_sel:WORD_0\n\t"
    "ds_read_b64 v[96:97], v107 offset:0\n\t"
    "ds_read_b64 v[98:99], v98 offset:0\n\t"
    "buffer_load_dwordx4 v[104:107], v127, %12, s86 offen offset:1024 sc1\n\t"
    "v_add_u32_sdwa v126, s92, v108 dst_sel:DWORD dst_unused:UNUSED_PAD src0_sel:DWORD src1_sel:WORD_1\n\t"
    "v_cvt_pk_bf16_f32 v0, v116, v117\n\t"
    "v_cvt_pk_bf16_f32 v1, v118, v119\n\t"
    "v_and_b32 v119, s67, v3\n\t"
    "v_lshlrev_b32 v118, 16, v3\n\t"
    "v_and_b32 v117, s67, v2\n\t"
    "v_lshlrev_b32 v116, 16, v2\n\t"
    "v_add_u32_sdwa v108, s92, v108 dst_sel:DWORD dst_unused:UNUSED_PAD src0_sel:DWORD src1_sel:WORD_0\n\t"
    "s_waitcnt lgkmcnt(4)\n\t"
    "v_smfmac_f32_16x16x64_bf16 v[116:119], v[120:123], v[84:91], v124\n\t"
    "ds_read_b64 v[84:85], v108 offset:0\n\t"
    "ds_read_b64 v[86:87], v126 offset:0\n\t"
    "v_cvt_pk_bf16_f32 v80, v80, v81\n\t"
    "v_cvt_pk_bf16_f32 v81, v82, v83\n\t"
    "ds_write_b64 v125, v[80:81] offset:32000\n\t"
    "v_mfma_f32_16x16x32_bf16 v[80:83], v[64:67], v[172:175], 0\n\t"
    "v_add_u32_sdwa v90, s92, v109 dst_sel:DWORD dst_unused:UNUSED_PAD src0_sel:DWORD src1_sel:WORD_1\n\t"
    "v_add_u32_sdwa v109, s92, v109 dst_sel:DWORD dst_unused:UNUSED_PAD src0_sel:DWORD src1_sel:WORD_0\n\t"
    "ds_read_b64 v[88:89], v109 offset:0\n\t"
    "v_mfma_f32_16x16x32_bf16 v[80:83], v[68:71], v[176:179], v[80:83]\n\t"
    "ds_read_b64 v[90:91], v90 offset:0\n\t"
    "v_mfma_f32_16x16x32_bf16 v[80:83], v[72:75], v[228:231], v[80:83]\n\t"
    "v_mfma_f32_16x16x32_bf16 v[80:83], v[76:79], v[232:235], v[80:83]\n\t"
    "v_add_u32_sdwa v126, s92, v110 dst_sel:DWORD dst_unused:UNUSED_PAD src0_sel:DWORD src1_sel:WORD_1\n\t"
    "v_add_u32_sdwa v110, s92, v110 dst_sel:DWORD dst_unused:UNUSED_PAD src0_sel:DWORD src1_sel:WORD_0\n\t"
    "s_waitcnt vmcnt(2) lgkmcnt(5)\n\t"
    "v_smfmac_f32_16x16x64_bf16 v[116:119], v[120:123], v[92:99], v124\n\t"
    "ds_read_b64 v[92:93], v110 offset:0\n\t"
    "ds_read_b64 v[94:95], v126 offset:0\n\t"
    "v_add_u32_sdwa v98, s92, v111 dst_sel:DWORD dst_unused:UNUSED_PAD src0_sel:DWORD src1_sel:WORD_1\n\t"
    "v_add_u32_sdwa v111, s92, v111 dst_sel:DWORD dst_unused:UNUSED_PAD src0_sel:DWORD src1_sel:WORD_0\n\t"
    "ds_read_b64 v[96:97], v111 offset:0\n\t"
    "ds_read_b64 v[98:99], v98 offset:0\n\t"
    "s_add_u32 s87, s87, s94\n\t"
    "buffer_load_dwordx4 v[108:111], v127, %12, s87 offen offset:0 sc1\n\t"
    "v_add_u32_sdwa v126, s92, v112 dst_sel:DWORD dst_unused:UNUSED_PAD src0_sel:DWORD src1_sel:WORD_1\n\t"
    "v_cvt_pk_bf16_f32 v2, v116, v117\n\t"
    "v_cvt_pk_bf16_f32 v3, v118, v119\n\t"
    "v_and_b32 v119, s67, v9\n\t"
    "v_lshlrev_b32 v118, 16, v9\n\t"
    "v_and_b32 v117, s67, v8\n\t"
    "v_lshlrev_b32 v116, 16, v8\n\t"
    "v_add_u32_sdwa v112, s92, v112 dst_sel:DWORD dst_unused:UNUSED_PAD src0_sel:DWORD src1_sel:WORD_0\n\t"
    "s_waitcnt lgkmcnt(4)\n\t"
    "v_smfmac_f32_16x16x64_bf16 v[116:119], v[120:123], v[84:91], v124\n\t"
    "ds_read_b64 v[84:85], v112 offset:0\n\t"
    "ds_read_b64 v[86:87], v126 offset:0\n\t"
    "v_cvt_pk_bf16_f32 v80, v80, v81\n\t"
    "v_cvt_pk_bf16_f32 v81, v82, v83\n\t"
    "ds_write_b64 v125, v[80:81] offset:32128\n\t"
    "v_add_u32_sdwa v90, s92, v113 dst_sel:DWORD dst_unused:UNUSED_PAD src0_sel:DWORD src1_sel:WORD_1\n\t"
    "v_add_u32_sdwa v113, s92, v113 dst_sel:DWORD dst_unused:UNUSED_PAD src0_sel:DWORD src1_sel:WORD_0\n\t"
    "ds_read_b64 v[88:89], v113 offset:0\n\t"
    "ds_read_b64 v[90:91], v90 offset:0\n\t"
    "v_add_u32_sdwa v126, s92, v114 dst_sel:DWORD dst_unused:UNUSED_PAD src0_sel:DWORD src1_sel:WORD_1\n\t"
    "v_add_u32_sdwa v114, s92, v114 dst_sel:DWORD dst_unused:UNUSED_PAD src0_sel:DWORD src1_sel:WORD_0\n\t"
    "s_waitcnt vmcnt(2) lgkmcnt(5)\n\t"
    "v_smfmac_f32_16x16x64_bf16 v[116:119], v[120:123], v[92:99], v124\n\t"
    "ds_read_b64 v[92:93], v114 offset:0\n\t"
    "ds_read_b64 v[94:95], v126 offset:0\n\t"
    "v_add_u32_sdwa v98, s92, v115 dst_sel:DWORD dst_unused:UNUSED_PAD src0_sel:DWORD src1_sel:WORD_1\n\t"
    "v_add_u32_sdwa v115, s92, v115 dst_sel:DWORD dst_unused:UNUSED_PAD src0_sel:DWORD src1_sel:WORD_0\n\t"
    "ds_read_b64 v[96:97], v115 offset:0\n\t"
    "ds_read_b64 v[98:99], v98 offset:0\n\t"
    "buffer_load_dwordx4 v[112:115], v127, %12, s87 offen offset:1024 sc1\n\t"
    "v_add_u32_sdwa v126, s92, v100 dst_sel:DWORD dst_unused:UNUSED_PAD src0_sel:DWORD src1_sel:WORD_1\n\t"
    "v_cvt_pk_bf16_f32 v8, v116, v117\n\t"
    "v_cvt_pk_bf16_f32 v9, v118, v119\n\t"
    "v_and_b32 v119, s67, v11\n\t"
    "v_lshlrev_b32 v118, 16, v11\n\t"
    "v_and_b32 v117, s67, v10\n\t"
    "v_lshlrev_b32 v116, 16, v10\n\t"
    "v_add_u32_sdwa v100, s92, v100 dst_sel:DWORD dst_unused:UNUSED_PAD src0_sel:DWORD src1_sel:WORD_0\n\t"
    "s_waitcnt lgkmcnt(4)\n\t"
    "v_smfmac_f32_16x16x64_bf16 v[116:119], v[120:123], v[84:91], v124\n\t"
    "ds_read_b64 v[84:85], v100 offset:0\n\t"
    "v_mfma_f32_16x16x32_bf16 v[80:83], v[64:67], v[180:183], 0\n\t"
    "ds_read_b64 v[86:87], v126 offset:0\n\t"
    "v_mfma_f32_16x16x32_bf16 v[80:83], v[68:71], v[184:187], v[80:83]\n\t"
    "v_add_u32_sdwa v90, s92, v101 dst_sel:DWORD dst_unused:UNUSED_PAD src0_sel:DWORD src1_sel:WORD_1\n\t"
    "v_add_u32_sdwa v101, s92, v101 dst_sel:DWORD dst_unused:UNUSED_PAD src0_sel:DWORD src1_sel:WORD_0\n\t"
    "ds_read_b64 v[88:89], v101 offset:0\n\t"
    "v_mfma_f32_16x16x32_bf16 v[80:83], v[72:75], v[236:239], v[80:83]\n\t"
    "ds_read_b64 v[90:91], v90 offset:0\n\t"
    "v_mfma_f32_16x16x32_bf16 v[80:83], v[76:79], v[240:243], v[80:83]\n\t"
    "v_add_u32_sdwa v126, s92, v102 dst_sel:DWORD dst_unused:UNUSED_PAD src0_sel:DWORD src1_sel:WORD_1\n\t"
    "v_add_u32_sdwa v102, s92, v102 dst_sel:DWORD dst_unused:UNUSED_PAD src0_sel:DWORD src1_sel:WORD_0\n\t"
    "s_waitcnt vmcnt(2) lgkmcnt(4)\n\t"
    "v_smfmac_f32_16x16x64_bf16 v[116:119], v[120:123], v[92:99], v124\n\t"
    "ds_read_b64 v[92:93], v102 offset:0\n\t"
    "ds_read_b64 v[94:95], v126 offset:0\n\t"
    "v_add_u32_sdwa v98, s92, v103 dst_sel:DWORD dst_unused:UNUSED_PAD src0_sel:DWORD src1_sel:WORD_1\n\t"
    "v_add_u32_sdwa v103, s92, v103 dst_sel:DWORD dst_unused:UNUSED_PAD src0_sel:DWORD src1_sel:WORD_0\n\t"
    "ds_read_b64 v[96:97], v103 offset:0\n\t"
    "ds_read_b64 v[98:99], v98 offset:0\n\t"
    "s_add_u32 s88, s88, s94\n\t"
    "buffer_load_dwordx4 v[100:103], v127, %12, s88 offen offset:0 sc1\n\t"
    "v_add_u32_sdwa v126, s92, v104 dst_sel:DWORD dst_unused:UNUSED_PAD src0_sel:DWORD src1_sel:WORD_1\n\t"
    "v_cvt_pk_bf16_f32 v10, v116, v117\n\t"
    "v_cvt_pk_bf16_f32 v11, v118, v119\n\t"
    "v_and_b32 v119, s67, v17\n\t"
    "v_lshlrev_b32 v118, 16, v17\n\t"
    "v_and_b32 v117, s67, v16\n\t"
    "v_lshlrev_b32 v116, 16, v16\n\t"
    "v_add_u32_sdwa v104, s92, v104 dst_sel:DWORD dst_unused:UNUSED_PAD src0_sel:DWORD src1_sel:WORD_0\n\t"
    "s_waitcnt lgkmcnt(4)\n\t"
    "v_smfmac_f32_16x16x64_bf16 v[116:119], v[120:123], v[84:91], v124\n\t"
    "ds_read_b64 v[84:85], v104 offset:0\n\t"
    "ds_read_b64 v[86:87], v126 offset:0\n\t"
    "v_cvt_pk_bf16_f32 v80, v80, v81\n\t"
    "v_cvt_pk_bf16_f32 v81, v82, v83\n\t"
    "ds_write_b64 v125, v[80:81] offset:32256\n\t"
    "v_add_u32_sdwa v90, s92, v105 dst_sel:DWORD dst_unused:UNUSED_PAD src0_sel:DWORD src1_sel:WORD_1\n\t"
    "v_add_u32_sdwa v105, s92, v105 dst_sel:DWORD dst_unused:UNUSED_PAD src0_sel:DWORD src1_sel:WORD_0\n\t"
    "ds_read_b64 v[88:89], v105 offset:0\n\t"
    "ds_read_b64 v[90:91], v90 offset:0\n\t"
    "v_add_u32_sdwa v126, s92, v106 dst_sel:DWORD dst_unused:UNUSED_PAD src0_sel:DWORD src1_sel:WORD_1\n\t"
    "v_add_u32_sdwa v106, s92, v106 dst_sel:DWORD dst_unused:UNUSED_PAD src0_sel:DWORD src1_sel:WORD_0\n\t"
    "s_waitcnt vmcnt(2) lgkmcnt(5)\n\t"
    "v_smfmac_f32_16x16x64_bf16 v[116:119], v[120:123], v[92:99], v124\n\t"
    "ds_read_b64 v[92:93], v106 offset:0\n\t"
    "ds_read_b64 v[94:95], v126 offset:0\n\t"
    "v_add_u32_sdwa v98, s92, v107 dst_sel:DWORD dst_unused:UNUSED_PAD src0_sel:DWORD src1_sel:WORD_1\n\t"
    "v_add_u32_sdwa v107, s92, v107 dst_sel:DWORD dst_unused:UNUSED_PAD src0_sel:DWORD src1_sel:WORD_0\n\t"
    "ds_read_b64 v[96:97], v107 offset:0\n\t"
    "ds_read_b64 v[98:99], v98 offset:0\n\t"
    "buffer_load_dwordx4 v[104:107], v127, %12, s88 offen offset:1024 sc1\n\t"
    "v_add_u32_sdwa v126, s92, v108 dst_sel:DWORD dst_unused:UNUSED_PAD src0_sel:DWORD src1_sel:WORD_1\n\t"
    "v_cvt_pk_bf16_f32 v16, v116, v117\n\t"
    "v_cvt_pk_bf16_f32 v17, v118, v119\n\t"
    "v_and_b32 v119, s67, v19\n\t"
    "v_lshlrev_b32 v118, 16, v19\n\t"
    "v_and_b32 v117, s67, v18\n\t"
    "v_lshlrev_b32 v116, 16, v18\n\t"
    "v_add_u32_sdwa v108, s92, v108 dst_sel:DWORD dst_unused:UNUSED_PAD src0_sel:DWORD src1_sel:WORD_0\n\t"
    "s_waitcnt lgkmcnt(4)\n\t"
    "v_smfmac_f32_16x16x64_bf16 v[116:119], v[120:123], v[84:91], v124\n\t"
    "ds_read_b64 v[84:85], v108 offset:0\n\t"
    "v_mfma_f32_16x16x32_bf16 v[80:83], v[64:67], v[188:191], 0\n\t"
    "ds_read_b64 v[86:87], v126 offset:0\n\t"
    "v_mfma_f32_16x16x32_bf16 v[80:83], v[68:71], v[192:195], v[80:83]\n\t"
    "v_add_u32_sdwa v90, s92, v109 dst_sel:DWORD dst_unused:UNUSED_PAD src0_sel:DWORD src1_sel:WORD_1\n\t"
    "v_add_u32_sdwa v109, s92, v109 dst_sel:DWORD dst_unused:UNUSED_PAD src0_sel:DWORD src1_sel:WORD_0\n\t"
    "ds_read_b64 v[88:89], v109 offset:0\n\t"
    "v_mfma_f32_16x16x32_bf16 v[80:83], v[72:75], v[244:247], v[80:83]\n\t"
    "ds_read_b64 v[90:91], v90 offset:0\n\t"
    "v_mfma_f32_16x16x32_bf16 v[80:83], v[76:79], v[248:251], v[80:83]\n\t"
    "v_add_u32_sdwa v126, s92, v110 dst_sel:DWORD dst_unused:UNUSED_PAD src0_sel:DWORD src1_sel:WORD_1\n\t"
    "v_add_u32_sdwa v110, s92, v110 dst_sel:DWORD dst_unused:UNUSED_PAD src0_sel:DWORD src1_sel:WORD_0\n\t"
    "s_waitcnt vmcnt(2) lgkmcnt(4)\n\t"
    "v_smfmac_f32_16x16x64_bf16 v[116:119], v[120:123], v[92:99], v124\n\t"
    "ds_read_b64 v[92:93], v110 offset:0\n\t"
    "ds_read_b64 v[94:95], v126 offset:0\n\t"
    "v_add_u32_sdwa v98, s92, v111 dst_sel:DWORD dst_unused:UNUSED_PAD src0_sel:DWORD src1_sel:WORD_1\n\t"
    "v_add_u32_sdwa v111, s92, v111 dst_sel:DWORD dst_unused:UNUSED_PAD src0_sel:DWORD src1_sel:WORD_0\n\t"
    "ds_read_b64 v[96:97], v111 offset:0\n\t"
    "ds_read_b64 v[98:99], v98 offset:0\n\t"
    "s_add_u32 s89, s89, s94\n\t"
    "buffer_load_dwordx4 v[108:111], v127, %12, s89 offen offset:0 sc1\n\t"
    "v_add_u32_sdwa v126, s92, v112 dst_sel:DWORD dst_unused:UNUSED_PAD src0_sel:DWORD src1_sel:WORD_1\n\t"
    "v_cvt_pk_bf16_f32 v18, v116, v117\n\t"
    "v_cvt_pk_bf16_f32 v19, v118, v119\n\t"
    "v_and_b32 v119, s67, v25\n\t"
    "v_lshlrev_b32 v118, 16, v25\n\t"
    "v_and_b32 v117, s67, v24\n\t"
    "v_lshlrev_b32 v116, 16, v24\n\t"
    "v_add_u32_sdwa v112, s92, v112 dst_sel:DWORD dst_unused:UNUSED_PAD src0_sel:DWORD src1_sel:WORD_0\n\t"
    "s_waitcnt lgkmcnt(4)\n\t"
    "v_smfmac_f32_16x16x64_bf16 v[116:119], v[120:123], v[84:91], v124\n\t"
    "ds_read_b64 v[84:85], v112 offset:0\n\t"
    "ds_read_b64 v[86:87], v126 offset:0\n\t"
    "v_cvt_pk_bf16_f32 v80, v80, v81\n\t"
    "v_cvt_pk_bf16_f32 v81, v82, v83\n\t"
    "ds_write_b64 v125, v[80:81] offset:32384\n\t"
    "v_add_u32_sdwa v90, s92, v113 dst_sel:DWORD dst_unused:UNUSED_PAD src0_sel:DWORD src1_sel:WORD_1\n\t"
    "v_add_u32_sdwa v113, s92, v113 dst_sel:DWORD dst_unused:UNUSED_PAD src0_sel:DWORD src1_sel:WORD_0\n\t"
    "ds_read_b64 v[88:89], v113 offset:0\n\t"
    "ds_read_b64 v[90:91], v90 offset:0\n\t"
    "v_add_u32_sdwa v126, s92, v114 dst_sel:DWORD dst_unused:UNUSED_PAD src0_sel:DWORD src1_sel:WORD_1\n\t"
    "v_add_u32_sdwa v114, s92, v114 dst_sel:DWORD dst_unused:UNUSED_PAD src0_sel:DWORD src1_sel:WORD_0\n\t"
    "s_waitcnt vmcnt(2) lgkmcnt(5)\n\t"
    "v_smfmac_f32_16x16x64_bf16 v[116:119], v[120:123], v[92:99], v124\n\t"
    "ds_read_b64 v[92:93], v114 offset:0\n\t"
    "ds_read_b64 v[94:95], v126 offset:0\n\t"
    "v_add_u32_sdwa v98, s92, v115 dst_sel:DWORD dst_unused:UNUSED_PAD src0_sel:DWORD src1_sel:WORD_1\n\t"
    "v_add_u32_sdwa v115, s92, v115 dst_sel:DWORD dst_unused:UNUSED_PAD src0_sel:DWORD src1_sel:WORD_0\n\t"
    "ds_read_b64 v[96:97], v115 offset:0\n\t"
    "ds_read_b64 v[98:99], v98 offset:0\n\t"
    "buffer_load_dwordx4 v[112:115], v127, %12, s89 offen offset:1024 sc1\n\t"
    "v_add_u32_sdwa v126, s92, v100 dst_sel:DWORD dst_unused:UNUSED_PAD src0_sel:DWORD src1_sel:WORD_1\n\t"
    "v_cvt_pk_bf16_f32 v24, v116, v117\n\t"
    "v_cvt_pk_bf16_f32 v25, v118, v119\n\t"
    "v_and_b32 v119, s67, v27\n\t"
    "v_lshlrev_b32 v118, 16, v27\n\t"
    "v_and_b32 v117, s67, v26\n\t"
    "v_lshlrev_b32 v116, 16, v26\n\t"
    "v_add_u32_sdwa v100, s92, v100 dst_sel:DWORD dst_unused:UNUSED_PAD src0_sel:DWORD src1_sel:WORD_0\n\t"
    "s_waitcnt lgkmcnt(4)\n\t"
    "v_smfmac_f32_16x16x64_bf16 v[116:119], v[120:123], v[84:91], v124\n\t"
    "ds_read_b64 v[84:85], v100 offset:0\n\t"
    "v_mfma_f32_16x16x32_bf16 v[80:83], v[64:67], v[196:199], 0\n\t"
    "ds_read_b64 v[86:87], v126 offset:0\n\t"
    "v_mfma_f32_16x16x32_bf16 v[80:83], v[68:71], v[200:203], v[80:83]\n\t"
    "v_add_u32_sdwa v90, s92, v101 dst_sel:DWORD dst_unused:UNUSED_PAD src0_sel:DWORD src1_sel:WORD_1\n\t"
    "v_add_u32_sdwa v101, s92, v101 dst_sel:DWORD dst_unused:UNUSED_PAD src0_sel:DWORD src1_sel:WORD_0\n\t"
    "ds_read_b64 v[88:89], v101 offset:0\n\t"
    "v_mfma_f32_16x16x32_bf16 v[80:83], v[72:75], v[252:255], v[80:83]\n\t"
    "ds_read_b64 v[90:91], v90 offset:0\n\t"
    "v_mfma_f32_16x16x32_bf16 v[80:83], v[76:79], v[128:131], v[80:83]\n\t"
    "v_add_u32_sdwa v126, s92, v102 dst_sel:DWORD dst_unused:UNUSED_PAD src0_sel:DWORD src1_sel:WORD_1\n\t"
    "v_add_u32_sdwa v102, s92, v102 dst_sel:DWORD dst_unused:UNUSED_PAD src0_sel:DWORD src1_sel:WORD_0\n\t"
    "s_waitcnt vmcnt(2) lgkmcnt(4)\n\t"
    "v_smfmac_f32_16x16x64_bf16 v[116:119], v[120:123], v[92:99], v124\n\t"
    "ds_read_b64 v[92:93], v102 offset:0\n\t"
    "ds_read_b64 v[94:95], v126 offset:0\n\t"
    "v_add_u32_sdwa v98, s92, v103 dst_sel:DWORD dst_unused:UNUSED_PAD src0_sel:DWORD src1_sel:WORD_1\n\t"
    "v_add_u32_sdwa v103, s92, v103 dst_sel:DWORD dst_unused:UNUSED_PAD src0_sel:DWORD src1_sel:WORD_0\n\t"
    "ds_read_b64 v[96:97], v103 offset:0\n\t"
    "ds_read_b64 v[98:99], v98 offset:0\n\t"
    "s_add_u32 s90, s90, s94\n\t"
    "buffer_load_dwordx4 v[100:103], v127, %12, s90 offen offset:0 sc1\n\t"
    "v_add_u32_sdwa v126, s92, v104 dst_sel:DWORD dst_unused:UNUSED_PAD src0_sel:DWORD src1_sel:WORD_1\n\t"
    "v_cvt_pk_bf16_f32 v26, v116, v117\n\t"
    "v_cvt_pk_bf16_f32 v27, v118, v119\n\t"
    "v_and_b32 v119, s67, v33\n\t"
    "v_lshlrev_b32 v118, 16, v33\n\t"
    "v_and_b32 v117, s67, v32\n\t"
    "v_lshlrev_b32 v116, 16, v32\n\t"
    "v_add_u32_sdwa v104, s92, v104 dst_sel:DWORD dst_unused:UNUSED_PAD src0_sel:DWORD src1_sel:WORD_0\n\t"
    "s_waitcnt lgkmcnt(4)\n\t"
    "v_smfmac_f32_16x16x64_bf16 v[116:119], v[120:123], v[84:91], v124\n\t"
    "ds_read_b64 v[84:85], v104 offset:0\n\t"
    "ds_read_b64 v[86:87], v126 offset:0\n\t"
    "v_cvt_pk_bf16_f32 v80, v80, v81\n\t"
    "v_cvt_pk_bf16_f32 v81, v82, v83\n\t"
    "ds_write_b64 v125, v[80:81] offset:32512\n\t"
    "v_add_u32_sdwa v90, s92, v105 dst_sel:DWORD dst_unused:UNUSED_PAD src0_sel:DWORD src1_sel:WORD_1\n\t"
    "v_add_u32_sdwa v105, s92, v105 dst_sel:DWORD dst_unused:UNUSED_PAD src0_sel:DWORD src1_sel:WORD_0\n\t"
    "ds_read_b64 v[88:89], v105 offset:0\n\t"
    "ds_read_b64 v[90:91], v90 offset:0\n\t"
    "v_add_u32_sdwa v126, s92, v106 dst_sel:DWORD dst_unused:UNUSED_PAD src0_sel:DWORD src1_sel:WORD_1\n\t"
    "v_add_u32_sdwa v106, s92, v106 dst_sel:DWORD dst_unused:UNUSED_PAD src0_sel:DWORD src1_sel:WORD_0\n\t"
    "s_waitcnt vmcnt(2) lgkmcnt(5)\n\t"
    "v_smfmac_f32_16x16x64_bf16 v[116:119], v[120:123], v[92:99], v124\n\t"
    "ds_read_b64 v[92:93], v106 offset:0\n\t"
    "ds_read_b64 v[94:95], v126 offset:0\n\t"
    "v_add_u32_sdwa v98, s92, v107 dst_sel:DWORD dst_unused:UNUSED_PAD src0_sel:DWORD src1_sel:WORD_1\n\t"
    "v_add_u32_sdwa v107, s92, v107 dst_sel:DWORD dst_unused:UNUSED_PAD src0_sel:DWORD src1_sel:WORD_0\n\t"
    "ds_read_b64 v[96:97], v107 offset:0\n\t"
    "ds_read_b64 v[98:99], v98 offset:0\n\t"
    "buffer_load_dwordx4 v[104:107], v127, %12, s90 offen offset:1024 sc1\n\t"
    "v_add_u32_sdwa v126, s92, v108 dst_sel:DWORD dst_unused:UNUSED_PAD src0_sel:DWORD src1_sel:WORD_1\n\t"
    "v_cvt_pk_bf16_f32 v32, v116, v117\n\t"
    "v_cvt_pk_bf16_f32 v33, v118, v119\n\t"
    "v_and_b32 v119, s67, v35\n\t"
    "v_lshlrev_b32 v118, 16, v35\n\t"
    "v_and_b32 v117, s67, v34\n\t"
    "v_lshlrev_b32 v116, 16, v34\n\t"
    "v_add_u32_sdwa v108, s92, v108 dst_sel:DWORD dst_unused:UNUSED_PAD src0_sel:DWORD src1_sel:WORD_0\n\t"
    "s_waitcnt lgkmcnt(4)\n\t"
    "v_smfmac_f32_16x16x64_bf16 v[116:119], v[120:123], v[84:91], v124\n\t"
    "ds_read_b64 v[84:85], v108 offset:0\n\t"
    "v_mfma_f32_16x16x32_bf16 v[80:83], v[64:67], v[204:207], 0\n\t"
    "ds_read_b64 v[86:87], v126 offset:0\n\t"
    "v_mfma_f32_16x16x32_bf16 v[80:83], v[68:71], v[208:211], v[80:83]\n\t"
    "v_add_u32_sdwa v90, s92, v109 dst_sel:DWORD dst_unused:UNUSED_PAD src0_sel:DWORD src1_sel:WORD_1\n\t"
    "v_add_u32_sdwa v109, s92, v109 dst_sel:DWORD dst_unused:UNUSED_PAD src0_sel:DWORD src1_sel:WORD_0\n\t"
    "ds_read_b64 v[88:89], v109 offset:0\n\t"
    "v_mfma_f32_16x16x32_bf16 v[80:83], v[72:75], v[132:135], v[80:83]\n\t"
    "ds_read_b64 v[90:91], v90 offset:0\n\t"
    "v_mfma_f32_16x16x32_bf16 v[80:83], v[76:79], v[136:139], v[80:83]\n\t"
    "v_add_u32_sdwa v126, s92, v110 dst_sel:DWORD dst_unused:UNUSED_PAD src0_sel:DWORD src1_sel:WORD_1\n\t"
    "v_add_u32_sdwa v110, s92, v110 dst_sel:DWORD dst_unused:UNUSED_PAD src0_sel:DWORD src1_sel:WORD_0\n\t"
    "s_cmp_lt_u32 %16, 4\n\t"
    "s_cbranch_scc1 L_tp51_%=\n\t"
    "s_setprio 0\n\t"
    "L_tp51_%=:\n\t"
    "s_waitcnt vmcnt(2) lgkmcnt(4)\n\t"
    "v_smfmac_f32_16x16x64_bf16 v[116:119], v[120:123], v[92:99], v124\n\t"
    "ds_read_b64 v[92:93], v110 offset:0\n\t"
    "ds_read_b64 v[94:95], v126 offset:0\n\t"
    "v_add_u32_sdwa v98, s92, v111 dst_sel:DWORD dst_unused:UNUSED_PAD src0_sel:DWORD src1_sel:WORD_1\n\t"
    "v_add_u32_sdwa v111, s92, v111 dst_sel:DWORD dst_unused:UNUSED_PAD src0_sel:DWORD src1_sel:WORD_0\n\t"
    "ds_read_b64 v[96:97], v111 offset:0\n\t"
    "ds_read_b64 v[98:99], v98 offset:0\n\t"
    "s_add_u32 s91, s91, s94\n\t"
    "buffer_load_dwordx4 v[108:111], v127, %12, s91 offen offset:0 sc1\n\t"
    "v_add_u32_sdwa v126, s92, v112 dst_sel:DWORD dst_unused:UNUSED_PAD src0_sel:DWORD src1_sel:WORD_1\n\t"
    "v_cvt_pk_bf16_f32 v34, v116, v117\n\t"
    "v_cvt_pk_bf16_f32 v35, v118, v119\n\t"
    "v_and_b32 v119, s67, v41\n\t"
    "v_lshlrev_b32 v118, 16, v41\n\t"
    "v_and_b32 v117, s67, v40\n\t"
    "v_lshlrev_b32 v116, 16, v40\n\t"
    "v_add_u32_sdwa v112, s92, v112 dst_sel:DWORD dst_unused:UNUSED_PAD src0_sel:DWORD src1_sel:WORD_0\n\t"
    "s_waitcnt lgkmcnt(4)\n\t"
    "v_smfmac_f32_16x16x64_bf16 v[116:119], v[120:123], v[84:91], v124\n\t"
    "ds_read_b64 v[84:85], v112 offset:0\n\t"
    "ds_read_b64 v[86:87], v126 offset:0\n\t"
    "v_cvt_pk_bf16_f32 v80, v80, v81\n\t"
    "v_cvt_pk_bf16_f32 v81, v82, v83\n\t"
    "ds_write_b64 v125, v[80:81] offset:32640\n\t"
    "v_add_u32_sdwa v90, s92, v113 dst_sel:DWORD dst_unused:UNUSED_PAD src0_sel:DWORD src1_sel:WORD_1\n\t"
    "v_add_u32_sdwa v113, s92, v113 dst_sel:DWORD dst_unused:UNUSED_PAD src0_sel:DWORD src1_sel:WORD_0\n\t"
    "ds_read_b64 v[88:89], v113 offset:0\n\t"
    "ds_read_b64 v[90:91], v90 offset:0\n\t"
    "v_add_u32_sdwa v126, s92, v114 dst_sel:DWORD dst_unused:UNUSED_PAD src0_sel:DWORD src1_sel:WORD_1\n\t"
    "v_add_u32_sdwa v114, s92, v114 dst_sel:DWORD dst_unused:UNUSED_PAD src0_sel:DWORD src1_sel:WORD_0\n\t"
    "s_waitcnt vmcnt(2) lgkmcnt(5)\n\t"
    "v_smfmac_f32_16x16x64_bf16 v[116:119], v[120:123], v[92:99], v124\n\t"
    "ds_read_b64 v[92:93], v114 offset:0\n\t"
    "ds_read_b64 v[94:95], v126 offset:0\n\t"
    "v_add_u32_sdwa v98, s92, v115 dst_sel:DWORD dst_unused:UNUSED_PAD src0_sel:DWORD src1_sel:WORD_1\n\t"
    "v_add_u32_sdwa v115, s92, v115 dst_sel:DWORD dst_unused:UNUSED_PAD src0_sel:DWORD src1_sel:WORD_0\n\t"
    "ds_read_b64 v[96:97], v115 offset:0\n\t"
    "ds_read_b64 v[98:99], v98 offset:0\n\t"
    "buffer_load_dwordx4 v[112:115], v127, %12, s91 offen offset:1024 sc1\n\t"
    "v_add_u32_sdwa v126, s92, v100 dst_sel:DWORD dst_unused:UNUSED_PAD src0_sel:DWORD src1_sel:WORD_1\n\t"
    "v_cvt_pk_bf16_f32 v40, v116, v117\n\t"
    "v_cvt_pk_bf16_f32 v41, v118, v119\n\t"
    "v_and_b32 v119, s67, v43\n\t"
    "v_lshlrev_b32 v118, 16, v43\n\t"
    "v_and_b32 v117, s67, v42\n\t"
    "v_lshlrev_b32 v116, 16, v42\n\t"
    "v_add_u32_sdwa v100, s92, v100 dst_sel:DWORD dst_unused:UNUSED_PAD src0_sel:DWORD src1_sel:WORD_0\n\t"
    "s_waitcnt lgkmcnt(4)\n\t"
    "v_smfmac_f32_16x16x64_bf16 v[116:119], v[120:123], v[84:91], v124\n\t"
    "ds_read_b64 v[84:85], v100 offset:0\n\t"
    "v_mfma_f32_16x16x32_bf16 v[80:83], v[64:67], v[156:159], 0\n\t"
    "ds_read_b64 v[86:87], v126 offset:0\n\t"
    "v_mfma_f32_16x16x32_bf16 v[80:83], v[68:71], v[160:163], v[80:83]\n\t"
    "v_add_u32_sdwa v90, s92, v101 dst_sel:DWORD dst_unused:UNUSED_PAD src0_sel:DWORD src1_sel:WORD_1\n\t"
    "v_add_u32_sdwa v101, s92, v101 dst_sel:DWORD dst_unused:UNUSED_PAD src0_sel:DWORD src1_sel:WORD_0\n\t"
    "ds_read_b64 v[88:89], v101 offset:0\n\t"
    "v_mfma_f32_16x16x32_bf16 v[80:83], v[72:75], v[140:143], v[80:83]\n\t"
    "ds_read_b64 v[90:91], v90 offset:0\n\t"
    "v_mfma_f32_16x16x32_bf16 v[80:83], v[76:79], v[144:147], v[80:83]\n\t"
    "v_add_u32_sdwa v126, s92, v102 dst_sel:DWORD dst_unused:UNUSED_PAD src0_sel:DWORD src1_sel:WORD_1\n\t"
    "v_add_u32_sdwa v102, s92, v102 dst_sel:DWORD dst_unused:UNUSED_PAD src0_sel:DWORD src1_sel:WORD_0\n\t"
    "s_waitcnt vmcnt(2) lgkmcnt(4)\n\t"
    "v_smfmac_f32_16x16x64_bf16 v[116:119], v[120:123], v[92:99], v124\n\t"
    "ds_read_b64 v[92:93], v102 offset:0\n\t"
    "ds_read_b64 v[94:95], v126 offset:0\n\t"
    "v_add_u32_sdwa v98, s92, v103 dst_sel:DWORD dst_unused:UNUSED_PAD src0_sel:DWORD src1_sel:WORD_1\n\t"
    "v_add_u32_sdwa v103, s92, v103 dst_sel:DWORD dst_unused:UNUSED_PAD src0_sel:DWORD src1_sel:WORD_0\n\t"
    "ds_read_b64 v[96:97], v103 offset:0\n\t"
    "ds_read_b64 v[98:99], v98 offset:0\n\t"
    "s_add_u32 s84, s84, s95\n\t"
    "buffer_load_dwordx4 v[100:103], v127, %12, s84 offen offset:0 sc1\n\t"
    "v_add_u32_sdwa v126, s92, v104 dst_sel:DWORD dst_unused:UNUSED_PAD src0_sel:DWORD src1_sel:WORD_1\n\t"
    "v_cvt_pk_bf16_f32 v42, v116, v117\n\t"
    "v_cvt_pk_bf16_f32 v43, v118, v119\n\t"
    "v_and_b32 v119, s67, v49\n\t"
    "v_lshlrev_b32 v118, 16, v49\n\t"
    "v_and_b32 v117, s67, v48\n\t"
    "v_lshlrev_b32 v116, 16, v48\n\t"
    "v_add_u32_sdwa v104, s92, v104 dst_sel:DWORD dst_unused:UNUSED_PAD src0_sel:DWORD src1_sel:WORD_0\n\t"
    "s_waitcnt lgkmcnt(4)\n\t"
    "v_smfmac_f32_16x16x64_bf16 v[116:119], v[120:123], v[84:91], v124\n\t"
    "ds_read_b64 v[84:85], v104 offset:0\n\t"
    "ds_read_b64 v[86:87], v126 offset:0\n\t"
    "v_cvt_pk_bf16_f32 v80, v80, v81\n\t"
    "v_cvt_pk_bf16_f32 v81, v82, v83\n\t"
    "ds_write_b64 v125, v[80:81] offset:32768\n\t"
    "v_add_u32_sdwa v90, s92, v105 dst_sel:DWORD dst_unused:UNUSED_PAD src0_sel:DWORD src1_sel:WORD_1\n\t"
    "v_add_u32_sdwa v105, s92, v105 dst_sel:DWORD dst_unused:UNUSED_PAD src0_sel:DWORD src1_sel:WORD_0\n\t"
    "ds_read_b64 v[88:89], v105 offset:0\n\t"
    "ds_read_b64 v[90:91], v90 offset:0\n\t"
    "v_add_u32_sdwa v126, s92, v106 dst_sel:DWORD dst_unused:UNUSED_PAD src0_sel:DWORD src1_sel:WORD_1\n\t"
    "v_add_u32_sdwa v106, s92, v106 dst_sel:DWORD dst_unused:UNUSED_PAD src0_sel:DWORD src1_sel:WORD_0\n\t"
    "s_waitcnt vmcnt(2) lgkmcnt(5)\n\t"
    "v_smfmac_f32_16x16x64_bf16 v[116:119], v[120:123], v[92:99], v124\n\t"
    "ds_read_b64 v[92:93], v106 offset:0\n\t"
    "ds_read_b64 v[94:95], v126 offset:0\n\t"
    "v_add_u32_sdwa v98, s92, v107 dst_sel:DWORD dst_unused:UNUSED_PAD src0_sel:DWORD src1_sel:WORD_1\n\t"
    "v_add_u32_sdwa v107, s92, v107 dst_sel:DWORD dst_unused:UNUSED_PAD src0_sel:DWORD src1_sel:WORD_0\n\t"
    "ds_read_b64 v[96:97], v107 offset:0\n\t"
    "ds_read_b64 v[98:99], v98 offset:0\n\t"
    "buffer_load_dwordx4 v[104:107], v127, %12, s84 offen offset:1024 sc1\n\t"
    "v_add_u32_sdwa v126, s92, v108 dst_sel:DWORD dst_unused:UNUSED_PAD src0_sel:DWORD src1_sel:WORD_1\n\t"
    "v_cvt_pk_bf16_f32 v48, v116, v117\n\t"
    "v_cvt_pk_bf16_f32 v49, v118, v119\n\t"
    "v_and_b32 v119, s67, v51\n\t"
    "v_lshlrev_b32 v118, 16, v51\n\t"
    "v_and_b32 v117, s67, v50\n\t"
    "v_lshlrev_b32 v116, 16, v50\n\t"
    "v_add_u32_sdwa v108, s92, v108 dst_sel:DWORD dst_unused:UNUSED_PAD src0_sel:DWORD src1_sel:WORD_0\n\t"
    "s_waitcnt lgkmcnt(4)\n\t"
    "v_smfmac_f32_16x16x64_bf16 v[116:119], v[120:123], v[84:91], v124\n\t"
    "ds_read_b64 v[84:85], v108 offset:0\n\t"
    "v_mfma_f32_16x16x32_bf16 v[80:83], v[64:67], v[212:215], 0\n\t"
    "ds_read_b64 v[86:87], v126 offset:0\n\t"
    "v_mfma_f32_16x16x32_bf16 v[80:83], v[68:71], v[216:219], v[80:83]\n\t"
    "v_add_u32_sdwa v90, s92, v109 dst_sel:DWORD dst_unused:UNUSED_PAD src0_sel:DWORD src1_sel:WORD_1\n\t"
    "v_add_u32_sdwa v109, s92, v109 dst_sel:DWORD dst_unused:UNUSED_PAD src0_sel:DWORD src1_sel:WORD_0\n\t"
    "ds_read_b64 v[88:89], v109 offset:0\n\t"
    "v_mfma_f32_16x16x32_bf16 v[80:83], v[72:75], v[148:151], v[80:83]\n\t"
    "ds_read_b64 v[90:91], v90 offset:0\n\t"
    "v_mfma_f32_16x16x32_bf16 v[80:83], v[76:79], v[152:155], v[80:83]\n\t"
    "v_add_u32_sdwa v126, s92, v110 dst_sel:DWORD dst_unused:UNUSED_PAD src0_sel:DWORD src1_sel:WORD_1\n\t"
    "v_add_u32_sdwa v110, s92, v110 dst_sel:DWORD dst_unused:UNUSED_PAD src0_sel:DWORD src1_sel:WORD_0\n\t"
    "s_waitcnt vmcnt(2) lgkmcnt(4)\n\t"
    "v_smfmac_f32_16x16x64_bf16 v[116:119], v[120:123], v[92:99], v124\n\t"
    "ds_read_b64 v[92:93], v110 offset:0\n\t"
    "ds_read_b64 v[94:95], v126 offset:0\n\t"
    "v_add_u32_sdwa v98, s92, v111 dst_sel:DWORD dst_unused:UNUSED_PAD src0_sel:DWORD src1_sel:WORD_1\n\t"
    "v_add_u32_sdwa v111, s92, v111 dst_sel:DWORD dst_unused:UNUSED_PAD src0_sel:DWORD src1_sel:WORD_0\n\t"
    "ds_read_b64 v[96:97], v111 offset:0\n\t"
    "ds_read_b64 v[98:99], v98 offset:0\n\t"
    "s_add_u32 s85, s85, s95\n\t"
    "buffer_load_dwordx4 v[108:111], v127, %12, s85 offen offset:0 sc1\n\t"
    "v_add_u32_sdwa v126, s92, v112 dst_sel:DWORD dst_unused:UNUSED_PAD src0_sel:DWORD src1_sel:WORD_1\n\t"
    "v_cvt_pk_bf16_f32 v50, v116, v117\n\t"
    "v_cvt_pk_bf16_f32 v51, v118, v119\n\t"
    "v_and_b32 v119, s67, v57\n\t"
    "v_lshlrev_b32 v118, 16, v57\n\t"
    "v_and_b32 v117, s67, v56\n\t"
    "v_lshlrev_b32 v116, 16, v56\n\t"
    "v_add_u32_sdwa v112, s92, v112 dst_sel:DWORD dst_unused:UNUSED_PAD src0_sel:DWORD src1_sel:WORD_0\n\t"
    "s_waitcnt lgkmcnt(4)\n\t"
    "v_smfmac_f32_16x16x64_bf16 v[116:119], v[120:123], v[84:91], v124\n\t"
    "ds_read_b64 v[84:85], v112 offset:0\n\t"
    "ds_read_b64 v[86:87], v126 offset:0\n\t"
    "v_cvt_pk_bf16_f32 v80, v80, v81\n\t"
    "v_cvt_pk_bf16_f32 v81, v82, v83\n\t"
    "ds_write_b64 v125, v[80:81] offset:32872\n\t"
    "v_add_u32_sdwa v90, s92, v113 dst_sel:DWORD dst_unused:UNUSED_PAD src0_sel:DWORD src1_sel:WORD_1\n\t"
    "v_add_u32_sdwa v113, s92, v113 dst_sel:DWORD dst_unused:UNUSED_PAD src0_sel:DWORD src1_sel:WORD_0\n\t"
    "ds_read_b64 v[88:89], v113 offset:0\n\t"
    "ds_read_b64 v[90:91], v90 offset:0\n\t"
    "v_add_u32_sdwa v126, s92, v114 dst_sel:DWORD dst_unused:UNUSED_PAD src0_sel:DWORD src1_sel:WORD_1\n\t"
    "v_add_u32_sdwa v114, s92, v114 dst_sel:DWORD dst_unused:UNUSED_PAD src0_sel:DWORD src1_sel:WORD_0\n\t"
    "s_waitcnt vmcnt(2) lgkmcnt(5)\n\t"
    "v_smfmac_f32_16x16x64_bf16 v[116:119], v[120:123], v[92:99], v124\n\t"
    "ds_read_b64 v[92:93], v114 offset:0\n\t"
    "ds_read_b64 v[94:95], v126 offset:0\n\t"
    "v_add_u32_sdwa v98, s92, v115 dst_sel:DWORD dst_unused:UNUSED_PAD src0_sel:DWORD src1_sel:WORD_1\n\t"
    "v_add_u32_sdwa v115, s92, v115 dst_sel:DWORD dst_unused:UNUSED_PAD src0_sel:DWORD src1_sel:WORD_0\n\t"
    "ds_read_b64 v[96:97], v115 offset:0\n\t"
    "ds_read_b64 v[98:99], v98 offset:0\n\t"
    "buffer_load_dwordx4 v[112:115], v127, %12, s85 offen offset:1024 sc1\n\t"
    "s_sub_u32 s92, 64000, s92\n\t"
    "v_add_u32_sdwa v126, s92, v100 dst_sel:DWORD dst_unused:UNUSED_PAD src0_sel:DWORD src1_sel:WORD_1\n\t"
    "v_cvt_pk_bf16_f32 v56, v116, v117\n\t"
    "v_cvt_pk_bf16_f32 v57, v118, v119\n\t"
    "v_and_b32 v119, s67, v59\n\t"
    "v_lshlrev_b32 v118, 16, v59\n\t"
    "v_and_b32 v117, s67, v58\n\t"
    "v_lshlrev_b32 v116, 16, v58\n\t"
    "v_add_u32_sdwa v100, s92, v100 dst_sel:DWORD dst_unused:UNUSED_PAD src0_sel:DWORD src1_sel:WORD_0\n\t"
    "s_waitcnt lgkmcnt(0)\n\t"
    "s_barrier\n\t"
    "v_add_u32 v125, s93, v125\n\t"
    "s_sub_u32 s93, 0, s93\n\t"
    "v_smfmac_f32_16x16x64_bf16 v[116:119], v[120:123], v[84:91], v124\n\t"
    "ds_read_b64 v[84:85], v100 offset:0\n\t"
    "ds_read_b64 v[86:87], v126 offset:0\n\t"
    "v_add_u32_sdwa v90, s92, v101 dst_sel:DWORD dst_unused:UNUSED_PAD src0_sel:DWORD src1_sel:WORD_1\n\t"
    "v_add_u32_sdwa v101, s92, v101 dst_sel:DWORD dst_unused:UNUSED_PAD src0_sel:DWORD src1_sel:WORD_0\n\t"
    "ds_read_b64 v[88:89], v101 offset:0\n\t"
    "ds_read_b64 v[90:91], v90 offset:0\n\t"
    "v_add_u32_sdwa v126, s92, v102 dst_sel:DWORD dst_unused:UNUSED_PAD src0_sel:DWORD src1_sel:WORD_1\n\t"
    "v_add_u32_sdwa v102, s92, v102 dst_sel:DWORD dst_unused:UNUSED_PAD src0_sel:DWORD src1_sel:WORD_0\n\t"
    "s_cmp_lt_u32 %16, 4\n\t"
    "s_cbranch_scc1 L_tp63_%=\n\t"
    "s_setprio 1\n\t"
    "L_tp63_%=:\n\t"
    "s_waitcnt vmcnt(2)\n\t"
    "v_smfmac_f32_16x16x64_bf16 v[116:119], v[120:123], v[92:99], v124\n\t"
    "ds_read_b64 v[92:93], v102 offset:0\n\t"
    "ds_read_b64 v[94:95], v126 offset:0\n\t"
    "v_add_u32_sdwa v98, s92, v103 dst_sel:DWORD dst_unused:UNUSED_PAD src0_sel:DWORD src1_sel:WORD_1\n\t"
    "v_add_u32_sdwa v103, s92, v103 dst_sel:DWORD dst_unused:UNUSED_PAD src0_sel:DWORD src1_sel:WORD_0\n\t"
    "ds_read_b64 v[96:97], v103 offset:0\n\t"
    "ds_read_b64 v[98:99], v98 offset:0\n\t"
    "s_add_u32 s86, s86, s95\n\t"
    "buffer_load_dwordx4 v[100:103], v127, %12, s86 offen offset:0 sc1\n\t"
    "v_add_u32_sdwa v126, s92, v104 dst_sel:DWORD dst_unused:UNUSED_PAD src0_sel:DWORD src1_sel:WORD_1\n\t"
    "v_cvt_pk_bf16_f32 v58, v116, v117\n\t"
    "v_cvt_pk_bf16_f32 v59, v118, v119\n\t"
    "v_and_b32 v119, s67, v5\n\t"
    "v_lshlrev_b32 v118, 16, v5\n\t"
    "v_and_b32 v117, s67, v4\n\t"
    "v_lshlrev_b32 v116, 16, v4\n\t"
    "v_add_u32_sdwa v104, s92, v104 dst_sel:DWORD dst_unused:UNUSED_PAD src0_sel:DWORD src1_sel:WORD_0\n\t"
    "v_add_u32 v79, s96, v127\n\t"
    "s_add_u32 s96, s96, 0x1000\n\t"
    "s_and_b32 s96, s96, 0x3000\n\t"
    "v_add_u32 v79, 128000, v79\n\t"
    "ds_read_b128 v[64:67], v79 offset:0\n\t"
    "ds_read_b128 v[68:71], v79 offset:1024\n\t"
    "ds_read_b128 v[72:75], v79 offset:2048\n\t"
    "ds_read_b128 v[76:79], v79 offset:3072\n\t"
    "s_cmp_eq_u32 s66, s48\n\t"
    "s_cbranch_scc1 L_pub_%=\n\t"
    "L_pubb_%=:\n\t"
    "s_sub_u32 s66, s66, 1\n\t"
    "s_cmp_lg_u32 s66, 0\n\t"
    "s_cbranch_scc1 L_loop_%=\n\t"
    "s_mov_b32 s92, 0xffff\n\t"
    "s_waitcnt lgkmcnt(8)\n\t"
    "v_smfmac_f32_16x16x64_bf16 v[116:119], v[120:123], v[84:91], v124\n\t"
    "ds_read_b64 v[84:85], v104 offset:0\n\t"
    "ds_read_b64 v[86:87], v126 offset:0\n\t"
    "v_lshrrev_b32 v90, 16, v105\n\t"
    "v_and_b32 v105, s92, v105\n\t"
    "ds_read_b64 v[88:89], v105 offset:0\n\t"
    "ds_read_b64 v[90:91], v90 offset:0\n\t"
    "v_lshrrev_b32 v126, 16, v106\n\t"
    "v_and_b32 v106, s92, v106\n\t"
    "s_waitcnt vmcnt(2) lgkmcnt(8)\n\t"
    "v_smfmac_f32_16x16x64_bf16 v[116:119], v[120:123], v[92:99], v124\n\t"
    "ds_read_b64 v[92:93], v106 offset:0\n\t"
    "s_waitcnt lgkmcnt(5)\n\t"
    "v_mfma_f32_16x16x32_bf16 v[80:83], v[64:67], v[164:167], 0\n\t"
    "ds_read_b64 v[94:95], v126 offset:0\n\t"
    "v_mfma_f32_16x16x32_bf16 v[80:83], v[68:71], v[168:171], v[80:83]\n\t"
    "v_lshrrev_b32 v98, 16, v107\n\t"
    "v_and_b32 v107, s92, v107\n\t"
    "ds_read_b64 v[96:97], v107 offset:0\n\t"
    "v_mfma_f32_16x16x32_bf16 v[80:83], v[72:75], v[220:223], v[80:83]\n\t"
    "ds_read_b64 v[98:99], v98 offset:0\n\t"
    "v_mfma_f32_16x16x32_bf16 v[80:83], v[76:79], v[224:227], v[80:83]\n\t"
    "v_lshrrev_b32 v126, 16, v108\n\t"
    "v_cvt_pk_bf16_f32 v4, v116, v117\n\t"
    "v_cvt_pk_bf16_f32 v5, v118, v119\n\t"
    "v_and_b32 v119, s67, v7\n\t"
    "v_lshlrev_b32 v118, 16, v7\n\t"
    "v_and_b32 v117, s67, v6\n\t"
    "v_lshlrev_b32 v116, 16, v6\n\t"
    "v_and_b32 v108, s92, v108\n\t"
    "s_waitcnt lgkmcnt(4)\n\t"
    "v_smfmac_f32_16x16x64_bf16 v[116:119], v[120:123], v[84:91], v124\n\t"
    "ds_read_b64 v[84:85], v108 offset:0\n\t"
    "v_mfma_f32_16x16x32_bf16 v[104:107], v[64:67], v[172:175], 0\n\t"
    "ds_read_b64 v[86:87], v126 offset:0\n\t"
    "v_mfma_f32_16x16x32_bf16 v[104:107], v[68:71], v[176:179], v[104:107]\n\t"
    "v_lshrrev_b32 v90, 16, v109\n\t"
    "v_and_b32 v109, s92, v109\n\t"
    "ds_read_b64 v[88:89], v109 offset:0\n\t"
    "v_mfma_f32_16x16x32_bf16 v[104:107], v[72:75], v[228:231], v[104:107]\n\t"
    "ds_read_b64 v[90:91], v90 offset:0\n\t"
    "v_mfma_f32_16x16x32_bf16 v[104:107], v[76:79], v[232:235], v[104:107]\n\t"
    "v_lshrrev_b32 v126, 16, v110\n\t"
    "v_cvt_pk_bf16_f32 v80, v80, v81\n\t"
    "v_cvt_pk_bf16_f32 v81, v82, v83\n\t"
    "ds_write_b64 v125, v[80:81] offset:0\n\t"
    "v_mfma_f32_16x16x32_bf16 v[80:83], v[64:67], v[180:183], 0\n\t"
    "v_and_b32 v110, s92, v110\n\t"
    "v_mfma_f32_16x16x32_bf16 v[80:83], v[68:71], v[184:187], v[80:83]\n\t"
    "v_mfma_f32_16x16x32_bf16 v[80:83], v[72:75], v[236:239], v[80:83]\n\t"
    "s_waitcnt vmcnt(1) lgkmcnt(5)\n\t"
    "v_smfmac_f32_16x16x64_bf16 v[116:119], v[120:123], v[92:99], v124\n\t"
    "ds_read_b64 v[92:93], v110 offset:0\n\t"
    "v_mfma_f32_16x16x32_bf16 v[80:83], v[76:79], v[240:243], v[80:83]\n\t"
    "ds_read_b64 v[94:95], v126 offset:0\n\t"
    "v_cvt_pk_bf16_f32 v104, v104, v105\n\t"
    "v_cvt_pk_bf16_f32 v105, v106, v107\n\t"
    "ds_write_b64 v125, v[104:105] offset:128\n\t"
    "v_mfma_f32_16x16x32_bf16 v[104:107], v[64:67], v[188:191], 0\n\t"
    "v_lshrrev_b32 v98, 16, v111\n\t"
    "v_and_b32 v111, s92, v111\n\t"
    "ds_read_b64 v[96:97], v111 offset:0\n\t"
    "v_mfma_f32_16x16x32_bf16 v[104:107], v[68:71], v[192:195], v[104:107]\n\t"
    "ds_read_b64 v[98:99], v98 offset:0\n\t"
    "v_mfma_f32_16x16x32_bf16 v[104:107], v[72:75], v[244:247], v[104:107]\n\t"
    "buffer_load_dwordx4 v[108:111], v127, %12, s86 offen offset:1024 sc1\n\t"
    "v_lshrrev_b32 v126, 16, v112\n\t"
    "v_cvt_pk_bf16_f32 v6, v116, v117\n\t"
    "v_cvt_pk_bf16_f32 v7, v118, v119\n\t"
    "v_and_b32 v119, s67, v13\n\t"
    "v_lshlrev_b32 v118, 16, v13\n\t"
    "v_and_b32 v117, s67, v12\n\t"
    "v_lshlrev_b32 v116, 16, v12\n\t"
    "v_and_b32 v112, s92, v112\n\t"
    "s_waitcnt lgkmcnt(6)\n\t"
    "v_smfmac_f32_16x16x64_bf16 v[116:119], v[120:123], v[84:91], v124\n\t"
    "ds_read_b64 v[84:85], v112 offset:0\n\t"
    "v_mfma_f32_16x16x32_bf16 v[104:107], v[76:79], v[248:251], v[104:107]\n\t"
    "ds_read_b64 v[86:87], v126 offset:0\n\t"
    "v_cvt_pk_bf16_f32 v80, v80, v81\n\t"
    "v_cvt_pk_bf16_f32 v81, v82, v83\n\t"
    "ds_write_b64 v125, v[80:81] offset:256\n\t"
    "v_mfma_f32_16x16x32_bf16 v[80:83], v[64:67], v[196:199], 0\n\t"
    "v_lshrrev_b32 v90, 16, v113\n\t"
    "v_and_b32 v113, s92, v113\n\t"
    "ds_read_b64 v[88:89], v113 offset:0\n\t"
    "v_mfma_f32_16x16x32_bf16 v[80:83], v[68:71], v[200:203], v[80:83]\n\t"
    "ds_read_b64 v[90:91], v90 offset:0\n\t"
    "v_mfma_f32_16x16x32_bf16 v[80:83], v[72:75], v[252:255], v[80:83]\n\t"
    "v_lshrrev_b32 v126, 16, v114\n\t"
    "v_mfma_f32_16x16x32_bf16 v[80:83], v[76:79], v[128:131], v[80:83]\n\t"
    "v_and_b32 v114, s92, v114\n\t"
    "v_cvt_pk_bf16_f32 v104, v104, v105\n\t"
    "v_cvt_pk_bf16_f32 v105, v106, v107\n\t"
    "ds_write_b64 v125, v[104:105] offset:384\n\t"
    "v_mfma_f32_16x16x32_bf16 v[104:107], v[64:67], v[204:207], 0\n\t"
    "s_waitcnt vmcnt(1) lgkmcnt(6)\n\t"
    "v_smfmac_f32_16x16x64_bf16 v[116:119], v[120:123], v[92:99], v124\n\t"
    "ds_read_b64 v[92:93], v114 offset:0\n\t"
    "v_mfma_f32_16x16x32_bf16 v[104:107], v[68:71], v[208:211], v[104:107]\n\t"
    "ds_read_b64 v[94:95], v126 offset:0\n\t"
    "v_mfma_f32_16x16x32_bf16 v[104:107], v[72:75], v[132:135], v[104:107]\n\t"
    "v_lshrrev_b32 v98, 16, v115\n\t"
    "v_and_b32 v115, s92, v115\n\t"
    "ds_read_b64 v[96:97], v115 offset:0\n\t"
    "v_mfma_f32_16x16x32_bf16 v[104:107], v[76:79], v[136:139], v[104:107]\n\t"
    "ds_read_b64 v[98:99], v98 offset:0\n\t"
    "v_cvt_pk_bf16_f32 v80, v80, v81\n\t"
    "v_cvt_pk_bf16_f32 v81, v82, v83\n\t"
    "ds_write_b64 v125, v[80:81] offset:512\n\t"
    "v_mfma_f32_16x16x32_bf16 v[80:83], v[64:67], v[156:159], 0\n\t"
    "s_add_u32 s87, s87, s95\n\t"
    "buffer_load_dwordx4 v[112:115], v127, %12, s87 offen offset:0 sc1\n\t"
    "v_lshrrev_b32 v126, 16, v100\n\t"
    "v_mfma_f32_16x16x32_bf16 v[80:83], v[68:71], v[160:163], v[80:83]\n\t"
    "v_cvt_pk_bf16_f32 v12, v116, v117\n\t"
    "v_cvt_pk_bf16_f32 v13, v118, v119\n\t"
    "v_and_b32 v119, s67, v15\n\t"
    "v_lshlrev_b32 v118, 16, v15\n\t"
    "v_and_b32 v117, s67, v14\n\t"
    "v_lshlrev_b32 v116, 16, v14\n\t"
    "v_and_b32 v100, s92, v100\n\t"
    "s_waitcnt lgkmcnt(6)\n\t"
    "v_smfmac_f32_16x16x64_bf16 v[116:119], v[120:123], v[84:91], v124\n\t"
    "ds_read_b64 v[84:85], v100 offset:0\n\t"
    "v_mfma_f32_16x16x32_bf16 v[80:83], v[72:75], v[140:143], v[80:83]\n\t"
    "ds_read_b64 v[86:87], v126 offset:0\n\t"
    "v_mfma_f32_16x16x32_bf16 v[80:83], v[76:79], v[144:147], v[80:83]\n\t"
    "v_lshrrev_b32 v90, 16, v101\n\t"
    "v_and_b32 v101, s92, v101\n\t"
    "ds_read_b64 v[88:89], v101 offset:0\n\t"
    "v_cvt_pk_bf16_f32 v104, v104, v105\n\t"
    "v_cvt_pk_bf16_f32 v105, v106, v107\n\t"
    "ds_write_b64 v125, v[104:105] offset:640\n\t"
    "v_mfma_f32_16x16x32_bf16 v[104:107], v[64:67], v[212:215], 0\n\t"
    "ds_read_b64 v[90:91], v90 offset:0\n\t"
    "v_mfma_f32_16x16x32_bf16 v[104:107], v[68:71], v[216:219], v[104:107]\n\t"
    "v_lshrrev_b32 v126, 16, v102\n\t"
    "v_mfma_f32_16x16x32_bf16 v[104:107], v[72:75], v[148:151], v[104:107]\n\t"
    "v_and_b32 v102, s92, v102\n\t"
    "v_mfma_f32_16x16x32_bf16 v[104:107], v[76:79], v[152:155], v[104:107]\n\t"
    "v_add_u32 v79, s96, v127\n\t"
    "s_add_u32 s96, s96, 0x1000\n\t"
    "s_and_b32 s96, s96, 0x3000\n\t"
    "v_add_u32 v79, 128000, v79\n\t"
    "ds_read_b128 v[64:67], v79 offset:0\n\t"
    "ds_read_b128 v[68:71], v79 offset:1024\n\t"
    "ds_read_b128 v[72:75], v79 offset:2048\n\t"
    "ds_read_b128 v[76:79], v79 offset:3072\n\t"
    "v_cvt_pk_bf16_f32 v80, v80, v81\n\t"
    "v_cvt_pk_bf16_f32 v81, v82, v83\n\t"
    "ds_write_b64 v125, v[80:81] offset:768\n\t"
    "s_waitcnt vmcnt(1) lgkmcnt(11)\n\t"
    "v_smfmac_f32_16x16x64_bf16 v[116:119], v[120:123], v[92:99], v124\n\t"
    "ds_read_b64 v[92:93], v102 offset:0\n\t"
    "ds_read_b64 v[94:95], v126 offset:0\n\t"
    "v_lshrrev_b32 v98, 16, v103\n\t"
    "v_and_b32 v103, s92, v103\n\t"
    "ds_read_b64 v[96:97], v103 offset:0\n\t"
    "ds_read_b64 v[98:99], v98 offset:0\n\t"
    "buffer_load_dwordx4 v[100:103], v127, %12, s87 offen offset:1024 sc1\n\t"
    "v_lshrrev_b32 v126, 16, v108\n\t"
    "v_cvt_pk_bf16_f32 v14, v116, v117\n\t"
    "v_cvt_pk_bf16_f32 v15, v118, v119\n\t"
    "v_and_b32 v119, s67, v21\n\t"
    "v_lshlrev_b32 v118, 16, v21\n\t"
    "v_and_b32 v117, s67, v20\n\t"
    "v_lshlrev_b32 v116, 16, v20\n\t"
    "v_and_b32 v108, s92, v108\n\t"
    "s_waitcnt lgkmcnt(9)\n\t"
    "v_smfmac_f32_16x16x64_bf16 v[116:119], v[120:123], v[84:91], v124\n\t"
    "ds_read_b64 v[84:85], v108 offset:0\n\t"
    "s_waitcnt lgkmcnt(6)\n\t"
    "v_mfma_f32_16x16x32_bf16 v[80:83], v[64:67], v[164:167], 0\n\t"
    "ds_read_b64 v[86:87], v126 offset:0\n\t"
    "v_mfma_f32_16x16x32_bf16 v[80:83], v[68:71], v[168:171], v[80:83]\n\t"
    "v_lshrrev_b32 v90, 16, v109\n\t"
    "v_and_b32 v109, s92, v109\n\t"
    "ds_read_b64 v[88:89], v109 offset:0\n\t"
    "v_mfma_f32_16x16x32_bf16 v[80:83], v[72:75], v[220:223], v[80:83]\n\t"
    "ds_read_b64 v[90:91], v90 offset:0\n\t"
    "v_mfma_f32_16x16x32_bf16 v[80:83], v[76:79], v[224:227], v[80:83]\n\t"
    "v_lshrrev_b32 v126, 16, v110\n\t"
    "v_cvt_pk_bf16_f32 v104, v104, v105\n\t"
    "v_cvt_pk_bf16_f32 v105, v106, v107\n\t"
    "ds_write_b64 v125, v[104:105] offset:872\n\t"
    "v_mfma_f32_16x16x32_bf16 v[104:107], v[64:67], v[172:175], 0\n\t"
    "v_and_b32 v110, s92, v110\n\t"
    "v_mfma_f32_16x16x32_bf16 v[104:107], v[68:71], v[176:179], v[104:107]\n\t"
    "s_waitcnt vmcnt(1) lgkmcnt(5)\n\t"
    "v_smfmac_f32_16x16x64_bf16 v[116:119], v[120:123], v[92:99], v124\n\t"
    "ds_read_b64 v[92:93], v110 offset:0\n\t"
    "v_mfma_f32_16x16x32_bf16 v[104:107], v[72:75], v[228:231], v[104:107]\n\t"
    "ds_read_b64 v[94:95], v126 offset:0\n\t"
    "v_mfma_f32_16x16x32_bf16 v[104:107], v[76:79], v[232:235], v[104:107]\n\t"
    "v_lshrrev_b32 v98, 16, v111\n\t"
    "v_and_b32 v111, s92, v111\n\t"
    "ds_read_b64 v[96:97], v111 offset:0\n\t"
    "v_cvt_pk_bf16_f32 v80, v80, v81\n\t"
    "v_cvt_pk_bf16_f32 v81, v82, v83\n\t"
    "ds_write_b64 v125, v[80:81] offset:32000\n\t"
    "v_mfma_f32_16x16x32_bf16 v[80:83], v[64:67], v[180:183], 0\n\t"
    "ds_read_b64 v[98:99], v98 offset:0\n\t"
    "v_mfma_f32_16x16x32_bf16 v[80:83], v[68:71], v[184:187], v[80:83]\n\t"
    "s_add_u32 s84, s84, 0x8000\n\t"
    "buffer_load_dwordx4 v[108:111], v127, %12, s84 offen offset:0 sc1\n\t"
    "v_lshrrev_b32 v126, 16, v112\n\t"
    "v_mfma_f32_16x16x32_bf16 v[80:83], v[72:75], v[236:239], v[80:83]\n\t"
    "v_cvt_pk_bf16_f32 v20, v116, v117\n\t"
    "v_cvt_pk_bf16_f32 v21, v118, v119\n\t"
    "v_and_b32 v119, s67, v23\n\t"
    "v_lshlrev_b32 v118, 16, v23\n\t"
    "v_and_b32 v117, s67, v22\n\t"
    "v_lshlrev_b32 v116, 16, v22\n\t"
    "v_and_b32 v112, s92, v112\n\t"
    "s_waitcnt lgkmcnt(6)\n\t"
    "v_smfmac_f32_16x16x64_bf16 v[116:119], v[120:123], v[84:91], v124\n\t"
    "ds_read_b64 v[84:85], v112 offset:0\n\t"
    "v_mfma_f32_16x16x32_bf16 v[80:83], v[76:79], v[240:243], v[80:83]\n\t"
    "ds_read_b64 v[86:87], v126 offset:0\n\t"
    "v_cvt_pk_bf16_f32 v104, v104, v105\n\t"
    "v_cvt_pk_bf16_f32 v105, v106, v107\n\t"
    "ds_write_b64 v125, v[104:105] offset:32128\n\t"
    "v_mfma_f32_16x16x32_bf16 v[104:107], v[64:67], v[188:191], 0\n\t"
    "v_lshrrev_b32 v90, 16, v113\n\t"
    "v_and_b32 v113, s92, v113\n\t"
    "ds_read_b64 v[88:89], v113 offset:0\n\t"
    "v_mfma_f32_16x16x32_bf16 v[104:107], v[68:71], v[192:195], v[104:107]\n\t"
    "ds_read_b64 v[90:91], v90 offset:0\n\t"
    "v_mfma_f32_16x16x32_bf16 v[104:107], v[72:75], v[244:247], v[104:107]\n\t"
    "v_lshrrev_b32 v126, 16, v114\n\t"
    "v_mfma_f32_16x16x32_bf16 v[104:107], v[76:79], v[248:251], v[104:107]\n\t"
    "v_and_b32 v114, s92, v114\n\t"
    "v_cvt_pk_bf16_f32 v80, v80, v81\n\t"
    "v_cvt_pk_bf16_f32 v81, v82, v83\n\t"
    "ds_write_b64 v125, v[80:81] offset:32256\n\t"
    "v_mfma_f32_16x16x32_bf16 v[80:83], v[64:67], v[196:199], 0\n\t"
    "s_waitcnt vmcnt(1) lgkmcnt(6)\n\t"
    "v_smfmac_f32_16x16x64_bf16 v[116:119], v[120:123], v[92:99], v124\n\t"
    "ds_read_b64 v[92:93], v114 offset:0\n\t"
    "v_mfma_f32_16x16x32_bf16 v[80:83], v[68:71], v[200:203], v[80:83]\n\t"
    "ds_read_b64 v[94:95], v126 offset:0\n\t"
    "v_mfma_f32_16x16x32_bf16 v[80:83], v[72:75], v[252:255], v[80:83]\n\t"
    "v_lshrrev_b32 v98, 16, v115\n\t"
    "v_and_b32 v115, s92, v115\n\t"
    "ds_read_b64 v[96:97], v115 offset:0\n\t"
    "v_mfma_f32_16x16x32_bf16 v[80:83], v[76:79], v[128:131], v[80:83]\n\t"
    "ds_read_b64 v[98:99], v98 offset:0\n\t"
    "v_cvt_pk_bf16_f32 v104, v104, v105\n\t"
    "v_cvt_pk_bf16_f32 v105, v106, v107\n\t"
    "ds_write_b64 v125, v[104:105] offset:32384\n\t"
    "v_mfma_f32_16x16x32_bf16 v[104:107], v[64:67], v[204:207], 0\n\t"
    "buffer_load_dwordx4 v[112:115], v127, %12, s84 offen offset:1024 sc1\n\t"
    "v_lshrrev_b32 v126, 16, v100\n\t"
    "v_cvt_pk_bf16_f32 v22, v116, v117\n\t"
    "v_cvt_pk_bf16_f32 v23, v118, v119\n\t"
    "v_and_b32 v119, s67, v29\n\t"
    "v_lshlrev_b32 v118, 16, v29\n\t"
    "v_and_b32 v117, s67, v28\n\t"
    "v_lshlrev_b32 v116, 16, v28\n\t"
    "v_and_b32 v100, s92, v100\n\t"
    "s_waitcnt lgkmcnt(6)\n\t"
    "v_smfmac_f32_16x16x64_bf16 v[116:119], v[120:123], v[84:91], v124\n\t"
    "ds_read_b64 v[84:85], v100 offset:0\n\t"
    "v_mfma_f32_16x16x32_bf16 v[104:107], v[68:71], v[208:211], v[104:107]\n\t"
    "ds_read_b64 v[86:87], v126 offset:0\n\t"
    "v_mfma_f32_16x16x32_bf16 v[104:107], v[72:75], v[132:135], v[104:107]\n\t"
    "v_lshrrev_b32 v90, 16, v101\n\t"
    "v_and_b32 v101, s92, v101\n\t"
    "ds_read_b64 v[88:89], v101 offset:0\n\t"
    "v_mfma_f32_16x16x32_bf16 v[104:107], v[76:79], v[136:139], v[104:107]\n\t"
    "ds_read_b64 v[90:91], v90 offset:0\n\t"
    "v_cvt_pk_bf16_f32 v80, v80, v81\n\t"
    "v_cvt_pk_bf16_f32 v81, v82, v83\n\t"
    "ds_write_b64 v125, v[80:81] offset:32512\n\t"
    "v_mfma_f32_16x16x32_bf16 v[80:83], v[64:67], v[156:159], 0\n\t"
    "v_lshrrev_b32 v126, 16, v102\n\t"
    "v_mfma_f32_16x16x32_bf16 v[80:83], v[68:71], v[160:163], v[80:83]\n\t"
    "v_and_b32 v102, s92, v102\n\t"
    "v_mfma_f32_16x16x32_bf16 v[80:83], v[72:75], v[140:143], v[80:83]\n\t"
    "v_mfma_f32_16x16x32_bf16 v[80:83], v[76:79], v[144:147], v[80:83]\n\t"
    "v_cvt_pk_bf16_f32 v104, v104, v105\n\t"
    "v_cvt_pk_bf16_f32 v105, v106, v107\n\t"
    "ds_write_b64 v125, v[104:105] offset:32640\n\t"
    "s_waitcnt vmcnt(1) lgkmcnt(7)\n\t"
    "v_smfmac_f32_16x16x64_bf16 v[116:119], v[120:123], v[92:99], v124\n\t"
    "ds_read_b64 v[92:93], v102 offset:0\n\t"
    "v_mfma_f32_16x16x32_bf16 v[104:107], v[64:67], v[212:215], 0\n\t"
    "ds_read_b64 v[94:95], v126 offset:0\n\t"
    "v_mfma_f32_16x16x32_bf16 v[104:107], v[68:71], v[216:219], v[104:107]\n\t"
    "v_lshrrev_b32 v98, 16, v103\n\t"
    "v_and_b32 v103, s92, v103\n\t"
    "ds_read_b64 v[96:97], v103 offset:0\n\t"
    "v_mfma_f32_16x16x32_bf16 v[104:107], v[72:75], v[148:151], v[104:107]\n\t"
    "ds_read_b64 v[98:99], v98 offset:0\n\t"
    "v_mfma_f32_16x16x32_bf16 v[104:107], v[76:79], v[152:155], v[104:107]\n\t"
    "s_add_u32 s85, s85, 0x8000\n\t"
    "buffer_load_dwordx4 v[100:103], v127, %12, s85 offen offset:0 sc1\n\t"
    "v_lshrrev_b32 v126, 16, v108\n\t"
    "v_cvt_pk_bf16_f32 v80, v80, v81\n\t"
    "v_cvt_pk_bf16_f32 v81, v82, v83\n\t"
    "ds_write_b64 v125, v[80:81] offset:32768\n\t"
    "s_nop 1\n\t"
    "v_cvt_pk_bf16_f32 v104, v104, v105\n\t"
    "v_cvt_pk_bf16_f32 v105, v106, v107\n\t"
    "ds_write_b64 v125, v[104:105] offset:32872\n\t"
    "v_cvt_pk_bf16_f32 v28, v116, v117\n\t"
    "v_cvt_pk_bf16_f32 v29, v118, v119\n\t"
    "v_and_b32 v119, s67, v31\n\t"
    "v_lshlrev_b32 v118, 16, v31\n\t"
    "v_and_b32 v117, s67, v30\n\t"
    "v_lshlrev_b32 v116, 16, v30\n\t"
    "v_and_b32 v108, s92, v108\n\t"
    "s_waitcnt lgkmcnt(0)\n\t"
    "s_barrier\n\t"
    "v_smfmac_f32_16x16x64_bf16 v[116:119], v[120:123], v[84:91], v124\n\t"
    "ds_read_b64 v[84:85], v108 offset:64000\n\t"
    "ds_read_b64 v[86:87], v126 offset:64000\n\t"
    "v_lshrrev_b32 v90, 16, v109\n\t"
    "v_and_b32 v109, s92, v109\n\t"
    "ds_read_b64 v[88:89], v109 offset:64000\n\t"
    "ds_read_b64 v[90:91], v90 offset:64000\n\t"
    "buffer_load_dwordx4 v[104:107], v127, %12, s85 offen offset:1024 sc1\n\t"
    "v_lshrrev_b32 v126, 16, v110\n\t"
    "v_and_b32 v110, s92, v110\n\t"
    "s_waitcnt vmcnt(2)\n\t"
    "v_smfmac_f32_16x16x64_bf16 v[116:119], v[120:123], v[92:99], v124\n\t"
    "ds_read_b64 v[92:93], v110 offset:64000\n\t"
    "ds_read_b64 v[94:95], v126 offset:64000\n\t"
    "v_lshrrev_b32 v98, 16, v111\n\t"
    "v_and_b32 v111, s92, v111\n\t"
    "ds_read_b64 v[96:97], v111 offset:64000\n\t"
    "ds_read_b64 v[98:99], v98 offset:64000\n\t"
    "s_add_u32 s86, s86, 0x8000\n\t"
    "buffer_load_dwordx4 v[108:111], v127, %12, s86 offen offset:0 sc1\n\t"
    "v_lshrrev_b32 v252, 16, v112\n\t"
    "v_and_b32 v112, s92, v112\n\t"
    "v_lshrrev_b32 v253, 16, v113\n\t"
    "v_cvt_pk_bf16_f32 v30, v116, v117\n\t"
    "v_cvt_pk_bf16_f32 v31, v118, v119\n\t"
    "v_and_b32 v119, s67, v5\n\t"
    "v_lshlrev_b32 v118, 16, v5\n\t"
    "v_and_b32 v117, s67, v4\n\t"
    "v_lshlrev_b32 v116, 16, v4\n\t"
    "v_and_b32 v113, s92, v113\n\t"
    "v_mov_b32 v82, 0xbdd2d3e8\n\t"
    "v_mov_b32 v83, 0xbdd2d3e8\n\t"
    "v_mov_b32 v81, 0xc0135761\n\t"
    "v_lshrrev_b32 v125, 2, v127\n\t"
    "v_mov_b32 v80, 0xc0135761\n\t"
    "s_waitcnt lgkmcnt(4)\n\t"
    "v_smfmac_f32_16x16x64_bf16 v[116:119], v[120:123], v[84:91], v124\n\t"
    "ds_read_b64 v[84:85], v112 offset:64000\n\t"
    "ds_read_b64 v[86:87], v252 offset:64000\n\t"
    "ds_read_b64 v[88:89], v113 offset:64000\n\t"
    "ds_read_b64 v[90:91], v253 offset:64000\n\t"
    "v_lshrrev_b32 v254, 16, v114\n\t"
    "v_and_b32 v114, s92, v114\n\t"
    "v_lshrrev_b32 v255, 16, v115\n\t"
    "v_and_b32 v115, s92, v115\n\t"
    "s_lshl_b32 s65, %16, 10\n\t"
    "s_add_u32 s65, s65, 31457280\n\t"
    "s_xor_b32 s61, %14, 8\n\t"
    "s_mul_i32 s61, s61, 69632\n\t"
    "s_add_u32 s61, s61, s65\n\t"
    "s_mul_i32 s94, %14, 69632\n\t"
    "s_add_u32 s94, s94, s65\n\t"
    "s_lshl_b32 s65, %16, 9\n\t"
    "s_sub_u32 s65, 0x10000, s65\n\t"
    "s_add_u32 s93, s94, s65\n\t"
    "s_add_u32 s96, s61, s65\n\t"
    "v_mov_b32 v165, 1\n\t"
    "s_or_b32 s97, %14, %16\n\t"
    "s_cbranch_scc0 L_part0_%=\n\t"
    "L_partb0_%=:\n\t"
    "s_waitcnt vmcnt(2) lgkmcnt(4)\n\t"
    "v_smfmac_f32_16x16x64_bf16 v[116:119], v[120:123], v[92:99], v124\n\t"
    "ds_read_b64 v[92:93], v114 offset:64000\n\t"
    "ds_read_b64 v[94:95], v254 offset:64000\n\t"
    "ds_read_b64 v[96:97], v115 offset:64000\n\t"
    "ds_read_b64 v[98:99], v255 offset:64000\n\t"
    "buffer_load_dwordx4 v[172:175], v127, %12, s86 offen offset:1024 sc1\n\t"
    "v_lshrrev_b32 v252, 16, v100\n\t"
    "v_and_b32 v100, s92, v100\n\t"
    "v_lshrrev_b32 v253, 16, v101\n\t"
    "v_cvt_pk_bf16_f32 v4, v116, v117\n\t"
    "v_cvt_pk_bf16_f32 v5, v118, v119\n\t"
    "v_and_b32 v119, s67, v7\n\t"
    "v_lshlrev_b32 v118, 16, v7\n\t"
    "v_and_b32 v117, s67, v6\n\t"
    "v_lshlrev_b32 v116, 16, v6\n\t"
    "v_and_b32 v101, s92, v101\n\t"
    "v_mov_b32 v156, v120\n\t"
    "v_mov_b32 v160, 0\n\t"
    "v_mov_b32 v161, v121\n\t"
    "v_mov_b32 v157, 0\n\t"
    "v_mov_b32 v158, v122\n\t"
    "v_mov_b32 v162, 0\n\t"
    "v_mov_b32 v163, v123\n\t"
    "v_mov_b32 v159, 0\n\t"
    "s_waitcnt lgkmcnt(4)\n\t"
    "v_smfmac_f32_16x16x64_bf16 v[116:119], v[120:123], v[84:91], v124\n\t"
    "ds_read_b64 v[84:85], v100 offset:64000\n\t"
    "ds_read_b64 v[86:87], v252 offset:64000\n\t"
    "ds_read_b64 v[88:89], v101 offset:64000\n\t"
    "ds_read_b64 v[90:91], v253 offset:64000\n\t"
    "s_add_u32 s87, s87, 0x8000\n\t"
    "buffer_load_dwordx4 v[176:179], v127, %12, s87 offen offset:0 sc1\n\t"
    "v_lshrrev_b32 v254, 16, v102\n\t"
    "v_and_b32 v102, s92, v102\n\t"
    "v_lshrrev_b32 v255, 16, v103\n\t"
    "v_and_b32 v103, s92, v103\n\t"
    "s_cmp_lt_u32 %16, 4\n\t"
    "s_cbranch_scc1 L_tp163_%=\n\t"
    "s_setprio 0\n\t"
    "L_tp163_%=:\n\t"
    "s_waitcnt vmcnt(3) lgkmcnt(4)\n\t"
    "v_smfmac_f32_16x16x64_bf16 v[116:119], v[120:123], v[92:99], v124\n\t"
    "ds_read_b64 v[92:93], v102 offset:64000\n\t"
    "ds_read_b64 v[94:95], v254 offset:64000\n\t"
    "ds_read_b64 v[96:97], v103 offset:64000\n\t"
    "ds_read_b64 v[98:99], v255 offset:64000\n\t"
    "buffer_load_dwordx4 v[180:183], v127, %12, s87 offen offset:1024 sc1\n\t"
    "v_lshrrev_b32 v252, 16, v104\n\t"
    "v_and_b32 v104, s92, v104\n\t"
    "v_lshrrev_b32 v253, 16, v105\n\t"
    "v_cvt_pk_bf16_f32 v6, v116, v117\n\t"
    "v_cvt_pk_bf16_f32 v7, v118, v119\n\t"
    "v_and_b32 v119, s67, v13\n\t"
    "v_lshlrev_b32 v118, 16, v13\n\t"
    "v_and_b32 v117, s67, v12\n\t"
    "v_lshlrev_b32 v116, 16, v12\n\t"
    "v_and_b32 v105, s92, v105\n\t"
    "buffer_store_dwordx4 v[4:7], v127, %12, s61 offen sc1\n\t"
    "s_waitcnt lgkmcnt(4)\n\t"
    "v_smfmac_f32_16x16x64_bf16 v[116:119], v[120:123], v[84:91], v124\n\t"
    "ds_read_b64 v[84:85], v104 offset:64000\n\t"
    "ds_read_b64 v[86:87], v252 offset:64000\n\t"
    "ds_read_b64 v[88:89], v105 offset:64000\n\t"
    "ds_read_b64 v[90:91], v253 offset:64000\n\t"
    "s_add_u32 s88, s88, s95\n\t"
    "buffer_load_dwordx4 v[184:187], v127, %12, s88 offen offset:0 sc1\n\t"
    "v_lshrrev_b32 v254, 16, v106\n\t"
    "v_and_b32 v106, s92, v106\n\t"
    "v_lshrrev_b32 v255, 16, v107\n\t"
    "v_and_b32 v107, s92, v107\n\t"
    "s_waitcnt vmcnt(5) lgkmcnt(4)\n\t"
    "v_smfmac_f32_16x16x64_bf16 v[116:119], v[120:123], v[92:99], v124\n\t"
    "ds_read_b64 v[92:93], v106 offset:64000\n\t"
    "ds_read_b64 v[94:95], v254 offset:64000\n\t"
    "ds_read_b64 v[96:97], v107 offset:64000\n\t"
    "ds_read_b64 v[98:99], v255 offset:64000\n\t"
    "s_add_u32 s88, s88, 0x8000\n\t"
    "buffer_load_dwordx4 v[188:191], v127, %12, s88 offen offset:0 sc1\n\t"
    "v_lshrrev_b32 v252, 16, v108\n\t"
    "v_and_b32 v108, s92, v108\n\t"
    "v_lshrrev_b32 v253, 16, v109\n\t"
    "v_cvt_pk_bf16_f32 v12, v116, v117\n\t"
    "v_cvt_pk_bf16_f32 v13, v118, v119\n\t"
    "v_and_b32 v119, s67, v15\n\t"
    "v_lshlrev_b32 v118, 16, v15\n\t"
    "v_and_b32 v117, s67, v14\n\t"
    "v_lshlrev_b32 v116, 16, v14\n\t"
    "v_and_b32 v109, s92, v109\n\t"
    "s_waitcnt lgkmcnt(4)\n\t"
    "v_smfmac_f32_16x16x64_bf16 v[116:119], v[120:123], v[84:91], v124\n\t"
    "ds_read_b64 v[84:85], v108 offset:64000\n\t"
    "ds_read_b64 v[86:87], v252 offset:64000\n\t"
    "ds_read_b64 v[88:89], v109 offset:64000\n\t"
    "ds_read_b64 v[90:91], v253 offset:64000\n\t"
    "s_sub_u32 s88, s88, 0x8000\n\t"
    "buffer_load_dwordx4 v[236:239], v127, %12, s88 offen offset:1024 sc1\n\t"
    "v_lshrrev_b32 v254, 16, v110\n\t"
    "v_and_b32 v110, s92, v110\n\t"
    "v_lshrrev_b32 v255, 16, v111\n\t"
    "v_and_b32 v111, s92, v111\n\t"
    "s_waitcnt vmcnt(6) lgkmcnt(4)\n\t"
    "v_smfmac_f32_16x16x64_bf16 v[116:119], v[120:123], v[92:99], v124\n\t"
    "ds_read_b64 v[92:93], v110 offset:64000\n\t"
    "ds_read_b64 v[94:95], v254 offset:64000\n\t"
    "ds_read_b64 v[96:97], v111 offset:64000\n\t"
    "ds_read_b64 v[98:99], v255 offset:64000\n\t"
    "s_add_u32 s88, s88, 0x8000\n\t"
    "buffer_load_dwordx4 v[192:195], v127, %12, s88 offen offset:1024 sc1\n\t"
    "v_lshrrev_b32 v252, 16, v172\n\t"
    "v_and_b32 v172, s92, v172\n\t"
    "v_lshrrev_b32 v253, 16, v173\n\t"
    "v_cvt_pk_bf16_f32 v14, v116, v117\n\t"
    "v_cvt_pk_bf16_f32 v15, v118, v119\n\t"
    "v_and_b32 v119, s67, v21\n\t"
    "v_lshlrev_b32 v118, 16, v21\n\t"
    "v_and_b32 v117, s67, v20\n\t"
    "v_lshlrev_b32 v116, 16, v20\n\t"
    "v_and_b32 v173, s92, v173\n\t"
    "s_waitcnt lgkmcnt(4)\n\t"
    "v_smfmac_f32_16x16x64_bf16 v[116:119], v[120:123], v[84:91], v124\n\t"
    "ds_read_b64 v[84:85], v172 offset:64000\n\t"
    "ds_read_b64 v[86:87], v252 offset:64000\n\t"
    "ds_read_b64 v[88:89], v173 offset:64000\n\t"
    "ds_read_b64 v[90:91], v253 offset:64000\n\t"
    "v_lshrrev_b32 v254, 16, v174\n\t"
    "v_and_b32 v174, s92, v174\n\t"
    "v_lshrrev_b32 v255, 16, v175\n\t"
    "v_and_b32 v175, s92, v175\n\t"
    "s_cmp_lg_u32 s97, 0\n\t"
    "s_cbranch_scc0 L_part1_%=\n\t"
    "L_partb1_%=:\n\t"
    "s_waitcnt vmcnt(6) lgkmcnt(4)\n\t"
    "v_smfmac_f32_16x16x64_bf16 v[116:119], v[120:123], v[92:99], v124\n\t"
    "ds_read_b64 v[92:93], v174 offset:64000\n\t"
    "ds_read_b64 v[94:95], v254 offset:64000\n\t"
    "ds_read_b64 v[96:97], v175 offset:64000\n\t"
    "ds_read_b64 v[98:99], v255 offset:64000\n\t"
    "s_add_u32 s89, s89, s95\n\t"
    "buffer_load_dwordx4 v[196:199], v127, %12, s89 offen offset:0 sc1\n\t"
    "v_lshrrev_b32 v252, 16, v176\n\t"
    "v_and_b32 v176, s92, v176\n\t"
    "v_lshrrev_b32 v253, 16, v177\n\t"
    "v_cvt_pk_bf16_f32 v20, v116, v117\n\t"
    "v_cvt_pk_bf16_f32 v21, v118, v119\n\t"
    "v_and_b32 v119, s67, v23\n\t"
    "v_lshlrev_b32 v118, 16, v23\n\t"
    "v_and_b32 v117, s67, v22\n\t"
    "v_lshlrev_b32 v116, 16, v22\n\t"
    "v_and_b32 v177, s92, v177\n\t"
    "s_waitcnt lgkmcnt(4)\n\t"
    "v_smfmac_f32_16x16x64_bf16 v[116:119], v[120:123], v[84:91], v124\n\t"
    "ds_read_b64 v[84:85], v176 offset:64000\n\t"
    "ds_read_b64 v[86:87], v252 offset:64000\n\t"
    "ds_read_b64 v[88:89], v177 offset:64000\n\t"
    "ds_read_b64 v[90:91], v253 offset:64000\n\t"
    "v_lshrrev_b32 v254, 16, v178\n\t"
    "v_and_b32 v178, s92, v178\n\t"
    "v_lshrrev_b32 v255, 16, v179\n\t"
    "v_and_b32 v179, s92, v179\n\t"
    "s_waitcnt vmcnt(6) lgkmcnt(4)\n\t"
    "v_smfmac_f32_16x16x64_bf16 v[116:119], v[120:123], v[92:99], v124\n\t"
    "ds_read_b64 v[92:93], v178 offset:64000\n\t"
    "ds_read_b64 v[94:95], v254 offset:64000\n\t"
    "ds_read_b64 v[96:97], v179 offset:64000\n\t"
    "ds_read_b64 v[98:99], v255 offset:64000\n\t"
    "s_add_u32 s89, s89, 0x8000\n\t"
    "buffer_load_dwordx4 v[200:203], v127, %12, s89 offen offset:0 sc1\n\t"
    "v_lshrrev_b32 v252, 16, v180\n\t"
    "v_and_b32 v180, s92, v180\n\t"
    "v_lshrrev_b32 v253, 16, v181\n\t"
    "v_cvt_pk_bf16_f32 v22, v116, v117\n\t"
    "v_cvt_pk_bf16_f32 v23, v118, v119\n\t"
    "v_and_b32 v119, s67, v29\n\t"
    "v_lshlrev_b32 v118, 16, v29\n\t"
    "v_and_b32 v117, s67, v28\n\t"
    "v_lshlrev_b32 v116, 16, v28\n\t"
    "v_and_b32 v181, s92, v181\n\t"
    "s_add_u32 s61, s61, 0x2000\n\t"
    "buffer_store_dwordx4 v[12:15], v127, %12, s61 offen sc1\n\t"
    "s_waitcnt lgkmcnt(4)\n\t"
    "v_smfmac_f32_16x16x64_bf16 v[116:119], v[120:123], v[84:91], v124\n\t"
    "ds_read_b64 v[84:85], v180 offset:64000\n\t"
    "ds_read_b64 v[86:87], v252 offset:64000\n\t"
    "ds_read_b64 v[88:89], v181 offset:64000\n\t"
    "ds_read_b64 v[90:91], v253 offset:64000\n\t"
    "v_lshrrev_b32 v254, 16, v182\n\t"
    "v_and_b32 v182, s92, v182\n\t"
    "v_lshrrev_b32 v255, 16, v183\n\t"
    "v_and_b32 v183, s92, v183\n\t"
    "s_waitcnt vmcnt(6) lgkmcnt(4)\n\t"
    "v_smfmac_f32_16x16x64_bf16 v[116:119], v[120:123], v[92:99], v124\n\t"
    "ds_read_b64 v[92:93], v182 offset:64000\n\t"
    "ds_read_b64 v[94:95], v254 offset:64000\n\t"
    "ds_read_b64 v[96:97], v183 offset:64000\n\t"
    "ds_read_b64 v[98:99], v255 offset:64000\n\t"
    "s_sub_u32 s89, s89, 0x8000\n\t"
    "buffer_load_dwordx4 v[204:207], v127, %12, s89 offen offset:1024 sc1\n\t"
    "v_lshrrev_b32 v252, 16, v184\n\t"
    "v_and_b32 v184, s92, v184\n\t"
    "v_lshrrev_b32 v253, 16, v185\n\t"
    "v_cvt_pk_bf16_f32 v28, v116, v117\n\t"
    "v_cvt_pk_bf16_f32 v29, v118, v119\n\t"
    "v_and_b32 v119, s67, v31\n\t"
    "v_lshlrev_b32 v118, 16, v31\n\t"
    "v_and_b32 v117, s67, v30\n\t"
    "v_lshlrev_b32 v116, 16, v30\n\t"
    "v_and_b32 v185, s92, v185\n\t"
    "s_waitcnt lgkmcnt(4)\n\t"
    "v_smfmac_f32_16x16x64_bf16 v[116:119], v[120:123], v[84:91], v124\n\t"
    "ds_read_b64 v[84:85], v184 offset:0\n\t"
    "ds_read_b64 v[86:87], v252 offset:0\n\t"
    "ds_read_b64 v[88:89], v185 offset:0\n\t"
    "ds_read_b64 v[90:91], v253 offset:0\n\t"
    "v_lshrrev_b32 v254, 16, v186\n\t"
    "v_and_b32 v186, s92, v186\n\t"
    "v_lshrrev_b32 v255, 16, v187\n\t"
    "v_and_b32 v187, s92, v187\n\t"
    "s_cmp_lt_u32 %16, 4\n\t"
    "s_cbranch_scc1 L_tp175_%=\n\t"
    "s_setprio 1\n\t"
    "L_tp175_%=:\n\t"
    "s_waitcnt vmcnt(6) lgkmcnt(4)\n\t"
    "v_smfmac_f32_16x16x64_bf16 v[116:119], v[120:123], v[92:99], v124\n\t"
    "ds_read_b64 v[92:93], v186 offset:0\n\t"
    "ds_read_b64 v[94:95], v254 offset:0\n\t"
    "ds_read_b64 v[96:97], v187 offset:0\n\t"
    "ds_read_b64 v[98:99], v255 offset:0\n\t"
    "s_add_u32 s89, s89, 0x8000\n\t"
    "buffer_load_dwordx4 v[144:147], v127, %12, s89 offen offset:1024 sc1\n\t"
    "v_lshrrev_b32 v252, 16, v188\n\t"
    "v_and_b32 v188, s92, v188\n\t"
    "v_lshrrev_b32 v253, 16, v189\n\t"
    "v_cvt_pk_bf16_f32 v30, v116, v117\n\t"
    "v_cvt_pk_bf16_f32 v31, v118, v119\n\t"
    "v_and_b32 v119, s67, v37\n\t"
    "v_lshlrev_b32 v118, 16, v37\n\t"
    "v_and_b32 v117, s67, v36\n\t"
    "v_lshlrev_b32 v116, 16, v36\n\t"
    "v_and_b32 v189, s92, v189\n\t"
    "s_waitcnt lgkmcnt(4)\n\t"
    "v_smfmac_f32_16x16x64_bf16 v[116:119], v[120:123], v[84:91], v124\n\t"
    "ds_read_b64 v[84:85], v188 offset:64000\n\t"
    "ds_read_b64 v[86:87], v252 offset:64000\n\t"
    "ds_read_b64 v[88:89], v189 offset:64000\n\t"
    "ds_read_b64 v[90:91], v253 offset:64000\n\t"
    "v_lshrrev_b32 v254, 16, v190\n\t"
    "v_and_b32 v190, s92, v190\n\t"
    "v_lshrrev_b32 v255, 16, v191\n\t"
    "v_and_b32 v191, s92, v191\n\t"
    "s_waitcnt vmcnt(6) lgkmcnt(4)\n\t"
    "v_smfmac_f32_16x16x64_bf16 v[116:119], v[120:123], v[92:99], v124\n\t"
    "ds_read_b64 v[92:93], v190 offset:64000\n\t"
    "ds_read_b64 v[94:95], v254 offset:64000\n\t"
    "ds_read_b64 v[96:97], v191 offset:64000\n\t"
    "ds_read_b64 v[98:99], v255 offset:64000\n\t"
    "s_add_u32 s90, s90, s95\n\t"
    "buffer_load_dwordx4 v[148:151], v127, %12, s90 offen offset:0 sc1\n\t"
    "v_lshrrev_b32 v252, 16, v236\n\t"
    "v_and_b32 v236, s92, v236\n\t"
    "v_lshrrev_b32 v253, 16, v237\n\t"
    "v_and_b32 v237, s92, v237\n\t"
    "s_waitcnt lgkmcnt(4)\n\t"
    "v_smfmac_f32_16x16x64_bf16 v[116:119], v[120:123], v[84:91], v124\n\t"
    "ds_read_b64 v[84:85], v236 offset:0\n\t"
    "ds_read_b64 v[86:87], v252 offset:0\n\t"
    "ds_read_b64 v[88:89], v237 offset:0\n\t"
    "ds_read_b64 v[90:91], v253 offset:0\n\t"
    "v_lshrrev_b32 v254, 16, v238\n\t"
    "v_and_b32 v238, s92, v238\n\t"
    "v_lshrrev_b32 v255, 16, v239\n\t"
    "v_and_b32 v239, s92, v239\n\t"
    "s_waitcnt vmcnt(6) lgkmcnt(4)\n\t"
    "v_smfmac_f32_16x16x64_bf16 v[116:119], v[120:123], v[92:99], v124\n\t"
    "ds_read_b64 v[92:93], v238 offset:0\n\t"
    "ds_read_b64 v[94:95], v254 offset:0\n\t"
    "ds_read_b64 v[96:97], v239 offset:0\n\t"
    "ds_read_b64 v[98:99], v255 offset:0\n\t"
    "s_add_u32 s90, s90, 0x8000\n\t"
    "buffer_load_dwordx4 v[152:155], v127, %12, s90 offen offset:0 sc1\n\t"
    "v_lshrrev_b32 v252, 16, v192\n\t"
    "v_and_b32 v192, s92, v192\n\t"
    "v_lshrrev_b32 v253, 16, v193\n\t"
    "v_cvt_pk_bf16_f32 v36, v116, v117\n\t"
    "v_cvt_pk_bf16_f32 v37, v118, v119\n\t"
    "v_and_b32 v119, s67, v39\n\t"
    "v_lshlrev_b32 v118, 16, v39\n\t"
    "v_and_b32 v117, s67, v38\n\t"
    "v_lshlrev_b32 v116, 16, v38\n\t"
    "v_and_b32 v193, s92, v193\n\t"
    "s_waitcnt vmcnt(4)\n\t"
    "s_mov_b64 exec, 0xffffffff\n\t"
    "buffer_store_dword v165, v125, %12, s96 offen offset:0 sc1\n\t"
    "s_mov_b64 exec, -1\n\t"
    "s_add_u32 s61, s61, 0x2000\n\t"
    "buffer_store_dwordx4 v[20:23], v127, %12, s61 offen sc1\n\t"
    "s_waitcnt lgkmcnt(4)\n\t"
    "v_smfmac_f32_16x16x64_bf16 v[116:119], v[120:123], v[84:91], v124\n\t"
    "ds_read_b64 v[84:85], v192 offset:64000\n\t"
    "ds_read_b64 v[86:87], v252 offset:64000\n\t"
    "ds_read_b64 v[88:89], v193 offset:64000\n\t"
    "ds_read_b64 v[90:91], v253 offset:64000\n\t"
    "v_lshrrev_b32 v254, 16, v194\n\t"
    "v_and_b32 v194, s92, v194\n\t"
    "v_lshrrev_b32 v255, 16, v195\n\t"
    "v_and_b32 v195, s92, v195\n\t"
    "s_waitcnt lgkmcnt(4)\n\t"
    "v_smfmac_f32_16x16x64_bf16 v[116:119], v[120:123], v[92:99], v124\n\t"
    "ds_read_b64 v[92:93], v194 offset:64000\n\t"
    "ds_read_b64 v[94:95], v254 offset:64000\n\t"
    "ds_read_b64 v[96:97], v195 offset:64000\n\t"
    "ds_read_b64 v[98:99], v255 offset:64000\n\t"
    "s_sub_u32 s90, s90, 0x8000\n\t"
    "buffer_load_dwordx4 v[240:243], v127, %12, s90 offen offset:1024 sc1\n\t"
    "v_lshrrev_b32 v252, 16, v196\n\t"
    "v_and_b32 v196, s92, v196\n\t"
    "v_lshrrev_b32 v253, 16, v197\n\t"
    "v_and_b32 v197, s92, v197\n\t"
    "s_waitcnt lgkmcnt(4)\n\t"
    "v_smfmac_f32_16x16x64_bf16 v[116:119], v[120:123], v[84:91], v124\n\t"
    "ds_read_b64 v[84:85], v196 offset:0\n\t"
    "ds_read_b64 v[86:87], v252 offset:0\n\t"
    "ds_read_b64 v[88:89], v197 offset:0\n\t"
    "ds_read_b64 v[90:91], v253 offset:0\n\t"
    "v_lshrrev_b32 v254, 16, v198\n\t"
    "v_and_b32 v198, s92, v198\n\t"
    "v_lshrrev_b32 v255, 16, v199\n\t"
    "v_and_b32 v199, s92, v199\n\t"
    "s_waitcnt lgkmcnt(4)\n\t"
    "v_smfmac_f32_16x16x64_bf16 v[116:119], v[120:123], v[92:99], v124\n\t"
    "ds_read_b64 v[92:93], v198 offset:0\n\t"
    "ds_read_b64 v[94:95], v254 offset:0\n\t"
    "ds_read_b64 v[96:97], v199 offset:0\n\t"
    "ds_read_b64 v[98:99], v255 offset:0\n\t"
    "s_add_u32 s90, s90, 0x8000\n\t"
    "buffer_load_dwordx4 v[208:211], v127, %12, s90 offen offset:1024 sc1\n\t"
    "v_lshrrev_b32 v252, 16, v200\n\t"
    "v_and_b32 v200, s92, v200\n\t"
    "v_lshrrev_b32 v253, 16, v201\n\t"
    "v_cvt_pk_bf16_f32 v38, v116, v117\n\t"
    "v_cvt_pk_bf16_f32 v39, v118, v119\n\t"
    "v_and_b32 v119, s67, v45\n\t"
    "v_lshlrev_b32 v118, 16, v45\n\t"
    "v_and_b32 v117, s67, v44\n\t"
    "v_lshlrev_b32 v116, 16, v44\n\t"
    "v_and_b32 v201, s92, v201\n\t"
    "s_waitcnt lgkmcnt(4)\n\t"
    "v_smfmac_f32_16x16x64_bf16 v[116:119], v[120:123], v[84:91], v124\n\t"
    "ds_read_b64 v[84:85], v200 offset:64000\n\t"
    "ds_read_b64 v[86:87], v252 offset:64000\n\t"
    "ds_read_b64 v[88:89], v201 offset:64000\n\t"
    "ds_read_b64 v[90:91], v253 offset:64000\n\t"
    "v_lshrrev_b32 v254, 16, v202\n\t"
    "v_and_b32 v202, s92, v202\n\t"
    "v_lshrrev_b32 v255, 16, v203\n\t"
    "v_and_b32 v203, s92, v203\n\t"
    "s_waitcnt vmcnt(7) lgkmcnt(4)\n\t"
    "v_smfmac_f32_16x16x64_bf16 v[116:119], v[120:123], v[92:99], v124\n\t"
    "ds_read_b64 v[92:93], v202 offset:64000\n\t"
    "ds_read_b64 v[94:95], v254 offset:64000\n\t"
    "ds_read_b64 v[96:97], v203 offset:64000\n\t"
    "ds_read_b64 v[98:99], v255 offset:64000\n\t"
    "s_add_u32 s91, s91, s95\n\t"
    "buffer_load_dwordx4 v[244:247], v127, %12, s91 offen offset:0 sc1\n\t"
    "v_lshrrev_b32 v252, 16, v204\n\t"
    "v_and_b32 v204, s92, v204\n\t"
    "v_lshrrev_b32 v253, 16, v205\n\t"
    "v_and_b32 v205, s92, v205\n\t"
    "s_waitcnt lgkmcnt(4)\n\t"
    "v_smfmac_f32_16x16x64_bf16 v[116:119], v[120:123], v[84:91], v124\n\t"
    "ds_read_b64 v[84:85], v204 offset:0\n\t"
    "ds_read_b64 v[86:87], v252 offset:0\n\t"
    "ds_read_b64 v[88:89], v205 offset:0\n\t"
    "ds_read_b64 v[90:91], v253 offset:0\n\t"
    "v_lshrrev_b32 v254, 16, v206\n\t"
    "v_and_b32 v206, s92, v206\n\t"
    "v_lshrrev_b32 v255, 16, v207\n\t"
    "v_and_b32 v207, s92, v207\n\t"
    "s_waitcnt vmcnt(7) lgkmcnt(4)\n\t"
    "v_smfmac_f32_16x16x64_bf16 v[116:119], v[120:123], v[92:99], v124\n\t"
    "ds_read_b64 v[92:93], v206 offset:0\n\t"
    "ds_read_b64 v[94:95], v254 offset:0\n\t"
    "ds_read_b64 v[96:97], v207 offset:0\n\t"
    "ds_read_b64 v[98:99], v255 offset:0\n\t"
    "s_add_u32 s91, s91, 0x8000\n\t"
    "buffer_load_dwordx4 v[248:251], v127, %12, s91 offen offset:0 sc1\n\t"
    "v_lshrrev_b32 v252, 16, v144\n\t"
    "v_and_b32 v144, s92, v144\n\t"
    "v_lshrrev_b32 v253, 16, v145\n\t"
    "v_cvt_pk_bf16_f32 v44, v116, v117\n\t"
    "v_cvt_pk_bf16_f32 v45, v118, v119\n\t"
    "v_and_b32 v119, s67, v47\n\t"
    "v_lshlrev_b32 v118, 16, v47\n\t"
    "v_and_b32 v117, s67, v46\n\t"
    "v_lshlrev_b32 v116, 16, v46\n\t"
    "v_and_b32 v145, s92, v145\n\t"
    "s_add_u32 s61, s61, 0x2000\n\t"
    "buffer_store_dwordx4 v[28:31], v127, %12, s61 offen sc1\n\t"
    "s_waitcnt lgkmcnt(4)\n\t"
    "v_smfmac_f32_16x16x64_bf16 v[116:119], v[120:123], v[84:91], v124\n\t"
    "ds_read_b64 v[84:85], v144 offset:64000\n\t"
    "ds_read_b64 v[86:87], v252 offset:64000\n\t"
    "ds_read_b64 v[88:89], v145 offset:64000\n\t"
    "ds_read_b64 v[90:91], v253 offset:64000\n\t"
    "v_lshrrev_b32 v254, 16, v146\n\t"
    "v_and_b32 v146, s92, v146\n\t"
    "v_lshrrev_b32 v255, 16, v147\n\t"
    "v_and_b32 v147, s92, v147\n\t"
    "s_waitcnt vmcnt(8) lgkmcnt(4)\n\t"
    "v_smfmac_f32_16x16x64_bf16 v[116:119], v[120:123], v[92:99], v124\n\t"
    "ds_read_b64 v[92:93], v146 offset:64000\n\t"
    "ds_read_b64 v[94:95], v254 offset:64000\n\t"
    "ds_read_b64 v[96:97], v147 offset:64000\n\t"
    "ds_read_b64 v[98:99], v255 offset:64000\n\t"
    "s_sub_u32 s91, s91, 0x8000\n\t"
    "buffer_load_dwordx4 v[212:215], v127, %12, s91 offen offset:1024 sc1\n\t"
    "v_lshrrev_b32 v252, 16, v148\n\t"
    "v_and_b32 v148, s92, v148\n\t"
    "v_lshrrev_b32 v253, 16, v149\n\t"
    "v_and_b32 v149, s92, v149\n\t"
    "s_waitcnt lgkmcnt(4)\n\t"
    "v_smfmac_f32_16x16x64_bf16 v[116:119], v[120:123], v[84:91], v124\n\t"
    "ds_read_b64 v[84:85], v148 offset:0\n\t"
    "ds_read_b64 v[86:87], v252 offset:0\n\t"
    "ds_read_b64 v[88:89], v149 offset:0\n\t"
    "ds_read_b64 v[90:91], v253 offset:0\n\t"
    "v_lshrrev_b32 v254, 16, v150\n\t"
    "v_and_b32 v150, s92, v150\n\t"
    "v_lshrrev_b32 v255, 16, v151\n\t"
    "v_and_b32 v151, s92, v151\n\t"
    "s_waitcnt vmcnt(8) lgkmcnt(4)\n\t"
    "v_smfmac_f32_16x16x64_bf16 v[116:119], v[120:123], v[92:99], v124\n\t"
    "ds_read_b64 v[92:93], v150 offset:0\n\t"
    "ds_read_b64 v[94:95], v254 offset:0\n\t"
    "ds_read_b64 v[96:97], v151 offset:0\n\t"
    "ds_read_b64 v[98:99], v255 offset:0\n\t"
    "s_add_u32 s91, s91, 0x8000\n\t"
    "buffer_load_dwordx4 v[216:219], v127, %12, s91 offen offset:1024 sc1\n\t"
    "v_lshrrev_b32 v252, 16, v152\n\t"
    "v_and_b32 v152, s92, v152\n\t"
    "v_lshrrev_b32 v253, 16, v153\n\t"
    "v_cvt_pk_bf16_f32 v46, v116, v117\n\t"
    "v_cvt_pk_bf16_f32 v47, v118, v119\n\t"
    "v_and_b32 v119, s67, v53\n\t"
    "v_lshlrev_b32 v118, 16, v53\n\t"
    "v_and_b32 v117, s67, v52\n\t"
    "v_lshlrev_b32 v116, 16, v52\n\t"
    "v_and_b32 v153, s92, v153\n\t"
    "s_waitcnt lgkmcnt(4)\n\t"
    "v_smfmac_f32_16x16x64_bf16 v[116:119], v[120:123], v[84:91], v124\n\t"
    "ds_read_b64 v[84:85], v152 offset:64000\n\t"
    "ds_read_b64 v[86:87], v252 offset:64000\n\t"
    "ds_read_b64 v[88:89], v153 offset:64000\n\t"
    "ds_read_b64 v[90:91], v253 offset:64000\n\t"
    "v_lshrrev_b32 v254, 16, v154\n\t"
    "v_and_b32 v154, s92, v154\n\t"
    "v_lshrrev_b32 v255, 16, v155\n\t"
    "v_and_b32 v155, s92, v155\n\t"
    "s_waitcnt vmcnt(6) lgkmcnt(4)\n\t"
    "v_smfmac_f32_16x16x64_bf16 v[116:119], v[120:123], v[92:99], v124\n\t"
    "ds_read_b64 v[92:93], v154 offset:64000\n\t"
    "ds_read_b64 v[94:95], v254 offset:64000\n\t"
    "ds_read_b64 v[96:97], v155 offset:64000\n\t"
    "ds_read_b64 v[98:99], v255 offset:64000\n\t"
    "s_sub_u32 s84, s84, s95\n\t"
    "buffer_load_dwordx4 v[220:223], v127, %12, s84 offen offset:0 sc1\n\t"
    "v_lshrrev_b32 v252, 16, v240\n\t"
    "v_and_b32 v240, s92, v240\n\t"
    "v_lshrrev_b32 v253, 16, v241\n\t"
    "v_and_b32 v241, s92, v241\n\t"
    "s_waitcnt lgkmcnt(4)\n\t"
    "v_smfmac_f32_16x16x64_bf16 v[116:119], v[120:123], v[84:91], v124\n\t"
    "ds_read_b64 v[84:85], v240 offset:0\n\t"
    "ds_read_b64 v[86:87], v252 offset:0\n\t"
    "ds_read_b64 v[88:89], v241 offset:0\n\t"
    "ds_read_b64 v[90:91], v253 offset:0\n\t"
    "v_lshrrev_b32 v254, 16, v242\n\t"
    "v_and_b32 v242, s92, v242\n\t"
    "v_lshrrev_b32 v255, 16, v243\n\t"
    "v_and_b32 v243, s92, v243\n\t"
    "s_cmp_lt_u32 %16, 4\n\t"
    "s_cbranch_scc1 L_tp195_%=\n\t"
    "s_setprio 0\n\t"
    "L_tp195_%=:\n\t"
    "s_waitcnt vmcnt(6) lgkmcnt(4)\n\t"
    "v_smfmac_f32_16x16x64_bf16 v[116:119], v[120:123], v[92:99], v124\n\t"
    "ds_read_b64 v[92:93], v242 offset:0\n\t"
    "ds_read_b64 v[94:95], v254 offset:0\n\t"
    "ds_read_b64 v[96:97], v243 offset:0\n\t"
    "ds_read_b64 v[98:99], v255 offset:0\n\t"
    "buffer_load_dwordx4 v[224:227], v127, %12, s84 offen offset:1024 sc1\n\t"
    "v_lshrrev_b32 v252, 16, v208\n\t"
    "v_and_b32 v208, s92, v208\n\t"
    "v_lshrrev_b32 v253, 16, v209\n\t"
    "v_cvt_pk_bf16_f32 v52, v116, v117\n\t"
    "v_cvt_pk_bf16_f32 v53, v118, v119\n\t"
    "v_and_b32 v119, s67, v55\n\t"
    "v_lshlrev_b32 v118, 16, v55\n\t"
    "v_and_b32 v117, s67, v54\n\t"
    "v_lshlrev_b32 v116, 16, v54\n\t"
    "v_and_b32 v209, s92, v209\n\t"
    "s_waitcnt vmcnt(4)\n\t"
    "s_mov_b64 exec, 0xffffffff\n\t"
    "buffer_store_dword v165, v125, %12, s96 offen offset:128 sc1\n\t"
    "s_mov_b64 exec, -1\n\t"
    "s_add_u32 s61, s61, 0x2000\n\t"
    "buffer_store_dwordx4 v[36:39], v127, %12, s61 offen sc1\n\t"
    "s_waitcnt lgkmcnt(4)\n\t"
    "v_smfmac_f32_16x16x64_bf16 v[116:119], v[120:123], v[84:91], v124\n\t"
    "ds_read_b64 v[84:85], v208 offset:64000\n\t"
    "ds_read_b64 v[86:87], v252 offset:64000\n\t"
    "ds_read_b64 v[88:89], v209 offset:64000\n\t"
    "ds_read_b64 v[90:91], v253 offset:64000\n\t"
    "v_lshrrev_b32 v254, 16, v210\n\t"
    "v_and_b32 v210, s92, v210\n\t"
    "v_lshrrev_b32 v255, 16, v211\n\t"
    "v_and_b32 v211, s92, v211\n\t"
    "s_waitcnt lgkmcnt(4)\n\t"
    "v_smfmac_f32_16x16x64_bf16 v[116:119], v[120:123], v[92:99], v124\n\t"
    "ds_read_b64 v[92:93], v210 offset:64000\n\t"
    "ds_read_b64 v[94:95], v254 offset:64000\n\t"
    "ds_read_b64 v[96:97], v211 offset:64000\n\t"
    "ds_read_b64 v[98:99], v255 offset:64000\n\t"
    "s_sub_u32 s85, s85, s95\n\t"
    "buffer_load_dwordx4 v[228:231], v127, %12, s85 offen offset:0 sc1\n\t"
    "v_lshrrev_b32 v252, 16, v244\n\t"
    "v_and_b32 v244, s92, v244\n\t"
    "v_lshrrev_b32 v253, 16, v245\n\t"
    "v_and_b32 v245, s92, v245\n\t"
    "s_waitcnt lgkmcnt(4)\n\t"
    "v_smfmac_f32_16x16x64_bf16 v[116:119], v[120:123], v[84:91], v124\n\t"
    "ds_read_b64 v[84:85], v244 offset:0\n\t"
    "ds_read_b64 v[86:87], v252 offset:0\n\t"
    "ds_read_b64 v[88:89], v245 offset:0\n\t"
    "ds_read_b64 v[90:91], v253 offset:0\n\t"
    "v_lshrrev_b32 v254, 16, v246\n\t"
    "v_and_b32 v246, s92, v246\n\t"
    "v_lshrrev_b32 v255, 16, v247\n\t"
    "v_and_b32 v247, s92, v247\n\t"
    "s_waitcnt lgkmcnt(4)\n\t"
    "v_smfmac_f32_16x16x64_bf16 v[116:119], v[120:123], v[92:99], v124\n\t"
    "ds_read_b64 v[92:93], v246 offset:0\n\t"
    "ds_read_b64 v[94:95], v254 offset:0\n\t"
    "ds_read_b64 v[96:97], v247 offset:0\n\t"
    "ds_read_b64 v[98:99], v255 offset:0\n\t"
    "buffer_load_dwordx4 v[232:235], v127, %12, s85 offen offset:1024 sc1\n\t"
    "v_lshrrev_b32 v252, 16, v248\n\t"
    "v_and_b32 v248, s92, v248\n\t"
    "v_lshrrev_b32 v253, 16, v249\n\t"
    "v_cvt_pk_bf16_f32 v54, v116, v117\n\t"
    "v_cvt_pk_bf16_f32 v55, v118, v119\n\t"
    "v_and_b32 v119, s67, v61\n\t"
    "v_lshlrev_b32 v118, 16, v61\n\t"
    "v_and_b32 v117, s67, v60\n\t"
    "v_lshlrev_b32 v116, 16, v60\n\t"
    "v_and_b32 v249, s92, v249\n\t"
    "s_waitcnt lgkmcnt(4)\n\t"
    "v_smfmac_f32_16x16x64_bf16 v[116:119], v[120:123], v[84:91], v124\n\t"
    "ds_read_b64 v[84:85], v248 offset:64000\n\t"
    "ds_read_b64 v[86:87], v252 offset:64000\n\t"
    "ds_read_b64 v[88:89], v249 offset:64000\n\t"
    "ds_read_b64 v[90:91], v253 offset:64000\n\t"
    "v_lshrrev_b32 v254, 16, v250\n\t"
    "v_and_b32 v250, s92, v250\n\t"
    "v_lshrrev_b32 v255, 16, v251\n\t"
    "v_and_b32 v251, s92, v251\n\t"
    "s_waitcnt vmcnt(7) lgkmcnt(4)\n\t"
    "v_smfmac_f32_16x16x64_bf16 v[116:119], v[120:123], v[92:99], v124\n\t"
    "ds_read_b64 v[92:93], v250 offset:64000\n\t"
    "ds_read_b64 v[94:95], v254 offset:64000\n\t"
    "ds_read_b64 v[96:97], v251 offset:64000\n\t"
    "ds_read_b64 v[98:99], v255 offset:64000\n\t"
    "s_sub_u32 s86, s86, s95\n\t"
    "buffer_load_dwordx4 v[168:171], v127, %12, s86 offen offset:0 sc1\n\t"
    "v_lshrrev_b32 v252, 16, v212\n\t"
    "v_and_b32 v212, s92, v212\n\t"
    "v_lshrrev_b32 v253, 16, v213\n\t"
    "v_and_b32 v213, s92, v213\n\t"
    "s_waitcnt lgkmcnt(4)\n\t"
    "v_smfmac_f32_16x16x64_bf16 v[116:119], v[120:123], v[84:91], v124\n\t"
    "ds_read_b64 v[84:85], v212 offset:0\n\t"
    "ds_read_b64 v[86:87], v252 offset:0\n\t"
    "ds_read_b64 v[88:89], v213 offset:0\n\t"
    "ds_read_b64 v[90:91], v253 offset:0\n\t"
    "v_lshrrev_b32 v254, 16, v214\n\t"
    "v_and_b32 v214, s92, v214\n\t"
    "v_lshrrev_b32 v255, 16, v215\n\t"
    "v_and_b32 v215, s92, v215\n\t"
    "s_waitcnt vmcnt(7) lgkmcnt(4)\n\t"
    "v_smfmac_f32_16x16x64_bf16 v[116:119], v[120:123], v[92:99], v124\n\t"
    "ds_read_b64 v[92:93], v214 offset:0\n\t"
    "ds_read_b64 v[94:95], v254 offset:0\n\t"
    "ds_read_b64 v[96:97], v215 offset:0\n\t"
    "ds_read_b64 v[98:99], v255 offset:0\n\t"
    "s_add_u32 s84, s84, 0x8000\n\t"
    "buffer_load_dwordx4 v[112:115], v127, %12, s84 offen offset:0 sc1\n\t"
    "v_lshrrev_b32 v252, 16, v216\n\t"
    "v_and_b32 v216, s92, v216\n\t"
    "v_lshrrev_b32 v253, 16, v217\n\t"
    "v_cvt_pk_bf16_f32 v60, v116, v117\n\t"
    "v_cvt_pk_bf16_f32 v61, v118, v119\n\t"
    "v_and_b32 v119, s67, v63\n\t"
    "v_lshlrev_b32 v118, 16, v63\n\t"
    "v_and_b32 v117, s67, v62\n\t"
    "v_lshlrev_b32 v116, 16, v62\n\t"
    "v_and_b32 v217, s92, v217\n\t"
    "s_add_u32 s61, s61, 0x2000\n\t"
    "buffer_store_dwordx4 v[44:47], v127, %12, s61 offen sc1\n\t"
    "s_waitcnt lgkmcnt(4)\n\t"
    "v_smfmac_f32_16x16x64_bf16 v[116:119], v[120:123], v[84:91], v124\n\t"
    "ds_read_b64 v[84:85], v216 offset:64000\n\t"
    "ds_read_b64 v[86:87], v252 offset:64000\n\t"
    "ds_read_b64 v[88:89], v217 offset:64000\n\t"
    "ds_read_b64 v[90:91], v253 offset:64000\n\t"
    "v_lshrrev_b32 v254, 16, v218\n\t"
    "v_and_b32 v218, s92, v218\n\t"
    "v_lshrrev_b32 v255, 16, v219\n\t"
    "v_and_b32 v219, s92, v219\n\t"
    "s_mov_b64 exec, 1\n\t"
    "buffer_load_dword v164, off, %12, s93 offset:0 sc1\n\t"
    "s_mov_b64 exec, -1\n\t"
    "s_waitcnt vmcnt(9) lgkmcnt(4)\n\t"
    "v_smfmac_f32_16x16x64_bf16 v[116:119], v[120:123], v[92:99], v124\n\t"
    "ds_read_b64 v[92:93], v218 offset:64000\n\t"
    "ds_read_b64 v[94:95], v254 offset:64000\n\t"
    "ds_read_b64 v[96:97], v219 offset:64000\n\t"
    "ds_read_b64 v[98:99], v255 offset:64000\n\t"
    "buffer_load_dwordx4 v[100:103], v127, %12, s86 offen offset:1024 sc1\n\t"
    "v_lshrrev_b32 v252, 16, v220\n\t"
    "v_and_b32 v220, s92, v220\n\t"
    "v_lshrrev_b32 v253, 16, v221\n\t"
    "v_and_b32 v221, s92, v221\n\t"
    "s_waitcnt lgkmcnt(4)\n\t"
    "v_smfmac_f32_16x16x64_bf16 v[116:119], v[120:123], v[84:91], v124\n\t"
    "ds_read_b64 v[84:85], v220 offset:0\n\t"
    "ds_read_b64 v[86:87], v252 offset:0\n\t"
    "ds_read_b64 v[88:89], v221 offset:0\n\t"
    "ds_read_b64 v[90:91], v253 offset:0\n\t"
    "v_lshrrev_b32 v254, 16, v222\n\t"
    "v_and_b32 v222, s92, v222\n\t"
    "v_lshrrev_b32 v255, 16, v223\n\t"
    "v_and_b32 v223, s92, v223\n\t"
    "s_cmp_lt_u32 %16, 4\n\t"
    "s_cbranch_scc1 L_tp207_%=\n\t"
    "s_setprio 1\n\t"
    "L_tp207_%=:\n\t"
    "s_waitcnt vmcnt(9) lgkmcnt(4)\n\t"
    "v_smfmac_f32_16x16x64_bf16 v[116:119], v[120:123], v[92:99], v124\n\t"
    "ds_read_b64 v[92:93], v222 offset:0\n\t"
    "ds_read_b64 v[94:95], v254 offset:0\n\t"
    "ds_read_b64 v[96:97], v223 offset:0\n\t"
    "ds_read_b64 v[98:99], v255 offset:0\n\t"
    "buffer_load_dwordx4 v[104:107], v127, %12, s84 offen offset:1024 sc1\n\t"
    "v_lshrrev_b32 v252, 16, v224\n\t"
    "v_and_b32 v224, s92, v224\n\t"
    "v_lshrrev_b32 v253, 16, v225\n\t"
    "v_cvt_pk_bf16_f32 v62, v116, v117\n\t"
    "v_cvt_pk_bf16_f32 v63, v118, v119\n\t"
    "v_mov_b64 v[172:173], s[70:71]\n\t"
    "v_mov_b64 v[174:175], s[72:73]\n\t"
    "v_and_b32 v225, s92, v225\n\t"
    "s_waitcnt lgkmcnt(4)\n\t"
    "v_smfmac_f32_16x16x64_bf16 v[172:175], v[120:123], v[84:91], v124\n\t"
    "ds_read_b64 v[84:85], v224 offset:0\n\t"
    "ds_read_b64 v[86:87], v252 offset:0\n\t"
    "ds_read_b64 v[88:89], v225 offset:0\n\t"
    "ds_read_b64 v[90:91], v253 offset:0\n\t"
    "v_lshrrev_b32 v254, 16, v226\n\t"
    "v_and_b32 v226, s92, v226\n\t"
    "v_lshrrev_b32 v255, 16, v227\n\t"
    "v_and_b32 v227, s92, v227\n\t"
    "s_waitcnt vmcnt(7) lgkmcnt(4)\n\t"
    "v_smfmac_f32_16x16x64_bf16 v[172:175], v[120:123], v[92:99], v124\n\t"
    "ds_read_b64 v[92:93], v226 offset:0\n\t"
    "ds_read_b64 v[94:95], v254 offset:0\n\t"
    "ds_read_b64 v[96:97], v227 offset:0\n\t"
    "ds_read_b64 v[98:99], v255 offset:0\n\t"
    "s_sub_u32 s87, s87, s95\n\t"
    "buffer_load_dwordx4 v[108:111], v127, %12, s87 offen offset:0 sc1\n\t"
    "v_lshrrev_b32 v252, 16, v228\n\t"
    "v_and_b32 v228, s92, v228\n\t"
    "v_lshrrev_b32 v253, 16, v229\n\t"
    "v_mov_b64 v[176:177], s[70:71]\n\t"
    "v_mov_b64 v[178:179], s[72:73]\n\t"
    "v_and_b32 v229, s92, v229\n\t"
    "s_waitcnt lgkmcnt(4)\n\t"
    "v_smfmac_f32_16x16x64_bf16 v[176:179], v[120:123], v[84:91], v124\n\t"
    "ds_read_b64 v[84:85], v228 offset:0\n\t"
    "ds_read_b64 v[86:87], v252 offset:0\n\t"
    "ds_read_b64 v[88:89], v229 offset:0\n\t"
    "ds_read_b64 v[90:91], v253 offset:0\n\t"
    "v_lshrrev_b32 v254, 16, v230\n\t"
    "v_and_b32 v230, s92, v230\n\t"
    "v_lshrrev_b32 v255, 16, v231\n\t"
    "v_and_b32 v231, s92, v231\n\t"
    "s_waitcnt vmcnt(3)\n\t"
    "v_readfirstlane_b32 s65, v164\n\t"
    "s_nop 0\n\t"
    "s_cmp_eq_u32 s65, 0\n\t"
    "s_cbranch_scc1 L_xw0_%=\n\t"
    "L_xwb0_%=:\n\t"
    "buffer_load_dwordx4 v[4:7], v127, %12, s94 offen sc1\n\t"
    "s_waitcnt lgkmcnt(4)\n\t"
    "v_smfmac_f32_16x16x64_bf16 v[176:179], v[120:123], v[92:99], v124\n\t"
    "ds_read_b64 v[92:93], v230 offset:0\n\t"
    "ds_read_b64 v[94:95], v254 offset:0\n\t"
    "ds_read_b64 v[96:97], v231 offset:0\n\t"
    "ds_read_b64 v[98:99], v255 offset:0\n\t"
    "s_add_u32 s85, s85, 0x8000\n\t"
    "buffer_load_dwordx4 v[200:203], v127, %12, s85 offen offset:0 sc1\n\t"
    "v_lshrrev_b32 v252, 16, v232\n\t"
    "v_and_b32 v232, s92, v232\n\t"
    "v_lshrrev_b32 v253, 16, v233\n\t"
    "v_mov_b64 v[180:181], s[70:71]\n\t"
    "v_mov_b64 v[182:183], s[72:73]\n\t"
    "v_and_b32 v233, s92, v233\n\t"
    "s_mov_b64 exec, 0xffffffff\n\t"
    "buffer_store_dword v165, v125, %12, s96 offen offset:256 sc1\n\t"
    "s_mov_b64 exec, -1\n\t"
    "s_add_u32 s61, s61, 0x2000\n\t"
    "buffer_store_dwordx4 v[52:55], v127, %12, s61 offen sc1\n\t"
    "s_waitcnt lgkmcnt(4)\n\t"
    "v_smfmac_f32_16x16x64_bf16 v[180:183], v[120:123], v[84:91], v124\n\t"
    "ds_read_b64 v[84:85], v232 offset:0\n\t"
    "ds_read_b64 v[86:87], v252 offset:0\n\t"
    "ds_read_b64 v[88:89], v233 offset:0\n\t"
    "ds_read_b64 v[90:91], v253 offset:0\n\t"
    "v_lshrrev_b32 v254, 16, v234\n\t"
    "v_and_b32 v234, s92, v234\n\t"
    "v_lshrrev_b32 v255, 16, v235\n\t"
    "v_and_b32 v235, s92, v235\n\t"
    "s_waitcnt lgkmcnt(4)\n\t"
    "v_smfmac_f32_16x16x64_bf16 v[180:183], v[120:123], v[92:99], v124\n\t"
    "ds_read_b64 v[92:93], v234 offset:0\n\t"
    "ds_read_b64 v[94:95], v254 offset:0\n\t"
    "ds_read_b64 v[96:97], v235 offset:0\n\t"
    "ds_read_b64 v[98:99], v255 offset:0\n\t"
    "buffer_load_dwordx4 v[204:207], v127, %12, s87 offen offset:1024 sc1\n\t"
    "v_lshrrev_b32 v252, 16, v168\n\t"
    "v_and_b32 v168, s92, v168\n\t"
    "v_lshrrev_b32 v253, 16, v169\n\t"
    "v_mov_b64 v[184:185], s[70:71]\n\t"
    "v_mov_b64 v[186:187], s[72:73]\n\t"
    "v_and_b32 v169, s92, v169\n\t"
    "s_waitcnt lgkmcnt(4)\n\t"
    "v_smfmac_f32_16x16x64_bf16 v[184:187], v[120:123], v[84:91], v124\n\t"
    "ds_read_b64 v[84:85], v168 offset:0\n\t"
    "ds_read_b64 v[86:87], v252 offset:0\n\t"
    "ds_read_b64 v[88:89], v169 offset:0\n\t"
    "ds_read_b64 v[90:91], v253 offset:0\n\t"
    "v_lshrrev_b32 v254, 16, v170\n\t"
    "v_and_b32 v170, s92, v170\n\t"
    "v_lshrrev_b32 v255, 16, v171\n\t"
    "v_and_b32 v171, s92, v171\n\t"
    "s_add_u32 s94, s94, 0x2000\n\t"
    "buffer_load_dwordx4 v[12:15], v127, %12, s94 offen sc1\n\t"
    "s_waitcnt lgkmcnt(4)\n\t"
    "v_smfmac_f32_16x16x64_bf16 v[184:187], v[120:123], v[92:99], v124\n\t"
    "ds_read_b64 v[92:93], v170 offset:0\n\t"
    "ds_read_b64 v[94:95], v254 offset:0\n\t"
    "ds_read_b64 v[96:97], v171 offset:0\n\t"
    "ds_read_b64 v[98:99], v255 offset:0\n\t"
    "buffer_load_dwordx4 v[144:147], v127, %12, s85 offen offset:1024 sc1\n\t"
    "v_lshrrev_b32 v252, 16, v112\n\t"
    "v_and_b32 v112, s92, v112\n\t"
    "v_lshrrev_b32 v253, 16, v113\n\t"
    "v_mov_b64 v[188:189], s[70:71]\n\t"
    "v_mov_b64 v[190:191], s[72:73]\n\t"
    "v_and_b32 v113, s92, v113\n\t"
    "s_waitcnt lgkmcnt(4)\n\t"
    "v_smfmac_f32_16x16x64_bf16 v[188:191], v[120:123], v[84:91], v124\n\t"
    "ds_read_b64 v[84:85], v112 offset:64000\n\t"
    "ds_read_b64 v[86:87], v252 offset:64000\n\t"
    "ds_read_b64 v[88:89], v113 offset:64000\n\t"
    "ds_read_b64 v[90:91], v253 offset:64000\n\t"
    "v_lshrrev_b32 v254, 16, v114\n\t"
    "v_and_b32 v114, s92, v114\n\t"
    "v_lshrrev_b32 v255, 16, v115\n\t"
    "v_and_b32 v115, s92, v115\n\t"
    "s_waitcnt vmcnt(9) lgkmcnt(4)\n\t"
    "v_smfmac_f32_16x16x64_bf16 v[188:191], v[120:123], v[92:99], v124\n\t"
    "ds_read_b64 v[92:93], v114 offset:64000\n\t"
    "ds_read_b64 v[94:95], v254 offset:64000\n\t"
    "ds_read_b64 v[96:97], v115 offset:64000\n\t"
    "ds_read_b64 v[98:99], v255 offset:64000\n\t"
    "s_sub_u32 s88, s88, s95\n\t"
    "buffer_load_dwordx4 v[148:151], v127, %12, s88 offen offset:0 sc1\n\t"
    "v_lshrrev_b32 v252, 16, v100\n\t"
    "v_and_b32 v100, s92, v100\n\t"
    "v_lshrrev_b32 v253, 16, v101\n\t"
    "v_and_b32 v101, s92, v101\n\t"
    "s_waitcnt lgkmcnt(4)\n\t"
    "v_smfmac_f32_16x16x64_bf16 v[172:175], v[120:123], v[84:91], v124\n\t"
    "ds_read_b64 v[84:85], v100 offset:0\n\t"
    "ds_read_b64 v[86:87], v252 offset:0\n\t"
    "ds_read_b64 v[88:89], v101 offset:0\n\t"
    "ds_read_b64 v[90:91], v253 offset:0\n\t"
    "v_lshrrev_b32 v254, 16, v102\n\t"
    "v_and_b32 v102, s92, v102\n\t"
    "v_lshrrev_b32 v255, 16, v103\n\t"
    "v_and_b32 v103, s92, v103\n\t"
    "s_waitcnt vmcnt(7)\n\t"
    "v_smfmac_f32_16x16x64_bf16 v[172:175], v[156:159], v[0:7], v124\n\t"
    "s_waitcnt lgkmcnt(4)\n\t"
    "v_smfmac_f32_16x16x64_bf16 v[172:175], v[120:123], v[92:99], v124\n\t"
    "ds_read_b64 v[92:93], v102 offset:0\n\t"
    "ds_read_b64 v[94:95], v254 offset:0\n\t"
    "ds_read_b64 v[96:97], v103 offset:0\n\t"
    "ds_read_b64 v[98:99], v255 offset:0\n\t"
    "s_add_u32 s86, s86, 0x8000\n\t"
    "buffer_load_dwordx4 v[152:155], v127, %12, s86 offen offset:0 sc1\n\t"
    "v_lshrrev_b32 v252, 16, v104\n\t"
    "v_and_b32 v104, s92, v104\n\t"
    "v_lshrrev_b32 v253, 16, v105\n\t"
    "v_mov_b64 v[192:193], s[70:71]\n\t"
    "v_mov_b64 v[194:195], s[72:73]\n\t"
    "v_and_b32 v105, s92, v105\n\t"
    "s_add_u32 s61, s61, 0x2000\n\t"
    "buffer_store_dwordx4 v[60:63], v127, %12, s61 offen sc1\n\t"
    "v_smfmac_f32_16x16x64_bf16 v[176:179], v[160:163], v[0:7], v124\n\t"
    "v_pk_mul_f32 v[64:65], v[172:173], v[172:173]\n\t"
    "v_pk_mul_f32 v[66:67], v[174:175], v[174:175]\n\t"
    "v_pk_fma_f32 v[64:65], v[64:65], v[82:83], v[80:81]\n\t"
    "v_pk_fma_f32 v[66:67], v[66:67], v[82:83], v[80:81]\n\t"
    "v_pk_mul_f32 v[64:65], v[64:65], v[172:173]\n\t"
    "s_waitcnt lgkmcnt(4)\n\t"
    "v_smfmac_f32_16x16x64_bf16 v[192:195], v[120:123], v[84:91], v124\n\t"
    "ds_read_b64 v[84:85], v104 offset:64000\n\t"
    "ds_read_b64 v[86:87], v252 offset:64000\n\t"
    "ds_read_b64 v[88:89], v105 offset:64000\n\t"
    "ds_read_b64 v[90:91], v253 offset:64000\n\t"
    "v_lshrrev_b32 v254, 16, v106\n\t"
    "v_and_b32 v106, s92, v106\n\t"
    "v_lshrrev_b32 v255, 16, v107\n\t"
    "v_and_b32 v107, s92, v107\n\t"
    "s_mov_b64 exec, 1\n\t"
    "buffer_load_dword v164, off, %12, s93 offset:128 sc1\n\t"
    "s_mov_b64 exec, -1\n\t"
    "v_pk_mul_f32 v[66:67], v[66:67], v[174:175]\n\t"
    "v_exp_f32 v68, v64\n\t"
    "v_exp_f32 v69, v65\n\t"
    "v_exp_f32 v70, v66\n\t"
    "v_exp_f32 v71, v67\n\t"
    "s_waitcnt lgkmcnt(4)\n\t"
    "v_smfmac_f32_16x16x64_bf16 v[192:195], v[120:123], v[92:99], v124\n\t"
    "ds_read_b64 v[92:93], v106 offset:64000\n\t"
    "ds_read_b64 v[94:95], v254 offset:64000\n\t"
    "ds_read_b64 v[96:97], v107 offset:64000\n\t"
    "ds_read_b64 v[98:99], v255 offset:64000\n\t"
    "buffer_load_dwordx4 v[244:247], v127, %12, s88 offen offset:1024 sc1\n\t"
    "v_lshrrev_b32 v252, 16, v108\n\t"
    "v_and_b32 v108, s92, v108\n\t"
    "v_lshrrev_b32 v253, 16, v109\n\t"
    "v_and_b32 v109, s92, v109\n\t"
    "v_pk_fma_f32 v[68:69], v[68:69], s[40:41], s[40:41]\n\t"
    "v_pk_fma_f32 v[70:71], v[70:71], s[42:43], s[42:43]\n\t"
    "v_rcp_f32 v68, v68\n\t"
    "v_rcp_f32 v69, v69\n\t"
    "v_rcp_f32 v70, v70\n\t"
    "s_waitcnt lgkmcnt(4)\n\t"
    "v_smfmac_f32_16x16x64_bf16 v[176:179], v[120:123], v[84:91], v124\n\t"
    "ds_read_b64 v[84:85], v108 offset:0\n\t"
    "ds_read_b64 v[86:87], v252 offset:0\n\t"
    "ds_read_b64 v[88:89], v109 offset:0\n\t"
    "ds_read_b64 v[90:91], v253 offset:0\n\t"
    "v_lshrrev_b32 v254, 16, v110\n\t"
    "v_and_b32 v110, s92, v110\n\t"
    "v_lshrrev_b32 v255, 16, v111\n\t"
    "v_and_b32 v111, s92, v111\n\t"
    "s_waitcnt vmcnt(6)\n\t"
    "v_smfmac_f32_16x16x64_bf16 v[180:183], v[156:159], v[8:15], v124\n\t"
    "v_rcp_f32 v71, v71\n\t"
    "v_pk_mul_f32 v[68:69], v[68:69], v[172:173]\n\t"
    "v_pk_fma_f32 v[68:69], v[70:71], v[174:175], v[68:69]\n\t"
    "v_add_f32 v236, v68, v69\n\t"
    "s_waitcnt lgkmcnt(4)\n\t"
    "v_smfmac_f32_16x16x64_bf16 v[176:179], v[120:123], v[92:99], v124\n\t"
    "ds_read_b64 v[92:93], v110 offset:0\n\t"
    "ds_read_b64 v[94:95], v254 offset:0\n\t"
    "ds_read_b64 v[96:97], v111 offset:0\n\t"
    "ds_read_b64 v[98:99], v255 offset:0\n\t"
    "buffer_load_dwordx4 v[248:251], v127, %12, s86 offen offset:1024 sc1\n\t"
    "v_lshrrev_b32 v252, 16, v200\n\t"
    "v_and_b32 v200, s92, v200\n\t"
    "v_lshrrev_b32 v253, 16, v201\n\t"
    "v_mov_b64 v[196:197], s[70:71]\n\t"
    "v_mov_b64 v[198:199], s[72:73]\n\t"
    "v_and_b32 v201, s92, v201\n\t"
    "v_smfmac_f32_16x16x64_bf16 v[184:187], v[160:163], v[8:15], v124\n\t"
    "v_pk_mul_f32 v[72:73], v[176:177], v[176:177]\n\t"
    "v_pk_mul_f32 v[74:75], v[178:179], v[178:179]\n\t"
    "v_pk_fma_f32 v[72:73], v[72:73], v[82:83], v[80:81]\n\t"
    "v_pk_fma_f32 v[74:75], v[74:75], v[82:83], v[80:81]\n\t"
    "v_pk_mul_f32 v[72:73], v[72:73], v[176:177]\n\t"
    "s_waitcnt lgkmcnt(4)\n\t"
    "v_smfmac_f32_16x16x64_bf16 v[196:199], v[120:123], v[84:91], v124\n\t"
    "ds_read_b64 v[84:85], v200 offset:64000\n\t"
    "ds_read_b64 v[86:87], v252 offset:64000\n\t"
    "ds_read_b64 v[88:89], v201 offset:64000\n\t"
    "ds_read_b64 v[90:91], v253 offset:64000\n\t"
    "v_lshrrev_b32 v254, 16, v202\n\t"
    "v_and_b32 v202, s92, v202\n\t"
    "v_lshrrev_b32 v255, 16, v203\n\t"
    "v_and_b32 v203, s92, v203\n\t"
    "v_pk_mul_f32 v[74:75], v[74:75], v[178:179]\n\t"
    "v_exp_f32 v76, v72\n\t"
    "v_exp_f32 v77, v73\n\t"
    "v_exp_f32 v78, v74\n\t"
    "v_exp_f32 v79, v75\n\t"
    "s_waitcnt lgkmcnt(4)\n\t"
    "v_smfmac_f32_16x16x64_bf16 v[196:199], v[120:123], v[92:99], v124\n\t"
    "ds_read_b64 v[92:93], v202 offset:64000\n\t"
    "ds_read_b64 v[94:95], v254 offset:64000\n\t"
    "ds_read_b64 v[96:97], v203 offset:64000\n\t"
    "ds_read_b64 v[98:99], v255 offset:64000\n\t"
    "s_sub_u32 s89, s89, s95\n\t"
    "buffer_load_dwordx4 v[212:215], v127, %12, s89 offen offset:0 sc1\n\t"
    "v_lshrrev_b32 v252, 16, v204\n\t"
    "v_and_b32 v204, s92, v204\n\t"
    "v_lshrrev_b32 v253, 16, v205\n\t"
    "v_and_b32 v205, s92, v205\n\t"
    "v_pk_fma_f32 v[76:77], v[76:77], s[40:41], s[40:41]\n\t"
    "v_pk_fma_f32 v[78:79], v[78:79], s[42:43], s[42:43]\n\t"
    "v_rcp_f32 v76, v76\n\t"
    "v_rcp_f32 v77, v77\n\t"
    "v_rcp_f32 v78, v78\n\t"
    "s_waitcnt lgkmcnt(4)\n\t"
    "v_smfmac_f32_16x16x64_bf16 v[180:183], v[120:123], v[84:91], v124\n\t"
    "ds_read_b64 v[84:85], v204 offset:0\n\t"
    "ds_read_b64 v[86:87], v252 offset:0\n\t"
    "ds_read_b64 v[88:89], v205 offset:0\n\t"
    "ds_read_b64 v[90:91], v253 offset:0\n\t"
    "v_lshrrev_b32 v254, 16, v206\n\t"
    "v_and_b32 v206, s92, v206\n\t"
    "v_lshrrev_b32 v255, 16, v207\n\t"
    "v_and_b32 v207, s92, v207\n\t"
    "s_waitcnt vmcnt(3)\n\t"
    "v_readfirstlane_b32 s65, v164\n\t"
    "s_nop 0\n\t"
    "s_cmp_eq_u32 s65, 0\n\t"
    "s_cbranch_scc1 L_xw1_%=\n\t"
    "L_xwb1_%=:\n\t"
    "s_add_u32 s94, s94, 0x2000\n\t"
    "buffer_load_dwordx4 v[20:23], v127, %12, s94 offen sc1\n\t"
    "v_rcp_f32 v79, v79\n\t"
    "v_pk_mul_f32 v[76:77], v[76:77], v[176:177]\n\t"
    "v_pk_fma_f32 v[76:77], v[78:79], v[178:179], v[76:77]\n\t"
    "v_add_f32 v237, v76, v77\n\t"
    "s_cmp_lt_u32 %16, 4\n\t"
    "s_cbranch_scc1 L_tp227_%=\n\t"
    "s_setprio 0\n\t"
    "L_tp227_%=:\n\t"
    "s_waitcnt lgkmcnt(4)\n\t"
    "v_smfmac_f32_16x16x64_bf16 v[180:183], v[120:123], v[92:99], v124\n\t"
    "ds_read_b64 v[92:93], v206 offset:0\n\t"
    "ds_read_b64 v[94:95], v254 offset:0\n\t"
    "ds_read_b64 v[96:97], v207 offset:0\n\t"
    "ds_read_b64 v[98:99], v255 offset:0\n\t"
    "s_add_u32 s87, s87, 0x8000\n\t"
    "buffer_load_dwordx4 v[216:219], v127, %12, s87 offen offset:0 sc1\n\t"
    "v_lshrrev_b32 v252, 16, v144\n\t"
    "v_and_b32 v144, s92, v144\n\t"
    "v_lshrrev_b32 v253, 16, v145\n\t"
    "v_mov_b64 v[200:201], s[70:71]\n\t"
    "v_mov_b64 v[202:203], s[72:73]\n\t"
    "v_and_b32 v145, s92, v145\n\t"
    "s_mov_b64 exec, 0xffffffff\n\t"
    "buffer_store_dword v165, v125, %12, s96 offen offset:384 sc1\n\t"
    "s_mov_b64 exec, -1\n\t"
    "v_pk_mul_f32 v[64:65], v[180:181], v[180:181]\n\t"
    "v_pk_mul_f32 v[66:67], v[182:183], v[182:183]\n\t"
    "v_pk_fma_f32 v[64:65], v[64:65], v[82:83], v[80:81]\n\t"
    "v_pk_fma_f32 v[66:67], v[66:67], v[82:83], v[80:81]\n\t"
    "v_pk_mul_f32 v[64:65], v[64:65], v[180:181]\n\t"
    "s_waitcnt lgkmcnt(4)\n\t"
    "v_smfmac_f32_16x16x64_bf16 v[200:203], v[120:123], v[84:91], v124\n\t"
    "ds_read_b64 v[84:85], v144 offset:64000\n\t"
    "ds_read_b64 v[86:87], v252 offset:64000\n\t"
    "ds_read_b64 v[88:89], v145 offset:64000\n\t"
    "ds_read_b64 v[90:91], v253 offset:64000\n\t"
    "v_lshrrev_b32 v254, 16, v146\n\t"
    "v_and_b32 v146, s92, v146\n\t"
    "v_lshrrev_b32 v255, 16, v147\n\t"
    "v_and_b32 v147, s92, v147\n\t"
    "v_pk_mul_f32 v[66:67], v[66:67], v[182:183]\n\t"
    "v_exp_f32 v68, v64\n\t"
    "v_exp_f32 v69, v65\n\t"
    "v_exp_f32 v70, v66\n\t"
    "v_exp_f32 v71, v67\n\t"
    "s_waitcnt lgkmcnt(4)\n\t"
    "v_smfmac_f32_16x16x64_bf16 v[200:203], v[120:123], v[92:99], v124\n\t"
    "ds_read_b64 v[92:93], v146 offset:64000\n\t"
    "ds_read_b64 v[94:95], v254 offset:64000\n\t"
    "ds_read_b64 v[96:97], v147 offset:64000\n\t"
    "ds_read_b64 v[98:99], v255 offset:64000\n\t"
    "buffer_load_dwordx4 v[220:223], v127, %12, s89 offen offset:1024 sc1\n\t"
    "v_lshrrev_b32 v252, 16, v148\n\t"
    "v_and_b32 v148, s92, v148\n\t"
    "v_lshrrev_b32 v253, 16, v149\n\t"
    "v_and_b32 v149, s92, v149\n\t"
    "v_pk_fma_f32 v[68:69], v[68:69], s[40:41], s[40:41]\n\t"
    "v_pk_fma_f32 v[70:71], v[70:71], s[42:43], s[42:43]\n\t"
    "v_rcp_f32 v68, v68\n\t"
    "v_rcp_f32 v69, v69\n\t"
    "v_rcp_f32 v70, v70\n\t"
    "s_waitcnt lgkmcnt(4)\n\t"
    "v_smfmac_f32_16x16x64_bf16 v[184:187], v[120:123], v[84:91], v124\n\t"
    "ds_read_b64 v[84:85], v148 offset:0\n\t"
    "ds_read_b64 v[86:87], v252 offset:0\n\t"
    "ds_read_b64 v[88:89], v149 offset:0\n\t"
    "ds_read_b64 v[90:91], v253 offset:0\n\t"
    "v_lshrrev_b32 v254, 16, v150\n\t"
    "v_and_b32 v150, s92, v150\n\t"
    "v_lshrrev_b32 v255, 16, v151\n\t"
    "v_and_b32 v151, s92, v151\n\t"
    "s_add_u32 s94, s94, 0x2000\n\t"
    "buffer_load_dwordx4 v[28:31], v127, %12, s94 offen sc1\n\t"
    "v_rcp_f32 v71, v71\n\t"
    "v_pk_mul_f32 v[68:69], v[68:69], v[180:181]\n\t"
    "v_pk_fma_f32 v[68:69], v[70:71], v[182:183], v[68:69]\n\t"
    "v_add_f32 v238, v68, v69\n\t"
    "s_waitcnt lgkmcnt(4)\n\t"
    "v_smfmac_f32_16x16x64_bf16 v[184:187], v[120:123], v[92:99], v124\n\t"
    "ds_read_b64 v[92:93], v150 offset:0\n\t"
    "ds_read_b64 v[94:95], v254 offset:0\n\t"
    "ds_read_b64 v[96:97], v151 offset:0\n\t"
    "ds_read_b64 v[98:99], v255 offset:0\n\t"
    "buffer_load_dwordx4 v[116:119], v127, %12, s87 offen offset:1024 sc1\n\t"
    "v_lshrrev_b32 v252, 16, v152\n\t"
    "v_and_b32 v152, s92, v152\n\t"
    "v_lshrrev_b32 v253, 16, v153\n\t"
    "v_mov_b64 v[204:205], s[70:71]\n\t"
    "v_mov_b64 v[206:207], s[72:73]\n\t"
    "v_and_b32 v153, s92, v153\n\t"
    "v_pk_mul_f32 v[72:73], v[184:185], v[184:185]\n\t"
    "v_pk_mul_f32 v[74:75], v[186:187], v[186:187]\n\t"
    "v_pk_fma_f32 v[72:73], v[72:73], v[82:83], v[80:81]\n\t"
    "v_pk_fma_f32 v[74:75], v[74:75], v[82:83], v[80:81]\n\t"
    "v_pk_mul_f32 v[72:73], v[72:73], v[184:185]\n\t"
    "s_waitcnt lgkmcnt(4)\n\t"
    "v_smfmac_f32_16x16x64_bf16 v[204:207], v[120:123], v[84:91], v124\n\t"
    "ds_read_b64 v[84:85], v152 offset:64000\n\t"
    "ds_read_b64 v[86:87], v252 offset:64000\n\t"
    "ds_read_b64 v[88:89], v153 offset:64000\n\t"
    "ds_read_b64 v[90:91], v253 offset:64000\n\t"
    "v_lshrrev_b32 v254, 16, v154\n\t"
    "v_and_b32 v154, s92, v154\n\t"
    "v_lshrrev_b32 v255, 16, v155\n\t"
    "v_and_b32 v155, s92, v155\n\t"
    "v_pk_mul_f32 v[74:75], v[74:75], v[186:187]\n\t"
    "v_exp_f32 v76, v72\n\t"
    "v_exp_f32 v77, v73\n\t"
    "v_exp_f32 v78, v74\n\t"
    "v_exp_f32 v79, v75\n\t"
    "s_waitcnt vmcnt(8) lgkmcnt(4)\n\t"
    "v_smfmac_f32_16x16x64_bf16 v[204:207], v[120:123], v[92:99], v124\n\t"
    "ds_read_b64 v[92:93], v154 offset:64000\n\t"
    "ds_read_b64 v[94:95], v254 offset:64000\n\t"
    "ds_read_b64 v[96:97], v155 offset:64000\n\t"
    "ds_read_b64 v[98:99], v255 offset:64000\n\t"
    "s_sub_u32 s90, s90, s95\n\t"
    "buffer_load_dwordx4 v[224:227], v127, %12, s90 offen offset:0 sc1\n\t"
    "v_lshrrev_b32 v252, 16, v244\n\t"
    "v_and_b32 v244, s92, v244\n\t"
    "v_lshrrev_b32 v253, 16, v245\n\t"
    "v_and_b32 v245, s92, v245\n\t"
    "v_pk_fma_f32 v[76:77], v[76:77], s[40:41], s[40:41]\n\t"
    "v_pk_fma_f32 v[78:79], v[78:79], s[42:43], s[42:43]\n\t"
    "v_rcp_f32 v76, v76\n\t"
    "v_rcp_f32 v77, v77\n\t"
    "v_rcp_f32 v78, v78\n\t"
    "s_waitcnt lgkmcnt(4)\n\t"
    "v_smfmac_f32_16x16x64_bf16 v[188:191], v[120:123], v[84:91], v124\n\t"
    "ds_read_b64 v[84:85], v244 offset:0\n\t"
    "ds_read_b64 v[86:87], v252 offset:0\n\t"
    "ds_read_b64 v[88:89], v245 offset:0\n\t"
    "ds_read_b64 v[90:91], v253 offset:0\n\t"
    "v_lshrrev_b32 v254, 16, v246\n\t"
    "v_and_b32 v246, s92, v246\n\t"
    "v_lshrrev_b32 v255, 16, v247\n\t"
    "v_and_b32 v247, s92, v247\n\t"
    "s_waitcnt vmcnt(6)\n\t"
    "v_smfmac_f32_16x16x64_bf16 v[188:191], v[156:159], v[16:23], v124\n\t"
    "v_rcp_f32 v79, v79\n\t"
    "v_pk_mul_f32 v[76:77], v[76:77], v[184:185]\n\t"
    "v_pk_fma_f32 v[76:77], v[78:79], v[186:187], v[76:77]\n\t"
    "v_add_f32 v239, v76, v77\n\t"
    "v_lshrrev_b32 v147, 4, v127\n\t"
    "v_add_u32 v148, 140288, v127\n\t"
    "s_lshl_b32 s65, %14, 12\n\t"
    "s_add_u32 s65, s65, 16777216\n\t"
    "buffer_load_dwordx4 v[0:3], v127, %12, s65 offen offset:0 sc1\n\t"
    "s_waitcnt lgkmcnt(4)\n\t"
    "v_smfmac_f32_16x16x64_bf16 v[188:191], v[120:123], v[92:99], v124\n\t"
    "ds_read_b64 v[92:93], v246 offset:0\n\t"
    "ds_read_b64 v[94:95], v254 offset:0\n\t"
    "ds_read_b64 v[96:97], v247 offset:0\n\t"
    "ds_read_b64 v[98:99], v255 offset:0\n\t"
    "s_add_u32 s88, s88, 0x8000\n\t"
    "buffer_load_dwordx4 v[228:231], v127, %12, s88 offen offset:0 sc1\n\t"
    "v_lshrrev_b32 v252, 16, v248\n\t"
    "v_and_b32 v248, s92, v248\n\t"
    "v_lshrrev_b32 v253, 16, v249\n\t"
    "v_mov_b64 v[208:209], s[70:71]\n\t"
    "v_mov_b64 v[210:211], s[72:73]\n\t"
    "v_and_b32 v249, s92, v249\n\t"
    "v_smfmac_f32_16x16x64_bf16 v[192:195], v[160:163], v[16:23], v124\n\t"
    "v_pk_mul_f32 v[64:65], v[188:189], v[188:189]\n\t"
    "v_pk_mul_f32 v[66:67], v[190:191], v[190:191]\n\t"
    "v_pk_fma_f32 v[64:65], v[64:65], v[82:83], v[80:81]\n\t"
    "v_pk_fma_f32 v[66:67], v[66:67], v[82:83], v[80:81]\n\t"
    "v_pk_mul_f32 v[64:65], v[64:65], v[188:189]\n\t"
    "s_lshl_b32 s65, %14, 12\n\t"
    "s_add_u32 s65, s65, 16777216\n\t"
    "buffer_load_dwordx4 v[4:7], v127, %12, s65 offen offset:1024 sc1\n\t"
    "s_waitcnt lgkmcnt(4)\n\t"
    "v_smfmac_f32_16x16x64_bf16 v[208:211], v[120:123], v[84:91], v124\n\t"
    "ds_read_b64 v[84:85], v248 offset:64000\n\t"
    "ds_read_b64 v[86:87], v252 offset:64000\n\t"
    "ds_read_b64 v[88:89], v249 offset:64000\n\t"
    "ds_read_b64 v[90:91], v253 offset:64000\n\t"
    "v_lshrrev_b32 v254, 16, v250\n\t"
    "v_and_b32 v250, s92, v250\n\t"
    "v_lshrrev_b32 v255, 16, v251\n\t"
    "v_and_b32 v251, s92, v251\n\t"
    "s_mov_b64 exec, 1\n\t"
    "buffer_load_dword v164, off, %12, s93 offset:256 sc1\n\t"
    "s_mov_b64 exec, -1\n\t"
    "v_pk_mul_f32 v[66:67], v[66:67], v[190:191]\n\t"
    "v_exp_f32 v68, v64\n\t"
    "v_exp_f32 v69, v65\n\t"
    "v_exp_f32 v70, v66\n\t"
    "v_exp_f32 v71, v67\n\t"
    "s_lshl_b32 s65, %14, 12\n\t"
    "s_add_u32 s65, s65, 16777216\n\t"
    "buffer_load_dwordx4 v[8:11], v127, %12, s65 offen offset:2048 sc1\n\t"
    "s_waitcnt lgkmcnt(4)\n\t"
    "v_smfmac_f32_16x16x64_bf16 v[208:211], v[120:123], v[92:99], v124\n\t"
    "ds_read_b64 v[92:93], v250 offset:64000\n\t"
    "ds_read_b64 v[94:95], v254 offset:64000\n\t"
    "ds_read_b64 v[96:97], v251 offset:64000\n\t"
    "ds_read_b64 v[98:99], v255 offset:64000\n\t"
    "buffer_load_dwordx4 v[232:235], v127, %12, s90 offen offset:1024 sc1\n\t"
    "v_lshrrev_b32 v252, 16, v212\n\t"
    "v_and_b32 v212, s92, v212\n\t"
    "v_lshrrev_b32 v253, 16, v213\n\t"
    "v_and_b32 v213, s92, v213\n\t"
    "v_pk_fma_f32 v[68:69], v[68:69], s[40:41], s[40:41]\n\t"
    "v_pk_fma_f32 v[70:71], v[70:71], s[42:43], s[42:43]\n\t"
    "v_rcp_f32 v68, v68\n\t"
    "v_rcp_f32 v69, v69\n\t"
    "v_rcp_f32 v70, v70\n\t"
    "s_lshl_b32 s65, %14, 12\n\t"
    "s_add_u32 s65, s65, 16777216\n\t"
    "buffer_load_dwordx4 v[12:15], v127, %12, s65 offen offset:3072 sc1\n\t"
    "s_waitcnt lgkmcnt(4)\n\t"
    "v_smfmac_f32_16x16x64_bf16 v[192:195], v[120:123], v[84:91], v124\n\t"
    "ds_read_b64 v[84:85], v212 offset:0\n\t"
    "ds_read_b64 v[86:87], v252 offset:0\n\t"
    "ds_read_b64 v[88:89], v213 offset:0\n\t"
    "ds_read_b64 v[90:91], v253 offset:0\n\t"
    "v_lshrrev_b32 v254, 16, v214\n\t"
    "v_and_b32 v214, s92, v214\n\t"
    "v_lshrrev_b32 v255, 16, v215\n\t"
    "v_and_b32 v215, s92, v215\n\t"
    "s_waitcnt vmcnt(9)\n\t"
    "v_smfmac_f32_16x16x64_bf16 v[196:199], v[156:159], v[24:31], v124\n\t"
    "v_rcp_f32 v71, v71\n\t"
    "v_pk_mul_f32 v[68:69], v[68:69], v[188:189]\n\t"
    "v_pk_fma_f32 v[68:69], v[70:71], v[190:191], v[68:69]\n\t"
    "v_add_f32 v240, v68, v69\n\t"
    "ds_read_b128 v[16:19], v148 offset:4096\n\t"
    "s_cmp_lt_u32 %16, 4\n\t"
    "s_cbranch_scc1 L_tp239_%=\n\t"
    "s_setprio 1\n\t"
    "L_tp239_%=:\n\t"
    "s_waitcnt lgkmcnt(5)\n\t"
    "v_smfmac_f32_16x16x64_bf16 v[192:195], v[120:123], v[92:99], v124\n\t"
    "ds_read_b64 v[92:93], v214 offset:0\n\t"
    "ds_read_b64 v[94:95], v254 offset:0\n\t"
    "ds_read_b64 v[96:97], v215 offset:0\n\t"
    "ds_read_b64 v[98:99], v255 offset:0\n\t"
    "buffer_load_dwordx4 v[168:171], v127, %12, s88 offen offset:1024 sc1\n\t"
    "v_lshrrev_b32 v252, 16, v216\n\t"
    "v_and_b32 v216, s92, v216\n\t"
    "v_lshrrev_b32 v253, 16, v217\n\t"
    "v_mov_b64 v[172:173], s[70:71]\n\t"
    "v_mov_b64 v[174:175], s[72:73]\n\t"
    "v_and_b32 v217, s92, v217\n\t"
    "v_smfmac_f32_16x16x64_bf16 v[200:203], v[160:163], v[24:31], v124\n\t"
    "v_pk_mul_f32 v[72:73], v[192:193], v[192:193]\n\t"
    "v_pk_mul_f32 v[74:75], v[194:195], v[194:195]\n\t"
    "v_pk_fma_f32 v[72:73], v[72:73], v[82:83], v[80:81]\n\t"
    "v_pk_fma_f32 v[74:75], v[74:75], v[82:83], v[80:81]\n\t"
    "v_pk_mul_f32 v[72:73], v[72:73], v[192:193]\n\t"
    "ds_read_b128 v[20:23], v148 offset:5120\n\t"
    "s_waitcnt lgkmcnt(6)\n\t"
    "v_smfmac_f32_16x16x64_bf16 v[172:175], v[120:123], v[84:91], v124\n\t"
    "ds_read_b64 v[84:85], v216 offset:64000\n\t"
    "ds_read_b64 v[86:87], v252 offset:64000\n\t"
    "ds_read_b64 v[88:89], v217 offset:64000\n\t"
    "ds_read_b64 v[90:91], v253 offset:64000\n\t"
    "v_lshrrev_b32 v254, 16, v218\n\t"
    "v_and_b32 v218, s92, v218\n\t"
    "v_lshrrev_b32 v255, 16, v219\n\t"
    "v_and_b32 v219, s92, v219\n\t"
    "v_pk_mul_f32 v[74:75], v[74:75], v[194:195]\n\t"
    "v_exp_f32 v76, v72\n\t"
    "v_exp_f32 v77, v73\n\t"
    "v_exp_f32 v78, v74\n\t"
    "v_exp_f32 v79, v75\n\t"
    "ds_read_b128 v[24:27], v148 offset:6144\n\t"
    "s_waitcnt lgkmcnt(6)\n\t"
    "v_smfmac_f32_16x16x64_bf16 v[172:175], v[120:123], v[92:99], v124\n\t"
    "ds_read_b64 v[92:93], v218 offset:64000\n\t"
    "ds_read_b64 v[94:95], v254 offset:64000\n\t"
    "ds_read_b64 v[96:97], v219 offset:64000\n\t"
    "ds_read_b64 v[98:99], v255 offset:64000\n\t"
    "s_sub_u32 s91, s91, s95\n\t"
    "buffer_load_dwordx4 v[112:115], v127, %12, s91 offen offset:0 sc1\n\t"
    "v_lshrrev_b32 v252, 16, v220\n\t"
    "v_and_b32 v220, s92, v220\n\t"
    "v_lshrrev_b32 v253, 16, v221\n\t"
    "v_and_b32 v221, s92, v221\n\t"
    "v_pk_fma_f32 v[76:77], v[76:77], s[40:41], s[40:41]\n\t"
    "v_pk_fma_f32 v[78:79], v[78:79], s[42:43], s[42:43]\n\t"
    "v_rcp_f32 v76, v76\n\t"
    "v_rcp_f32 v77, v77\n\t"
    "v_rcp_f32 v78, v78\n\t"
    "ds_read_b128 v[28:31], v148 offset:7168\n\t"
    "s_waitcnt lgkmcnt(6)\n\t"
    "v_smfmac_f32_16x16x64_bf16 v[196:199], v[120:123], v[84:91], v124\n\t"
    "ds_read_b64 v[84:85], v220 offset:0\n\t"
    "ds_read_b64 v[86:87], v252 offset:0\n\t"
    "ds_read_b64 v[88:89], v221 offset:0\n\t"
    "ds_read_b64 v[90:91], v253 offset:0\n\t"
    "v_lshrrev_b32 v254, 16, v222\n\t"
    "v_and_b32 v222, s92, v222\n\t"
    "v_lshrrev_b32 v255, 16, v223\n\t"
    "v_and_b32 v223, s92, v223\n\t"
    "s_waitcnt vmcnt(5)\n\t"
    "v_readfirstlane_b32 s65, v164\n\t"
    "s_nop 0\n\t"
    "s_cmp_eq_u32 s65, 0\n\t"
    "s_cbranch_scc1 L_xw2_%=\n\t"
    "L_xwb2_%=:\n\t"
    "s_add_u32 s94, s94, 0x2000\n\t"
    "buffer_load_dwordx4 v[36:39], v127, %12, s94 offen sc1\n\t"
    "v_rcp_f32 v79, v79\n\t"
    "v_pk_mul_f32 v[76:77], v[76:77], v[192:193]\n\t"
    "v_pk_fma_f32 v[76:77], v[78:79], v[194:195], v[76:77]\n\t"
    "v_add_f32 v241, v76, v77\n\t"
    "s_waitcnt lgkmcnt(5)\n\t"
    "v_smfmac_f32_16x16x64_bf16 v[196:199], v[120:123], v[92:99], v124\n\t"
    "ds_read_b64 v[92:93], v222 offset:0\n\t"
    "ds_read_b64 v[94:95], v254 offset:0\n\t"
    "ds_read_b64 v[96:97], v223 offset:0\n\t"
    "ds_read_b64 v[98:99], v255 offset:0\n\t"
    "s_add_u32 s89, s89, 0x8000\n\t"
    "buffer_load_dwordx4 v[100:103], v127, %12, s89 offen offset:0 sc1\n\t"
    "v_lshrrev_b32 v252, 16, v116\n\t"
    "v_and_b32 v116, s92, v116\n\t"
    "v_lshrrev_b32 v253, 16, v117\n\t"
    "v_mov_b64 v[176:177], s[70:71]\n\t"
    "v_mov_b64 v[178:179], s[72:73]\n\t"
    "v_and_b32 v117, s92, v117\n\t"
    "v_pk_mul_f32 v[64:65], v[196:197], v[196:197]\n\t"
    "v_pk_mul_f32 v[66:67], v[198:199], v[198:199]\n\t"
    "v_pk_fma_f32 v[64:65], v[64:65], v[82:83], v[80:81]\n\t"
    "v_pk_fma_f32 v[66:67], v[66:67], v[82:83], v[80:81]\n\t"
    "v_pk_mul_f32 v[64:65], v[64:65], v[196:197]\n\t"
    "s_waitcnt lgkmcnt(4)\n\t"
    "v_smfmac_f32_16x16x64_bf16 v[176:179], v[120:123], v[84:91], v124\n\t"
    "ds_read_b64 v[84:85], v116 offset:64000\n\t"
    "ds_read_b64 v[86:87], v252 offset:64000\n\t"
    "ds_read_b64 v[88:89], v117 offset:64000\n\t"
    "ds_read_b64 v[90:91], v253 offset:64000\n\t"
    "v_lshrrev_b32 v254, 16, v118\n\t"
    "v_and_b32 v118, s92, v118\n\t"
    "v_lshrrev_b32 v255, 16, v119\n\t"
    "v_and_b32 v119, s92, v119\n\t"
    "v_pk_mul_f32 v[66:67], v[66:67], v[198:199]\n\t"
    "v_exp_f32 v68, v64\n\t"
    "v_exp_f32 v69, v65\n\t"
    "v_exp_f32 v70, v66\n\t"
    "v_exp_f32 v71, v67\n\t"
    "s_waitcnt lgkmcnt(4)\n\t"
    "v_smfmac_f32_16x16x64_bf16 v[176:179], v[120:123], v[92:99], v124\n\t"
    "ds_read_b64 v[92:93], v118 offset:64000\n\t"
    "ds_read_b64 v[94:95], v254 offset:64000\n\t"
    "ds_read_b64 v[96:97], v119 offset:64000\n\t"
    "ds_read_b64 v[98:99], v255 offset:64000\n\t"
    "buffer_load_dwordx4 v[104:107], v127, %12, s91 offen offset:1024 sc1\n\t"
    "v_lshrrev_b32 v252, 16, v224\n\t"
    "v_and_b32 v224, s92, v224\n\t"
    "v_lshrrev_b32 v253, 16, v225\n\t"
    "v_and_b32 v225, s92, v225\n\t"
    "v_pk_fma_f32 v[68:69], v[68:69], s[40:41], s[40:41]\n\t"
    "v_pk_fma_f32 v[70:71], v[70:71], s[42:43], s[42:43]\n\t"
    "v_rcp_f32 v68, v68\n\t"
    "v_rcp_f32 v69, v69\n\t"
    "v_rcp_f32 v70, v70\n\t"
    "s_waitcnt lgkmcnt(4)\n\t"
    "v_smfmac_f32_16x16x64_bf16 v[200:203], v[120:123], v[84:91], v124\n\t"
    "ds_read_b64 v[84:85], v224 offset:0\n\t"
    "ds_read_b64 v[86:87], v252 offset:0\n\t"
    "ds_read_b64 v[88:89], v225 offset:0\n\t"
    "ds_read_b64 v[90:91], v253 offset:0\n\t"
    "v_lshrrev_b32 v254, 16, v226\n\t"
    "v_and_b32 v226, s92, v226\n\t"
    "v_lshrrev_b32 v255, 16, v227\n\t"
    "v_and_b32 v227, s92, v227\n\t"
    "s_add_u32 s94, s94, 0x2000\n\t"
    "buffer_load_dwordx4 v[44:47], v127, %12, s94 offen sc1\n\t"
    "v_rcp_f32 v71, v71\n\t"
    "v_pk_mul_f32 v[68:69], v[68:69], v[196:197]\n\t"
    "v_pk_fma_f32 v[68:69], v[70:71], v[198:199], v[68:69]\n\t"
    "v_add_f32 v242, v68, v69\n\t"
    "s_lshl_b32 s46, %14, 2\n\t"
    "s_add_u32 s46, s46, 49294336\n\t"
    "s_mov_b64 exec, 1\n\t"
    "buffer_load_dword v144, off, %12, s46 sc1\n\t"
    "s_mov_b64 exec, -1\n\t"
    "s_waitcnt lgkmcnt(4)\n\t"
    "v_smfmac_f32_16x16x64_bf16 v[200:203], v[120:123], v[92:99], v124\n\t"
    "ds_read_b64 v[92:93], v226 offset:0\n\t"
    "ds_read_b64 v[94:95], v254 offset:0\n\t"
    "ds_read_b64 v[96:97], v227 offset:0\n\t"
    "ds_read_b64 v[98:99], v255 offset:0\n\t"
    "buffer_load_dwordx4 v[108:111], v127, %12, s89 offen offset:1024 sc1\n\t"
    "v_lshrrev_b32 v252, 16, v228\n\t"
    "v_and_b32 v228, s92, v228\n\t"
    "v_lshrrev_b32 v253, 16, v229\n\t"
    "v_mov_b64 v[180:181], s[70:71]\n\t"
    "v_mov_b64 v[182:183], s[72:73]\n\t"
    "v_and_b32 v229, s92, v229\n\t"
    "v_pk_mul_f32 v[72:73], v[200:201], v[200:201]\n\t"
    "v_pk_mul_f32 v[74:75], v[202:203], v[202:203]\n\t"
    "v_pk_fma_f32 v[72:73], v[72:73], v[82:83], v[80:81]\n\t"
    "v_pk_fma_f32 v[74:75], v[74:75], v[82:83], v[80:81]\n\t"
    "v_pk_mul_f32 v[72:73], v[72:73], v[200:201]\n\t"
    "s_waitcnt lgkmcnt(4)\n\t"
    "v_smfmac_f32_16x16x64_bf16 v[180:183], v[120:123], v[84:91], v124\n\t"
    "ds_read_b64 v[84:85], v228 offset:64000\n\t"
    "ds_read_b64 v[86:87], v252 offset:64000\n\t"
    "ds_read_b64 v[88:89], v229 offset:64000\n\t"
    "ds_read_b64 v[90:91], v253 offset:64000\n\t"
    "v_lshrrev_b32 v254, 16, v230\n\t"
    "v_and_b32 v230, s92, v230\n\t"
    "v_lshrrev_b32 v255, 16, v231\n\t"
    "v_and_b32 v231, s92, v231\n\t"
    "v_pk_mul_f32 v[74:75], v[74:75], v[202:203]\n\t"
    "v_exp_f32 v76, v72\n\t"
    "v_exp_f32 v77, v73\n\t"
    "v_exp_f32 v78, v74\n\t"
    "v_exp_f32 v79, v75\n\t"
    "s_waitcnt vmcnt(9) lgkmcnt(4)\n\t"
    "v_smfmac_f32_16x16x64_bf16 v[180:183], v[120:123], v[92:99], v124\n\t"
    "ds_read_b64 v[92:93], v230 offset:64000\n\t"
    "ds_read_b64 v[94:95], v254 offset:64000\n\t"
    "ds_read_b64 v[96:97], v231 offset:64000\n\t"
    "ds_read_b64 v[98:99], v255 offset:64000\n\t"
    "s_add_u32 s90, s90, 0x8000\n\t"
    "buffer_load_dwordx4 v[248:251], v127, %12, s90 offen offset:0 sc1\n\t"
    "v_lshrrev_b32 v252, 16, v232\n\t"
    "v_and_b32 v232, s92, v232\n\t"
    "v_lshrrev_b32 v253, 16, v233\n\t"
    "v_and_b32 v233, s92, v233\n\t"
    "v_pk_fma_f32 v[76:77], v[76:77], s[40:41], s[40:41]\n\t"
    "v_pk_fma_f32 v[78:79], v[78:79], s[42:43], s[42:43]\n\t"
    "v_rcp_f32 v76, v76\n\t"
    "v_rcp_f32 v77, v77\n\t"
    "v_rcp_f32 v78, v78\n\t"
    "s_waitcnt lgkmcnt(4)\n\t"
    "v_smfmac_f32_16x16x64_bf16 v[204:207], v[120:123], v[84:91], v124\n\t"
    "ds_read_b64 v[84:85], v232 offset:0\n\t"
    "ds_read_b64 v[86:87], v252 offset:0\n\t"
    "ds_read_b64 v[88:89], v233 offset:0\n\t"
    "ds_read_b64 v[90:91], v253 offset:0\n\t"
    "v_lshrrev_b32 v254, 16, v234\n\t"
    "v_and_b32 v234, s92, v234\n\t"
    "v_lshrrev_b32 v255, 16, v235\n\t"
    "v_and_b32 v235, s92, v235\n\t"
    "s_mov_b64 exec, 1\n\t"
    "buffer_load_dword v164, off, %12, s93 offset:384 sc1\n\t"
    "s_mov_b64 exec, -1\n\t"
    "s_waitcnt vmcnt(7)\n\t"
    "v_smfmac_f32_16x16x64_bf16 v[204:207], v[156:159], v[32:39], v124\n\t"
    "v_rcp_f32 v79, v79\n\t"
    "v_pk_mul_f32 v[76:77], v[76:77], v[200:201]\n\t"
    "v_pk_fma_f32 v[76:77], v[78:79], v[202:203], v[76:77]\n\t"
    "v_add_f32 v243, v76, v77\n\t"
    "s_waitcnt lgkmcnt(4)\n\t"
    "v_smfmac_f32_16x16x64_bf16 v[204:207], v[120:123], v[92:99], v124\n\t"
    "ds_read_b64 v[92:93], v234 offset:0\n\t"
    "ds_read_b64 v[94:95], v254 offset:0\n\t"
    "ds_read_b64 v[96:97], v235 offset:0\n\t"
    "ds_read_b64 v[98:99], v255 offset:0\n\t"
    "buffer_load_dwordx4 v[212:215], v127, %12, s90 offen offset:1024 sc1\n\t"
    "v_lshrrev_b32 v252, 16, v168\n\t"
    "v_and_b32 v168, s92, v168\n\t"
    "v_lshrrev_b32 v253, 16, v169\n\t"
    "v_mov_b64 v[184:185], s[70:71]\n\t"
    "v_mov_b64 v[186:187], s[72:73]\n\t"
    "v_and_b32 v169, s92, v169\n\t"
    "v_smfmac_f32_16x16x64_bf16 v[208:211], v[160:163], v[32:39], v124\n\t"
    "v_pk_mul_f32 v[64:65], v[204:205], v[204:205]\n\t"
    "v_pk_mul_f32 v[66:67], v[206:207], v[206:207]\n\t"
    "v_pk_fma_f32 v[64:65], v[64:65], v[82:83], v[80:81]\n\t"
    "v_pk_fma_f32 v[66:67], v[66:67], v[82:83], v[80:81]\n\t"
    "v_pk_mul_f32 v[64:65], v[64:65], v[204:205]\n\t"
    "ds_read_b128 v[32:35], v148 offset:8192\n\t"
    "s_waitcnt lgkmcnt(5)\n\t"
    "v_smfmac_f32_16x16x64_bf16 v[184:187], v[120:123], v[84:91], v124\n\t"
    "ds_read_b64 v[84:85], v168 offset:64000\n\t"
    "ds_read_b64 v[86:87], v252 offset:64000\n\t"
    "ds_read_b64 v[88:89], v169 offset:64000\n\t"
    "ds_read_b64 v[90:91], v253 offset:64000\n\t"
    "v_lshrrev_b32 v254, 16, v170\n\t"
    "v_and_b32 v170, s92, v170\n\t"
    "v_lshrrev_b32 v255, 16, v171\n\t"
    "v_and_b32 v171, s92, v171\n\t"
    "v_pk_mul_f32 v[66:67], v[66:67], v[206:207]\n\t"
    "v_exp_f32 v68, v64\n\t"
    "v_exp_f32 v69, v65\n\t"
    "v_exp_f32 v70, v66\n\t"
    "v_exp_f32 v71, v67\n\t"
    "ds_read_b128 v[36:39], v148 offset:9216\n\t"
    "s_waitcnt lgkmcnt(6)\n\t"
    "v_smfmac_f32_16x16x64_bf16 v[184:187], v[120:123], v[92:99], v124\n\t"
    "ds_read_b64 v[92:93], v170 offset:64000\n\t"
    "ds_read_b64 v[94:95], v254 offset:64000\n\t"
    "ds_read_b64 v[96:97], v171 offset:64000\n\t"
    "ds_read_b64 v[98:99], v255 offset:64000\n\t"
    "s_add_u32 s91, s91, 0x8000\n\t"
    "buffer_load_dwordx4 v[216:219], v127, %12, s91 offen offset:0 sc1\n\t"
    "v_lshrrev_b32 v252, 16, v112\n\t"
    "v_and_b32 v112, s92, v112\n\t"
    "v_lshrrev_b32 v253, 16, v113\n\t"
    "v_and_b32 v113, s92, v113\n\t"
    "v_pk_fma_f32 v[68:69], v[68:69], s[40:41], s[40:41]\n\t"
    "v_pk_fma_f32 v[70:71], v[70:71], s[42:43], s[42:43]\n\t"
    "v_rcp_f32 v68, v68\n\t"
    "v_rcp_f32 v69, v69\n\t"
    "v_rcp_f32 v70, v70\n\t"
    "s_waitcnt lgkmcnt(5)\n\t"
    "v_smfmac_f32_16x16x64_bf16 v[208:211], v[120:123], v[84:91], v124\n\t"
    "ds_read_b64 v[84:85], v112 offset:0\n\t"
    "ds_read_b64 v[86:87], v252 offset:0\n\t"
    "ds_read_b64 v[88:89], v113 offset:0\n\t"
    "ds_read_b64 v[90:91], v253 offset:0\n\t"
    "v_lshrrev_b32 v254, 16, v114\n\t"
    "v_and_b32 v114, s92, v114\n\t"
    "v_lshrrev_b32 v255, 16, v115\n\t"
    "v_and_b32 v115, s92, v115\n\t"
    "s_waitcnt vmcnt(6)\n\t"
    "v_smfmac_f32_16x16x64_bf16 v[172:175], v[156:159], v[40:47], v124\n\t"
    "v_rcp_f32 v71, v71\n\t"
    "v_pk_mul_f32 v[68:69], v[68:69], v[204:205]\n\t"
    "v_pk_fma_f32 v[68:69], v[70:71], v[206:207], v[68:69]\n\t"
    "v_add_f32 v244, v68, v69\n\t"
    "s_waitcnt vmcnt(5)\n\t"
    "v_readfirstlane_b32 s38, v144\n\t"
    "s_nop 0\n\t"
    "s_cmp_eq_u32 s38, 0\n\t"
    "s_cbranch_scc1 L_rw_%=\n\t"
    "L_rok_%=:\n\t"
    "s_and_b32 s46, %16, 3\n\t"
    "s_mul_i32 s49, s46, 1040\n\t"
    "s_add_u32 s49, s49, 2064\n\t"
    "s_and_b32 s37, s38, 0xffff\n\t"
    "s_lshr_b32 s44, s38, 16\n\t"
    "s_sub_u32 s44, s44, s37\n\t"
    "s_lshl_b32 s37, s37, 2\n\t"
    "s_lshl_b32 s38, s44, 2\n\t"
    "s_mul_i32 s51, s46, s44\n\t"
    "s_add_u32 s48, s51, s44\n\t"
    "s_lshl_b32 s48, s48, 16\n\t"
    "s_add_u32 s48, s48, s51\n\t"
    "s_lshr_b32 s47, %16, 2\n\t"
    "s_add_u32 s48, s48, s47\n\t"
    "s_add_u32 s48, s48, 0x200\n\t"
    "s_mul_i32 s46, s37, 400\n\t"
    "s_add_u32 s96, %18, s46\n\t"
    "s_addc_u32 s97, %19, 0\n\t"
    "s_sub_u32 s98, 6553600, s46\n\t"
    "s_mov_b32 s99, 0x20000\n\t"
    "v_bfe_u32 v154, v147, 2, 2\n\t"
    "v_and_b32 v155, 3, v147\n\t"
    "v_mul_u32_u24 v154, 25, v154\n\t"
    "v_mad_u32_u24 v154, v155, 6, v154\n\t"
    "v_lshlrev_b32 v153, 2, v154\n\t"
    "s_and_b32 s53, s48, 0xff\n\t"
    "s_bfe_u32 s55, s48, 0x80008\n\t"
    "s_add_u32 s54, s53, s55\n\t"
    "s_add_u32 s55, s54, s55\n\t"
    "s_lshr_b32 s64, s48, 16\n\t"
    "s_mul_i32 s66, s53, 400\n\t"
    "buffer_load_dwordx3 v[128:130], v153, s[96:99], s66 offen\n\t"
    "buffer_load_dwordx4 v[132:135], v153, s[96:99], s66 offen offset:12\n\t"
    "s_mul_i32 s66, s54, 400\n\t"
    "buffer_load_dwordx3 v[136:138], v153, s[96:99], s66 offen\n\t"
    "buffer_load_dwordx4 v[140:143], v153, s[96:99], s66 offen offset:12\n\t"
    "v_cmp_eq_u32 s[62:63], 0, v155\n\t"
    "v_and_b32 v151, 48, v147\n\t"
    "v_or_b32 v155, 0x100000, v151\n\t"
    "v_cndmask_b32 v152, v155, v151, s[62:63]\n\t"
    "s_waitcnt lgkmcnt(4)\n\t"
    "v_smfmac_f32_16x16x64_bf16 v[208:211], v[120:123], v[92:99], v124\n\t"
    "ds_read_b64 v[92:93], v114 offset:0\n\t"
    "ds_read_b64 v[94:95], v254 offset:0\n\t"
    "ds_read_b64 v[96:97], v115 offset:0\n\t"
    "ds_read_b64 v[98:99], v255 offset:0\n\t"
    "buffer_load_dwordx4 v[220:223], v127, %12, s91 offen offset:1024 sc1\n\t"
    "v_lshrrev_b32 v252, 16, v100\n\t"
    "v_and_b32 v100, s92, v100\n\t"
    "v_lshrrev_b32 v253, 16, v101\n\t"
    "v_mov_b64 v[188:189], s[70:71]\n\t"
    "v_mov_b64 v[190:191], s[72:73]\n\t"
    "v_and_b32 v101, s92, v101\n\t"
    "v_smfmac_f32_16x16x64_bf16 v[176:179], v[160:163], v[40:47], v124\n\t"
    "v_pk_mul_f32 v[72:73], v[208:209], v[208:209]\n\t"
    "v_pk_mul_f32 v[74:75], v[210:211], v[210:211]\n\t"
    "v_pk_fma_f32 v[72:73], v[72:73], v[82:83], v[80:81]\n\t"
    "v_pk_fma_f32 v[74:75], v[74:75], v[82:83], v[80:81]\n\t"
    "v_pk_mul_f32 v[72:73], v[72:73], v[208:209]\n\t"
    "ds_read_b128 v[40:43], v148 offset:10240\n\t"
    "s_setprio 0\n\t"
    "s_waitcnt lgkmcnt(5)\n\t"
    "v_smfmac_f32_16x16x64_bf16 v[188:191], v[120:123], v[84:91], v124\n\t"
    "ds_read_b64 v[84:85], v100 offset:64000\n\t"
    "ds_read_b64 v[86:87], v252 offset:64000\n\t"
    "ds_read_b64 v[88:89], v101 offset:64000\n\t"
    "ds_read_b64 v[90:91], v253 offset:64000\n\t"
    "v_lshrrev_b32 v254, 16, v102\n\t"
    "v_and_b32 v102, s92, v102\n\t"
    "v_lshrrev_b32 v255, 16, v103\n\t"
    "v_and_b32 v103, s92, v103\n\t"
    "s_waitcnt vmcnt(7)\n\t"
    "v_readfirstlane_b32 s65, v164\n\t"
    "s_nop 0\n\t"
    "s_cmp_eq_u32 s65, 0\n\t"
    "s_cbranch_scc1 L_xw3_%=\n\t"
    "L_xwb3_%=:\n\t"
    "s_add_u32 s94, s94, 0x2000\n\t"
    "buffer_load_dwordx4 v[52:55], v127, %12, s94 offen sc1\n\t"
    "v_pk_mul_f32 v[74:75], v[74:75], v[210:211]\n\t"
    "v_exp_f32 v76, v72\n\t"
    "v_exp_f32 v77, v73\n\t"
    "v_exp_f32 v78, v74\n\t"
    "v_exp_f32 v79, v75\n\t"
    "ds_read_b128 v[44:47], v148 offset:11264\n\t"
    "s_waitcnt lgkmcnt(6)\n\t"
    "v_smfmac_f32_16x16x64_bf16 v[188:191], v[120:123], v[92:99], v124\n\t"
    "ds_read_b64 v[92:93], v102 offset:64000\n\t"
    "ds_read_b64 v[94:95], v254 offset:64000\n\t"
    "ds_read_b64 v[96:97], v103 offset:64000\n\t"
    "ds_read_b64 v[98:99], v255 offset:64000\n\t"
    "v_lshrrev_b32 v252, 16, v104\n\t"
    "v_and_b32 v104, s92, v104\n\t"
    "v_lshrrev_b32 v253, 16, v105\n\t"
    "v_and_b32 v105, s92, v105\n\t"
    "v_pk_fma_f32 v[76:77], v[76:77], s[40:41], s[40:41]\n\t"
    "v_pk_fma_f32 v[78:79], v[78:79], s[42:43], s[42:43]\n\t"
    "v_rcp_f32 v76, v76\n\t"
    "v_rcp_f32 v77, v77\n\t"
    "v_rcp_f32 v78, v78\n\t"
    "s_waitcnt lgkmcnt(5)\n\t"
    "v_smfmac_f32_16x16x64_bf16 v[172:175], v[120:123], v[84:91], v124\n\t"
    "ds_read_b64 v[84:85], v104 offset:0\n\t"
    "ds_read_b64 v[86:87], v252 offset:0\n\t"
    "ds_read_b64 v[88:89], v105 offset:0\n\t"
    "ds_read_b64 v[90:91], v253 offset:0\n\t"
    "v_lshrrev_b32 v254, 16, v106\n\t"
    "v_and_b32 v106, s92, v106\n\t"
    "v_lshrrev_b32 v255, 16, v107\n\t"
    "v_and_b32 v107, s92, v107\n\t"
    "v_rcp_f32 v79, v79\n\t"
    "v_pk_mul_f32 v[76:77], v[76:77], v[208:209]\n\t"
    "v_pk_fma_f32 v[76:77], v[78:79], v[210:211], v[76:77]\n\t"
    "v_add_f32 v245, v76, v77\n\t"
    "s_waitcnt lgkmcnt(4)\n\t"
    "v_smfmac_f32_16x16x64_bf16 v[172:175], v[120:123], v[92:99], v124\n\t"
    "ds_read_b64 v[92:93], v106 offset:0\n\t"
    "ds_read_b64 v[94:95], v254 offset:0\n\t"
    "ds_read_b64 v[96:97], v107 offset:0\n\t"
    "ds_read_b64 v[98:99], v255 offset:0\n\t"
    "v_lshrrev_b32 v252, 16, v108\n\t"
    "v_and_b32 v108, s92, v108\n\t"
    "v_lshrrev_b32 v253, 16, v109\n\t"
    "v_mov_b64 v[192:193], s[70:71]\n\t"
    "v_mov_b64 v[194:195], s[72:73]\n\t"
    "v_and_b32 v109, s92, v109\n\t"
    "v_pk_mul_f32 v[64:65], v[172:173], v[172:173]\n\t"
    "v_pk_mul_f32 v[66:67], v[174:175], v[174:175]\n\t"
    "v_pk_fma_f32 v[64:65], v[64:65], v[82:83], v[80:81]\n\t"
    "v_pk_fma_f32 v[66:67], v[66:67], v[82:83], v[80:81]\n\t"
    "v_pk_mul_f32 v[64:65], v[64:65], v[172:173]\n\t"
    "s_waitcnt lgkmcnt(4)\n\t"
    "v_smfmac_f32_16x16x64_bf16 v[192:195], v[120:123], v[84:91], v124\n\t"
    "ds_read_b64 v[84:85], v108 offset:64000\n\t"
    "ds_read_b64 v[86:87], v252 offset:64000\n\t"
    "ds_read_b64 v[88:89], v109 offset:64000\n\t"
    "ds_read_b64 v[90:91], v253 offset:64000\n\t"
    "v_lshrrev_b32 v254, 16, v110\n\t"
    "v_and_b32 v110, s92, v110\n\t"
    "v_lshrrev_b32 v255, 16, v111\n\t"
    "v_and_b32 v111, s92, v111\n\t"
    "s_add_u32 s94, s94, 0x2000\n\t"
    "buffer_load_dwordx4 v[60:63], v127, %12, s94 offen sc1\n\t"
    "v_pk_mul_f32 v[66:67], v[66:67], v[174:175]\n\t"
    "v_exp_f32 v68, v64\n\t"
    "v_exp_f32 v69, v65\n\t"
    "v_exp_f32 v70, v66\n\t"
    "v_exp_f32 v71, v67\n\t"
    "v_lshrrev_b32 v166, 7, v127\n\t"
    "v_and_b32 v167, 0x70, v127\n\t"
    "v_lshl_add_u32 v126, v166, 8, v167\n\t"
    "v_add_u32 v126, 128, v126\n\t"
    "s_bfe_u32 s46, %14, 0x30003\n\t"
    "s_min_u32 s46, s46, 6\n\t"
    "s_lshl_b32 s69, s46, 3\n\t"
    "s_add_u32 s69, s69, %16\n\t"
    "s_waitcnt lgkmcnt(4)\n\t"
    "v_smfmac_f32_16x16x64_bf16 v[192:195], v[120:123], v[92:99], v124\n\t"
    "ds_read_b64 v[92:93], v110 offset:64000\n\t"
    "ds_read_b64 v[94:95], v254 offset:64000\n\t"
    "ds_read_b64 v[96:97], v111 offset:64000\n\t"
    "ds_read_b64 v[98:99], v255 offset:64000\n\t"
    "v_lshrrev_b32 v252, 16, v248\n\t"
    "v_and_b32 v248, s92, v248\n\t"
    "v_lshrrev_b32 v253, 16, v249\n\t"
    "v_and_b32 v249, s92, v249\n\t"
    "v_pk_fma_f32 v[68:69], v[68:69], s[40:41], s[40:41]\n\t"
    "v_pk_fma_f32 v[70:71], v[70:71], s[42:43], s[42:43]\n\t"
    "v_rcp_f32 v68, v68\n\t"
    "v_rcp_f32 v69, v69\n\t"
    "v_rcp_f32 v70, v70\n\t"
    "s_barrier\n\t"
    "s_waitcnt lgkmcnt(4)\n\t"
    "v_smfmac_f32_16x16x64_bf16 v[176:179], v[120:123], v[84:91], v124\n\t"
    "ds_read_b64 v[84:85], v248 offset:64000\n\t"
    "ds_read_b64 v[86:87], v252 offset:64000\n\t"
    "ds_read_b64 v[88:89], v249 offset:64000\n\t"
    "ds_read_b64 v[90:91], v253 offset:64000\n\t"
    "v_lshrrev_b32 v254, 16, v250\n\t"
    "v_and_b32 v250, s92, v250\n\t"
    "v_lshrrev_b32 v255, 16, v251\n\t"
    "v_and_b32 v251, s92, v251\n\t"
    "v_rcp_f32 v71, v71\n\t"
    "v_pk_mul_f32 v[68:69], v[68:69], v[172:173]\n\t"
    "v_pk_fma_f32 v[68:69], v[70:71], v[174:175], v[68:69]\n\t"
    "v_add_f32 v246, v68, v69\n\t"
    "s_mul_i32 s44, s69, 1040\n\t"
    "s_add_u32 m0, s44, 0\n\t"
    "s_lshl_b32 s44, s69, 11\n\t"
    "buffer_load_dwordx4 v126, %15, s44 offen lds\n\t"
    "s_add_u32 s69, s69, 8\n\t"
    "s_sub_u32 s44, s69, 61\n\t"
    "s_cmp_ge_u32 s69, 61\n\t"
    "s_cselect_b32 s69, s44, s69\n\t"
    "s_waitcnt vmcnt(9) lgkmcnt(4)\n\t"
    "v_smfmac_f32_16x16x64_bf16 v[176:179], v[120:123], v[92:99], v124\n\t"
    "ds_read_b64 v[92:93], v250 offset:64000\n\t"
    "ds_read_b64 v[94:95], v254 offset:64000\n\t"
    "ds_read_b64 v[96:97], v251 offset:64000\n\t"
    "ds_read_b64 v[98:99], v255 offset:64000\n\t"
    "v_lshrrev_b32 v252, 16, v212\n\t"
    "v_and_b32 v212, s92, v212\n\t"
    "v_lshrrev_b32 v253, 16, v213\n\t"
    "v_and_b32 v213, s92, v213\n\t"
    "s_mul_i32 s44, s69, 1040\n\t"
    "s_add_u32 m0, s44, 0\n\t"
    "s_lshl_b32 s44, s69, 11\n\t"
    "buffer_load_dwordx4 v126, %15, s44 offen lds\n\t"
    "s_add_u32 s69, s69, 8\n\t"
    "s_sub_u32 s44, s69, 61\n\t"
    "s_cmp_ge_u32 s69, 61\n\t"
    "s_cselect_b32 s69, s44, s69\n\t"
    "v_lshlrev_b32 v146, 2, v147\n\t"
    "s_lshl_b32 s50, s37, 8\n\t"
    "s_add_u32 s50, s50, 27262976\n\t"
    "s_lshl_b32 s46, s53, 8\n\t"
    "s_add_u32 s46, s46, s50\n\t"
    "buffer_load_dword v144, v146, %12, s46 offen sc1\n\t"
    "buffer_load_dword v145, v146, %12, s46 offen offset:512 sc1\n\t"
    "v_mov_b32 v149, s49\n\t"
    "s_add_u32 s46, s51, 6\n\t"
    "v_mov_b32 v155, s46\n\t"
    "ds_write_b32 v149, v155\n\t"
    "s_mov_b32 s61, 0\n\t"
    "v_mov_b32 v149, 1024\n\t"
    "v_mov_b32 v150, 0\n\t"
    "ds_write_b32 v149, v150\n\t"
    "s_mov_b32 s45, 1\n\t"
    "s_waitcnt lgkmcnt(6)\n\t"
    "v_smfmac_f32_16x16x64_bf16 v[180:183], v[120:123], v[84:91], v124\n\t"
    "ds_read_b64 v[84:85], v212 offset:64000\n\t"
    "ds_read_b64 v[86:87], v252 offset:64000\n\t"
    "ds_read_b64 v[88:89], v213 offset:64000\n\t"
    "ds_read_b64 v[90:91], v253 offset:64000\n\t"
    "v_lshrrev_b32 v254, 16, v214\n\t"
    "v_and_b32 v214, s92, v214\n\t"
    "v_lshrrev_b32 v255, 16, v215\n\t"
    "v_and_b32 v215, s92, v215\n\t"
    "s_waitcnt vmcnt(5)\n\t"
    "v_smfmac_f32_16x16x64_bf16 v[180:183], v[156:159], v[48:55], v124\n\t"
    "v_pk_mul_f32 v[72:73], v[176:177], v[176:177]\n\t"
    "v_pk_mul_f32 v[74:75], v[178:179], v[178:179]\n\t"
    "v_pk_fma_f32 v[72:73], v[72:73], v[82:83], v[80:81]\n\t"
    "v_pk_fma_f32 v[74:75], v[74:75], v[82:83], v[80:81]\n\t"
    "v_pk_mul_f32 v[72:73], v[72:73], v[176:177]\n\t"
    "v_pk_mul_f32 v[74:75], v[74:75], v[178:179]\n\t"
    "v_exp_f32 v76, v72\n\t"
    "s_mul_i32 s44, s69, 1040\n\t"
    "s_add_u32 m0, s44, 0\n\t"
    "s_lshl_b32 s44, s69, 11\n\t"
    "buffer_load_dwordx4 v126, %15, s44 offen lds\n\t"
    "s_add_u32 s69, s69, 8\n\t"
    "s_sub_u32 s44, s69, 61\n\t"
    "s_cmp_ge_u32 s69, 61\n\t"
    "s_cselect_b32 s69, s44, s69\n\t"
    "s_waitcnt lgkmcnt(6)\n\t"
    "v_smfmac_f32_16x16x64_bf16 v[180:183], v[120:123], v[92:99], v124\n\t"
    "ds_read_b64 v[92:93], v214 offset:64000\n\t"
    "ds_read_b64 v[94:95], v254 offset:64000\n\t"
    "ds_read_b64 v[96:97], v215 offset:64000\n\t"
    "ds_read_b64 v[98:99], v255 offset:64000\n\t"
    "v_lshrrev_b32 v252, 16, v216\n\t"
    "v_and_b32 v216, s92, v216\n\t"
    "v_lshrrev_b32 v253, 16, v217\n\t"
    "v_and_b32 v217, s92, v217\n\t"
    "v_smfmac_f32_16x16x64_bf16 v[184:187], v[160:163], v[48:55], v124\n\t"
    "v_exp_f32 v77, v73\n\t"
    "v_exp_f32 v78, v74\n\t"
    "v_exp_f32 v79, v75\n\t"
    "v_pk_fma_f32 v[76:77], v[76:77], s[40:41], s[40:41]\n\t"
    "v_pk_fma_f32 v[78:79], v[78:79], s[42:43], s[42:43]\n\t"
    "v_rcp_f32 v76, v76\n\t"
    "v_rcp_f32 v77, v77\n\t"
    "s_mul_i32 s44, s69, 1040\n\t"
    "s_add_u32 m0, s44, 0\n\t"
    "s_lshl_b32 s44, s69, 11\n\t"
    "buffer_load_dwordx4 v126, %15, s44 offen lds\n\t"
    "s_add_u32 s69, s69, 8\n\t"
    "s_sub_u32 s44, s69, 61\n\t"
    "s_cmp_ge_u32 s69, 61\n\t"
    "s_cselect_b32 s69, s44, s69\n\t"
    "ds_read_b128 v[48:51], v148 offset:12288\n\t"
    "s_waitcnt lgkmcnt(5)\n\t"
    "v_smfmac_f32_16x16x64_bf16 v[184:187], v[120:123], v[84:91], v124\n\t"
    "ds_read_b64 v[84:85], v216 offset:64000\n\t"
    "ds_read_b64 v[86:87], v252 offset:64000\n\t"
    "ds_read_b64 v[88:89], v217 offset:64000\n\t"
    "ds_read_b64 v[90:91], v253 offset:64000\n\t"
    "v_lshrrev_b32 v254, 16, v218\n\t"
    "v_and_b32 v218, s92, v218\n\t"
    "v_lshrrev_b32 v255, 16, v219\n\t"
    "v_and_b32 v219, s92, v219\n\t"
    "v_rcp_f32 v78, v78\n\t"
    "v_rcp_f32 v79, v79\n\t"
    "v_pk_mul_f32 v[76:77], v[76:77], v[176:177]\n\t"
    "v_pk_fma_f32 v[76:77], v[78:79], v[178:179], v[76:77]\n\t"
    "v_add_f32 v247, v76, v77\n\t"
    "v_pk_mul_f32 v[64:65], v[180:181], v[180:181]\n\t"
    "v_pk_mul_f32 v[66:67], v[182:183], v[182:183]\n\t"
    "s_mul_i32 s44, s69, 1040\n\t"
    "s_add_u32 m0, s44, 0\n\t"
    "s_lshl_b32 s44, s69, 11\n\t"
    "buffer_load_dwordx4 v126, %15, s44 offen lds\n\t"
    "s_add_u32 s69, s69, 8\n\t"
    "s_sub_u32 s44, s69, 61\n\t"
    "s_cmp_ge_u32 s69, 61\n\t"
    "s_cselect_b32 s69, s44, s69\n\t"
    "ds_read_b128 v[52:55], v148 offset:13312\n\t"
    "s_waitcnt lgkmcnt(6)\n\t"
    "v_smfmac_f32_16x16x64_bf16 v[184:187], v[120:123], v[92:99], v124\n\t"
    "ds_read_b64 v[92:93], v218 offset:64000\n\t"
    "ds_read_b64 v[94:95], v254 offset:64000\n\t"
    "ds_read_b64 v[96:97], v219 offset:64000\n\t"
    "ds_read_b64 v[98:99], v255 offset:64000\n\t"
    "v_lshrrev_b32 v252, 16, v220\n\t"
    "v_and_b32 v220, s92, v220\n\t"
    "v_lshrrev_b32 v253, 16, v221\n\t"
    "v_and_b32 v221, s92, v221\n\t"
    "v_pk_fma_f32 v[64:65], v[64:65], v[82:83], v[80:81]\n\t"
    "v_pk_fma_f32 v[66:67], v[66:67], v[82:83], v[80:81]\n\t"
    "v_pk_mul_f32 v[64:65], v[64:65], v[180:181]\n\t"
    "v_pk_mul_f32 v[66:67], v[66:67], v[182:183]\n\t"
    "v_exp_f32 v68, v64\n\t"
    "v_exp_f32 v69, v65\n\t"
    "v_exp_f32 v70, v66\n\t"
    "s_mul_i32 s44, s69, 1040\n\t"
    "s_add_u32 m0, s44, 0\n\t"
    "s_lshl_b32 s44, s69, 11\n\t"
    "buffer_load_dwordx4 v126, %15, s44 offen lds\n\t"
    "s_add_u32 s69, s69, 8\n\t"
    "s_sub_u32 s44, s69, 61\n\t"
    "s_cmp_ge_u32 s69, 61\n\t"
    "s_cselect_b32 s69, s44, s69\n\t"
    "s_waitcnt lgkmcnt(5)\n\t"
    "v_smfmac_f32_16x16x64_bf16 v[188:191], v[120:123], v[84:91], v124\n\t"
    "ds_read_b64 v[84:85], v220 offset:64000\n\t"
    "ds_read_b64 v[86:87], v252 offset:64000\n\t"
    "ds_read_b64 v[88:89], v221 offset:64000\n\t"
    "ds_read_b64 v[90:91], v253 offset:64000\n\t"
    "v_lshrrev_b32 v254, 16, v222\n\t"
    "v_and_b32 v222, s92, v222\n\t"
    "v_lshrrev_b32 v255, 16, v223\n\t"
    "v_and_b32 v223, s92, v223\n\t"
    "s_waitcnt vmcnt(8)\n\t"
    "v_smfmac_f32_16x16x64_bf16 v[188:191], v[156:159], v[56:63], v124\n\t"
    "v_exp_f32 v71, v67\n\t"
    "v_pk_fma_f32 v[68:69], v[68:69], s[40:41], s[40:41]\n\t"
    "v_pk_fma_f32 v[70:71], v[70:71], s[42:43], s[42:43]\n\t"
    "v_rcp_f32 v68, v68\n\t"
    "v_rcp_f32 v69, v69\n\t"
    "v_rcp_f32 v70, v70\n\t"
    "v_rcp_f32 v71, v71\n\t"
    "s_mul_i32 s44, s69, 1040\n\t"
    "s_add_u32 m0, s44, 0\n\t"
    "s_lshl_b32 s44, s69, 11\n\t"
    "buffer_load_dwordx4 v126, %15, s44 offen lds\n\t"
    "s_add_u32 s69, s69, 8\n\t"
    "s_sub_u32 s44, s69, 61\n\t"
    "s_cmp_ge_u32 s69, 61\n\t"
    "s_cselect_b32 s69, s44, s69\n\t"
    "s_waitcnt lgkmcnt(4)\n\t"
    "v_smfmac_f32_16x16x64_bf16 v[188:191], v[120:123], v[92:99], v124\n\t"
    "ds_read_b64 v[92:93], v222 offset:64000\n\t"
    "ds_read_b64 v[94:95], v254 offset:64000\n\t"
    "ds_read_b64 v[96:97], v223 offset:64000\n\t"
    "ds_read_b64 v[98:99], v255 offset:64000\n\t"
    "v_smfmac_f32_16x16x64_bf16 v[192:195], v[160:163], v[56:63], v124\n\t"
    "v_pk_mul_f32 v[68:69], v[68:69], v[180:181]\n\t"
    "v_pk_fma_f32 v[68:69], v[70:71], v[182:183], v[68:69]\n\t"
    "v_add_f32 v248, v68, v69\n\t"
    "v_pk_mul_f32 v[72:73], v[184:185], v[184:185]\n\t"
    "v_pk_mul_f32 v[74:75], v[186:187], v[186:187]\n\t"
    "v_pk_fma_f32 v[72:73], v[72:73], v[82:83], v[80:81]\n\t"
    "v_pk_fma_f32 v[74:75], v[74:75], v[82:83], v[80:81]\n\t"
    "s_mul_i32 s44, s69, 1040\n\t"
    "s_add_u32 m0, s44, 0\n\t"
    "s_lshl_b32 s44, s69, 11\n\t"
    "buffer_load_dwordx4 v126, %15, s44 offen lds\n\t"
    "s_add_u32 s69, s69, 8\n\t"
    "s_sub_u32 s44, s69, 61\n\t"
    "s_cmp_ge_u32 s69, 61\n\t"
    "s_cselect_b32 s69, s44, s69\n\t"
    "ds_read_b128 v[56:59], v148 offset:14336\n\t"
    "s_waitcnt lgkmcnt(5)\n\t"
    "v_smfmac_f32_16x16x64_bf16 v[192:195], v[120:123], v[84:91], v124\n\t"
    "v_pk_mul_f32 v[72:73], v[72:73], v[184:185]\n\t"
    "v_pk_mul_f32 v[74:75], v[74:75], v[186:187]\n\t"
    "v_exp_f32 v76, v72\n\t"
    "v_exp_f32 v77, v73\n\t"
    "v_exp_f32 v78, v74\n\t"
    "v_exp_f32 v79, v75\n\t"
    "v_pk_fma_f32 v[76:77], v[76:77], s[40:41], s[40:41]\n\t"
    "ds_read_b128 v[60:63], v148 offset:15360\n\t"
    "s_waitcnt lgkmcnt(2)\n\t"
    "v_smfmac_f32_16x16x64_bf16 v[192:195], v[120:123], v[92:99], v124\n\t"
    "v_pk_fma_f32 v[78:79], v[78:79], s[42:43], s[42:43]\n\t"
    "v_rcp_f32 v76, v76\n\t"
    "v_rcp_f32 v77, v77\n\t"
    "v_rcp_f32 v78, v78\n\t"
    "v_rcp_f32 v79, v79\n\t"
    "v_pk_mul_f32 v[76:77], v[76:77], v[184:185]\n\t"
    "v_pk_fma_f32 v[76:77], v[78:79], v[186:187], v[76:77]\n\t"
    "s_barrier\n\t"
    "v_add_u32 v126, 124928, v126\n\t"
    "s_bfe_u32 s46, %14, 0x30003\n\t"
    "s_min_u32 s46, s46, 6\n\t"
    "s_lshl_b32 s69, s46, 3\n\t"
    "s_add_u32 s69, s69, %16\n\t"
    "s_mul_i32 s44, s69, 1040\n\t"
    "s_add_u32 m0, s44, 63440\n\t"
    "s_lshl_b32 s44, s69, 11\n\t"
    "buffer_load_dwordx4 v126, %15, s44 offen lds\n\t"
    "s_add_u32 s69, s69, 8\n\t"
    "s_sub_u32 s44, s69, 64\n\t"
    "s_cmp_ge_u32 s69, 64\n\t"
    "s_cselect_b32 s69, s44, s69\n\t"
    "s_mul_i32 s44, s69, 1040\n\t"
    "s_add_u32 m0, s44, 63440\n\t"
    "s_lshl_b32 s44, s69, 11\n\t"
    "buffer_load_dwordx4 v126, %15, s44 offen lds\n\t"
    "s_add_u32 s69, s69, 8\n\t"
    "s_sub_u32 s44, s69, 64\n\t"
    "s_cmp_ge_u32 s69, 64\n\t"
    "s_cselect_b32 s69, s44, s69\n\t"
    "s_mul_i32 s44, s69, 1040\n\t"
    "s_add_u32 m0, s44, 63440\n\t"
    "s_lshl_b32 s44, s69, 11\n\t"
    "buffer_load_dwordx4 v126, %15, s44 offen lds\n\t"
    "s_add_u32 s69, s69, 8\n\t"
    "s_sub_u32 s44, s69, 64\n\t"
    "s_cmp_ge_u32 s69, 64\n\t"
    "s_cselect_b32 s69, s44, s69\n\t"
    "s_mul_i32 s44, s69, 1040\n\t"
    "s_add_u32 m0, s44, 63440\n\t"
    "s_lshl_b32 s44, s69, 11\n\t"
    "buffer_load_dwordx4 v126, %15, s44 offen lds\n\t"
    "s_add_u32 s69, s69, 8\n\t"
    "s_sub_u32 s44, s69, 64\n\t"
    "s_cmp_ge_u32 s69, 64\n\t"
    "s_cselect_b32 s69, s44, s69\n\t"
    "s_mul_i32 s44, s69, 1040\n\t"
    "s_add_u32 m0, s44, 63440\n\t"
    "s_lshl_b32 s44, s69, 11\n\t"
    "buffer_load_dwordx4 v126, %15, s44 offen lds\n\t"
    "s_add_u32 s69, s69, 8\n\t"
    "s_sub_u32 s44, s69, 64\n\t"
    "s_cmp_ge_u32 s69, 64\n\t"
    "s_cselect_b32 s69, s44, s69\n\t"
    "s_mul_i32 s44, s69, 1040\n\t"
    "s_add_u32 m0, s44, 63440\n\t"
    "s_lshl_b32 s44, s69, 11\n\t"
    "buffer_load_dwordx4 v126, %15, s44 offen lds\n\t"
    "s_add_u32 s69, s69, 8\n\t"
    "s_sub_u32 s44, s69, 64\n\t"
    "s_cmp_ge_u32 s69, 64\n\t"
    "s_cselect_b32 s69, s44, s69\n\t"
    "s_mul_i32 s44, s69, 1040\n\t"
    "s_add_u32 m0, s44, 63440\n\t"
    "s_lshl_b32 s44, s69, 11\n\t"
    "buffer_load_dwordx4 v126, %15, s44 offen lds\n\t"
    "s_add_u32 s69, s69, 8\n\t"
    "s_sub_u32 s44, s69, 64\n\t"
    "s_cmp_ge_u32 s69, 64\n\t"
    "s_cselect_b32 s69, s44, s69\n\t"
    "s_mul_i32 s44, s69, 1040\n\t"
    "s_add_u32 m0, s44, 63440\n\t"
    "s_lshl_b32 s44, s69, 11\n\t"
    "buffer_load_dwordx4 v126, %15, s44 offen lds\n\t"
    "s_add_u32 s69, s69, 8\n\t"
    "s_sub_u32 s44, s69, 64\n\t"
    "s_cmp_ge_u32 s69, 64\n\t"
    "s_cselect_b32 s69, s44, s69\n\t"
    "ds_read_b128 v[84:87], v148 offset:21504\n\t"
    "ds_read_b128 v[88:91], v148 offset:22528\n\t"
    "v_add_f32 v249, v76, v77\n\t"
    "v_pk_mul_f32 v[64:65], v[188:189], v[188:189]\n\t"
    "v_pk_mul_f32 v[66:67], v[190:191], v[190:191]\n\t"
    "v_pk_fma_f32 v[64:65], v[64:65], v[82:83], v[80:81]\n\t"
    "v_pk_fma_f32 v[66:67], v[66:67], v[82:83], v[80:81]\n\t"
    "v_pk_mul_f32 v[64:65], v[64:65], v[188:189]\n\t"
    "v_pk_mul_f32 v[66:67], v[66:67], v[190:191]\n\t"
    "v_exp_f32 v68, v64\n\t"
    "v_exp_f32 v69, v65\n\t"
    "v_exp_f32 v70, v66\n\t"
    "v_exp_f32 v71, v67\n\t"
    "v_pk_fma_f32 v[68:69], v[68:69], s[40:41], s[40:41]\n\t"
    "v_pk_fma_f32 v[70:71], v[70:71], s[42:43], s[42:43]\n\t"
    "v_rcp_f32 v68, v68\n\t"
    "v_rcp_f32 v69, v69\n\t"
    "v_rcp_f32 v70, v70\n\t"
    "v_rcp_f32 v71, v71\n\t"
    "v_pk_mul_f32 v[68:69], v[68:69], v[188:189]\n\t"
    "v_pk_fma_f32 v[68:69], v[70:71], v[190:191], v[68:69]\n\t"
    "v_add_f32 v250, v68, v69\n\t"
    "v_pk_mul_f32 v[72:73], v[192:193], v[192:193]\n\t"
    "v_pk_mul_f32 v[74:75], v[194:195], v[194:195]\n\t"
    "v_pk_fma_f32 v[72:73], v[72:73], v[82:83], v[80:81]\n\t"
    "v_pk_fma_f32 v[74:75], v[74:75], v[82:83], v[80:81]\n\t"
    "v_pk_mul_f32 v[72:73], v[72:73], v[192:193]\n\t"
    "v_pk_mul_f32 v[74:75], v[74:75], v[194:195]\n\t"
    "v_exp_f32 v76, v72\n\t"
    "v_exp_f32 v77, v73\n\t"
    "v_exp_f32 v78, v74\n\t"
    "v_exp_f32 v79, v75\n\t"
    "v_pk_fma_f32 v[76:77], v[76:77], s[40:41], s[40:41]\n\t"
    "v_pk_fma_f32 v[78:79], v[78:79], s[42:43], s[42:43]\n\t"
    "v_rcp_f32 v76, v76\n\t"
    "v_rcp_f32 v77, v77\n\t"
    "v_rcp_f32 v78, v78\n\t"
    "v_rcp_f32 v79, v79\n\t"
    "v_pk_mul_f32 v[76:77], v[76:77], v[192:193]\n\t"
    "v_pk_fma_f32 v[76:77], v[78:79], v[194:195], v[76:77]\n\t"
    "v_add_f32 v251, v76, v77\n\t"
    "ds_read_b128 v[64:67], v148 offset:16384\n\t"
    "ds_read_b128 v[68:71], v148 offset:17408\n\t"
    "ds_read_b128 v[72:75], v148 offset:18432\n\t"
    "ds_read_b128 v[76:79], v148 offset:19456\n\t"
    "ds_read_b128 v[80:83], v148 offset:20480\n\t"
    "s_waitcnt vmcnt(0) lgkmcnt(0)\n\t"
    "s_mov_b32 s68, 130\n\t"
    "v_mad_u32_u24 v128, v128, s68, v152\n\t"
    "v_and_b32 v128, -16, v128\n\t"
    "v_mad_u32_u24 v129, v129, s68, v151\n\t"
    "v_and_b32 v129, -16, v129\n\t"
    "v_mad_u32_u24 v130, v130, s68, v151\n\t"
    "v_and_b32 v130, -16, v130\n\t"
    "v_mad_u32_u24 v132, v132, s68, v151\n\t"
    "v_and_b32 v132, -16, v132\n\t"
    "v_mad_u32_u24 v133, v133, s68, v151\n\t"
    "v_and_b32 v133, -16, v133\n\t"
    "v_mad_u32_u24 v134, v134, s68, v151\n\t"
    "v_and_b32 v134, -16, v134\n\t"
    "v_mad_u32_u24 v135, v135, s68, v151\n\t"
    "v_and_b32 v135, -16, v135\n\t"
    "v_lshrrev_b32 v122, 4, v147\n\t"
    "v_and_b32 v155, 15, v147\n\t"
    "v_lshlrev_b32 v155, 4, v155\n\t"
    "v_mul_u32_u24 v122, 65536, v122\n\t"
    "v_add_u32 v122, v122, v155\n\t"
    "v_mov_b32 v150, 1\n\t"
    "v_mov_b32 v123, 0\n\t"
    "v_lshlrev_b32 v149, 20, v147\n\t"
    "v_add_u32 v149, s49, v149\n\t"
    "s_mov_b32 s52, s53\n\t"
    "s_cmp_ge_u32 %16, 4\n\t"
    "s_cbranch_scc1 L_st4_%=\n\t"
    "s_bfe_u32 s65, s84, 0xc0002\n\t"
    "s_add_u32 s65, s65, s36\n\t"
    "buffer_store_dword v236, v125, %12, s65 offen offset:0 sc1\n\t"
    "buffer_store_dword v237, v125, %12, s65 offen offset:256 sc1\n\t"
    "s_bfe_u32 s65, s85, 0xc0002\n\t"
    "s_add_u32 s65, s65, s36\n\t"
    "buffer_store_dword v238, v125, %12, s65 offen offset:0 sc1\n\t"
    "buffer_store_dword v239, v125, %12, s65 offen offset:256 sc1\n\t"
    "s_bfe_u32 s65, s86, 0xc0002\n\t"
    "s_add_u32 s65, s65, s36\n\t"
    "buffer_store_dword v240, v125, %12, s65 offen offset:0 sc1\n\t"
    "buffer_store_dword v241, v125, %12, s65 offen offset:256 sc1\n\t"
    "s_bfe_u32 s65, s87, 0xc0002\n\t"
    "s_add_u32 s65, s65, s36\n\t"
    "buffer_store_dword v242, v125, %12, s65 offen offset:0 sc1\n\t"
    "buffer_store_dword v243, v125, %12, s65 offen offset:256 sc1\n\t"
    "s_bfe_u32 s65, s88, 0xc0002\n\t"
    "s_add_u32 s65, s65, s36\n\t"
    "buffer_store_dword v244, v125, %12, s65 offen offset:0 sc1\n\t"
    "buffer_store_dword v245, v125, %12, s65 offen offset:256 sc1\n\t"
    "s_bfe_u32 s65, s89, 0xc0002\n\t"
    "s_add_u32 s65, s65, s36\n\t"
    "buffer_store_dword v246, v125, %12, s65 offen offset:0 sc1\n\t"
    "buffer_store_dword v247, v125, %12, s65 offen offset:256 sc1\n\t"
    "s_bfe_u32 s65, s90, 0xc0002\n\t"
    "s_add_u32 s65, s65, s36\n\t"
    "buffer_store_dword v248, v125, %12, s65 offen offset:0 sc1\n\t"
    "buffer_store_dword v249, v125, %12, s65 offen offset:256 sc1\n\t"
    "s_bfe_u32 s65, s91, 0xc0002\n\t"
    "s_add_u32 s65, s65, s36\n\t"
    "buffer_store_dword v250, v125, %12, s65 offen offset:0 sc1\n\t"
    "buffer_store_dword v251, v125, %12, s65 offen offset:256 sc1\n\t"
    "s_sub_u32 s61, s64, 3\n\t"
    "L_st4_%=:\n\t"
    "s_barrier\n\t"
    "s_mov_b32 s68, 130\n\t"
    "ds_read_b128 v[156:159], v128 offset:0\n\t"
    "ds_read_b128 v[160:163], v128 offset:64\n\t"
    "ds_read_b128 v[164:167], v129 offset:0\n\t"
    "ds_read_b128 v[168:171], v129 offset:64\n\t"
    "ds_read_b128 v[172:175], v130 offset:0\n\t"
    "ds_read_b128 v[176:179], v130 offset:64\n\t"
    "ds_read_b128 v[180:183], v132 offset:0\n\t"
    "ds_read_b128 v[184:187], v132 offset:64\n\t"
    "ds_read_b128 v[188:191], v133 offset:0\n\t"
    "ds_read_b128 v[192:195], v133 offset:64\n\t"
    "ds_read_b128 v[196:199], v134 offset:0\n\t"
    "ds_read_b128 v[200:203], v134 offset:64\n\t"
    "ds_read_b128 v[204:207], v135 offset:0\n\t"
    "ds_read_b128 v[208:211], v135 offset:64\n\t"
    "s_setprio 1\n\t"
    "L_pnn_%=:\n\t"
    "s_mul_i32 s69, s52, 272\n\t"
    "s_add_u32 s69, s69, 130048\n\t"
    "s_mul_i32 s66, s55, 400\n\t"
    "s_waitcnt lgkmcnt(8)\n\t"
    "v_mfma_f32_16x16x32_bf16 v[92:95], v[156:159], v[156:159], 0\n\t"
    "v_add_u32 v147, s69, v146\n\t"
    "v_fmac_f32 v121, v108, v72\n\t"
    "ds_add_rtn_u32 v148, v149, v150\n\t"
    "v_mfma_f32_16x16x32_bf16 v[92:95], v[160:163], v[160:163], v[92:95]\n\t"
    "v_fmac_f32 v120, v109, v73\n\t"
    "v_fmac_f32 v121, v110, v74\n\t"
    "buffer_load_dwordx3 v[128:130], v153, s[96:99], s66 offen\n\t"
    "v_mfma_f32_16x16x32_bf16 v[96:99], v[164:167], v[164:167], 0\n\t"
    "v_fmac_f32 v120, v111, v75\n\t"
    "v_fmac_f32 v121, v112, v76\n\t"
    "v_mfma_f32_16x16x32_bf16 v[96:99], v[168:171], v[168:171], v[96:99]\n\t"
    "v_fmac_f32 v120, v113, v77\n\t"
    "v_fmac_f32 v121, v114, v78\n\t"
    "buffer_load_dwordx4 v[132:135], v153, s[96:99], s66 offen offset:12\n\t"
    "v_mfma_f32_16x16x32_bf16 v[100:103], v[164:167], v[172:175], 0\n\t"
    "v_fmac_f32 v120, v115, v79\n\t"
    "v_fmac_f32 v121, v116, v80\n\t"
    "v_mfma_f32_16x16x32_bf16 v[100:103], v[168:171], v[176:179], v[100:103]\n\t"
    "v_fmac_f32 v120, v117, v81\n\t"
    "v_fmac_f32 v121, v118, v82\n\t"
    "s_waitcnt vmcnt(3)\n\t"
    "s_cmp_ge_u32 s55, s61\n\t"
    "s_cbranch_scc1 L_req0_%=\n\t"
    "L_back0_%=:\n\t"
    "v_mfma_f32_16x16x32_bf16 v[104:107], v[172:175], v[172:175], 0\n\t"
    "v_mad_u32_u24 v136, v136, s68, v152\n\t"
    "v_and_b32 v136, -16, v136\n\t"
    "v_mfma_f32_16x16x32_bf16 v[104:107], v[176:179], v[176:179], v[104:107]\n\t"
    "v_fmac_f32 v120, v119, v83\n\t"
    "v_add_f32 v154, v120, v121\n\t"
    "ds_write_b32 v147, v154\n\t"
    "v_mfma_f32_16x16x32_bf16 v[108:111], v[156:159], v[164:167], 0\n\t"
    "v_fma_f32 v120, v92, v84, v144\n\t"
    "v_mul_f32 v121, v96, v0\n\t"
    "v_mfma_f32_16x16x32_bf16 v[108:111], v[160:163], v[168:171], v[108:111]\n\t"
    "v_fmac_f32 v120, v97, v1\n\t"
    "v_fmac_f32 v121, v98, v2\n\t"
    "s_waitcnt lgkmcnt(8)\n\t"
    "v_mfma_f32_16x16x32_bf16 v[112:115], v[164:167], v[180:183], 0\n\t"
    "v_fmac_f32 v120, v99, v3\n\t"
    "v_fmac_f32 v121, v100, v4\n\t"
    "v_mfma_f32_16x16x32_bf16 v[112:115], v[168:171], v[184:187], v[112:115]\n\t"
    "v_fmac_f32 v120, v101, v5\n\t"
    "v_fmac_f32 v121, v102, v6\n\t"
    "v_mfma_f32_16x16x32_bf16 v[116:119], v[172:175], v[180:183], 0\n\t"
    "v_fmac_f32 v120, v103, v7\n\t"
    "v_fmac_f32 v121, v104, v8\n\t"
    "v_mfma_f32_16x16x32_bf16 v[116:119], v[176:179], v[184:187], v[116:119]\n\t"
    "v_fmac_f32 v120, v105, v9\n\t"
    "v_fmac_f32 v121, v106, v10\n\t"
    "ds_read_b128 v[212:215], v136 offset:0\n\t"
    "ds_read_b128 v[216:219], v136 offset:64\n\t"
    "v_mfma_f32_16x16x32_bf16 v[92:95], v[180:183], v[180:183], 0\n\t"
    "v_fmac_f32 v120, v107, v11\n\t"
    "v_mad_u32_u24 v137, v137, s68, v151\n\t"
    "v_mfma_f32_16x16x32_bf16 v[92:95], v[184:187], v[184:187], v[92:95]\n\t"
    "v_and_b32 v137, -16, v137\n\t"
    "v_fmac_f32 v121, v108, v85\n\t"
    "v_mfma_f32_16x16x32_bf16 v[96:99], v[156:159], v[172:175], 0\n\t"
    "v_fmac_f32 v120, v112, v12\n\t"
    "v_fmac_f32 v121, v113, v13\n\t"
    "v_mfma_f32_16x16x32_bf16 v[96:99], v[160:163], v[176:179], v[96:99]\n\t"
    "v_fmac_f32 v120, v114, v14\n\t"
    "v_fmac_f32 v121, v115, v15\n\t"
    "s_lshl_b32 s51, s55, 8\n\t"
    "s_add_u32 s51, s51, s50\n\t"
    "s_waitcnt lgkmcnt(8)\n\t"
    "v_mfma_f32_16x16x32_bf16 v[100:103], v[164:167], v[188:191], 0\n\t"
    "v_mad_u32_u24 v138, v138, s68, v151\n\t"
    "v_and_b32 v138, -16, v138\n\t"
    "v_mfma_f32_16x16x32_bf16 v[100:103], v[168:171], v[192:195], v[100:103]\n\t"
    "v_fmac_f32 v120, v116, v16\n\t"
    "v_fmac_f32 v121, v117, v17\n\t"
    "buffer_load_dword v144, v146, %12, s51 offen sc1\n\t"
    "v_mfma_f32_16x16x32_bf16 v[104:107], v[172:175], v[188:191], 0\n\t"
    "v_fmac_f32 v120, v118, v18\n\t"
    "v_fmac_f32 v121, v119, v19\n\t"
    "v_mfma_f32_16x16x32_bf16 v[104:107], v[176:179], v[192:195], v[104:107]\n\t"
    "v_fmac_f32 v120, v92, v20\n\t"
    "v_fmac_f32 v121, v93, v21\n\t"
    "s_waitcnt lgkmcnt(6)\n\t"
    "v_mfma_f32_16x16x32_bf16 v[108:111], v[164:167], v[196:199], 0\n\t"
    "v_fmac_f32 v120, v94, v22\n\t"
    "v_fmac_f32 v121, v95, v23\n\t"
    "v_mfma_f32_16x16x32_bf16 v[108:111], v[168:171], v[200:203], v[108:111]\n\t"
    "v_fmac_f32 v120, v96, v86\n\t"
    "v_mad_u32_u24 v140, v140, s68, v151\n\t"
    "v_mfma_f32_16x16x32_bf16 v[112:115], v[156:159], v[180:183], 0\n\t"
    "v_fmac_f32 v121, v100, v24\n\t"
    "v_fmac_f32 v120, v101, v25\n\t"
    "v_mfma_f32_16x16x32_bf16 v[112:115], v[160:163], v[184:187], v[112:115]\n\t"
    "v_fmac_f32 v121, v102, v26\n\t"
    "v_fmac_f32 v120, v103, v27\n\t"
    "s_waitcnt lgkmcnt(3)\n\t"
    "v_mfma_f32_16x16x32_bf16 v[116:119], v[164:167], v[204:207], 0\n\t"
    "v_readfirstlane_b32 s60, v148\n\t"
    "v_and_b32 v140, -16, v140\n\t"
    "v_mfma_f32_16x16x32_bf16 v[116:119], v[168:171], v[208:211], v[116:119]\n\t"
    "v_fmac_f32 v121, v104, v28\n\t"
    "v_fmac_f32 v120, v105, v29\n\t"
    "ds_read_b128 v[164:167], v137 offset:0\n\t"
    "ds_read_b128 v[168:171], v137 offset:64\n\t"
    "v_mfma_f32_16x16x32_bf16 v[92:95], v[172:175], v[196:199], 0\n\t"
    "v_fmac_f32 v121, v106, v30\n\t"
    "v_fmac_f32 v120, v107, v31\n\t"
    "v_mfma_f32_16x16x32_bf16 v[92:95], v[176:179], v[200:203], v[92:95]\n\t"
    "v_fmac_f32 v121, v108, v32\n\t"
    "v_fmac_f32 v120, v109, v33\n\t"
    "v_mfma_f32_16x16x32_bf16 v[96:99], v[172:175], v[204:207], 0\n\t"
    "v_fmac_f32 v121, v110, v34\n\t"
    "v_fmac_f32 v120, v111, v35\n\t"
    "v_mfma_f32_16x16x32_bf16 v[96:99], v[176:179], v[208:211], v[96:99]\n\t"
    "v_fmac_f32 v121, v112, v87\n\t"
    "v_mad_u32_u24 v141, v141, s68, v151\n\t"
    "ds_read_b128 v[172:175], v138 offset:0\n\t"
    "ds_read_b128 v[176:179], v138 offset:64\n\t"
    "v_mfma_f32_16x16x32_bf16 v[100:103], v[156:159], v[188:191], 0\n\t"
    "v_fmac_f32 v120, v116, v36\n\t"
    "v_fmac_f32 v121, v117, v37\n\t"
    "v_mfma_f32_16x16x32_bf16 v[100:103], v[160:163], v[192:195], v[100:103]\n\t"
    "v_fmac_f32 v120, v118, v38\n\t"
    "v_fmac_f32 v121, v119, v39\n\t"
    "v_mfma_f32_16x16x32_bf16 v[104:107], v[180:183], v[188:191], 0\n\t"
    "v_and_b32 v141, -16, v141\n\t"
    "v_fmac_f32 v120, v92, v40\n\t"
    "v_mfma_f32_16x16x32_bf16 v[104:107], v[184:187], v[192:195], v[104:107]\n\t"
    "v_fmac_f32 v121, v93, v41\n\t"
    "v_fmac_f32 v120, v94, v42\n\t"
    "v_mfma_f32_16x16x32_bf16 v[108:111], v[180:183], v[196:199], 0\n\t"
    "v_fmac_f32 v121, v95, v43\n\t"
    "v_fmac_f32 v120, v96, v44\n\t"
    "v_mfma_f32_16x16x32_bf16 v[108:111], v[184:187], v[200:203], v[108:111]\n\t"
    "v_fmac_f32 v121, v97, v45\n\t"
    "v_fmac_f32 v120, v98, v46\n\t"
    "v_mfma_f32_16x16x32_bf16 v[112:115], v[180:183], v[204:207], 0\n\t"
    "v_fmac_f32 v121, v99, v47\n\t"
    "v_fmac_f32 v120, v100, v88\n\t"
    "v_mfma_f32_16x16x32_bf16 v[112:115], v[184:187], v[208:211], v[112:115]\n\t"
    "v_mad_u32_u24 v142, v142, s68, v151\n\t"
    "v_and_b32 v142, -16, v142\n\t"
    "ds_read_b128 v[180:183], v140 offset:0\n\t"
    "ds_read_b128 v[184:187], v140 offset:64\n\t"
    "s_cmp_eq_u32 s45, 1\n\t"
    "s_cbranch_scc1 L_sig_%=\n\t"
    "L_sigb_%=:\n\t"
    "v_mfma_f32_16x16x32_bf16 v[116:119], v[156:159], v[196:199], 0\n\t"
    "v_fmac_f32 v121, v104, v48\n\t"
    "v_fmac_f32 v120, v105, v49\n\t"
    "v_mfma_f32_16x16x32_bf16 v[116:119], v[160:163], v[200:203], v[116:119]\n\t"
    "v_fmac_f32 v121, v106, v50\n\t"
    "v_fmac_f32 v120, v107, v51\n\t"
    "v_mfma_f32_16x16x32_bf16 v[92:95], v[188:191], v[188:191], 0\n\t"
    "v_mad_u32_u24 v143, v143, s68, v151\n\t"
    "v_and_b32 v143, -16, v143\n\t"
    "v_mfma_f32_16x16x32_bf16 v[92:95], v[192:195], v[192:195], v[92:95]\n\t"
    "v_fmac_f32 v121, v108, v52\n\t"
    "v_fmac_f32 v120, v109, v53\n\t"
    "v_mfma_f32_16x16x32_bf16 v[96:99], v[188:191], v[196:199], 0\n\t"
    "v_fmac_f32 v121, v110, v54\n\t"
    "v_fmac_f32 v120, v111, v55\n\t"
    "v_mfma_f32_16x16x32_bf16 v[96:99], v[192:195], v[200:203], v[96:99]\n\t"
    "v_fmac_f32 v121, v112, v56\n\t"
    "v_fmac_f32 v120, v113, v57\n\t"
    "v_mfma_f32_16x16x32_bf16 v[100:103], v[188:191], v[204:207], 0\n\t"
    "v_fmac_f32 v121, v114, v58\n\t"
    "v_fmac_f32 v120, v115, v59\n\t"
    "v_mfma_f32_16x16x32_bf16 v[100:103], v[192:195], v[208:211], v[100:103]\n\t"
    "v_fmac_f32 v121, v116, v89\n\t"
    "ds_read_b128 v[188:191], v141 offset:0\n\t"
    "ds_read_b128 v[192:195], v141 offset:64\n\t"
    "v_mfma_f32_16x16x32_bf16 v[104:107], v[156:159], v[204:207], 0\n\t"
    "v_fmac_f32 v120, v92, v60\n\t"
    "v_fmac_f32 v121, v93, v61\n\t"
    "v_mfma_f32_16x16x32_bf16 v[104:107], v[160:163], v[208:211], v[104:107]\n\t"
    "v_fmac_f32 v120, v94, v62\n\t"
    "v_fmac_f32 v121, v95, v63\n\t"
    "v_mfma_f32_16x16x32_bf16 v[108:111], v[196:199], v[196:199], 0\n\t"
    "v_fmac_f32 v120, v96, v64\n\t"
    "v_fmac_f32 v121, v97, v65\n\t"
    "v_mfma_f32_16x16x32_bf16 v[108:111], v[200:203], v[200:203], v[108:111]\n\t"
    "v_fmac_f32 v120, v98, v66\n\t"
    "v_fmac_f32 v121, v99, v67\n\t"
    "v_mfma_f32_16x16x32_bf16 v[112:115], v[196:199], v[204:207], 0\n\t"
    "v_fmac_f32 v120, v100, v68\n\t"
    "v_fmac_f32 v121, v101, v69\n\t"
    "v_mfma_f32_16x16x32_bf16 v[112:115], v[200:203], v[208:211], v[112:115]\n\t"
    "v_fmac_f32 v120, v102, v70\n\t"
    "v_fmac_f32 v121, v103, v71\n\t"
    "ds_read_b128 v[196:199], v142 offset:0\n\t"
    "ds_read_b128 v[200:203], v142 offset:64\n\t"
    "v_mfma_f32_16x16x32_bf16 v[116:119], v[204:207], v[204:207], 0\n\t"
    "v_fmac_f32 v120, v104, v90\n\t"
    "v_mfma_f32_16x16x32_bf16 v[116:119], v[208:211], v[208:211], v[116:119]\n\t"
    "ds_read_b128 v[204:207], v143 offset:0\n\t"
    "ds_read_b128 v[208:211], v143 offset:64\n\t"
    "s_mov_b32 s52, s53\n\t"
    "s_mov_b32 s53, s54\n\t"
    "s_mov_b32 s54, s55\n\t"
    "s_mov_b32 s55, s60\n\t"
    "s_cmp_lt_u32 s54, s64\n\t"
    "s_cbranch_scc1 L_pr0_%=\n\t"
    "s_setprio 0\n\t"
    "L_pr0_%=:\n\t"
    "s_cmp_lt_u32 s53, s64\n\t"
    "s_cbranch_scc0 L_done_%=\n\t"
    "s_mul_i32 s69, s52, 272\n\t"
    "s_add_u32 s69, s69, 130048\n\t"
    "s_mul_i32 s66, s55, 400\n\t"
    "s_waitcnt lgkmcnt(8)\n\t"
    "v_mfma_f32_16x16x32_bf16 v[92:95], v[212:215], v[212:215], 0\n\t"
    "v_add_u32 v147, s69, v146\n\t"
    "v_fmac_f32 v121, v108, v72\n\t"
    "ds_add_rtn_u32 v148, v149, v150\n\t"
    "v_mfma_f32_16x16x32_bf16 v[92:95], v[216:219], v[216:219], v[92:95]\n\t"
    "v_fmac_f32 v120, v109, v73\n\t"
    "v_fmac_f32 v121, v110, v74\n\t"
    "buffer_load_dwordx3 v[136:138], v153, s[96:99], s66 offen\n\t"
    "v_mfma_f32_16x16x32_bf16 v[96:99], v[164:167], v[164:167], 0\n\t"
    "v_fmac_f32 v120, v111, v75\n\t"
    "v_fmac_f32 v121, v112, v76\n\t"
    "v_mfma_f32_16x16x32_bf16 v[96:99], v[168:171], v[168:171], v[96:99]\n\t"
    "v_fmac_f32 v120, v113, v77\n\t"
    "v_fmac_f32 v121, v114, v78\n\t"
    "buffer_load_dwordx4 v[140:143], v153, s[96:99], s66 offen offset:12\n\t"
    "v_mfma_f32_16x16x32_bf16 v[100:103], v[164:167], v[172:175], 0\n\t"
    "v_fmac_f32 v120, v115, v79\n\t"
    "v_fmac_f32 v121, v116, v80\n\t"
    "v_mfma_f32_16x16x32_bf16 v[100:103], v[168:171], v[176:179], v[100:103]\n\t"
    "v_fmac_f32 v120, v117, v81\n\t"
    "v_fmac_f32 v121, v118, v82\n\t"
    "s_waitcnt vmcnt(3)\n\t"
    "s_cmp_ge_u32 s55, s61\n\t"
    "s_cbranch_scc1 L_req1_%=\n\t"
    "L_back1_%=:\n\t"
    "v_mfma_f32_16x16x32_bf16 v[104:107], v[172:175], v[172:175], 0\n\t"
    "v_mad_u32_u24 v128, v128, s68, v152\n\t"
    "v_and_b32 v128, -16, v128\n\t"
    "v_mfma_f32_16x16x32_bf16 v[104:107], v[176:179], v[176:179], v[104:107]\n\t"
    "v_fmac_f32 v120, v119, v83\n\t"
    "v_add_f32 v154, v120, v121\n\t"
    "ds_write_b32 v147, v154\n\t"
    "v_mfma_f32_16x16x32_bf16 v[108:111], v[212:215], v[164:167], 0\n\t"
    "v_fma_f32 v120, v92, v84, v145\n\t"
    "v_mul_f32 v121, v96, v0\n\t"
    "v_mfma_f32_16x16x32_bf16 v[108:111], v[216:219], v[168:171], v[108:111]\n\t"
    "v_fmac_f32 v120, v97, v1\n\t"
    "v_fmac_f32 v121, v98, v2\n\t"
    "s_waitcnt lgkmcnt(8)\n\t"
    "v_mfma_f32_16x16x32_bf16 v[112:115], v[164:167], v[180:183], 0\n\t"
    "v_fmac_f32 v120, v99, v3\n\t"
    "v_fmac_f32 v121, v100, v4\n\t"
    "v_mfma_f32_16x16x32_bf16 v[112:115], v[168:171], v[184:187], v[112:115]\n\t"
    "v_fmac_f32 v120, v101, v5\n\t"
    "v_fmac_f32 v121, v102, v6\n\t"
    "v_mfma_f32_16x16x32_bf16 v[116:119], v[172:175], v[180:183], 0\n\t"
    "v_fmac_f32 v120, v103, v7\n\t"
    "v_fmac_f32 v121, v104, v8\n\t"
    "v_mfma_f32_16x16x32_bf16 v[116:119], v[176:179], v[184:187], v[116:119]\n\t"
    "v_fmac_f32 v120, v105, v9\n\t"
    "v_fmac_f32 v121, v106, v10\n\t"
    "ds_read_b128 v[156:159], v128 offset:0\n\t"
    "ds_read_b128 v[160:163], v128 offset:64\n\t"
    "v_mfma_f32_16x16x32_bf16 v[92:95], v[180:183], v[180:183], 0\n\t"
    "v_fmac_f32 v120, v107, v11\n\t"
    "v_mad_u32_u24 v129, v129, s68, v151\n\t"
    "v_mfma_f32_16x16x32_bf16 v[92:95], v[184:187], v[184:187], v[92:95]\n\t"
    "v_and_b32 v129, -16, v129\n\t"
    "v_fmac_f32 v121, v108, v85\n\t"
    "v_mfma_f32_16x16x32_bf16 v[96:99], v[212:215], v[172:175], 0\n\t"
    "v_fmac_f32 v120, v112, v12\n\t"
    "v_fmac_f32 v121, v113, v13\n\t"
    "v_mfma_f32_16x16x32_bf16 v[96:99], v[216:219], v[176:179], v[96:99]\n\t"
    "v_fmac_f32 v120, v114, v14\n\t"
    "v_fmac_f32 v121, v115, v15\n\t"
    "s_lshl_b32 s51, s55, 8\n\t"
    "s_add_u32 s51, s51, s50\n\t"
    "s_waitcnt lgkmcnt(8)\n\t"
    "v_mfma_f32_16x16x32_bf16 v[100:103], v[164:167], v[188:191], 0\n\t"
    "v_mad_u32_u24 v130, v130, s68, v151\n\t"
    "v_and_b32 v130, -16, v130\n\t"
    "v_mfma_f32_16x16x32_bf16 v[100:103], v[168:171], v[192:195], v[100:103]\n\t"
    "v_fmac_f32 v120, v116, v16\n\t"
    "v_fmac_f32 v121, v117, v17\n\t"
    "buffer_load_dword v145, v146, %12, s51 offen sc1\n\t"
    "v_mfma_f32_16x16x32_bf16 v[104:107], v[172:175], v[188:191], 0\n\t"
    "v_fmac_f32 v120, v118, v18\n\t"
    "v_fmac_f32 v121, v119, v19\n\t"
    "v_mfma_f32_16x16x32_bf16 v[104:107], v[176:179], v[192:195], v[104:107]\n\t"
    "v_fmac_f32 v120, v92, v20\n\t"
    "v_fmac_f32 v121, v93, v21\n\t"
    "s_waitcnt lgkmcnt(6)\n\t"
    "v_mfma_f32_16x16x32_bf16 v[108:111], v[164:167], v[196:199], 0\n\t"
    "v_fmac_f32 v120, v94, v22\n\t"
    "v_fmac_f32 v121, v95, v23\n\t"
    "v_mfma_f32_16x16x32_bf16 v[108:111], v[168:171], v[200:203], v[108:111]\n\t"
    "v_fmac_f32 v120, v96, v86\n\t"
    "v_mad_u32_u24 v132, v132, s68, v151\n\t"
    "v_mfma_f32_16x16x32_bf16 v[112:115], v[212:215], v[180:183], 0\n\t"
    "v_fmac_f32 v121, v100, v24\n\t"
    "v_fmac_f32 v120, v101, v25\n\t"
    "v_mfma_f32_16x16x32_bf16 v[112:115], v[216:219], v[184:187], v[112:115]\n\t"
    "v_fmac_f32 v121, v102, v26\n\t"
    "v_fmac_f32 v120, v103, v27\n\t"
    "s_waitcnt lgkmcnt(3)\n\t"
    "v_mfma_f32_16x16x32_bf16 v[116:119], v[164:167], v[204:207], 0\n\t"
    "v_readfirstlane_b32 s60, v148\n\t"
    "v_and_b32 v132, -16, v132\n\t"
    "v_mfma_f32_16x16x32_bf16 v[116:119], v[168:171], v[208:211], v[116:119]\n\t"
    "v_fmac_f32 v121, v104, v28\n\t"
    "v_fmac_f32 v120, v105, v29\n\t"
    "ds_read_b128 v[164:167], v129 offset:0\n\t"
    "ds_read_b128 v[168:171], v129 offset:64\n\t"
    "v_mfma_f32_16x16x32_bf16 v[92:95], v[172:175], v[196:199], 0\n\t"
    "v_fmac_f32 v121, v106, v30\n\t"
    "v_fmac_f32 v120, v107, v31\n\t"
    "v_mfma_f32_16x16x32_bf16 v[92:95], v[176:179], v[200:203], v[92:95]\n\t"
    "v_fmac_f32 v121, v108, v32\n\t"
    "v_fmac_f32 v120, v109, v33\n\t"
    "v_mfma_f32_16x16x32_bf16 v[96:99], v[172:175], v[204:207], 0\n\t"
    "v_fmac_f32 v121, v110, v34\n\t"
    "v_fmac_f32 v120, v111, v35\n\t"
    "v_mfma_f32_16x16x32_bf16 v[96:99], v[176:179], v[208:211], v[96:99]\n\t"
    "v_fmac_f32 v121, v112, v87\n\t"
    "v_mad_u32_u24 v133, v133, s68, v151\n\t"
    "ds_read_b128 v[172:175], v130 offset:0\n\t"
    "ds_read_b128 v[176:179], v130 offset:64\n\t"
    "v_mfma_f32_16x16x32_bf16 v[100:103], v[212:215], v[188:191], 0\n\t"
    "v_fmac_f32 v120, v116, v36\n\t"
    "v_fmac_f32 v121, v117, v37\n\t"
    "v_mfma_f32_16x16x32_bf16 v[100:103], v[216:219], v[192:195], v[100:103]\n\t"
    "v_fmac_f32 v120, v118, v38\n\t"
    "v_fmac_f32 v121, v119, v39\n\t"
    "v_mfma_f32_16x16x32_bf16 v[104:107], v[180:183], v[188:191], 0\n\t"
    "v_and_b32 v133, -16, v133\n\t"
    "v_fmac_f32 v120, v92, v40\n\t"
    "v_mfma_f32_16x16x32_bf16 v[104:107], v[184:187], v[192:195], v[104:107]\n\t"
    "v_fmac_f32 v121, v93, v41\n\t"
    "v_fmac_f32 v120, v94, v42\n\t"
    "v_mfma_f32_16x16x32_bf16 v[108:111], v[180:183], v[196:199], 0\n\t"
    "v_fmac_f32 v121, v95, v43\n\t"
    "v_fmac_f32 v120, v96, v44\n\t"
    "v_mfma_f32_16x16x32_bf16 v[108:111], v[184:187], v[200:203], v[108:111]\n\t"
    "v_fmac_f32 v121, v97, v45\n\t"
    "v_fmac_f32 v120, v98, v46\n\t"
    "v_mfma_f32_16x16x32_bf16 v[112:115], v[180:183], v[204:207], 0\n\t"
    "v_fmac_f32 v121, v99, v47\n\t"
    "v_fmac_f32 v120, v100, v88\n\t"
    "v_mfma_f32_16x16x32_bf16 v[112:115], v[184:187], v[208:211], v[112:115]\n\t"
    "v_mad_u32_u24 v134, v134, s68, v151\n\t"
    "v_and_b32 v134, -16, v134\n\t"
    "ds_read_b128 v[180:183], v132 offset:0\n\t"
    "ds_read_b128 v[184:187], v132 offset:64\n\t"
    "v_mfma_f32_16x16x32_bf16 v[116:119], v[212:215], v[196:199], 0\n\t"
    "v_fmac_f32 v121, v104, v48\n\t"
    "v_fmac_f32 v120, v105, v49\n\t"
    "v_mfma_f32_16x16x32_bf16 v[116:119], v[216:219], v[200:203], v[116:119]\n\t"
    "v_fmac_f32 v121, v106, v50\n\t"
    "v_fmac_f32 v120, v107, v51\n\t"
    "v_mfma_f32_16x16x32_bf16 v[92:95], v[188:191], v[188:191], 0\n\t"
    "v_mad_u32_u24 v135, v135, s68, v151\n\t"
    "v_and_b32 v135, -16, v135\n\t"
    "v_mfma_f32_16x16x32_bf16 v[92:95], v[192:195], v[192:195], v[92:95]\n\t"
    "v_fmac_f32 v121, v108, v52\n\t"
    "v_fmac_f32 v120, v109, v53\n\t"
    "v_mfma_f32_16x16x32_bf16 v[96:99], v[188:191], v[196:199], 0\n\t"
    "v_fmac_f32 v121, v110, v54\n\t"
    "v_fmac_f32 v120, v111, v55\n\t"
    "v_mfma_f32_16x16x32_bf16 v[96:99], v[192:195], v[200:203], v[96:99]\n\t"
    "v_fmac_f32 v121, v112, v56\n\t"
    "v_fmac_f32 v120, v113, v57\n\t"
    "v_mfma_f32_16x16x32_bf16 v[100:103], v[188:191], v[204:207], 0\n\t"
    "v_fmac_f32 v121, v114, v58\n\t"
    "v_fmac_f32 v120, v115, v59\n\t"
    "v_mfma_f32_16x16x32_bf16 v[100:103], v[192:195], v[208:211], v[100:103]\n\t"
    "v_fmac_f32 v121, v116, v89\n\t"
    "ds_read_b128 v[188:191], v133 offset:0\n\t"
    "ds_read_b128 v[192:195], v133 offset:64\n\t"
    "v_mfma_f32_16x16x32_bf16 v[104:107], v[212:215], v[204:207], 0\n\t"
    "v_fmac_f32 v120, v92, v60\n\t"
    "v_fmac_f32 v121, v93, v61\n\t"
    "v_mfma_f32_16x16x32_bf16 v[104:107], v[216:219], v[208:211], v[104:107]\n\t"
    "v_fmac_f32 v120, v94, v62\n\t"
    "v_fmac_f32 v121, v95, v63\n\t"
    "v_mfma_f32_16x16x32_bf16 v[108:111], v[196:199], v[196:199], 0\n\t"
    "v_fmac_f32 v120, v96, v64\n\t"
    "v_fmac_f32 v121, v97, v65\n\t"
    "v_mfma_f32_16x16x32_bf16 v[108:111], v[200:203], v[200:203], v[108:111]\n\t"
    "v_fmac_f32 v120, v98, v66\n\t"
    "v_fmac_f32 v121, v99, v67\n\t"
    "v_mfma_f32_16x16x32_bf16 v[112:115], v[196:199], v[204:207], 0\n\t"
    "v_fmac_f32 v120, v100, v68\n\t"
    "v_fmac_f32 v121, v101, v69\n\t"
    "v_mfma_f32_16x16x32_bf16 v[112:115], v[200:203], v[208:211], v[112:115]\n\t"
    "v_fmac_f32 v120, v102, v70\n\t"
    "v_fmac_f32 v121, v103, v71\n\t"
    "ds_read_b128 v[196:199], v134 offset:0\n\t"
    "ds_read_b128 v[200:203], v134 offset:64\n\t"
    "v_mfma_f32_16x16x32_bf16 v[116:119], v[204:207], v[204:207], 0\n\t"
    "v_fmac_f32 v120, v104, v90\n\t"
    "v_mfma_f32_16x16x32_bf16 v[116:119], v[208:211], v[208:211], v[116:119]\n\t"
    "ds_read_b128 v[204:207], v135 offset:0\n\t"
    "ds_read_b128 v[208:211], v135 offset:64\n\t"
    "s_mov_b32 s52, s53\n\t"
    "s_mov_b32 s53, s54\n\t"
    "s_mov_b32 s54, s55\n\t"
    "s_mov_b32 s55, s60\n\t"
    "s_cmp_lt_u32 s54, s64\n\t"
    "s_cbranch_scc1 L_pr1_%=\n\t"
    "s_setprio 0\n\t"
    "L_pr1_%=:\n\t"
    "s_cmp_lt_u32 s53, s64\n\t"
    "s_cbranch_scc1 L_pnn_%=\n\t"
    "L_done_%=:\n\t"
    "s_mul_i32 s69, s52, 272\n\t"
    "s_add_u32 s69, s69, 130048\n\t"
    "v_add_u32 v147, s69, v146\n\t"
    "v_fmac_f32 v121, v108, v72\n\t"
    "v_fmac_f32 v120, v109, v73\n\t"
    "v_fmac_f32 v121, v110, v74\n\t"
    "v_fmac_f32 v120, v111, v75\n\t"
    "v_fmac_f32 v121, v112, v76\n\t"
    "v_fmac_f32 v120, v113, v77\n\t"
    "v_fmac_f32 v121, v114, v78\n\t"
    "v_fmac_f32 v120, v115, v79\n\t"
    "v_fmac_f32 v121, v116, v80\n\t"
    "v_fmac_f32 v120, v117, v81\n\t"
    "v_fmac_f32 v121, v118, v82\n\t"
    "v_fmac_f32 v120, v119, v83\n\t"
    "v_add_f32 v154, v120, v121\n\t"
    "ds_write_b32 v147, v154\n\t"
    "v_lshrrev_b32 v147, 2, v146\n\t"
    "s_mov_b64 s[92:93], %17\n\t"
    "s_and_b32 s93, s93, 0xffff\n\t"
    "s_mov_b32 s94, 32768\n\t"
    "s_mov_b32 s95, 0x20000\n\t"
    "s_lshl_b32 s44, s37, 1\n\t"
    "v_and_b32 v155, 15, v147\n\t"
    "v_lshl_add_u32 v155, %16, 4, v155\n\t"
    "v_lshrrev_b32 v154, 4, v147\n\t"
    "v_mul_u32_u24 v122, 272, v155\n\t"
    "v_lshl_add_u32 v122, v154, 6, v122\n\t"
    "v_add_u32 v122, 130048, v122\n\t"
    "v_mul_u32_u24 v154, 2688, v154\n\t"
    "v_lshl_add_u32 v154, v155, 2, v154\n\t"
    "v_add_u32 v154, 152896, v154\n\t"
    "v_cmp_le_u32 vcc, 48, v147\n\t"
    "v_cmp_gt_u32 s[46:47], s38, v155\n\t"
    "v_lshlrev_b32 v155, 1, v155\n\t"
    "s_and_b64 vcc, vcc, s[46:47]\n\t"
    "s_waitcnt lgkmcnt(0)\n\t"
    "s_barrier\n\t"
    "s_cbranch_vccz L_fin_%=\n\t"
    "ds_read_b128 v[92:95], v122 offset:0\n\t"
    "ds_read_b128 v[96:99], v122 offset:16\n\t"
    "ds_read_b128 v[100:103], v122 offset:32\n\t"
    "ds_read_b128 v[104:107], v122 offset:48\n\t"
    "ds_read2_b32 v[108:109], v154 offset0:0 offset1:84\n\t"
    "ds_read2_b32 v[110:111], v154 offset0:168 offset1:252\n\t"
    "v_add_u32 v154, 1344, v154\n\t"
    "ds_read2_b32 v[112:113], v154 offset0:0 offset1:84\n\t"
    "ds_read2_b32 v[114:115], v154 offset0:168 offset1:252\n\t"
    "s_waitcnt lgkmcnt(6)\n\t"
    "v_add_f32 v92, v92, v96\n\t"
    "v_add_f32 v93, v93, v97\n\t"
    "v_add_f32 v94, v94, v98\n\t"
    "v_add_f32 v95, v95, v99\n\t"
    "s_waitcnt lgkmcnt(5)\n\t"
    "v_add_f32 v92, v92, v100\n\t"
    "v_add_f32 v93, v93, v101\n\t"
    "v_add_f32 v94, v94, v102\n\t"
    "v_add_f32 v95, v95, v103\n\t"
    "s_waitcnt lgkmcnt(4)\n\t"
    "v_add_f32 v92, v92, v104\n\t"
    "v_add_f32 v93, v93, v105\n\t"
    "v_add_f32 v94, v94, v106\n\t"
    "v_add_f32 v95, v95, v107\n\t"
    "s_waitcnt lgkmcnt(2)\n\t"
    "v_add_f32 v92, v92, v108\n\t"
    "v_add_f32 v93, v93, v109\n\t"
    "v_add_f32 v94, v94, v110\n\t"
    "v_add_f32 v95, v95, v111\n\t"
    "s_waitcnt lgkmcnt(0)\n\t"
    "v_add_f32 v92, v92, v112\n\t"
    "v_add_f32 v93, v93, v113\n\t"
    "v_add_f32 v94, v94, v114\n\t"
    "v_add_f32 v95, v95, v115\n\t"
    "v_add_f32 v92, v92, v93\n\t"
    "v_add_f32 v94, v94, v95\n\t"
    "v_add_f32 v92, v92, v94\n\t"
    "v_mov_b32 v93, v92\n\t"
    "s_nop 1\n\t"
    "v_permlane16_swap_b32 v93, v92\n\t"
    "s_nop 1\n\t"
    "v_add_f32 v92, v92, v93\n\t"
    "v_mov_b32 v93, v92\n\t"
    "s_nop 1\n\t"
    "v_permlane32_swap_b32 v93, v92\n\t"
    "s_nop 1\n\t"
    "v_add_f32 v92, v92, v93\n\t"
    "v_add_f32 v92, s39, v92\n\t"
    "v_cvt_pk_bf16_f32 v92, v92, v92\n\t"
    "s_mov_b64 exec, vcc\n\t"
    "buffer_store_short v92, v155, s[92:95], s44 offen\n\t"
    "s_mov_b64 exec, -1\n\t"
    "s_branch L_fin_%=\n\t"
    "L_req0_%=:\n\t"
    "s_cmp_lg_u32 s61, 0\n\t"
    "s_cbranch_scc1 L_req0b_%=\n\t"
    "s_bfe_u32 s65, s84, 0xc0002\n\t"
    "s_add_u32 s65, s65, s36\n\t"
    "buffer_store_dword v236, v125, %12, s65 offen offset:0 sc1\n\t"
    "buffer_store_dword v237, v125, %12, s65 offen offset:256 sc1\n\t"
    "s_bfe_u32 s65, s85, 0xc0002\n\t"
    "s_add_u32 s65, s65, s36\n\t"
    "buffer_store_dword v238, v125, %12, s65 offen offset:0 sc1\n\t"
    "buffer_store_dword v239, v125, %12, s65 offen offset:256 sc1\n\t"
    "s_bfe_u32 s65, s86, 0xc0002\n\t"
    "s_add_u32 s65, s65, s36\n\t"
    "buffer_store_dword v240, v125, %12, s65 offen offset:0 sc1\n\t"
    "buffer_store_dword v241, v125, %12, s65 offen offset:256 sc1\n\t"
    "s_bfe_u32 s65, s87, 0xc0002\n\t"
    "s_add_u32 s65, s65, s36\n\t"
    "buffer_store_dword v242, v125, %12, s65 offen offset:0 sc1\n\t"
    "buffer_store_dword v243, v125, %12, s65 offen offset:256 sc1\n\t"
    "s_bfe_u32 s65, s88, 0xc0002\n\t"
    "s_add_u32 s65, s65, s36\n\t"
    "buffer_store_dword v244, v125, %12, s65 offen offset:0 sc1\n\t"
    "buffer_store_dword v245, v125, %12, s65 offen offset:256 sc1\n\t"
    "s_bfe_u32 s65, s89, 0xc0002\n\t"
    "s_add_u32 s65, s65, s36\n\t"
    "buffer_store_dword v246, v125, %12, s65 offen offset:0 sc1\n\t"
    "buffer_store_dword v247, v125, %12, s65 offen offset:256 sc1\n\t"
    "s_bfe_u32 s65, s90, 0xc0002\n\t"
    "s_add_u32 s65, s65, s36\n\t"
    "buffer_store_dword v248, v125, %12, s65 offen offset:0 sc1\n\t"
    "buffer_store_dword v249, v125, %12, s65 offen offset:256 sc1\n\t"
    "s_bfe_u32 s65, s91, 0xc0002\n\t"
    "s_add_u32 s65, s65, s36\n\t"
    "buffer_store_dword v250, v125, %12, s65 offen offset:0 sc1\n\t"
    "buffer_store_dword v251, v125, %12, s65 offen offset:256 sc1\n\t"
    "s_sub_u32 s61, s64, 3\n\t"
    "s_branch L_back0_%=\n\t"
    "L_req0b_%=:\n\t"
    "s_cmp_lg_u32 s61, 1\n\t"
    "s_cbranch_scc1 L_req0c_%=\n\t"
    "v_lshrrev_b32 v154, 16, v122\n\t"
    "v_lshl_add_u32 v154, %16, 2, v154\n\t"
    "v_mul_u32_u24 v154, 336, v154\n\t"
    "v_and_b32 v122, 0xf0, v122\n\t"
    "v_cmp_gt_u32 vcc, 80, v122\n\t"
    "v_add3_u32 v122, v122, v154, s45\n\t"
    "s_mov_b32 s61, -1\n\t"
    "v_add_f32 v124, v220, v224\n\t"
    "v_add_f32 v154, v228, v232\n\t"
    "v_add_f32 v124, v124, v154\n\t"
    "v_add_f32 v125, v221, v225\n\t"
    "v_add_f32 v154, v229, v233\n\t"
    "v_add_f32 v125, v125, v154\n\t"
    "v_add_f32 v126, v222, v226\n\t"
    "v_add_f32 v154, v230, v234\n\t"
    "v_add_f32 v126, v126, v154\n\t"
    "v_add_f32 v127, v223, v227\n\t"
    "v_add_f32 v154, v231, v235\n\t"
    "v_add_f32 v127, v127, v154\n\t"
    "ds_write_b128 v122, v[124:127] offset:0\n\t"
    "s_cmp_le_u32 s38, 64\n\t"
    "s_cbranch_scc1 L_back0_%=\n\t"
    "v_add_f32 v124, v236, v240\n\t"
    "v_add_f32 v154, v244, v248\n\t"
    "v_add_f32 v124, v124, v154\n\t"
    "v_add_f32 v125, v237, v241\n\t"
    "v_add_f32 v154, v245, v249\n\t"
    "v_add_f32 v125, v125, v154\n\t"
    "v_add_f32 v126, v238, v242\n\t"
    "v_add_f32 v154, v246, v250\n\t"
    "v_add_f32 v126, v126, v154\n\t"
    "v_add_f32 v127, v239, v243\n\t"
    "v_add_f32 v154, v247, v251\n\t"
    "v_add_f32 v127, v127, v154\n\t"
    "s_and_saveexec_b64 s[46:47], vcc\n\t"
    "ds_write_b128 v122, v[124:127] offset:256\n\t"
    "s_mov_b64 exec, s[46:47]\n\t"
    "s_branch L_back0_%=\n\t"
    "L_req0c_%=:\n\t"
    "s_lshl_b32 s46, %14, 7\n\t"
    "s_add_u32 s46, s46, 49295360\n\t"
    "s_mov_b32 s47, 1048576\n\t"
    "s_mov_b64 exec, 1\n\t"
    "s_cmp_ge_u32 s55, s64\n\t"
    "s_cbranch_scc1 L_pq0_%=\n\t"
    "buffer_load_dword v123, off, %12, s46 sc1\n\t"
    "s_mov_b64 exec, -1\n\t"
    "s_branch L_back0_%=\n\t"
    "L_pq0_%=:\n\t"
    "v_cmp_eq_u32 vcc, 0, v123\n\t"
    "s_cbranch_vccz L_in0_%=\n\t"
    "L_poll0_%=:\n\t"
    "buffer_load_dword v155, off, %12, s46 sc1\n\t"
    "s_sub_u32 s47, s47, 1\n\t"
    "s_cmp_eq_u32 s47, 0\n\t"
    "s_cbranch_scc1 L_in0_%=\n\t"
    "s_waitcnt vmcnt(0)\n\t"
    "v_cmp_eq_u32 vcc, 0, v155\n\t"
    "s_cbranch_vccz L_in0_%=\n\t"
    "s_sleep 8\n\t"
    "s_branch L_poll0_%=\n\t"
    "L_in0_%=:\n\t"
    "s_mov_b64 exec, -1\n\t"
    "s_lshl_b32 s46, s37, 2\n\t"
    "s_mul_i32 s47, %16, 1048576\n\t"
    "s_add_u32 s46, s46, s47\n\t"
    "s_add_u32 s46, s46, 17825792\n\t"
    "v_and_b32 v155, 0x80, v122\n\t"
    "v_lshl_add_u32 v155, v155, 23, v122\n\t"
    "buffer_load_dwordx4 v[220:223], v122, %12, s46 offen sc1\n\t"
    "s_add_u32 s46, s46, 262144\n\t"
    "buffer_load_dwordx4 v[224:227], v122, %12, s46 offen sc1\n\t"
    "s_add_u32 s46, s46, 262144\n\t"
    "buffer_load_dwordx4 v[228:231], v122, %12, s46 offen sc1\n\t"
    "s_add_u32 s46, s46, 262144\n\t"
    "buffer_load_dwordx4 v[232:235], v122, %12, s46 offen sc1\n\t"
    "s_add_u32 s46, s46, 262144\n\t"
    "s_mov_b32 s61, 1\n\t"
    "s_cmp_le_u32 s38, 64\n\t"
    "s_cbranch_scc1 L_back0_%=\n\t"
    "s_sub_u32 s46, s46, 262144\n\t"
    "buffer_load_dwordx4 v[248:251], v155, %12, s46 offen offset:256 sc1\n\t"
    "s_sub_u32 s46, s46, 262144\n\t"
    "buffer_load_dwordx4 v[244:247], v155, %12, s46 offen offset:256 sc1\n\t"
    "s_sub_u32 s46, s46, 262144\n\t"
    "buffer_load_dwordx4 v[240:243], v155, %12, s46 offen offset:256 sc1\n\t"
    "s_sub_u32 s46, s46, 262144\n\t"
    "buffer_load_dwordx4 v[236:239], v155, %12, s46 offen offset:256 sc1\n\t"
    "s_branch L_back0_%=\n\t"
    "L_req1_%=:\n\t"
    "s_cmp_lg_u32 s61, 1\n\t"
    "s_cbranch_scc1 L_req1c_%=\n\t"
    "v_lshrrev_b32 v154, 16, v122\n\t"
    "v_lshl_add_u32 v154, %16, 2, v154\n\t"
    "v_mul_u32_u24 v154, 336, v154\n\t"
    "v_and_b32 v122, 0xf0, v122\n\t"
    "v_cmp_gt_u32 vcc, 80, v122\n\t"
    "v_add3_u32 v122, v122, v154, s45\n\t"
    "s_mov_b32 s61, -1\n\t"
    "v_add_f32 v124, v220, v224\n\t"
    "v_add_f32 v154, v228, v232\n\t"
    "v_add_f32 v124, v124, v154\n\t"
    "v_add_f32 v125, v221, v225\n\t"
    "v_add_f32 v154, v229, v233\n\t"
    "v_add_f32 v125, v125, v154\n\t"
    "v_add_f32 v126, v222, v226\n\t"
    "v_add_f32 v154, v230, v234\n\t"
    "v_add_f32 v126, v126, v154\n\t"
    "v_add_f32 v127, v223, v227\n\t"
    "v_add_f32 v154, v231, v235\n\t"
    "v_add_f32 v127, v127, v154\n\t"
    "ds_write_b128 v122, v[124:127] offset:0\n\t"
    "s_cmp_le_u32 s38, 64\n\t"
    "s_cbranch_scc1 L_back1_%=\n\t"
    "v_add_f32 v124, v236, v240\n\t"
    "v_add_f32 v154, v244, v248\n\t"
    "v_add_f32 v124, v124, v154\n\t"
    "v_add_f32 v125, v237, v241\n\t"
    "v_add_f32 v154, v245, v249\n\t"
    "v_add_f32 v125, v125, v154\n\t"
    "v_add_f32 v126, v238, v242\n\t"
    "v_add_f32 v154, v246, v250\n\t"
    "v_add_f32 v126, v126, v154\n\t"
    "v_add_f32 v127, v239, v243\n\t"
    "v_add_f32 v154, v247, v251\n\t"
    "v_add_f32 v127, v127, v154\n\t"
    "s_and_saveexec_b64 s[46:47], vcc\n\t"
    "ds_write_b128 v122, v[124:127] offset:256\n\t"
    "s_mov_b64 exec, s[46:47]\n\t"
    "s_branch L_back1_%=\n\t"
    "L_req1c_%=:\n\t"
    "s_lshl_b32 s46, %14, 7\n\t"
    "s_add_u32 s46, s46, 49295360\n\t"
    "s_mov_b32 s47, 1048576\n\t"
    "s_mov_b64 exec, 1\n\t"
    "s_cmp_ge_u32 s55, s64\n\t"
    "s_cbranch_scc1 L_pq1_%=\n\t"
    "buffer_load_dword v123, off, %12, s46 sc1\n\t"
    "s_mov_b64 exec, -1\n\t"
    "s_branch L_back1_%=\n\t"
    "L_pq1_%=:\n\t"
    "v_cmp_eq_u32 vcc, 0, v123\n\t"
    "s_cbranch_vccz L_in1_%=\n\t"
    "L_poll1_%=:\n\t"
    "buffer_load_dword v155, off, %12, s46 sc1\n\t"
    "s_sub_u32 s47, s47, 1\n\t"
    "s_cmp_eq_u32 s47, 0\n\t"
    "s_cbranch_scc1 L_in1_%=\n\t"
    "s_waitcnt vmcnt(0)\n\t"
    "v_cmp_eq_u32 vcc, 0, v155\n\t"
    "s_cbranch_vccz L_in1_%=\n\t"
    "s_sleep 8\n\t"
    "s_branch L_poll1_%=\n\t"
    "L_in1_%=:\n\t"
    "s_mov_b64 exec, -1\n\t"
    "s_lshl_b32 s46, s37, 2\n\t"
    "s_mul_i32 s47, %16, 1048576\n\t"
    "s_add_u32 s46, s46, s47\n\t"
    "s_add_u32 s46, s46, 17825792\n\t"
    "v_and_b32 v155, 0x80, v122\n\t"
    "v_lshl_add_u32 v155, v155, 23, v122\n\t"
    "buffer_load_dwordx4 v[220:223], v122, %12, s46 offen sc1\n\t"
    "s_add_u32 s46, s46, 262144\n\t"
    "buffer_load_dwordx4 v[224:227], v122, %12, s46 offen sc1\n\t"
    "s_add_u32 s46, s46, 262144\n\t"
    "buffer_load_dwordx4 v[228:231], v122, %12, s46 offen sc1\n\t"
    "s_add_u32 s46, s46, 262144\n\t"
    "buffer_load_dwordx4 v[232:235], v122, %12, s46 offen sc1\n\t"
    "s_add_u32 s46, s46, 262144\n\t"
    "s_mov_b32 s61, 1\n\t"
    "s_cmp_le_u32 s38, 64\n\t"
    "s_cbranch_scc1 L_back1_%=\n\t"
    "s_sub_u32 s46, s46, 262144\n\t"
    "buffer_load_dwordx4 v[248:251], v155, %12, s46 offen offset:256 sc1\n\t"
    "s_sub_u32 s46, s46, 262144\n\t"
    "buffer_load_dwordx4 v[244:247], v155, %12, s46 offen offset:256 sc1\n\t"
    "s_sub_u32 s46, s46, 262144\n\t"
    "buffer_load_dwordx4 v[240:243], v155, %12, s46 offen offset:256 sc1\n\t"
    "s_sub_u32 s46, s46, 262144\n\t"
    "buffer_load_dwordx4 v[236:239], v155, %12, s46 offen offset:256 sc1\n\t"
    "s_branch L_back1_%=\n\t"
    "L_sig_%=:\n\t"
    "v_subrev_u32 v155, s49, v149\n\t"
    "v_add_u32 v155, 1024, v155\n\t"
    "s_waitcnt vmcnt(0)\n\t"
    "ds_add_rtn_u32 v148, v155, v150\n\t"
    "s_mov_b32 s45, 152896\n\t"
    "s_mov_b32 s46, 49283072\n\t"
    "v_mov_b32 v155, 1\n\t"
    "s_waitcnt lgkmcnt(0)\n\t"
    "v_readfirstlane_b32 s47, v148\n\t"
    "s_nop 0\n\t"
    "s_cmp_lg_u32 s47, 7\n\t"
    "s_cbranch_scc1 L_sigb_%=\n\t"
    "s_mov_b64 exec, 1\n\t"
    "buffer_atomic_add v155, off, %12, s46 sc0 sc1\n\t"
    "s_mov_b64 exec, -1\n\t"
    "s_waitcnt vmcnt(0)\n\t"
    "v_readfirstlane_b32 s47, v155\n\t"
    "v_lshlrev_b32 v155, 5, v146\n\t"
    "s_cmp_lg_u32 s47, 255\n\t"
    "s_cbranch_scc1 L_sigb_%=\n\t"
    "s_mov_b32 s46, 49295360\n\t"
    "buffer_store_dword v150, v155, %12, s46 offen sc1\n\t"
    "s_add_u32 s46, s46, 0x2000\n\t"
    "buffer_store_dword v150, v155, %12, s46 offen sc1\n\t"
    "s_add_u32 s46, s46, 0x2000\n\t"
    "buffer_store_dword v150, v155, %12, s46 offen sc1\n\t"
    "s_add_u32 s46, s46, 0x2000\n\t"
    "buffer_store_dword v150, v155, %12, s46 offen sc1\n\t"
    "s_add_u32 s46, s46, 0x2000\n\t"
    "s_branch L_sigb_%=\n\t"
    "L_fin_%=:\n\t"
    "s_branch L_pend_%=\n\t"
    "L_pub_%=:\n\t"
    "s_memrealtime s[46:47]\n\t"
    "s_lshl_b32 s49, %14, 2\n\t"
    "s_add_u32 s49, s49, 49293312\n\t"
    "s_waitcnt lgkmcnt(0)\n\t"
    "s_sub_u32 s47, s46, s44\n\t"
    "s_mul_i32 s47, s47, 320\n\t"
    "s_lshr_b32 s47, s47, 8\n\t"
    "s_add_u32 s46, s46, s47\n\t"
    "s_or_b32 s46, s46, 1\n\t"
    "v_mov_b32 v83, s46\n\t"
    "s_mov_b64 exec, 1\n\t"
    "buffer_store_dword v83, off, %12, s49 sc1\n\t"
    "s_mov_b64 exec, -1\n\t"
    "s_branch L_pubb_%=\n\t"
    "L_part0_%=:\n\t"
    "s_mov_b32 s47, 0\n\t"
    "s_call_b64 s[98:99], L_bld_%=\n\t"
    "s_branch L_partb0_%=\n\t"
    "L_part1_%=:\n\t"
    "s_mov_b32 s47, 1048576\n\t"
    "s_waitcnt vmcnt(8)\n\t"
    "s_call_b64 s[98:99], L_bchk_%=\n\t"
    "s_branch L_partb1_%=\n\t"
    "L_bchk_%=:\n\t"
    "v_min_u32 v132, v128, v129\n\t"
    "v_min3_u32 v132, v132, v130, v131\n\t"
    "v_cmp_eq_u32 vcc, 0, v132\n\t"
    "s_cbranch_vccz L_bok_%=\n\t"
    "L_bld_%=:\n\t"
    "s_mov_b32 s46, 49293312\n\t"
    "buffer_load_dwordx4 v[128:131], v127, %12, s46 offen sc1\n\t"
    "s_cmp_eq_u32 s47, 0\n\t"
    "s_cbranch_scc1 L_bret_%=\n\t"
    "s_sub_u32 s47, s47, 1\n\t"
    "s_sleep 16\n\t"
    "s_waitcnt vmcnt(0)\n\t"
    "s_branch L_bchk_%=\n\t"
    "L_bok_%=:\n\t"
    "v_readfirstlane_b32 s46, v128\n\t"
    "v_mov_b32 v138, -4096\n\t"
    "v_mov_b32 v139, 4096\n\t"
    "v_subrev_u32 v128, s46, v128\n\t"
    "v_subrev_u32 v129, s46, v129\n\t"
    "v_subrev_u32 v130, s46, v130\n\t"
    "v_subrev_u32 v131, s46, v131\n\t"
    "v_writelane_b32 v128, 50, 0\n\t"
    "v_add3_u32 v132, v128, v129, v130\n\t"
    "v_add_u32 v132, v132, v131\n\t"
    "s_nop 1\n\t"
    "v_add_u32_dpp v132, v132, v132 row_shr:1 row_mask:0xf bank_mask:0xf\n\t"
    "s_nop 1\n\t"
    "v_add_u32_dpp v132, v132, v132 row_shr:2 row_mask:0xf bank_mask:0xf\n\t"
    "s_nop 1\n\t"
    "v_add_u32_dpp v132, v132, v132 row_shr:4 row_mask:0xf bank_mask:0xf\n\t"
    "s_nop 1\n\t"
    "v_add_u32_dpp v132, v132, v132 row_shr:8 row_mask:0xf bank_mask:0xf\n\t"
    "s_nop 1\n\t"
    "v_add_u32_dpp v132, v132, v132 row_bcast:15 row_mask:0xa bank_mask:0xf\n\t"
    "s_nop 1\n\t"
    "v_add_u32_dpp v132, v132, v132 row_bcast:31 row_mask:0xc bank_mask:0xf\n\t"
    "s_nop 0\n\t"
    "v_readlane_b32 s46, v132, 63\n\t"
    "s_movk_i32 s47, 20\n\t"
    "v_mov_b32 v133, -8192\n\t"
    "v_mov_b32 v134, 5120\n\t"
    "s_ashr_i32 s46, s46, 8\n\t"
    "v_sub_u32 v128, s46, v128\n\t"
    "v_med3_i32 v128, v128, v138, v139\n\t"
    "v_mul_i32_i24 v128, s47, v128\n\t"
    "v_med3_i32 v128, v128, v133, v134\n\t"
    "v_and_b32 v140, 0x3ff, v128\n\t"
    "v_ashrrev_i32 v128, 10, v128\n\t"
    "v_sub_u32 v129, s46, v129\n\t"
    "v_med3_i32 v129, v129, v138, v139\n\t"
    "v_mul_i32_i24 v129, s47, v129\n\t"
    "v_med3_i32 v129, v129, v133, v134\n\t"
    "v_and_b32 v141, 0x3ff, v129\n\t"
    "v_ashrrev_i32 v129, 10, v129\n\t"
    "v_sub_u32 v130, s46, v130\n\t"
    "v_med3_i32 v130, v130, v138, v139\n\t"
    "v_mul_i32_i24 v130, s47, v130\n\t"
    "v_med3_i32 v130, v130, v133, v134\n\t"
    "v_and_b32 v142, 0x3ff, v130\n\t"
    "v_ashrrev_i32 v130, 10, v130\n\t"
    "v_sub_u32 v131, s46, v131\n\t"
    "v_med3_i32 v131, v131, v138, v139\n\t"
    "v_mul_i32_i24 v131, s47, v131\n\t"
    "v_med3_i32 v131, v131, v133, v134\n\t"
    "v_and_b32 v143, 0x3ff, v131\n\t"
    "v_ashrrev_i32 v131, 10, v131\n\t"
    "v_add3_u32 v132, v128, v129, v130\n\t"
    "v_add_u32 v132, v132, v131\n\t"
    "s_nop 1\n\t"
    "v_add_u32_dpp v132, v132, v132 row_shr:1 row_mask:0xf bank_mask:0xf\n\t"
    "s_nop 1\n\t"
    "v_add_u32_dpp v132, v132, v132 row_shr:2 row_mask:0xf bank_mask:0xf\n\t"
    "s_nop 1\n\t"
    "v_add_u32_dpp v132, v132, v132 row_shr:4 row_mask:0xf bank_mask:0xf\n\t"
    "s_nop 1\n\t"
    "v_add_u32_dpp v132, v132, v132 row_shr:8 row_mask:0xf bank_mask:0xf\n\t"
    "s_nop 1\n\t"
    "v_add_u32_dpp v132, v132, v132 row_bcast:15 row_mask:0xa bank_mask:0xf\n\t"
    "s_nop 1\n\t"
    "v_add_u32_dpp v132, v132, v132 row_bcast:31 row_mask:0xc bank_mask:0xf\n\t"
    "s_nop 0\n\t"
    "v_readlane_b32 s46, v132, 63\n\t"
    "s_movk_i32 s47, 0x400\n\t"
    "s_sub_u32 s48, s47, 512\n\t"
    "s_mov_b32 s49, s46\n\t"
    "v_cmp_le_u32 vcc, s48, v140\n\t"
    "s_bcnt1_i32_b64 s44, vcc\n\t"
    "s_add_u32 s49, s49, s44\n\t"
    "v_cmp_le_u32 vcc, s48, v141\n\t"
    "s_bcnt1_i32_b64 s44, vcc\n\t"
    "s_add_u32 s49, s49, s44\n\t"
    "v_cmp_le_u32 vcc, s48, v142\n\t"
    "s_bcnt1_i32_b64 s44, vcc\n\t"
    "s_add_u32 s49, s49, s44\n\t"
    "v_cmp_le_u32 vcc, s48, v143\n\t"
    "s_bcnt1_i32_b64 s44, vcc\n\t"
    "s_add_u32 s49, s49, s44\n\t"
    "s_cmp_lt_i32 s49, 0\n\t"
    "s_cselect_b32 s47, s48, s47\n\t"
    "s_sub_u32 s48, s47, 256\n\t"
    "s_mov_b32 s49, s46\n\t"
    "v_cmp_le_u32 vcc, s48, v140\n\t"
    "s_bcnt1_i32_b64 s44, vcc\n\t"
    "s_add_u32 s49, s49, s44\n\t"
    "v_cmp_le_u32 vcc, s48, v141\n\t"
    "s_bcnt1_i32_b64 s44, vcc\n\t"
    "s_add_u32 s49, s49, s44\n\t"
    "v_cmp_le_u32 vcc, s48, v142\n\t"
    "s_bcnt1_i32_b64 s44, vcc\n\t"
    "s_add_u32 s49, s49, s44\n\t"
    "v_cmp_le_u32 vcc, s48, v143\n\t"
    "s_bcnt1_i32_b64 s44, vcc\n\t"
    "s_add_u32 s49, s49, s44\n\t"
    "s_cmp_lt_i32 s49, 0\n\t"
    "s_cselect_b32 s47, s48, s47\n\t"
    "s_sub_u32 s48, s47, 128\n\t"
    "s_mov_b32 s49, s46\n\t"
    "v_cmp_le_u32 vcc, s48, v140\n\t"
    "s_bcnt1_i32_b64 s44, vcc\n\t"
    "s_add_u32 s49, s49, s44\n\t"
    "v_cmp_le_u32 vcc, s48, v141\n\t"
    "s_bcnt1_i32_b64 s44, vcc\n\t"
    "s_add_u32 s49, s49, s44\n\t"
    "v_cmp_le_u32 vcc, s48, v142\n\t"
    "s_bcnt1_i32_b64 s44, vcc\n\t"
    "s_add_u32 s49, s49, s44\n\t"
    "v_cmp_le_u32 vcc, s48, v143\n\t"
    "s_bcnt1_i32_b64 s44, vcc\n\t"
    "s_add_u32 s49, s49, s44\n\t"
    "s_cmp_lt_i32 s49, 0\n\t"
    "s_cselect_b32 s47, s48, s47\n\t"
    "s_sub_u32 s48, s47, 64\n\t"
    "s_mov_b32 s49, s46\n\t"
    "v_cmp_le_u32 vcc, s48, v140\n\t"
    "s_bcnt1_i32_b64 s44, vcc\n\t"
    "s_add_u32 s49, s49, s44\n\t"
    "v_cmp_le_u32 vcc, s48, v141\n\t"
    "s_bcnt1_i32_b64 s44, vcc\n\t"
    "s_add_u32 s49, s49, s44\n\t"
    "v_cmp_le_u32 vcc, s48, v142\n\t"
    "s_bcnt1_i32_b64 s44, vcc\n\t"
    "s_add_u32 s49, s49, s44\n\t"
    "v_cmp_le_u32 vcc, s48, v143\n\t"
    "s_bcnt1_i32_b64 s44, vcc\n\t"
    "s_add_u32 s49, s49, s44\n\t"
    "s_cmp_lt_i32 s49, 0\n\t"
    "s_cselect_b32 s47, s48, s47\n\t"
    "s_sub_u32 s48, s47, 32\n\t"
    "s_mov_b32 s49, s46\n\t"
    "v_cmp_le_u32 vcc, s48, v140\n\t"
    "s_bcnt1_i32_b64 s44, vcc\n\t"
    "s_add_u32 s49, s49, s44\n\t"
    "v_cmp_le_u32 vcc, s48, v141\n\t"
    "s_bcnt1_i32_b64 s44, vcc\n\t"
    "s_add_u32 s49, s49, s44\n\t"
    "v_cmp_le_u32 vcc, s48, v142\n\t"
    "s_bcnt1_i32_b64 s44, vcc\n\t"
    "s_add_u32 s49, s49, s44\n\t"
    "v_cmp_le_u32 vcc, s48, v143\n\t"
    "s_bcnt1_i32_b64 s44, vcc\n\t"
    "s_add_u32 s49, s49, s44\n\t"
    "s_cmp_lt_i32 s49, 0\n\t"
    "s_cselect_b32 s47, s48, s47\n\t"
    "s_sub_u32 s47, s47, 32\n\t"
    "s_max_u32 s47, s47, 1\n\t"
    "v_cmp_le_u32 vcc, s47, v140\n\t"
    "s_nop 1\n\t"
    "v_addc_co_u32 v128, vcc, 0, v128, vcc\n\t"
    "v_cmp_le_u32 vcc, s47, v141\n\t"
    "s_nop 1\n\t"
    "v_addc_co_u32 v129, vcc, 0, v129, vcc\n\t"
    "v_cmp_le_u32 vcc, s47, v142\n\t"
    "s_nop 1\n\t"
    "v_addc_co_u32 v130, vcc, 0, v130, vcc\n\t"
    "v_cmp_le_u32 vcc, s47, v143\n\t"
    "s_nop 1\n\t"
    "v_addc_co_u32 v131, vcc, 0, v131, vcc\n\t"
    "v_add3_u32 v132, v128, v129, v130\n\t"
    "v_add_u32 v132, v132, v131\n\t"
    "s_nop 1\n\t"
    "v_add_u32_dpp v132, v132, v132 row_shr:1 row_mask:0xf bank_mask:0xf\n\t"
    "s_nop 1\n\t"
    "v_add_u32_dpp v132, v132, v132 row_shr:2 row_mask:0xf bank_mask:0xf\n\t"
    "s_nop 1\n\t"
    "v_add_u32_dpp v132, v132, v132 row_shr:4 row_mask:0xf bank_mask:0xf\n\t"
    "s_nop 1\n\t"
    "v_add_u32_dpp v132, v132, v132 row_shr:8 row_mask:0xf bank_mask:0xf\n\t"
    "s_nop 1\n\t"
    "v_add_u32_dpp v132, v132, v132 row_bcast:15 row_mask:0xa bank_mask:0xf\n\t"
    "s_nop 1\n\t"
    "v_add_u32_dpp v132, v132, v132 row_bcast:31 row_mask:0xc bank_mask:0xf\n\t"
    "s_nop 0\n\t"
    "v_readlane_b32 s46, v132, 63\n\t"
    "v_lshrrev_b32 v135, 2, v127\n\t"
    "s_cmp_le_u32 s46, 256\n\t"
    "s_cselect_b32 s47, -1, 0\n\t"
    "v_add_u32 v133, 4, v135\n\t"
    "v_mul_u32_u24 v134, s46, v133\n\t"
    "v_lshrrev_b32 v134, 8, v134\n\t"
    "v_sub_u32 v134, v132, v134\n\t"
    "v_and_b32 v134, s47, v134\n\t"
    "v_lshl_add_u32 v136, v133, 4, v134\n\t"
    "v_sub_u32 v132, v132, v131\n\t"
    "v_add_u32 v133, 3, v135\n\t"
    "v_mul_u32_u24 v134, s46, v133\n\t"
    "v_lshrrev_b32 v134, 8, v134\n\t"
    "v_sub_u32 v134, v132, v134\n\t"
    "v_and_b32 v134, s47, v134\n\t"
    "v_lshl_add_u32 v137, v133, 4, v134\n\t"
    "v_lshl_or_b32 v143, v136, 16, v137\n\t"
    "v_sub_u32 v132, v132, v130\n\t"
    "v_add_u32 v133, 2, v135\n\t"
    "v_mul_u32_u24 v134, s46, v133\n\t"
    "v_lshrrev_b32 v134, 8, v134\n\t"
    "v_sub_u32 v134, v132, v134\n\t"
    "v_and_b32 v134, s47, v134\n\t"
    "v_lshl_add_u32 v136, v133, 4, v134\n\t"
    "v_lshl_or_b32 v142, v137, 16, v136\n\t"
    "v_sub_u32 v132, v132, v129\n\t"
    "v_add_u32 v133, 1, v135\n\t"
    "v_mul_u32_u24 v134, s46, v133\n\t"
    "v_lshrrev_b32 v134, 8, v134\n\t"
    "v_sub_u32 v134, v132, v134\n\t"
    "v_and_b32 v134, s47, v134\n\t"
    "v_lshl_add_u32 v137, v133, 4, v134\n\t"
    "v_lshl_or_b32 v141, v136, 16, v137\n\t"
    "v_sub_u32 v132, v132, v128\n\t"
    "v_add_u32 v133, 0, v135\n\t"
    "v_mul_u32_u24 v134, s46, v133\n\t"
    "v_lshrrev_b32 v134, 8, v134\n\t"
    "v_sub_u32 v134, v132, v134\n\t"
    "v_and_b32 v134, s47, v134\n\t"
    "v_lshl_add_u32 v136, v133, 4, v134\n\t"
    "v_lshl_or_b32 v140, v137, 16, v136\n\t"
    "s_mov_b32 s46, 49294336\n\t"
    "buffer_store_dwordx4 v[140:143], v127, %12, s46 offen sc1\n\t"
    "s_mov_b32 s97, 1\n\t"
    "L_bret_%=:\n\t"
    "s_setpc_b64 s[98:99]\n\t"
    "L_rw_%=:\n\t"
    "s_mov_b32 s47, 1048576\n\t"
    "L_rw2_%=:\n\t"
    "s_sleep 16\n\t"
    "s_lshl_b32 s46, %14, 2\n\t"
    "s_add_u32 s46, s46, 49294336\n\t"
    "s_mov_b64 exec, 1\n\t"
    "buffer_load_dword v144, off, %12, s46 sc1\n\t"
    "s_mov_b64 exec, -1\n\t"
    "s_waitcnt vmcnt(0)\n\t"
    "v_readfirstlane_b32 s38, v144\n\t"
    "s_sub_u32 s47, s47, 1\n\t"
    "s_cmp_eq_u32 s47, 0\n\t"
    "s_cselect_b32 s38, 0x100000, s38\n\t"
    "s_cmp_eq_u32 s38, 0\n\t"
    "s_cbranch_scc1 L_rw2_%=\n\t"
    "s_branch L_rok_%=\n\t"
    "L_xw0_%=:\n\t"
    "s_mov_b32 s68, 1048576\n\t"
    "L_xv0_%=:\n\t"
    "s_sleep 4\n\t"
    "s_mov_b64 exec, 1\n\t"
    "buffer_load_dword v164, off, %12, s93 offset:0 sc1\n\t"
    "s_mov_b64 exec, -1\n\t"
    "s_sub_u32 s68, s68, 1\n\t"
    "s_cmp_eq_u32 s68, 0\n\t"
    "s_cbranch_scc1 L_xwb0_%=\n\t"
    "s_waitcnt vmcnt(0)\n\t"
    "v_readfirstlane_b32 s65, v164\n\t"
    "s_nop 0\n\t"
    "s_cmp_eq_u32 s65, 0\n\t"
    "s_cbranch_scc1 L_xv0_%=\n\t"
    "s_branch L_xwb0_%=\n\t"
    "L_xw1_%=:\n\t"
    "s_mov_b32 s68, 1048576\n\t"
    "L_xv1_%=:\n\t"
    "s_sleep 4\n\t"
    "s_mov_b64 exec, 1\n\t"
    "buffer_load_dword v164, off, %12, s93 offset:128 sc1\n\t"
    "s_mov_b64 exec, -1\n\t"
    "s_sub_u32 s68, s68, 1\n\t"
    "s_cmp_eq_u32 s68, 0\n\t"
    "s_cbranch_scc1 L_xwb1_%=\n\t"
    "s_waitcnt vmcnt(0)\n\t"
    "v_readfirstlane_b32 s65, v164\n\t"
    "s_nop 0\n\t"
    "s_cmp_eq_u32 s65, 0\n\t"
    "s_cbranch_scc1 L_xv1_%=\n\t"
    "s_branch L_xwb1_%=\n\t"
    "L_xw2_%=:\n\t"
    "s_mov_b32 s68, 1048576\n\t"
    "L_xv2_%=:\n\t"
    "s_sleep 4\n\t"
    "s_mov_b64 exec, 1\n\t"
    "buffer_load_dword v164, off, %12, s93 offset:256 sc1\n\t"
    "s_mov_b64 exec, -1\n\t"
    "s_sub_u32 s68, s68, 1\n\t"
    "s_cmp_eq_u32 s68, 0\n\t"
    "s_cbranch_scc1 L_xwb2_%=\n\t"
    "s_waitcnt vmcnt(0)\n\t"
    "v_readfirstlane_b32 s65, v164\n\t"
    "s_nop 0\n\t"
    "s_cmp_eq_u32 s65, 0\n\t"
    "s_cbranch_scc1 L_xv2_%=\n\t"
    "s_branch L_xwb2_%=\n\t"
    "L_xw3_%=:\n\t"
    "s_mov_b32 s68, 1048576\n\t"
    "L_xv3_%=:\n\t"
    "s_sleep 4\n\t"
    "s_mov_b64 exec, 1\n\t"
    "buffer_load_dword v164, off, %12, s93 offset:384 sc1\n\t"
    "s_mov_b64 exec, -1\n\t"
    "s_sub_u32 s68, s68, 1\n\t"
    "s_cmp_eq_u32 s68, 0\n\t"
    "s_cbranch_scc1 L_xwb3_%=\n\t"
    "s_waitcnt vmcnt(0)\n\t"
    "v_readfirstlane_b32 s65, v164\n\t"
    "s_nop 0\n\t"
    "s_cmp_eq_u32 s65, 0\n\t"
    "s_cbranch_scc1 L_xv3_%=\n\t"
    "s_branch L_xwb3_%=\n\t"
    "L_mp2_%=:\n\t"
    "v_lshrrev_b32 v57, 4, v127\n\t"
    "v_add_u32 v57, 32, v57\n\t"
    "s_and_b32 s65, %14, 7\n\t"
    "s_add_u32 s65, s65, 24\n\t"
    "v_writelane_b32 v57, s65, 16\n\t"
    "v_mov_b32 v58, 16\n\t"
    "v_lshlrev_b32 v57, 7, v57\n\t"
    "v_writelane_b32 v58, 32, 16\n\t"
    "s_mov_b32 s65, 49283072\n\t"
    "v_mov_b32 v56, 32\n\t"
    "s_mov_b32 s66, 1048576\n\t"
    "s_mov_b32 s65, 49283072\n\t"
    "s_mov_b64 exec, 0x1ffff\n\t"
    "buffer_load_dword v56, v57, %12, s65 offen sc1\n\t"
    "s_mov_b64 exec, -1\n\t"
    "s_branch L_mp2b_%=\n\t"
    "L_mp3_%=:\n\t"
    "v_cmp_gt_u32 vcc, v58, v56\n\t"
    "s_cbranch_vccz L_mp3b_%=\n\t"
    "s_sub_u32 s66, s66, 1\n\t"
    "s_cmp_eq_u32 s66, 0\n\t"
    "s_cbranch_scc1 L_mp3b_%=\n\t"
    "s_sleep 8\n\t"
    "s_mov_b32 s65, 49283072\n\t"
    "s_mov_b64 exec, 0x1ffff\n\t"
    "buffer_load_dword v56, v57, %12, s65 offen sc1\n\t"
    "s_mov_b64 exec, -1\n\t"
    "s_waitcnt vmcnt(0)\n\t"
    "s_branch L_mp3_%=\n\t"
    "L_pend_%=:\n\t"
    ".Ldcp_end:\n\t"
    :: "s"(s_idx), "s"(p_wp), "s"(p_w1), "n"(0), "n"(0), "n"(0), "s"(pb1), "s"(pw2), "s"(pb2), "s"(b2s),
       "n"(0),
       "s"((unsigned long long)ws & 0xFFFFFFFFFFFFull), "s"(s_ws), "n"(0), "s"(bid), "s"(s_tab), "s"(w), "s"(p_out),
       "s"((unsigned)(unsigned long long)idx), "s"((unsigned)((unsigned long long)idx >> 32) & 0xFFFFu)
    : "memory", "vcc", "scc", "m0", "s36", "s37", "s38", "s39", "s40", "s41", "s42", "s43", "s44", "s45", "s46", "s47", "s48", "s49", "s50", "s51", "s52", "s53", "s54", "s55", "s56", "s57", "s58", "s59", "s60", "s61", "s62", "s63", "s64", "s65", "s66", "s67", "s68", "s69", "s70", "s71", "s72", "s73", "s74", "s75", "s76", "s77", "s78", "s79", "s80", "s81", "s82", "s83", "s84", "s85", "s86", "s87", "s88", "s89", "s90", "s91", "s92", "s93", "s94", "s95", "s96", "s97", "s98", "s99", "v0", "v1", "v2", "v3", "v4", "v5", "v6", "v7", "v8", "v9", "v10", "v11", "v12", "v13", "v14", "v15", "v16", "v17", "v18", "v19", "v20", "v21", "v22", "v23", "v24", "v25", "v26", "v27", "v28", "v29", "v30", "v31", "v32", "v33", "v34", "v35", "v36", "v37", "v38", "v39", "v40", "v41", "v42", "v43", "v44", "v45", "v46", "v47", "v48", "v49", "v50", "v51", "v52", "v53", "v54", "v55", "v56", "v57", "v58", "v59", "v60", "v61", "v62", "v63", "v64", "v65", "v66", "v67", "v68", "v69", "v70", "v71", "v72", "v73", "v74", "v75", "v76", "v77", "v78", "v79", "v80", "v81", "v82", "v83", "v84", "v85", "v86", "v87", "v88", "v89", "v90", "v91", "v92", "v93", "v94", "v95", "v96", "v97", "v98", "v99", "v100", "v101", "v102", "v103", "v104", "v105", "v106", "v107", "v108", "v109", "v110", "v111", "v112", "v113", "v114", "v115", "v116", "v117", "v118", "v119", "v120", "v121", "v122", "v123", "v124", "v125", "v126", "v127", "v128", "v129", "v130", "v131", "v132", "v133", "v134", "v135", "v136", "v137", "v138", "v139", "v140", "v141", "v142", "v143", "v144", "v145", "v146", "v147", "v148", "v149", "v150", "v151", "v152", "v153", "v154", "v155", "v156", "v157", "v158", "v159", "v160", "v161", "v162", "v163", "v164", "v165", "v166", "v167", "v168", "v169", "v170", "v171", "v172", "v173", "v174", "v175", "v176", "v177", "v178", "v179", "v180", "v181", "v182", "v183", "v184", "v185", "v186", "v187", "v188", "v189", "v190", "v191", "v192", "v193", "v194", "v195", "v196", "v197", "v198", "v199", "v200", "v201", "v202", "v203", "v204", "v205", "v206", "v207", "v208", "v209", "v210", "v211", "v212", "v213", "v214", "v215", "v216", "v217", "v218", "v219", "v220", "v221", "v222", "v223", "v224", "v225", "v226", "v227", "v228", "v229", "v230", "v231", "v232", "v233", "v234", "v235", "v236", "v237", "v238", "v239", "v240", "v241", "v242", "v243", "v244", "v245", "v246", "v247", "v248", "v249", "v250", "v251", "v252", "v253", "v254", "v255");
}

extern "C" void kernel_launch(void* const* d_in, const int* in_sizes, int n_in, void* d_out, int out_size, void* d_ws,
                              size_t ws_size, hipStream_t stream) {
  const int* idx = (const int*)d_in[0];
  const unsigned short* table = (const unsigned short*)d_in[1];
  const unsigned short* w_p = (const unsigned short*)d_in[2];
  const unsigned short* w1 = (const unsigned short*)d_in[3];
  const unsigned short* b1 = (const unsigned short*)d_in[4];
  const unsigned short* w2 = (const unsigned short*)d_in[5];
  const unsigned short* b2 = (const unsigned short*)d_in[6];
  char* ws = (char*)d_ws;
  dcp_all<<<dim3(256), dim3(512), 0, stream>>>(idx, table, w_p, w1, b1, w2, b2, ws, (__hip_bfloat16*)d_out);
}
```

```cpp
#include <hip/hip_runtime.h>
#include <hip/hip_bf16.h>
#include <stdint.h>

typedef __attribute__((ext_vector_type(2))) unsigned u32x2;

constexpr int BATCH = 16384, NF = 100, DIM = 128, VOC = 1000, HID = 512;
constexpr size_t NWIN = 13;
constexpr size_t WS_IDX = 0;
constexpr size_t IDX_STREAM = 212992;
constexpr size_t WS_W1F = 3407872;
constexpr size_t W1F_SLAB = 102400;
constexpr size_t WS_TF = 16515072;
constexpr size_t WS_WT = 16777216;
constexpr size_t WS_PART = 17825792;
constexpr size_t WS_P0 = 27262976;
constexpr size_t WS_XCH = 31457280;
constexpr size_t XCH_PITCH = 69632;
constexpr size_t WS_CNT = 49283072;
constexpr size_t WS_CH = 49291264;
constexpr size_t WS_RP = 49292288;
constexpr size_t WS_TS = 49293312;
constexpr size_t WS_RNG = 49294336;
constexpr size_t WS_RDY = 49295360;
constexpr size_t WS_DBG = 49328128;
constexpr size_t WS_END = 49721344;

__device__ __forceinline__ __amdgpu_buffer_rsrc_t mk_srd(const void* p, unsigned bytes) {
  return __builtin_amdgcn_make_buffer_rsrc((void*)p, (short)0, (int)bytes, 0x00020000);
}

__global__ __launch_bounds__(512, 2) void dcp_all(const int* __restrict__ idx, const unsigned short* __restrict__ table,
                                                  const unsigned short* __restrict__ w_p, const unsigned short* __restrict__ w1,
                                                  const unsigned short* __restrict__ b1, const unsigned short* __restrict__ w2,
                                                  const unsigned short* __restrict__ b2, char* __restrict__ ws,
                                                  __hip_bfloat16* __restrict__ out) {
  __shared__ __attribute__((aligned(1024))) char smem[163840];
  const int w = __builtin_amdgcn_readfirstlane(threadIdx.x >> 6) + (int)(uintptr_t)smem;
  const int bid = blockIdx.x;
  asm volatile(
    "s_getpc_b64 s[36:37]\n\t"
    ".Ldcp_pc:\n\t"
    "s_bfe_u32 s38, %0, 0x40003\n\t"
    "s_lshl_b32 s38, s38, 13\n\t"
    "v_and_b32 v122, 63, %1\n\t"
    "v_lshlrev_b32 v122, 7, v122\n\t"
    "v_add_u32 v122, s38, v122\n\t"
    "v_min_u32 v122, .Ldcp_end-.Ldcp_pc-4, v122\n\t"
    "s_cmp_lg_u32 %2, 3\n\t"
    "s_cbranch_scc1 L_nopf_%=\n\t"
    "global_load_dword v123, v122, s[36:37]\n\t"
    "L_nopf_%=:\n\t"
    :: "s"(bid), "v"(threadIdx.x), "s"(w) : "memory", "scc", "s36", "s37", "s38", "s74", "s75", "v122", "v123");
  const int xcd = bid & 7, loc = bid >> 3;
  const int cg = xcd * 16 + (loc >> 1);
  const __amdgpu_buffer_rsrc_t s_idx = mk_srd(idx + (size_t)bid * 64 * NF, (unsigned)((BATCH - bid * 64) * NF * 4));
  const __amdgpu_buffer_rsrc_t s_tab = mk_srd(table, VOC * DIM * 2);
  const __amdgpu_buffer_rsrc_t s_ws = mk_srd(ws, (unsigned)WS_END);
  const unsigned short* pb1 = b1 + 4 * cg;
  const unsigned short* pw2 = w2 + 4 * cg;
  const unsigned long long pb2 = (unsigned long long)b2 & ~3ull;
  const unsigned b2s = ((unsigned)(unsigned long long)b2 & 2u) * 8u;
  const unsigned long long p_wp = (unsigned long long)w_p & 0xFFFFFFFFFFFFull, p_w1 = (unsigned long long)w1 & 0xFFFFFFFFFFFFull;
  const unsigned long long p_out = (unsigned long long)out & 0xFFFFFFFFFFFFull;
  asm volatile(
    "v_mbcnt_lo_u32_b32 v147, -1, 0\n\t"
    "v_mbcnt_hi_u32_b32 v147, -1, v147\n\t"
    "s_mov_b64 s[92:93], %1\n\t"
    "s_mov_b32 s94, 20000\n\t"
    "s_mov_b32 s95, 0x20000\n\t"
    "s_lshr_b32 s46, %14, 3\n\t"
    "s_lshl_b32 s51, s46, 2\n\t"
    "v_lshlrev_b32 v122, 4, v147\n\t"
    "s_cmp_ge_u32 %16, 2\n\t"
    "s_cbranch_scc1 L_aimg_%=\n\t"
    "s_lshl_b32 s44, %16, 10\n\t"
    "s_add_u32 m0, s44, 140288\n\t"
    "s_nop 0\n\t"
    "buffer_load_dwordx4 v122, s[92:95], s44 offen lds\n\t"
    "s_add_u32 s44, s44, 0x800\n\t"
    "s_add_u32 m0, s44, 140288\n\t"
    "s_nop 0\n\t"
    "buffer_load_dwordx4 v122, s[92:95], s44 offen lds\n\t"
    "s_add_u32 s44, s44, 0x800\n\t"
    "s_add_u32 m0, s44, 140288\n\t"
    "s_nop 0\n\t"
    "buffer_load_dwordx4 v122, s[92:95], s44 offen lds\n\t"
    "s_add_u32 s44, s44, 0x800\n\t"
    "s_add_u32 m0, s44, 140288\n\t"
    "s_nop 0\n\t"
    "buffer_load_dwordx4 v122, s[92:95], s44 offen lds\n\t"
    "s_add_u32 s44, s44, 0x800\n\t"
    "s_add_u32 m0, s44, 140288\n\t"
    "s_nop 0\n\t"
    "buffer_load_dwordx4 v122, s[92:95], s44 offen lds\n\t"
    "s_add_u32 s44, s44, 0x800\n\t"
    "s_add_u32 m0, s44, 140288\n\t"
    "s_nop 0\n\t"
    "buffer_load_dwordx4 v122, s[92:95], s44 offen lds\n\t"
    "s_add_u32 s44, s44, 0x800\n\t"
    "s_add_u32 m0, s44, 140288\n\t"
    "s_nop 0\n\t"
    "buffer_load_dwordx4 v122, s[92:95], s44 offen lds\n\t"
    "s_add_u32 s44, s44, 0x800\n\t"
    "s_add_u32 m0, s44, 140288\n\t"
    "s_nop 0\n\t"
    "buffer_load_dwordx4 v122, s[92:95], s44 offen lds\n\t"
    "s_add_u32 s44, s44, 0x800\n\t"
    "s_add_u32 m0, s44, 140288\n\t"
    "s_nop 0\n\t"
    "buffer_load_dwordx4 v122, s[92:95], s44 offen lds\n\t"
    "s_add_u32 s44, s44, 0x800\n\t"
    "s_add_u32 m0, s44, 140288\n\t"
    "s_nop 0\n\t"
    "buffer_load_dwordx4 v122, s[92:95], s44 offen lds\n\t"
    "s_add_u32 s44, s44, 0x800\n\t"
    "s_mov_b32 s98, 13107200\n\t"
    "s_mov_b32 s99, 0x20000\n\t"
    "s_cmp_eq_u32 %16, 0\n\t"
    "s_cbranch_scc1 L_aside_%=\n\t"
    "s_branch L_ahelp_%=\n\t"
    "L_aimg_%=:\n\t"
    "s_cmp_lg_u32 %16, 2\n\t"
    "s_cbranch_scc1 L_anoinit_%=\n\t"
    "v_mov_b32 v0, 0\n\t"
    "v_mov_b32 v1, 0\n\t"
    "v_mov_b32 v2, 0\n\t"
    "v_mov_b32 v3, 0\n\t"
    "v_lshlrev_b32 v8, 4, v147\n\t"
    "v_add_u32 v8, 130048, v8\n\t"
    "v_mov_b32 v9, 1024\n\t"
    "ds_write_b128 v9, v[0:3]\n\t"
    "ds_write_b128 v8, v[0:3] offset:0\n\t"
    "ds_write_b128 v8, v[0:3] offset:1024\n\t"
    "ds_write_b128 v8, v[0:3] offset:2048\n\t"
    "ds_write_b32 v9, v0 offset:2084\n\t"
    "ds_write_b32 v9, v0 offset:2088\n\t"
    "ds_write_b32 v9, v0 offset:1044\n\t"
    "ds_write_b32 v9, v0 offset:1052\n\t"
    "v_mov_b32 v4, 3\n\t"
    "ds_write_b32 v9, v4 offset:1040\n\t"
    "v_mov_b32 v5, 16\n\t"
    "ds_write_b32 v9, v5 offset:2080\n\t"
    "v_mov_b32 v6, 32\n\t"
    "ds_write_b32 v9, v6 offset:3120\n\t"
    "v_mov_b32 v7, 51\n\t"
    "ds_write_b32 v9, v7 offset:4160\n\t"
    "L_anoinit_%=:\n\t"
    "s_and_b32 s46, %16, 3\n\t"
    "s_mul_i32 s49, s46, 1040\n\t"
    "s_add_u32 s49, s49, 2064\n\t"
    "s_lshl_b32 s46, s46, 3\n\t"
    "s_lshr_b32 s47, 1076697610, s46\n\t"
    "s_and_b32 s47, s47, 0xff\n\t"
    "s_lshl_b32 s48, s47, 16\n\t"
    "s_lshr_b32 s47, 756681216, s46\n\t"
    "s_and_b32 s47, s47, 0xff\n\t"
    "s_add_u32 s48, s48, s47\n\t"
    "s_lshr_b32 s47, %16, 2\n\t"
    "s_add_u32 s47, s47, 0x200\n\t"
    "s_cmp_eq_u32 s46, 0\n\t"
    "s_cselect_b32 s47, 0x100, s47\n\t"
    "s_add_u32 s48, s48, s47\n\t"
    "v_lshrrev_b32 v126, 3, v147\n\t"
    "v_and_b32 v154, 7, v147\n\t"
    "v_lshlrev_b32 v126, 8, v126\n\t"
    "v_lshl_add_u32 v126, v154, 4, v126\n\t"
    "v_lshlrev_b32 v117, 20, v147\n\t"
    "v_add_u32 v117, 1024, v117\n\t"
    "v_mov_b32 v118, 1\n\t"
    "s_and_b32 s46, %16, 6\n\t"
    "s_cmp_eq_u32 s46, 4\n\t"
    "s_cbranch_scc1 L_adma_%=\n\t"
    "s_lshr_b32 s61, %16, 2\n\t"
    "s_lshl_b32 s61, s61, 1\n\t"
    "s_and_b32 s46, %16, 1\n\t"
    "s_add_u32 s61, s61, s46\n\t"
    "s_add_u32 s46, s61, 84\n\t"
    "s_add_u32 s69, s51, s46\n\t"
    "s_sub_u32 s44, s69, 125\n\t"
    "s_cmp_ge_u32 s69, 125\n\t"
    "s_cselect_b32 s69, s44, s69\n\t"
    "s_mul_i32 m0, s69, 1040\n\t"
    "s_lshl_b32 s44, s69, 11\n\t"
    "buffer_load_dwordx4 v126, %15, s44 offen lds\n\t"
    "s_add_u32 s69, s69, 4\n\t"
    "s_sub_u32 s44, s69, 125\n\t"
    "s_cmp_ge_u32 s69, 125\n\t"
    "s_cselect_b32 s69, s44, s69\n\t"
    "s_mul_i32 m0, s69, 1040\n\t"
    "s_lshl_b32 s44, s69, 11\n\t"
    "buffer_load_dwordx4 v126, %15, s44 offen lds\n\t"
    "s_add_u32 s69, s69, 4\n\t"
    "s_sub_u32 s44, s69, 125\n\t"
    "s_cmp_ge_u32 s69, 125\n\t"
    "s_cselect_b32 s69, s44, s69\n\t"
    "s_waitcnt lgkmcnt(0)\n\t"
    "s_barrier\n\t"
    "s_load_dwordx2 s[72:73], %6, 0\n\t"
    "s_load_dwordx2 s[88:89], %7, 0\n\t"
    "s_load_dword s91, %8, 0\n\t"
    "s_mul_i32 m0, s69, 1040\n\t"
    "s_lshl_b32 s44, s69, 11\n\t"
    "buffer_load_dwordx4 v126, %15, s44 offen lds\n\t"
    "s_add_u32 s69, s69, 4\n\t"
    "s_sub_u32 s44, s69, 125\n\t"
    "s_cmp_ge_u32 s69, 125\n\t"
    "s_cselect_b32 s69, s44, s69\n\t"
    "s_mul_i32 m0, s69, 1040\n\t"
    "s_lshl_b32 s44, s69, 11\n\t"
    "buffer_load_dwordx4 v126, %15, s44 offen lds\n\t"
    "s_add_u32 s69, s69, 4\n\t"
    "s_sub_u32 s44, s69, 125\n\t"
    "s_cmp_ge_u32 s69, 125\n\t"
    "s_cselect_b32 s69, s44, s69\n\t"
    "s_mul_i32 m0, s69, 1040\n\t"
    "s_lshl_b32 s44, s69, 11\n\t"
    "buffer_load_dwordx4 v126, %15, s44 offen lds\n\t"
    "s_add_u32 s69, s69, 4\n\t"
    "s_sub_u32 s44, s69, 125\n\t"
    "s_cmp_ge_u32 s69, 125\n\t"
    "s_cselect_b32 s69, s44, s69\n\t"
    "s_mul_i32 m0, s69, 1040\n\t"
    "s_lshl_b32 s44, s69, 11\n\t"
    "buffer_load_dwordx4 v126, %15, s44 offen lds\n\t"
    "s_add_u32 s69, s69, 4\n\t"
    "s_sub_u32 s44, s69, 125\n\t"
    "s_cmp_ge_u32 s69, 125\n\t"
    "s_cselect_b32 s69, s44, s69\n\t"
    "v_bfe_u32 v154, v147, 2, 2\n\t"
    "v_and_b32 v155, 3, v147\n\t"
    "v_mul_u32_u24 v154, 25, v154\n\t"
    "v_mad_u32_u24 v154, v155, 6, v154\n\t"
    "v_lshlrev_b32 v153, 2, v154\n\t"
    "s_and_b32 s53, s48, 0xff\n\t"
    "s_bfe_u32 s55, s48, 0x80008\n\t"
    "s_add_u32 s54, s53, s55\n\t"
    "s_add_u32 s55, s54, s55\n\t"
    "s_lshr_b32 s64, s48, 16\n\t"
    "v_cmp_eq_u32 s[62:63], 0, v155\n\t"
    "v_and_b32 v151, 48, v147\n\t"
    "v_or_b32 v155, 0x100000, v151\n\t"
    "v_cndmask_b32 v152, v155, v151, s[62:63]\n\t"
    "v_lshrrev_b32 v94, 4, v147\n\t"
    "v_mul_u32_u24 v92, 0x1388, v94\n\t"
    "v_lshl_add_u32 v92, v154, 1, v92\n\t"
    "v_add_u32 v92, 140288, v92\n\t"
    "v_mul_u32_u24 v93, 200, v154\n\t"
    "v_mad_u32_u24 v93, v94, 50, v93\n\t"
    "v_add_u32 v93, 140288, v93\n\t"
    "v_add_u32 v95, 140288, v122\n\t"
    "v_mov_b32 v0, 0\n\t"
    "v_mov_b32 v28, 0\n\t"
    "v_mov_b32 v1, 0\n\t"
    "v_mov_b32 v29, 0\n\t"
    "v_mov_b32 v2, 0\n\t"
    "v_mov_b32 v30, 0\n\t"
    "v_mov_b32 v3, 0\n\t"
    "v_mov_b32 v31, 0\n\t"
    "v_mov_b32 v4, 0\n\t"
    "v_mov_b32 v32, 0\n\t"
    "v_mov_b32 v5, 0\n\t"
    "v_mov_b32 v33, 0\n\t"
    "v_mov_b32 v6, 0\n\t"
    "v_mov_b32 v34, 0\n\t"
    "v_mov_b32 v7, 0\n\t"
    "v_mov_b32 v35, 0\n\t"
    "v_mov_b32 v8, 0\n\t"
    "v_mov_b32 v36, 0\n\t"
    "v_mov_b32 v9, 0\n\t"
    "v_mov_b32 v37, 0\n\t"
    "v_mov_b32 v10, 0\n\t"
    "v_mov_b32 v38, 0\n\t"
    "v_mov_b32 v11, 0\n\t"
    "v_mov_b32 v39, 0\n\t"
    "v_mov_b32 v12, 0\n\t"
    "v_mov_b32 v40, 0\n\t"
    "v_mov_b32 v13, 0\n\t"
    "v_mov_b32 v41, 0\n\t"
    "v_mov_b32 v14, 0\n\t"
    "v_mov_b32 v42, 0\n\t"
    "v_mov_b32 v15, 0\n\t"
    "v_mov_b32 v43, 0\n\t"
    "v_mov_b32 v16, 0\n\t"
    "v_mov_b32 v44, 0\n\t"
    "v_mov_b32 v17, 0\n\t"
    "v_mov_b32 v45, 0\n\t"
    "v_mov_b32 v18, 0\n\t"
    "v_mov_b32 v46, 0\n\t"
    "v_mov_b32 v19, 0\n\t"
    "v_mov_b32 v47, 0\n\t"
    "v_mov_b32 v20, 0\n\t"
    "v_mov_b32 v48, 0\n\t"
    "v_mov_b32 v21, 0\n\t"
    "v_mov_b32 v49, 0\n\t"
    "v_mov_b32 v22, 0\n\t"
    "v_mov_b32 v50, 0\n\t"
    "v_mov_b32 v23, 0\n\t"
    "v_mov_b32 v51, 0\n\t"
    "s_cmp_eq_u32 s61, 1\n\t"
    "s_cbranch_scc1 L_afold1_%=\n\t"
    "s_cmp_eq_u32 s61, 2\n\t"
    "s_cbranch_scc1 L_afold2_%=\n\t"
    "s_cmp_eq_u32 s61, 3\n\t"
    "s_cbranch_scc1 L_afold3_%=\n\t"
    "L_afold0_%=:\n\t"
    "L_asp0_%=:\n\t"
    "ds_read_b32 v119, v117 offset:1044\n\t"
    "s_waitcnt lgkmcnt(0)\n\t"
    "v_readfirstlane_b32 s46, v119\n\t"
    "s_nop 0\n\t"
    "s_cmp_ge_u32 s46, 2\n\t"
    "s_cbranch_scc1 L_ago0_%=\n\t"
    "s_sleep 1\n\t"
    "s_branch L_asp0_%=\n\t"
    "L_ago0_%=:\n\t"
    "ds_read_u16_d16_hi v0, v92 offset:208\n\t"
    "ds_read_u16_d16_hi v28, v93 offset:802\n\t"
    "ds_read_u16_d16_hi v1, v92 offset:1408\n\t"
    "ds_read_u16_d16_hi v29, v93 offset:814\n\t"
    "ds_read_u16_d16_hi v2, v92 offset:2608\n\t"
    "ds_read_u16_d16_hi v30, v93 offset:826\n\t"
    "ds_read_u16_d16_hi v3, v92 offset:3808\n\t"
    "ds_read_u16_d16_hi v31, v93 offset:838\n\t"
    "ds_read_u16_d16_hi v4, v92 offset:410\n\t"
    "ds_read_u16_d16_hi v32, v93 offset:1004\n\t"
    "ds_read_u16_d16_hi v5, v92 offset:1610\n\t"
    "ds_read_u16_d16_hi v33, v93 offset:1016\n\t"
    "ds_read_u16_d16_hi v6, v92 offset:2810\n\t"
    "ds_read_u16_d16_hi v34, v93 offset:1028\n\t"
    "ds_read_u16_d16_hi v7, v92 offset:4010\n\t"
    "ds_read_u16_d16_hi v35, v93 offset:1040\n\t"
    "ds_read_u16_d16_hi v8, v92 offset:612\n\t"
    "ds_read_u16_d16_hi v36, v93 offset:1206\n\t"
    "ds_read_u16_d16_hi v9, v92 offset:1812\n\t"
    "ds_read_u16_d16_hi v37, v93 offset:1218\n\t"
    "ds_read_u16_d16_hi v10, v92 offset:3012\n\t"
    "ds_read_u16_d16_hi v38, v93 offset:1230\n\t"
    "ds_read_u16_d16_hi v11, v92 offset:4212\n\t"
    "ds_read_u16_d16_hi v39, v93 offset:1242\n\t"
    "ds_read_u16_d16_hi v12, v92 offset:0\n\t"
    "ds_read_u16_d16_hi v13, v92 offset:2\n\t"
    "ds_read_u16_d16_hi v41, v93 offset:200\n\t"
    "ds_read_u16_d16_hi v14, v92 offset:4\n\t"
    "ds_read_u16_d16_hi v42, v93 offset:400\n\t"
    "ds_read_u16_d16_hi v15, v92 offset:6\n\t"
    "ds_read_u16_d16_hi v43, v93 offset:600\n\t"
    "ds_read_u16_d16_hi v16, v92 offset:8\n\t"
    "ds_read_u16_d16_hi v44, v93 offset:800\n\t"
    "ds_read_u16_d16_hi v17, v92 offset:10\n\t"
    "ds_read_u16_d16_hi v45, v93 offset:1000\n\t"
    "ds_read_u16_d16_hi v18, v92 offset:12\n\t"
    "ds_read_u16_d16_hi v46, v93 offset:1200\n\t"
    "ds_add_u32 v117, v118 offset:0\n\t"
    "s_waitcnt lgkmcnt(15)\n\t"
    "v_add_f32 v0, v0, v28\n\t"
    "v_add_f32 v1, v1, v29\n\t"
    "v_add_f32 v2, v2, v30\n\t"
    "v_add_f32 v3, v3, v31\n\t"
    "v_add_f32 v4, v4, v32\n\t"
    "v_add_f32 v5, v5, v33\n\t"
    "v_add_f32 v6, v6, v34\n\t"
    "v_add_f32 v7, v7, v35\n\t"
    "s_waitcnt lgkmcnt(14)\n\t"
    "v_add_f32 v8, v8, v36\n\t"
    "v_add_f32 v9, v9, v37\n\t"
    "v_add_f32 v10, v10, v38\n\t"
    "v_add_f32 v11, v11, v39\n\t"
    "s_waitcnt lgkmcnt(7)\n\t"
    "v_cndmask_b32 v12, 0, v12, s[62:63]\n\t"
    "v_add_f32 v13, v13, v41\n\t"
    "v_add_f32 v14, v14, v42\n\t"
    "v_add_f32 v15, v15, v43\n\t"
    "s_waitcnt lgkmcnt(1)\n\t"
    "v_add_f32 v16, v16, v44\n\t"
    "v_add_f32 v17, v17, v45\n\t"
    "v_add_f32 v18, v18, v46\n\t"
    "s_call_b64 s[98:99], L_setup_%=\n\t"
    "L_asp1_%=:\n\t"
    "ds_read_b32 v119, v117 offset:0\n\t"
    "s_waitcnt lgkmcnt(0)\n\t"
    "v_readfirstlane_b32 s46, v119\n\t"
    "s_nop 0\n\t"
    "s_cmp_ge_u32 s46, 4\n\t"
    "s_cbranch_scc1 L_ago1_%=\n\t"
    "s_sleep 1\n\t"
    "s_branch L_asp1_%=\n\t"
    "L_ago1_%=:\n\t"
    "ds_write_b128 v95, v[0:3] offset:6144\n\t"
    "ds_write_b128 v95, v[4:7] offset:10240\n\t"
    "ds_write_b128 v95, v[8:11] offset:14336\n\t"
    "ds_write_b128 v95, v[12:15] offset:21504\n\t"
    "ds_write_b128 v95, v[16:19] offset:22528\n\t"
    "ds_add_u32 v117, v118 offset:4\n\t"
    "s_branch L_ab0_%=\n\t"
    "L_afold1_%=:\n\t"
    "L_asp2_%=:\n\t"
    "ds_read_b32 v119, v117 offset:1044\n\t"
    "s_waitcnt lgkmcnt(0)\n\t"
    "v_readfirstlane_b32 s46, v119\n\t"
    "s_nop 0\n\t"
    "s_cmp_ge_u32 s46, 2\n\t"
    "s_cbranch_scc1 L_ago2_%=\n\t"
    "s_sleep 1\n\t"
    "s_branch L_asp2_%=\n\t"
    "L_ago2_%=:\n\t"
    "ds_read_u16_d16_hi v0, v92 offset:204\n\t"
    "ds_read_u16_d16_hi v28, v93 offset:402\n\t"
    "ds_read_u16_d16_hi v1, v92 offset:1404\n\t"
    "ds_read_u16_d16_hi v29, v93 offset:414\n\t"
    "ds_read_u16_d16_hi v2, v92 offset:2604\n\t"
    "ds_read_u16_d16_hi v30, v93 offset:426\n\t"
    "ds_read_u16_d16_hi v3, v92 offset:3804\n\t"
    "ds_read_u16_d16_hi v31, v93 offset:438\n\t"
    "ds_read_u16_d16_hi v4, v92 offset:408\n\t"
    "ds_read_u16_d16_hi v32, v93 offset:804\n\t"
    "ds_read_u16_d16_hi v5, v92 offset:1608\n\t"
    "ds_read_u16_d16_hi v33, v93 offset:816\n\t"
    "ds_read_u16_d16_hi v6, v92 offset:2808\n\t"
    "ds_read_u16_d16_hi v34, v93 offset:828\n\t"
    "ds_read_u16_d16_hi v7, v92 offset:4008\n\t"
    "ds_read_u16_d16_hi v35, v93 offset:840\n\t"
    "ds_read_u16_d16_hi v8, v92 offset:412\n\t"
    "ds_read_u16_d16_hi v36, v93 offset:1204\n\t"
    "ds_read_u16_d16_hi v9, v92 offset:1612\n\t"
    "ds_read_u16_d16_hi v37, v93 offset:1216\n\t"
    "ds_read_u16_d16_hi v10, v92 offset:2812\n\t"
    "ds_read_u16_d16_hi v38, v93 offset:1228\n\t"
    "ds_read_u16_d16_hi v11, v92 offset:4012\n\t"
    "ds_read_u16_d16_hi v39, v93 offset:1240\n\t"
    "ds_read_u16_d16_hi v12, v92 offset:810\n\t"
    "ds_read_u16_d16_hi v40, v93 offset:1008\n\t"
    "ds_read_u16_d16_hi v13, v92 offset:2010\n\t"
    "ds_read_u16_d16_hi v41, v93 offset:1020\n\t"
    "ds_read_u16_d16_hi v14, v92 offset:3210\n\t"
    "ds_read_u16_d16_hi v42, v93 offset:1032\n\t"
    "ds_read_u16_d16_hi v15, v92 offset:4410\n\t"
    "ds_read_u16_d16_hi v43, v93 offset:1044\n\t"
    "ds_read_u16_d16_hi v16, v92 offset:202\n\t"
    "ds_read_u16_d16_hi v17, v92 offset:1402\n\t"
    "ds_read_u16_d16_hi v18, v92 offset:2602\n\t"
    "ds_read_u16_d16_hi v19, v92 offset:3802\n\t"
    "ds_read_u16_d16_hi v20, v92 offset:808\n\t"
    "ds_read_u16_d16_hi v21, v92 offset:2008\n\t"
    "ds_read_u16_d16_hi v22, v92 offset:3208\n\t"
    "ds_read_u16_d16_hi v23, v92 offset:4408\n\t"
    "ds_add_u32 v117, v118 offset:0\n\t"
    "s_waitcnt lgkmcnt(15)\n\t"
    "v_add_f32 v0, v0, v28\n\t"
    "v_add_f32 v1, v1, v29\n\t"
    "v_add_f32 v2, v2, v30\n\t"
    "v_add_f32 v3, v3, v31\n\t"
    "v_add_f32 v4, v4, v32\n\t"
    "v_add_f32 v5, v5, v33\n\t"
    "v_add_f32 v6, v6, v34\n\t"
    "v_add_f32 v7, v7, v35\n\t"
    "v_add_f32 v8, v8, v36\n\t"
    "v_add_f32 v9, v9, v37\n\t"
    "v_add_f32 v10, v10, v38\n\t"
    "v_add_f32 v11, v11, v39\n\t"
    "s_waitcnt lgkmcnt(9)\n\t"
    "v_add_f32 v12, v12, v40\n\t"
    "v_add_f32 v13, v13, v41\n\t"
    "v_add_f32 v14, v14, v42\n\t"
    "v_add_f32 v15, v15, v43\n\t"
    "s_waitcnt lgkmcnt(5)\n\t"
    "s_waitcnt lgkmcnt(1)\n\t"
    "s_call_b64 s[98:99], L_setup_%=\n\t"
    "L_asp3_%=:\n\t"
    "ds_read_b32 v119, v117 offset:0\n\t"
    "s_waitcnt lgkmcnt(0)\n\t"
    "v_readfirstlane_b32 s46, v119\n\t"
    "s_nop 0\n\t"
    "s_cmp_ge_u32 s46, 4\n\t"
    "s_cbranch_scc1 L_ago3_%=\n\t"
    "s_sleep 1\n\t"
    "s_branch L_asp3_%=\n\t"
    "L_ago3_%=:\n\t"
    "ds_write_b128 v95, v[0:3] offset:1024\n\t"
    "ds_write_b128 v95, v[4:7] offset:7168\n\t"
    "ds_write_b128 v95, v[8:11] offset:11264\n\t"
    "ds_write_b128 v95, v[12:15] offset:16384\n\t"
    "ds_write_b128 v95, v[16:19] offset:0\n\t"
    "ds_write_b128 v95, v[20:23] offset:15360\n\t"
    "ds_add_u32 v117, v118 offset:4\n\t"
    "s_branch L_ab0_%=\n\t"
    "L_afold2_%=:\n\t"
    "s_call_b64 s[98:99], L_setup_%=\n\t"
    "L_asp4_%=:\n\t"
    "ds_read_b32 v119, v117 offset:1044\n\t"
    "s_waitcnt lgkmcnt(0)\n\t"
    "v_readfirstlane_b32 s46, v119\n\t"
    "s_nop 0\n\t"
    "s_cmp_ge_u32 s46, 2\n\t"
    "s_cbranch_scc1 L_ago4_%=\n\t"
    "s_sleep 1\n\t"
    "s_branch L_asp4_%=\n\t"
    "L_ago4_%=:\n\t"
    "ds_read_u16_d16_hi v0, v92 offset:206\n\t"
    "ds_read_u16_d16_hi v28, v93 offset:602\n\t"
    "ds_read_u16_d16_hi v1, v92 offset:1406\n\t"
    "ds_read_u16_d16_hi v29, v93 offset:614\n\t"
    "ds_read_u16_d16_hi v2, v92 offset:2606\n\t"
    "ds_read_u16_d16_hi v30, v93 offset:626\n\t"
    "ds_read_u16_d16_hi v3, v92 offset:3806\n\t"
    "ds_read_u16_d16_hi v31, v93 offset:638\n\t"
    "ds_read_u16_d16_hi v4, v92 offset:210\n\t"
    "ds_read_u16_d16_hi v32, v93 offset:1002\n\t"
    "ds_read_u16_d16_hi v5, v92 offset:1410\n\t"
    "ds_read_u16_d16_hi v33, v93 offset:1014\n\t"
    "ds_read_u16_d16_hi v6, v92 offset:2610\n\t"
    "ds_read_u16_d16_hi v34, v93 offset:1026\n\t"
    "ds_read_u16_d16_hi v7, v92 offset:3810\n\t"
    "ds_read_u16_d16_hi v35, v93 offset:1038\n\t"
    "ds_read_u16_d16_hi v8, v92 offset:608\n\t"
    "ds_read_u16_d16_hi v36, v93 offset:806\n\t"
    "ds_read_u16_d16_hi v9, v92 offset:1808\n\t"
    "ds_read_u16_d16_hi v37, v93 offset:818\n\t"
    "ds_read_u16_d16_hi v10, v92 offset:3008\n\t"
    "ds_read_u16_d16_hi v38, v93 offset:830\n\t"
    "ds_read_u16_d16_hi v11, v92 offset:4208\n\t"
    "ds_read_u16_d16_hi v39, v93 offset:842\n\t"
    "ds_read_u16_d16_hi v12, v92 offset:812\n\t"
    "ds_read_u16_d16_hi v40, v93 offset:1208\n\t"
    "ds_read_u16_d16_hi v13, v92 offset:2012\n\t"
    "ds_read_u16_d16_hi v41, v93 offset:1220\n\t"
    "ds_read_u16_d16_hi v14, v92 offset:3212\n\t"
    "ds_read_u16_d16_hi v42, v93 offset:1232\n\t"
    "ds_read_u16_d16_hi v15, v92 offset:4412\n\t"
    "ds_read_u16_d16_hi v43, v93 offset:1244\n\t"
    "ds_read_u16_d16_hi v16, v92 offset:404\n\t"
    "ds_read_u16_d16_hi v17, v92 offset:1604\n\t"
    "ds_read_u16_d16_hi v18, v92 offset:2804\n\t"
    "ds_read_u16_d16_hi v19, v92 offset:4004\n\t"
    "ds_read_u16_d16_hi v20, v92 offset:1010\n\t"
    "ds_read_u16_d16_hi v21, v92 offset:2210\n\t"
    "ds_read_u16_d16_hi v22, v92 offset:3410\n\t"
    "ds_read_u16_d16_hi v23, v92 offset:4610\n\t"
    "ds_add_u32 v117, v118 offset:0\n\t"
    "s_waitcnt lgkmcnt(15)\n\t"
    "v_add_f32 v0, v0, v28\n\t"
    "v_add_f32 v1, v1, v29\n\t"
    "v_add_f32 v2, v2, v30\n\t"
    "v_add_f32 v3, v3, v31\n\t"
    "v_add_f32 v4, v4, v32\n\t"
    "v_add_f32 v5, v5, v33\n\t"
    "v_add_f32 v6, v6, v34\n\t"
    "v_add_f32 v7, v7, v35\n\t"
    "v_add_f32 v8, v8, v36\n\t"
    "v_add_f32 v9, v9, v37\n\t"
    "v_add_f32 v10, v10, v38\n\t"
    "v_add_f32 v11, v11, v39\n\t"
    "s_waitcnt lgkmcnt(9)\n\t"
    "v_add_f32 v12, v12, v40\n\t"
    "v_add_f32 v13, v13, v41\n\t"
    "v_add_f32 v14, v14, v42\n\t"
    "v_add_f32 v15, v15, v43\n\t"
    "s_waitcnt lgkmcnt(5)\n\t"
    "s_waitcnt lgkmcnt(1)\n\t"
    "L_asp5_%=:\n\t"
    "ds_read_b32 v119, v117 offset:0\n\t"
    "s_waitcnt lgkmcnt(0)\n\t"
    "v_readfirstlane_b32 s46, v119\n\t"
    "s_nop 0\n\t"
    "s_cmp_ge_u32 s46, 4\n\t"
    "s_cbranch_scc1 L_ago5_%=\n\t"
    "s_sleep 1\n\t"
    "s_branch L_asp5_%=\n\t"
    "L_ago5_%=:\n\t"
    "ds_write_b128 v95, v[0:3] offset:3072\n\t"
    "ds_write_b128 v95, v[4:7] offset:8192\n\t"
    "ds_write_b128 v95, v[8:11] offset:12288\n\t"
    "ds_write_b128 v95, v[12:15] offset:17408\n\t"
    "ds_write_b128 v95, v[16:19] offset:2048\n\t"
    "ds_write_b128 v95, v[20:23] offset:18432\n\t"
    "ds_add_u32 v117, v118 offset:4\n\t"
    "s_branch L_ab0_%=\n\t"
    "L_afold3_%=:\n\t"
    "s_call_b64 s[98:99], L_setup_%=\n\t"
    "L_asp6_%=:\n\t"
    "ds_read_b32 v119, v117 offset:1044\n\t"
    "s_waitcnt lgkmcnt(0)\n\t"
    "v_readfirstlane_b32 s46, v119\n\t"
    "s_nop 0\n\t"
    "s_cmp_ge_u32 s46, 2\n\t"
    "s_cbranch_scc1 L_ago6_%=\n\t"
    "s_sleep 1\n\t"
    "s_branch L_asp6_%=\n\t"
    "L_ago6_%=:\n\t"
    "ds_read_u16_d16_hi v0, v92 offset:406\n\t"
    "ds_read_u16_d16_hi v28, v93 offset:604\n\t"
    "ds_read_u16_d16_hi v1, v92 offset:1606\n\t"
    "ds_read_u16_d16_hi v29, v93 offset:616\n\t"
    "ds_read_u16_d16_hi v2, v92 offset:2806\n\t"
    "ds_read_u16_d16_hi v30, v93 offset:628\n\t"
    "ds_read_u16_d16_hi v3, v92 offset:4006\n\t"
    "ds_read_u16_d16_hi v31, v93 offset:640\n\t"
    "ds_read_u16_d16_hi v4, v92 offset:212\n\t"
    "ds_read_u16_d16_hi v32, v93 offset:1202\n\t"
    "ds_read_u16_d16_hi v5, v92 offset:1412\n\t"
    "ds_read_u16_d16_hi v33, v93 offset:1214\n\t"
    "ds_read_u16_d16_hi v6, v92 offset:2612\n\t"
    "ds_read_u16_d16_hi v34, v93 offset:1226\n\t"
    "ds_read_u16_d16_hi v7, v92 offset:3812\n\t"
    "ds_read_u16_d16_hi v35, v93 offset:1238\n\t"
    "ds_read_u16_d16_hi v8, v92 offset:610\n\t"
    "ds_read_u16_d16_hi v36, v93 offset:1006\n\t"
    "ds_read_u16_d16_hi v9, v92 offset:1810\n\t"
    "ds_read_u16_d16_hi v37, v93 offset:1018\n\t"
    "ds_read_u16_d16_hi v10, v92 offset:3010\n\t"
    "ds_read_u16_d16_hi v38, v93 offset:1030\n\t"
    "ds_read_u16_d16_hi v11, v92 offset:4210\n\t"
    "ds_read_u16_d16_hi v39, v93 offset:1042\n\t"
    "ds_read_u16_d16_hi v12, v92 offset:1012\n\t"
    "ds_read_u16_d16_hi v40, v93 offset:1210\n\t"
    "ds_read_u16_d16_hi v13, v92 offset:2212\n\t"
    "ds_read_u16_d16_hi v41, v93 offset:1222\n\t"
    "ds_read_u16_d16_hi v14, v92 offset:3412\n\t"
    "ds_read_u16_d16_hi v42, v93 offset:1234\n\t"
    "ds_read_u16_d16_hi v15, v92 offset:4612\n\t"
    "ds_read_u16_d16_hi v43, v93 offset:1246\n\t"
    "ds_read_u16_d16_hi v16, v92 offset:606\n\t"
    "ds_read_u16_d16_hi v17, v92 offset:1806\n\t"
    "ds_read_u16_d16_hi v18, v92 offset:3006\n\t"
    "ds_read_u16_d16_hi v19, v92 offset:4206\n\t"
    "ds_read_u16_d16_hi v20, v92 offset:1212\n\t"
    "ds_read_u16_d16_hi v21, v92 offset:2412\n\t"
    "ds_read_u16_d16_hi v22, v92 offset:3612\n\t"
    "ds_read_u16_d16_hi v23, v92 offset:4812\n\t"
    "ds_add_u32 v117, v118 offset:0\n\t"
    "s_waitcnt lgkmcnt(15)\n\t"
    "v_add_f32 v0, v0, v28\n\t"
    "v_add_f32 v1, v1, v29\n\t"
    "v_add_f32 v2, v2, v30\n\t"
    "v_add_f32 v3, v3, v31\n\t"
    "v_add_f32 v4, v4, v32\n\t"
    "v_add_f32 v5, v5, v33\n\t"
    "v_add_f32 v6, v6, v34\n\t"
    "v_add_f32 v7, v7, v35\n\t"
    "v_add_f32 v8, v8, v36\n\t"
    "v_add_f32 v9, v9, v37\n\t"
    "v_add_f32 v10, v10, v38\n\t"
    "v_add_f32 v11, v11, v39\n\t"
    "s_waitcnt lgkmcnt(9)\n\t"
    "v_add_f32 v12, v12, v40\n\t"
    "v_add_f32 v13, v13, v41\n\t"
    "v_add_f32 v14, v14, v42\n\t"
    "v_add_f32 v15, v15, v43\n\t"
    "s_waitcnt lgkmcnt(5)\n\t"
    "s_waitcnt lgkmcnt(1)\n\t"
    "L_asp7_%=:\n\t"
    "ds_read_b32 v119, v117 offset:0\n\t"
    "s_waitcnt lgkmcnt(0)\n\t"
    "v_readfirstlane_b32 s46, v119\n\t"
    "s_nop 0\n\t"
    "s_cmp_ge_u32 s46, 4\n\t"
    "s_cbranch_scc1 L_ago7_%=\n\t"
    "s_sleep 1\n\t"
    "s_branch L_asp7_%=\n\t"
    "L_ago7_%=:\n\t"
    "ds_write_b128 v95, v[0:3] offset:4096\n\t"
    "ds_write_b128 v95, v[4:7] offset:9216\n\t"
    "ds_write_b128 v95, v[8:11] offset:13312\n\t"
    "ds_write_b128 v95, v[12:15] offset:19456\n\t"
    "ds_write_b128 v95, v[16:19] offset:5120\n\t"
    "ds_write_b128 v95, v[20:23] offset:20480\n\t"
    "ds_add_u32 v117, v118 offset:4\n\t"
    "L_ab0_%=:\n\t"
    "s_mul_i32 m0, s69, 1040\n\t"
    "s_lshl_b32 s44, s69, 11\n\t"
    "buffer_load_dwordx4 v126, %15, s44 offen lds\n\t"
    "s_add_u32 s69, s69, 4\n\t"
    "s_sub_u32 s44, s69, 125\n\t"
    "s_cmp_ge_u32 s69, 125\n\t"
    "s_cselect_b32 s69, s44, s69\n\t"
    "s_mul_i32 m0, s69, 1040\n\t"
    "s_lshl_b32 s44, s69, 11\n\t"
    "buffer_load_dwordx4 v126, %15, s44 offen lds\n\t"
    "s_add_u32 s69, s69, 4\n\t"
    "s_sub_u32 s44, s69, 125\n\t"
    "s_cmp_ge_u32 s69, 125\n\t"
    "s_cselect_b32 s69, s44, s69\n\t"
    "s_mul_i32 m0, s69, 1040\n\t"
    "s_lshl_b32 s44, s69, 11\n\t"
    "buffer_load_dwordx4 v126, %15, s44 offen lds\n\t"
    "s_add_u32 s69, s69, 4\n\t"
    "s_sub_u32 s44, s69, 125\n\t"
    "s_cmp_ge_u32 s69, 125\n\t"
    "s_cselect_b32 s69, s44, s69\n\t"
    "s_mul_i32 m0, s69, 1040\n\t"
    "s_lshl_b32 s44, s69, 11\n\t"
    "buffer_load_dwordx4 v126, %15, s44 offen lds\n\t"
    "s_add_u32 s69, s69, 4\n\t"
    "s_sub_u32 s44, s69, 125\n\t"
    "s_cmp_ge_u32 s69, 125\n\t"
    "s_cselect_b32 s69, s44, s69\n\t"
    "s_mul_i32 m0, s69, 1040\n\t"
    "s_lshl_b32 s44, s69, 11\n\t"
    "buffer_load_dwordx4 v126, %15, s44 offen lds\n\t"
    "s_add_u32 s69, s69, 4\n\t"
    "s_sub_u32 s44, s69, 125\n\t"
    "s_cmp_ge_u32 s69, 125\n\t"
    "s_cselect_b32 s69, s44, s69\n\t"
    "s_branch L_ab_%=\n\t"
    "L_adma_%=:\n\t"
    "s_sub_u32 s46, %16, 4\n\t"
    "s_add_u32 s69, s51, s46\n\t"
    "s_sub_u32 s44, s69, 125\n\t"
    "s_cmp_ge_u32 s69, 125\n\t"
    "s_cselect_b32 s69, s44, s69\n\t"
    "s_mul_i32 m0, s69, 1040\n\t"
    "s_lshl_b32 s44, s69, 11\n\t"
    "buffer_load_dwordx4 v126, %15, s44 offen lds\n\t"
    "s_add_u32 s69, s69, 2\n\t"
    "s_sub_u32 s44, s69, 125\n\t"
    "s_cmp_ge_u32 s69, 125\n\t"
    "s_cselect_b32 s69, s44, s69\n\t"
    "s_mul_i32 m0, s69, 1040\n\t"
    "s_lshl_b32 s44, s69, 11\n\t"
    "buffer_load_dwordx4 v126, %15, s44 offen lds\n\t"
    "s_add_u32 s69, s69, 2\n\t"
    "s_sub_u32 s44, s69, 125\n\t"
    "s_cmp_ge_u32 s69, 125\n\t"
    "s_cselect_b32 s69, s44, s69\n\t"
    "s_barrier\n\t"
    "s_load_dwordx2 s[72:73], %6, 0\n\t"
    "s_load_dwordx2 s[88:89], %7, 0\n\t"
    "s_load_dword s91, %8, 0\n\t"
    "s_mul_i32 m0, s69, 1040\n\t"
    "s_lshl_b32 s44, s69, 11\n\t"
    "buffer_load_dwordx4 v126, %15, s44 offen lds\n\t"
    "s_add_u32 s69, s69, 2\n\t"
    "s_sub_u32 s44, s69, 125\n\t"
    "s_cmp_ge_u32 s69, 125\n\t"
    "s_cselect_b32 s69, s44, s69\n\t"
    "s_mul_i32 m0, s69, 1040\n\t"
    "s_lshl_b32 s44, s69, 11\n\t"
    "buffer_load_dwordx4 v126, %15, s44 offen lds\n\t"
    "s_add_u32 s69, s69, 2\n\t"
    "s_sub_u32 s44, s69, 125\n\t"
    "s_cmp_ge_u32 s69, 125\n\t"
    "s_cselect_b32 s69, s44, s69\n\t"
    "s_mul_i32 m0, s69, 1040\n\t"
    "s_lshl_b32 s44, s69, 11\n\t"
    "buffer_load_dwordx4 v126, %15, s44 offen lds\n\t"
    "s_add_u32 s69, s69, 2\n\t"
    "s_sub_u32 s44, s69, 125\n\t"
    "s_cmp_ge_u32 s69, 125\n\t"
    "s_cselect_b32 s69, s44, s69\n\t"
    "s_mul_i32 m0, s69, 1040\n\t"
    "s_lshl_b32 s44, s69, 11\n\t"
    "buffer_load_dwordx4 v126, %15, s44 offen lds\n\t"
    "s_add_u32 s69, s69, 2\n\t"
    "s_sub_u32 s44, s69, 125\n\t"
    "s_cmp_ge_u32 s69, 125\n\t"
    "s_cselect_b32 s69, s44, s69\n\t"
    "s_mul_i32 m0, s69, 1040\n\t"
    "s_lshl_b32 s44, s69, 11\n\t"
    "buffer_load_dwordx4 v126, %15, s44 offen lds\n\t"
    "s_add_u32 s69, s69, 2\n\t"
    "s_sub_u32 s44, s69, 125\n\t"
    "s_cmp_ge_u32 s69, 125\n\t"
    "s_cselect_b32 s69, s44, s69\n\t"
    "s_mul_i32 m0, s69, 1040\n\t"
    "s_lshl_b32 s44, s69, 11\n\t"
    "buffer_load_dwordx4 v126, %15, s44 offen lds\n\t"
    "s_add_u32 s69, s69, 2\n\t"
    "s_sub_u32 s44, s69, 125\n\t"
    "s_cmp_ge_u32 s69, 125\n\t"
    "s_cselect_b32 s69, s44, s69\n\t"
    "s_mul_i32 m0, s69, 1040\n\t"
    "s_lshl_b32 s44, s69, 11\n\t"
    "buffer_load_dwordx4 v126, %15, s44 offen lds\n\t"
    "s_add_u32 s69, s69, 2\n\t"
    "s_sub_u32 s44, s69, 125\n\t"
    "s_cmp_ge_u32 s69, 125\n\t"
    "s_cselect_b32 s69, s44, s69\n\t"
    "s_mul_i32 m0, s69, 1040\n\t"
    "s_lshl_b32 s44, s69, 11\n\t"
    "buffer_load_dwordx4 v126, %15, s44 offen lds\n\t"
    "s_add_u32 s69, s69, 2\n\t"
    "s_sub_u32 s44, s69, 125\n\t"
    "s_cmp_ge_u32 s69, 125\n\t"
    "s_cselect_b32 s69, s44, s69\n\t"
    "s_mul_i32 m0, s69, 1040\n\t"
    "s_lshl_b32 s44, s69, 11\n\t"
    "buffer_load_dwordx4 v126, %15, s44 offen lds\n\t"
    "s_add_u32 s69, s69, 2\n\t"
    "s_sub_u32 s44, s69, 125\n\t"
    "s_cmp_ge_u32 s69, 125\n\t"
    "s_cselect_b32 s69, s44, s69\n\t"
    "s_mul_i32 m0, s69, 1040\n\t"
    "s_lshl_b32 s44, s69, 11\n\t"
    "buffer_load_dwordx4 v126, %15, s44 offen lds\n\t"
    "s_add_u32 s69, s69, 2\n\t"
    "s_sub_u32 s44, s69, 125\n\t"
    "s_cmp_ge_u32 s69, 125\n\t"
    "s_cselect_b32 s69, s44, s69\n\t"
    "v_bfe_u32 v154, v147, 2, 2\n\t"
    "v_and_b32 v155, 3, v147\n\t"
    "v_mul_u32_u24 v154, 25, v154\n\t"
    "v_mad_u32_u24 v154, v155, 6, v154\n\t"
    "v_lshlrev_b32 v153, 2, v154\n\t"
    "s_and_b32 s53, s48, 0xff\n\t"
    "s_bfe_u32 s55, s48, 0x80008\n\t"
    "s_add_u32 s54, s53, s55\n\t"
    "s_add_u32 s55, s54, s55\n\t"
    "s_lshr_b32 s64, s48, 16\n\t"
    "v_cmp_eq_u32 s[62:63], 0, v155\n\t"
    "v_and_b32 v151, 48, v147\n\t"
    "v_or_b32 v155, 0x100000, v151\n\t"
    "v_cndmask_b32 v152, v155, v151, s[62:63]\n\t"
    "v_add_u32 v95, 140288, v122\n\t"
    "s_mul_i32 m0, s69, 1040\n\t"
    "s_lshl_b32 s44, s69, 11\n\t"
    "buffer_load_dwordx4 v126, %15, s44 offen lds\n\t"
    "s_add_u32 s69, s69, 2\n\t"
    "s_sub_u32 s44, s69, 125\n\t"
    "s_cmp_ge_u32 s69, 125\n\t"
    "s_cselect_b32 s69, s44, s69\n\t"
    "s_mul_i32 m0, s69, 1040\n\t"
    "s_lshl_b32 s44, s69, 11\n\t"
    "buffer_load_dwordx4 v126, %15, s44 offen lds\n\t"
    "s_add_u32 s69, s69, 2\n\t"
    "s_sub_u32 s44, s69, 125\n\t"
    "s_cmp_ge_u32 s69, 125\n\t"
    "s_cselect_b32 s69, s44, s69\n\t"
    "s_mul_i32 m0, s69, 1040\n\t"
    "s_lshl_b32 s44, s69, 11\n\t"
    "buffer_load_dwordx4 v126, %15, s44 offen lds\n\t"
    "s_add_u32 s69, s69, 2\n\t"
    "s_sub_u32 s44, s69, 125\n\t"
    "s_cmp_ge_u32 s69, 125\n\t"
    "s_cselect_b32 s69, s44, s69\n\t"
    "s_mul_i32 m0, s69, 1040\n\t"
    "s_lshl_b32 s44, s69, 11\n\t"
    "buffer_load_dwordx4 v126, %15, s44 offen lds\n\t"
    "s_add_u32 s69, s69, 2\n\t"
    "s_sub_u32 s44, s69, 125\n\t"
    "s_cmp_ge_u32 s69, 125\n\t"
    "s_cselect_b32 s69, s44, s69\n\t"
    "s_mul_i32 m0, s69, 1040\n\t"
    "s_lshl_b32 s44, s69, 11\n\t"
    "buffer_load_dwordx4 v126, %15, s44 offen lds\n\t"
    "s_add_u32 s69, s69, 2\n\t"
    "s_sub_u32 s44, s69, 125\n\t"
    "s_cmp_ge_u32 s69, 125\n\t"
    "s_cselect_b32 s69, s44, s69\n\t"
    "s_mul_i32 m0, s69, 1040\n\t"
    "s_lshl_b32 s44, s69, 11\n\t"
    "buffer_load_dwordx4 v126, %15, s44 offen lds\n\t"
    "s_add_u32 s69, s69, 2\n\t"
    "s_sub_u32 s44, s69, 125\n\t"
    "s_cmp_ge_u32 s69, 125\n\t"
    "s_cselect_b32 s69, s44, s69\n\t"
    "s_mul_i32 m0, s69, 1040\n\t"
    "s_lshl_b32 s44, s69, 11\n\t"
    "buffer_load_dwordx4 v126, %15, s44 offen lds\n\t"
    "s_add_u32 s69, s69, 2\n\t"
    "s_sub_u32 s44, s69, 125\n\t"
    "s_cmp_ge_u32 s69, 125\n\t"
    "s_cselect_b32 s69, s44, s69\n\t"
    "s_mul_i32 m0, s69, 1040\n\t"
    "s_lshl_b32 s44, s69, 11\n\t"
    "buffer_load_dwordx4 v126, %15, s44 offen lds\n\t"
    "s_add_u32 s69, s69, 2\n\t"
    "s_sub_u32 s44, s69, 125\n\t"
    "s_cmp_ge_u32 s69, 125\n\t"
    "s_cselect_b32 s69, s44, s69\n\t"
    "s_mul_i32 m0, s69, 1040\n\t"
    "s_lshl_b32 s44, s69, 11\n\t"
    "buffer_load_dwordx4 v126, %15, s44 offen lds\n\t"
    "s_add_u32 s69, s69, 2\n\t"
    "s_sub_u32 s44, s69, 125\n\t"
    "s_cmp_ge_u32 s69, 125\n\t"
    "s_cselect_b32 s69, s44, s69\n\t"
    "s_mul_i32 m0, s69, 1040\n\t"
    "s_lshl_b32 s44, s69, 11\n\t"
    "buffer_load_dwordx4 v126, %15, s44 offen lds\n\t"
    "s_add_u32 s69, s69, 2\n\t"
    "s_sub_u32 s44, s69, 125\n\t"
    "s_cmp_ge_u32 s69, 125\n\t"
    "s_cselect_b32 s69, s44, s69\n\t"
    "s_mul_i32 m0, s69, 1040\n\t"
    "s_lshl_b32 s44, s69, 11\n\t"
    "buffer_load_dwordx4 v126, %15, s44 offen lds\n\t"
    "s_add_u32 s69, s69, 2\n\t"
    "s_sub_u32 s44, s69, 125\n\t"
    "s_cmp_ge_u32 s69, 125\n\t"
    "s_cselect_b32 s69, s44, s69\n\t"
    "s_mul_i32 m0, s69, 1040\n\t"
    "s_lshl_b32 s44, s69, 11\n\t"
    "buffer_load_dwordx4 v126, %15, s44 offen lds\n\t"
    "s_add_u32 s69, s69, 2\n\t"
    "s_sub_u32 s44, s69, 125\n\t"
    "s_cmp_ge_u32 s69, 125\n\t"
    "s_cselect_b32 s69, s44, s69\n\t"
    "s_mul_i32 m0, s69, 1040\n\t"
    "s_lshl_b32 s44, s69, 11\n\t"
    "buffer_load_dwordx4 v126, %15, s44 offen lds\n\t"
    "s_add_u32 s69, s69, 2\n\t"
    "s_sub_u32 s44, s69, 125\n\t"
    "s_cmp_ge_u32 s69, 125\n\t"
    "s_cselect_b32 s69, s44, s69\n\t"
    "s_mul_i32 m0, s69, 1040\n\t"
    "s_lshl_b32 s44, s69, 11\n\t"
    "buffer_load_dwordx4 v126, %15, s44 offen lds\n\t"
    "s_add_u32 s69, s69, 2\n\t"
    "s_sub_u32 s44, s69, 125\n\t"
    "s_cmp_ge_u32 s69, 125\n\t"
    "s_cselect_b32 s69, s44, s69\n\t"
    "s_mul_i32 m0, s69, 1040\n\t"
    "s_lshl_b32 s44, s69, 11\n\t"
    "buffer_load_dwordx4 v126, %15, s44 offen lds\n\t"
    "s_add_u32 s69, s69, 2\n\t"
    "s_sub_u32 s44, s69, 125\n\t"
    "s_cmp_ge_u32 s69, 125\n\t"
    "s_cselect_b32 s69, s44, s69\n\t"
    "s_mul_i32 m0, s69, 1040\n\t"
    "s_lshl_b32 s44, s69, 11\n\t"
    "buffer_load_dwordx4 v126, %15, s44 offen lds\n\t"
    "s_add_u32 s69, s69, 2\n\t"
    "s_sub_u32 s44, s69, 125\n\t"
    "s_cmp_ge_u32 s69, 125\n\t"
    "s_cselect_b32 s69, s44, s69\n\t"
    "s_mul_i32 m0, s69, 1040\n\t"
    "s_lshl_b32 s44, s69, 11\n\t"
    "buffer_load_dwordx4 v126, %15, s44 offen lds\n\t"
    "s_add_u32 s69, s69, 2\n\t"
    "s_sub_u32 s44, s69, 125\n\t"
    "s_cmp_ge_u32 s69, 125\n\t"
    "s_cselect_b32 s69, s44, s69\n\t"
    "s_mul_i32 m0, s69, 1040\n\t"
    "s_lshl_b32 s44, s69, 11\n\t"
    "buffer_load_dwordx4 v126, %15, s44 offen lds\n\t"
    "s_add_u32 s69, s69, 2\n\t"
    "s_sub_u32 s44, s69, 125\n\t"
    "s_cmp_ge_u32 s69, 125\n\t"
    "s_cselect_b32 s69, s44, s69\n\t"
    "s_mul_i32 m0, s69, 1040\n\t"
    "s_lshl_b32 s44, s69, 11\n\t"
    "buffer_load_dwordx4 v126, %15, s44 offen lds\n\t"
    "s_add_u32 s69, s69, 2\n\t"
    "s_sub_u32 s44, s69, 125\n\t"
    "s_cmp_ge_u32 s69, 125\n\t"
    "s_cselect_b32 s69, s44, s69\n\t"
    "s_mul_i32 m0, s69, 1040\n\t"
    "s_lshl_b32 s44, s69, 11\n\t"
    "buffer_load_dwordx4 v126, %15, s44 offen lds\n\t"
    "s_add_u32 s69, s69, 2\n\t"
    "s_sub_u32 s44, s69, 125\n\t"
    "s_cmp_ge_u32 s69, 125\n\t"
    "s_cselect_b32 s69, s44, s69\n\t"
    "s_mul_i32 m0, s69, 1040\n\t"
    "s_lshl_b32 s44, s69, 11\n\t"
    "buffer_load_dwordx4 v126, %15, s44 offen lds\n\t"
    "s_add_u32 s69, s69, 2\n\t"
    "s_sub_u32 s44, s69, 125\n\t"
    "s_cmp_ge_u32 s69, 125\n\t"
    "s_cselect_b32 s69, s44, s69\n\t"
    "s_mul_i32 m0, s69, 1040\n\t"
    "s_lshl_b32 s44, s69, 11\n\t"
    "buffer_load_dwordx4 v126, %15, s44 offen lds\n\t"
    "s_add_u32 s69, s69, 2\n\t"
    "s_sub_u32 s44, s69, 125\n\t"
    "s_cmp_ge_u32 s69, 125\n\t"
    "s_cselect_b32 s69, s44, s69\n\t"
    "s_mul_i32 m0, s69, 1040\n\t"
    "s_lshl_b32 s44, s69, 11\n\t"
    "buffer_load_dwordx4 v126, %15, s44 offen lds\n\t"
    "s_add_u32 s69, s69, 2\n\t"
    "s_sub_u32 s44, s69, 125\n\t"
    "s_cmp_ge_u32 s69, 125\n\t"
    "s_cselect_b32 s69, s44, s69\n\t"
    "s_mul_i32 m0, s69, 1040\n\t"
    "s_lshl_b32 s44, s69, 11\n\t"
    "buffer_load_dwordx4 v126, %15, s44 offen lds\n\t"
    "s_add_u32 s69, s69, 2\n\t"
    "s_sub_u32 s44, s69, 125\n\t"
    "s_cmp_ge_u32 s69, 125\n\t"
    "s_cselect_b32 s69, s44, s69\n\t"
    "s_mul_i32 m0, s69, 1040\n\t"
    "s_lshl_b32 s44, s69, 11\n\t"
    "buffer_load_dwordx4 v126, %15, s44 offen lds\n\t"
    "s_add_u32 s69, s69, 2\n\t"
    "s_sub_u32 s44, s69, 125\n\t"
    "s_cmp_ge_u32 s69, 125\n\t"
    "s_cselect_b32 s69, s44, s69\n\t"
    "s_mul_i32 m0, s69, 1040\n\t"
    "s_lshl_b32 s44, s69, 11\n\t"
    "buffer_load_dwordx4 v126, %15, s44 offen lds\n\t"
    "s_add_u32 s69, s69, 2\n\t"
    "s_sub_u32 s44, s69, 125\n\t"
    "s_cmp_ge_u32 s69, 125\n\t"
    "s_cselect_b32 s69, s44, s69\n\t"
    "s_mul_i32 m0, s69, 1040\n\t"
    "s_lshl_b32 s44, s69, 11\n\t"
    "buffer_load_dwordx4 v126, %15, s44 offen lds\n\t"
    "s_add_u32 s69, s69, 2\n\t"
    "s_sub_u32 s44, s69, 125\n\t"
    "s_cmp_ge_u32 s69, 125\n\t"
    "s_cselect_b32 s69, s44, s69\n\t"
    "s_mul_i32 m0, s69, 1040\n\t"
    "s_lshl_b32 s44, s69, 11\n\t"
    "buffer_load_dwordx4 v126, %15, s44 offen lds\n\t"
    "s_add_u32 s69, s69, 2\n\t"
    "s_sub_u32 s44, s69, 125\n\t"
    "s_cmp_ge_u32 s69, 125\n\t"
    "s_cselect_b32 s69, s44, s69\n\t"
    "s_mul_i32 m0, s69, 1040\n\t"
    "s_lshl_b32 s44, s69, 11\n\t"
    "buffer_load_dwordx4 v126, %15, s44 offen lds\n\t"
    "s_add_u32 s69, s69, 2\n\t"
    "s_sub_u32 s44, s69, 125\n\t"
    "s_cmp_ge_u32 s69, 125\n\t"
    "s_cselect_b32 s69, s44, s69\n\t"
    "s_mul_i32 m0, s69, 1040\n\t"
    "s_lshl_b32 s44, s69, 11\n\t"
    "buffer_load_dwordx4 v126, %15, s44 offen lds\n\t"
    "s_add_u32 s69, s69, 2\n\t"
    "s_sub_u32 s44, s69, 125\n\t"
    "s_cmp_ge_u32 s69, 125\n\t"
    "s_cselect_b32 s69, s44, s69\n\t"
    "s_call_b64 s[98:99], L_setup_%=\n\t"
    "L_ab_%=:\n\t"
    "s_mul_i32 s66, s53, 400\n\t"
    "buffer_load_dwordx3 v[128:130], v153, %0, s66 offen\n\t"
    "buffer_load_dwordx4 v[132:135], v153, %0, s66 offen offset:12\n\t"
    "s_mul_i32 s66, s54, 400\n\t"
    "buffer_load_dwordx3 v[136:138], v153, %0, s66 offen\n\t"
    "buffer_load_dwordx4 v[140:143], v153, %0, s66 offen offset:12\n\t"
    "L_asp8_%=:\n\t"
    "ds_read_b32 v119, v117 offset:4\n\t"
    "s_waitcnt lgkmcnt(0)\n\t"
    "v_readfirstlane_b32 s46, v119\n\t"
    "s_nop 0\n\t"
    "s_cmp_ge_u32 s46, 4\n\t"
    "s_cbranch_scc1 L_ago8_%=\n\t"
    "s_sleep 1\n\t"
    "s_branch L_asp8_%=\n\t"
    "L_ago8_%=:\n\t"
    "ds_read_b128 v[0:3], v95 offset:0\n\t"
    "ds_read_b128 v[4:7], v95 offset:1024\n\t"
    "ds_read_b128 v[8:11], v95 offset:2048\n\t"
    "ds_read_b128 v[12:15], v95 offset:3072\n\t"
    "ds_read_b128 v[16:19], v95 offset:4096\n\t"
    "ds_read_b128 v[20:23], v95 offset:5120\n\t"
    "ds_read_b128 v[24:27], v95 offset:6144\n\t"
    "ds_read_b128 v[28:31], v95 offset:7168\n\t"
    "ds_read_b128 v[32:35], v95 offset:8192\n\t"
    "ds_read_b128 v[36:39], v95 offset:9216\n\t"
    "ds_read_b128 v[40:43], v95 offset:10240\n\t"
    "ds_read_b128 v[44:47], v95 offset:11264\n\t"
    "ds_read_b128 v[48:51], v95 offset:12288\n\t"
    "ds_read_b128 v[52:55], v95 offset:13312\n\t"
    "ds_read_b128 v[56:59], v95 offset:14336\n\t"
    "ds_read_b128 v[60:63], v95 offset:15360\n\t"
    "ds_read_b128 v[64:67], v95 offset:16384\n\t"
    "ds_read_b128 v[68:71], v95 offset:17408\n\t"
    "ds_read_b128 v[72:75], v95 offset:18432\n\t"
    "ds_read_b128 v[76:79], v95 offset:19456\n\t"
    "ds_read_b128 v[80:83], v95 offset:20480\n\t"
    "ds_read_b128 v[84:87], v95 offset:21504\n\t"
    "ds_read_b128 v[88:91], v95 offset:22528\n\t"
    "s_waitcnt vmcnt(4)\n\t"
    "ds_add_u32 v117, v118 offset:8\n\t"
    "s_waitcnt vmcnt(0)\n\t"
    "s_branch L_awd_%=\n\t"
    "s_waitcnt vmcnt(4)\n\t"
    "ds_add_u32 v117, v118 offset:8\n\t"
    "s_branch L_await_%=\n\t"
    "L_setup_%=:\n\t"
    "v_mbcnt_lo_u32_b32 v100, -1, 0\n\t"
    "v_mbcnt_hi_u32_b32 v100, -1, v100\n\t"
    "s_lshl_b32 s61, %16, 15\n\t"
    "s_add_u32 s61, s61, 16515072\n\t"
    "v_lshlrev_b32 v127, 4, v100\n\t"
    "s_lshr_b32 s37, %14, 4\n\t"
    "s_and_b32 s39, %14, 7\n\t"
    "v_and_b32 v101, 15, v100\n\t"
    "s_lshl_b32 s39, s39, 4\n\t"
    "s_add_u32 s39, s39, s37\n\t"
    "s_mul_i32 s65, %16, 125\n\t"
    "v_add_u32 v155, s65, v101\n\t"
    "s_mul_i32 s63, s39, 102400\n\t"
    "s_add_u32 s63, s63, 3407872\n\t"
    "v_lshrrev_b32 v102, 2, v101\n\t"
    "s_and_b32 s37, s37, 7\n\t"
    "s_lshl_b32 s37, s37, 11\n\t"
    "v_lshrrev_b32 v103, 3, v100\n\t"
    "s_mul_i32 s36, s39, 65536\n\t"
    "s_bfe_u32 s65, %14, 0x10003\n\t"
    "v_and_b32 v103, 2, v103\n\t"
    "s_lshl_b32 s38, s65, 15\n\t"
    "s_add_u32 s36, s36, s38\n\t"
    "v_and_b32 v104, 1, v100\n\t"
    "s_lshl_b32 s38, %16, 12\n\t"
    "s_add_u32 s36, s36, s38\n\t"
    "v_lshlrev_b32 v104, 4, v104\n\t"
    "s_add_u32 s36, s36, 17825792\n\t"
    "v_mov_b32 v101, 0x3f80\n\t"
    "v_lshlrev_b32 v104, v104, v101\n\t"
    "v_cmp_eq_u32 vcc, v102, v103\n\t"
    "s_waitcnt lgkmcnt(0)\n\t"
    "s_lshr_b32 s39, s91, %9\n\t"
    "s_lshl_b32 s39, s39, 16\n\t"
    "v_cndmask_b32 v120, 0, v104, vcc\n\t"
    "v_cndmask_b32 v121, 0, v104, vcc\n\t"
    "v_add_u32 v103, 1, v103\n\t"
    "v_cmp_eq_u32 vcc, v102, v103\n\t"
    "s_lshl_b32 s40, s88, 16\n\t"
    "s_and_b32 s41, s88, 0xffff0000\n\t"
    "v_cndmask_b32 v122, 0, v104, vcc\n\t"
    "v_cndmask_b32 v123, 0, v104, vcc\n\t"
    "v_and_b32 v101, 2, v100\n\t"
    "v_mov_b32 v102, 0x44444444\n\t"
    "v_mov_b32 v103, 0xeeeeeeee\n\t"
    "v_cmp_eq_u32 vcc, 0, v101\n\t"
    "s_lshl_b32 s42, s89, 16\n\t"
    "s_and_b32 s43, s89, 0xffff0000\n\t"
    "v_cndmask_b32 v124, v103, v102, vcc\n\t"
    "v_lshrrev_b32 v101, 4, v100\n\t"
    "v_mul_u32_u24 v101, 8000, v101\n\t"
    "v_lshl_add_u32 v125, v155, 3, v101\n\t"
    "v_add_u32 v125, 64000, v125\n\t"
    "s_lshl_b32 s70, s72, 16\n\t"
    "s_and_b32 s71, s72, 0xffff0000\n\t"
    "s_lshl_b32 s72, s73, 16\n\t"
    "s_and_b32 s73, s73, 0xffff0000\n\t"
    "s_mov_b32 s67, 0xffff0000\n\t"
    "s_and_b32 s65, %14, 8\n\t"
    "s_add_u32 s38, s65, %16\n\t"
    "s_mul_i32 s38, s38, 212992\n\t"
    "s_add_u32 s38, s38, 196608\n\t"
    "s_add_u32 s84, s37, s38\n\t"
    "s_add_u32 s37, s37, 0x800\n\t"
    "s_and_b32 s37, s37, 0x3800\n\t"
    "s_add_u32 s85, s37, s38\n\t"
    "s_add_u32 s37, s37, 0x800\n\t"
    "s_and_b32 s37, s37, 0x3800\n\t"
    "s_add_u32 s86, s37, s38\n\t"
    "s_add_u32 s37, s37, 0x800\n\t"
    "s_and_b32 s37, s37, 0x3800\n\t"
    "s_add_u32 s87, s37, s38\n\t"
    "s_add_u32 s37, s37, 0x800\n\t"
    "s_and_b32 s37, s37, 0x3800\n\t"
    "s_add_u32 s88, s37, s38\n\t"
    "s_add_u32 s37, s37, 0x800\n\t"
    "s_and_b32 s37, s37, 0x3800\n\t"
    "s_add_u32 s89, s37, s38\n\t"
    "s_add_u32 s37, s37, 0x800\n\t"
    "s_and_b32 s37, s37, 0x3800\n\t"
    "s_add_u32 s90, s37, s38\n\t"
    "s_add_u32 s37, s37, 0x800\n\t"
    "s_and_b32 s37, s37, 0x3800\n\t"
    "s_add_u32 s91, s37, s38\n\t"
    "s_mul_i32 s94, s65, 425984\n\t"
    "s_sub_u32 s94, s94, 1703936\n\t"
    "s_sub_u32 s95, 0x8000, s94\n\t"
    "s_lshl_b32 s97, s65, 11\n\t"
    "s_sub_u32 s97, s97, 229376\n\t"
    "s_mov_b32 s92, 64000\n\t"
    "s_mov_b32 s93, -64000\n\t"
    "s_mov_b32 s96, 0x2000\n\t"
    "s_mov_b32 s62, 2\n\t"
    "v_rcp_f32 v101, s40\n\t"
    "v_rcp_f32 v102, s41\n\t"
    "v_rcp_f32 v103, s42\n\t"
    "v_rcp_f32 v104, s43\n\t"
    "v_mov_b32 v100, 0x7f7fffff\n\t"
    "v_med3_f32 v101, v101, -v100, v100\n\t"
    "v_med3_f32 v102, v102, -v100, v100\n\t"
    "v_med3_f32 v103, v103, -v100, v100\n\t"
    "v_med3_f32 v104, v104, -v100, v100\n\t"
    "v_readfirstlane_b32 s40, v101\n\t"
    "v_readfirstlane_b32 s41, v102\n\t"
    "v_readfirstlane_b32 s42, v103\n\t"
    "v_readfirstlane_b32 s43, v104\n\t"
    "s_setpc_b64 s[98:99]\n\t"
    "L_aside_%=:\n\t"
    "s_setprio 3\n\t"
    "v_cmp_gt_u32 vcc, 48, v147\n\t"
    "v_mov_b32 v133, 0x40000000\n\t"
    "v_lshrrev_b32 v134, 3, v147\n\t"
    "v_bfe_u32 v135, v147, 2, 1\n\t"
    "v_and_b32 v130, 3, v147\n\t"
    "v_lshl_add_u32 v132, v135, 5, v130\n\t"
    "v_mul_u32_u24 v64, 400, v132\n\t"
    "v_lshl_add_u32 v64, v134, 5, v64\n\t"
    "s_movk_i32 s50, 0\n\t"
    "buffer_load_dwordx4 v[0:3], v64, %0, s50 offen offset:16\n\t"
    "buffer_load_dwordx4 v[4:7], v64, %0, s50 offen offset:32\n\t"
    "v_lshlrev_b32 v66, 14, v134\n\t"
    "v_lshl_add_u32 v66, v132, 4, v66\n\t"
    "v_lshlrev_b32 v68, 8, v134\n\t"
    "v_lshl_add_u32 v68, v135, 2, v68\n\t"
    "v_add_u32 v68, 130048, v68\n\t"
    "v_cndmask_b32 v68, v133, v68, vcc\n\t"
    "v_cndmask_b32 v66, v133, v66, vcc\n\t"
    "v_lshrrev_b32 v134, 2, v147\n\t"
    "v_bfe_u32 v135, v147, 1, 1\n\t"
    "v_and_b32 v130, 1, v147\n\t"
    "v_lshl_add_u32 v132, v135, 5, v130\n\t"
    "v_mul_u32_u24 v65, 400, v132\n\t"
    "v_lshl_add_u32 v65, v134, 5, v65\n\t"
    "v_lshlrev_b32 v67, 14, v134\n\t"
    "v_lshl_add_u32 v67, v132, 4, v67\n\t"
    "v_lshlrev_b32 v69, 8, v134\n\t"
    "v_lshl_add_u32 v69, v135, 2, v69\n\t"
    "v_add_u32 v69, 130048, v69\n\t"
    "v_cndmask_b32 v69, v133, v69, vcc\n\t"
    "v_cndmask_b32 v67, v133, v67, vcc\n\t"
    "s_barrier\n\t"
    "s_load_dwordx2 s[72:73], %6, 0\n\t"
    "s_load_dwordx2 s[88:89], %7, 0\n\t"
    "s_load_dword s91, %8, 0\n\t"
    "v_lshlrev_b32 v128, 4, v147\n\t"
    "v_add_u32 v128, 133120, v128\n\t"
    "v_and_b32 v130, 1, v147\n\t"
    "v_cndmask_b32 v128, v133, v128, vcc\n\t"
    "v_mul_u32_u24 v127, 7, v130\n\t"
    "v_cmp_eq_u32 vcc, 1, v130\n\t"
    "v_mov_b32 v126, 0x07060100\n\t"
    "v_mov_b32 v133, 0x05040100\n\t"
    "v_mov_b32 v136, 1\n\t"
    "v_cndmask_b32 v126, v126, v133, vcc\n\t"
    "s_mov_b32 s36, 0\n\t"
    "s_mov_b32 s37, 8000\n\t"
    "s_mov_b32 s38, 16000\n\t"
    "s_mov_b32 s39, 24000\n\t"
    "s_mov_b32 s40, 32000\n\t"
    "s_mov_b32 s41, 40000\n\t"
    "s_mov_b32 s42, 48000\n\t"
    "s_mov_b32 s43, 56000\n\t"
    "s_movk_i32 s44, 0xf8\n\t"
    "s_mov_b32 s45, 0x01010000\n\t"
    "s_mov_b32 s46, 0x020c000c\n\t"
    "s_lshr_b32 s49, %14, 4\n\t"
    "s_mul_i32 s49, s49, 212992\n\t"
    "s_and_b32 s50, %14, 15\n\t"
    "s_lshl_b32 s50, s50, 10\n\t"
    "s_add_u32 s49, s49, s50\n\t"
    "v_lshrrev_b32 v134, 4, v147\n\t"
    "v_bfe_u32 v135, v147, 2, 2\n\t"
    "v_and_b32 v130, 3, v147\n\t"
    "v_lshlrev_b32 v137, 17, v134\n\t"
    "v_lshl_add_u32 v137, v135, 10, v137\n\t"
    "v_lshl_add_u32 v137, v130, 4, v137\n\t"
    "v_add_u32 v138, 0x1000, v137\n\t"
    "v_mul_u32_u24 v141, 0x210, v134\n\t"
    "v_lshl_add_u32 v141, v135, 6, v141\n\t"
    "v_lshl_add_u32 v141, v130, 4, v141\n\t"
    "v_add_u32 v141, 134656, v141\n\t"
    "v_mul_u32_u24 v142, 0x210, v130\n\t"
    "v_lshl_add_u32 v142, v135, 6, v142\n\t"
    "v_lshl_add_u32 v142, v134, 3, v142\n\t"
    "v_add_u32 v142, 134656, v142\n\t"
    "s_mov_b32 s50, 102400\n\t"
    "v_mul_u32_u24 v139, s50, v134\n\t"
    "v_and_b32 v132, 15, v147\n\t"
    "v_lshl_add_u32 v139, v132, 4, v139\n\t"
    "v_add_u32 v140, 409600, v139\n\t"
    "s_lshr_b32 s50, %14, 4\n\t"
    "s_lshl_b32 s47, s50, 13\n\t"
    "s_lshl_b32 s48, s50, 8\n\t"
    "s_and_b32 s50, %14, 7\n\t"
    "s_lshl_b32 s51, s50, 7\n\t"
    "s_add_u32 s47, s47, s51\n\t"
    "s_mul_i32 s51, s50, 1638400\n\t"
    "s_add_u32 s48, s48, s51\n\t"
    "s_bfe_u32 s50, %14, 0x10003\n\t"
    "s_lshl_b32 s51, s50, 19\n\t"
    "s_add_u32 s47, s47, s51\n\t"
    "s_lshl_b32 s51, s50, 12\n\t"
    "s_add_u32 s48, s48, s51\n\t"
    "s_add_u32 s48, s48, 3407872\n\t"
    "s_waitcnt vmcnt(2)\n\t"
    "v_lshlrev_b32 v134, 20, v147\n\t"
    "v_add_u32 v134, 2068, v134\n\t"
    "ds_add_u32 v134, v136\n\t"
    "v_mov_b32 v110, 65664\n\t"
    "v_mov_b32 v111, 131136\n\t"
    "v_mov_b32 v112, 262176\n\t"
    "v_mov_b32 v113, 524304\n\t"
    "v_mov_b32 v114, 1048584\n\t"
    "v_mov_b32 v115, 2097156\n\t"
    "v_mov_b32 v116, 4194306\n\t"
    "v_mov_b32 v117, 8388609\n\t"
    "s_waitcnt vmcnt(0)\n\t"
    "v_lshl_add_u32 v76, v0, 3, s36\n\t"
    "v_lshl_add_u32 v77, v1, 3, s37\n\t"
    "v_lshl_add_u32 v78, v2, 3, s38\n\t"
    "v_lshl_add_u32 v79, v3, 3, s39\n\t"
    "v_lshl_add_u32 v80, v4, 3, s40\n\t"
    "v_lshl_add_u32 v81, v5, 3, s41\n\t"
    "v_lshl_add_u32 v82, v6, 3, s42\n\t"
    "v_lshl_add_u32 v83, v7, 3, s43\n\t"
    "v_and_or_b32 v92, v76, s44, v68\n\t"
    "ds_or_rtn_b32 v118, v92, v110\n\t"
    "v_and_or_b32 v93, v77, s44, v68\n\t"
    "ds_or_rtn_b32 v119, v93, v111\n\t"
    "v_and_or_b32 v94, v78, s44, v68\n\t"
    "ds_or_rtn_b32 v120, v94, v112\n\t"
    "v_and_or_b32 v95, v79, s44, v68\n\t"
    "ds_or_rtn_b32 v121, v95, v113\n\t"
    "v_and_or_b32 v96, v80, s44, v68\n\t"
    "ds_or_rtn_b32 v122, v96, v114\n\t"
    "v_and_or_b32 v97, v81, s44, v68\n\t"
    "ds_or_rtn_b32 v123, v97, v115\n\t"
    "v_and_or_b32 v98, v82, s44, v68\n\t"
    "ds_or_rtn_b32 v124, v98, v116\n\t"
    "v_and_or_b32 v99, v83, s44, v68\n\t"
    "ds_or_rtn_b32 v125, v99, v117\n\t"
    "v_lshl_or_b32 v164, v77, 16, v76\n\t"
    "v_lshl_or_b32 v165, v79, 16, v78\n\t"
    "v_lshl_or_b32 v166, v81, 16, v80\n\t"
    "v_lshl_or_b32 v167, v83, 16, v82\n\t"
    "s_movk_i32 s50, 0\n\t"
    "buffer_load_dwordx4 v[8:11], v64, %0, s50 offen offset:1616\n\t"
    "buffer_load_dwordx4 v[12:15], v64, %0, s50 offen offset:1632\n\t"
    "s_movk_i32 s50, 3200\n\t"
    "buffer_load_dwordx4 v[16:19], v64, %0, s50 offen offset:16\n\t"
    "buffer_load_dwordx4 v[20:23], v64, %0, s50 offen offset:32\n\t"
    "s_waitcnt vmcnt(2)\n\t"
    "v_lshl_add_u32 v84, v8, 3, s36\n\t"
    "v_lshl_add_u32 v85, v9, 3, s37\n\t"
    "v_lshl_add_u32 v86, v10, 3, s38\n\t"
    "v_lshl_add_u32 v87, v11, 3, s39\n\t"
    "v_lshl_add_u32 v88, v12, 3, s40\n\t"
    "v_lshl_add_u32 v89, v13, 3, s41\n\t"
    "v_lshl_add_u32 v90, v14, 3, s42\n\t"
    "v_lshl_add_u32 v91, v15, 3, s43\n\t"
    "s_waitcnt lgkmcnt(4)\n\t"
    "v_and_b32 v118, v118, v110\n\t"
    "v_perm_b32 v118, v118, v118, s46\n\t"
    "ds_or_b32 v92, v118\n\t"
    "v_and_b32 v119, v119, v111\n\t"
    "v_perm_b32 v119, v119, v119, s46\n\t"
    "ds_or_b32 v93, v119\n\t"
    "v_and_b32 v120, v120, v112\n\t"
    "v_perm_b32 v120, v120, v120, s46\n\t"
    "ds_or_b32 v94, v120\n\t"
    "v_and_b32 v121, v121, v113\n\t"
    "v_perm_b32 v121, v121, v121, s46\n\t"
    "ds_or_b32 v95, v121\n\t"
    "s_waitcnt lgkmcnt(4)\n\t"
    "v_and_b32 v122, v122, v114\n\t"
    "v_perm_b32 v122, v122, v122, s46\n\t"
    "ds_or_b32 v96, v122\n\t"
    "v_and_b32 v123, v123, v115\n\t"
    "v_perm_b32 v123, v123, v123, s46\n\t"
    "ds_or_b32 v97, v123\n\t"
    "v_and_b32 v124, v124, v116\n\t"
    "v_perm_b32 v124, v124, v124, s46\n\t"
    "ds_or_b32 v98, v124\n\t"
    "v_and_b32 v125, v125, v117\n\t"
    "v_perm_b32 v125, v125, v125, s46\n\t"
    "ds_or_b32 v99, v125\n\t"
    "buffer_store_dwordx4 v[164:167], v66, %12, s49 offen offset:0 sc1\n\t"
    "v_and_or_b32 v100, v84, s44, v68\n\t"
    "ds_read_b32 v100, v100\n\t"
    "v_and_or_b32 v101, v85, s44, v68\n\t"
    "ds_read_b32 v101, v101\n\t"
    "v_and_or_b32 v102, v86, s44, v68\n\t"
    "ds_read_b32 v102, v102\n\t"
    "v_and_or_b32 v103, v87, s44, v68\n\t"
    "ds_read_b32 v103, v103\n\t"
    "v_and_or_b32 v104, v88, s44, v68\n\t"
    "ds_read_b32 v104, v104\n\t"
    "v_and_or_b32 v105, v89, s44, v68\n\t"
    "ds_read_b32 v105, v105\n\t"
    "v_and_or_b32 v106, v90, s44, v68\n\t"
    "ds_read_b32 v106, v106\n\t"
    "v_and_or_b32 v107, v91, s44, v68\n\t"
    "ds_read_b32 v107, v107\n\t"
    "s_waitcnt vmcnt(1)\n\t"
    "v_lshl_add_u32 v76, v16, 3, s36\n\t"
    "v_lshl_add_u32 v77, v17, 3, s37\n\t"
    "v_lshl_add_u32 v78, v18, 3, s38\n\t"
    "v_lshl_add_u32 v79, v19, 3, s39\n\t"
    "v_lshl_add_u32 v80, v20, 3, s40\n\t"
    "v_lshl_add_u32 v81, v21, 3, s41\n\t"
    "v_lshl_add_u32 v82, v22, 3, s42\n\t"
    "v_lshl_add_u32 v83, v23, 3, s43\n\t"
    "s_movk_i32 s50, 3200\n\t"
    "buffer_load_dwordx4 v[24:27], v64, %0, s50 offen offset:1616\n\t"
    "buffer_load_dwordx4 v[28:31], v64, %0, s50 offen offset:1632\n\t"
    "s_movk_i32 s50, 6400\n\t"
    "buffer_load_dwordx4 v[32:35], v64, %0, s50 offen offset:16\n\t"
    "buffer_load_dwordx4 v[36:39], v64, %0, s50 offen offset:32\n\t"
    "v_mov_b32 v129, 0xffff0000\n\t"
    "s_waitcnt lgkmcnt(4)\n\t"
    "v_perm_b32 v100, v100, v84, v126\n\t"
    "v_perm_b32 v101, v101, v85, v126\n\t"
    "v_perm_b32 v102, v102, v86, v126\n\t"
    "v_perm_b32 v103, v103, v87, v126\n\t"
    "s_waitcnt lgkmcnt(0)\n\t"
    "v_perm_b32 v104, v104, v88, v126\n\t"
    "v_perm_b32 v105, v105, v89, v126\n\t"
    "v_perm_b32 v106, v106, v90, v126\n\t"
    "v_perm_b32 v107, v107, v91, v126\n\t"
    "v_max_u32 v108, v100, v101\n\t"
    "v_min_u32 v101, v100, v101\n\t"
    "v_max_u32 v100, v102, v103\n\t"
    "v_min_u32 v103, v102, v103\n\t"
    "v_max_u32 v102, v104, v105\n\t"
    "v_min_u32 v105, v104, v105\n\t"
    "v_max_u32 v104, v106, v107\n\t"
    "v_min_u32 v107, v106, v107\n\t"
    "v_max_u32 v106, v108, v100\n\t"
    "v_min_u32 v100, v108, v100\n\t"
    "v_max_u32 v108, v101, v103\n\t"
    "v_min_u32 v103, v101, v103\n\t"
    "v_max_u32 v101, v102, v104\n\t"
    "v_min_u32 v104, v102, v104\n\t"
    "v_max_u32 v102, v105, v107\n\t"
    "v_min_u32 v107, v105, v107\n\t"
    "v_bfi_b32 v130, v106, 0, v129\n\t"
    "v_cmp_eq_u32 vcc, 0, v130\n\t"
    "v_and_or_b32 v92, v106, s44, v68\n\t"
    "s_nop 0\n\t"
    "v_cndmask_b32 v130, v130, v129, vcc\n\t"
    "v_ffbl_b32 v131, v130\n\t"
    "v_and_b32 v134, 7, v131\n\t"
    "v_lshlrev_b32 v134, v134, s45\n\t"
    "v_xor_b32 v129, v129, v134\n\t"
    "v_xor_b32 v132, v131, v127\n\t"
    "v_bfi_b32 v130, v101, 0, v129\n\t"
    "v_cmp_eq_u32 vcc, 0, v130\n\t"
    "v_and_or_b32 v93, v101, s44, v68\n\t"
    "v_xor_b32 v133, 23, v132\n\t"
    "v_lshlrev_b32 v110, v132, v136\n\t"
    "v_and_b32 v132, 7, v132\n\t"
    "v_lshl_or_b32 v110, v136, v133, v110\n\t"
    "v_lshl_add_u32 v132, v132, 1, v128\n\t"
    "ds_or_rtn_b32 v118, v92, v110\n\t"
    "ds_write_b16 v132, v106\n\t"
    "v_cndmask_b32 v130, v130, v129, vcc\n\t"
    "v_ffbl_b32 v131, v130\n\t"
    "v_and_b32 v134, 7, v131\n\t"
    "v_lshlrev_b32 v134, v134, s45\n\t"
    "v_xor_b32 v129, v129, v134\n\t"
    "v_xor_b32 v132, v131, v127\n\t"
    "v_bfi_b32 v130, v108, 0, v129\n\t"
    "v_cmp_eq_u32 vcc, 0, v130\n\t"
    "v_and_or_b32 v94, v108, s44, v68\n\t"
    "v_xor_b32 v133, 23, v132\n\t"
    "v_lshlrev_b32 v111, v132, v136\n\t"
    "v_and_b32 v132, 7, v132\n\t"
    "v_lshl_or_b32 v111, v136, v133, v111\n\t"
    "v_lshl_add_u32 v132, v132, 1, v128\n\t"
    "ds_or_rtn_b32 v119, v93, v111\n\t"
    "ds_write_b16 v132, v101\n\t"
    "v_cndmask_b32 v130, v130, v129, vcc\n\t"
    "v_ffbl_b32 v131, v130\n\t"
    "v_and_b32 v134, 7, v131\n\t"
    "v_lshlrev_b32 v134, v134, s45\n\t"
    "v_xor_b32 v129, v129, v134\n\t"
    "v_xor_b32 v132, v131, v127\n\t"
    "v_bfi_b32 v130, v100, 0, v129\n\t"
    "v_cmp_eq_u32 vcc, 0, v130\n\t"
    "v_and_or_b32 v95, v100, s44, v68\n\t"
    "v_xor_b32 v133, 23, v132\n\t"
    "v_lshlrev_b32 v112, v132, v136\n\t"
    "v_and_b32 v132, 7, v132\n\t"
    "v_lshl_or_b32 v112, v136, v133, v112\n\t"
    "v_lshl_add_u32 v132, v132, 1, v128\n\t"
    "ds_or_rtn_b32 v120, v94, v112\n\t"
    "ds_write_b16 v132, v108\n\t"
    "v_cndmask_b32 v130, v130, v129, vcc\n\t"
    "v_ffbl_b32 v131, v130\n\t"
    "v_and_b32 v134, 7, v131\n\t"
    "v_lshlrev_b32 v134, v134, s45\n\t"
    "v_xor_b32 v129, v129, v134\n\t"
    "v_xor_b32 v132, v131, v127\n\t"
    "v_bfi_b32 v130, v102, 0, v129\n\t"
    "v_cmp_eq_u32 vcc, 0, v130\n\t"
    "v_and_or_b32 v96, v102, s44, v68\n\t"
    "v_xor_b32 v133, 23, v132\n\t"
    "v_lshlrev_b32 v113, v132, v136\n\t"
    "v_and_b32 v132, 7, v132\n\t"
    "v_lshl_or_b32 v113, v136, v133, v113\n\t"
    "v_lshl_add_u32 v132, v132, 1, v128\n\t"
    "ds_or_rtn_b32 v121, v95, v113\n\t"
    "ds_write_b16 v132, v100\n\t"
    "v_cndmask_b32 v130, v130, v129, vcc\n\t"
    "v_ffbl_b32 v131, v130\n\t"
    "v_and_b32 v134, 7, v131\n\t"
    "v_lshlrev_b32 v134, v134, s45\n\t"
    "v_xor_b32 v129, v129, v134\n\t"
    "v_xor_b32 v132, v131, v127\n\t"
    "s_waitcnt lgkmcnt(7)\n\t"
    "v_and_b32 v118, v118, v110\n\t"
    "v_perm_b32 v118, v118, v118, s46\n\t"
    "ds_or_b32 v92, v118\n\t"
    "v_bfi_b32 v130, v104, 0, v129\n\t"
    "v_cmp_eq_u32 vcc, 0, v130\n\t"
    "v_and_or_b32 v97, v104, s44, v68\n\t"
    "v_xor_b32 v133, 23, v132\n\t"
    "v_lshlrev_b32 v114, v132, v136\n\t"
    "v_and_b32 v132, 7, v132\n\t"
    "v_lshl_or_b32 v114, v136, v133, v114\n\t"
    "v_lshl_add_u32 v132, v132, 1, v128\n\t"
    "ds_or_rtn_b32 v122, v96, v114\n\t"
    "ds_write_b16 v132, v102\n\t"
    "v_cndmask_b32 v130, v130, v129, vcc\n\t"
    "v_ffbl_b32 v131, v130\n\t"
    "v_and_b32 v134, 7, v131\n\t"
    "v_lshlrev_b32 v134, v134, s45\n\t"
    "v_xor_b32 v129, v129, v134\n\t"
    "v_xor_b32 v132, v131, v127\n\t"
    "s_waitcnt lgkmcnt(8)\n\t"
    "v_and_b32 v119, v119, v111\n\t"
    "v_perm_b32 v119, v119, v119, s46\n\t"
    "ds_or_b32 v93, v119\n\t"
    "v_bfi_b32 v130, v103, 0, v129\n\t"
    "v_cmp_eq_u32 vcc, 0, v130\n\t"
    "v_and_or_b32 v98, v103, s44, v68\n\t"
    "v_xor_b32 v133, 23, v132\n\t"
    "v_lshlrev_b32 v115, v132, v136\n\t"
    "v_and_b32 v132, 7, v132\n\t"
    "v_lshl_or_b32 v115, v136, v133, v115\n\t"
    "v_lshl_add_u32 v132, v132, 1, v128\n\t"
    "ds_or_rtn_b32 v123, v97, v115\n\t"
    "ds_write_b16 v132, v104\n\t"
    "v_cndmask_b32 v130, v130, v129, vcc\n\t"
    "v_ffbl_b32 v131, v130\n\t"
    "v_and_b32 v134, 7, v131\n\t"
    "v_lshlrev_b32 v134, v134, s45\n\t"
    "v_xor_b32 v129, v129, v134\n\t"
    "v_xor_b32 v132, v131, v127\n\t"
    "s_waitcnt lgkmcnt(9)\n\t"
    "v_and_b32 v120, v120, v112\n\t"
    "v_perm_b32 v120, v120, v120, s46\n\t"
    "ds_or_b32 v94, v120\n\t"
    "v_bfi_b32 v130, v107, 0, v129\n\t"
    "v_cmp_eq_u32 vcc, 0, v130\n\t"
    "v_and_or_b32 v99, v107, s44, v68\n\t"
    "v_xor_b32 v133, 23, v132\n\t"
    "v_lshlrev_b32 v116, v132, v136\n\t"
    "v_and_b32 v132, 7, v132\n\t"
    "v_lshl_or_b32 v116, v136, v133, v116\n\t"
    "v_lshl_add_u32 v132, v132, 1, v128\n\t"
    "ds_or_rtn_b32 v124, v98, v116\n\t"
    "ds_write_b16 v132, v103\n\t"
    "v_cndmask_b32 v130, v130, v129, vcc\n\t"
    "v_ffbl_b32 v131, v130\n\t"
    "v_xor_b32 v132, v131, v127\n\t"
    "s_waitcnt lgkmcnt(10)\n\t"
    "v_and_b32 v121, v121, v113\n\t"
    "v_perm_b32 v121, v121, v121, s46\n\t"
    "ds_or_b32 v95, v121\n\t"
    "v_xor_b32 v133, 23, v132\n\t"
    "v_lshlrev_b32 v117, v132, v136\n\t"
    "v_and_b32 v132, 7, v132\n\t"
    "v_lshl_or_b32 v117, v136, v133, v117\n\t"
    "v_lshl_add_u32 v132, v132, 1, v128\n\t"
    "ds_or_rtn_b32 v125, v99, v117\n\t"
    "ds_write_b16 v132, v107\n\t"
    "s_waitcnt lgkmcnt(10)\n\t"
    "v_and_b32 v122, v122, v114\n\t"
    "v_perm_b32 v122, v122, v122, s46\n\t"
    "ds_or_b32 v96, v122\n\t"
    "ds_read_b128 v[72:75], v128\n\t"
    "v_and_or_b32 v100, v76, s44, v68\n\t"
    "ds_read_b32 v100, v100\n\t"
    "v_and_or_b32 v101, v77, s44, v68\n\t"
    "ds_read_b32 v101, v101\n\t"
    "v_and_or_b32 v102, v78, s44, v68\n\t"
    "ds_read_b32 v102, v102\n\t"
    "v_and_or_b32 v103, v79, s44, v68\n\t"
    "ds_read_b32 v103, v103\n\t"
    "v_and_or_b32 v104, v80, s44, v68\n\t"
    "ds_read_b32 v104, v104\n\t"
    "v_and_or_b32 v105, v81, s44, v68\n\t"
    "ds_read_b32 v105, v105\n\t"
    "v_and_or_b32 v106, v82, s44, v68\n\t"
    "ds_read_b32 v106, v106\n\t"
    "v_and_or_b32 v107, v83, s44, v68\n\t"
    "ds_read_b32 v107, v107\n\t"
    "s_waitcnt vmcnt(2)\n\t"
    "v_lshl_add_u32 v84, v24, 3, s36\n\t"
    "v_lshl_add_u32 v85, v25, 3, s37\n\t"
    "v_lshl_add_u32 v86, v26, 3, s38\n\t"
    "v_lshl_add_u32 v87, v27, 3, s39\n\t"
    "v_lshl_add_u32 v88, v28, 3, s40\n\t"
    "v_lshl_add_u32 v89, v29, 3, s41\n\t"
    "v_lshl_add_u32 v90, v30, 3, s42\n\t"
    "v_lshl_add_u32 v91, v31, 3, s43\n\t"
    "s_movk_i32 s50, 6400\n\t"
    "buffer_load_dwordx4 v[40:43], v65, %0, s50 offen offset:1616\n\t"
    "buffer_load_dwordx4 v[44:47], v65, %0, s50 offen offset:1632\n\t"
    "s_movk_i32 s50, 6400\n\t"
    "buffer_load_dwordx4 v[48:51], v65, %0, s50 offen offset:2416\n\t"
    "buffer_load_dwordx4 v[52:55], v65, %0, s50 offen offset:2432\n\t"
    "s_movk_i32 s50, 9600\n\t"
    "buffer_load_dwordx4 v[56:59], v65, %0, s50 offen offset:16\n\t"
    "buffer_load_dwordx4 v[60:63], v65, %0, s50 offen offset:32\n\t"
    "s_movk_i32 s50, 9600\n\t"
    "buffer_load_dwordx4 v[0:3], v65, %0, s50 offen offset:816\n\t"
    "buffer_load_dwordx4 v[4:7], v65, %0, s50 offen offset:832\n\t"
    "s_movk_i32 s50, 9600\n\t"
    "buffer_load_dwordx4 v[8:11], v65, %0, s50 offen offset:1616\n\t"
    "buffer_load_dwordx4 v[12:15], v65, %0, s50 offen offset:1632\n\t"
    "v_mov_b32 v134, 1032\n\t"
    "L_asvsp8_%=:\n\t"
    "ds_read_b32 v130, v134\n\t"
    "s_waitcnt lgkmcnt(0)\n\t"
    "v_readfirstlane_b32 s50, v130\n\t"
    "s_nop 0\n\t"
    "s_cmp_lt_u32 s50, 6\n\t"
    "s_cbranch_scc1 L_asvsp8_%=\n\t"
    "s_mov_b64 s[96:97], %2\n\t"
    "s_and_b32 s97, s97, 0xffff\n\t"
    "buffer_load_dwordx4 v[156:159], v137, s[96:99], s47 offen offset:0\n\t"
    "buffer_load_dwordx4 v[160:163], v138, s[96:99], s47 offen offset:0\n\t"
    "buffer_load_dwordx4 v[164:167], v137, s[96:99], s47 offen offset:64\n\t"
    "buffer_load_dwordx4 v[168:171], v138, s[96:99], s47 offen offset:64\n\t"
    "s_add_u32 s47, s47, 0x100000\n\t"
    "buffer_load_dwordx4 v[172:175], v137, s[96:99], s47 offen offset:0\n\t"
    "buffer_load_dwordx4 v[176:179], v138, s[96:99], s47 offen offset:0\n\t"
    "buffer_load_dwordx4 v[180:183], v137, s[96:99], s47 offen offset:64\n\t"
    "buffer_load_dwordx4 v[184:187], v138, s[96:99], s47 offen offset:64\n\t"
    "s_add_u32 s47, s47, 0x100000\n\t"
    "buffer_load_dwordx4 v[188:191], v137, s[96:99], s47 offen offset:0\n\t"
    "buffer_load_dwordx4 v[192:195], v138, s[96:99], s47 offen offset:0\n\t"
    "buffer_load_dwordx4 v[196:199], v137, s[96:99], s47 offen offset:64\n\t"
    "buffer_load_dwordx4 v[200:203], v138, s[96:99], s47 offen offset:64\n\t"
    "s_add_u32 s47, s47, 0x100000\n\t"
    "v_and_b32 v123, v123, v115\n\t"
    "v_perm_b32 v123, v123, v123, s46\n\t"
    "ds_or_b32 v97, v123\n\t"
    "v_and_b32 v124, v124, v116\n\t"
    "v_perm_b32 v124, v124, v124, s46\n\t"
    "ds_or_b32 v98, v124\n\t"
    "v_and_b32 v125, v125, v117\n\t"
    "v_perm_b32 v125, v125, v125, s46\n\t"
    "ds_or_b32 v99, v125\n\t"
    "buffer_store_dwordx4 v[72:75], v66, %12, s49 offen offset:64 sc1\n\t"
    "v_mov_b32 v129, 0xffff0000\n\t"
    "v_perm_b32 v100, v100, v76, v126\n\t"
    "v_perm_b32 v101, v101, v77, v126\n\t"
    "v_perm_b32 v102, v102, v78, v126\n\t"
    "v_perm_b32 v103, v103, v79, v126\n\t"
    "v_perm_b32 v104, v104, v80, v126\n\t"
    "v_perm_b32 v105, v105, v81, v126\n\t"
    "v_perm_b32 v106, v106, v82, v126\n\t"
    "v_perm_b32 v107, v107, v83, v126\n\t"
    "v_max_u32 v108, v100, v101\n\t"
    "v_min_u32 v101, v100, v101\n\t"
    "v_max_u32 v100, v102, v103\n\t"
    "v_min_u32 v103, v102, v103\n\t"
    "v_max_u32 v102, v104, v105\n\t"
    "v_min_u32 v105, v104, v105\n\t"
    "v_max_u32 v104, v106, v107\n\t"
    "v_min_u32 v107, v106, v107\n\t"
    "v_max_u32 v106, v108, v100\n\t"
    "v_min_u32 v100, v108, v100\n\t"
    "v_max_u32 v108, v101, v103\n\t"
    "v_min_u32 v103, v101, v103\n\t"
    "v_max_u32 v101, v102, v104\n\t"
    "v_min_u32 v104, v102, v104\n\t"
    "v_max_u32 v102, v105, v107\n\t"
    "v_min_u32 v107, v105, v107\n\t"
    "v_bfi_b32 v130, v106, 0, v129\n\t"
    "v_cmp_eq_u32 vcc, 0, v130\n\t"
    "v_and_or_b32 v92, v106, s44, v68\n\t"
    "s_nop 0\n\t"
    "v_cndmask_b32 v130, v130, v129, vcc\n\t"
    "v_ffbl_b32 v131, v130\n\t"
    "v_and_b32 v134, 7, v131\n\t"
    "v_lshlrev_b32 v134, v134, s45\n\t"
    "v_xor_b32 v129, v129, v134\n\t"
    "v_xor_b32 v132, v131, v127\n\t"
    "v_bfi_b32 v130, v101, 0, v129\n\t"
    "v_cmp_eq_u32 vcc, 0, v130\n\t"
    "v_and_or_b32 v93, v101, s44, v68\n\t"
    "v_xor_b32 v133, 23, v132\n\t"
    "v_lshlrev_b32 v110, v132, v136\n\t"
    "v_and_b32 v132, 7, v132\n\t"
    "v_lshl_or_b32 v110, v136, v133, v110\n\t"
    "v_lshl_add_u32 v132, v132, 1, v128\n\t"
    "ds_or_rtn_b32 v118, v92, v110\n\t"
    "ds_write_b16 v132, v106\n\t"
    "v_cndmask_b32 v130, v130, v129, vcc\n\t"
    "v_ffbl_b32 v131, v130\n\t"
    "v_and_b32 v134, 7, v131\n\t"
    "v_lshlrev_b32 v134, v134, s45\n\t"
    "v_xor_b32 v129, v129, v134\n\t"
    "v_xor_b32 v132, v131, v127\n\t"
    "v_bfi_b32 v130, v108, 0, v129\n\t"
    "v_cmp_eq_u32 vcc, 0, v130\n\t"
    "v_and_or_b32 v94, v108, s44, v68\n\t"
    "v_xor_b32 v133, 23, v132\n\t"
    "v_lshlrev_b32 v111, v132, v136\n\t"
    "v_and_b32 v132, 7, v132\n\t"
    "v_lshl_or_b32 v111, v136, v133, v111\n\t"
    "v_lshl_add_u32 v132, v132, 1, v128\n\t"
    "ds_or_rtn_b32 v119, v93, v111\n\t"
    "ds_write_b16 v132, v101\n\t"
    "v_cndmask_b32 v130, v130, v129, vcc\n\t"
    "v_ffbl_b32 v131, v130\n\t"
    "v_and_b32 v134, 7, v131\n\t"
    "v_lshlrev_b32 v134, v134, s45\n\t"
    "v_xor_b32 v129, v129, v134\n\t"
    "v_xor_b32 v132, v131, v127\n\t"
    "v_bfi_b32 v130, v100, 0, v129\n\t"
    "v_cmp_eq_u32 vcc, 0, v130\n\t"
    "v_and_or_b32 v95, v100, s44, v68\n\t"
    "v_xor_b32 v133, 23, v132\n\t"
    "v_lshlrev_b32 v112, v132, v136\n\t"
    "v_and_b32 v132, 7, v132\n\t"
    "v_lshl_or_b32 v112, v136, v133, v112\n\t"
    "v_lshl_add_u32 v132, v132, 1, v128\n\t"
    "ds_or_rtn_b32 v120, v94, v112\n\t"
    "ds_write_b16 v132, v108\n\t"
    "v_cndmask_b32 v130, v130, v129, vcc\n\t"
    "v_ffbl_b32 v131, v130\n\t"
    "v_and_b32 v134, 7, v131\n\t"
    "v_lshlrev_b32 v134, v134, s45\n\t"
    "v_xor_b32 v129, v129, v134\n\t"
    "v_xor_b32 v132, v131, v127\n\t"
    "v_bfi_b32 v130, v102, 0, v129\n\t"
    "v_cmp_eq_u32 vcc, 0, v130\n\t"
    "v_and_or_b32 v96, v102, s44, v68\n\t"
    "v_xor_b32 v133, 23, v132\n\t"
    "v_lshlrev_b32 v113, v132, v136\n\t"
    "v_and_b32 v132, 7, v132\n\t"
    "v_lshl_or_b32 v113, v136, v133, v113\n\t"
    "v_lshl_add_u32 v132, v132, 1, v128\n\t"
    "ds_or_rtn_b32 v121, v95, v113\n\t"
    "ds_write_b16 v132, v100\n\t"
    "v_cndmask_b32 v130, v130, v129, vcc\n\t"
    "v_ffbl_b32 v131, v130\n\t"
    "v_and_b32 v134, 7, v131\n\t"
    "v_lshlrev_b32 v134, v134, s45\n\t"
    "v_xor_b32 v129, v129, v134\n\t"
    "v_xor_b32 v132, v131, v127\n\t"
    "s_waitcnt lgkmcnt(7)\n\t"
    "v_and_b32 v118, v118, v110\n\t"
    "v_perm_b32 v118, v118, v118, s46\n\t"
    "ds_or_b32 v92, v118\n\t"
    "v_bfi_b32 v130, v104, 0, v129\n\t"
    "v_cmp_eq_u32 vcc, 0, v130\n\t"
    "v_and_or_b32 v97, v104, s44, v68\n\t"
    "v_xor_b32 v133, 23, v132\n\t"
    "v_lshlrev_b32 v114, v132, v136\n\t"
    "v_and_b32 v132, 7, v132\n\t"
    "v_lshl_or_b32 v114, v136, v133, v114\n\t"
    "v_lshl_add_u32 v132, v132, 1, v128\n\t"
    "ds_or_rtn_b32 v122, v96, v114\n\t"
    "ds_write_b16 v132, v102\n\t"
    "v_cndmask_b32 v130, v130, v129, vcc\n\t"
    "v_ffbl_b32 v131, v130\n\t"
    "v_and_b32 v134, 7, v131\n\t"
    "v_lshlrev_b32 v134, v134, s45\n\t"
    "v_xor_b32 v129, v129, v134\n\t"
    "v_xor_b32 v132, v131, v127\n\t"
    "s_waitcnt lgkmcnt(8)\n\t"
    "v_and_b32 v119, v119, v111\n\t"
    "v_perm_b32 v119, v119, v119, s46\n\t"
    "ds_or_b32 v93, v119\n\t"
    "v_bfi_b32 v130, v103, 0, v129\n\t"
    "v_cmp_eq_u32 vcc, 0, v130\n\t"
    "v_and_or_b32 v98, v103, s44, v68\n\t"
    "v_xor_b32 v133, 23, v132\n\t"
    "v_lshlrev_b32 v115, v132, v136\n\t"
    "v_and_b32 v132, 7, v132\n\t"
    "v_lshl_or_b32 v115, v136, v133, v115\n\t"
    "v_lshl_add_u32 v132, v132, 1, v128\n\t"
    "ds_or_rtn_b32 v123, v97, v115\n\t"
    "ds_write_b16 v132, v104\n\t"
    "v_cndmask_b32 v130, v130, v129, vcc\n\t"
    "v_ffbl_b32 v131, v130\n\t"
    "v_and_b32 v134, 7, v131\n\t"
    "v_lshlrev_b32 v134, v134, s45\n\t"
    "v_xor_b32 v129, v129, v134\n\t"
    "v_xor_b32 v132, v131, v127\n\t"
    "s_waitcnt lgkmcnt(9)\n\t"
    "v_and_b32 v120, v120, v112\n\t"
    "v_perm_b32 v120, v120, v120, s46\n\t"
    "ds_or_b32 v94, v120\n\t"
    "v_bfi_b32 v130, v107, 0, v129\n\t"
    "v_cmp_eq_u32 vcc, 0, v130\n\t"
    "v_and_or_b32 v99, v107, s44, v68\n\t"
    "v_xor_b32 v133, 23, v132\n\t"
    "v_lshlrev_b32 v116, v132, v136\n\t"
    "v_and_b32 v132, 7, v132\n\t"
    "v_lshl_or_b32 v116, v136, v133, v116\n\t"
    "v_lshl_add_u32 v132, v132, 1, v128\n\t"
    "ds_or_rtn_b32 v124, v98, v116\n\t"
    "ds_write_b16 v132, v103\n\t"
    "v_cndmask_b32 v130, v130, v129, vcc\n\t"
    "v_ffbl_b32 v131, v130\n\t"
    "v_xor_b32 v132, v131, v127\n\t"
    "s_waitcnt lgkmcnt(10)\n\t"
    "v_and_b32 v121, v121, v113\n\t"
    "v_perm_b32 v121, v121, v121, s46\n\t"
    "ds_or_b32 v95, v121\n\t"
    "v_xor_b32 v133, 23, v132\n\t"
    "v_lshlrev_b32 v117, v132, v136\n\t"
    "v_and_b32 v132, 7, v132\n\t"
    "v_lshl_or_b32 v117, v136, v133, v117\n\t"
    "v_lshl_add_u32 v132, v132, 1, v128\n\t"
    "ds_or_rtn_b32 v125, v99, v117\n\t"
    "ds_write_b16 v132, v107\n\t"
    "s_waitcnt lgkmcnt(10)\n\t"
    "v_and_b32 v122, v122, v114\n\t"
    "v_perm_b32 v122, v122, v122, s46\n\t"
    "ds_or_b32 v96, v122\n\t"
    "ds_read_b128 v[72:75], v128\n\t"
    "v_and_or_b32 v100, v84, s44, v68\n\t"
    "ds_read_b32 v100, v100\n\t"
    "v_and_or_b32 v101, v85, s44, v68\n\t"
    "ds_read_b32 v101, v101\n\t"
    "v_and_or_b32 v102, v86, s44, v68\n\t"
    "ds_read_b32 v102, v102\n\t"
    "v_and_or_b32 v103, v87, s44, v68\n\t"
    "ds_read_b32 v103, v103\n\t"
    "v_and_or_b32 v104, v88, s44, v68\n\t"
    "ds_read_b32 v104, v104\n\t"
    "v_and_or_b32 v105, v89, s44, v68\n\t"
    "ds_read_b32 v105, v105\n\t"
    "v_and_or_b32 v106, v90, s44, v68\n\t"
    "ds_read_b32 v106, v106\n\t"
    "v_and_or_b32 v107, v91, s44, v68\n\t"
    "ds_read_b32 v107, v107\n\t"
    "s_waitcnt vmcnt(23)\n\t"
    "v_lshl_add_u32 v76, v32, 3, s36\n\t"
    "v_lshl_add_u32 v77, v33, 3, s37\n\t"
    "v_lshl_add_u32 v78, v34, 3, s38\n\t"
    "v_lshl_add_u32 v79, v35, 3, s39\n\t"
    "v_lshl_add_u32 v80, v36, 3, s40\n\t"
    "v_lshl_add_u32 v81, v37, 3, s41\n\t"
    "v_lshl_add_u32 v82, v38, 3, s42\n\t"
    "v_lshl_add_u32 v83, v39, 3, s43\n\t"
    "s_movk_i32 s50, 9600\n\t"
    "buffer_load_dwordx4 v[16:19], v65, %0, s50 offen offset:2416\n\t"
    "buffer_load_dwordx4 v[20:23], v65, %0, s50 offen offset:2432\n\t"
    "buffer_load_dwordx4 v[204:207], v137, s[96:99], s47 offen offset:0\n\t"
    "buffer_load_dwordx4 v[208:211], v138, s[96:99], s47 offen offset:0\n\t"
    "buffer_load_dwordx4 v[212:215], v137, s[96:99], s47 offen offset:64\n\t"
    "buffer_load_dwordx4 v[216:219], v138, s[96:99], s47 offen offset:64\n\t"
    "s_add_u32 s47, s47, 0x100000\n\t"
    "buffer_load_dwordx4 v[220:223], v137, s[96:99], s47 offen offset:0\n\t"
    "buffer_load_dwordx4 v[224:227], v138, s[96:99], s47 offen offset:0\n\t"
    "buffer_load_dwordx4 v[228:231], v137, s[96:99], s47 offen offset:64\n\t"
    "buffer_load_dwordx4 v[232:235], v138, s[96:99], s47 offen offset:64\n\t"
    "s_add_u32 s47, s47, 0x100000\n\t"
    "buffer_load_dwordx4 v[236:239], v137, s[96:99], s47 offen offset:0\n\t"
    "buffer_load_dwordx4 v[240:243], v138, s[96:99], s47 offen offset:0\n\t"
    "buffer_load_dwordx4 v[244:247], v137, s[96:99], s47 offen offset:64\n\t"
    "buffer_load_dwordx4 v[248:251], v138, s[96:99], s47 offen offset:64\n\t"
    "s_add_u32 s47, s47, 0x100000\n\t"
    "s_waitcnt lgkmcnt(11)\n\t"
    "v_and_b32 v123, v123, v115\n\t"
    "v_perm_b32 v123, v123, v123, s46\n\t"
    "ds_or_b32 v97, v123\n\t"
    "v_and_b32 v124, v124, v116\n\t"
    "v_perm_b32 v124, v124, v124, s46\n\t"
    "ds_or_b32 v98, v124\n\t"
    "v_and_b32 v125, v125, v117\n\t"
    "v_perm_b32 v125, v125, v125, s46\n\t"
    "ds_or_b32 v99, v125\n\t"
    "s_waitcnt lgkmcnt(11)\n\t"
    "buffer_store_dwordx4 v[72:75], v66, %12, s49 offen offset:128 sc1\n\t"
    "v_mov_b32 v129, 0xffff0000\n\t"
    "s_waitcnt lgkmcnt(7)\n\t"
    "v_perm_b32 v100, v100, v84, v126\n\t"
    "v_perm_b32 v101, v101, v85, v126\n\t"
    "v_perm_b32 v102, v102, v86, v126\n\t"
    "v_perm_b32 v103, v103, v87, v126\n\t"
    "s_waitcnt lgkmcnt(3)\n\t"
    "v_perm_b32 v104, v104, v88, v126\n\t"
    "v_perm_b32 v105, v105, v89, v126\n\t"
    "v_perm_b32 v106, v106, v90, v126\n\t"
    "v_perm_b32 v107, v107, v91, v126\n\t"
    "v_max_u32 v108, v100, v101\n\t"
    "v_min_u32 v101, v100, v101\n\t"
    "v_max_u32 v100, v102, v103\n\t"
    "v_min_u32 v103, v102, v103\n\t"
    "v_max_u32 v102, v104, v105\n\t"
    "v_min_u32 v105, v104, v105\n\t"
    "v_max_u32 v104, v106, v107\n\t"
    "v_min_u32 v107, v106, v107\n\t"
    "v_max_u32 v106, v108, v100\n\t"
    "v_min_u32 v100, v108, v100\n\t"
    "v_max_u32 v108, v101, v103\n\t"
    "v_min_u32 v103, v101, v103\n\t"
    "v_max_u32 v101, v102, v104\n\t"
    "v_min_u32 v104, v102, v104\n\t"
    "v_max_u32 v102, v105, v107\n\t"
    "v_min_u32 v107, v105, v107\n\t"
    "v_bfi_b32 v130, v106, 0, v129\n\t"
    "v_cmp_eq_u32 vcc, 0, v130\n\t"
    "v_and_or_b32 v92, v106, s44, v68\n\t"
    "s_nop 0\n\t"
    "v_cndmask_b32 v130, v130, v129, vcc\n\t"
    "v_ffbl_b32 v131, v130\n\t"
    "v_and_b32 v134, 7, v131\n\t"
    "v_lshlrev_b32 v134, v134, s45\n\t"
    "v_xor_b32 v129, v129, v134\n\t"
    "v_xor_b32 v132, v131, v127\n\t"
    "v_bfi_b32 v130, v101, 0, v129\n\t"
    "v_cmp_eq_u32 vcc, 0, v130\n\t"
    "v_and_or_b32 v93, v101, s44, v68\n\t"
    "v_xor_b32 v133, 23, v132\n\t"
    "v_lshlrev_b32 v110, v132, v136\n\t"
    "v_and_b32 v132, 7, v132\n\t"
    "v_lshl_or_b32 v110, v136, v133, v110\n\t"
    "v_lshl_add_u32 v132, v132, 1, v128\n\t"
    "ds_or_rtn_b32 v118, v92, v110\n\t"
    "ds_write_b16 v132, v106\n\t"
    "v_cndmask_b32 v130, v130, v129, vcc\n\t"
    "v_ffbl_b32 v131, v130\n\t"
    "v_and_b32 v134, 7, v131\n\t"
    "v_lshlrev_b32 v134, v134, s45\n\t"
    "v_xor_b32 v129, v129, v134\n\t"
    "v_xor_b32 v132, v131, v127\n\t"
    "v_bfi_b32 v130, v108, 0, v129\n\t"
    "v_cmp_eq_u32 vcc, 0, v130\n\t"
    "v_and_or_b32 v94, v108, s44, v68\n\t"
    "v_xor_b32 v133, 23, v132\n\t"
    "v_lshlrev_b32 v111, v132, v136\n\t"
    "v_and_b32 v132, 7, v132\n\t"
    "v_lshl_or_b32 v111, v136, v133, v111\n\t"
    "v_lshl_add_u32 v132, v132, 1, v128\n\t"
    "ds_or_rtn_b32 v119, v93, v111\n\t"
    "ds_write_b16 v132, v101\n\t"
    "v_cndmask_b32 v130, v130, v129, vcc\n\t"
    "v_ffbl_b32 v131, v130\n\t"
    "v_and_b32 v134, 7, v131\n\t"
    "v_lshlrev_b32 v134, v134, s45\n\t"
    "v_xor_b32 v129, v129, v134\n\t"
    "v_xor_b32 v132, v131, v127\n\t"
    "v_bfi_b32 v130, v100, 0, v129\n\t"
    "v_cmp_eq_u32 vcc, 0, v130\n\t"
    "v_and_or_b32 v95, v100, s44, v68\n\t"
    "v_xor_b32 v133, 23, v132\n\t"
    "v_lshlrev_b32 v112, v132, v136\n\t"
    "v_and_b32 v132, 7, v132\n\t"
    "v_lshl_or_b32 v112, v136, v133, v112\n\t"
    "v_lshl_add_u32 v132, v132, 1, v128\n\t"
    "ds_or_rtn_b32 v120, v94, v112\n\t"
    "ds_write_b16 v132, v108\n\t"
    "v_cndmask_b32 v130, v130, v129, vcc\n\t"
    "v_ffbl_b32 v131, v130\n\t"
    "v_and_b32 v134, 7, v131\n\t"
    "v_lshlrev_b32 v134, v134, s45\n\t"
    "v_xor_b32 v129, v129, v134\n\t"
    "v_xor_b32 v132, v131, v127\n\t"
    "v_bfi_b32 v130, v102, 0, v129\n\t"
    "v_cmp_eq_u32 vcc, 0, v130\n\t"
    "v_and_or_b32 v96, v102, s44, v68\n\t"
    "v_xor_b32 v133, 23, v132\n\t"
    "v_lshlrev_b32 v113, v132, v136\n\t"
    "v_and_b32 v132, 7, v132\n\t"
    "v_lshl_or_b32 v113, v136, v133, v113\n\t"
    "v_lshl_add_u32 v132, v132, 1, v128\n\t"
    "ds_or_rtn_b32 v121, v95, v113\n\t"
    "ds_write_b16 v132, v100\n\t"
    "v_cndmask_b32 v130, v130, v129, vcc\n\t"
    "v_ffbl_b32 v131, v130\n\t"
    "v_and_b32 v134, 7, v131\n\t"
    "v_lshlrev_b32 v134, v134, s45\n\t"
    "v_xor_b32 v129, v129, v134\n\t"
    "v_xor_b32 v132, v131, v127\n\t"
    "s_waitcnt lgkmcnt(7)\n\t"
    "v_and_b32 v118, v118, v110\n\t"
    "v_perm_b32 v118, v118, v118, s46\n\t"
    "ds_or_b32 v92, v118\n\t"
    "v_bfi_b32 v130, v104, 0, v129\n\t"
    "v_cmp_eq_u32 vcc, 0, v130\n\t"
    "v_and_or_b32 v97, v104, s44, v68\n\t"
    "v_xor_b32 v133, 23, v132\n\t"
    "v_lshlrev_b32 v114, v132, v136\n\t"
    "v_and_b32 v132, 7, v132\n\t"
    "v_lshl_or_b32 v114, v136, v133, v114\n\t"
    "v_lshl_add_u32 v132, v132, 1, v128\n\t"
    "ds_or_rtn_b32 v122, v96, v114\n\t"
    "ds_write_b16 v132, v102\n\t"
    "v_cndmask_b32 v130, v130, v129, vcc\n\t"
    "v_ffbl_b32 v131, v130\n\t"
    "v_and_b32 v134, 7, v131\n\t"
    "v_lshlrev_b32 v134, v134, s45\n\t"
    "v_xor_b32 v129, v129, v134\n\t"
    "v_xor_b32 v132, v131, v127\n\t"
    "s_waitcnt lgkmcnt(8)\n\t"
    "v_and_b32 v119, v119, v111\n\t"
    "v_perm_b32 v119, v119, v119, s46\n\t"
    "ds_or_b32 v93, v119\n\t"
    "v_bfi_b32 v130, v103, 0, v129\n\t"
    "v_cmp_eq_u32 vcc, 0, v130\n\t"
    "v_and_or_b32 v98, v103, s44, v68\n\t"
    "v_xor_b32 v133, 23, v132\n\t"
    "v_lshlrev_b32 v115, v132, v136\n\t"
    "v_and_b32 v132, 7, v132\n\t"
    "v_lshl_or_b32 v115, v136, v133, v115\n\t"
    "v_lshl_add_u32 v132, v132, 1, v128\n\t"
    "ds_or_rtn_b32 v123, v97, v115\n\t"
    "ds_write_b16 v132, v104\n\t"
    "v_cndmask_b32 v130, v130, v129, vcc\n\t"
    "v_ffbl_b32 v131, v130\n\t"
    "v_and_b32 v134, 7, v131\n\t"
    "v_lshlrev_b32 v134, v134, s45\n\t"
    "v_xor_b32 v129, v129, v134\n\t"
    "v_xor_b32 v132, v131, v127\n\t"
    "s_waitcnt lgkmcnt(9)\n\t"
    "v_and_b32 v120, v120, v112\n\t"
    "v_perm_b32 v120, v120, v120, s46\n\t"
    "ds_or_b32 v94, v120\n\t"
    "v_bfi_b32 v130, v107, 0, v129\n\t"
    "v_cmp_eq_u32 vcc, 0, v130\n\t"
    "v_and_or_b32 v99, v107, s44, v68\n\t"
    "v_xor_b32 v133, 23, v132\n\t"
    "v_lshlrev_b32 v116, v132, v136\n\t"
    "v_and_b32 v132, 7, v132\n\t"
    "v_lshl_or_b32 v116, v136, v133, v116\n\t"
    "v_lshl_add_u32 v132, v132, 1, v128\n\t"
    "ds_or_rtn_b32 v124, v98, v116\n\t"
    "ds_write_b16 v132, v103\n\t"
    "v_cndmask_b32 v130, v130, v129, vcc\n\t"
    "v_ffbl_b32 v131, v130\n\t"
    "v_xor_b32 v132, v131, v127\n\t"
    "s_waitcnt lgkmcnt(10)\n\t"
    "v_and_b32 v121, v121, v113\n\t"
    "v_perm_b32 v121, v121, v121, s46\n\t"
    "ds_or_b32 v95, v121\n\t"
    "v_xor_b32 v133, 23, v132\n\t"
    "v_lshlrev_b32 v117, v132, v136\n\t"
    "v_and_b32 v132, 7, v132\n\t"
    "v_lshl_or_b32 v117, v136, v133, v117\n\t"
    "v_lshl_add_u32 v132, v132, 1, v128\n\t"
    "ds_or_rtn_b32 v125, v99, v117\n\t"
    "ds_write_b16 v132, v107\n\t"
    "s_waitcnt lgkmcnt(10)\n\t"
    "v_and_b32 v122, v122, v114\n\t"
    "v_perm_b32 v122, v122, v122, s46\n\t"
    "ds_or_b32 v96, v122\n\t"
    "ds_read_b128 v[72:75], v128\n\t"
    "v_and_or_b32 v100, v76, s44, v68\n\t"
    "ds_read_b32 v100, v100\n\t"
    "v_and_or_b32 v101, v77, s44, v68\n\t"
    "ds_read_b32 v101, v101\n\t"
    "v_and_or_b32 v102, v78, s44, v68\n\t"
    "ds_read_b32 v102, v102\n\t"
    "v_and_or_b32 v103, v79, s44, v68\n\t"
    "ds_read_b32 v103, v103\n\t"
    "v_and_or_b32 v104, v80, s44, v68\n\t"
    "ds_read_b32 v104, v104\n\t"
    "v_and_or_b32 v105, v81, s44, v68\n\t"
    "ds_read_b32 v105, v105\n\t"
    "v_and_or_b32 v106, v82, s44, v68\n\t"
    "ds_read_b32 v106, v106\n\t"
    "v_and_or_b32 v107, v83, s44, v68\n\t"
    "ds_read_b32 v107, v107\n\t"
    "s_waitcnt vmcnt(36)\n\t"
    "v_lshl_add_u32 v84, v40, 3, s36\n\t"
    "v_lshl_add_u32 v85, v41, 3, s37\n\t"
    "v_lshl_add_u32 v86, v42, 3, s38\n\t"
    "v_lshl_add_u32 v87, v43, 3, s39\n\t"
    "v_lshl_add_u32 v88, v44, 3, s40\n\t"
    "v_lshl_add_u32 v89, v45, 3, s41\n\t"
    "v_lshl_add_u32 v90, v46, 3, s42\n\t"
    "v_lshl_add_u32 v91, v47, 3, s43\n\t"
    "buffer_load_dwordx4 v[24:27], v137, s[96:99], s47 offen offset:0\n\t"
    "buffer_load_dwordx4 v[28:31], v138, s[96:99], s47 offen offset:0\n\t"
    "buffer_load_dwordx4 v[32:35], v137, s[96:99], s47 offen offset:64\n\t"
    "buffer_load_dwordx4 v[36:39], v138, s[96:99], s47 offen offset:64\n\t"
    "s_add_u32 s47, s47, 0x100000\n\t"
    "buffer_load_dwordx4 v[40:43], v137, s[96:99], s47 offen offset:0\n\t"
    "buffer_load_dwordx4 v[44:47], v138, s[96:99], s47 offen offset:0\n\t"
    "s_waitcnt lgkmcnt(11)\n\t"
    "v_and_b32 v123, v123, v115\n\t"
    "v_perm_b32 v123, v123, v123, s46\n\t"
    "ds_or_b32 v97, v123\n\t"
    "v_and_b32 v124, v124, v116\n\t"
    "v_perm_b32 v124, v124, v124, s46\n\t"
    "ds_or_b32 v98, v124\n\t"
    "v_and_b32 v125, v125, v117\n\t"
    "v_perm_b32 v125, v125, v125, s46\n\t"
    "ds_or_b32 v99, v125\n\t"
    "s_waitcnt lgkmcnt(11)\n\t"
    "buffer_store_dwordx4 v[72:75], v66, %12, s49 offen offset:192 sc1\n\t"
    "v_mov_b32 v129, 0xffff0000\n\t"
    "s_waitcnt lgkmcnt(7)\n\t"
    "v_perm_b32 v100, v100, v76, v126\n\t"
    "v_perm_b32 v101, v101, v77, v126\n\t"
    "v_perm_b32 v102, v102, v78, v126\n\t"
    "v_perm_b32 v103, v103, v79, v126\n\t"
    "s_waitcnt lgkmcnt(3)\n\t"
    "v_perm_b32 v104, v104, v80, v126\n\t"
    "v_perm_b32 v105, v105, v81, v126\n\t"
    "v_perm_b32 v106, v106, v82, v126\n\t"
    "v_perm_b32 v107, v107, v83, v126\n\t"
    "s_waitcnt vmcnt(33)\n\t"
    "ds_write_b128 v141, v[156:159] offset:0\n\t"
    "ds_write_b128 v141, v[160:163] offset:256\n\t"
    "ds_read_b64_tr_b16 v[156:157], v142 offset:0\n\t"
    "ds_read_b64_tr_b16 v[158:159], v142 offset:256\n\t"
    "ds_read_b64_tr_b16 v[160:161], v142 offset:32\n\t"
    "ds_read_b64_tr_b16 v[162:163], v142 offset:288\n\t"
    "s_waitcnt vmcnt(31)\n\t"
    "ds_write_b128 v141, v[164:167] offset:2112\n\t"
    "ds_write_b128 v141, v[168:171] offset:2368\n\t"
    "ds_read_b64_tr_b16 v[164:165], v142 offset:2112\n\t"
    "ds_read_b64_tr_b16 v[166:167], v142 offset:2368\n\t"
    "ds_read_b64_tr_b16 v[168:169], v142 offset:2144\n\t"
    "ds_read_b64_tr_b16 v[170:171], v142 offset:2400\n\t"
    "v_max_u32 v108, v100, v101\n\t"
    "v_min_u32 v101, v100, v101\n\t"
    "v_max_u32 v100, v102, v103\n\t"
    "v_min_u32 v103, v102, v103\n\t"
    "v_max_u32 v102, v104, v105\n\t"
    "v_min_u32 v105, v104, v105\n\t"
    "v_max_u32 v104, v106, v107\n\t"
    "v_min_u32 v107, v106, v107\n\t"
    "v_max_u32 v106, v108, v100\n\t"
    "v_min_u32 v100, v108, v100\n\t"
    "v_max_u32 v108, v101, v103\n\t"
    "v_min_u32 v103, v101, v103\n\t"
    "v_max_u32 v101, v102, v104\n\t"
    "v_min_u32 v104, v102, v104\n\t"
    "v_max_u32 v102, v105, v107\n\t"
    "v_min_u32 v107, v105, v107\n\t"
    "v_bfi_b32 v130, v106, 0, v129\n\t"
    "v_cmp_eq_u32 vcc, 0, v130\n\t"
    "v_and_or_b32 v92, v106, s44, v68\n\t"
    "s_nop 0\n\t"
    "v_cndmask_b32 v130, v130, v129, vcc\n\t"
    "v_ffbl_b32 v131, v130\n\t"
    "v_and_b32 v134, 7, v131\n\t"
    "v_lshlrev_b32 v134, v134, s45\n\t"
    "v_xor_b32 v129, v129, v134\n\t"
    "v_xor_b32 v132, v131, v127\n\t"
    "v_bfi_b32 v130, v101, 0, v129\n\t"
    "v_cmp_eq_u32 vcc, 0, v130\n\t"
    "v_and_or_b32 v93, v101, s44, v68\n\t"
    "v_xor_b32 v133, 23, v132\n\t"
    "v_lshlrev_b32 v110, v132, v136\n\t"
    "v_and_b32 v132, 7, v132\n\t"
    "v_lshl_or_b32 v110, v136, v133, v110\n\t"
    "v_lshl_add_u32 v132, v132, 1, v128\n\t"
    "ds_or_rtn_b32 v118, v92, v110\n\t"
    "ds_write_b16 v132, v106\n\t"
    "v_cndmask_b32 v130, v130, v129, vcc\n\t"
    "v_ffbl_b32 v131, v130\n\t"
    "v_and_b32 v134, 7, v131\n\t"
    "v_lshlrev_b32 v134, v134, s45\n\t"
    "v_xor_b32 v129, v129, v134\n\t"
    "v_xor_b32 v132, v131, v127\n\t"
    "v_bfi_b32 v130, v108, 0, v129\n\t"
    "v_cmp_eq_u32 vcc, 0, v130\n\t"
    "v_and_or_b32 v94, v108, s44, v68\n\t"
    "v_xor_b32 v133, 23, v132\n\t"
    "v_lshlrev_b32 v111, v132, v136\n\t"
    "v_and_b32 v132, 7, v132\n\t"
    "v_lshl_or_b32 v111, v136, v133, v111\n\t"
    "v_lshl_add_u32 v132, v132, 1, v128\n\t"
    "ds_or_rtn_b32 v119, v93, v111\n\t"
    "ds_write_b16 v132, v101\n\t"
    "v_cndmask_b32 v130, v130, v129, vcc\n\t"
    "v_ffbl_b32 v131, v130\n\t"
    "v_and_b32 v134, 7, v131\n\t"
    "v_lshlrev_b32 v134, v134, s45\n\t"
    "v_xor_b32 v129, v129, v134\n\t"
    "v_xor_b32 v132, v131, v127\n\t"
    "s_waitcnt lgkmcnt(10)\n\t"
    "buffer_store_dwordx4 v[156:159], v139, %12, s48 offen sc1\n\t"
    "buffer_store_dwordx4 v[160:163], v140, %12, s48 offen sc1\n\t"
    "s_add_u32 s48, s48, 819200\n\t"
    "s_waitcnt lgkmcnt(4)\n\t"
    "buffer_store_dwordx4 v[164:167], v139, %12, s48 offen sc1\n\t"
    "buffer_store_dwordx4 v[168:171], v140, %12, s48 offen sc1\n\t"
    "s_add_u32 s48, s48, -811008\n\t"
    "s_waitcnt vmcnt(33)\n\t"
    "ds_write_b128 v141, v[172:175] offset:0\n\t"
    "ds_write_b128 v141, v[176:179] offset:256\n\t"
    "ds_read_b64_tr_b16 v[172:173], v142 offset:0\n\t"
    "ds_read_b64_tr_b16 v[174:175], v142 offset:256\n\t"
    "ds_read_b64_tr_b16 v[176:177], v142 offset:32\n\t"
    "ds_read_b64_tr_b16 v[178:179], v142 offset:288\n\t"
    "s_waitcnt vmcnt(31)\n\t"
    "ds_write_b128 v141, v[180:183] offset:2112\n\t"
    "ds_write_b128 v141, v[184:187] offset:2368\n\t"
    "ds_read_b64_tr_b16 v[180:181], v142 offset:2112\n\t"
    "ds_read_b64_tr_b16 v[182:183], v142 offset:2368\n\t"
    "ds_read_b64_tr_b16 v[184:185], v142 offset:2144\n\t"
    "ds_read_b64_tr_b16 v[186:187], v142 offset:2400\n\t"
    "v_bfi_b32 v130, v100, 0, v129\n\t"
    "v_cmp_eq_u32 vcc, 0, v130\n\t"
    "v_and_or_b32 v95, v100, s44, v68\n\t"
    "v_xor_b32 v133, 23, v132\n\t"
    "v_lshlrev_b32 v112, v132, v136\n\t"
    "v_and_b32 v132, 7, v132\n\t"
    "v_lshl_or_b32 v112, v136, v133, v112\n\t"
    "v_lshl_add_u32 v132, v132, 1, v128\n\t"
    "ds_or_rtn_b32 v120, v94, v112\n\t"
    "ds_write_b16 v132, v108\n\t"
    "v_cndmask_b32 v130, v130, v129, vcc\n\t"
    "v_ffbl_b32 v131, v130\n\t"
    "v_and_b32 v134, 7, v131\n\t"
    "v_lshlrev_b32 v134, v134, s45\n\t"
    "v_xor_b32 v129, v129, v134\n\t"
    "v_xor_b32 v132, v131, v127\n\t"
    "v_bfi_b32 v130, v102, 0, v129\n\t"
    "v_cmp_eq_u32 vcc, 0, v130\n\t"
    "v_and_or_b32 v96, v102, s44, v68\n\t"
    "v_xor_b32 v133, 23, v132\n\t"
    "v_lshlrev_b32 v113, v132, v136\n\t"
    "v_and_b32 v132, 7, v132\n\t"
    "v_lshl_or_b32 v113, v136, v133, v113\n\t"
    "v_lshl_add_u32 v132, v132, 1, v128\n\t"
    "ds_or_rtn_b32 v121, v95, v113\n\t"
    "ds_write_b16 v132, v100\n\t"
    "v_cndmask_b32 v130, v130, v129, vcc\n\t"
    "v_ffbl_b32 v131, v130\n\t"
    "v_and_b32 v134, 7, v131\n\t"
    "v_lshlrev_b32 v134, v134, s45\n\t"
    "v_xor_b32 v129, v129, v134\n\t"
    "v_xor_b32 v132, v131, v127\n\t"
    "s_waitcnt lgkmcnt(15)\n\t"
    "v_and_b32 v118, v118, v110\n\t"
    "v_perm_b32 v118, v118, v118, s46\n\t"
    "ds_or_b32 v92, v118\n\t"
    "s_lshl_b32 s52, %14, 2\n\t"
    "s_add_u32 s52, s52, 49291264\n\t"
    "buffer_load_dword v71, off, %12, s52 offset:1024 sc1\n\t"
    "v_bfi_b32 v130, v104, 0, v129\n\t"
    "v_cmp_eq_u32 vcc, 0, v130\n\t"
    "v_and_or_b32 v97, v104, s44, v68\n\t"
    "v_xor_b32 v133, 23, v132\n\t"
    "v_lshlrev_b32 v114, v132, v136\n\t"
    "v_and_b32 v132, 7, v132\n\t"
    "v_lshl_or_b32 v114, v136, v133, v114\n\t"
    "v_lshl_add_u32 v132, v132, 1, v128\n\t"
    "ds_or_rtn_b32 v122, v96, v114\n\t"
    "ds_write_b16 v132, v102\n\t"
    "v_cndmask_b32 v130, v130, v129, vcc\n\t"
    "v_ffbl_b32 v131, v130\n\t"
    "v_and_b32 v134, 7, v131\n\t"
    "v_lshlrev_b32 v134, v134, s45\n\t"
    "v_xor_b32 v129, v129, v134\n\t"
    "v_xor_b32 v132, v131, v127\n\t"
    "v_and_b32 v119, v119, v111\n\t"
    "v_perm_b32 v119, v119, v119, s46\n\t"
    "ds_or_b32 v93, v119\n\t"
    "s_waitcnt lgkmcnt(14)\n\t"
    "buffer_store_dwordx4 v[172:175], v139, %12, s48 offen sc1\n\t"
    "buffer_store_dwordx4 v[176:179], v140, %12, s48 offen sc1\n\t"
    "s_add_u32 s48, s48, 819200\n\t"
    "s_waitcnt lgkmcnt(8)\n\t"
    "buffer_store_dwordx4 v[180:183], v139, %12, s48 offen sc1\n\t"
    "buffer_store_dwordx4 v[184:187], v140, %12, s48 offen sc1\n\t"
    "s_add_u32 s48, s48, -811008\n\t"
    "s_waitcnt vmcnt(34)\n\t"
    "ds_write_b128 v141, v[188:191] offset:0\n\t"
    "ds_write_b128 v141, v[192:195] offset:256\n\t"
    "ds_read_b64_tr_b16 v[188:189], v142 offset:0\n\t"
    "ds_read_b64_tr_b16 v[190:191], v142 offset:256\n\t"
    "ds_read_b64_tr_b16 v[192:193], v142 offset:32\n\t"
    "ds_read_b64_tr_b16 v[194:195], v142 offset:288\n\t"
    "s_waitcnt vmcnt(32)\n\t"
    "ds_write_b128 v141, v[196:199] offset:2112\n\t"
    "ds_write_b128 v141, v[200:203] offset:2368\n\t"
    "ds_read_b64_tr_b16 v[196:197], v142 offset:2112\n\t"
    "ds_read_b64_tr_b16 v[198:199], v142 offset:2368\n\t"
    "ds_read_b64_tr_b16 v[200:201], v142 offset:2144\n\t"
    "ds_read_b64_tr_b16 v[202:203], v142 offset:2400\n\t"
    "v_bfi_b32 v130, v103, 0, v129\n\t"
    "v_cmp_eq_u32 vcc, 0, v130\n\t"
    "v_and_or_b32 v98, v103, s44, v68\n\t"
    "v_xor_b32 v133, 23, v132\n\t"
    "v_lshlrev_b32 v115, v132, v136\n\t"
    "v_and_b32 v132, 7, v132\n\t"
    "v_lshl_or_b32 v115, v136, v133, v115\n\t"
    "v_lshl_add_u32 v132, v132, 1, v128\n\t"
    "ds_or_rtn_b32 v123, v97, v115\n\t"
    "ds_write_b16 v132, v104\n\t"
    "v_cndmask_b32 v130, v130, v129, vcc\n\t"
    "v_ffbl_b32 v131, v130\n\t"
    "v_and_b32 v134, 7, v131\n\t"
    "v_lshlrev_b32 v134, v134, s45\n\t"
    "v_xor_b32 v129, v129, v134\n\t"
    "v_xor_b32 v132, v131, v127\n\t"
    "s_waitcnt lgkmcnt(15)\n\t"
    "v_and_b32 v120, v120, v112\n\t"
    "v_perm_b32 v120, v120, v120, s46\n\t"
    "ds_or_b32 v94, v120\n\t"
    "v_bfi_b32 v130, v107, 0, v129\n\t"
    "v_cmp_eq_u32 vcc, 0, v130\n\t"
    "v_and_or_b32 v99, v107, s44, v68\n\t"
    "v_xor_b32 v133, 23, v132\n\t"
    "v_lshlrev_b32 v116, v132, v136\n\t"
    "v_and_b32 v132, 7, v132\n\t"
    "v_lshl_or_b32 v116, v136, v133, v116\n\t"
    "v_lshl_add_u32 v132, v132, 1, v128\n\t"
    "ds_or_rtn_b32 v124, v98, v116\n\t"
    "ds_write_b16 v132, v103\n\t"
    "v_cndmask_b32 v130, v130, v129, vcc\n\t"
    "v_ffbl_b32 v131, v130\n\t"
    "v_xor_b32 v132, v131, v127\n\t"
    "v_and_b32 v121, v121, v113\n\t"
    "v_perm_b32 v121, v121, v121, s46\n\t"
    "ds_or_b32 v95, v121\n\t"
    "v_xor_b32 v133, 23, v132\n\t"
    "v_lshlrev_b32 v117, v132, v136\n\t"
    "v_and_b32 v132, 7, v132\n\t"
    "v_lshl_or_b32 v117, v136, v133, v117\n\t"
    "v_lshl_add_u32 v132, v132, 1, v128\n\t"
    "ds_or_rtn_b32 v125, v99, v117\n\t"
    "ds_write_b16 v132, v107\n\t"
    "v_and_b32 v122, v122, v114\n\t"
    "v_perm_b32 v122, v122, v122, s46\n\t"
    "ds_or_b32 v96, v122\n\t"
    "ds_read_b128 v[72:75], v128\n\t"
    "s_waitcnt lgkmcnt(15)\n\t"
    "buffer_store_dwordx4 v[188:191], v139, %12, s48 offen sc1\n\t"
    "buffer_store_dwordx4 v[192:195], v140, %12, s48 offen sc1\n\t"
    "s_add_u32 s48, s48, 819200\n\t"
    "s_waitcnt lgkmcnt(10)\n\t"
    "buffer_store_dwordx4 v[196:199], v139, %12, s48 offen sc1\n\t"
    "buffer_store_dwordx4 v[200:203], v140, %12, s48 offen sc1\n\t"
    "s_add_u32 s48, s48, -811008\n\t"
    "v_mov_b32 v134, 1036\n\t"
    "L_asvsp12_%=:\n\t"
    "ds_read_b32 v130, v134\n\t"
    "s_waitcnt lgkmcnt(0)\n\t"
    "v_readfirstlane_b32 s50, v130\n\t"
    "s_nop 0\n\t"
    "s_cmp_lt_u32 s50, 1\n\t"
    "s_cbranch_scc1 L_asvsp12_%=\n\t"
    "v_and_or_b32 v100, v84, s44, v69\n\t"
    "ds_read_b32 v100, v100\n\t"
    "v_and_or_b32 v101, v85, s44, v69\n\t"
    "ds_read_b32 v101, v101\n\t"
    "v_and_or_b32 v102, v86, s44, v69\n\t"
    "ds_read_b32 v102, v102\n\t"
    "v_and_or_b32 v103, v87, s44, v69\n\t"
    "ds_read_b32 v103, v103\n\t"
    "v_and_or_b32 v104, v88, s44, v69\n\t"
    "ds_read_b32 v104, v104\n\t"
    "v_and_or_b32 v105, v89, s44, v69\n\t"
    "ds_read_b32 v105, v105\n\t"
    "v_and_or_b32 v106, v90, s44, v69\n\t"
    "ds_read_b32 v106, v106\n\t"
    "v_and_or_b32 v107, v91, s44, v69\n\t"
    "ds_read_b32 v107, v107\n\t"
    "v_lshl_add_u32 v76, v48, 3, s36\n\t"
    "v_lshl_add_u32 v77, v49, 3, s37\n\t"
    "v_lshl_add_u32 v78, v50, 3, s38\n\t"
    "v_lshl_add_u32 v79, v51, 3, s39\n\t"
    "v_lshl_add_u32 v80, v52, 3, s40\n\t"
    "v_lshl_add_u32 v81, v53, 3, s41\n\t"
    "v_lshl_add_u32 v82, v54, 3, s42\n\t"
    "v_lshl_add_u32 v83, v55, 3, s43\n\t"
    "v_mov_b32 v134, 2076\n\t"
    "L_asvsp1052_%=:\n\t"
    "ds_read_b32 v130, v134\n\t"
    "s_waitcnt lgkmcnt(0)\n\t"
    "v_readfirstlane_b32 s50, v130\n\t"
    "s_nop 0\n\t"
    "s_cmp_lt_u32 s50, 1\n\t"
    "s_cbranch_scc1 L_asvsp1052_%=\n\t"
    "v_mov_b32 v70, 2072\n\t"
    "ds_read_b32 v70, v70\n\t"
    "s_and_b32 s50, %14, 7\n\t"
    "s_lshr_b32 s51, %14, 5\n\t"
    "s_add_u32 s50, s50, 8\n\t"
    "s_lshl_b32 s50, s50, 7\n\t"
    "s_lshl_b32 s51, s51, 7\n\t"
    "v_mov_b32 v132, s51\n\t"
    "v_writelane_b32 v132, s50, 0\n\t"
    "s_waitcnt vmcnt(8)\n\t"
    "s_waitcnt vmcnt(0)\n\t"
    "s_waitcnt lgkmcnt(0)\n\t"
    "v_cmp_ne_u32 vcc, v71, v70\n\t"
    "s_cbranch_vccz L_ahsok_%=\n\t"
    "s_mov_b32 s51, 1048576\n\t"
    "L_ahs_%=:\n\t"
    "buffer_load_dword v71, off, %12, s52 offset:1024 sc1\n\t"
    "s_sub_u32 s51, s51, 1\n\t"
    "s_waitcnt vmcnt(0)\n\t"
    "s_cmp_eq_u32 s51, 0\n\t"
    "s_cbranch_scc1 L_ahsok_%=\n\t"
    "v_cmp_ne_u32 vcc, v71, v70\n\t"
    "s_cbranch_vccnz L_ahs_%=\n\t"
    "L_ahsok_%=:\n\t"
    "s_mov_b32 s50, 49284096\n\t"
    "s_mov_b64 exec, 3\n\t"
    "buffer_atomic_add v136, v132, %12, s50 offen sc1\n\t"
    "s_mov_b64 exec, -1\n\t"
    "buffer_load_dwordx4 v[156:159], v137, s[96:99], s47 offen offset:64\n\t"
    "buffer_load_dwordx4 v[160:163], v138, s[96:99], s47 offen offset:64\n\t"
    "s_add_u32 s47, s47, 0x100000\n\t"
    "buffer_load_dwordx4 v[164:167], v137, s[96:99], s47 offen offset:0\n\t"
    "buffer_load_dwordx4 v[168:171], v138, s[96:99], s47 offen offset:0\n\t"
    "buffer_load_dwordx4 v[172:175], v137, s[96:99], s47 offen offset:64\n\t"
    "buffer_load_dwordx4 v[176:179], v138, s[96:99], s47 offen offset:64\n\t"
    "s_add_u32 s47, s47, 0x100000\n\t"
    "buffer_load_dwordx4 v[180:183], v137, s[96:99], s47 offen offset:0\n\t"
    "buffer_load_dwordx4 v[184:187], v138, s[96:99], s47 offen offset:0\n\t"
    "buffer_load_dwordx4 v[188:191], v137, s[96:99], s47 offen offset:64\n\t"
    "buffer_load_dwordx4 v[192:195], v138, s[96:99], s47 offen offset:64\n\t"
    "s_add_u32 s47, s47, 0x100000\n\t"
    "buffer_load_dwordx4 v[196:199], v137, s[96:99], s47 offen offset:0\n\t"
    "buffer_load_dwordx4 v[200:203], v138, s[96:99], s47 offen offset:0\n\t"
    "v_and_b32 v123, v123, v115\n\t"
    "v_perm_b32 v123, v123, v123, s46\n\t"
    "ds_or_b32 v97, v123\n\t"
    "v_and_b32 v124, v124, v116\n\t"
    "v_perm_b32 v124, v124, v124, s46\n\t"
    "ds_or_b32 v98, v124\n\t"
    "v_and_b32 v125, v125, v117\n\t"
    "v_perm_b32 v125, v125, v125, s46\n\t"
    "ds_or_b32 v99, v125\n\t"
    "buffer_store_dwordx4 v[72:75], v66, %12, s49 offen offset:256 sc1\n\t"
    "v_mov_b32 v129, 0xffff0000\n\t"
    "v_perm_b32 v100, v100, v84, v126\n\t"
    "v_perm_b32 v101, v101, v85, v126\n\t"
    "v_perm_b32 v102, v102, v86, v126\n\t"
    "v_perm_b32 v103, v103, v87, v126\n\t"
    "v_perm_b32 v104, v104, v88, v126\n\t"
    "v_perm_b32 v105, v105, v89, v126\n\t"
    "v_perm_b32 v106, v106, v90, v126\n\t"
    "v_perm_b32 v107, v107, v91, v126\n\t"
    "ds_write_b128 v141, v[204:207] offset:0\n\t"
    "ds_write_b128 v141, v[208:211] offset:256\n\t"
    "ds_read_b64_tr_b16 v[204:205], v142 offset:0\n\t"
    "ds_read_b64_tr_b16 v[206:207], v142 offset:256\n\t"
    "ds_read_b64_tr_b16 v[208:209], v142 offset:32\n\t"
    "ds_read_b64_tr_b16 v[210:211], v142 offset:288\n\t"
    "ds_write_b128 v141, v[212:215] offset:2112\n\t"
    "ds_write_b128 v141, v[216:219] offset:2368\n\t"
    "ds_read_b64_tr_b16 v[212:213], v142 offset:2112\n\t"
    "ds_read_b64_tr_b16 v[214:215], v142 offset:2368\n\t"
    "ds_read_b64_tr_b16 v[216:217], v142 offset:2144\n\t"
    "ds_read_b64_tr_b16 v[218:219], v142 offset:2400\n\t"
    "v_max_u32 v108, v100, v101\n\t"
    "v_min_u32 v101, v100, v101\n\t"
    "v_max_u32 v100, v102, v103\n\t"
    "v_min_u32 v103, v102, v103\n\t"
    "v_max_u32 v102, v104, v105\n\t"
    "v_min_u32 v105, v104, v105\n\t"
    "v_max_u32 v104, v106, v107\n\t"
    "v_min_u32 v107, v106, v107\n\t"
    "v_max_u32 v106, v108, v100\n\t"
    "v_min_u32 v100, v108, v100\n\t"
    "v_max_u32 v108, v101, v103\n\t"
    "v_min_u32 v103, v101, v103\n\t"
    "v_max_u32 v101, v102, v104\n\t"
    "v_min_u32 v104, v102, v104\n\t"
    "v_max_u32 v102, v105, v107\n\t"
    "v_min_u32 v107, v105, v107\n\t"
    "v_bfi_b32 v130, v106, 0, v129\n\t"
    "v_cmp_eq_u32 vcc, 0, v130\n\t"
    "v_and_or_b32 v92, v106, s44, v69\n\t"
    "s_nop 0\n\t"
    "v_cndmask_b32 v130, v130, v129, vcc\n\t"
    "v_ffbl_b32 v131, v130\n\t"
    "v_and_b32 v134, 7, v131\n\t"
    "v_lshlrev_b32 v134, v134, s45\n\t"
    "v_xor_b32 v129, v129, v134\n\t"
    "v_xor_b32 v132, v131, v127\n\t"
    "v_bfi_b32 v130, v101, 0, v129\n\t"
    "v_cmp_eq_u32 vcc, 0, v130\n\t"
    "v_and_or_b32 v93, v101, s44, v69\n\t"
    "v_xor_b32 v133, 23, v132\n\t"
    "v_lshlrev_b32 v110, v132, v136\n\t"
    "v_and_b32 v132, 7, v132\n\t"
    "v_lshl_or_b32 v110, v136, v133, v110\n\t"
    "v_lshl_add_u32 v132, v132, 1, v128\n\t"
    "ds_or_rtn_b32 v118, v92, v110\n\t"
    "ds_write_b16 v132, v106\n\t"
    "v_cndmask_b32 v130, v130, v129, vcc\n\t"
    "v_ffbl_b32 v131, v130\n\t"
    "v_and_b32 v134, 7, v131\n\t"
    "v_lshlrev_b32 v134, v134, s45\n\t"
    "v_xor_b32 v129, v129, v134\n\t"
    "v_xor_b32 v132, v131, v127\n\t"
    "v_bfi_b32 v130, v108, 0, v129\n\t"
    "v_cmp_eq_u32 vcc, 0, v130\n\t"
    "v_and_or_b32 v94, v108, s44, v69\n\t"
    "v_xor_b32 v133, 23, v132\n\t"
    "v_lshlrev_b32 v111, v132, v136\n\t"
    "v_and_b32 v132, 7, v132\n\t"
    "v_lshl_or_b32 v111, v136, v133, v111\n\t"
    "v_lshl_add_u32 v132, v132, 1, v128\n\t"
    "ds_or_rtn_b32 v119, v93, v111\n\t"
    "ds_write_b16 v132, v101\n\t"
    "v_cndmask_b32 v130, v130, v129, vcc\n\t"
    "v_ffbl_b32 v131, v130\n\t"
    "v_and_b32 v134, 7, v131\n\t"
    "v_lshlrev_b32 v134, v134, s45\n\t"
    "v_xor_b32 v129, v129, v134\n\t"
    "v_xor_b32 v132, v131, v127\n\t"
    "s_waitcnt lgkmcnt(10)\n\t"
    "buffer_store_dwordx4 v[204:207], v139, %12, s48 offen sc1\n\t"
    "buffer_store_dwordx4 v[208:211], v140, %12, s48 offen sc1\n\t"
    "s_add_u32 s48, s48, 819200\n\t"
    "s_waitcnt lgkmcnt(4)\n\t"
    "buffer_store_dwordx4 v[212:215], v139, %12, s48 offen sc1\n\t"
    "buffer_store_dwordx4 v[216:219], v140, %12, s48 offen sc1\n\t"
    "s_add_u32 s48, s48, -811008\n\t"
    "ds_write_b128 v141, v[220:223] offset:0\n\t"
    "ds_write_b128 v141, v[224:227] offset:256\n\t"
    "ds_read_b64_tr_b16 v[220:221], v142 offset:0\n\t"
    "ds_read_b64_tr_b16 v[222:223], v142 offset:256\n\t"
    "ds_read_b64_tr_b16 v[224:225], v142 offset:32\n\t"
    "ds_read_b64_tr_b16 v[226:227], v142 offset:288\n\t"
    "ds_write_b128 v141, v[228:231] offset:2112\n\t"
    "ds_write_b128 v141, v[232:235] offset:2368\n\t"
    "ds_read_b64_tr_b16 v[228:229], v142 offset:2112\n\t"
    "ds_read_b64_tr_b16 v[230:231], v142 offset:2368\n\t"
    "ds_read_b64_tr_b16 v[232:233], v142 offset:2144\n\t"
    "ds_read_b64_tr_b16 v[234:235], v142 offset:2400\n\t"
    "v_bfi_b32 v130, v100, 0, v129\n\t"
    "v_cmp_eq_u32 vcc, 0, v130\n\t"
    "v_and_or_b32 v95, v100, s44, v69\n\t"
    "v_xor_b32 v133, 23, v132\n\t"
    "v_lshlrev_b32 v112, v132, v136\n\t"
    "v_and_b32 v132, 7, v132\n\t"
    "v_lshl_or_b32 v112, v136, v133, v112\n\t"
    "v_lshl_add_u32 v132, v132, 1, v128\n\t"
    "ds_or_rtn_b32 v120, v94, v112\n\t"
    "ds_write_b16 v132, v108\n\t"
    "v_cndmask_b32 v130, v130, v129, vcc\n\t"
    "v_ffbl_b32 v131, v130\n\t"
    "v_and_b32 v134, 7, v131\n\t"
    "v_lshlrev_b32 v134, v134, s45\n\t"
    "v_xor_b32 v129, v129, v134\n\t"
    "v_xor_b32 v132, v131, v127\n\t"
    "v_bfi_b32 v130, v102, 0, v129\n\t"
    "v_cmp_eq_u32 vcc, 0, v130\n\t"
    "v_and_or_b32 v96, v102, s44, v69\n\t"
    "v_xor_b32 v133, 23, v132\n\t"
    "v_lshlrev_b32 v113, v132, v136\n\t"
    "v_and_b32 v132, 7, v132\n\t"
    "v_lshl_or_b32 v113, v136, v133, v113\n\t"
    "v_lshl_add_u32 v132, v132, 1, v128\n\t"
    "ds_or_rtn_b32 v121, v95, v113\n\t"
    "ds_write_b16 v132, v100\n\t"
    "v_cndmask_b32 v130, v130, v129, vcc\n\t"
    "v_ffbl_b32 v131, v130\n\t"
    "v_and_b32 v134, 7, v131\n\t"
    "v_lshlrev_b32 v134, v134, s45\n\t"
    "v_xor_b32 v129, v129, v134\n\t"
    "v_xor_b32 v132, v131, v127\n\t"
    "s_waitcnt lgkmcnt(15)\n\t"
    "v_and_b32 v118, v118, v110\n\t"
    "v_perm_b32 v118, v118, v118, s46\n\t"
    "ds_or_b32 v92, v118\n\t"
    "v_bfi_b32 v130, v104, 0, v129\n\t"
    "v_cmp_eq_u32 vcc, 0, v130\n\t"
    "v_and_or_b32 v97, v104, s44, v69\n\t"
    "v_xor_b32 v133, 23, v132\n\t"
    "v_lshlrev_b32 v114, v132, v136\n\t"
    "v_and_b32 v132, 7, v132\n\t"
    "v_lshl_or_b32 v114, v136, v133, v114\n\t"
    "v_lshl_add_u32 v132, v132, 1, v128\n\t"
    "ds_or_rtn_b32 v122, v96, v114\n\t"
    "ds_write_b16 v132, v102\n\t"
    "v_cndmask_b32 v130, v130, v129, vcc\n\t"
    "v_ffbl_b32 v131, v130\n\t"
    "v_and_b32 v134, 7, v131\n\t"
    "v_lshlrev_b32 v134, v134, s45\n\t"
    "v_xor_b32 v129, v129, v134\n\t"
    "v_xor_b32 v132, v131, v127\n\t"
    "v_and_b32 v119, v119, v111\n\t"
    "v_perm_b32 v119, v119, v119, s46\n\t"
    "ds_or_b32 v93, v119\n\t"
    "s_waitcnt lgkmcnt(14)\n\t"
    "buffer_store_dwordx4 v[220:223], v139, %12, s48 offen sc1\n\t"
    "buffer_store_dwordx4 v[224:227], v140, %12, s48 offen sc1\n\t"
    "s_add_u32 s48, s48, 819200\n\t"
    "s_waitcnt lgkmcnt(8)\n\t"
    "buffer_store_dwordx4 v[228:231], v139, %12, s48 offen sc1\n\t"
    "buffer_store_dwordx4 v[232:235], v140, %12, s48 offen sc1\n\t"
    "s_add_u32 s48, s48, -811008\n\t"
    "ds_write_b128 v141, v[236:239] offset:0\n\t"
    "ds_write_b128 v141, v[240:243] offset:256\n\t"
    "ds_read_b64_tr_b16 v[236:237], v142 offset:0\n\t"
    "ds_read_b64_tr_b16 v[238:239], v142 offset:256\n\t"
    "ds_read_b64_tr_b16 v[240:241], v142 offset:32\n\t"
    "ds_read_b64_tr_b16 v[242:243], v142 offset:288\n\t"
    "ds_write_b128 v141, v[244:247] offset:2112\n\t"
    "ds_write_b128 v141, v[248:251] offset:2368\n\t"
    "ds_read_b64_tr_b16 v[244:245], v142 offset:2112\n\t"
    "ds_read_b64_tr_b16 v[246:247], v142 offset:2368\n\t"
    "ds_read_b64_tr_b16 v[248:249], v142 offset:2144\n\t"
    "ds_read_b64_tr_b16 v[250:251], v142 offset:2400\n\t"
    "v_bfi_b32 v130, v103, 0, v129\n\t"
    "v_cmp_eq_u32 vcc, 0, v130\n\t"
    "v_and_or_b32 v98, v103, s44, v69\n\t"
    "v_xor_b32 v133, 23, v132\n\t"
    "v_lshlrev_b32 v115, v132, v136\n\t"
    "v_and_b32 v132, 7, v132\n\t"
    "v_lshl_or_b32 v115, v136, v133, v115\n\t"
    "v_lshl_add_u32 v132, v132, 1, v128\n\t"
    "ds_or_rtn_b32 v123, v97, v115\n\t"
    "ds_write_b16 v132, v104\n\t"
    "v_cndmask_b32 v130, v130, v129, vcc\n\t"
    "v_ffbl_b32 v131, v130\n\t"
    "v_and_b32 v134, 7, v131\n\t"
    "v_lshlrev_b32 v134, v134, s45\n\t"
    "v_xor_b32 v129, v129, v134\n\t"
    "v_xor_b32 v132, v131, v127\n\t"
    "s_waitcnt lgkmcnt(15)\n\t"
    "v_and_b32 v120, v120, v112\n\t"
    "v_perm_b32 v120, v120, v120, s46\n\t"
    "ds_or_b32 v94, v120\n\t"
    "v_bfi_b32 v130, v107, 0, v129\n\t"
    "v_cmp_eq_u32 vcc, 0, v130\n\t"
    "v_and_or_b32 v99, v107, s44, v69\n\t"
    "v_xor_b32 v133, 23, v132\n\t"
    "v_lshlrev_b32 v116, v132, v136\n\t"
    "v_and_b32 v132, 7, v132\n\t"
    "v_lshl_or_b32 v116, v136, v133, v116\n\t"
    "v_lshl_add_u32 v132, v132, 1, v128\n\t"
    "ds_or_rtn_b32 v124, v98, v116\n\t"
    "ds_write_b16 v132, v103\n\t"
    "v_cndmask_b32 v130, v130, v129, vcc\n\t"
    "v_ffbl_b32 v131, v130\n\t"
    "v_xor_b32 v132, v131, v127\n\t"
    "v_and_b32 v121, v121, v113\n\t"
    "v_perm_b32 v121, v121, v121, s46\n\t"
    "ds_or_b32 v95, v121\n\t"
    "v_xor_b32 v133, 23, v132\n\t"
    "v_lshlrev_b32 v117, v132, v136\n\t"
    "v_and_b32 v132, 7, v132\n\t"
    "v_lshl_or_b32 v117, v136, v133, v117\n\t"
    "v_lshl_add_u32 v132, v132, 1, v128\n\t"
    "ds_or_rtn_b32 v125, v99, v117\n\t"
    "ds_write_b16 v132, v107\n\t"
    "v_and_b32 v122, v122, v114\n\t"
    "v_perm_b32 v122, v122, v122, s46\n\t"
    "ds_or_b32 v96, v122\n\t"
    "ds_read_b128 v[72:75], v128\n\t"
    "v_and_or_b32 v100, v76, s44, v69\n\t"
    "ds_read_b32 v100, v100\n\t"
    "v_and_or_b32 v101, v77, s44, v69\n\t"
    "ds_read_b32 v101, v101\n\t"
    "v_and_or_b32 v102, v78, s44, v69\n\t"
    "ds_read_b32 v102, v102\n\t"
    "v_and_or_b32 v103, v79, s44, v69\n\t"
    "ds_read_b32 v103, v103\n\t"
    "v_and_or_b32 v104, v80, s44, v69\n\t"
    "ds_read_b32 v104, v104\n\t"
    "v_and_or_b32 v105, v81, s44, v69\n\t"
    "ds_read_b32 v105, v105\n\t"
    "v_and_or_b32 v106, v82, s44, v69\n\t"
    "ds_read_b32 v106, v106\n\t"
    "v_and_or_b32 v107, v83, s44, v69\n\t"
    "ds_read_b32 v107, v107\n\t"
    "v_lshl_add_u32 v84, v56, 3, s36\n\t"
    "v_lshl_add_u32 v85, v57, 3, s37\n\t"
    "v_lshl_add_u32 v86, v58, 3, s38\n\t"
    "v_lshl_add_u32 v87, v59, 3, s39\n\t"
    "v_lshl_add_u32 v88, v60, 3, s40\n\t"
    "v_lshl_add_u32 v89, v61, 3, s41\n\t"
    "v_lshl_add_u32 v90, v62, 3, s42\n\t"
    "v_lshl_add_u32 v91, v63, 3, s43\n\t"
    "s_waitcnt lgkmcnt(15)\n\t"
    "buffer_store_dwordx4 v[236:239], v139, %12, s48 offen sc1\n\t"
    "buffer_store_dwordx4 v[240:243], v140, %12, s48 offen sc1\n\t"
    "s_add_u32 s48, s48, 819200\n\t"
    "buffer_store_dwordx4 v[244:247], v139, %12, s48 offen sc1\n\t"
    "buffer_store_dwordx4 v[248:251], v140, %12, s48 offen sc1\n\t"
    "s_add_u32 s48, s48, -811008\n\t"
    "buffer_load_dwordx4 v[48:51], v137, s[96:99], s47 offen offset:64\n\t"
    "buffer_load_dwordx4 v[52:55], v138, s[96:99], s47 offen offset:64\n\t"
    "s_add_u32 s47, s47, 0x100000\n\t"
    "buffer_load_dwordx4 v[204:207], v137, s[96:99], s47 offen offset:0\n\t"
    "buffer_load_dwordx4 v[208:211], v138, s[96:99], s47 offen offset:0\n\t"
    "buffer_load_dwordx4 v[212:215], v137, s[96:99], s47 offen offset:64\n\t"
    "buffer_load_dwordx4 v[216:219], v138, s[96:99], s47 offen offset:64\n\t"
    "s_add_u32 s47, s47, 0x100000\n\t"
    "buffer_load_dwordx4 v[220:223], v137, s[96:99], s47 offen offset:0\n\t"
    "buffer_load_dwordx4 v[224:227], v138, s[96:99], s47 offen offset:0\n\t"
    "buffer_load_dwordx4 v[228:231], v137, s[96:99], s47 offen offset:64\n\t"
    "buffer_load_dwordx4 v[232:235], v138, s[96:99], s47 offen offset:64\n\t"
    "s_add_u32 s47, s47, 0x100000\n\t"
    "s_waitcnt lgkmcnt(11)\n\t"
    "v_and_b32 v123, v123, v115\n\t"
    "v_perm_b32 v123, v123, v123, s46\n\t"
    "ds_or_b32 v97, v123\n\t"
    "v_and_b32 v124, v124, v116\n\t"
    "v_perm_b32 v124, v124, v124, s46\n\t"
    "ds_or_b32 v98, v124\n\t"
    "v_and_b32 v125, v125, v117\n\t"
    "v_perm_b32 v125, v125, v125, s46\n\t"
    "ds_or_b32 v99, v125\n\t"
    "s_waitcnt lgkmcnt(11)\n\t"
    "buffer_store_dwordx4 v[72:75], v67, %12, s49 offen offset:320 sc1\n\t"
    "v_mov_b32 v129, 0xffff0000\n\t"
    "s_waitcnt lgkmcnt(7)\n\t"
    "v_perm_b32 v100, v100, v76, v126\n\t"
    "v_perm_b32 v101, v101, v77, v126\n\t"
    "v_perm_b32 v102, v102, v78, v126\n\t"
    "v_perm_b32 v103, v103, v79, v126\n\t"
    "s_waitcnt lgkmcnt(3)\n\t"
    "v_perm_b32 v104, v104, v80, v126\n\t"
    "v_perm_b32 v105, v105, v81, v126\n\t"
    "v_perm_b32 v106, v106, v82, v126\n\t"
    "v_perm_b32 v107, v107, v83, v126\n\t"
    "ds_write_b128 v141, v[24:27] offset:0\n\t"
    "ds_write_b128 v141, v[28:31] offset:256\n\t"
    "ds_read_b64_tr_b16 v[24:25], v142 offset:0\n\t"
    "ds_read_b64_tr_b16 v[26:27], v142 offset:256\n\t"
    "ds_read_b64_tr_b16 v[28:29], v142 offset:32\n\t"
    "ds_read_b64_tr_b16 v[30:31], v142 offset:288\n\t"
    "ds_write_b128 v141, v[32:35] offset:2112\n\t"
    "ds_write_b128 v141, v[36:39] offset:2368\n\t"
    "ds_read_b64_tr_b16 v[32:33], v142 offset:2112\n\t"
    "ds_read_b64_tr_b16 v[34:35], v142 offset:2368\n\t"
    "ds_read_b64_tr_b16 v[36:37], v142 offset:2144\n\t"
    "ds_read_b64_tr_b16 v[38:39], v142 offset:2400\n\t"
    "v_max_u32 v108, v100, v101\n\t"
    "v_min_u32 v101, v100, v101\n\t"
    "v_max_u32 v100, v102, v103\n\t"
    "v_min_u32 v103, v102, v103\n\t"
    "v_max_u32 v102, v104, v105\n\t"
    "v_min_u32 v105, v104, v105\n\t"
    "v_max_u32 v104, v106, v107\n\t"
    "v_min_u32 v107, v106, v107\n\t"
    "v_max_u32 v106, v108, v100\n\t"
    "v_min_u32 v100, v108, v100\n\t"
    "v_max_u32 v108, v101, v103\n\t"
    "v_min_u32 v103, v101, v103\n\t"
    "v_max_u32 v101, v102, v104\n\t"
    "v_min_u32 v104, v102, v104\n\t"
    "v_max_u32 v102, v105, v107\n\t"
    "v_min_u32 v107, v105, v107\n\t"
    "v_bfi_b32 v130, v106, 0, v129\n\t"
    "v_cmp_eq_u32 vcc, 0, v130\n\t"
    "v_and_or_b32 v92, v106, s44, v69\n\t"
    "s_nop 0\n\t"
    "v_cndmask_b32 v130, v130, v129, vcc\n\t"
    "v_ffbl_b32 v131, v130\n\t"
    "v_and_b32 v134, 7, v131\n\t"
    "v_lshlrev_b32 v134, v134, s45\n\t"
    "v_xor_b32 v129, v129, v134\n\t"
    "v_xor_b32 v132, v131, v127\n\t"
    "v_bfi_b32 v130, v101, 0, v129\n\t"
    "v_cmp_eq_u32 vcc, 0, v130\n\t"
    "v_and_or_b32 v93, v101, s44, v69\n\t"
    "v_xor_b32 v133, 23, v132\n\t"
    "v_lshlrev_b32 v110, v132, v136\n\t"
    "v_and_b32 v132, 7, v132\n\t"
    "v_lshl_or_b32 v110, v136, v133, v110\n\t"
    "v_lshl_add_u32 v132, v132, 1, v128\n\t"
    "ds_or_rtn_b32 v118, v92, v110\n\t"
    "ds_write_b16 v132, v106\n\t"
    "v_cndmask_b32 v130, v130, v129, vcc\n\t"
    "v_ffbl_b32 v131, v130\n\t"
    "v_and_b32 v134, 7, v131\n\t"
    "v_lshlrev_b32 v134, v134, s45\n\t"
    "v_xor_b32 v129, v129, v134\n\t"
    "v_xor_b32 v132, v131, v127\n\t"
    "v_bfi_b32 v130, v108, 0, v129\n\t"
    "v_cmp_eq_u32 vcc, 0, v130\n\t"
    "v_and_or_b32 v94, v108, s44, v69\n\t"
    "v_xor_b32 v133, 23, v132\n\t"
    "v_lshlrev_b32 v111, v132, v136\n\t"
    "v_and_b32 v132, 7, v132\n\t"
    "v_lshl_or_b32 v111, v136, v133, v111\n\t"
    "v_lshl_add_u32 v132, v132, 1, v128\n\t"
    "ds_or_rtn_b32 v119, v93, v111\n\t"
    "ds_write_b16 v132, v101\n\t"
    "v_cndmask_b32 v130, v130, v129, vcc\n\t"
    "v_ffbl_b32 v131, v130\n\t"
    "v_and_b32 v134, 7, v131\n\t"
    "v_lshlrev_b32 v134, v134, s45\n\t"
    "v_xor_b32 v129, v129, v134\n\t"
    "v_xor_b32 v132, v131, v127\n\t"
    "s_waitcnt lgkmcnt(10)\n\t"
    "buffer_store_dwordx4 v[24:27], v139, %12, s48 offen sc1\n\t"
    "buffer_store_dwordx4 v[28:31], v140, %12, s48 offen sc1\n\t"
    "s_add_u32 s48, s48, 819200\n\t"
    "s_waitcnt lgkmcnt(4)\n\t"
    "buffer_store_dwordx4 v[32:35], v139, %12, s48 offen sc1\n\t"
    "buffer_store_dwordx4 v[36:39], v140, %12, s48 offen sc1\n\t"
    "s_add_u32 s48, s48, -811008\n\t"
    "ds_write_b128 v141, v[40:43] offset:0\n\t"
    "ds_write_b128 v141, v[44:47] offset:256\n\t"
    "ds_read_b64_tr_b16 v[40:41], v142 offset:0\n\t"
    "ds_read_b64_tr_b16 v[42:43], v142 offset:256\n\t"
    "ds_read_b64_tr_b16 v[44:45], v142 offset:32\n\t"
    "ds_read_b64_tr_b16 v[46:47], v142 offset:288\n\t"
    "v_bfi_b32 v130, v100, 0, v129\n\t"
    "v_cmp_eq_u32 vcc, 0, v130\n\t"
    "v_and_or_b32 v95, v100, s44, v69\n\t"
    "v_xor_b32 v133, 23, v132\n\t"
    "v_lshlrev_b32 v112, v132, v136\n\t"
    "v_and_b32 v132, 7, v132\n\t"
    "v_lshl_or_b32 v112, v136, v133, v112\n\t"
    "v_lshl_add_u32 v132, v132, 1, v128\n\t"
    "ds_or_rtn_b32 v120, v94, v112\n\t"
    "ds_write_b16 v132, v108\n\t"
    "v_cndmask_b32 v130, v130, v129, vcc\n\t"
    "v_ffbl_b32 v131, v130\n\t"
    "v_and_b32 v134, 7, v131\n\t"
    "v_lshlrev_b32 v134, v134, s45\n\t"
    "v_xor_b32 v129, v129, v134\n\t"
    "v_xor_b32 v132, v131, v127\n\t"
    "v_bfi_b32 v130, v102, 0, v129\n\t"
    "v_cmp_eq_u32 vcc, 0, v130\n\t"
    "v_and_or_b32 v96, v102, s44, v69\n\t"
    "v_xor_b32 v133, 23, v132\n\t"
    "v_lshlrev_b32 v113, v132, v136\n\t"
    "v_and_b32 v132, 7, v132\n\t"
    "v_lshl_or_b32 v113, v136, v133, v113\n\t"
    "v_lshl_add_u32 v132, v132, 1, v128\n\t"
    "ds_or_rtn_b32 v121, v95, v113\n\t"
    "ds_write_b16 v132, v100\n\t"
    "v_cndmask_b32 v130, v130, v129, vcc\n\t"
    "v_ffbl_b32 v131, v130\n\t"
    "v_and_b32 v134, 7, v131\n\t"
    "v_lshlrev_b32 v134, v134, s45\n\t"
    "v_xor_b32 v129, v129, v134\n\t"
    "v_xor_b32 v132, v131, v127\n\t"
    "s_waitcnt lgkmcnt(13)\n\t"
    "v_and_b32 v118, v118, v110\n\t"
    "v_perm_b32 v118, v118, v118, s46\n\t"
    "ds_or_b32 v92, v118\n\t"
    "v_bfi_b32 v130, v104, 0, v129\n\t"
    "v_cmp_eq_u32 vcc, 0, v130\n\t"
    "v_and_or_b32 v97, v104, s44, v69\n\t"
    "v_xor_b32 v133, 23, v132\n\t"
    "v_lshlrev_b32 v114, v132, v136\n\t"
    "v_and_b32 v132, 7, v132\n\t"
    "v_lshl_or_b32 v114, v136, v133, v114\n\t"
    "v_lshl_add_u32 v132, v132, 1, v128\n\t"
    "ds_or_rtn_b32 v122, v96, v114\n\t"
    "ds_write_b16 v132, v102\n\t"
    "v_cndmask_b32 v130, v130, v129, vcc\n\t"
    "v_ffbl_b32 v131, v130\n\t"
    "v_and_b32 v134, 7, v131\n\t"
    "v_lshlrev_b32 v134, v134, s45\n\t"
    "v_xor_b32 v129, v129, v134\n\t"
    "v_xor_b32 v132, v131, v127\n\t"
    "s_waitcnt lgkmcnt(14)\n\t"
    "v_and_b32 v119, v119, v111\n\t"
    "v_perm_b32 v119, v119, v119, s46\n\t"
    "ds_or_b32 v93, v119\n\t"
    "s_waitcnt lgkmcnt(8)\n\t"
    "buffer_store_dwordx4 v[40:43], v139, %12, s48 offen sc1\n\t"
    "buffer_store_dwordx4 v[44:47], v140, %12, s48 offen sc1\n\t"
    "s_add_u32 s48, s48, 819200\n\t"
    "v_bfi_b32 v130, v103, 0, v129\n\t"
    "v_cmp_eq_u32 vcc, 0, v130\n\t"
    "v_and_or_b32 v98, v103, s44, v69\n\t"
    "v_xor_b32 v133, 23, v132\n\t"
    "v_lshlrev_b32 v115, v132, v136\n\t"
    "v_and_b32 v132, 7, v132\n\t"
    "v_lshl_or_b32 v115, v136, v133, v115\n\t"
    "v_lshl_add_u32 v132, v132, 1, v128\n\t"
    "ds_or_rtn_b32 v123, v97, v115\n\t"
    "ds_write_b16 v132, v104\n\t"
    "v_cndmask_b32 v130, v130, v129, vcc\n\t"
    "v_ffbl_b32 v131, v130\n\t"
    "v_and_b32 v134, 7, v131\n\t"
    "v_lshlrev_b32 v134, v134, s45\n\t"
    "v_xor_b32 v129, v129, v134\n\t"
    "v_xor_b32 v132, v131, v127\n\t"
    "s_waitcnt lgkmcnt(9)\n\t"
    "v_and_b32 v120, v120, v112\n\t"
    "v_perm_b32 v120, v120, v120, s46\n\t"
    "ds_or_b32 v94, v120\n\t"
    "v_bfi_b32 v130, v107, 0, v129\n\t"
    "v_cmp_eq_u32 vcc, 0, v130\n\t"
    "v_and_or_b32 v99, v107, s44, v69\n\t"
    "v_xor_b32 v133, 23, v132\n\t"
    "v_lshlrev_b32 v116, v132, v136\n\t"
    "v_and_b32 v132, 7, v132\n\t"
    "v_lshl_or_b32 v116, v136, v133, v116\n\t"
    "v_lshl_add_u32 v132, v132, 1, v128\n\t"
    "ds_or_rtn_b32 v124, v98, v116\n\t"
    "ds_write_b16 v132, v103\n\t"
    "v_cndmask_b32 v130, v130, v129, vcc\n\t"
    "v_ffbl_b32 v131, v130\n\t"
    "v_xor_b32 v132, v131, v127\n\t"
    "s_waitcnt lgkmcnt(10)\n\t"
    "v_and_b32 v121, v121, v113\n\t"
    "v_perm_b32 v121, v121, v121, s46\n\t"
    "ds_or_b32 v95, v121\n\t"
    "v_xor_b32 v133, 23, v132\n\t"
    "v_lshlrev_b32 v117, v132, v136\n\t"
    "v_and_b32 v132, 7, v132\n\t"
    "v_lshl_or_b32 v117, v136, v133, v117\n\t"
    "v_lshl_add_u32 v132, v132, 1, v128\n\t"
    "ds_or_rtn_b32 v125, v99, v117\n\t"
    "ds_write_b16 v132, v107\n\t"
    "s_waitcnt lgkmcnt(10)\n\t"
    "v_and_b32 v122, v122, v114\n\t"
    "v_perm_b32 v122, v122, v122, s46\n\t"
    "ds_or_b32 v96, v122\n\t"
    "ds_read_b128 v[72:75], v128\n\t"
    "v_and_or_b32 v100, v84, s44, v69\n\t"
    "ds_read_b32 v100, v100\n\t"
    "v_and_or_b32 v101, v85, s44, v69\n\t"
    "ds_read_b32 v101, v101\n\t"
    "v_and_or_b32 v102, v86, s44, v69\n\t"
    "ds_read_b32 v102, v102\n\t"
    "v_and_or_b32 v103, v87, s44, v69\n\t"
    "ds_read_b32 v103, v103\n\t"
    "v_and_or_b32 v104, v88, s44, v69\n\t"
    "ds_read_b32 v104, v104\n\t"
    "v_and_or_b32 v105, v89, s44, v69\n\t"
    "ds_read_b32 v105, v105\n\t"
    "v_and_or_b32 v106, v90, s44, v69\n\t"
    "ds_read_b32 v106, v106\n\t"
    "v_and_or_b32 v107, v91, s44, v69\n\t"
    "ds_read_b32 v107, v107\n\t"
    "v_lshl_add_u32 v76, v0, 3, s36\n\t"
    "v_lshl_add_u32 v77, v1, 3, s37\n\t"
    "v_lshl_add_u32 v78, v2, 3, s38\n\t"
    "v_lshl_add_u32 v79, v3, 3, s39\n\t"
    "v_lshl_add_u32 v80, v4, 3, s40\n\t"
    "v_lshl_add_u32 v81, v5, 3, s41\n\t"
    "v_lshl_add_u32 v82, v6, 3, s42\n\t"
    "v_lshl_add_u32 v83, v7, 3, s43\n\t"
    "v_mbcnt_lo_u32_b32 v70, -1, 0\n\t"
    "v_mbcnt_hi_u32_b32 v70, -1, v70\n\t"
    "v_add_u32 v70, 8, v70\n\t"
    "s_and_b32 s50, %14, 7\n\t"
    "s_add_u32 s50, s50, 16\n\t"
    "v_writelane_b32 v70, s50, 8\n\t"
    "v_mov_b32 v148, 32\n\t"
    "v_lshlrev_b32 v70, 7, v70\n\t"
    "s_mov_b32 s52, 49283072\n\t"
    "s_mov_b64 exec, 0x1ff\n\t"
    "buffer_load_dword v71, v70, %12, s52 offen sc1\n\t"
    "s_mov_b64 exec, -1\n\t"
    "s_mov_b32 s54, 0x40000000\n\t"
    "s_mov_b32 s55, 0\n\t"
    "s_waitcnt lgkmcnt(11)\n\t"
    "v_and_b32 v123, v123, v115\n\t"
    "v_perm_b32 v123, v123, v123, s46\n\t"
    "ds_or_b32 v97, v123\n\t"
    "v_and_b32 v124, v124, v116\n\t"
    "v_perm_b32 v124, v124, v124, s46\n\t"
    "ds_or_b32 v98, v124\n\t"
    "v_and_b32 v125, v125, v117\n\t"
    "v_perm_b32 v125, v125, v125, s46\n\t"
    "ds_or_b32 v99, v125\n\t"
    "s_waitcnt lgkmcnt(11)\n\t"
    "buffer_store_dwordx4 v[72:75], v67, %12, s49 offen offset:352 sc1\n\t"
    "v_mov_b32 v129, 0xffff0000\n\t"
    "s_waitcnt lgkmcnt(7)\n\t"
    "v_perm_b32 v100, v100, v84, v126\n\t"
    "v_perm_b32 v101, v101, v85, v126\n\t"
    "v_perm_b32 v102, v102, v86, v126\n\t"
    "v_perm_b32 v103, v103, v87, v126\n\t"
    "s_waitcnt lgkmcnt(3)\n\t"
    "v_perm_b32 v104, v104, v88, v126\n\t"
    "v_perm_b32 v105, v105, v89, v126\n\t"
    "v_perm_b32 v106, v106, v90, v126\n\t"
    "v_perm_b32 v107, v107, v91, v126\n\t"
    "s_waitcnt vmcnt(42)\n\t"
    "ds_write_b128 v141, v[156:159] offset:2112\n\t"
    "ds_write_b128 v141, v[160:163] offset:2368\n\t"
    "ds_read_b64_tr_b16 v[156:157], v142 offset:2112\n\t"
    "ds_read_b64_tr_b16 v[158:159], v142 offset:2368\n\t"
    "ds_read_b64_tr_b16 v[160:161], v142 offset:2144\n\t"
    "ds_read_b64_tr_b16 v[162:163], v142 offset:2400\n\t"
    "s_waitcnt vmcnt(40)\n\t"
    "ds_write_b128 v141, v[164:167] offset:0\n\t"
    "ds_write_b128 v141, v[168:171] offset:256\n\t"
    "ds_read_b64_tr_b16 v[164:165], v142 offset:0\n\t"
    "ds_read_b64_tr_b16 v[166:167], v142 offset:256\n\t"
    "ds_read_b64_tr_b16 v[168:169], v142 offset:32\n\t"
    "ds_read_b64_tr_b16 v[170:171], v142 offset:288\n\t"
    "v_max_u32 v108, v100, v101\n\t"
    "v_min_u32 v101, v100, v101\n\t"
    "v_max_u32 v100, v102, v103\n\t"
    "v_min_u32 v103, v102, v103\n\t"
    "v_max_u32 v102, v104, v105\n\t"
    "v_min_u32 v105, v104, v105\n\t"
    "v_max_u32 v104, v106, v107\n\t"
    "v_min_u32 v107, v106, v107\n\t"
    "v_max_u32 v106, v108, v100\n\t"
    "v_min_u32 v100, v108, v100\n\t"
    "v_max_u32 v108, v101, v103\n\t"
    "v_min_u32 v103, v101, v103\n\t"
    "v_max_u32 v101, v102, v104\n\t"
    "v_min_u32 v104, v102, v104\n\t"
    "v_max_u32 v102, v105, v107\n\t"
    "v_min_u32 v107, v105, v107\n\t"
    "v_bfi_b32 v130, v106, 0, v129\n\t"
    "v_cmp_eq_u32 vcc, 0, v130\n\t"
    "v_and_or_b32 v92, v106, s44, v69\n\t"
    "s_nop 0\n\t"
    "v_cndmask_b32 v130, v130, v129, vcc\n\t"
    "v_ffbl_b32 v131, v130\n\t"
    "v_and_b32 v134, 7, v131\n\t"
    "v_lshlrev_b32 v134, v134, s45\n\t"
    "v_xor_b32 v129, v129, v134\n\t"
    "v_xor_b32 v132, v131, v127\n\t"
    "v_bfi_b32 v130, v101, 0, v129\n\t"
    "v_cmp_eq_u32 vcc, 0, v130\n\t"
    "v_and_or_b32 v93, v101, s44, v69\n\t"
    "v_xor_b32 v133, 23, v132\n\t"
    "v_lshlrev_b32 v110, v132, v136\n\t"
    "v_and_b32 v132, 7, v132\n\t"
    "v_lshl_or_b32 v110, v136, v133, v110\n\t"
    "v_lshl_add_u32 v132, v132, 1, v128\n\t"
    "ds_or_b32 v92, v110\n\t"
    "ds_write_b16 v132, v106\n\t"
    "v_cndmask_b32 v130, v130, v129, vcc\n\t"
    "v_ffbl_b32 v131, v130\n\t"
    "v_and_b32 v134, 7, v131\n\t"
    "v_lshlrev_b32 v134, v134, s45\n\t"
    "v_xor_b32 v129, v129, v134\n\t"
    "v_xor_b32 v132, v131, v127\n\t"
    "v_bfi_b32 v130, v108, 0, v129\n\t"
    "v_cmp_eq_u32 vcc, 0, v130\n\t"
    "v_and_or_b32 v94, v108, s44, v69\n\t"
    "v_xor_b32 v133, 23, v132\n\t"
    "v_lshlrev_b32 v111, v132, v136\n\t"
    "v_and_b32 v132, 7, v132\n\t"
    "v_lshl_or_b32 v111, v136, v133, v111\n\t"
    "v_lshl_add_u32 v132, v132, 1, v128\n\t"
    "ds_or_b32 v93, v111\n\t"
    "ds_write_b16 v132, v101\n\t"
    "v_cndmask_b32 v130, v130, v129, vcc\n\t"
    "v_ffbl_b32 v131, v130\n\t"
    "v_and_b32 v134, 7, v131\n\t"
    "v_lshlrev_b32 v134, v134, s45\n\t"
    "v_xor_b32 v129, v129, v134\n\t"
    "v_xor_b32 v132, v131, v127\n\t"
    "s_waitcnt lgkmcnt(10)\n\t"
    "buffer_store_dwordx4 v[156:159], v139, %12, s48 offen sc1\n\t"
    "buffer_store_dwordx4 v[160:163], v140, %12, s48 offen sc1\n\t"
    "s_add_u32 s48, s48, -811008\n\t"
    "s_waitcnt lgkmcnt(4)\n\t"
    "buffer_store_dwordx4 v[164:167], v139, %12, s48 offen sc1\n\t"
    "buffer_store_dwordx4 v[168:171], v140, %12, s48 offen sc1\n\t"
    "s_add_u32 s48, s48, 819200\n\t"
    "s_waitcnt vmcnt(42)\n\t"
    "ds_write_b128 v141, v[172:175] offset:2112\n\t"
    "ds_write_b128 v141, v[176:179] offset:2368\n\t"
    "ds_read_b64_tr_b16 v[172:173], v142 offset:2112\n\t"
    "ds_read_b64_tr_b16 v[174:175], v142 offset:2368\n\t"
    "ds_read_b64_tr_b16 v[176:177], v142 offset:2144\n\t"
    "ds_read_b64_tr_b16 v[178:179], v142 offset:2400\n\t"
    "s_waitcnt vmcnt(40)\n\t"
    "ds_write_b128 v141, v[180:183] offset:0\n\t"
    "ds_write_b128 v141, v[184:187] offset:256\n\t"
    "ds_read_b64_tr_b16 v[180:181], v142 offset:0\n\t"
    "ds_read_b64_tr_b16 v[182:183], v142 offset:256\n\t"
    "ds_read_b64_tr_b16 v[184:185], v142 offset:32\n\t"
    "ds_read_b64_tr_b16 v[186:187], v142 offset:288\n\t"
    "v_bfi_b32 v130, v100, 0, v129\n\t"
    "v_cmp_eq_u32 vcc, 0, v130\n\t"
    "v_and_or_b32 v95, v100, s44, v69\n\t"
    "v_xor_b32 v133, 23, v132\n\t"
    "v_lshlrev_b32 v112, v132, v136\n\t"
    "v_and_b32 v132, 7, v132\n\t"
    "v_lshl_or_b32 v112, v136, v133, v112\n\t"
    "v_lshl_add_u32 v132, v132, 1, v128\n\t"
    "ds_or_b32 v94, v112\n\t"
    "ds_write_b16 v132, v108\n\t"
    "v_cndmask_b32 v130, v130, v129, vcc\n\t"
    "v_ffbl_b32 v131, v130\n\t"
    "v_and_b32 v134, 7, v131\n\t"
    "v_lshlrev_b32 v134, v134, s45\n\t"
    "v_xor_b32 v129, v129, v134\n\t"
    "v_xor_b32 v132, v131, v127\n\t"
    "v_bfi_b32 v130, v102, 0, v129\n\t"
    "v_cmp_eq_u32 vcc, 0, v130\n\t"
    "v_and_or_b32 v96, v102, s44, v69\n\t"
    "v_xor_b32 v133, 23, v132\n\t"
    "v_lshlrev_b32 v113, v132, v136\n\t"
    "v_and_b32 v132, 7, v132\n\t"
    "v_lshl_or_b32 v113, v136, v133, v113\n\t"
    "v_lshl_add_u32 v132, v132, 1, v128\n\t"
    "ds_or_b32 v95, v113\n\t"
    "ds_write_b16 v132, v100\n\t"
    "v_cndmask_b32 v130, v130, v129, vcc\n\t"
    "v_ffbl_b32 v131, v130\n\t"
    "v_and_b32 v134, 7, v131\n\t"
    "v_lshlrev_b32 v134, v134, s45\n\t"
    "v_xor_b32 v129, v129, v134\n\t"
    "v_xor_b32 v132, v131, v127\n\t"
    "s_waitcnt vmcnt(5)\n\t"
    "s_call_b64 s[54:55], L_af1w_%=\n\t"
    "s_mov_b64 s[54:55], 0\n\t"
    "L_af1n15_%=:\n\t"
    "v_bfi_b32 v130, v104, 0, v129\n\t"
    "v_cmp_eq_u32 vcc, 0, v130\n\t"
    "v_and_or_b32 v97, v104, s44, v69\n\t"
    "v_xor_b32 v133, 23, v132\n\t"
    "v_lshlrev_b32 v114, v132, v136\n\t"
    "v_and_b32 v132, 7, v132\n\t"
    "v_lshl_or_b32 v114, v136, v133, v114\n\t"
    "v_lshl_add_u32 v132, v132, 1, v128\n\t"
    "ds_or_b32 v96, v114\n\t"
    "ds_write_b16 v132, v102\n\t"
    "v_cndmask_b32 v130, v130, v129, vcc\n\t"
    "v_ffbl_b32 v131, v130\n\t"
    "v_and_b32 v134, 7, v131\n\t"
    "v_lshlrev_b32 v134, v134, s45\n\t"
    "v_xor_b32 v129, v129, v134\n\t"
    "v_xor_b32 v132, v131, v127\n\t"
    "s_waitcnt lgkmcnt(12)\n\t"
    "buffer_store_dwordx4 v[172:175], v139, %12, s48 offen sc1\n\t"
    "buffer_store_dwordx4 v[176:179], v140, %12, s48 offen sc1\n\t"
    "s_add_u32 s48, s48, -811008\n\t"
    "s_waitcnt lgkmcnt(6)\n\t"
    "buffer_store_dwordx4 v[180:183], v139, %12, s48 offen sc1\n\t"
    "buffer_store_dwordx4 v[184:187], v140, %12, s48 offen sc1\n\t"
    "s_add_u32 s48, s48, 819200\n\t"
    "ds_write_b128 v141, v[188:191] offset:2112\n\t"
    "ds_write_b128 v141, v[192:195] offset:2368\n\t"
    "ds_read_b64_tr_b16 v[188:189], v142 offset:2112\n\t"
    "ds_read_b64_tr_b16 v[190:191], v142 offset:2368\n\t"
    "ds_read_b64_tr_b16 v[192:193], v142 offset:2144\n\t"
    "ds_read_b64_tr_b16 v[194:195], v142 offset:2400\n\t"
    "ds_write_b128 v141, v[196:199] offset:0\n\t"
    "ds_write_b128 v141, v[200:203] offset:256\n\t"
    "ds_read_b64_tr_b16 v[196:197], v142 offset:0\n\t"
    "ds_read_b64_tr_b16 v[198:199], v142 offset:256\n\t"
    "ds_read_b64_tr_b16 v[200:201], v142 offset:32\n\t"
    "ds_read_b64_tr_b16 v[202:203], v142 offset:288\n\t"
    "v_bfi_b32 v130, v103, 0, v129\n\t"
    "v_cmp_eq_u32 vcc, 0, v130\n\t"
    "v_and_or_b32 v98, v103, s44, v69\n\t"
    "v_xor_b32 v133, 23, v132\n\t"
    "v_lshlrev_b32 v115, v132, v136\n\t"
    "v_and_b32 v132, 7, v132\n\t"
    "v_lshl_or_b32 v115, v136, v133, v115\n\t"
    "v_lshl_add_u32 v132, v132, 1, v128\n\t"
    "ds_or_b32 v97, v115\n\t"
    "ds_write_b16 v132, v104\n\t"
    "v_cndmask_b32 v130, v130, v129, vcc\n\t"
    "v_ffbl_b32 v131, v130\n\t"
    "v_and_b32 v134, 7, v131\n\t"
    "v_lshlrev_b32 v134, v134, s45\n\t"
    "v_xor_b32 v129, v129, v134\n\t"
    "v_xor_b32 v132, v131, v127\n\t"
    "v_bfi_b32 v130, v107, 0, v129\n\t"
    "v_cmp_eq_u32 vcc, 0, v130\n\t"
    "v_and_or_b32 v99, v107, s44, v69\n\t"
    "v_xor_b32 v133, 23, v132\n\t"
    "v_lshlrev_b32 v116, v132, v136\n\t"
    "v_and_b32 v132, 7, v132\n\t"
    "v_lshl_or_b32 v116, v136, v133, v116\n\t"
    "v_lshl_add_u32 v132, v132, 1, v128\n\t"
    "ds_or_b32 v98, v116\n\t"
    "ds_write_b16 v132, v103\n\t"
    "v_cndmask_b32 v130, v130, v129, vcc\n\t"
    "v_ffbl_b32 v131, v130\n\t"
    "v_xor_b32 v132, v131, v127\n\t"
    "v_xor_b32 v133, 23, v132\n\t"
    "v_lshlrev_b32 v117, v132, v136\n\t"
    "v_and_b32 v132, 7, v132\n\t"
    "v_lshl_or_b32 v117, v136, v133, v117\n\t"
    "v_lshl_add_u32 v132, v132, 1, v128\n\t"
    "ds_or_b32 v99, v117\n\t"
    "ds_write_b16 v132, v107\n\t"
    "ds_read_b128 v[72:75], v128\n\t"
    "v_and_or_b32 v100, v76, s44, v69\n\t"
    "ds_read_b32 v100, v100\n\t"
    "v_and_or_b32 v101, v77, s44, v69\n\t"
    "ds_read_b32 v101, v101\n\t"
    "v_and_or_b32 v102, v78, s44, v69\n\t"
    "ds_read_b32 v102, v102\n\t"
    "v_and_or_b32 v103, v79, s44, v69\n\t"
    "ds_read_b32 v103, v103\n\t"
    "v_and_or_b32 v104, v80, s44, v69\n\t"
    "ds_read_b32 v104, v104\n\t"
    "v_and_or_b32 v105, v81, s44, v69\n\t"
    "ds_read_b32 v105, v105\n\t"
    "v_and_or_b32 v106, v82, s44, v69\n\t"
    "ds_read_b32 v106, v106\n\t"
    "v_and_or_b32 v107, v83, s44, v69\n\t"
    "ds_read_b32 v107, v107\n\t"
    "v_lshl_add_u32 v84, v8, 3, s36\n\t"
    "v_lshl_add_u32 v85, v9, 3, s37\n\t"
    "v_lshl_add_u32 v86, v10, 3, s38\n\t"
    "v_lshl_add_u32 v87, v11, 3, s39\n\t"
    "v_lshl_add_u32 v88, v12, 3, s40\n\t"
    "v_lshl_add_u32 v89, v13, 3, s41\n\t"
    "v_lshl_add_u32 v90, v14, 3, s42\n\t"
    "v_lshl_add_u32 v91, v15, 3, s43\n\t"
    "s_waitcnt lgkmcnt(15)\n\t"
    "buffer_store_dwordx4 v[188:191], v139, %12, s48 offen sc1\n\t"
    "buffer_store_dwordx4 v[192:195], v140, %12, s48 offen sc1\n\t"
    "s_add_u32 s48, s48, -811008\n\t"
    "buffer_store_dwordx4 v[196:199], v139, %12, s48 offen sc1\n\t"
    "buffer_store_dwordx4 v[200:203], v140, %12, s48 offen sc1\n\t"
    "s_add_u32 s48, s48, 819200\n\t"
    "s_waitcnt lgkmcnt(8)\n\t"
    "buffer_store_dwordx4 v[72:75], v67, %12, s49 offen offset:384 sc1\n\t"
    "v_mov_b32 v129, 0xffff0000\n\t"
    "s_waitcnt lgkmcnt(4)\n\t"
    "v_perm_b32 v100, v100, v76, v126\n\t"
    "v_perm_b32 v101, v101, v77, v126\n\t"
    "v_perm_b32 v102, v102, v78, v126\n\t"
    "v_perm_b32 v103, v103, v79, v126\n\t"
    "s_waitcnt lgkmcnt(0)\n\t"
    "v_perm_b32 v104, v104, v80, v126\n\t"
    "v_perm_b32 v105, v105, v81, v126\n\t"
    "v_perm_b32 v106, v106, v82, v126\n\t"
    "v_perm_b32 v107, v107, v83, v126\n\t"
    "ds_write_b128 v141, v[48:51] offset:2112\n\t"
    "ds_write_b128 v141, v[52:55] offset:2368\n\t"
    "ds_read_b64_tr_b16 v[48:49], v142 offset:2112\n\t"
    "ds_read_b64_tr_b16 v[50:51], v142 offset:2368\n\t"
    "ds_read_b64_tr_b16 v[52:53], v142 offset:2144\n\t"
    "ds_read_b64_tr_b16 v[54:55], v142 offset:2400\n\t"
    "ds_write_b128 v141, v[204:207] offset:0\n\t"
    "ds_write_b128 v141, v[208:211] offset:256\n\t"
    "ds_read_b64_tr_b16 v[204:205], v142 offset:0\n\t"
    "ds_read_b64_tr_b16 v[206:207], v142 offset:256\n\t"
    "ds_read_b64_tr_b16 v[208:209], v142 offset:32\n\t"
    "ds_read_b64_tr_b16 v[210:211], v142 offset:288\n\t"
    "v_max_u32 v108, v100, v101\n\t"
    "v_min_u32 v101, v100, v101\n\t"
    "v_max_u32 v100, v102, v103\n\t"
    "v_min_u32 v103, v102, v103\n\t"
    "v_max_u32 v102, v104, v105\n\t"
    "v_min_u32 v105, v104, v105\n\t"
    "v_max_u32 v104, v106, v107\n\t"
    "v_min_u32 v107, v106, v107\n\t"
    "v_max_u32 v106, v108, v100\n\t"
    "v_min_u32 v100, v108, v100\n\t"
    "v_max_u32 v108, v101, v103\n\t"
    "v_min_u32 v103, v101, v103\n\t"
    "v_max_u32 v101, v102, v104\n\t"
    "v_min_u32 v104, v102, v104\n\t"
    "v_max_u32 v102, v105, v107\n\t"
    "v_min_u32 v107, v105, v107\n\t"
    "v_bfi_b32 v130, v106, 0, v129\n\t"
    "v_cmp_eq_u32 vcc, 0, v130\n\t"
    "v_and_or_b32 v92, v106, s44, v69\n\t"
    "s_nop 0\n\t"
    "v_cndmask_b32 v130, v130, v129, vcc\n\t"
    "v_ffbl_b32 v131, v130\n\t"
    "v_and_b32 v134, 7, v131\n\t"
    "v_lshlrev_b32 v134, v134, s45\n\t"
    "v_xor_b32 v129, v129, v134\n\t"
    "v_xor_b32 v132, v131, v127\n\t"
    "v_bfi_b32 v130, v101, 0, v129\n\t"
    "v_cmp_eq_u32 vcc, 0, v130\n\t"
    "v_and_or_b32 v93, v101, s44, v69\n\t"
    "v_xor_b32 v133, 23, v132\n\t"
    "v_lshlrev_b32 v110, v132, v136\n\t"
    "v_and_b32 v132, 7, v132\n\t"
    "v_lshl_or_b32 v110, v136, v133, v110\n\t"
    "v_lshl_add_u32 v132, v132, 1, v128\n\t"
    "ds_or_b32 v92, v110\n\t"
    "ds_write_b16 v132, v106\n\t"
    "v_cndmask_b32 v130, v130, v129, vcc\n\t"
    "v_ffbl_b32 v131, v130\n\t"
    "v_and_b32 v134, 7, v131\n\t"
    "v_lshlrev_b32 v134, v134, s45\n\t"
    "v_xor_b32 v129, v129, v134\n\t"
    "v_xor_b32 v132, v131, v127\n\t"
    "v_bfi_b32 v130, v108, 0, v129\n\t"
    "v_cmp_eq_u32 vcc, 0, v130\n\t"
    "v_and_or_b32 v94, v108, s44, v69\n\t"
    "v_xor_b32 v133, 23, v132\n\t"
    "v_lshlrev_b32 v111, v132, v136\n\t"
    "v_and_b32 v132, 7, v132\n\t"
    "v_lshl_or_b32 v111, v136, v133, v111\n\t"
    "v_lshl_add_u32 v132, v132, 1, v128\n\t"
    "ds_or_b32 v93, v111\n\t"
    "ds_write_b16 v132, v101\n\t"
    "v_cndmask_b32 v130, v130, v129, vcc\n\t"
    "v_ffbl_b32 v131, v130\n\t"
    "v_and_b32 v134, 7, v131\n\t"
    "v_lshlrev_b32 v134, v134, s45\n\t"
    "v_xor_b32 v129, v129, v134\n\t"
    "v_xor_b32 v132, v131, v127\n\t"
    "s_waitcnt lgkmcnt(10)\n\t"
    "buffer_store_dwordx4 v[48:51], v139, %12, s48 offen sc1\n\t"
    "buffer_store_dwordx4 v[52:55], v140, %12, s48 offen sc1\n\t"
    "s_add_u32 s48, s48, -811008\n\t"
    "s_waitcnt lgkmcnt(4)\n\t"
    "buffer_store_dwordx4 v[204:207], v139, %12, s48 offen sc1\n\t"
    "buffer_store_dwordx4 v[208:211], v140, %12, s48 offen sc1\n\t"
    "s_add_u32 s48, s48, 819200\n\t"
    "ds_write_b128 v141, v[212:215] offset:2112\n\t"
    "ds_write_b128 v141, v[216:219] offset:2368\n\t"
    "ds_read_b64_tr_b16 v[212:213], v142 offset:2112\n\t"
    "ds_read_b64_tr_b16 v[214:215], v142 offset:2368\n\t"
    "ds_read_b64_tr_b16 v[216:217], v142 offset:2144\n\t"
    "ds_read_b64_tr_b16 v[218:219], v142 offset:2400\n\t"
    "ds_write_b128 v141, v[220:223] offset:0\n\t"
    "ds_write_b128 v141, v[224:227] offset:256\n\t"
    "ds_read_b64_tr_b16 v[220:221], v142 offset:0\n\t"
    "ds_read_b64_tr_b16 v[222:223], v142 offset:256\n\t"
    "ds_read_b64_tr_b16 v[224:225], v142 offset:32\n\t"
    "ds_read_b64_tr_b16 v[226:227], v142 offset:288\n\t"
    "v_bfi_b32 v130, v100, 0, v129\n\t"
    "v_cmp_eq_u32 vcc, 0, v130\n\t"
    "v_and_or_b32 v95, v100, s44, v69\n\t"
    "v_xor_b32 v133, 23, v132\n\t"
    "v_lshlrev_b32 v112, v132, v136\n\t"
    "v_and_b32 v132, 7, v132\n\t"
    "v_lshl_or_b32 v112, v136, v133, v112\n\t"
    "v_lshl_add_u32 v132, v132, 1, v128\n\t"
    "ds_or_b32 v94, v112\n\t"
    "ds_write_b16 v132, v108\n\t"
    "v_cndmask_b32 v130, v130, v129, vcc\n\t"
    "v_ffbl_b32 v131, v130\n\t"
    "v_and_b32 v134, 7, v131\n\t"
    "v_lshlrev_b32 v134, v134, s45\n\t"
    "v_xor_b32 v129, v129, v134\n\t"
    "v_xor_b32 v132, v131, v127\n\t"
    "v_bfi_b32 v130, v102, 0, v129\n\t"
    "v_cmp_eq_u32 vcc, 0, v130\n\t"
    "v_and_or_b32 v96, v102, s44, v69\n\t"
    "v_xor_b32 v133, 23, v132\n\t"
    "v_lshlrev_b32 v113, v132, v136\n\t"
    "v_and_b32 v132, 7, v132\n\t"
    "v_lshl_or_b32 v113, v136, v133, v113\n\t"
    "v_lshl_add_u32 v132, v132, 1, v128\n\t"
    "ds_or_b32 v95, v113\n\t"
    "ds_write_b16 v132, v100\n\t"
    "v_cndmask_b32 v130, v130, v129, vcc\n\t"
    "v_ffbl_b32 v131, v130\n\t"
    "v_and_b32 v134, 7, v131\n\t"
    "v_lshlrev_b32 v134, v134, s45\n\t"
    "v_xor_b32 v129, v129, v134\n\t"
    "v_xor_b32 v132, v131, v127\n\t"
    "v_bfi_b32 v130, v104, 0, v129\n\t"
    "v_cmp_eq_u32 vcc, 0, v130\n\t"
    "v_and_or_b32 v97, v104, s44, v69\n\t"
    "v_xor_b32 v133, 23, v132\n\t"
    "v_lshlrev_b32 v114, v132, v136\n\t"
    "v_and_b32 v132, 7, v132\n\t"
    "v_lshl_or_b32 v114, v136, v133, v114\n\t"
    "v_lshl_add_u32 v132, v132, 1, v128\n\t"
    "ds_or_b32 v96, v114\n\t"
    "ds_write_b16 v132, v102\n\t"
    "v_cndmask_b32 v130, v130, v129, vcc\n\t"
    "v_ffbl_b32 v131, v130\n\t"
    "v_and_b32 v134, 7, v131\n\t"
    "v_lshlrev_b32 v134, v134, s45\n\t"
    "v_xor_b32 v129, v129, v134\n\t"
    "v_xor_b32 v132, v131, v127\n\t"
    "s_waitcnt lgkmcnt(12)\n\t"
    "buffer_store_dwordx4 v[212:215], v139, %12, s48 offen sc1\n\t"
    "buffer_store_dwordx4 v[216:219], v140, %12, s48 offen sc1\n\t"
    "s_add_u32 s48, s48, -811008\n\t"
    "s_waitcnt lgkmcnt(6)\n\t"
    "s_bitcmp1_b32 %14, 3\n\t"
    "s_cselect_b32 s48, 0x40000000, s48\n\t"
    "buffer_store_dwordx4 v[220:223], v139, %12, s48 offen sc1\n\t"
    "buffer_store_dwordx4 v[224:227], v140, %12, s48 offen sc1\n\t"
    "s_add_u32 s48, s48, 819200\n\t"
    "ds_write_b128 v141, v[228:231] offset:2112\n\t"
    "ds_write_b128 v141, v[232:235] offset:2368\n\t"
    "ds_read_b64_tr_b16 v[228:229], v142 offset:2112\n\t"
    "ds_read_b64_tr_b16 v[230:231], v142 offset:2368\n\t"
    "ds_read_b64_tr_b16 v[232:233], v142 offset:2144\n\t"
    "ds_read_b64_tr_b16 v[234:235], v142 offset:2400\n\t"
    "v_bfi_b32 v130, v103, 0, v129\n\t"
    "v_cmp_eq_u32 vcc, 0, v130\n\t"
    "v_and_or_b32 v98, v103, s44, v69\n\t"
    "v_xor_b32 v133, 23, v132\n\t"
    "v_lshlrev_b32 v115, v132, v136\n\t"
    "v_and_b32 v132, 7, v132\n\t"
    "v_lshl_or_b32 v115, v136, v133, v115\n\t"
    "v_lshl_add_u32 v132, v132, 1, v128\n\t"
    "ds_or_b32 v97, v115\n\t"
    "ds_write_b16 v132, v104\n\t"
    "v_cndmask_b32 v130, v130, v129, vcc\n\t"
    "v_ffbl_b32 v131, v130\n\t"
    "v_and_b32 v134, 7, v131\n\t"
    "v_lshlrev_b32 v134, v134, s45\n\t"
    "v_xor_b32 v129, v129, v134\n\t"
    "v_xor_b32 v132, v131, v127\n\t"
    "v_bfi_b32 v130, v107, 0, v129\n\t"
    "v_cmp_eq_u32 vcc, 0, v130\n\t"
    "v_and_or_b32 v99, v107, s44, v69\n\t"
    "v_xor_b32 v133, 23, v132\n\t"
    "v_lshlrev_b32 v116, v132, v136\n\t"
    "v_and_b32 v132, 7, v132\n\t"
    "v_lshl_or_b32 v116, v136, v133, v116\n\t"
    "v_lshl_add_u32 v132, v132, 1, v128\n\t"
    "ds_or_b32 v98, v116\n\t"
    "ds_write_b16 v132, v103\n\t"
    "v_cndmask_b32 v130, v130, v129, vcc\n\t"
    "v_ffbl_b32 v131, v130\n\t"
    "v_xor_b32 v132, v131, v127\n\t"
    "v_xor_b32 v133, 23, v132\n\t"
    "v_lshlrev_b32 v117, v132, v136\n\t"
    "v_and_b32 v132, 7, v132\n\t"
    "v_lshl_or_b32 v117, v136, v133, v117\n\t"
    "v_lshl_add_u32 v132, v132, 1, v128\n\t"
    "ds_or_b32 v99, v117\n\t"
    "ds_write_b16 v132, v107\n\t"
    "ds_read_b128 v[72:75], v128\n\t"
    "v_and_or_b32 v100, v84, s44, v69\n\t"
    "ds_read_b32 v100, v100\n\t"
    "v_and_or_b32 v101, v85, s44, v69\n\t"
    "ds_read_b32 v101, v101\n\t"
    "v_and_or_b32 v102, v86, s44, v69\n\t"
    "ds_read_b32 v102, v102\n\t"
    "v_and_or_b32 v103, v87, s44, v69\n\t"
    "ds_read_b32 v103, v103\n\t"
    "v_and_or_b32 v104, v88, s44, v69\n\t"
    "ds_read_b32 v104, v104\n\t"
    "v_and_or_b32 v105, v89, s44, v69\n\t"
    "ds_read_b32 v105, v105\n\t"
    "v_and_or_b32 v106, v90, s44, v69\n\t"
    "ds_read_b32 v106, v106\n\t"
    "v_and_or_b32 v107, v91, s44, v69\n\t"
    "ds_read_b32 v107, v107\n\t"
    "v_lshl_add_u32 v76, v16, 3, s36\n\t"
    "v_lshl_add_u32 v77, v17, 3, s37\n\t"
    "v_lshl_add_u32 v78, v18, 3, s38\n\t"
    "v_lshl_add_u32 v79, v19, 3, s39\n\t"
    "v_lshl_add_u32 v80, v20, 3, s40\n\t"
    "v_lshl_add_u32 v81, v21, 3, s41\n\t"
    "v_lshl_add_u32 v82, v22, 3, s42\n\t"
    "v_lshl_add_u32 v83, v23, 3, s43\n\t"
    "s_waitcnt lgkmcnt(15)\n\t"
    "buffer_store_dwordx4 v[228:231], v139, %12, s48 offen sc1\n\t"
    "buffer_store_dwordx4 v[232:235], v140, %12, s48 offen sc1\n\t"
    "s_add_u32 s48, s48, -811008\n\t"
    "s_waitcnt lgkmcnt(8)\n\t"
    "buffer_store_dwordx4 v[72:75], v67, %12, s49 offen offset:416 sc1\n\t"
    "v_mov_b32 v129, 0xffff0000\n\t"
    "s_waitcnt lgkmcnt(4)\n\t"
    "v_perm_b32 v100, v100, v84, v126\n\t"
    "v_perm_b32 v101, v101, v85, v126\n\t"
    "v_perm_b32 v102, v102, v86, v126\n\t"
    "v_perm_b32 v103, v103, v87, v126\n\t"
    "s_waitcnt lgkmcnt(0)\n\t"
    "v_perm_b32 v104, v104, v88, v126\n\t"
    "v_perm_b32 v105, v105, v89, v126\n\t"
    "v_perm_b32 v106, v106, v90, v126\n\t"
    "v_perm_b32 v107, v107, v91, v126\n\t"
    "v_max_u32 v108, v100, v101\n\t"
    "v_min_u32 v101, v100, v101\n\t"
    "v_max_u32 v100, v102, v103\n\t"
    "v_min_u32 v103, v102, v103\n\t"
    "v_max_u32 v102, v104, v105\n\t"
    "v_min_u32 v105, v104, v105\n\t"
    "v_max_u32 v104, v106, v107\n\t"
    "v_min_u32 v107, v106, v107\n\t"
    "v_max_u32 v106, v108, v100\n\t"
    "v_min_u32 v100, v108, v100\n\t"
    "v_max_u32 v108, v101, v103\n\t"
    "v_min_u32 v103, v101, v103\n\t"
    "v_max_u32 v101, v102, v104\n\t"
    "v_min_u32 v104, v102, v104\n\t"
    "v_max_u32 v102, v105, v107\n\t"
    "v_min_u32 v107, v105, v107\n\t"
    "v_bfi_b32 v130, v106, 0, v129\n\t"
    "v_cmp_eq_u32 vcc, 0, v130\n\t"
    "v_and_or_b32 v92, v106, s44, v69\n\t"
    "s_nop 0\n\t"
    "v_cndmask_b32 v130, v130, v129, vcc\n\t"
    "v_ffbl_b32 v131, v130\n\t"
    "v_and_b32 v134, 7, v131\n\t"
    "v_lshlrev_b32 v134, v134, s45\n\t"
    "v_xor_b32 v129, v129, v134\n\t"
    "v_xor_b32 v132, v131, v127\n\t"
    "v_bfi_b32 v130, v101, 0, v129\n\t"
    "v_cmp_eq_u32 vcc, 0, v130\n\t"
    "v_and_or_b32 v93, v101, s44, v69\n\t"
    "v_xor_b32 v133, 23, v132\n\t"
    "v_lshlrev_b32 v110, v132, v136\n\t"
    "v_and_b32 v132, 7, v132\n\t"
    "v_lshl_or_b32 v110, v136, v133, v110\n\t"
    "v_lshl_add_u32 v132, v132, 1, v128\n\t"
    "ds_or_b32 v92, v110\n\t"
    "ds_write_b16 v132, v106\n\t"
    "v_cndmask_b32 v130, v130, v129, vcc\n\t"
    "v_ffbl_b32 v131, v130\n\t"
    "v_and_b32 v134, 7, v131\n\t"
    "v_lshlrev_b32 v134, v134, s45\n\t"
    "v_xor_b32 v129, v129, v134\n\t"
    "v_xor_b32 v132, v131, v127\n\t"
    "v_bfi_b32 v130, v108, 0, v129\n\t"
    "v_cmp_eq_u32 vcc, 0, v130\n\t"
    "v_and_or_b32 v94, v108, s44, v69\n\t"
    "v_xor_b32 v133, 23, v132\n\t"
    "v_lshlrev_b32 v111, v132, v136\n\t"
    "v_and_b32 v132, 7, v132\n\t"
    "v_lshl_or_b32 v111, v136, v133, v111\n\t"
    "v_lshl_add_u32 v132, v132, 1, v128\n\t"
    "ds_or_b32 v93, v111\n\t"
    "ds_write_b16 v132, v101\n\t"
    "v_cndmask_b32 v130, v130, v129, vcc\n\t"
    "v_ffbl_b32 v131, v130\n\t"
    "v_and_b32 v134, 7, v131\n\t"
    "v_lshlrev_b32 v134, v134, s45\n\t"
    "v_xor_b32 v129, v129, v134\n\t"
    "v_xor_b32 v132, v131, v127\n\t"
    "v_bfi_b32 v130, v100, 0, v129\n\t"
    "v_cmp_eq_u32 vcc, 0, v130\n\t"
    "v_and_or_b32 v95, v100, s44, v69\n\t"
    "v_xor_b32 v133, 23, v132\n\t"
    "v_lshlrev_b32 v112, v132, v136\n\t"
    "v_and_b32 v132, 7, v132\n\t"
    "v_lshl_or_b32 v112, v136, v133, v112\n\t"
    "v_lshl_add_u32 v132, v132, 1, v128\n\t"
    "ds_or_b32 v94, v112\n\t"
    "ds_write_b16 v132, v108\n\t"
    "v_cndmask_b32 v130, v130, v129, vcc\n\t"
    "v_ffbl_b32 v131, v130\n\t"
    "v_and_b32 v134, 7, v131\n\t"
    "v_lshlrev_b32 v134, v134, s45\n\t"
    "v_xor_b32 v129, v129, v134\n\t"
    "v_xor_b32 v132, v131, v127\n\t"
    "v_bfi_b32 v130, v102, 0, v129\n\t"
    "v_cmp_eq_u32 vcc, 0, v130\n\t"
    "v_and_or_b32 v96, v102, s44, v69\n\t"
    "v_xor_b32 v133, 23, v132\n\t"
    "v_lshlrev_b32 v113, v132, v136\n\t"
    "v_and_b32 v132, 7, v132\n\t"
    "v_lshl_or_b32 v113, v136, v133, v113\n\t"
    "v_lshl_add_u32 v132, v132, 1, v128\n\t"
    "ds_or_b32 v95, v113\n\t"
    "ds_write_b16 v132, v100\n\t"
    "v_cndmask_b32 v130, v130, v129, vcc\n\t"
    "v_ffbl_b32 v131, v130\n\t"
    "v_and_b32 v134, 7, v131\n\t"
    "v_lshlrev_b32 v134, v134, s45\n\t"
    "v_xor_b32 v129, v129, v134\n\t"
    "v_xor_b32 v132, v131, v127\n\t"
    "v_bfi_b32 v130, v104, 0, v129\n\t"
    "v_cmp_eq_u32 vcc, 0, v130\n\t"
    "v_and_or_b32 v97, v104, s44, v69\n\t"
    "v_xor_b32 v133, 23, v132\n\t"
    "v_lshlrev_b32 v114, v132, v136\n\t"
    "v_and_b32 v132, 7, v132\n\t"
    "v_lshl_or_b32 v114, v136, v133, v114\n\t"
    "v_lshl_add_u32 v132, v132, 1, v128\n\t"
    "ds_or_b32 v96, v114\n\t"
    "ds_write_b16 v132, v102\n\t"
    "v_cndmask_b32 v130, v130, v129, vcc\n\t"
    "v_ffbl_b32 v131, v130\n\t"
    "v_and_b32 v134, 7, v131\n\t"
    "v_lshlrev_b32 v134, v134, s45\n\t"
    "v_xor_b32 v129, v129, v134\n\t"
    "v_xor_b32 v132, v131, v127\n\t"
    "v_bfi_b32 v130, v103, 0, v129\n\t"
    "v_cmp_eq_u32 vcc, 0, v130\n\t"
    "v_and_or_b32 v98, v103, s44, v69\n\t"
    "v_xor_b32 v133, 23, v132\n\t"
    "v_lshlrev_b32 v115, v132, v136\n\t"
    "v_and_b32 v132, 7, v132\n\t"
    "v_lshl_or_b32 v115, v136, v133, v115\n\t"
    "v_lshl_add_u32 v132, v132, 1, v128\n\t"
    "ds_or_b32 v97, v115\n\t"
    "ds_write_b16 v132, v104\n\t"
    "v_cndmask_b32 v130, v130, v129, vcc\n\t"
    "v_ffbl_b32 v131, v130\n\t"
    "v_and_b32 v134, 7, v131\n\t"
    "v_lshlrev_b32 v134, v134, s45\n\t"
    "v_xor_b32 v129, v129, v134\n\t"
    "v_xor_b32 v132, v131, v127\n\t"
    "v_bfi_b32 v130, v107, 0, v129\n\t"
    "v_cmp_eq_u32 vcc, 0, v130\n\t"
    "v_and_or_b32 v99, v107, s44, v69\n\t"
    "v_xor_b32 v133, 23, v132\n\t"
    "v_lshlrev_b32 v116, v132, v136\n\t"
    "v_and_b32 v132, 7, v132\n\t"
    "v_lshl_or_b32 v116, v136, v133, v116\n\t"
    "v_lshl_add_u32 v132, v132, 1, v128\n\t"
    "ds_or_b32 v98, v116\n\t"
    "ds_write_b16 v132, v103\n\t"
    "v_cndmask_b32 v130, v130, v129, vcc\n\t"
    "v_ffbl_b32 v131, v130\n\t"
    "v_xor_b32 v132, v131, v127\n\t"
    "v_xor_b32 v133, 23, v132\n\t"
    "v_lshlrev_b32 v117, v132, v136\n\t"
    "v_and_b32 v132, 7, v132\n\t"
    "v_lshl_or_b32 v117, v136, v133, v117\n\t"
    "v_lshl_add_u32 v132, v132, 1, v128\n\t"
    "ds_or_b32 v99, v117\n\t"
    "ds_write_b16 v132, v107\n\t"
    "ds_read_b128 v[72:75], v128\n\t"
    "v_and_or_b32 v100, v76, s44, v69\n\t"
    "ds_read_b32 v100, v100\n\t"
    "v_and_or_b32 v101, v77, s44, v69\n\t"
    "ds_read_b32 v101, v101\n\t"
    "v_and_or_b32 v102, v78, s44, v69\n\t"
    "ds_read_b32 v102, v102\n\t"
    "v_and_or_b32 v103, v79, s44, v69\n\t"
    "ds_read_b32 v103, v103\n\t"
    "v_and_or_b32 v104, v80, s44, v69\n\t"
    "ds_read_b32 v104, v104\n\t"
    "v_and_or_b32 v105, v81, s44, v69\n\t"
    "ds_read_b32 v105, v105\n\t"
    "v_and_or_b32 v106, v82, s44, v69\n\t"
    "ds_read_b32 v106, v106\n\t"
    "v_and_or_b32 v107, v83, s44, v69\n\t"
    "ds_read_b32 v107, v107\n\t"
    "s_waitcnt lgkmcnt(8)\n\t"
    "buffer_store_dwordx4 v[72:75], v67, %12, s49 offen offset:448 sc1\n\t"
    "v_mov_b32 v129, 0xffff0000\n\t"
    "s_waitcnt lgkmcnt(4)\n\t"
    "v_perm_b32 v100, v100, v76, v126\n\t"
    "v_perm_b32 v101, v101, v77, v126\n\t"
    "v_perm_b32 v102, v102, v78, v126\n\t"
    "v_perm_b32 v103, v103, v79, v126\n\t"
    "s_waitcnt lgkmcnt(0)\n\t"
    "v_perm_b32 v104, v104, v80, v126\n\t"
    "v_perm_b32 v105, v105, v81, v126\n\t"
    "v_perm_b32 v106, v106, v82, v126\n\t"
    "v_perm_b32 v107, v107, v83, v126\n\t"
    "v_max_u32 v108, v100, v101\n\t"
    "v_min_u32 v101, v100, v101\n\t"
    "v_max_u32 v100, v102, v103\n\t"
    "v_min_u32 v103, v102, v103\n\t"
    "v_max_u32 v102, v104, v105\n\t"
    "v_min_u32 v105, v104, v105\n\t"
    "v_max_u32 v104, v106, v107\n\t"
    "v_min_u32 v107, v106, v107\n\t"
    "v_max_u32 v106, v108, v100\n\t"
    "v_min_u32 v100, v108, v100\n\t"
    "v_max_u32 v108, v101, v103\n\t"
    "v_min_u32 v103, v101, v103\n\t"
    "v_max_u32 v101, v102, v104\n\t"
    "v_min_u32 v104, v102, v104\n\t"
    "v_max_u32 v102, v105, v107\n\t"
    "v_min_u32 v107, v105, v107\n\t"
    "v_bfi_b32 v130, v106, 0, v129\n\t"
    "v_cmp_eq_u32 vcc, 0, v130\n\t"
    "v_and_or_b32 v92, v106, s44, v69\n\t"
    "s_nop 0\n\t"
    "v_cndmask_b32 v130, v130, v129, vcc\n\t"
    "v_ffbl_b32 v131, v130\n\t"
    "v_and_b32 v134, 7, v131\n\t"
    "v_lshlrev_b32 v134, v134, s45\n\t"
    "v_xor_b32 v129, v129, v134\n\t"
    "v_xor_b32 v132, v131, v127\n\t"
    "v_bfi_b32 v130, v101, 0, v129\n\t"
    "v_cmp_eq_u32 vcc, 0, v130\n\t"
    "v_and_b32 v132, 7, v132\n\t"
    "v_lshl_add_u32 v132, v132, 1, v128\n\t"
    "ds_write_b16 v132, v106\n\t"
    "v_cndmask_b32 v130, v130, v129, vcc\n\t"
    "v_ffbl_b32 v131, v130\n\t"
    "v_and_b32 v134, 7, v131\n\t"
    "v_lshlrev_b32 v134, v134, s45\n\t"
    "v_xor_b32 v129, v129, v134\n\t"
    "v_xor_b32 v132, v131, v127\n\t"
    "v_bfi_b32 v130, v108, 0, v129\n\t"
    "v_cmp_eq_u32 vcc, 0, v130\n\t"
    "v_and_b32 v132, 7, v132\n\t"
    "v_lshl_add_u32 v132, v132, 1, v128\n\t"
    "ds_write_b16 v132, v101\n\t"
    "v_cndmask_b32 v130, v130, v129, vcc\n\t"
    "v_ffbl_b32 v131, v130\n\t"
    "v_and_b32 v134, 7, v131\n\t"
    "v_lshlrev_b32 v134, v134, s45\n\t"
    "v_xor_b32 v129, v129, v134\n\t"
    "v_xor_b32 v132, v131, v127\n\t"
    "v_bfi_b32 v130, v100, 0, v129\n\t"
    "v_cmp_eq_u32 vcc, 0, v130\n\t"
    "v_and_b32 v132, 7, v132\n\t"
    "v_lshl_add_u32 v132, v132, 1, v128\n\t"
    "ds_write_b16 v132, v108\n\t"
    "v_cndmask_b32 v130, v130, v129, vcc\n\t"
    "v_ffbl_b32 v131, v130\n\t"
    "v_and_b32 v134, 7, v131\n\t"
    "v_lshlrev_b32 v134, v134, s45\n\t"
    "v_xor_b32 v129, v129, v134\n\t"
    "v_xor_b32 v132, v131, v127\n\t"
    "v_bfi_b32 v130, v102, 0, v129\n\t"
    "v_cmp_eq_u32 vcc, 0, v130\n\t"
    "v_and_b32 v132, 7, v132\n\t"
    "v_lshl_add_u32 v132, v132, 1, v128\n\t"
    "ds_write_b16 v132, v100\n\t"
    "v_cndmask_b32 v130, v130, v129, vcc\n\t"
    "v_ffbl_b32 v131, v130\n\t"
    "v_and_b32 v134, 7, v131\n\t"
    "v_lshlrev_b32 v134, v134, s45\n\t"
    "v_xor_b32 v129, v129, v134\n\t"
    "v_xor_b32 v132, v131, v127\n\t"
    "v_bfi_b32 v130, v104, 0, v129\n\t"
    "v_cmp_eq_u32 vcc, 0, v130\n\t"
    "v_and_b32 v132, 7, v132\n\t"
    "v_lshl_add_u32 v132, v132, 1, v128\n\t"
    "ds_write_b16 v132, v102\n\t"
    "v_cndmask_b32 v130, v130, v129, vcc\n\t"
    "v_ffbl_b32 v131, v130\n\t"
    "v_and_b32 v134, 7, v131\n\t"
    "v_lshlrev_b32 v134, v134, s45\n\t"
    "v_xor_b32 v129, v129, v134\n\t"
    "v_xor_b32 v132, v131, v127\n\t"
    "v_bfi_b32 v130, v103, 0, v129\n\t"
    "v_cmp_eq_u32 vcc, 0, v130\n\t"
    "v_and_b32 v132, 7, v132\n\t"
    "v_lshl_add_u32 v132, v132, 1, v128\n\t"
    "ds_write_b16 v132, v104\n\t"
    "v_cndmask_b32 v130, v130, v129, vcc\n\t"
    "v_ffbl_b32 v131, v130\n\t"
    "v_and_b32 v134, 7, v131\n\t"
    "v_lshlrev_b32 v134, v134, s45\n\t"
    "v_xor_b32 v129, v129, v134\n\t"
    "v_xor_b32 v132, v131, v127\n\t"
    "v_bfi_b32 v130, v107, 0, v129\n\t"
    "v_cmp_eq_u32 vcc, 0, v130\n\t"
    "v_and_b32 v132, 7, v132\n\t"
    "v_lshl_add_u32 v132, v132, 1, v128\n\t"
    "ds_write_b16 v132, v103\n\t"
    "v_cndmask_b32 v130, v130, v129, vcc\n\t"
    "v_ffbl_b32 v131, v130\n\t"
    "v_xor_b32 v132, v131, v127\n\t"
    "v_and_b32 v132, 7, v132\n\t"
    "v_lshl_add_u32 v132, v132, 1, v128\n\t"
    "ds_write_b16 v132, v107\n\t"
    "ds_read_b128 v[72:75], v128\n\t"
    "s_waitcnt lgkmcnt(0)\n\t"
    "buffer_store_dwordx4 v[72:75], v67, %12, s49 offen offset:480 sc1\n\t"
    "s_setprio 0\n\t"
    "v_mov_b32 v134, 1036\n\t"
    "L_asvsp12d_%=:\n\t"
    "ds_read_b32 v130, v134\n\t"
    "s_waitcnt lgkmcnt(0)\n\t"
    "v_readfirstlane_b32 s50, v130\n\t"
    "s_nop 0\n\t"
    "s_cmp_lt_u32 s50, 4\n\t"
    "s_cbranch_scc1 L_asvsp12d_%=\n\t"
    "s_call_b64 s[98:99], L_setup_%=\n\t"
    "v_lshrrev_b32 v105, 4, v127\n\t"
    "v_and_b32 v105, 48, v105\n\t"
    "s_movk_i32 s65, 130\n\t"
    "v_mad_u32_u24 v104, v155, s65, v105\n\t"
    "v_add_u32 v105, 14170, v104\n\t"
    "v_and_b32 v104, -16, v104\n\t"
    "v_and_b32 v105, -16, v105\n\t"
    "ds_read_b128 v[164:167], v104 offset:0\n\t"
    "ds_read_b128 v[168:171], v104 offset:64\n\t"
    "ds_read_b128 v[172:175], v104 offset:2080\n\t"
    "ds_read_b128 v[176:179], v104 offset:2144\n\t"
    "ds_read_b128 v[180:183], v104 offset:4160\n\t"
    "ds_read_b128 v[184:187], v104 offset:4224\n\t"
    "ds_read_b128 v[188:191], v104 offset:6240\n\t"
    "ds_read_b128 v[192:195], v104 offset:6304\n\t"
    "ds_read_b128 v[196:199], v104 offset:8320\n\t"
    "ds_read_b128 v[200:203], v104 offset:8384\n\t"
    "ds_read_b128 v[204:207], v104 offset:10400\n\t"
    "ds_read_b128 v[208:211], v104 offset:10464\n\t"
    "s_cmp_lt_u32 s62, 2\n\t"
    "s_cbranch_scc1 L_mel0_%=\n\t"
    "buffer_load_dwordx4 v[220:223], v127, %12, s61 offen offset:2048 sc1\n\t"
    "buffer_load_dwordx4 v[224:227], v127, %12, s61 offen offset:3072 sc1\n\t"
    "s_add_u32 s61, s61, 0x1000\n\t"
    "buffer_load_dwordx4 v[228:231], v127, %12, s61 offen offset:2048 sc1\n\t"
    "buffer_load_dwordx4 v[232:235], v127, %12, s61 offen offset:3072 sc1\n\t"
    "s_add_u32 s61, s61, 0x1000\n\t"
    "buffer_load_dwordx4 v[236:239], v127, %12, s61 offen offset:2048 sc1\n\t"
    "buffer_load_dwordx4 v[240:243], v127, %12, s61 offen offset:3072 sc1\n\t"
    "s_add_u32 s61, s61, 0x1000\n\t"
    "buffer_load_dwordx4 v[244:247], v127, %12, s61 offen offset:2048 sc1\n\t"
    "buffer_load_dwordx4 v[248:251], v127, %12, s61 offen offset:3072 sc1\n\t"
    "s_add_u32 s61, s61, 0x1000\n\t"
    "buffer_load_dwordx4 v[252:255], v127, %12, s61 offen offset:2048 sc1\n\t"
    "s_mov_b32 s62, 1\n\t"
    "L_mel0_%=:\n\t"
    "buffer_load_dwordx4 v[0:3], v127, %12, s63 offen offset:0 sc1\n\t"
    "buffer_load_dwordx4 v[4:7], v127, %12, s63 offen offset:1024 sc1\n\t"
    "buffer_load_dwordx4 v[8:11], v127, %12, s63 offen offset:2048 sc1\n\t"
    "buffer_load_dwordx4 v[12:15], v127, %12, s63 offen offset:3072 sc1\n\t"
    "s_add_u32 s63, s63, 0x1000\n\t"
    "s_mov_b32 s62, 0\n\t"
    "s_and_b32 s65, %14, 8\n\t"
    "s_lshl_b32 s65, s65, 10\n\t"
    "s_add_u32 s65, s65, s63\n\t"
    "s_mov_b32 m0, 136192\n\t"
    "s_add_u32 s65, s65, 0\n\t"
    "buffer_load_dwordx4 v127, %12, s65 offen offset:0 sc1 lds\n\t"
    "buffer_load_dwordx4 v127, %12, s65 offen offset:1024 sc1 lds\n\t"
    "buffer_load_dwordx4 v127, %12, s65 offen offset:2048 sc1 lds\n\t"
    "buffer_load_dwordx4 v127, %12, s65 offen offset:3072 sc1 lds\n\t"
    "s_mov_b32 m0, 140288\n\t"
    "s_add_u32 s65, s65, 4096\n\t"
    "buffer_load_dwordx4 v127, %12, s65 offen offset:0 sc1 lds\n\t"
    "buffer_load_dwordx4 v127, %12, s65 offen offset:1024 sc1 lds\n\t"
    "buffer_load_dwordx4 v127, %12, s65 offen offset:2048 sc1 lds\n\t"
    "buffer_load_dwordx4 v127, %12, s65 offen offset:3072 sc1 lds\n\t"
    "s_waitcnt vmcnt(21)\n\t"
    "s_and_b32 s50, %14, 7\n\t"
    "s_lshr_b32 s51, %14, 4\n\t"
    "s_add_u32 s51, s51, 8\n\t"
    "s_lshl_b32 s50, s50, 7\n\t"
    "s_lshl_b32 s51, s51, 7\n\t"
    "v_mov_b32 v132, s51\n\t"
    "v_writelane_b32 v132, s50, 0\n\t"
    "s_mov_b32 s50, 49286144\n\t"
    "s_mov_b64 exec, 3\n\t"
    "buffer_atomic_add v136, v132, %12, s50 offen sc1\n\t"
    "s_mov_b64 exec, -1\n\t"
    "s_branch L_af1x_%=\n\t"
    "L_af1w_%=:\n\t"
    "s_mov_b32 s51, 1048576\n\t"
    "s_mov_b64 exec, 0x1ff\n\t"
    "L_af1_%=:\n\t"
    "v_cmp_gt_u32 vcc, v148, v71\n\t"
    "s_cbranch_vccz L_af1ok_%=\n\t"
    "s_sleep 8\n\t"
    "buffer_load_dword v71, v70, %12, s52 offen sc1\n\t"
    "s_sub_u32 s51, s51, 1\n\t"
    "s_waitcnt vmcnt(0)\n\t"
    "s_cmp_lg_u32 s51, 0\n\t"
    "s_cbranch_scc1 L_af1_%=\n\t"
    "L_af1ok_%=:\n\t"
    "s_mov_b64 exec, -1\n\t"
    "v_mov_b32 v134, 3112\n\t"
    "ds_write_b32 v134, v136\n\t"
    "v_mbcnt_lo_u32_b32 v70, -1, 0\n\t"
    "v_mbcnt_hi_u32_b32 v70, -1, v70\n\t"
    "s_and_b32 s50, %14, 7\n\t"
    "s_lshl_b32 s50, s50, 4\n\t"
    "s_lshr_b32 s51, %14, 4\n\t"
    "s_add_u32 s50, s50, s51\n\t"
    "s_mul_i32 s50, s50, 102400\n\t"
    "s_add_u32 s50, s50, 3407872\n\t"
    "v_lshlrev_b32 v148, 7, v70\n\t"
    "buffer_load_dword v71, v148, %12, s50 offen sc1\n\t"
    "s_and_b32 s51, %14, 8\n\t"
    "s_lshl_b32 s51, s51, 10\n\t"
    "s_add_u32 s50, s50, s51\n\t"
    "s_add_u32 s50, s50, 0x1000\n\t"
    "v_lshrrev_b32 v149, 4, v70\n\t"
    "v_lshl_add_u32 v149, v149, 11, v148\n\t"
    "buffer_load_dword v71, v148, %12, s50 offen sc1\n\t"
    "s_bfe_u32 s50, %14, 0x40003\n\t"
    "s_lshl_b32 s50, s50, 14\n\t"
    "s_add_u32 s50, s50, 16517120\n\t"
    "v_lshrrev_b32 v150, 3, v70\n\t"
    "v_lshl_add_u32 v150, v150, 10, v148\n\t"
    "buffer_load_dword v71, v149, %12, s50 offen sc1\n\t"
    "s_and_b32 s50, %14, 8\n\t"
    "s_bfe_u32 s51, %14, 0x30004\n\t"
    "s_add_u32 s50, s50, s51\n\t"
    "s_mul_i32 s50, s50, 212992\n\t"
    "s_add_u32 s50, s50, 196608\n\t"
    "buffer_load_dword v71, v150, %12, s50 offen sc1\n\t"
    "s_setpc_b64 s[54:55]\n\t"
    "L_af1x_%=:\n\t"
    "s_mov_b32 s69, 0x40000000\n\t"
    "s_branch L_aend2_%=\n\t"
    "L_ahelp_%=:\n\t"
    "s_setprio 3\n\t"
    "v_cmp_gt_u32 vcc, 48, v147\n\t"
    "v_mov_b32 v133, 0x40000000\n\t"
    "v_lshrrev_b32 v134, 3, v147\n\t"
    "v_add_u32 v134, 6, v134\n\t"
    "v_bfe_u32 v135, v147, 2, 1\n\t"
    "v_and_b32 v130, 3, v147\n\t"
    "v_lshl_add_u32 v132, v135, 5, v130\n\t"
    "v_mul_u32_u24 v64, 400, v132\n\t"
    "v_lshl_add_u32 v64, v134, 5, v64\n\t"
    "s_movk_i32 s50, 0\n\t"
    "buffer_load_dwordx4 v[0:3], v64, %0, s50 offen offset:16\n\t"
    "buffer_load_dwordx4 v[4:7], v64, %0, s50 offen offset:32\n\t"
    "v_lshlrev_b32 v66, 14, v134\n\t"
    "v_lshl_add_u32 v66, v132, 4, v66\n\t"
    "v_lshlrev_b32 v68, 8, v134\n\t"
    "v_lshl_add_u32 v68, v135, 2, v68\n\t"
    "v_add_u32 v68, 130048, v68\n\t"
    "v_cndmask_b32 v68, v133, v68, vcc\n\t"
    "v_cndmask_b32 v66, v133, v66, vcc\n\t"
    "s_barrier\n\t"
    "s_load_dwordx2 s[72:73], %6, 0\n\t"
    "s_load_dwordx2 s[88:89], %7, 0\n\t"
    "s_load_dword s91, %8, 0\n\t"
    "v_lshlrev_b32 v128, 4, v147\n\t"
    "v_add_u32 v128, 133888, v128\n\t"
    "v_and_b32 v130, 1, v147\n\t"
    "v_cndmask_b32 v128, v133, v128, vcc\n\t"
    "v_mul_u32_u24 v127, 7, v130\n\t"
    "v_cmp_eq_u32 vcc, 1, v130\n\t"
    "v_mov_b32 v126, 0x07060100\n\t"
    "v_mov_b32 v133, 0x05040100\n\t"
    "v_mov_b32 v136, 1\n\t"
    "v_cndmask_b32 v126, v126, v133, vcc\n\t"
    "s_mov_b32 s36, 0\n\t"
    "s_mov_b32 s37, 8000\n\t"
    "s_mov_b32 s38, 16000\n\t"
    "s_mov_b32 s39, 24000\n\t"
    "s_mov_b32 s40, 32000\n\t"
    "s_mov_b32 s41, 40000\n\t"
    "s_mov_b32 s42, 48000\n\t"
    "s_mov_b32 s43, 56000\n\t"
    "s_movk_i32 s44, 0xf8\n\t"
    "s_mov_b32 s45, 0x01010000\n\t"
    "s_mov_b32 s46, 0x020c000c\n\t"
    "s_lshr_b32 s49, %14, 4\n\t"
    "s_mul_i32 s49, s49, 212992\n\t"
    "s_and_b32 s50, %14, 15\n\t"
    "s_lshl_b32 s50, s50, 10\n\t"
    "s_add_u32 s49, s49, s50\n\t"
    "s_lshl_b32 s52, %14, 2\n\t"
    "s_add_u32 s52, s52, 49291264\n\t"
    "buffer_load_dword v70, off, %12, s52 offset:1024 sc1\n\t"
    "s_cmp_eq_u32 %14, 0\n\t"
    "s_cselect_b32 s47, 49291264, 0x40000000\n\t"
    "s_cselect_b32 s48, 49283072, 0x40000000\n\t"
    "v_lshlrev_b32 v152, 4, v147\n\t"
    "v_lshlrev_b32 v132, 7, v147\n\t"
    "v_mov_b32 v71, 0\n\t"
    "buffer_store_dword v71, v132, %12, s48 offen sc1\n\t"
    "v_mov_b32 v148, 0\n\t"
    "v_mov_b32 v149, 0\n\t"
    "v_mov_b32 v150, 0\n\t"
    "v_mov_b32 v151, 0\n\t"
    "buffer_store_dwordx4 v[148:151], v152, %12, s47 offen offset:2048 sc1\n\t"
    "buffer_store_dwordx4 v[148:151], v152, %12, s47 offen offset:3072 sc1\n\t"
    "s_add_u32 s50, s48, 0x1000\n\t"
    "s_waitcnt vmcnt(6)\n\t"
    "v_lshlrev_b32 v134, 20, v147\n\t"
    "v_add_u32 v134, 2068, v134\n\t"
    "ds_add_u32 v134, v136\n\t"
    "v_mov_b32 v110, 65664\n\t"
    "v_mov_b32 v111, 131136\n\t"
    "v_mov_b32 v112, 262176\n\t"
    "v_mov_b32 v113, 524304\n\t"
    "v_mov_b32 v114, 1048584\n\t"
    "v_mov_b32 v115, 2097156\n\t"
    "v_mov_b32 v116, 4194306\n\t"
    "v_mov_b32 v117, 8388609\n\t"
    "s_waitcnt vmcnt(4)\n\t"
    "v_lshl_add_u32 v76, v0, 3, s36\n\t"
    "v_lshl_add_u32 v77, v1, 3, s37\n\t"
    "v_lshl_add_u32 v78, v2, 3, s38\n\t"
    "v_lshl_add_u32 v79, v3, 3, s39\n\t"
    "v_lshl_add_u32 v80, v4, 3, s40\n\t"
    "v_lshl_add_u32 v81, v5, 3, s41\n\t"
    "v_lshl_add_u32 v82, v6, 3, s42\n\t"
    "v_lshl_add_u32 v83, v7, 3, s43\n\t"
    "v_and_or_b32 v92, v76, s44, v68\n\t"
    "ds_or_rtn_b32 v118, v92, v110\n\t"
    "v_and_or_b32 v93, v77, s44, v68\n\t"
    "ds_or_rtn_b32 v119, v93, v111\n\t"
    "v_and_or_b32 v94, v78, s44, v68\n\t"
    "ds_or_rtn_b32 v120, v94, v112\n\t"
    "v_and_or_b32 v95, v79, s44, v68\n\t"
    "ds_or_rtn_b32 v121, v95, v113\n\t"
    "v_and_or_b32 v96, v80, s44, v68\n\t"
    "ds_or_rtn_b32 v122, v96, v114\n\t"
    "v_and_or_b32 v97, v81, s44, v68\n\t"
    "ds_or_rtn_b32 v123, v97, v115\n\t"
    "v_and_or_b32 v98, v82, s44, v68\n\t"
    "ds_or_rtn_b32 v124, v98, v116\n\t"
    "v_and_or_b32 v99, v83, s44, v68\n\t"
    "ds_or_rtn_b32 v125, v99, v117\n\t"
    "v_lshl_or_b32 v164, v77, 16, v76\n\t"
    "v_lshl_or_b32 v165, v79, 16, v78\n\t"
    "v_lshl_or_b32 v166, v81, 16, v80\n\t"
    "v_lshl_or_b32 v167, v83, 16, v82\n\t"
    "s_movk_i32 s50, 0\n\t"
    "buffer_load_dwordx4 v[8:11], v64, %0, s50 offen offset:1616\n\t"
    "buffer_load_dwordx4 v[12:15], v64, %0, s50 offen offset:1632\n\t"
    "s_movk_i32 s50, 3200\n\t"
    "buffer_load_dwordx4 v[16:19], v64, %0, s50 offen offset:16\n\t"
    "buffer_load_dwordx4 v[20:23], v64, %0, s50 offen offset:32\n\t"
    "s_waitcnt vmcnt(7)\n\t"
    "v_add_u32 v70, 1, v70\n\t"
    "buffer_store_dword v70, off, %12, s52 sc1\n\t"
    "v_mov_b32 v71, 2072\n\t"
    "ds_write_b32 v71, v70\n\t"
    "s_waitcnt vmcnt(3)\n\t"
    "v_lshl_add_u32 v84, v8, 3, s36\n\t"
    "v_lshl_add_u32 v85, v9, 3, s37\n\t"
    "v_lshl_add_u32 v86, v10, 3, s38\n\t"
    "v_lshl_add_u32 v87, v11, 3, s39\n\t"
    "v_lshl_add_u32 v88, v12, 3, s40\n\t"
    "v_lshl_add_u32 v89, v13, 3, s41\n\t"
    "v_lshl_add_u32 v90, v14, 3, s42\n\t"
    "v_lshl_add_u32 v91, v15, 3, s43\n\t"
    "s_waitcnt lgkmcnt(5)\n\t"
    "v_and_b32 v118, v118, v110\n\t"
    "v_perm_b32 v118, v118, v118, s46\n\t"
    "ds_or_b32 v92, v118\n\t"
    "v_and_b32 v119, v119, v111\n\t"
    "v_perm_b32 v119, v119, v119, s46\n\t"
    "ds_or_b32 v93, v119\n\t"
    "v_and_b32 v120, v120, v112\n\t"
    "v_perm_b32 v120, v120, v120, s46\n\t"
    "ds_or_b32 v94, v120\n\t"
    "v_and_b32 v121, v121, v113\n\t"
    "v_perm_b32 v121, v121, v121, s46\n\t"
    "ds_or_b32 v95, v121\n\t"
    "s_waitcnt lgkmcnt(5)\n\t"
    "v_and_b32 v122, v122, v114\n\t"
    "v_perm_b32 v122, v122, v122, s46\n\t"
    "ds_or_b32 v96, v122\n\t"
    "v_and_b32 v123, v123, v115\n\t"
    "v_perm_b32 v123, v123, v123, s46\n\t"
    "ds_or_b32 v97, v123\n\t"
    "v_and_b32 v124, v124, v116\n\t"
    "v_perm_b32 v124, v124, v124, s46\n\t"
    "ds_or_b32 v98, v124\n\t"
    "v_and_b32 v125, v125, v117\n\t"
    "v_perm_b32 v125, v125, v125, s46\n\t"
    "ds_or_b32 v99, v125\n\t"
    "buffer_store_dwordx4 v[164:167], v66, %12, s49 offen offset:0 sc1\n\t"
    "v_and_or_b32 v100, v84, s44, v68\n\t"
    "ds_read_b32 v100, v100\n\t"
    "v_and_or_b32 v101, v85, s44, v68\n\t"
    "ds_read_b32 v101, v101\n\t"
    "v_and_or_b32 v102, v86, s44, v68\n\t"
    "ds_read_b32 v102, v102\n\t"
    "v_and_or_b32 v103, v87, s44, v68\n\t"
    "ds_read_b32 v103, v103\n\t"
    "v_and_or_b32 v104, v88, s44, v68\n\t"
    "ds_read_b32 v104, v104\n\t"
    "v_and_or_b32 v105, v89, s44, v68\n\t"
    "ds_read_b32 v105, v105\n\t"
    "v_and_or_b32 v106, v90, s44, v68\n\t"
    "ds_read_b32 v106, v106\n\t"
    "v_and_or_b32 v107, v91, s44, v68\n\t"
    "ds_read_b32 v107, v107\n\t"
    "s_waitcnt vmcnt(2)\n\t"
    "v_lshl_add_u32 v76, v16, 3, s36\n\t"
    "v_lshl_add_u32 v77, v17, 3, s37\n\t"
    "v_lshl_add_u32 v78, v18, 3, s38\n\t"
    "v_lshl_add_u32 v79, v19, 3, s39\n\t"
    "v_lshl_add_u32 v80, v20, 3, s40\n\t"
    "v_lshl_add_u32 v81, v21, 3, s41\n\t"
    "v_lshl_add_u32 v82, v22, 3, s42\n\t"
    "v_lshl_add_u32 v83, v23, 3, s43\n\t"
    "s_movk_i32 s50, 3200\n\t"
    "buffer_load_dwordx4 v[24:27], v64, %0, s50 offen offset:1616\n\t"
    "buffer_load_dwordx4 v[28:31], v64, %0, s50 offen offset:1632\n\t"
    "s_movk_i32 s50, 6400\n\t"
    "buffer_load_dwordx4 v[32:35], v64, %0, s50 offen offset:16\n\t"
    "buffer_load_dwordx4 v[36:39], v64, %0, s50 offen offset:32\n\t"
    "s_cmp_lg_u32 %14, 0\n\t"
    "s_cbranch_scc1 L_ahe184_%=\n\t"
    "buffer_load_dwordx4 v[148:151], v152, %12, s47 offen sc1\n\t"
    "L_ahe184_%=:\n\t"
    "v_mov_b32 v129, 0xffff0000\n\t"
    "s_waitcnt lgkmcnt(4)\n\t"
    "v_perm_b32 v100, v100, v84, v126\n\t"
    "v_perm_b32 v101, v101, v85, v126\n\t"
    "v_perm_b32 v102, v102, v86, v126\n\t"
    "v_perm_b32 v103, v103, v87, v126\n\t"
    "s_waitcnt lgkmcnt(0)\n\t"
    "v_perm_b32 v104, v104, v88, v126\n\t"
    "v_perm_b32 v105, v105, v89, v126\n\t"
    "v_perm_b32 v106, v106, v90, v126\n\t"
    "v_perm_b32 v107, v107, v91, v126\n\t"
    "v_max_u32 v108, v100, v101\n\t"
    "v_min_u32 v101, v100, v101\n\t"
    "v_max_u32 v100, v102, v103\n\t"
    "v_min_u32 v103, v102, v103\n\t"
    "v_max_u32 v102, v104, v105\n\t"
    "v_min_u32 v105, v104, v105\n\t"
    "v_max_u32 v104, v106, v107\n\t"
    "v_min_u32 v107, v106, v107\n\t"
    "v_max_u32 v106, v108, v100\n\t"
    "v_min_u32 v100, v108, v100\n\t"
    "v_max_u32 v108, v101, v103\n\t"
    "v_min_u32 v103, v101, v103\n\t"
    "v_max_u32 v101, v102, v104\n\t"
    "v_min_u32 v104, v102, v104\n\t"
    "v_max_u32 v102, v105, v107\n\t"
    "v_min_u32 v107, v105, v107\n\t"
    "v_bfi_b32 v130, v106, 0, v129\n\t"
    "v_cmp_eq_u32 vcc, 0, v130\n\t"
    "v_and_or_b32 v92, v106, s44, v68\n\t"
    "s_nop 0\n\t"
    "v_cndmask_b32 v130, v130, v129, vcc\n\t"
    "v_ffbl_b32 v131, v130\n\t"
    "v_and_b32 v134, 7, v131\n\t"
    "v_lshlrev_b32 v134, v134, s45\n\t"
    "v_xor_b32 v129, v129, v134\n\t"
    "v_xor_b32 v132, v131, v127\n\t"
    "v_bfi_b32 v130, v101, 0, v129\n\t"
    "v_cmp_eq_u32 vcc, 0, v130\n\t"
    "v_and_or_b32 v93, v101, s44, v68\n\t"
    "v_xor_b32 v133, 23, v132\n\t"
    "v_lshlrev_b32 v110, v132, v136\n\t"
    "v_and_b32 v132, 7, v132\n\t"
    "v_lshl_or_b32 v110, v136, v133, v110\n\t"
    "v_lshl_add_u32 v132, v132, 1, v128\n\t"
    "ds_or_rtn_b32 v118, v92, v110\n\t"
    "ds_write_b16 v132, v106\n\t"
    "v_cndmask_b32 v130, v130, v129, vcc\n\t"
    "v_ffbl_b32 v131, v130\n\t"
    "v_and_b32 v134, 7, v131\n\t"
    "v_lshlrev_b32 v134, v134, s45\n\t"
    "v_xor_b32 v129, v129, v134\n\t"
    "v_xor_b32 v132, v131, v127\n\t"
    "v_bfi_b32 v130, v108, 0, v129\n\t"
    "v_cmp_eq_u32 vcc, 0, v130\n\t"
    "v_and_or_b32 v94, v108, s44, v68\n\t"
    "v_xor_b32 v133, 23, v132\n\t"
    "v_lshlrev_b32 v111, v132, v136\n\t"
    "v_and_b32 v132, 7, v132\n\t"
    "v_lshl_or_b32 v111, v136, v133, v111\n\t"
    "v_lshl_add_u32 v132, v132, 1, v128\n\t"
    "ds_or_rtn_b32 v119, v93, v111\n\t"
    "ds_write_b16 v132, v101\n\t"
    "v_cndmask_b32 v130, v130, v129, vcc\n\t"
    "v_ffbl_b32 v131, v130\n\t"
    "v_and_b32 v134, 7, v131\n\t"
    "v_lshlrev_b32 v134, v134, s45\n\t"
    "v_xor_b32 v129, v129, v134\n\t"
    "v_xor_b32 v132, v131, v127\n\t"
    "v_bfi_b32 v130, v100, 0, v129\n\t"
    "v_cmp_eq_u32 vcc, 0, v130\n\t"
    "v_and_or_b32 v95, v100, s44, v68\n\t"
    "v_xor_b32 v133, 23, v132\n\t"
    "v_lshlrev_b32 v112, v132, v136\n\t"
    "v_and_b32 v132, 7, v132\n\t"
    "v_lshl_or_b32 v112, v136, v133, v112\n\t"
    "v_lshl_add_u32 v132, v132, 1, v128\n\t"
    "ds_or_rtn_b32 v120, v94, v112\n\t"
    "ds_write_b16 v132, v108\n\t"
    "v_cndmask_b32 v130, v130, v129, vcc\n\t"
    "v_ffbl_b32 v131, v130\n\t"
    "v_and_b32 v134, 7, v131\n\t"
    "v_lshlrev_b32 v134, v134, s45\n\t"
    "v_xor_b32 v129, v129, v134\n\t"
    "v_xor_b32 v132, v131, v127\n\t"
    "v_bfi_b32 v130, v102, 0, v129\n\t"
    "v_cmp_eq_u32 vcc, 0, v130\n\t"
    "v_and_or_b32 v96, v102, s44, v68\n\t"
    "v_xor_b32 v133, 23, v132\n\t"
    "v_lshlrev_b32 v113, v132, v136\n\t"
    "v_and_b32 v132, 7, v132\n\t"
    "v_lshl_or_b32 v113, v136, v133, v113\n\t"
    "v_lshl_add_u32 v132, v132, 1, v128\n\t"
    "ds_or_rtn_b32 v121, v95, v113\n\t"
    "ds_write_b16 v132, v100\n\t"
    "v_cndmask_b32 v130, v130, v129, vcc\n\t"
    "v_ffbl_b32 v131, v130\n\t"
    "v_and_b32 v134, 7, v131\n\t"
    "v_lshlrev_b32 v134, v134, s45\n\t"
    "v_xor_b32 v129, v129, v134\n\t"
    "v_xor_b32 v132, v131, v127\n\t"
    "s_waitcnt lgkmcnt(7)\n\t"
    "v_and_b32 v118, v118, v110\n\t"
    "v_perm_b32 v118, v118, v118, s46\n\t"
    "ds_or_b32 v92, v118\n\t"
    "v_bfi_b32 v130, v104, 0, v129\n\t"
    "v_cmp_eq_u32 vcc, 0, v130\n\t"
    "v_and_or_b32 v97, v104, s44, v68\n\t"
    "v_xor_b32 v133, 23, v132\n\t"
    "v_lshlrev_b32 v114, v132, v136\n\t"
    "v_and_b32 v132, 7, v132\n\t"
    "v_lshl_or_b32 v114, v136, v133, v114\n\t"
    "v_lshl_add_u32 v132, v132, 1, v128\n\t"
    "ds_or_rtn_b32 v122, v96, v114\n\t"
    "ds_write_b16 v132, v102\n\t"
    "v_cndmask_b32 v130, v130, v129, vcc\n\t"
    "v_ffbl_b32 v131, v130\n\t"
    "v_and_b32 v134, 7, v131\n\t"
    "v_lshlrev_b32 v134, v134, s45\n\t"
    "v_xor_b32 v129, v129, v134\n\t"
    "v_xor_b32 v132, v131, v127\n\t"
    "s_waitcnt lgkmcnt(8)\n\t"
    "v_and_b32 v119, v119, v111\n\t"
    "v_perm_b32 v119, v119, v119, s46\n\t"
    "ds_or_b32 v93, v119\n\t"
    "v_bfi_b32 v130, v103, 0, v129\n\t"
    "v_cmp_eq_u32 vcc, 0, v130\n\t"
    "v_and_or_b32 v98, v103, s44, v68\n\t"
    "v_xor_b32 v133, 23, v132\n\t"
    "v_lshlrev_b32 v115, v132, v136\n\t"
    "v_and_b32 v132, 7, v132\n\t"
    "v_lshl_or_b32 v115, v136, v133, v115\n\t"
    "v_lshl_add_u32 v132, v132, 1, v128\n\t"
    "ds_or_rtn_b32 v123, v97, v115\n\t"
    "ds_write_b16 v132, v104\n\t"
    "v_cndmask_b32 v130, v130, v129, vcc\n\t"
    "v_ffbl_b32 v131, v130\n\t"
    "v_and_b32 v134, 7, v131\n\t"
    "v_lshlrev_b32 v134, v134, s45\n\t"
    "v_xor_b32 v129, v129, v134\n\t"
    "v_xor_b32 v132, v131, v127\n\t"
    "s_waitcnt lgkmcnt(9)\n\t"
    "v_and_b32 v120, v120, v112\n\t"
    "v_perm_b32 v120, v120, v120, s46\n\t"
    "ds_or_b32 v94, v120\n\t"
    "v_bfi_b32 v130, v107, 0, v129\n\t"
    "v_cmp_eq_u32 vcc, 0, v130\n\t"
    "v_and_or_b32 v99, v107, s44, v68\n\t"
    "v_xor_b32 v133, 23, v132\n\t"
    "v_lshlrev_b32 v116, v132, v136\n\t"
    "v_and_b32 v132, 7, v132\n\t"
    "v_lshl_or_b32 v116, v136, v133, v116\n\t"
    "v_lshl_add_u32 v132, v132, 1, v128\n\t"
    "ds_or_rtn_b32 v124, v98, v116\n\t"
    "ds_write_b16 v132, v103\n\t"
    "v_cndmask_b32 v130, v130, v129, vcc\n\t"
    "v_ffbl_b32 v131, v130\n\t"
    "v_xor_b32 v132, v131, v127\n\t"
    "s_waitcnt lgkmcnt(10)\n\t"
    "v_and_b32 v121, v121, v113\n\t"
    "v_perm_b32 v121, v121, v121, s46\n\t"
    "ds_or_b32 v95, v121\n\t"
    "v_xor_b32 v133, 23, v132\n\t"
    "v_lshlrev_b32 v117, v132, v136\n\t"
    "v_and_b32 v132, 7, v132\n\t"
    "v_lshl_or_b32 v117, v136, v133, v117\n\t"
    "v_lshl_add_u32 v132, v132, 1, v128\n\t"
    "ds_or_rtn_b32 v125, v99, v117\n\t"
    "ds_write_b16 v132, v107\n\t"
    "s_waitcnt lgkmcnt(10)\n\t"
    "v_and_b32 v122, v122, v114\n\t"
    "v_perm_b32 v122, v122, v122, s46\n\t"
    "ds_or_b32 v96, v122\n\t"
    "ds_read_b128 v[72:75], v128\n\t"
    "v_and_or_b32 v100, v76, s44, v68\n\t"
    "ds_read_b32 v100, v100\n\t"
    "v_and_or_b32 v101, v77, s44, v68\n\t"
    "ds_read_b32 v101, v101\n\t"
    "v_and_or_b32 v102, v78, s44, v68\n\t"
    "ds_read_b32 v102, v102\n\t"
    "v_and_or_b32 v103, v79, s44, v68\n\t"
    "ds_read_b32 v103, v103\n\t"
    "v_and_or_b32 v104, v80, s44, v68\n\t"
    "ds_read_b32 v104, v104\n\t"
    "v_and_or_b32 v105, v81, s44, v68\n\t"
    "ds_read_b32 v105, v105\n\t"
    "v_and_or_b32 v106, v82, s44, v68\n\t"
    "ds_read_b32 v106, v106\n\t"
    "v_and_or_b32 v107, v83, s44, v68\n\t"
    "ds_read_b32 v107, v107\n\t"
    "s_cmp_lg_u32 %14, 0\n\t"
    "s_cbranch_scc1 L_ahe378_%=\n\t"
    "s_waitcnt vmcnt(0)\n\t"
    "buffer_store_dwordx4 v[148:151], v152, %12, s47 offen offset:1024 sc1\n\t"
    "L_ahe378_%=:\n\t"
    "s_waitcnt vmcnt(2)\n\t"
    "v_lshl_add_u32 v84, v24, 3, s36\n\t"
    "v_lshl_add_u32 v85, v25, 3, s37\n\t"
    "v_lshl_add_u32 v86, v26, 3, s38\n\t"
    "v_lshl_add_u32 v87, v27, 3, s39\n\t"
    "v_lshl_add_u32 v88, v28, 3, s40\n\t"
    "v_lshl_add_u32 v89, v29, 3, s41\n\t"
    "v_lshl_add_u32 v90, v30, 3, s42\n\t"
    "v_lshl_add_u32 v91, v31, 3, s43\n\t"
    "v_mul_u32_u24 v58, 400, v147\n\t"
    "buffer_load_dwordx4 v[48:51], v58, %0, 0 offen\n\t"
    "s_lshr_b32 s50, %14, 5\n\t"
    "s_mul_i32 s50, s50, 125\n\t"
    "s_bfe_u32 s51, %14, 0x30002\n\t"
    "s_lshl_b32 s51, s51, 4\n\t"
    "s_min_u32 s51, s51, 109\n\t"
    "s_add_u32 s50, s50, s51\n\t"
    "s_lshl_b32 s50, s50, 8\n\t"
    "s_and_b32 s51, %14, 3\n\t"
    "s_lshl_b32 s51, s51, 6\n\t"
    "s_add_u32 s50, s50, s51\n\t"
    "s_bitcmp1_b32 %14, 1\n\t"
    "s_cselect_b32 s50, s50, 0x40000000\n\t"
    "v_and_b32 v59, 48, v147\n\t"
    "v_and_b32 v60, 15, v147\n\t"
    "v_lshl_add_u32 v59, v60, 8, v59\n\t"
    "buffer_load_dwordx4 v[52:55], v59, %15, s50 offen\n\t"
    "s_cmp_lg_u32 %14, 0\n\t"
    "s_cbranch_scc1 L_ahe410_%=\n\t"
    "buffer_load_dwordx4 v[148:151], v152, %12, s47 offen sc1\n\t"
    "L_ahe410_%=:\n\t"
    "s_waitcnt lgkmcnt(11)\n\t"
    "v_and_b32 v123, v123, v115\n\t"
    "v_perm_b32 v123, v123, v123, s46\n\t"
    "ds_or_b32 v97, v123\n\t"
    "v_and_b32 v124, v124, v116\n\t"
    "v_perm_b32 v124, v124, v124, s46\n\t"
    "ds_or_b32 v98, v124\n\t"
    "v_and_b32 v125, v125, v117\n\t"
    "v_perm_b32 v125, v125, v125, s46\n\t"
    "ds_or_b32 v99, v125\n\t"
    "s_waitcnt lgkmcnt(11)\n\t"
    "buffer_store_dwordx4 v[72:75], v66, %12, s49 offen offset:64 sc1\n\t"
    "v_mov_b32 v129, 0xffff0000\n\t"
    "s_waitcnt lgkmcnt(7)\n\t"
    "v_perm_b32 v100, v100, v76, v126\n\t"
    "v_perm_b32 v101, v101, v77, v126\n\t"
    "v_perm_b32 v102, v102, v78, v126\n\t"
    "v_perm_b32 v103, v103, v79, v126\n\t"
    "s_waitcnt lgkmcnt(3)\n\t"
    "v_perm_b32 v104, v104, v80, v126\n\t"
    "v_perm_b32 v105, v105, v81, v126\n\t"
    "v_perm_b32 v106, v106, v82, v126\n\t"
    "v_perm_b32 v107, v107, v83, v126\n\t"
    "v_max_u32 v108, v100, v101\n\t"
    "v_min_u32 v101, v100, v101\n\t"
    "v_max_u32 v100, v102, v103\n\t"
    "v_min_u32 v103, v102, v103\n\t"
    "v_max_u32 v102, v104, v105\n\t"
    "v_min_u32 v105, v104, v105\n\t"
    "v_max_u32 v104, v106, v107\n\t"
    "v_min_u32 v107, v106, v107\n\t"
    "v_max_u32 v106, v108, v100\n\t"
    "v_min_u32 v100, v108, v100\n\t"
    "v_max_u32 v108, v101, v103\n\t"
    "v_min_u32 v103, v101, v103\n\t"
    "v_max_u32 v101, v102, v104\n\t"
    "v_min_u32 v104, v102, v104\n\t"
    "v_max_u32 v102, v105, v107\n\t"
    "v_min_u32 v107, v105, v107\n\t"
    "v_bfi_b32 v130, v106, 0, v129\n\t"
    "v_cmp_eq_u32 vcc, 0, v130\n\t"
    "v_and_or_b32 v92, v106, s44, v68\n\t"
    "s_nop 0\n\t"
    "v_cndmask_b32 v130, v130, v129, vcc\n\t"
    "v_ffbl_b32 v131, v130\n\t"
    "v_and_b32 v134, 7, v131\n\t"
    "v_lshlrev_b32 v134, v134, s45\n\t"
    "v_xor_b32 v129, v129, v134\n\t"
    "v_xor_b32 v132, v131, v127\n\t"
    "v_bfi_b32 v130, v101, 0, v129\n\t"
    "v_cmp_eq_u32 vcc, 0, v130\n\t"
    "v_and_or_b32 v93, v101, s44, v68\n\t"
    "v_xor_b32 v133, 23, v132\n\t"
    "v_lshlrev_b32 v110, v132, v136\n\t"
    "v_and_b32 v132, 7, v132\n\t"
    "v_lshl_or_b32 v110, v136, v133, v110\n\t"
    "v_lshl_add_u32 v132, v132, 1, v128\n\t"
    "ds_or_rtn_b32 v118, v92, v110\n\t"
    "ds_write_b16 v132, v106\n\t"
    "v_cndmask_b32 v130, v130, v129, vcc\n\t"
    "v_ffbl_b32 v131, v130\n\t"
    "v_and_b32 v134, 7, v131\n\t"
    "v_lshlrev_b32 v134, v134, s45\n\t"
    "v_xor_b32 v129, v129, v134\n\t"
    "v_xor_b32 v132, v131, v127\n\t"
    "v_bfi_b32 v130, v108, 0, v129\n\t"
    "v_cmp_eq_u32 vcc, 0, v130\n\t"
    "v_and_or_b32 v94, v108, s44, v68\n\t"
    "v_xor_b32 v133, 23, v132\n\t"
    "v_lshlrev_b32 v111, v132, v136\n\t"
    "v_and_b32 v132, 7, v132\n\t"
    "v_lshl_or_b32 v111, v136, v133, v111\n\t"
    "v_lshl_add_u32 v132, v132, 1, v128\n\t"
    "ds_or_rtn_b32 v119, v93, v111\n\t"
    "ds_write_b16 v132, v101\n\t"
    "v_cndmask_b32 v130, v130, v129, vcc\n\t"
    "v_ffbl_b32 v131, v130\n\t"
    "v_and_b32 v134, 7, v131\n\t"
    "v_lshlrev_b32 v134, v134, s45\n\t"
    "v_xor_b32 v129, v129, v134\n\t"
    "v_xor_b32 v132, v131, v127\n\t"
    "v_bfi_b32 v130, v100, 0, v129\n\t"
    "v_cmp_eq_u32 vcc, 0, v130\n\t"
    "v_and_or_b32 v95, v100, s44, v68\n\t"
    "v_xor_b32 v133, 23, v132\n\t"
    "v_lshlrev_b32 v112, v132, v136\n\t"
    "v_and_b32 v132, 7, v132\n\t"
    "v_lshl_or_b32 v112, v136, v133, v112\n\t"
    "v_lshl_add_u32 v132, v132, 1, v128\n\t"
    "ds_or_rtn_b32 v120, v94, v112\n\t"
    "ds_write_b16 v132, v108\n\t"
    "v_cndmask_b32 v130, v130, v129, vcc\n\t"
    "v_ffbl_b32 v131, v130\n\t"
    "v_and_b32 v134, 7, v131\n\t"
    "v_lshlrev_b32 v134, v134, s45\n\t"
    "v_xor_b32 v129, v129, v134\n\t"
    "v_xor_b32 v132, v131, v127\n\t"
    "v_bfi_b32 v130, v102, 0, v129\n\t"
    "v_cmp_eq_u32 vcc, 0, v130\n\t"
    "v_and_or_b32 v96, v102, s44, v68\n\t"
    "v_xor_b32 v133, 23, v132\n\t"
    "v_lshlrev_b32 v113, v132, v136\n\t"
    "v_and_b32 v132, 7, v132\n\t"
    "v_lshl_or_b32 v113, v136, v133, v113\n\t"
    "v_lshl_add_u32 v132, v132, 1, v128\n\t"
    "ds_or_rtn_b32 v121, v95, v113\n\t"
    "ds_write_b16 v132, v100\n\t"
    "v_cndmask_b32 v130, v130, v129, vcc\n\t"
    "v_ffbl_b32 v131, v130\n\t"
    "v_and_b32 v134, 7, v131\n\t"
    "v_lshlrev_b32 v134, v134, s45\n\t"
    "v_xor_b32 v129, v129, v134\n\t"
    "v_xor_b32 v132, v131, v127\n\t"
    "s_waitcnt lgkmcnt(7)\n\t"
    "v_and_b32 v118, v118, v110\n\t"
    "v_perm_b32 v118, v118, v118, s46\n\t"
    "ds_or_b32 v92, v118\n\t"
    "v_bfi_b32 v130, v104, 0, v129\n\t"
    "v_cmp_eq_u32 vcc, 0, v130\n\t"
    "v_and_or_b32 v97, v104, s44, v68\n\t"
    "v_xor_b32 v133, 23, v132\n\t"
    "v_lshlrev_b32 v114, v132, v136\n\t"
    "v_and_b32 v132, 7, v132\n\t"
    "v_lshl_or_b32 v114, v136, v133, v114\n\t"
    "v_lshl_add_u32 v132, v132, 1, v128\n\t"
    "ds_or_rtn_b32 v122, v96, v114\n\t"
    "ds_write_b16 v132, v102\n\t"
    "v_cndmask_b32 v130, v130, v129, vcc\n\t"
    "v_ffbl_b32 v131, v130\n\t"
    "v_and_b32 v134, 7, v131\n\t"
    "v_lshlrev_b32 v134, v134, s45\n\t"
    "v_xor_b32 v129, v129, v134\n\t"
    "v_xor_b32 v132, v131, v127\n\t"
    "s_waitcnt lgkmcnt(8)\n\t"
    "v_and_b32 v119, v119, v111\n\t"
    "v_perm_b32 v119, v119, v119, s46\n\t"
    "ds_or_b32 v93, v119\n\t"
    "v_bfi_b32 v130, v103, 0, v129\n\t"
    "v_cmp_eq_u32 vcc, 0, v130\n\t"
    "v_and_or_b32 v98, v103, s44, v68\n\t"
    "v_xor_b32 v133, 23, v132\n\t"
    "v_lshlrev_b32 v115, v132, v136\n\t"
    "v_and_b32 v132, 7, v132\n\t"
    "v_lshl_or_b32 v115, v136, v133, v115\n\t"
    "v_lshl_add_u32 v132, v132, 1, v128\n\t"
    "ds_or_rtn_b32 v123, v97, v115\n\t"
    "ds_write_b16 v132, v104\n\t"
    "v_cndmask_b32 v130, v130, v129, vcc\n\t"
    "v_ffbl_b32 v131, v130\n\t"
    "v_and_b32 v134, 7, v131\n\t"
    "v_lshlrev_b32 v134, v134, s45\n\t"
    "v_xor_b32 v129, v129, v134\n\t"
    "v_xor_b32 v132, v131, v127\n\t"
    "s_waitcnt lgkmcnt(9)\n\t"
    "v_and_b32 v120, v120, v112\n\t"
    "v_perm_b32 v120, v120, v120, s46\n\t"
    "ds_or_b32 v94, v120\n\t"
    "v_bfi_b32 v130, v107, 0, v129\n\t"
    "v_cmp_eq_u32 vcc, 0, v130\n\t"
    "v_and_or_b32 v99, v107, s44, v68\n\t"
    "v_xor_b32 v133, 23, v132\n\t"
    "v_lshlrev_b32 v116, v132, v136\n\t"
    "v_and_b32 v132, 7, v132\n\t"
    "v_lshl_or_b32 v116, v136, v133, v116\n\t"
    "v_lshl_add_u32 v132, v132, 1, v128\n\t"
    "ds_or_rtn_b32 v124, v98, v116\n\t"
    "ds_write_b16 v132, v103\n\t"
    "v_cndmask_b32 v130, v130, v129, vcc\n\t"
    "v_ffbl_b32 v131, v130\n\t"
    "v_xor_b32 v132, v131, v127\n\t"
    "s_waitcnt lgkmcnt(10)\n\t"
    "v_and_b32 v121, v121, v113\n\t"
    "v_perm_b32 v121, v121, v121, s46\n\t"
    "ds_or_b32 v95, v121\n\t"
    "v_xor_b32 v133, 23, v132\n\t"
    "v_lshlrev_b32 v117, v132, v136\n\t"
    "v_and_b32 v132, 7, v132\n\t"
    "v_lshl_or_b32 v117, v136, v133, v117\n\t"
    "v_lshl_add_u32 v132, v132, 1, v128\n\t"
    "ds_or_rtn_b32 v125, v99, v117\n\t"
    "ds_write_b16 v132, v107\n\t"
    "s_waitcnt lgkmcnt(10)\n\t"
    "v_and_b32 v122, v122, v114\n\t"
    "v_perm_b32 v122, v122, v122, s46\n\t"
    "ds_or_b32 v96, v122\n\t"
    "ds_read_b128 v[72:75], v128\n\t"
    "v_and_or_b32 v100, v84, s44, v68\n\t"
    "ds_read_b32 v100, v100\n\t"
    "v_and_or_b32 v101, v85, s44, v68\n\t"
    "ds_read_b32 v101, v101\n\t"
    "v_and_or_b32 v102, v86, s44, v68\n\t"
    "ds_read_b32 v102, v102\n\t"
    "v_and_or_b32 v103, v87, s44, v68\n\t"
    "ds_read_b32 v103, v103\n\t"
    "v_and_or_b32 v104, v88, s44, v68\n\t"
    "ds_read_b32 v104, v104\n\t"
    "v_and_or_b32 v105, v89, s44, v68\n\t"
    "ds_read_b32 v105, v105\n\t"
    "v_and_or_b32 v106, v90, s44, v68\n\t"
    "ds_read_b32 v106, v106\n\t"
    "v_and_or_b32 v107, v91, s44, v68\n\t"
    "ds_read_b32 v107, v107\n\t"
    "s_cmp_lg_u32 %14, 0\n\t"
    "s_cbranch_scc1 L_ahe616_%=\n\t"
    "s_waitcnt vmcnt(0)\n\t"
    "buffer_store_dwordx4 v[148:151], v152, %12, s47 offen offset:1024 sc1\n\t"
    "L_ahe616_%=:\n\t"
    "s_waitcnt vmcnt(3)\n\t"
    "v_lshl_add_u32 v76, v32, 3, s36\n\t"
    "v_lshl_add_u32 v77, v33, 3, s37\n\t"
    "v_lshl_add_u32 v78, v34, 3, s38\n\t"
    "v_lshl_add_u32 v79, v35, 3, s39\n\t"
    "v_lshl_add_u32 v80, v36, 3, s40\n\t"
    "v_lshl_add_u32 v81, v37, 3, s41\n\t"
    "v_lshl_add_u32 v82, v38, 3, s42\n\t"
    "v_lshl_add_u32 v83, v39, 3, s43\n\t"
    "s_waitcnt vmcnt(1)\n\t"
    "v_lshl_add_u32 v48, v48, 3, s36\n\t"
    "v_lshl_add_u32 v49, v49, 3, s37\n\t"
    "v_lshl_add_u32 v50, v50, 3, s38\n\t"
    "v_lshl_add_u32 v51, v51, 3, s39\n\t"
    "v_lshl_or_b32 v56, v49, 16, v48\n\t"
    "v_lshl_or_b32 v57, v51, 16, v50\n\t"
    "s_and_b32 s51, %14, 1\n\t"
    "s_mul_i32 s51, s51, 1016\n\t"
    "s_sub_u32 s50, s49, s51\n\t"
    "s_add_u32 s50, s50, 196608\n\t"
    "buffer_store_dwordx2 v[56:57], v152, %12, s50 offen sc1\n\t"
    "s_lshl_b32 s50, %14, 10\n\t"
    "s_add_u32 s50, s50, 16515072\n\t"
    "s_bitcmp1_b32 %14, 1\n\t"
    "s_cselect_b32 s50, s50, 0x40000000\n\t"
    "buffer_store_dwordx4 v[52:55], v152, %12, s50 offen sc1\n\t"
    "s_mul_i32 s50, %14, 69632\n\t"
    "s_add_u32 s50, s50, 31522816\n\t"
    "v_mov_b32 v60, 0\n\t"
    "v_lshlrev_b32 v61, 3, v152\n\t"
    "s_mov_b64 exec, 0xffffffff\n\t"
    "buffer_store_dword v60, v61, %12, s50 offen sc1\n\t"
    "s_lshl_b32 s50, %14, 7\n\t"
    "s_add_u32 s50, s50, 49295360\n\t"
    "v_lshrrev_b32 v62, 2, v152\n\t"
    "buffer_store_dword v60, v62, %12, s50 offen sc1\n\t"
    "s_mov_b64 exec, -1\n\t"
    "s_cmp_lg_u32 %14, 0\n\t"
    "s_cbranch_scc1 L_ahe658_%=\n\t"
    "buffer_load_dwordx4 v[148:151], v152, %12, s47 offen sc1\n\t"
    "L_ahe658_%=:\n\t"
    "s_waitcnt lgkmcnt(11)\n\t"
    "v_and_b32 v123, v123, v115\n\t"
    "v_perm_b32 v123, v123, v123, s46\n\t"
    "ds_or_b32 v97, v123\n\t"
    "v_and_b32 v124, v124, v116\n\t"
    "v_perm_b32 v124, v124, v124, s46\n\t"
    "ds_or_b32 v98, v124\n\t"
    "v_and_b32 v125, v125, v117\n\t"
    "v_perm_b32 v125, v125, v125, s46\n\t"
    "ds_or_b32 v99, v125\n\t"
    "s_waitcnt lgkmcnt(11)\n\t"
    "buffer_store_dwordx4 v[72:75], v66, %12, s49 offen offset:128 sc1\n\t"
    "v_mov_b32 v129, 0xffff0000\n\t"
    "s_waitcnt lgkmcnt(7)\n\t"
    "v_perm_b32 v100, v100, v84, v126\n\t"
    "v_perm_b32 v101, v101, v85, v126\n\t"
    "v_perm_b32 v102, v102, v86, v126\n\t"
    "v_perm_b32 v103, v103, v87, v126\n\t"
    "s_waitcnt lgkmcnt(3)\n\t"
    "v_perm_b32 v104, v104, v88, v126\n\t"
    "v_perm_b32 v105, v105, v89, v126\n\t"
    "v_perm_b32 v106, v106, v90, v126\n\t"
    "v_perm_b32 v107, v107, v91, v126\n\t"
    "v_max_u32 v108, v100, v101\n\t"
    "v_min_u32 v101, v100, v101\n\t"
    "v_max_u32 v100, v102, v103\n\t"
    "v_min_u32 v103, v102, v103\n\t"
    "v_max_u32 v102, v104, v105\n\t"
    "v_min_u32 v105, v104, v105\n\t"
    "v_max_u32 v104, v106, v107\n\t"
    "v_min_u32 v107, v106, v107\n\t"
    "v_max_u32 v106, v108, v100\n\t"
    "v_min_u32 v100, v108, v100\n\t"
    "v_max_u32 v108, v101, v103\n\t"
    "v_min_u32 v103, v101, v103\n\t"
    "v_max_u32 v101, v102, v104\n\t"
    "v_min_u32 v104, v102, v104\n\t"
    "v_max_u32 v102, v105, v107\n\t"
    "v_min_u32 v107, v105, v107\n\t"
    "v_bfi_b32 v130, v106, 0, v129\n\t"
    "v_cmp_eq_u32 vcc, 0, v130\n\t"
    "v_and_or_b32 v92, v106, s44, v68\n\t"
    "s_nop 0\n\t"
    "v_cndmask_b32 v130, v130, v129, vcc\n\t"
    "v_ffbl_b32 v131, v130\n\t"
    "v_and_b32 v134, 7, v131\n\t"
    "v_lshlrev_b32 v134, v134, s45\n\t"
    "v_xor_b32 v129, v129, v134\n\t"
    "v_xor_b32 v132, v131, v127\n\t"
    "v_bfi_b32 v130, v101, 0, v129\n\t"
    "v_cmp_eq_u32 vcc, 0, v130\n\t"
    "v_and_or_b32 v93, v101, s44, v68\n\t"
    "v_xor_b32 v133, 23, v132\n\t"
    "v_lshlrev_b32 v110, v132, v136\n\t"
    "v_and_b32 v132, 7, v132\n\t"
    "v_lshl_or_b32 v110, v136, v133, v110\n\t"
    "v_lshl_add_u32 v132, v132, 1, v128\n\t"
    "ds_or_rtn_b32 v118, v92, v110\n\t"
    "ds_write_b16 v132, v106\n\t"
    "v_cndmask_b32 v130, v130, v129, vcc\n\t"
    "v_ffbl_b32 v131, v130\n\t"
    "v_and_b32 v134, 7, v131\n\t"
    "v_lshlrev_b32 v134, v134, s45\n\t"
    "v_xor_b32 v129, v129, v134\n\t"
    "v_xor_b32 v132, v131, v127\n\t"
    "v_bfi_b32 v130, v108, 0, v129\n\t"
    "v_cmp_eq_u32 vcc, 0, v130\n\t"
    "v_and_or_b32 v94, v108, s44, v68\n\t"
    "v_xor_b32 v133, 23, v132\n\t"
    "v_lshlrev_b32 v111, v132, v136\n\t"
    "v_and_b32 v132, 7, v132\n\t"
    "v_lshl_or_b32 v111, v136, v133, v111\n\t"
    "v_lshl_add_u32 v132, v132, 1, v128\n\t"
    "ds_or_rtn_b32 v119, v93, v111\n\t"
    "ds_write_b16 v132, v101\n\t"
    "v_cndmask_b32 v130, v130, v129, vcc\n\t"
    "v_ffbl_b32 v131, v130\n\t"
    "v_and_b32 v134, 7, v131\n\t"
    "v_lshlrev_b32 v134, v134, s45\n\t"
    "v_xor_b32 v129, v129, v134\n\t"
    "v_xor_b32 v132, v131, v127\n\t"
    "v_bfi_b32 v130, v100, 0, v129\n\t"
    "v_cmp_eq_u32 vcc, 0, v130\n\t"
    "v_and_or_b32 v95, v100, s44, v68\n\t"
    "v_xor_b32 v133, 23, v132\n\t"
    "v_lshlrev_b32 v112, v132, v136\n\t"
    "v_and_b32 v132, 7, v132\n\t"
    "v_lshl_or_b32 v112, v136, v133, v112\n\t"
    "v_lshl_add_u32 v132, v132, 1, v128\n\t"
    "ds_or_rtn_b32 v120, v94, v112\n\t"
    "ds_write_b16 v132, v108\n\t"
    "v_cndmask_b32 v130, v130, v129, vcc\n\t"
    "v_ffbl_b32 v131, v130\n\t"
    "v_and_b32 v134, 7, v131\n\t"
    "v_lshlrev_b32 v134, v134, s45\n\t"
    "v_xor_b32 v129, v129, v134\n\t"
    "v_xor_b32 v132, v131, v127\n\t"
    "v_bfi_b32 v130, v102, 0, v129\n\t"
    "v_cmp_eq_u32 vcc, 0, v130\n\t"
    "v_and_or_b32 v96, v102, s44, v68\n\t"
    "v_xor_b32 v133, 23, v132\n\t"
    "v_lshlrev_b32 v113, v132, v136\n\t"
    "v_and_b32 v132, 7, v132\n\t"
    "v_lshl_or_b32 v113, v136, v133, v113\n\t"
    "v_lshl_add_u32 v132, v132, 1, v128\n\t"
    "ds_or_rtn_b32 v121, v95, v113\n\t"
    "ds_write_b16 v132, v100\n\t"
    "v_cndmask_b32 v130, v130, v129, vcc\n\t"
    "v_ffbl_b32 v131, v130\n\t"
    "v_and_b32 v134, 7, v131\n\t"
    "v_lshlrev_b32 v134, v134, s45\n\t"
    "v_xor_b32 v129, v129, v134\n\t"
    "v_xor_b32 v132, v131, v127\n\t"
    "s_waitcnt lgkmcnt(7)\n\t"
    "v_and_b32 v118, v118, v110\n\t"
    "v_perm_b32 v118, v118, v118, s46\n\t"
    "ds_or_b32 v92, v118\n\t"
    "v_bfi_b32 v130, v104, 0, v129\n\t"
    "v_cmp_eq_u32 vcc, 0, v130\n\t"
    "v_and_or_b32 v97, v104, s44, v68\n\t"
    "v_xor_b32 v133, 23, v132\n\t"
    "v_lshlrev_b32 v114, v132, v136\n\t"
    "v_and_b32 v132, 7, v132\n\t"
    "v_lshl_or_b32 v114, v136, v133, v114\n\t"
    "v_lshl_add_u32 v132, v132, 1, v128\n\t"
    "ds_or_rtn_b32 v122, v96, v114\n\t"
    "ds_write_b16 v132, v102\n\t"
    "v_cndmask_b32 v130, v130, v129, vcc\n\t"
    "v_ffbl_b32 v131, v130\n\t"
    "v_and_b32 v134, 7, v131\n\t"
    "v_lshlrev_b32 v134, v134, s45\n\t"
    "v_xor_b32 v129, v129, v134\n\t"
    "v_xor_b32 v132, v131, v127\n\t"
    "s_waitcnt lgkmcnt(8)\n\t"
    "v_and_b32 v119, v119, v111\n\t"
    "v_perm_b32 v119, v119, v119, s46\n\t"
    "ds_or_b32 v93, v119\n\t"
    "v_bfi_b32 v130, v103, 0, v129\n\t"
    "v_cmp_eq_u32 vcc, 0, v130\n\t"
    "v_and_or_b32 v98, v103, s44, v68\n\t"
    "v_xor_b32 v133, 23, v132\n\t"
    "v_lshlrev_b32 v115, v132, v136\n\t"
    "v_and_b32 v132, 7, v132\n\t"
    "v_lshl_or_b32 v115, v136, v133, v115\n\t"
    "v_lshl_add_u32 v132, v132, 1, v128\n\t"
    "ds_or_rtn_b32 v123, v97, v115\n\t"
    "ds_write_b16 v132, v104\n\t"
    "v_cndmask_b32 v130, v130, v129, vcc\n\t"
    "v_ffbl_b32 v131, v130\n\t"
    "v_and_b32 v134, 7, v131\n\t"
    "v_lshlrev_b32 v134, v134, s45\n\t"
    "v_xor_b32 v129, v129, v134\n\t"
    "v_xor_b32 v132, v131, v127\n\t"
    "s_waitcnt lgkmcnt(9)\n\t"
    "v_and_b32 v120, v120, v112\n\t"
    "v_perm_b32 v120, v120, v120, s46\n\t"
    "ds_or_b32 v94, v120\n\t"
    "v_bfi_b32 v130, v107, 0, v129\n\t"
    "v_cmp_eq_u32 vcc, 0, v130\n\t"
    "v_and_or_b32 v99, v107, s44, v68\n\t"
    "v_xor_b32 v133, 23, v132\n\t"
    "v_lshlrev_b32 v116, v132, v136\n\t"
    "v_and_b32 v132, 7, v132\n\t"
    "v_lshl_or_b32 v116, v136, v133, v116\n\t"
    "v_lshl_add_u32 v132, v132, 1, v128\n\t"
    "ds_or_rtn_b32 v124, v98, v116\n\t"
    "ds_write_b16 v132, v103\n\t"
    "v_cndmask_b32 v130, v130, v129, vcc\n\t"
    "v_ffbl_b32 v131, v130\n\t"
    "v_xor_b32 v132, v131, v127\n\t"
    "s_waitcnt lgkmcnt(10)\n\t"
    "v_and_b32 v121, v121, v113\n\t"
    "v_perm_b32 v121, v121, v121, s46\n\t"
    "ds_or_b32 v95, v121\n\t"
    "v_xor_b32 v133, 23, v132\n\t"
    "v_lshlrev_b32 v117, v132, v136\n\t"
    "v_and_b32 v132, 7, v132\n\t"
    "v_lshl_or_b32 v117, v136, v133, v117\n\t"
    "v_lshl_add_u32 v132, v132, 1, v128\n\t"
    "ds_or_rtn_b32 v125, v99, v117\n\t"
    "ds_write_b16 v132, v107\n\t"
    "s_waitcnt lgkmcnt(10)\n\t"
    "v_and_b32 v122, v122, v114\n\t"
    "v_perm_b32 v122, v122, v122, s46\n\t"
    "ds_or_b32 v96, v122\n\t"
    "ds_read_b128 v[72:75], v128\n\t"
    "v_and_or_b32 v100, v76, s44, v68\n\t"
    "ds_read_b32 v100, v100\n\t"
    "v_and_or_b32 v101, v77, s44, v68\n\t"
    "ds_read_b32 v101, v101\n\t"
    "v_and_or_b32 v102, v78, s44, v68\n\t"
    "ds_read_b32 v102, v102\n\t"
    "v_and_or_b32 v103, v79, s44, v68\n\t"
    "ds_read_b32 v103, v103\n\t"
    "v_and_or_b32 v104, v80, s44, v68\n\t"
    "ds_read_b32 v104, v104\n\t"
    "v_and_or_b32 v105, v81, s44, v68\n\t"
    "ds_read_b32 v105, v105\n\t"
    "v_and_or_b32 v106, v82, s44, v68\n\t"
    "ds_read_b32 v106, v106\n\t"
    "v_and_or_b32 v107, v83, s44, v68\n\t"
    "ds_read_b32 v107, v107\n\t"
    "s_cmp_lg_u32 %14, 0\n\t"
    "s_cbranch_scc1 L_ahe864_%=\n\t"
    "s_waitcnt vmcnt(0)\n\t"
    "buffer_store_dwordx4 v[148:151], v152, %12, s47 offen offset:1024 sc1\n\t"
    "L_ahe864_%=:\n\t"
    "s_cmp_lg_u32 %14, 0\n\t"
    "s_cbranch_scc1 L_ahe869_%=\n\t"
    "buffer_load_dwordx4 v[148:151], v152, %12, s47 offen sc1\n\t"
    "L_ahe869_%=:\n\t"
    "s_waitcnt lgkmcnt(11)\n\t"
    "v_and_b32 v123, v123, v115\n\t"
    "v_perm_b32 v123, v123, v123, s46\n\t"
    "ds_or_b32 v97, v123\n\t"
    "v_and_b32 v124, v124, v116\n\t"
    "v_perm_b32 v124, v124, v124, s46\n\t"
    "ds_or_b32 v98, v124\n\t"
    "v_and_b32 v125, v125, v117\n\t"
    "v_perm_b32 v125, v125, v125, s46\n\t"
    "ds_or_b32 v99, v125\n\t"
    "s_waitcnt lgkmcnt(11)\n\t"
    "buffer_store_dwordx4 v[72:75], v66, %12, s49 offen offset:192 sc1\n\t"
    "v_mov_b32 v129, 0xffff0000\n\t"
    "s_waitcnt lgkmcnt(7)\n\t"
    "v_perm_b32 v100, v100, v76, v126\n\t"
    "v_perm_b32 v101, v101, v77, v126\n\t"
    "v_perm_b32 v102, v102, v78, v126\n\t"
    "v_perm_b32 v103, v103, v79, v126\n\t"
    "s_waitcnt lgkmcnt(3)\n\t"
    "v_perm_b32 v104, v104, v80, v126\n\t"
    "v_perm_b32 v105, v105, v81, v126\n\t"
    "v_perm_b32 v106, v106, v82, v126\n\t"
    "v_perm_b32 v107, v107, v83, v126\n\t"
    "v_max_u32 v108, v100, v101\n\t"
    "v_min_u32 v101, v100, v101\n\t"
    "v_max_u32 v100, v102, v103\n\t"
    "v_min_u32 v103, v102, v103\n\t"
    "v_max_u32 v102, v104, v105\n\t"
    "v_min_u32 v105, v104, v105\n\t"
    "v_max_u32 v104, v106, v107\n\t"
    "v_min_u32 v107, v106, v107\n\t"
    "v_max_u32 v106, v108, v100\n\t"
    "v_min_u32 v100, v108, v100\n\t"
    "v_max_u32 v108, v101, v103\n\t"
    "v_min_u32 v103, v101, v103\n\t"
    "v_max_u32 v101, v102, v104\n\t"
    "v_min_u32 v104, v102, v104\n\t"
    "v_max_u32 v102, v105, v107\n\t"
    "v_min_u32 v107, v105, v107\n\t"
    "v_bfi_b32 v130, v106, 0, v129\n\t"
    "v_cmp_eq_u32 vcc, 0, v130\n\t"
    "v_and_or_b32 v92, v106, s44, v68\n\t"
    "s_nop 0\n\t"
    "v_cndmask_b32 v130, v130, v129, vcc\n\t"
    "v_ffbl_b32 v131, v130\n\t"
    "v_and_b32 v134, 7, v131\n\t"
    "v_lshlrev_b32 v134, v134, s45\n\t"
    "v_xor_b32 v129, v129, v134\n\t"
    "v_xor_b32 v132, v131, v127\n\t"
    "v_bfi_b32 v130, v101, 0, v129\n\t"
    "v_cmp_eq_u32 vcc, 0, v130\n\t"
    "v_and_or_b32 v93, v101, s44, v68\n\t"
    "v_xor_b32 v133, 23, v132\n\t"
    "v_lshlrev_b32 v110, v132, v136\n\t"
    "v_and_b32 v132, 7, v132\n\t"
    "v_lshl_or_b32 v110, v136, v133, v110\n\t"
    "v_lshl_add_u32 v132, v132, 1, v128\n\t"
    "ds_or_rtn_b32 v118, v92, v110\n\t"
    "ds_write_b16 v132, v106\n\t"
    "v_cndmask_b32 v130, v130, v129, vcc\n\t"
    "v_ffbl_b32 v131, v130\n\t"
    "v_and_b32 v134, 7, v131\n\t"
    "v_lshlrev_b32 v134, v134, s45\n\t"
    "v_xor_b32 v129, v129, v134\n\t"
    "v_xor_b32 v132, v131, v127\n\t"
    "v_bfi_b32 v130, v108, 0, v129\n\t"
    "v_cmp_eq_u32 vcc, 0, v130\n\t"
    "v_and_or_b32 v94, v108, s44, v68\n\t"
    "v_xor_b32 v133, 23, v132\n\t"
    "v_lshlrev_b32 v111, v132, v136\n\t"
    "v_and_b32 v132, 7, v132\n\t"
    "v_lshl_or_b32 v111, v136, v133, v111\n\t"
    "v_lshl_add_u32 v132, v132, 1, v128\n\t"
    "ds_or_rtn_b32 v119, v93, v111\n\t"
    "ds_write_b16 v132, v101\n\t"
    "v_cndmask_b32 v130, v130, v129, vcc\n\t"
    "v_ffbl_b32 v131, v130\n\t"
    "v_and_b32 v134, 7, v131\n\t"
    "v_lshlrev_b32 v134, v134, s45\n\t"
    "v_xor_b32 v129, v129, v134\n\t"
    "v_xor_b32 v132, v131, v127\n\t"
    "v_bfi_b32 v130, v100, 0, v129\n\t"
    "v_cmp_eq_u32 vcc, 0, v130\n\t"
    "v_and_or_b32 v95, v100, s44, v68\n\t"
    "v_xor_b32 v133, 23, v132\n\t"
    "v_lshlrev_b32 v112, v132, v136\n\t"
    "v_and_b32 v132, 7, v132\n\t"
    "v_lshl_or_b32 v112, v136, v133, v112\n\t"
    "v_lshl_add_u32 v132, v132, 1, v128\n\t"
    "ds_or_rtn_b32 v120, v94, v112\n\t"
    "ds_write_b16 v132, v108\n\t"
    "v_cndmask_b32 v130, v130, v129, vcc\n\t"
    "v_ffbl_b32 v131, v130\n\t"
    "v_and_b32 v134, 7, v131\n\t"
    "v_lshlrev_b32 v134, v134, s45\n\t"
    "v_xor_b32 v129, v129, v134\n\t"
    "v_xor_b32 v132, v131, v127\n\t"
    "v_bfi_b32 v130, v102, 0, v129\n\t"
    "v_cmp_eq_u32 vcc, 0, v130\n\t"
    "v_and_or_b32 v96, v102, s44, v68\n\t"
    "v_xor_b32 v133, 23, v132\n\t"
    "v_lshlrev_b32 v113, v132, v136\n\t"
    "v_and_b32 v132, 7, v132\n\t"
    "v_lshl_or_b32 v113, v136, v133, v113\n\t"
    "v_lshl_add_u32 v132, v132, 1, v128\n\t"
    "ds_or_rtn_b32 v121, v95, v113\n\t"
    "ds_write_b16 v132, v100\n\t"
    "v_cndmask_b32 v130, v130, v129, vcc\n\t"
    "v_ffbl_b32 v131, v130\n\t"
    "v_and_b32 v134, 7, v131\n\t"
    "v_lshlrev_b32 v134, v134, s45\n\t"
    "v_xor_b32 v129, v129, v134\n\t"
    "v_xor_b32 v132, v131, v127\n\t"
    "s_waitcnt lgkmcnt(7)\n\t"
    "v_and_b32 v118, v118, v110\n\t"
    "v_perm_b32 v118, v118, v118, s46\n\t"
    "ds_or_b32 v92, v118\n\t"
    "s_waitcnt vmcnt(2)\n\t"
    "ds_write_b32 v71, v136 offset:4\n\t"
    "v_bfi_b32 v130, v104, 0, v129\n\t"
    "v_cmp_eq_u32 vcc, 0, v130\n\t"
    "v_and_or_b32 v97, v104, s44, v68\n\t"
    "v_xor_b32 v133, 23, v132\n\t"
    "v_lshlrev_b32 v114, v132, v136\n\t"
    "v_and_b32 v132, 7, v132\n\t"
    "v_lshl_or_b32 v114, v136, v133, v114\n\t"
    "v_lshl_add_u32 v132, v132, 1, v128\n\t"
    "ds_or_rtn_b32 v122, v96, v114\n\t"
    "ds_write_b16 v132, v102\n\t"
    "v_cndmask_b32 v130, v130, v129, vcc\n\t"
    "v_ffbl_b32 v131, v130\n\t"
    "v_and_b32 v134, 7, v131\n\t"
    "v_lshlrev_b32 v134, v134, s45\n\t"
    "v_xor_b32 v129, v129, v134\n\t"
    "v_xor_b32 v132, v131, v127\n\t"
    "s_waitcnt lgkmcnt(9)\n\t"
    "v_and_b32 v119, v119, v111\n\t"
    "v_perm_b32 v119, v119, v119, s46\n\t"
    "ds_or_b32 v93, v119\n\t"
    "v_bfi_b32 v130, v103, 0, v129\n\t"
    "v_cmp_eq_u32 vcc, 0, v130\n\t"
    "v_and_or_b32 v98, v103, s44, v68\n\t"
    "v_xor_b32 v133, 23, v132\n\t"
    "v_lshlrev_b32 v115, v132, v136\n\t"
    "v_and_b32 v132, 7, v132\n\t"
    "v_lshl_or_b32 v115, v136, v133, v115\n\t"
    "v_lshl_add_u32 v132, v132, 1, v128\n\t"
    "ds_or_rtn_b32 v123, v97, v115\n\t"
    "ds_write_b16 v132, v104\n\t"
    "v_cndmask_b32 v130, v130, v129, vcc\n\t"
    "v_ffbl_b32 v131, v130\n\t"
    "v_and_b32 v134, 7, v131\n\t"
    "v_lshlrev_b32 v134, v134, s45\n\t"
    "v_xor_b32 v129, v129, v134\n\t"
    "v_xor_b32 v132, v131, v127\n\t"
    "s_waitcnt lgkmcnt(10)\n\t"
    "v_and_b32 v120, v120, v112\n\t"
    "v_perm_b32 v120, v120, v120, s46\n\t"
    "ds_or_b32 v94, v120\n\t"
    "v_bfi_b32 v130, v107, 0, v129\n\t"
    "v_cmp_eq_u32 vcc, 0, v130\n\t"
    "v_and_or_b32 v99, v107, s44, v68\n\t"
    "v_xor_b32 v133, 23, v132\n\t"
    "v_lshlrev_b32 v116, v132, v136\n\t"
    "v_and_b32 v132, 7, v132\n\t"
    "v_lshl_or_b32 v116, v136, v133, v116\n\t"
    "v_lshl_add_u32 v132, v132, 1, v128\n\t"
    "ds_or_rtn_b32 v124, v98, v116\n\t"
    "ds_write_b16 v132, v103\n\t"
    "v_cndmask_b32 v130, v130, v129, vcc\n\t"
    "v_ffbl_b32 v131, v130\n\t"
    "v_xor_b32 v132, v131, v127\n\t"
    "s_waitcnt lgkmcnt(11)\n\t"
    "v_and_b32 v121, v121, v113\n\t"
    "v_perm_b32 v121, v121, v121, s46\n\t"
    "ds_or_b32 v95, v121\n\t"
    "v_xor_b32 v133, 23, v132\n\t"
    "v_lshlrev_b32 v117, v132, v136\n\t"
    "v_and_b32 v132, 7, v132\n\t"
    "v_lshl_or_b32 v117, v136, v133, v117\n\t"
    "v_lshl_add_u32 v132, v132, 1, v128\n\t"
    "ds_or_rtn_b32 v125, v99, v117\n\t"
    "ds_write_b16 v132, v107\n\t"
    "s_waitcnt lgkmcnt(10)\n\t"
    "v_and_b32 v122, v122, v114\n\t"
    "v_perm_b32 v122, v122, v122, s46\n\t"
    "ds_or_b32 v96, v122\n\t"
    "ds_read_b128 v[72:75], v128\n\t"
    "s_waitcnt lgkmcnt(3)\n\t"
    "v_and_b32 v123, v123, v115\n\t"
    "v_perm_b32 v123, v123, v123, s46\n\t"
    "ds_or_b32 v97, v123\n\t"
    "v_and_b32 v124, v124, v116\n\t"
    "v_perm_b32 v124, v124, v124, s46\n\t"
    "ds_or_b32 v98, v124\n\t"
    "v_and_b32 v125, v125, v117\n\t"
    "v_perm_b32 v125, v125, v125, s46\n\t"
    "ds_or_b32 v99, v125\n\t"
    "s_waitcnt lgkmcnt(3)\n\t"
    "buffer_store_dwordx4 v[72:75], v66, %12, s49 offen offset:256 sc1\n\t"
    "v_mov_b32 v134, 1036\n\t"
    "ds_write_b32 v134, v136\n\t"
    "s_cmp_lg_u32 %14, 0\n\t"
    "s_cbranch_scc1 L_ahsend_%=\n\t"
    "s_waitcnt vmcnt(0)\n\t"
    "buffer_store_dwordx4 v[148:151], v152, %12, s47 offen offset:1024 sc1\n\t"
    "s_setprio 0\n\t"
    "v_and_b32 v132, 0x70, v152\n\t"
    "v_lshlrev_b32 v132, 3, v132\n\t"
    "s_mov_b32 s51, 1048576\n\t"
    "L_ahs2_%=:\n\t"
    "s_sleep 8\n\t"
    "buffer_load_dword v71, v132, %12, s48 offen offset:1024 sc1\n\t"
    "buffer_load_dwordx4 v[148:151], v152, %12, s47 offen sc1\n\t"
    "s_sub_u32 s51, s51, 1\n\t"
    "s_cmp_eq_u32 s51, 0\n\t"
    "s_cbranch_scc1 L_ahsend_%=\n\t"
    "s_waitcnt vmcnt(0)\n\t"
    "buffer_store_dwordx4 v[148:151], v152, %12, s47 offen offset:1024 sc1\n\t"
    "v_cmp_gt_u32 vcc, 32, v71\n\t"
    "s_cbranch_vccnz L_ahs2_%=\n\t"
    "L_ahsend_%=:\n\t"
    "s_setprio 0\n\t"
    "s_and_b32 s46, %16, 3\n\t"
    "s_mul_i32 s49, s46, 1040\n\t"
    "s_add_u32 s49, s49, 2064\n\t"
    "s_lshl_b32 s46, s46, 3\n\t"
    "s_lshr_b32 s47, 1076697610, s46\n\t"
    "s_and_b32 s47, s47, 0xff\n\t"
    "s_lshl_b32 s48, s47, 16\n\t"
    "s_lshr_b32 s47, 756681216, s46\n\t"
    "s_and_b32 s47, s47, 0xff\n\t"
    "s_add_u32 s48, s48, s47\n\t"
    "s_lshr_b32 s47, %16, 2\n\t"
    "s_add_u32 s47, s47, 0x200\n\t"
    "s_cmp_eq_u32 s46, 0\n\t"
    "s_cselect_b32 s47, 0x100, s47\n\t"
    "s_add_u32 s48, s48, s47\n\t"
    "v_bfe_u32 v154, v147, 2, 2\n\t"
    "v_and_b32 v155, 3, v147\n\t"
    "v_mul_u32_u24 v154, 25, v154\n\t"
    "v_mad_u32_u24 v154, v155, 6, v154\n\t"
    "v_lshlrev_b32 v153, 2, v154\n\t"
    "s_and_b32 s53, s48, 0xff\n\t"
    "s_bfe_u32 s55, s48, 0x80008\n\t"
    "s_add_u32 s54, s53, s55\n\t"
    "s_add_u32 s55, s54, s55\n\t"
    "s_lshr_b32 s64, s48, 16\n\t"
    "s_mul_i32 s66, s53, 400\n\t"
    "buffer_load_dwordx3 v[128:130], v153, %0, s66 offen\n\t"
    "buffer_load_dwordx4 v[132:135], v153, %0, s66 offen offset:12\n\t"
    "s_mul_i32 s66, s54, 400\n\t"
    "buffer_load_dwordx3 v[136:138], v153, %0, s66 offen\n\t"
    "buffer_load_dwordx4 v[140:143], v153, %0, s66 offen offset:12\n\t"
    "v_cmp_eq_u32 s[62:63], 0, v155\n\t"
    "v_and_b32 v151, 48, v147\n\t"
    "v_or_b32 v155, 0x100000, v151\n\t"
    "v_cndmask_b32 v152, v155, v151, s[62:63]\n\t"
    "v_lshlrev_b32 v117, 20, v147\n\t"
    "v_add_u32 v117, 1024, v117\n\t"
    "v_mov_b32 v118, 1\n\t"
    "v_lshlrev_b32 v95, 4, v147\n\t"
    "v_add_u32 v95, 140288, v95\n\t"
    "s_call_b64 s[98:99], L_setup_%=\n\t"
    "L_await_%=:\n\t"
    "L_asp9_%=:\n\t"
    "ds_read_b32 v118, v117 offset:8\n\t"
    "ds_read_b32 v119, v117 offset:4\n\t"
    "s_waitcnt lgkmcnt(0)\n\t"
    "v_readfirstlane_b32 s46, v119\n\t"
    "s_nop 0\n\t"
    "s_cmp_ge_u32 s46, 4\n\t"
    "s_cbranch_scc1 L_ago9_%=\n\t"
    "s_sleep 1\n\t"
    "s_branch L_asp9_%=\n\t"
    "L_ago9_%=:\n\t"
    "v_readfirstlane_b32 s47, v118\n\t"
    "ds_read_b128 v[0:3], v95 offset:0\n\t"
    "ds_read_b128 v[4:7], v95 offset:1024\n\t"
    "ds_read_b128 v[8:11], v95 offset:2048\n\t"
    "ds_read_b128 v[12:15], v95 offset:3072\n\t"
    "ds_read_b128 v[16:19], v95 offset:4096\n\t"
    "ds_read_b128 v[20:23], v95 offset:5120\n\t"
    "ds_read_b128 v[24:27], v95 offset:6144\n\t"
    "ds_read_b128 v[28:31], v95 offset:7168\n\t"
    "ds_read_b128 v[32:35], v95 offset:8192\n\t"
    "ds_read_b128 v[36:39], v95 offset:9216\n\t"
    "ds_read_b128 v[40:43], v95 offset:10240\n\t"
    "ds_read_b128 v[44:47], v95 offset:11264\n\t"
    "ds_read_b128 v[48:51], v95 offset:12288\n\t"
    "ds_read_b128 v[52:55], v95 offset:13312\n\t"
    "ds_read_b128 v[56:59], v95 offset:14336\n\t"
    "ds_read_b128 v[60:63], v95 offset:15360\n\t"
    "ds_read_b128 v[64:67], v95 offset:16384\n\t"
    "ds_read_b128 v[68:71], v95 offset:17408\n\t"
    "ds_read_b128 v[72:75], v95 offset:18432\n\t"
    "ds_read_b128 v[76:79], v95 offset:19456\n\t"
    "ds_read_b128 v[80:83], v95 offset:20480\n\t"
    "ds_read_b128 v[84:87], v95 offset:21504\n\t"
    "ds_read_b128 v[88:91], v95 offset:22528\n\t"
    "s_waitcnt vmcnt(0)\n\t"
    "s_cmp_lg_u32 %16, 1\n\t"
    "s_cbranch_scc1 L_anh3_%=\n\t"
    "v_mov_b32 v119, 4\n\t"
    "ds_write_b32 v117, v119 offset:12\n\t"
    "s_lshl_b32 s65, %14, 12\n\t"
    "s_add_u32 s65, s65, 16777216\n\t"
    "s_waitcnt lgkmcnt(15)\n\t"
    "buffer_store_dwordx4 v[0:3], v127, %12, s65 offen offset:0 sc1\n\t"
    "buffer_store_dwordx4 v[4:7], v127, %12, s65 offen offset:1024 sc1\n\t"
    "buffer_store_dwordx4 v[8:11], v127, %12, s65 offen offset:2048 sc1\n\t"
    "buffer_store_dwordx4 v[12:15], v127, %12, s65 offen offset:3072 sc1\n\t"
    "L_anh3_%=:\n\t"
    "s_cmp_ge_u32 s47, 6\n\t"
    "s_cbranch_scc1 L_adok_%=\n\t"
    "L_awd_%=:\n\t"
    "L_asp10_%=:\n\t"
    "ds_read_b32 v119, v117 offset:8\n\t"
    "s_waitcnt lgkmcnt(0)\n\t"
    "v_readfirstlane_b32 s46, v119\n\t"
    "s_nop 0\n\t"
    "s_cmp_ge_u32 s46, 6\n\t"
    "s_cbranch_scc1 L_ago10_%=\n\t"
    "s_sleep 1\n\t"
    "s_branch L_asp10_%=\n\t"
    "L_ago10_%=:\n\t"
    "L_adok_%=:\n\t"
    "L_amain_%=:\n\t"
    "s_mov_b32 s68, 130\n\t"
    "v_mad_u32_u24 v128, v128, s68, v152\n\t"
    "v_and_b32 v128, -16, v128\n\t"
    "ds_read_b128 v[156:159], v128 offset:0\n\t"
    "ds_read_b128 v[160:163], v128 offset:64\n\t"
    "v_mad_u32_u24 v129, v129, s68, v151\n\t"
    "v_and_b32 v129, -16, v129\n\t"
    "ds_read_b128 v[164:167], v129 offset:0\n\t"
    "ds_read_b128 v[168:171], v129 offset:64\n\t"
    "v_mad_u32_u24 v130, v130, s68, v151\n\t"
    "v_and_b32 v130, -16, v130\n\t"
    "ds_read_b128 v[172:175], v130 offset:0\n\t"
    "ds_read_b128 v[176:179], v130 offset:64\n\t"
    "s_mul_i32 s66, s55, 400\n\t"
    "buffer_load_dwordx3 v[128:130], v153, %0, s66 offen\n\t"
    "v_mad_u32_u24 v132, v132, s68, v151\n\t"
    "v_and_b32 v132, -16, v132\n\t"
    "ds_read_b128 v[180:183], v132 offset:0\n\t"
    "ds_read_b128 v[184:187], v132 offset:64\n\t"
    "v_mad_u32_u24 v133, v133, s68, v151\n\t"
    "v_and_b32 v133, -16, v133\n\t"
    "ds_read_b128 v[188:191], v133 offset:0\n\t"
    "ds_read_b128 v[192:195], v133 offset:64\n\t"
    "v_mad_u32_u24 v134, v134, s68, v151\n\t"
    "v_and_b32 v134, -16, v134\n\t"
    "ds_read_b128 v[196:199], v134 offset:0\n\t"
    "ds_read_b128 v[200:203], v134 offset:64\n\t"
    "v_mad_u32_u24 v135, v135, s68, v151\n\t"
    "v_and_b32 v135, -16, v135\n\t"
    "ds_read_b128 v[204:207], v135 offset:0\n\t"
    "ds_read_b128 v[208:211], v135 offset:64\n\t"
    "v_mov_b32 v150, 1\n\t"
    "v_lshlrev_b32 v149, 20, v147\n\t"
    "v_add_u32 v149, s49, v149\n\t"
    "v_lshlrev_b32 v146, 2, v147\n\t"
    "s_mov_b32 s52, s53\n\t"
    "s_lshl_b32 s50, %14, 14\n\t"
    "s_add_u32 s50, s50, 27262976\n\t"
    "L_apnn_%=:\n\t"
    "s_mul_i32 s69, s52, 256\n\t"
    "s_add_u32 s69, s69, s50\n\t"
    "s_waitcnt lgkmcnt(8)\n\t"
    "v_mfma_f32_16x16x32_bf16 v[92:95], v[156:159], v[156:159], 0\n\t"
    "v_fmac_f32 v145, v108, v72\n\t"
    "v_fmac_f32 v144, v109, v73\n\t"
    "ds_add_rtn_u32 v148, v149, v150\n\t"
    "v_mfma_f32_16x16x32_bf16 v[92:95], v[160:163], v[160:163], v[92:95]\n\t"
    "v_fmac_f32 v145, v110, v74\n\t"
    "v_fmac_f32 v144, v111, v75\n\t"
    "buffer_load_dwordx4 v[132:135], v153, %0, s66 offen offset:12\n\t"
    "v_mfma_f32_16x16x32_bf16 v[96:99], v[164:167], v[164:167], 0\n\t"
    "v_fmac_f32 v145, v112, v76\n\t"
    "v_fmac_f32 v144, v113, v77\n\t"
    "v_mfma_f32_16x16x32_bf16 v[96:99], v[168:171], v[168:171], v[96:99]\n\t"
    "v_fmac_f32 v145, v114, v78\n\t"
    "v_fmac_f32 v144, v115, v79\n\t"
    "v_mfma_f32_16x16x32_bf16 v[100:103], v[164:167], v[172:175], 0\n\t"
    "v_fmac_f32 v145, v116, v80\n\t"
    "v_fmac_f32 v144, v117, v81\n\t"
    "v_mfma_f32_16x16x32_bf16 v[100:103], v[168:171], v[176:179], v[100:103]\n\t"
    "v_fmac_f32 v145, v118, v82\n\t"
    "v_fmac_f32 v144, v119, v83\n\t"
    "s_waitcnt vmcnt(4)\n\t"
    "s_cmp_ge_u32 s55, s64\n\t"
    "s_cbranch_scc1 L_ahk00_%=\n\t"
    "L_ahb00_%=:\n\t"
    "v_mfma_f32_16x16x32_bf16 v[104:107], v[172:175], v[172:175], 0\n\t"
    "v_mad_u32_u24 v136, v136, s68, v152\n\t"
    "v_and_b32 v136, -16, v136\n\t"
    "v_mfma_f32_16x16x32_bf16 v[104:107], v[176:179], v[176:179], v[104:107]\n\t"
    "v_add_f32 v154, v144, v145\n\t"
    "buffer_store_dword v154, v146, %12, s69 offen sc1\n\t"
    "v_mul_f32 v144, v92, v84\n\t"
    "v_mfma_f32_16x16x32_bf16 v[108:111], v[156:159], v[164:167], 0\n\t"
    "v_mul_f32 v145, v96, v0\n\t"
    "v_fmac_f32 v144, v97, v1\n\t"
    "v_mfma_f32_16x16x32_bf16 v[108:111], v[160:163], v[168:171], v[108:111]\n\t"
    "v_fmac_f32 v145, v98, v2\n\t"
    "v_fmac_f32 v144, v99, v3\n\t"
    "s_waitcnt lgkmcnt(7)\n\t"
    "v_mfma_f32_16x16x32_bf16 v[112:115], v[164:167], v[180:183], 0\n\t"
    "v_fmac_f32 v145, v100, v4\n\t"
    "v_fmac_f32 v144, v101, v5\n\t"
    "v_mfma_f32_16x16x32_bf16 v[112:115], v[168:171], v[184:187], v[112:115]\n\t"
    "v_fmac_f32 v145, v102, v6\n\t"
    "v_fmac_f32 v144, v103, v7\n\t"
    "v_mfma_f32_16x16x32_bf16 v[116:119], v[172:175], v[180:183], 0\n\t"
    "v_fmac_f32 v145, v104, v8\n\t"
    "v_fmac_f32 v144, v105, v9\n\t"
    "v_mfma_f32_16x16x32_bf16 v[116:119], v[176:179], v[184:187], v[116:119]\n\t"
    "v_fmac_f32 v145, v106, v10\n\t"
    "v_fmac_f32 v144, v107, v11\n\t"
    "ds_read_b128 v[212:215], v136 offset:0\n\t"
    "ds_read_b128 v[216:219], v136 offset:64\n\t"
    "v_mfma_f32_16x16x32_bf16 v[92:95], v[180:183], v[180:183], 0\n\t"
    "v_mad_u32_u24 v137, v137, s68, v151\n\t"
    "v_and_b32 v137, -16, v137\n\t"
    "v_mfma_f32_16x16x32_bf16 v[92:95], v[184:187], v[184:187], v[92:95]\n\t"
    "v_fmac_f32 v145, v108, v85\n\t"
    "v_mad_u32_u24 v138, v138, s68, v151\n\t"
    "v_mfma_f32_16x16x32_bf16 v[96:99], v[156:159], v[172:175], 0\n\t"
    "v_fmac_f32 v144, v112, v12\n\t"
    "v_fmac_f32 v145, v113, v13\n\t"
    "v_mfma_f32_16x16x32_bf16 v[96:99], v[160:163], v[176:179], v[96:99]\n\t"
    "v_fmac_f32 v144, v114, v14\n\t"
    "v_fmac_f32 v145, v115, v15\n\t"
    "s_waitcnt lgkmcnt(7)\n\t"
    "v_mfma_f32_16x16x32_bf16 v[100:103], v[164:167], v[188:191], 0\n\t"
    "v_and_b32 v138, -16, v138\n\t"
    "v_fmac_f32 v144, v116, v16\n\t"
    "v_mfma_f32_16x16x32_bf16 v[100:103], v[168:171], v[192:195], v[100:103]\n\t"
    "v_fmac_f32 v145, v117, v17\n\t"
    "v_fmac_f32 v144, v118, v18\n\t"
    "v_mfma_f32_16x16x32_bf16 v[104:107], v[172:175], v[188:191], 0\n\t"
    "v_fmac_f32 v145, v119, v19\n\t"
    "v_fmac_f32 v144, v92, v20\n\t"
    "v_mfma_f32_16x16x32_bf16 v[104:107], v[176:179], v[192:195], v[104:107]\n\t"
    "v_fmac_f32 v145, v93, v21\n\t"
    "v_fmac_f32 v144, v94, v22\n\t"
    "s_waitcnt lgkmcnt(5)\n\t"
    "v_mfma_f32_16x16x32_bf16 v[108:111], v[164:167], v[196:199], 0\n\t"
    "v_fmac_f32 v145, v95, v23\n\t"
    "v_fmac_f32 v144, v96, v86\n\t"
    "v_mfma_f32_16x16x32_bf16 v[108:111], v[168:171], v[200:203], v[108:111]\n\t"
    "v_mfma_f32_16x16x32_bf16 v[112:115], v[156:159], v[180:183], 0\n\t"
    "v_fmac_f32 v145, v100, v24\n\t"
    "v_fmac_f32 v144, v101, v25\n\t"
    "v_mfma_f32_16x16x32_bf16 v[112:115], v[160:163], v[184:187], v[112:115]\n\t"
    "v_fmac_f32 v145, v102, v26\n\t"
    "v_fmac_f32 v144, v103, v27\n\t"
    "s_waitcnt lgkmcnt(2)\n\t"
    "v_mfma_f32_16x16x32_bf16 v[116:119], v[164:167], v[204:207], 0\n\t"
    "v_readfirstlane_b32 s60, v148\n\t"
    "v_fmac_f32 v145, v104, v28\n\t"
    "v_mfma_f32_16x16x32_bf16 v[116:119], v[168:171], v[208:211], v[116:119]\n\t"
    "v_fmac_f32 v144, v105, v29\n\t"
    "v_fmac_f32 v145, v106, v30\n\t"
    "ds_read_b128 v[164:167], v137 offset:0\n\t"
    "ds_read_b128 v[168:171], v137 offset:64\n\t"
    "v_mfma_f32_16x16x32_bf16 v[92:95], v[172:175], v[196:199], 0\n\t"
    "v_fmac_f32 v144, v107, v31\n\t"
    "v_fmac_f32 v145, v108, v32\n\t"
    "v_mfma_f32_16x16x32_bf16 v[92:95], v[176:179], v[200:203], v[92:95]\n\t"
    "v_fmac_f32 v144, v109, v33\n\t"
    "v_fmac_f32 v145, v110, v34\n\t"
    "s_waitcnt vmcnt(4)\n\t"
    "s_cmp_ge_u32 s55, s64\n\t"
    "s_cbranch_scc1 L_ahk10_%=\n\t"
    "L_ahb10_%=:\n\t"
    "v_mfma_f32_16x16x32_bf16 v[96:99], v[172:175], v[204:207], 0\n\t"
    "v_mad_u32_u24 v140, v140, s68, v151\n\t"
    "v_and_b32 v140, -16, v140\n\t"
    "v_mfma_f32_16x16x32_bf16 v[96:99], v[176:179], v[208:211], v[96:99]\n\t"
    "v_fmac_f32 v144, v111, v35\n\t"
    "v_fmac_f32 v145, v112, v87\n\t"
    "ds_read_b128 v[172:175], v138 offset:0\n\t"
    "ds_read_b128 v[176:179], v138 offset:64\n\t"
    "s_mul_i32 s66, s60, 400\n\t"
    "v_mfma_f32_16x16x32_bf16 v[100:103], v[156:159], v[188:191], 0\n\t"
    "v_fmac_f32 v144, v116, v36\n\t"
    "v_fmac_f32 v145, v117, v37\n\t"
    "v_mfma_f32_16x16x32_bf16 v[100:103], v[160:163], v[192:195], v[100:103]\n\t"
    "v_fmac_f32 v144, v118, v38\n\t"
    "v_fmac_f32 v145, v119, v39\n\t"
    "buffer_load_dwordx3 v[136:138], v153, %0, s66 offen\n\t"
    "v_mfma_f32_16x16x32_bf16 v[104:107], v[180:183], v[188:191], 0\n\t"
    "v_mad_u32_u24 v141, v141, s68, v151\n\t"
    "v_and_b32 v141, -16, v141\n\t"
    "v_mfma_f32_16x16x32_bf16 v[104:107], v[184:187], v[192:195], v[104:107]\n\t"
    "v_fmac_f32 v144, v92, v40\n\t"
    "v_fmac_f32 v145, v93, v41\n\t"
    "v_mfma_f32_16x16x32_bf16 v[108:111], v[180:183], v[196:199], 0\n\t"
    "v_fmac_f32 v144, v94, v42\n\t"
    "v_fmac_f32 v145, v95, v43\n\t"
    "v_mfma_f32_16x16x32_bf16 v[108:111], v[184:187], v[200:203], v[108:111]\n\t"
    "v_fmac_f32 v144, v96, v44\n\t"
    "v_fmac_f32 v145, v97, v45\n\t"
    "v_mfma_f32_16x16x32_bf16 v[112:115], v[180:183], v[204:207], 0\n\t"
    "v_fmac_f32 v144, v98, v46\n\t"
    "v_fmac_f32 v145, v99, v47\n\t"
    "v_mfma_f32_16x16x32_bf16 v[112:115], v[184:187], v[208:211], v[112:115]\n\t"
    "v_fmac_f32 v144, v100, v88\n\t"
    "v_mad_u32_u24 v142, v142, s68, v151\n\t"
    "ds_read_b128 v[180:183], v140 offset:0\n\t"
    "ds_read_b128 v[184:187], v140 offset:64\n\t"
    "v_mfma_f32_16x16x32_bf16 v[116:119], v[156:159], v[196:199], 0\n\t"
    "v_and_b32 v142, -16, v142\n\t"
    "v_fmac_f32 v145, v104, v48\n\t"
    "v_mfma_f32_16x16x32_bf16 v[116:119], v[160:163], v[200:203], v[116:119]\n\t"
    "v_fmac_f32 v144, v105, v49\n\t"
    "v_fmac_f32 v145, v106, v50\n\t"
    "v_mfma_f32_16x16x32_bf16 v[92:95], v[188:191], v[188:191], 0\n\t"
    "v_fmac_f32 v144, v107, v51\n\t"
    "v_mad_u32_u24 v143, v143, s68, v151\n\t"
    "v_mfma_f32_16x16x32_bf16 v[92:95], v[192:195], v[192:195], v[92:95]\n\t"
    "v_and_b32 v143, -16, v143\n\t"
    "v_fmac_f32 v145, v108, v52\n\t"
    "v_mfma_f32_16x16x32_bf16 v[96:99], v[188:191], v[196:199], 0\n\t"
    "v_fmac_f32 v144, v109, v53\n\t"
    "v_fmac_f32 v145, v110, v54\n\t"
    "v_mfma_f32_16x16x32_bf16 v[96:99], v[192:195], v[200:203], v[96:99]\n\t"
    "v_fmac_f32 v144, v111, v55\n\t"
    "v_fmac_f32 v145, v112, v56\n\t"
    "v_mfma_f32_16x16x32_bf16 v[100:103], v[188:191], v[204:207], 0\n\t"
    "v_fmac_f32 v144, v113, v57\n\t"
    "v_fmac_f32 v145, v114, v58\n\t"
    "v_mfma_f32_16x16x32_bf16 v[100:103], v[192:195], v[208:211], v[100:103]\n\t"
    "v_fmac_f32 v144, v115, v59\n\t"
    "v_fmac_f32 v145, v116, v89\n\t"
    "ds_read_b128 v[188:191], v141 offset:0\n\t"
    "ds_read_b128 v[192:195], v141 offset:64\n\t"
    "v_mfma_f32_16x16x32_bf16 v[104:107], v[156:159], v[204:207], 0\n\t"
    "v_fmac_f32 v144, v92, v60\n\t"
    "v_fmac_f32 v145, v93, v61\n\t"
    "v_mfma_f32_16x16x32_bf16 v[104:107], v[160:163], v[208:211], v[104:107]\n\t"
    "v_fmac_f32 v144, v94, v62\n\t"
    "v_fmac_f32 v145, v95, v63\n\t"
    "v_mfma_f32_16x16x32_bf16 v[108:111], v[196:199], v[196:199], 0\n\t"
    "v_fmac_f32 v144, v96, v64\n\t"
    "v_fmac_f32 v145, v97, v65\n\t"
    "v_mfma_f32_16x16x32_bf16 v[108:111], v[200:203], v[200:203], v[108:111]\n\t"
    "v_fmac_f32 v144, v98, v66\n\t"
    "v_fmac_f32 v145, v99, v67\n\t"
    "v_mfma_f32_16x16x32_bf16 v[112:115], v[196:199], v[204:207], 0\n\t"
    "v_fmac_f32 v144, v100, v68\n\t"
    "v_fmac_f32 v145, v101, v69\n\t"
    "v_mfma_f32_16x16x32_bf16 v[112:115], v[200:203], v[208:211], v[112:115]\n\t"
    "v_fmac_f32 v144, v102, v70\n\t"
    "v_fmac_f32 v145, v103, v71\n\t"
    "ds_read_b128 v[196:199], v142 offset:0\n\t"
    "ds_read_b128 v[200:203], v142 offset:64\n\t"
    "v_mfma_f32_16x16x32_bf16 v[116:119], v[204:207], v[204:207], 0\n\t"
    "v_fmac_f32 v144, v104, v90\n\t"
    "v_mfma_f32_16x16x32_bf16 v[116:119], v[208:211], v[208:211], v[116:119]\n\t"
    "ds_read_b128 v[204:207], v143 offset:0\n\t"
    "ds_read_b128 v[208:211], v143 offset:64\n\t"
    "s_mov_b32 s52, s53\n\t"
    "s_mov_b32 s53, s54\n\t"
    "s_mov_b32 s54, s55\n\t"
    "s_mov_b32 s55, s60\n\t"
    "s_cmp_lt_u32 s53, s64\n\t"
    "s_cbranch_scc0 L_adone_%=\n\t"
    "s_mul_i32 s69, s52, 256\n\t"
    "s_add_u32 s69, s69, s50\n\t"
    "s_waitcnt lgkmcnt(8)\n\t"
    "v_mfma_f32_16x16x32_bf16 v[92:95], v[212:215], v[212:215], 0\n\t"
    "v_fmac_f32 v145, v108, v72\n\t"
    "v_fmac_f32 v144, v109, v73\n\t"
    "ds_add_rtn_u32 v148, v149, v150\n\t"
    "v_mfma_f32_16x16x32_bf16 v[92:95], v[216:219], v[216:219], v[92:95]\n\t"
    "v_fmac_f32 v145, v110, v74\n\t"
    "v_fmac_f32 v144, v111, v75\n\t"
    "buffer_load_dwordx4 v[140:143], v153, %0, s66 offen offset:12\n\t"
    "v_mfma_f32_16x16x32_bf16 v[96:99], v[164:167], v[164:167], 0\n\t"
    "v_fmac_f32 v145, v112, v76\n\t"
    "v_fmac_f32 v144, v113, v77\n\t"
    "v_mfma_f32_16x16x32_bf16 v[96:99], v[168:171], v[168:171], v[96:99]\n\t"
    "v_fmac_f32 v145, v114, v78\n\t"
    "v_fmac_f32 v144, v115, v79\n\t"
    "v_mfma_f32_16x16x32_bf16 v[100:103], v[164:167], v[172:175], 0\n\t"
    "v_fmac_f32 v145, v116, v80\n\t"
    "v_fmac_f32 v144, v117, v81\n\t"
    "v_mfma_f32_16x16x32_bf16 v[100:103], v[168:171], v[176:179], v[100:103]\n\t"
    "v_fmac_f32 v145, v118, v82\n\t"
    "v_fmac_f32 v144, v119, v83\n\t"
    "s_waitcnt vmcnt(4)\n\t"
    "s_cmp_ge_u32 s55, s64\n\t"
    "s_cbranch_scc1 L_ahk01_%=\n\t"
    "L_ahb01_%=:\n\t"
    "v_mfma_f32_16x16x32_bf16 v[104:107], v[172:175], v[172:175], 0\n\t"
    "v_mad_u32_u24 v128, v128, s68, v152\n\t"
    "v_and_b32 v128, -16, v128\n\t"
    "v_mfma_f32_16x16x32_bf16 v[104:107], v[176:179], v[176:179], v[104:107]\n\t"
    "v_add_f32 v154, v144, v145\n\t"
    "buffer_store_dword v154, v146, %12, s69 offen sc1\n\t"
    "v_mul_f32 v144, v92, v84\n\t"
    "v_mfma_f32_16x16x32_bf16 v[108:111], v[212:215], v[164:167], 0\n\t"
    "v_mul_f32 v145, v96, v0\n\t"
    "v_fmac_f32 v144, v97, v1\n\t"
    "v_mfma_f32_16x16x32_bf16 v[108:111], v[216:219], v[168:171], v[108:111]\n\t"
    "v_fmac_f32 v145, v98, v2\n\t"
    "v_fmac_f32 v144, v99, v3\n\t"
    "s_waitcnt lgkmcnt(7)\n\t"
    "v_mfma_f32_16x16x32_bf16 v[112:115], v[164:167], v[180:183], 0\n\t"
    "v_fmac_f32 v145, v100, v4\n\t"
    "v_fmac_f32 v144, v101, v5\n\t"
    "v_mfma_f32_16x16x32_bf16 v[112:115], v[168:171], v[184:187], v[112:115]\n\t"
    "v_fmac_f32 v145, v102, v6\n\t"
    "v_fmac_f32 v144, v103, v7\n\t"
    "v_mfma_f32_16x16x32_bf16 v[116:119], v[172:175], v[180:183], 0\n\t"
    "v_fmac_f32 v145, v104, v8\n\t"
    "v_fmac_f32 v144, v105, v9\n\t"
    "v_mfma_f32_16x16x32_bf16 v[116:119], v[176:179], v[184:187], v[116:119]\n\t"
    "v_fmac_f32 v145, v106, v10\n\t"
    "v_fmac_f32 v144, v107, v11\n\t"
    "ds_read_b128 v[156:159], v128 offset:0\n\t"
    "ds_read_b128 v[160:163], v128 offset:64\n\t"
    "v_mfma_f32_16x16x32_bf16 v[92:95], v[180:183], v[180:183], 0\n\t"
    "v_mad_u32_u24 v129, v129, s68, v151\n\t"
    "v_and_b32 v129, -16, v129\n\t"
    "v_mfma_f32_16x16x32_bf16 v[92:95], v[184:187], v[184:187], v[92:95]\n\t"
    "v_fmac_f32 v145, v108, v85\n\t"
    "v_mad_u32_u24 v130, v130, s68, v151\n\t"
    "v_mfma_f32_16x16x32_bf16 v[96:99], v[212:215], v[172:175], 0\n\t"
    "v_fmac_f32 v144, v112, v12\n\t"
    "v_fmac_f32 v145, v113, v13\n\t"
    "v_mfma_f32_16x16x32_bf16 v[96:99], v[216:219], v[176:179], v[96:99]\n\t"
    "v_fmac_f32 v144, v114, v14\n\t"
    "v_fmac_f32 v145, v115, v15\n\t"
    "s_waitcnt lgkmcnt(7)\n\t"
    "v_mfma_f32_16x16x32_bf16 v[100:103], v[164:167], v[188:191], 0\n\t"
    "v_and_b32 v130, -16, v130\n\t"
    "v_fmac_f32 v144, v116, v16\n\t"
    "v_mfma_f32_16x16x32_bf16 v[100:103], v[168:171], v[192:195], v[100:103]\n\t"
    "v_fmac_f32 v145, v117, v17\n\t"
    "v_fmac_f32 v144, v118, v18\n\t"
    "v_mfma_f32_16x16x32_bf16 v[104:107], v[172:175], v[188:191], 0\n\t"
    "v_fmac_f32 v145, v119, v19\n\t"
    "v_fmac_f32 v144, v92, v20\n\t"
    "v_mfma_f32_16x16x32_bf16 v[104:107], v[176:179], v[192:195], v[104:107]\n\t"
    "v_fmac_f32 v145, v93, v21\n\t"
    "v_fmac_f32 v144, v94, v22\n\t"
    "s_waitcnt lgkmcnt(5)\n\t"
    "v_mfma_f32_16x16x32_bf16 v[108:111], v[164:167], v[196:199], 0\n\t"
    "v_fmac_f32 v145, v95, v23\n\t"
    "v_fmac_f32 v144, v96, v86\n\t"
    "v_mfma_f32_16x16x32_bf16 v[108:111], v[168:171], v[200:203], v[108:111]\n\t"
    "v_mfma_f32_16x16x32_bf16 v[112:115], v[212:215], v[180:183], 0\n\t"
    "v_fmac_f32 v145, v100, v24\n\t"
    "v_fmac_f32 v144, v101, v25\n\t"
    "v_mfma_f32_16x16x32_bf16 v[112:115], v[216:219], v[184:187], v[112:115]\n\t"
    "v_fmac_f32 v145, v102, v26\n\t"
    "v_fmac_f32 v144, v103, v27\n\t"
    "s_waitcnt lgkmcnt(2)\n\t"
    "v_mfma_f32_16x16x32_bf16 v[116:119], v[164:167], v[204:207], 0\n\t"
    "v_readfirstlane_b32 s60, v148\n\t"
    "v_fmac_f32 v145, v104, v28\n\t"
    "v_mfma_f32_16x16x32_bf16 v[116:119], v[168:171], v[208:211], v[116:119]\n\t"
    "v_fmac_f32 v144, v105, v29\n\t"
    "v_fmac_f32 v145, v106, v30\n\t"
    "ds_read_b128 v[164:167], v129 offset:0\n\t"
    "ds_read_b128 v[168:171], v129 offset:64\n\t"
    "v_mfma_f32_16x16x32_bf16 v[92:95], v[172:175], v[196:199], 0\n\t"
    "v_fmac_f32 v144, v107, v31\n\t"
    "v_fmac_f32 v145, v108, v32\n\t"
    "v_mfma_f32_16x16x32_bf16 v[92:95], v[176:179], v[200:203], v[92:95]\n\t"
    "v_fmac_f32 v144, v109, v33\n\t"
    "v_fmac_f32 v145, v110, v34\n\t"
    "s_waitcnt vmcnt(4)\n\t"
    "s_cmp_ge_u32 s55, s64\n\t"
    "s_cbranch_scc1 L_ahk11_%=\n\t"
    "L_ahb11_%=:\n\t"
    "v_mfma_f32_16x16x32_bf16 v[96:99], v[172:175], v[204:207], 0\n\t"
    "v_mad_u32_u24 v132, v132, s68, v151\n\t"
    "v_and_b32 v132, -16, v132\n\t"
    "v_mfma_f32_16x16x32_bf16 v[96:99], v[176:179], v[208:211], v[96:99]\n\t"
    "v_fmac_f32 v144, v111, v35\n\t"
    "v_fmac_f32 v145, v112, v87\n\t"
    "ds_read_b128 v[172:175], v130 offset:0\n\t"
    "ds_read_b128 v[176:179], v130 offset:64\n\t"
    "s_mul_i32 s66, s60, 400\n\t"
    "v_mfma_f32_16x16x32_bf16 v[100:103], v[212:215], v[188:191], 0\n\t"
    "v_fmac_f32 v144, v116, v36\n\t"
    "v_fmac_f32 v145, v117, v37\n\t"
    "v_mfma_f32_16x16x32_bf16 v[100:103], v[216:219], v[192:195], v[100:103]\n\t"
    "v_fmac_f32 v144, v118, v38\n\t"
    "v_fmac_f32 v145, v119, v39\n\t"
    "buffer_load_dwordx3 v[128:130], v153, %0, s66 offen\n\t"
    "v_mfma_f32_16x16x32_bf16 v[104:107], v[180:183], v[188:191], 0\n\t"
    "v_mad_u32_u24 v133, v133, s68, v151\n\t"
    "v_and_b32 v133, -16, v133\n\t"
    "v_mfma_f32_16x16x32_bf16 v[104:107], v[184:187], v[192:195], v[104:107]\n\t"
    "v_fmac_f32 v144, v92, v40\n\t"
    "v_fmac_f32 v145, v93, v41\n\t"
    "v_mfma_f32_16x16x32_bf16 v[108:111], v[180:183], v[196:199], 0\n\t"
    "v_fmac_f32 v144, v94, v42\n\t"
    "v_fmac_f32 v145, v95, v43\n\t"
    "v_mfma_f32_16x16x32_bf16 v[108:111], v[184:187], v[200:203], v[108:111]\n\t"
    "v_fmac_f32 v144, v96, v44\n\t"
    "v_fmac_f32 v145, v97, v45\n\t"
    "v_mfma_f32_16x16x32_bf16 v[112:115], v[180:183], v[204:207], 0\n\t"
    "v_fmac_f32 v144, v98, v46\n\t"
    "v_fmac_f32 v145, v99, v47\n\t"
    "v_mfma_f32_16x16x32_bf16 v[112:115], v[184:187], v[208:211], v[112:115]\n\t"
    "v_fmac_f32 v144, v100, v88\n\t"
    "v_mad_u32_u24 v134, v134, s68, v151\n\t"
    "ds_read_b128 v[180:183], v132 offset:0\n\t"
    "ds_read_b128 v[184:187], v132 offset:64\n\t"
    "v_mfma_f32_16x16x32_bf16 v[116:119], v[212:215], v[196:199], 0\n\t"
    "v_and_b32 v134, -16, v134\n\t"
    "v_fmac_f32 v145, v104, v48\n\t"
    "v_mfma_f32_16x16x32_bf16 v[116:119], v[216:219], v[200:203], v[116:119]\n\t"
    "v_fmac_f32 v144, v105, v49\n\t"
    "v_fmac_f32 v145, v106, v50\n\t"
    "v_mfma_f32_16x16x32_bf16 v[92:95], v[188:191], v[188:191], 0\n\t"
    "v_fmac_f32 v144, v107, v51\n\t"
    "v_mad_u32_u24 v135, v135, s68, v151\n\t"
    "v_mfma_f32_16x16x32_bf16 v[92:95], v[192:195], v[192:195], v[92:95]\n\t"
    "v_and_b32 v135, -16, v135\n\t"
    "v_fmac_f32 v145, v108, v52\n\t"
    "v_mfma_f32_16x16x32_bf16 v[96:99], v[188:191], v[196:199], 0\n\t"
    "v_fmac_f32 v144, v109, v53\n\t"
    "v_fmac_f32 v145, v110, v54\n\t"
    "v_mfma_f32_16x16x32_bf16 v[96:99], v[192:195], v[200:203], v[96:99]\n\t"
    "v_fmac_f32 v144, v111, v55\n\t"
    "v_fmac_f32 v145, v112, v56\n\t"
    "v_mfma_f32_16x16x32_bf16 v[100:103], v[188:191], v[204:207], 0\n\t"
    "v_fmac_f32 v144, v113, v57\n\t"
    "v_fmac_f32 v145, v114, v58\n\t"
    "v_mfma_f32_16x16x32_bf16 v[100:103], v[192:195], v[208:211], v[100:103]\n\t"
    "v_fmac_f32 v144, v115, v59\n\t"
    "v_fmac_f32 v145, v116, v89\n\t"
    "ds_read_b128 v[188:191], v133 offset:0\n\t"
    "ds_read_b128 v[192:195], v133 offset:64\n\t"
    "v_mfma_f32_16x16x32_bf16 v[104:107], v[212:215], v[204:207], 0\n\t"
    "v_fmac_f32 v144, v92, v60\n\t"
    "v_fmac_f32 v145, v93, v61\n\t"
    "v_mfma_f32_16x16x32_bf16 v[104:107], v[216:219], v[208:211], v[104:107]\n\t"
    "v_fmac_f32 v144, v94, v62\n\t"
    "v_fmac_f32 v145, v95, v63\n\t"
    "v_mfma_f32_16x16x32_bf16 v[108:111], v[196:199], v[196:199], 0\n\t"
    "v_fmac_f32 v144, v96, v64\n\t"
    "v_fmac_f32 v145, v97, v65\n\t"
    "v_mfma_f32_16x16x32_bf16 v[108:111], v[200:203], v[200:203], v[108:111]\n\t"
    "v_fmac_f32 v144, v98, v66\n\t"
    "v_fmac_f32 v145, v99, v67\n\t"
    "v_mfma_f32_16x16x32_bf16 v[112:115], v[196:199], v[204:207], 0\n\t"
    "v_fmac_f32 v144, v100, v68\n\t"
    "v_fmac_f32 v145, v101, v69\n\t"
    "v_mfma_f32_16x16x32_bf16 v[112:115], v[200:203], v[208:211], v[112:115]\n\t"
    "v_fmac_f32 v144, v102, v70\n\t"
    "v_fmac_f32 v145, v103, v71\n\t"
    "ds_read_b128 v[196:199], v134 offset:0\n\t"
    "ds_read_b128 v[200:203], v134 offset:64\n\t"
    "v_mfma_f32_16x16x32_bf16 v[116:119], v[204:207], v[204:207], 0\n\t"
    "v_fmac_f32 v144, v104, v90\n\t"
    "v_mfma_f32_16x16x32_bf16 v[116:119], v[208:211], v[208:211], v[116:119]\n\t"
    "ds_read_b128 v[204:207], v135 offset:0\n\t"
    "ds_read_b128 v[208:211], v135 offset:64\n\t"
    "s_mov_b32 s52, s53\n\t"
    "s_mov_b32 s53, s54\n\t"
    "s_mov_b32 s54, s55\n\t"
    "s_mov_b32 s55, s60\n\t"
    "s_cmp_lt_u32 s53, s64\n\t"
    "s_cbranch_scc1 L_apnn_%=\n\t"
    "L_adone_%=:\n\t"
    "s_mul_i32 s69, s52, 256\n\t"
    "s_add_u32 s69, s69, s50\n\t"
    "v_fmac_f32 v145, v108, v72\n\t"
    "v_fmac_f32 v144, v109, v73\n\t"
    "v_fmac_f32 v145, v110, v74\n\t"
    "v_fmac_f32 v144, v111, v75\n\t"
    "v_fmac_f32 v145, v112, v76\n\t"
    "v_fmac_f32 v144, v113, v77\n\t"
    "v_fmac_f32 v145, v114, v78\n\t"
    "v_fmac_f32 v144, v115, v79\n\t"
    "v_fmac_f32 v145, v116, v80\n\t"
    "v_fmac_f32 v144, v117, v81\n\t"
    "v_fmac_f32 v145, v118, v82\n\t"
    "v_fmac_f32 v144, v119, v83\n\t"
    "v_add_f32 v40, v144, v145\n\t"
    "L_aend_%=:\n\t"
    "s_branch L_aend2_%=\n\t"
    "L_ahk00_%=:\n\t"
    "v_mov_b32 v147, 3112\n\t"
    "s_cmp_lt_u32 s54, s64\n\t"
    "ds_read_b32 v147, v147\n\t"
    "s_cbranch_scc1 L_ahb00_%=\n\t"
    "s_waitcnt vmcnt(3)\n\t"
    "v_add_u32 v137, 0, v155\n\t"
    "v_add_u32 v138, 16, v155\n\t"
    "v_add_u32 v140, 32, v155\n\t"
    "v_add_u32 v141, 48, v155\n\t"
    "v_add_u32 v142, 64, v155\n\t"
    "v_add_u32 v143, 80, v155\n\t"
    "s_branch L_ahb00_%=\n\t"
    "L_ahk10_%=:\n\t"
    "s_waitcnt lgkmcnt(4)\n\t"
    "v_readfirstlane_b32 s65, v147\n\t"
    "s_nop 0\n\t"
    "s_cmp_eq_u32 s65, 0\n\t"
    "s_cbranch_scc1 L_ahb10_%=\n\t"
    "s_cmp_lt_u32 s62, 2\n\t"
    "s_cbranch_scc1 L_mel1_%=\n\t"
    "buffer_load_dwordx4 v[220:223], v127, %12, s61 offen offset:2048 sc1\n\t"
    "buffer_load_dwordx4 v[224:227], v127, %12, s61 offen offset:3072 sc1\n\t"
    "s_add_u32 s61, s61, 0x1000\n\t"
    "buffer_load_dwordx4 v[228:231], v127, %12, s61 offen offset:2048 sc1\n\t"
    "buffer_load_dwordx4 v[232:235], v127, %12, s61 offen offset:3072 sc1\n\t"
    "s_add_u32 s61, s61, 0x1000\n\t"
    "buffer_load_dwordx4 v[236:239], v127, %12, s61 offen offset:2048 sc1\n\t"
    "buffer_load_dwordx4 v[240:243], v127, %12, s61 offen offset:3072 sc1\n\t"
    "s_add_u32 s61, s61, 0x1000\n\t"
    "buffer_load_dwordx4 v[244:247], v127, %12, s61 offen offset:2048 sc1\n\t"
    "buffer_load_dwordx4 v[248:251], v127, %12, s61 offen offset:3072 sc1\n\t"
    "s_add_u32 s61, s61, 0x1000\n\t"
    "buffer_load_dwordx4 v[252:255], v127, %12, s61 offen offset:2048 sc1\n\t"
    "s_mov_b32 s62, 1\n\t"
    "L_mel1_%=:\n\t"
    "s_cmp_lt_u32 s54, s64\n\t"
    "s_cbranch_scc1 L_ahb10_%=\n\t"
    "buffer_load_dwordx4 v[0:3], v127, %12, s63 offen offset:0 sc1\n\t"
    "buffer_load_dwordx4 v[4:7], v127, %12, s63 offen offset:1024 sc1\n\t"
    "buffer_load_dwordx4 v[8:11], v127, %12, s63 offen offset:2048 sc1\n\t"
    "buffer_load_dwordx4 v[12:15], v127, %12, s63 offen offset:3072 sc1\n\t"
    "s_add_u32 s63, s63, 0x1000\n\t"
    "s_mov_b32 s62, 0\n\t"
    "s_branch L_ahb10_%=\n\t"
    "L_ahk01_%=:\n\t"
    "v_mov_b32 v147, 3112\n\t"
    "s_cmp_lt_u32 s54, s64\n\t"
    "ds_read_b32 v147, v147\n\t"
    "s_cbranch_scc1 L_ahb01_%=\n\t"
    "s_waitcnt vmcnt(3)\n\t"
    "v_add_u32 v129, 0, v155\n\t"
    "v_add_u32 v130, 16, v155\n\t"
    "v_add_u32 v132, 32, v155\n\t"
    "v_add_u32 v133, 48, v155\n\t"
    "v_add_u32 v134, 64, v155\n\t"
    "v_add_u32 v135, 80, v155\n\t"
    "s_branch L_ahb01_%=\n\t"
    "L_ahk11_%=:\n\t"
    "s_waitcnt lgkmcnt(4)\n\t"
    "v_readfirstlane_b32 s65, v147\n\t"
    "s_nop 0\n\t"
    "s_cmp_eq_u32 s65, 0\n\t"
    "s_cbranch_scc1 L_ahb11_%=\n\t"
    "s_cmp_lt_u32 s62, 2\n\t"
    "s_cbranch_scc1 L_mel3_%=\n\t"
    "buffer_load_dwordx4 v[220:223], v127, %12, s61 offen offset:2048 sc1\n\t"
    "buffer_load_dwordx4 v[224:227], v127, %12, s61 offen offset:3072 sc1\n\t"
    "s_add_u32 s61, s61, 0x1000\n\t"
    "buffer_load_dwordx4 v[228:231], v127, %12, s61 offen offset:2048 sc1\n\t"
    "buffer_load_dwordx4 v[232:235], v127, %12, s61 offen offset:3072 sc1\n\t"
    "s_add_u32 s61, s61, 0x1000\n\t"
    "buffer_load_dwordx4 v[236:239], v127, %12, s61 offen offset:2048 sc1\n\t"
    "buffer_load_dwordx4 v[240:243], v127, %12, s61 offen offset:3072 sc1\n\t"
    "s_add_u32 s61, s61, 0x1000\n\t"
    "buffer_load_dwordx4 v[244:247], v127, %12, s61 offen offset:2048 sc1\n\t"
    "buffer_load_dwordx4 v[248:251], v127, %12, s61 offen offset:3072 sc1\n\t"
    "s_add_u32 s61, s61, 0x1000\n\t"
    "buffer_load_dwordx4 v[252:255], v127, %12, s61 offen offset:2048 sc1\n\t"
    "s_mov_b32 s62, 1\n\t"
    "L_mel3_%=:\n\t"
    "s_cmp_lt_u32 s54, s64\n\t"
    "s_cbranch_scc1 L_ahb11_%=\n\t"
    "buffer_load_dwordx4 v[0:3], v127, %12, s63 offen offset:0 sc1\n\t"
    "buffer_load_dwordx4 v[4:7], v127, %12, s63 offen offset:1024 sc1\n\t"
    "buffer_load_dwordx4 v[8:11], v127, %12, s63 offen offset:2048 sc1\n\t"
    "buffer_load_dwordx4 v[12:15], v127, %12, s63 offen offset:3072 sc1\n\t"
    "s_add_u32 s63, s63, 0x1000\n\t"
    "s_mov_b32 s62, 0\n\t"
    "s_branch L_ahb11_%=\n\t"
    "L_aend2_%=:\n\t"
    "s_setprio 0\n\t"
    "v_lshlrev_b32 v113, 16, v127\n\t"
    "v_add_u32 v113, 3108, v113\n\t"
    "s_cmp_eq_u32 s62, 0\n\t"
    "s_cbranch_scc1 L_mpf_%=\n\t"
    "L_ml0_%=:\n\t"
    "ds_read_b32 v114, v113 offset:4\n\t"
    "s_waitcnt lgkmcnt(0)\n\t"
    "v_readfirstlane_b32 s65, v114\n\t"
    "s_nop 0\n\t"
    "s_cmp_lg_u32 s65, 0\n\t"
    "s_cbranch_scc1 L_mm0_%=\n\t"
    "s_sleep 2\n\t"
    "s_branch L_ml0_%=\n\t"
    "L_mm0_%=:\n\t"
    "s_cmp_lt_u32 s62, 2\n\t"
    "s_cbranch_scc1 L_mel5_%=\n\t"
    "buffer_load_dwordx4 v[220:223], v127, %12, s61 offen offset:2048 sc1\n\t"
    "buffer_load_dwordx4 v[224:227], v127, %12, s61 offen offset:3072 sc1\n\t"
    "s_add_u32 s61, s61, 0x1000\n\t"
    "buffer_load_dwordx4 v[228:231], v127, %12, s61 offen offset:2048 sc1\n\t"
    "buffer_load_dwordx4 v[232:235], v127, %12, s61 offen offset:3072 sc1\n\t"
    "s_add_u32 s61, s61, 0x1000\n\t"
    "buffer_load_dwordx4 v[236:239], v127, %12, s61 offen offset:2048 sc1\n\t"
    "buffer_load_dwordx4 v[240:243], v127, %12, s61 offen offset:3072 sc1\n\t"
    "s_add_u32 s61, s61, 0x1000\n\t"
    "buffer_load_dwordx4 v[244:247], v127, %12, s61 offen offset:2048 sc1\n\t"
    "buffer_load_dwordx4 v[248:251], v127, %12, s61 offen offset:3072 sc1\n\t"
    "s_add_u32 s61, s61, 0x1000\n\t"
    "buffer_load_dwordx4 v[252:255], v127, %12, s61 offen offset:2048 sc1\n\t"
    "s_mov_b32 s62, 1\n\t"
    "L_mel5_%=:\n\t"
    "buffer_load_dwordx4 v[0:3], v127, %12, s63 offen offset:0 sc1\n\t"
    "buffer_load_dwordx4 v[4:7], v127, %12, s63 offen offset:1024 sc1\n\t"
    "buffer_load_dwordx4 v[8:11], v127, %12, s63 offen offset:2048 sc1\n\t"
    "buffer_load_dwordx4 v[12:15], v127, %12, s63 offen offset:3072 sc1\n\t"
    "s_add_u32 s63, s63, 0x1000\n\t"
    "s_mov_b32 s62, 0\n\t"
    "L_mpf_%=:\n\t"
    "v_lshrrev_b32 v105, 4, v127\n\t"
    "v_and_b32 v105, 48, v105\n\t"
    "s_movk_i32 s65, 130\n\t"
    "v_mad_u32_u24 v104, v155, s65, v105\n\t"
    "v_add_u32 v105, 14170, v104\n\t"
    "v_and_b32 v104, -16, v104\n\t"
    "v_and_b32 v105, -16, v105\n\t"
    "buffer_load_dwordx4 v[128:131], v127, %12, s61 offen offset:3072 sc1\n\t"
    "s_add_u32 s61, s61, 0x1000\n\t"
    "buffer_load_dwordx4 v[132:135], v127, %12, s61 offen offset:2048 sc1\n\t"
    "ds_read_b128 v[156:159], v104 offset:12480\n\t"
    "ds_read_b128 v[160:163], v104 offset:12544\n\t"
    "buffer_load_dwordx4 v[136:139], v127, %12, s61 offen offset:3072 sc1\n\t"
    "s_add_u32 s61, s61, 0x1000\n\t"
    "buffer_load_dwordx4 v[140:143], v127, %12, s61 offen offset:2048 sc1\n\t"
    "ds_read_b128 v[212:215], v105 offset:0\n\t"
    "ds_read_b128 v[216:219], v105 offset:64\n\t"
    "buffer_load_dwordx4 v[144:147], v127, %12, s61 offen offset:3072 sc1\n\t"
    "s_add_u32 s61, s61, 0x1000\n\t"
    "buffer_load_dwordx4 v[148:151], v127, %12, s61 offen offset:2048 sc1\n\t"
    "buffer_load_dwordx4 v[152:155], v127, %12, s61 offen offset:3072 sc1\n\t"
    "s_add_u32 s61, s61, 0x1000\n\t"
    "v_mov_b32 v114, 1\n\t"
    "ds_add_u32 v113, v114\n\t"
    "s_waitcnt lgkmcnt(5)\n\t"
    "s_waitcnt vmcnt(7)\n\t"
    "v_mfma_f32_16x16x32_bf16 v[16:19], v[0:3], v[164:167], 0\n\t"
    "v_mfma_f32_16x16x32_bf16 v[16:19], v[4:7], v[168:171], v[16:19]\n\t"
    "v_mfma_f32_16x16x32_bf16 v[16:19], v[8:11], v[220:223], v[16:19]\n\t"
    "v_mfma_f32_16x16x32_bf16 v[16:19], v[12:15], v[224:227], v[16:19]\n\t"
    "v_mfma_f32_16x16x32_bf16 v[20:23], v[0:3], v[172:175], 0\n\t"
    "v_mfma_f32_16x16x32_bf16 v[20:23], v[4:7], v[176:179], v[20:23]\n\t"
    "v_mfma_f32_16x16x32_bf16 v[20:23], v[8:11], v[228:231], v[20:23]\n\t"
    "v_mfma_f32_16x16x32_bf16 v[20:23], v[12:15], v[232:235], v[20:23]\n\t"
    "v_mfma_f32_16x16x32_bf16 v[80:83], v[0:3], v[180:183], 0\n\t"
    "v_mfma_f32_16x16x32_bf16 v[80:83], v[4:7], v[184:187], v[80:83]\n\t"
    "v_mfma_f32_16x16x32_bf16 v[80:83], v[8:11], v[236:239], v[80:83]\n\t"
    "v_mfma_f32_16x16x32_bf16 v[80:83], v[12:15], v[240:243], v[80:83]\n\t"
    "v_cvt_pk_bf16_f32 v24, v16, v17\n\t"
    "v_cvt_pk_bf16_f32 v25, v18, v19\n\t"
    "v_mfma_f32_16x16x32_bf16 v[16:19], v[0:3], v[188:191], 0\n\t"
    "v_mfma_f32_16x16x32_bf16 v[16:19], v[4:7], v[192:195], v[16:19]\n\t"
    "v_mfma_f32_16x16x32_bf16 v[16:19], v[8:11], v[244:247], v[16:19]\n\t"
    "v_mfma_f32_16x16x32_bf16 v[16:19], v[12:15], v[248:251], v[16:19]\n\t"
    "v_cvt_pk_bf16_f32 v26, v20, v21\n\t"
    "v_cvt_pk_bf16_f32 v27, v22, v23\n\t"
    "s_waitcnt vmcnt(6)\n\t"
    "v_mfma_f32_16x16x32_bf16 v[20:23], v[0:3], v[196:199], 0\n\t"
    "v_mfma_f32_16x16x32_bf16 v[20:23], v[4:7], v[200:203], v[20:23]\n\t"
    "v_mfma_f32_16x16x32_bf16 v[20:23], v[8:11], v[252:255], v[20:23]\n\t"
    "v_mfma_f32_16x16x32_bf16 v[20:23], v[12:15], v[128:131], v[20:23]\n\t"
    "v_cvt_pk_bf16_f32 v28, v80, v81\n\t"
    "v_cvt_pk_bf16_f32 v29, v82, v83\n\t"
    "s_waitcnt lgkmcnt(0)\n\t"
    "s_waitcnt vmcnt(4)\n\t"
    "v_mfma_f32_16x16x32_bf16 v[80:83], v[0:3], v[204:207], 0\n\t"
    "v_mfma_f32_16x16x32_bf16 v[80:83], v[4:7], v[208:211], v[80:83]\n\t"
    "v_mfma_f32_16x16x32_bf16 v[80:83], v[8:11], v[132:135], v[80:83]\n\t"
    "v_mfma_f32_16x16x32_bf16 v[80:83], v[12:15], v[136:139], v[80:83]\n\t"
    "v_cvt_pk_bf16_f32 v30, v16, v17\n\t"
    "v_cvt_pk_bf16_f32 v31, v18, v19\n\t"
    "s_waitcnt vmcnt(2)\n\t"
    "v_mfma_f32_16x16x32_bf16 v[16:19], v[0:3], v[156:159], 0\n\t"
    "buffer_load_dwordx4 v[100:103], v127, %12, s84 offen offset:0 sc1\n\t"
    "s_add_u32 s84, s84, s97\n\t"
    "v_mfma_f32_16x16x32_bf16 v[16:19], v[4:7], v[160:163], v[16:19]\n\t"
    "buffer_load_dwordx4 v[104:107], v127, %12, s85 offen offset:0 sc1\n\t"
    "s_add_u32 s85, s85, s97\n\t"
    "v_mfma_f32_16x16x32_bf16 v[16:19], v[8:11], v[140:143], v[16:19]\n\t"
    "v_mfma_f32_16x16x32_bf16 v[16:19], v[12:15], v[144:147], v[16:19]\n\t"
    "v_cvt_pk_bf16_f32 v32, v20, v21\n\t"
    "v_cvt_pk_bf16_f32 v33, v22, v23\n\t"
    "s_waitcnt vmcnt(2)\n\t"
    "v_mfma_f32_16x16x32_bf16 v[20:23], v[0:3], v[212:215], 0\n\t"
    "buffer_load_dwordx4 v[108:111], v127, %12, s86 offen offset:0 sc1\n\t"
    "s_add_u32 s86, s86, s97\n\t"
    "v_mfma_f32_16x16x32_bf16 v[20:23], v[4:7], v[216:219], v[20:23]\n\t"
    "buffer_load_dwordx4 v[112:115], v127, %12, s87 offen offset:0 sc1\n\t"
    "s_add_u32 s87, s87, s97\n\t"
    "v_mfma_f32_16x16x32_bf16 v[20:23], v[8:11], v[148:151], v[20:23]\n\t"
    "v_mfma_f32_16x16x32_bf16 v[20:23], v[12:15], v[152:155], v[20:23]\n\t"
    "v_cvt_pk_bf16_f32 v34, v80, v81\n\t"
    "v_cvt_pk_bf16_f32 v35, v82, v83\n\t"
    "v_cvt_pk_bf16_f32 v36, v16, v17\n\t"
    "v_cvt_pk_bf16_f32 v37, v18, v19\n\t"
    "s_nop 3\n\t"
    "v_cvt_pk_bf16_f32 v38, v20, v21\n\t"
    "v_cvt_pk_bf16_f32 v39, v22, v23\n\t"
    "v_lshrrev_b32 v41, 2, v127\n\t"
    "s_and_b32 s65, %14, 8\n\t"
    "s_lshl_b32 s65, s65, 10\n\t"
    "s_add_u32 s63, s63, s65\n\t"
    "buffer_store_dword v40, v41, %12, s69 offen sc1\n\t"
    "v_mov_b32 v82, 3108\n\t"
    "s_cmp_eq_u32 %16, 0\n\t"
    "s_cselect_b32 s47, 0x1ff, 0\n\t"
    "L_my_%=:\n\t"
    "ds_read_b32 v81, v82\n\t"
    "s_waitcnt lgkmcnt(0)\n\t"
    "v_cmp_gt_u32 vcc, 8, v81\n\t"
    "s_cbranch_vccz L_mz_%=\n\t"
    "s_sleep 2\n\t"
    "s_branch L_my_%=\n\t"
    "L_mz_%=:\n\t"
    "ds_write_b64 v125, v[24:25] offset:0\n\t"
    "v_mov_b64 v[0:1], 0\n\t"
    "ds_write_b64 v125, v[26:27] offset:128\n\t"
    "v_mov_b64 v[2:3], 0\n\t"
    "ds_write_b64 v125, v[28:29] offset:256\n\t"
    "v_mov_b64 v[4:5], 0\n\t"
    "ds_write_b64 v125, v[30:31] offset:384\n\t"
    "v_mov_b64 v[6:7], 0\n\t"
    "ds_write_b64 v125, v[32:33] offset:512\n\t"
    "ds_write_b64 v125, v[34:35] offset:640\n\t"
    "ds_write_b64 v125, v[36:37] offset:768\n\t"
    "ds_write_b64 v125, v[38:39] offset:872\n\t"
    "v_add_u32 v125, s93, v125\n\t"
    "s_sub_u32 s93, 0, s93\n\t"
    "s_waitcnt lgkmcnt(0)\n\t"
    "s_barrier\n\t"
    "s_memrealtime s[44:45]\n\t"
    "s_waitcnt lgkmcnt(0)\n\t"
    "s_waitcnt vmcnt(4)\n\t"
    "v_add_u32_sdwa v126, s92, v100 dst_sel:DWORD dst_unused:UNUSED_PAD src0_sel:DWORD src1_sel:WORD_1\n\t"
    "v_add_u32_sdwa v100, s92, v100 dst_sel:DWORD dst_unused:UNUSED_PAD src0_sel:DWORD src1_sel:WORD_0\n\t"
    "ds_read_b64 v[84:85], v100 offset:0\n\t"
    "ds_read_b64 v[86:87], v126 offset:0\n\t"
    "v_add_u32_sdwa v90, s92, v101 dst_sel:DWORD dst_unused:UNUSED_PAD src0_sel:DWORD src1_sel:WORD_1\n\t"
    "v_add_u32_sdwa v101, s92, v101 dst_sel:DWORD dst_unused:UNUSED_PAD src0_sel:DWORD src1_sel:WORD_0\n\t"
    "ds_read_b64 v[88:89], v101 offset:0\n\t"
    "ds_read_b64 v[90:91], v90 offset:0\n\t"
    "v_add_u32 v63, s96, v127\n\t"
    "s_add_u32 s96, s96, 0x1000\n\t"
    "s_and_b32 s96, s96, 0x3000\n\t"
    "v_add_u32 v63, 128000, v63\n\t"
    "ds_read_b128 v[48:51], v63 offset:0\n\t"
    "ds_read_b128 v[52:55], v63 offset:1024\n\t"
    "ds_read_b128 v[56:59], v63 offset:2048\n\t"
    "ds_read_b128 v[60:63], v63 offset:3072\n\t"
    "v_add_u32_sdwa v126, s92, v102 dst_sel:DWORD dst_unused:UNUSED_PAD src0_sel:DWORD src1_sel:WORD_1\n\t"
    "v_add_u32_sdwa v102, s92, v102 dst_sel:DWORD dst_unused:UNUSED_PAD src0_sel:DWORD src1_sel:WORD_0\n\t"
    "ds_read_b64 v[92:93], v102 offset:0\n\t"
    "ds_read_b64 v[94:95], v126 offset:0\n\t"
    "v_add_u32_sdwa v98, s92, v103 dst_sel:DWORD dst_unused:UNUSED_PAD src0_sel:DWORD src1_sel:WORD_1\n\t"
    "v_add_u32_sdwa v103, s92, v103 dst_sel:DWORD dst_unused:UNUSED_PAD src0_sel:DWORD src1_sel:WORD_0\n\t"
    "ds_read_b64 v[96:97], v103 offset:0\n\t"
    "ds_read_b64 v[98:99], v98 offset:0\n\t"
    "buffer_load_dwordx4 v[100:103], v127, %12, s88 offen offset:0 sc1\n\t"
    "s_add_u32 s88, s88, s97\n\t"
    "s_waitcnt vmcnt(4)\n\t"
    "v_add_u32_sdwa v126, s92, v104 dst_sel:DWORD dst_unused:UNUSED_PAD src0_sel:DWORD src1_sel:WORD_1\n\t"
    "v_add_u32_sdwa v104, s92, v104 dst_sel:DWORD dst_unused:UNUSED_PAD src0_sel:DWORD src1_sel:WORD_0\n\t"
    "v_mov_b64 v[8:9], 0\n\t"
    "v_mov_b64 v[10:11], 0\n\t"
    "v_mov_b64 v[12:13], 0\n\t"
    "v_mov_b64 v[14:15], 0\n\t"
    "v_mov_b64 v[16:17], 0\n\t"
    "v_mov_b64 v[18:19], 0\n\t"
    "s_waitcnt lgkmcnt(8)\n\t"
    "v_smfmac_f32_16x16x64_bf16 v[0:3], v[120:123], v[84:91], v124\n\t"
    "ds_read_b64 v[84:85], v104 offset:0\n\t"
    "s_waitcnt lgkmcnt(5)\n\t"
    "v_mfma_f32_16x16x32_bf16 v[116:119], v[48:51], v[164:167], 0\n\t"
    "ds_read_b64 v[86:87], v126 offset:0\n\t"
    "v_mfma_f32_16x16x32_bf16 v[116:119], v[52:55], v[168:171], v[116:119]\n\t"
    "v_add_u32_sdwa v90, s92, v105 dst_sel:DWORD dst_unused:UNUSED_PAD src0_sel:DWORD src1_sel:WORD_1\n\t"
    "v_add_u32_sdwa v105, s92, v105 dst_sel:DWORD dst_unused:UNUSED_PAD src0_sel:DWORD src1_sel:WORD_0\n\t"
    "ds_read_b64 v[88:89], v105 offset:0\n\t"
    "v_mfma_f32_16x16x32_bf16 v[116:119], v[56:59], v[220:223], v[116:119]\n\t"
    "ds_read_b64 v[90:91], v90 offset:0\n\t"
    "v_mfma_f32_16x16x32_bf16 v[116:119], v[60:63], v[224:227], v[116:119]\n\t"
    "v_add_u32_sdwa v126, s92, v106 dst_sel:DWORD dst_unused:UNUSED_PAD src0_sel:DWORD src1_sel:WORD_1\n\t"
    "v_add_u32_sdwa v106, s92, v106 dst_sel:DWORD dst_unused:UNUSED_PAD src0_sel:DWORD src1_sel:WORD_0\n\t"
    "v_mfma_f32_16x16x32_bf16 v[80:83], v[48:51], v[172:175], 0\n\t"
    "v_mov_b64 v[20:21], 0\n\t"
    "v_mov_b64 v[22:23], 0\n\t"
    "v_mov_b64 v[24:25], 0\n\t"
    "v_mov_b64 v[26:27], 0\n\t"
    "v_mov_b64 v[28:29], 0\n\t"
    "v_mov_b64 v[30:31], 0\n\t"
    "s_waitcnt vmcnt(3) lgkmcnt(4)\n\t"
    "v_smfmac_f32_16x16x64_bf16 v[4:7], v[120:123], v[92:99], v124\n\t"
    "ds_read_b64 v[92:93], v106 offset:0\n\t"
    "v_mfma_f32_16x16x32_bf16 v[80:83], v[52:55], v[176:179], v[80:83]\n\t"
    "ds_read_b64 v[94:95], v126 offset:0\n\t"
    "v_mfma_f32_16x16x32_bf16 v[80:83], v[56:59], v[228:231], v[80:83]\n\t"
    "v_add_u32_sdwa v98, s92, v107 dst_sel:DWORD dst_unused:UNUSED_PAD src0_sel:DWORD src1_sel:WORD_1\n\t"
    "v_add_u32_sdwa v107, s92, v107 dst_sel:DWORD dst_unused:UNUSED_PAD src0_sel:DWORD src1_sel:WORD_0\n\t"
    "ds_read_b64 v[96:97], v107 offset:0\n\t"
    "v_mfma_f32_16x16x32_bf16 v[80:83], v[60:63], v[232:235], v[80:83]\n\t"
    "ds_read_b64 v[98:99], v98 offset:0\n\t"
    "v_cvt_pk_bf16_f32 v116, v116, v117\n\t"
    "v_cvt_pk_bf16_f32 v117, v118, v119\n\t"
    "ds_write_b64 v125, v[116:117] offset:0\n\t"
    "v_mfma_f32_16x16x32_bf16 v[116:119], v[48:51], v[180:183], 0\n\t"
    "buffer_load_dwordx4 v[104:107], v127, %12, s89 offen offset:0 sc1\n\t"
    "s_add_u32 s89, s89, s97\n\t"
    "v_add_u32_sdwa v126, s92, v108 dst_sel:DWORD dst_unused:UNUSED_PAD src0_sel:DWORD src1_sel:WORD_1\n\t"
    "v_add_u32_sdwa v108, s92, v108 dst_sel:DWORD dst_unused:UNUSED_PAD src0_sel:DWORD src1_sel:WORD_0\n\t"
    "v_mfma_f32_16x16x32_bf16 v[116:119], v[52:55], v[184:187], v[116:119]\n\t"
    "v_mov_b64 v[32:33], 0\n\t"
    "v_mov_b64 v[34:35], 0\n\t"
    "v_mov_b64 v[36:37], 0\n\t"
    "v_mov_b64 v[38:39], 0\n\t"
    "v_mov_b64 v[40:41], 0\n\t"
    "v_mov_b64 v[42:43], 0\n\t"
    "v_mov_b64 v[44:45], 0\n\t"
    "v_mov_b64 v[46:47], 0\n\t"
    "s_waitcnt lgkmcnt(5)\n\t"
    "v_smfmac_f32_16x16x64_bf16 v[8:11], v[120:123], v[84:91], v124\n\t"
    "ds_read_b64 v[84:85], v108 offset:0\n\t"
    "v_mfma_f32_16x16x32_bf16 v[116:119], v[56:59], v[236:239], v[116:119]\n\t"
    "ds_read_b64 v[86:87], v126 offset:0\n\t"
    "v_mfma_f32_16x16x32_bf16 v[116:119], v[60:63], v[240:243], v[116:119]\n\t"
    "v_add_u32_sdwa v90, s92, v109 dst_sel:DWORD dst_unused:UNUSED_PAD src0_sel:DWORD src1_sel:WORD_1\n\t"
    "v_add_u32_sdwa v109, s92, v109 dst_sel:DWORD dst_unused:UNUSED_PAD src0_sel:DWORD src1_sel:WORD_0\n\t"
    "ds_read_b64 v[88:89], v109 offset:0\n\t"
    "v_cvt_pk_bf16_f32 v80, v80, v81\n\t"
    "v_cvt_pk_bf16_f32 v81, v82, v83\n\t"
    "ds_write_b64 v125, v[80:81] offset:128\n\t"
    "v_mfma_f32_16x16x32_bf16 v[80:83], v[48:51], v[188:191], 0\n\t"
    "ds_read_b64 v[90:91], v90 offset:0\n\t"
    "v_mfma_f32_16x16x32_bf16 v[80:83], v[52:55], v[192:195], v[80:83]\n\t"
    "v_add_u32_sdwa v126, s92, v110 dst_sel:DWORD dst_unused:UNUSED_PAD src0_sel:DWORD src1_sel:WORD_1\n\t"
    "v_add_u32_sdwa v110, s92, v110 dst_sel:DWORD dst_unused:UNUSED_PAD src0_sel:DWORD src1_sel:WORD_0\n\t"
    "v_mfma_f32_16x16x32_bf16 v[80:83], v[56:59], v[244:247], v[80:83]\n\t"
    "s_waitcnt vmcnt(3) lgkmcnt(6)\n\t"
    "v_smfmac_f32_16x16x64_bf16 v[12:15], v[120:123], v[92:99], v124\n\t"
    "ds_read_b64 v[92:93], v110 offset:0\n\t"
    "v_mfma_f32_16x16x32_bf16 v[80:83], v[60:63], v[248:251], v[80:83]\n\t"
    "ds_read_b64 v[94:95], v126 offset:0\n\t"
    "v_cvt_pk_bf16_f32 v116, v116, v117\n\t"
    "v_cvt_pk_bf16_f32 v117, v118, v119\n\t"
    "ds_write_b64 v125, v[116:117] offset:256\n\t"
    "v_mfma_f32_16x16x32_bf16 v[116:119], v[48:51], v[196:199], 0\n\t"
    "v_add_u32_sdwa v98, s92, v111 dst_sel:DWORD dst_unused:UNUSED_PAD src0_sel:DWORD src1_sel:WORD_1\n\t"
    "v_add_u32_sdwa v111, s92, v111 dst_sel:DWORD dst_unused:UNUSED_PAD src0_sel:DWORD src1_sel:WORD_0\n\t"
    "ds_read_b64 v[96:97], v111 offset:0\n\t"
    "v_mfma_f32_16x16x32_bf16 v[116:119], v[52:55], v[200:203], v[116:119]\n\t"
    "ds_read_b64 v[98:99], v98 offset:0\n\t"
    "v_mfma_f32_16x16x32_bf16 v[116:119], v[56:59], v[252:255], v[116:119]\n\t"
    "buffer_load_dwordx4 v[108:111], v127, %12, s90 offen offset:0 sc1\n\t"
    "s_add_u32 s90, s90, s97\n\t"
    "v_add_u32_sdwa v126, s92, v112 dst_sel:DWORD dst_unused:UNUSED_PAD src0_sel:DWORD src1_sel:WORD_1\n\t"
    "v_add_u32_sdwa v112, s92, v112 dst_sel:DWORD dst_unused:UNUSED_PAD src0_sel:DWORD src1_sel:WORD_0\n\t"
    "v_mfma_f32_16x16x32_bf16 v[116:119], v[60:63], v[128:131], v[116:119]\n\t"
    "v_cvt_pk_bf16_f32 v0, v0, v1\n\t"
    "v_cvt_pk_bf16_f32 v1, v2, v3\n\t"
    "v_cvt_pk_bf16_f32 v80, v80, v81\n\t"
    "v_cvt_pk_bf16_f32 v81, v82, v83\n\t"
    "ds_write_b64 v125, v[80:81] offset:384\n\t"
    "v_add_u32 v79, s96, v127\n\t"
    "s_add_u32 s96, s96, 0x1000\n\t"
    "s_and_b32 s96, s96, 0x3000\n\t"
    "v_add_u32 v79, 128000, v79\n\t"
    "ds_read_b128 v[64:67], v79 offset:0\n\t"
    "ds_read_b128 v[68:71], v79 offset:1024\n\t"
    "ds_read_b128 v[72:75], v79 offset:2048\n\t"
    "ds_read_b128 v[76:79], v79 offset:3072\n\t"
    "s_waitcnt lgkmcnt(10)\n\t"
    "v_smfmac_f32_16x16x64_bf16 v[16:19], v[120:123], v[84:91], v124\n\t"
    "ds_read_b64 v[84:85], v112 offset:0\n\t"
    "v_mfma_f32_16x16x32_bf16 v[80:83], v[48:51], v[204:207], 0\n\t"
    "ds_read_b64 v[86:87], v126 offset:0\n\t"
    "v_mfma_f32_16x16x32_bf16 v[80:83], v[52:55], v[208:211], v[80:83]\n\t"
    "v_add_u32_sdwa v90, s92, v113 dst_sel:DWORD dst_unused:UNUSED_PAD src0_sel:DWORD src1_sel:WORD_1\n\t"
    "v_add_u32_sdwa v113, s92, v113 dst_sel:DWORD dst_unused:UNUSED_PAD src0_sel:DWORD src1_sel:WORD_0\n\t"
    "ds_read_b64 v[88:89], v113 offset:0\n\t"
    "v_mfma_f32_16x16x32_bf16 v[80:83], v[56:59], v[132:135], v[80:83]\n\t"
    "ds_read_b64 v[90:91], v90 offset:0\n\t"
    "v_mfma_f32_16x16x32_bf16 v[80:83], v[60:63], v[136:139], v[80:83]\n\t"
    "v_add_u32_sdwa v126, s92, v114 dst_sel:DWORD dst_unused:UNUSED_PAD src0_sel:DWORD src1_sel:WORD_1\n\t"
    "v_add_u32_sdwa v114, s92, v114 dst_sel:DWORD dst_unused:UNUSED_PAD src0_sel:DWORD src1_sel:WORD_0\n\t"
    "v_cvt_pk_bf16_f32 v116, v116, v117\n\t"
    "v_cvt_pk_bf16_f32 v117, v118, v119\n\t"
    "ds_write_b64 v125, v[116:117] offset:512\n\t"
    "v_mfma_f32_16x16x32_bf16 v[116:119], v[48:51], v[156:159], 0\n\t"
    "v_cvt_pk_bf16_f32 v2, v4, v5\n\t"
    "v_cvt_pk_bf16_f32 v3, v6, v7\n\t"
    "v_mov_b64 v[4:5], 0\n\t"
    "v_mov_b64 v[6:7], 0\n\t"
    "s_waitcnt vmcnt(2) lgkmcnt(10)\n\t"
    "v_smfmac_f32_16x16x64_bf16 v[20:23], v[120:123], v[92:99], v124\n\t"
    "ds_read_b64 v[92:93], v114 offset:0\n\t"
    "v_mfma_f32_16x16x32_bf16 v[116:119], v[52:55], v[160:163], v[116:119]\n\t"
    "ds_read_b64 v[94:95], v126 offset:0\n\t"
    "v_mfma_f32_16x16x32_bf16 v[116:119], v[56:59], v[140:143], v[116:119]\n\t"
    "v_add_u32_sdwa v98, s92, v115 dst_sel:DWORD dst_unused:UNUSED_PAD src0_sel:DWORD src1_sel:WORD_1\n\t"
    "v_add_u32_sdwa v115, s92, v115 dst_sel:DWORD dst_unused:UNUSED_PAD src0_sel:DWORD src1_sel:WORD_0\n\t"
    "ds_read_b64 v[96:97], v115 offset:0\n\t"
    "v_mfma_f32_16x16x32_bf16 v[116:119], v[60:63], v[144:147], v[116:119]\n\t"
    "ds_read_b64 v[98:99], v98 offset:0\n\t"
    "v_cvt_pk_bf16_f32 v80, v80, v81\n\t"
    "v_cvt_pk_bf16_f32 v81, v82, v83\n\t"
    "ds_write_b64 v125, v[80:81] offset:640\n\t"
    "v_mfma_f32_16x16x32_bf16 v[80:83], v[48:51], v[212:215], 0\n\t"
    "buffer_load_dwordx4 v[112:115], v127, %12, s91 offen offset:0 sc1\n\t"
    "s_add_u32 s91, s91, s97\n\t"
    "v_add_u32_sdwa v126, s92, v100 dst_sel:DWORD dst_unused:UNUSED_PAD src0_sel:DWORD src1_sel:WORD_1\n\t"
    "v_add_u32_sdwa v100, s92, v100 dst_sel:DWORD dst_unused:UNUSED_PAD src0_sel:DWORD src1_sel:WORD_0\n\t"
    "v_mfma_f32_16x16x32_bf16 v[80:83], v[52:55], v[216:219], v[80:83]\n\t"
    "v_cvt_pk_bf16_f32 v8, v8, v9\n\t"
    "v_cvt_pk_bf16_f32 v9, v10, v11\n\t"
    "s_waitcnt lgkmcnt(6)\n\t"
    "v_smfmac_f32_16x16x64_bf16 v[24:27], v[120:123], v[84:91], v124\n\t"
    "ds_read_b64 v[84:85], v100 offset:0\n\t"
    "v_mfma_f32_16x16x32_bf16 v[80:83], v[56:59], v[148:151], v[80:83]\n\t"
    "ds_read_b64 v[86:87], v126 offset:0\n\t"
    "v_mfma_f32_16x16x32_bf16 v[80:83], v[60:63], v[152:155], v[80:83]\n\t"
    "v_add_u32_sdwa v90, s92, v101 dst_sel:DWORD dst_unused:UNUSED_PAD src0_sel:DWORD src1_sel:WORD_1\n\t"
    "v_add_u32_sdwa v101, s92, v101 dst_sel:DWORD dst_unused:UNUSED_PAD src0_sel:DWORD src1_sel:WORD_0\n\t"
    "ds_read_b64 v[88:89], v101 offset:0\n\t"
    "v_cvt_pk_bf16_f32 v116, v116, v117\n\t"
    "v_cvt_pk_bf16_f32 v117, v118, v119\n\t"
    "ds_write_b64 v125, v[116:117] offset:768\n\t"
    "v_mfma_f32_16x16x32_bf16 v[116:119], v[64:67], v[164:167], 0\n\t"
    "ds_read_b64 v[90:91], v90 offset:0\n\t"
    "v_mfma_f32_16x16x32_bf16 v[116:119], v[68:71], v[168:171], v[116:119]\n\t"
    "v_add_u32_sdwa v126, s92, v102 dst_sel:DWORD dst_unused:UNUSED_PAD src0_sel:DWORD src1_sel:WORD_1\n\t"
    "v_add_u32_sdwa v102, s92, v102 dst_sel:DWORD dst_unused:UNUSED_PAD src0_sel:DWORD src1_sel:WORD_0\n\t"
    "v_mfma_f32_16x16x32_bf16 v[116:119], v[72:75], v[220:223], v[116:119]\n\t"
    "v_cvt_pk_bf16_f32 v10, v12, v13\n\t"
    "v_cvt_pk_bf16_f32 v11, v14, v15\n\t"
    "v_mov_b64 v[12:13], 0\n\t"
    "v_mov_b64 v[14:15], 0\n\t"
    "s_cmp_lg_u32 s47, 0\n\t"
    "s_cbranch_scc1 L_mp2_%=\n\t"
    "L_mp2b_%=:\n\t"
    "s_waitcnt vmcnt(2) lgkmcnt(6)\n\t"
    "v_smfmac_f32_16x16x64_bf16 v[28:31], v[120:123], v[92:99], v124\n\t"
    "ds_read_b64 v[92:93], v102 offset:0\n\t"
    "v_mfma_f32_16x16x32_bf16 v[116:119], v[76:79], v[224:227], v[116:119]\n\t"
    "ds_read_b64 v[94:95], v126 offset:0\n\t"
    "v_cvt_pk_bf16_f32 v80, v80, v81\n\t"
    "v_cvt_pk_bf16_f32 v81, v82, v83\n\t"
    "ds_write_b64 v125, v[80:81] offset:872\n\t"
    "v_mfma_f32_16x16x32_bf16 v[80:83], v[64:67], v[172:175], 0\n\t"
    "v_add_u32_sdwa v98, s92, v103 dst_sel:DWORD dst_unused:UNUSED_PAD src0_sel:DWORD src1_sel:WORD_1\n\t"
    "v_add_u32_sdwa v103, s92, v103 dst_sel:DWORD dst_unused:UNUSED_PAD src0_sel:DWORD src1_sel:WORD_0\n\t"
    "ds_read_b64 v[96:97], v103 offset:0\n\t"
    "v_mfma_f32_16x16x32_bf16 v[80:83], v[68:71], v[176:179], v[80:83]\n\t"
    "ds_read_b64 v[98:99], v98 offset:0\n\t"
    "v_mfma_f32_16x16x32_bf16 v[80:83], v[72:75], v[228:231], v[80:83]\n\t"
    "v_add_u32_sdwa v126, s92, v104 dst_sel:DWORD dst_unused:UNUSED_PAD src0_sel:DWORD src1_sel:WORD_1\n\t"
    "v_add_u32_sdwa v104, s92, v104 dst_sel:DWORD dst_unused:UNUSED_PAD src0_sel:DWORD src1_sel:WORD_0\n\t"
    "v_mfma_f32_16x16x32_bf16 v[80:83], v[76:79], v[232:235], v[80:83]\n\t"
    "v_cvt_pk_bf16_f32 v16, v16, v17\n\t"
    "v_cvt_pk_bf16_f32 v17, v18, v19\n\t"
    "v_cvt_pk_bf16_f32 v116, v116, v117\n\t"
    "v_cvt_pk_bf16_f32 v117, v118, v119\n\t"
    "ds_write_b64 v125, v[116:117] offset:32000\n\t"
    "s_waitcnt lgkmcnt(6)\n\t"
    "v_smfmac_f32_16x16x64_bf16 v[32:35], v[120:123], v[84:91], v124\n\t"
    "ds_read_b64 v[84:85], v104 offset:0\n\t"
    "v_mfma_f32_16x16x32_bf16 v[116:119], v[64:67], v[180:183], 0\n\t"
    "ds_read_b64 v[86:87], v126 offset:0\n\t"
    "v_mfma_f32_16x16x32_bf16 v[116:119], v[68:71], v[184:187], v[116:119]\n\t"
    "v_add_u32_sdwa v90, s92, v105 dst_sel:DWORD dst_unused:UNUSED_PAD src0_sel:DWORD src1_sel:WORD_1\n\t"
    "v_add_u32_sdwa v105, s92, v105 dst_sel:DWORD dst_unused:UNUSED_PAD src0_sel:DWORD src1_sel:WORD_0\n\t"
    "ds_read_b64 v[88:89], v105 offset:0\n\t"
    "v_mfma_f32_16x16x32_bf16 v[116:119], v[72:75], v[236:239], v[116:119]\n\t"
    "ds_read_b64 v[90:91], v90 offset:0\n\t"
    "v_mfma_f32_16x16x32_bf16 v[116:119], v[76:79], v[240:243], v[116:119]\n\t"
    "v_add_u32_sdwa v126, s92, v106 dst_sel:DWORD dst_unused:UNUSED_PAD src0_sel:DWORD src1_sel:WORD_1\n\t"
    "v_add_u32_sdwa v106, s92, v106 dst_sel:DWORD dst_unused:UNUSED_PAD src0_sel:DWORD src1_sel:WORD_0\n\t"
    "v_cvt_pk_bf16_f32 v80, v80, v81\n\t"
    "v_cvt_pk_bf16_f32 v81, v82, v83\n\t"
    "ds_write_b64 v125, v[80:81] offset:32128\n\t"
    "v_mfma_f32_16x16x32_bf16 v[80:83], v[64:67], v[188:191], 0\n\t"
    "v_cvt_pk_bf16_f32 v18, v20, v21\n\t"
    "v_cvt_pk_bf16_f32 v19, v22, v23\n\t"
    "v_mov_b64 v[20:21], 0\n\t"
    "v_mov_b64 v[22:23], 0\n\t"
    "s_waitcnt vmcnt(0)\n\t"
    "s_cmp_lg_u32 s47, 0\n\t"
    "s_cbranch_scc1 L_mp3_%=\n\t"
    "L_mp3b_%=:\n\t"
    "s_barrier\n\t"
    "s_add_u32 s84, s84, s95\n\t"
    "buffer_load_dwordx4 v[100:103], v127, %12, s84 offen offset:0 sc1\n\t"
    "s_lshl_b32 s50, %16, 10\n\t"
    "s_add_u32 s63, s63, s50\n\t"
    "s_add_u32 s50, s50, 0x2000\n\t"
    "s_xor_b32 s50, s50, 0x2000\n\t"
    "s_add_u32 m0, s50, 128000\n\t"
    "s_add_u32 s63, s63, 0x4000\n\t"
    "buffer_load_dwordx4 v127, %12, s63 offen sc1 lds\n\t"
    "s_cmp_lt_u32 %16, 4\n\t"
    "s_cbranch_scc1 L_sp1_%=\n\t"
    "s_setprio 1\n\t"
    "L_sp1_%=:\n\t"
    "s_waitcnt lgkmcnt(6)\n\t"
    "v_smfmac_f32_16x16x64_bf16 v[36:39], v[120:123], v[92:99], v124\n\t"
    "ds_read_b64 v[92:93], v106 offset:0\n\t"
    "v_mfma_f32_16x16x32_bf16 v[80:83], v[68:71], v[192:195], v[80:83]\n\t"
    "ds_read_b64 v[94:95], v126 offset:0\n\t"
    "v_mfma_f32_16x16x32_bf16 v[80:83], v[72:75], v[244:247], v[80:83]\n\t"
    "v_add_u32_sdwa v98, s92, v107 dst_sel:DWORD dst_unused:UNUSED_PAD src0_sel:DWORD src1_sel:WORD_1\n\t"
    "v_add_u32_sdwa v107, s92, v107 dst_sel:DWORD dst_unused:UNUSED_PAD src0_sel:DWORD src1_sel:WORD_0\n\t"
    "ds_read_b64 v[96:97], v107 offset:0\n\t"
    "v_mfma_f32_16x16x32_bf16 v[80:83], v[76:79], v[248:251], v[80:83]\n\t"
    "ds_read_b64 v[98:99], v98 offset:0\n\t"
    "v_cvt_pk_bf16_f32 v116, v116, v117\n\t"
    "v_cvt_pk_bf16_f32 v117, v118, v119\n\t"
    "ds_write_b64 v125, v[116:117] offset:32256\n\t"
    "v_mfma_f32_16x16x32_bf16 v[116:119], v[64:67], v[196:199], 0\n\t"
    "buffer_load_dwordx4 v[104:107], v127, %12, s84 offen offset:1024 sc1\n\t"
    "v_add_u32_sdwa v126, s92, v108 dst_sel:DWORD dst_unused:UNUSED_PAD src0_sel:DWORD src1_sel:WORD_1\n\t"
    "v_add_u32_sdwa v108, s92, v108 dst_sel:DWORD dst_unused:UNUSED_PAD src0_sel:DWORD src1_sel:WORD_0\n\t"
    "v_mfma_f32_16x16x32_bf16 v[116:119], v[68:71], v[200:203], v[116:119]\n\t"
    "v_cvt_pk_bf16_f32 v24, v24, v25\n\t"
    "v_cvt_pk_bf16_f32 v25, v26, v27\n\t"
    "v_mov_b64 v[48:49], 0\n\t"
    "v_mov_b64 v[50:51], 0\n\t"
    "v_mov_b64 v[52:53], 0\n\t"
    "v_mov_b64 v[54:55], 0\n\t"
    "s_waitcnt lgkmcnt(6)\n\t"
    "v_smfmac_f32_16x16x64_bf16 v[40:43], v[120:123], v[84:91], v124\n\t"
    "ds_read_b64 v[84:85], v108 offset:0\n\t"
    "v_mfma_f32_16x16x32_bf16 v[116:119], v[72:75], v[252:255], v[116:119]\n\t"
    "ds_read_b64 v[86:87], v126 offset:0\n\t"
    "v_mfma_f32_16x16x32_bf16 v[116:119], v[76:79], v[128:131], v[116:119]\n\t"
    "v_add_u32_sdwa v90, s92, v109 dst_sel:DWORD dst_unused:UNUSED_PAD src0_sel:DWORD src1_sel:WORD_1\n\t"
    "v_add_u32_sdwa v109, s92, v109 dst_sel:DWORD dst_unused:UNUSED_PAD src0_sel:DWORD src1_sel:WORD_0\n\t"
    "ds_read_b64 v[88:89], v109 offset:0\n\t"
    "v_cvt_pk_bf16_f32 v80, v80, v81\n\t"
    "v_cvt_pk_bf16_f32 v81, v82, v83\n\t"
    "ds_write_b64 v125, v[80:81] offset:32384\n\t"
    "v_mfma_f32_16x16x32_bf16 v[80:83], v[64:67], v[204:207], 0\n\t"
    "ds_read_b64 v[90:91], v90 offset:0\n\t"
    "v_mfma_f32_16x16x32_bf16 v[80:83], v[68:71], v[208:211], v[80:83]\n\t"
    "v_add_u32_sdwa v126, s92, v110 dst_sel:DWORD dst_unused:UNUSED_PAD src0_sel:DWORD src1_sel:WORD_1\n\t"
    "v_add_u32_sdwa v110, s92, v110 dst_sel:DWORD dst_unused:UNUSED_PAD src0_sel:DWORD src1_sel:WORD_0\n\t"
    "v_mfma_f32_16x16x32_bf16 v[80:83], v[72:75], v[132:135], v[80:83]\n\t"
    "v_cvt_pk_bf16_f32 v26, v28, v29\n\t"
    "v_cvt_pk_bf16_f32 v27, v30, v31\n\t"
    "v_mov_b64 v[28:29], 0\n\t"
    "v_mov_b64 v[30:31], 0\n\t"
    "s_waitcnt lgkmcnt(6)\n\t"
    "v_smfmac_f32_16x16x64_bf16 v[44:47], v[120:123], v[92:99], v124\n\t"
    "ds_read_b64 v[92:93], v110 offset:0\n\t"
    "v_mfma_f32_16x16x32_bf16 v[80:83], v[76:79], v[136:139], v[80:83]\n\t"
    "ds_read_b64 v[94:95], v126 offset:0\n\t"
    "v_cvt_pk_bf16_f32 v116, v116, v117\n\t"
    "v_cvt_pk_bf16_f32 v117, v118, v119\n\t"
    "ds_write_b64 v125, v[116:117] offset:32512\n\t"
    "v_mfma_f32_16x16x32_bf16 v[116:119], v[64:67], v[156:159], 0\n\t"
    "v_add_u32_sdwa v98, s92, v111 dst_sel:DWORD dst_unused:UNUSED_PAD src0_sel:DWORD src1_sel:WORD_1\n\t"
    "v_add_u32_sdwa v111, s92, v111 dst_sel:DWORD dst_unused:UNUSED_PAD src0_sel:DWORD src1_sel:WORD_0\n\t"
    "ds_read_b64 v[96:97], v111 offset:0\n\t"
    "v_mfma_f32_16x16x32_bf16 v[116:119], v[68:71], v[160:163], v[116:119]\n\t"
    "ds_read_b64 v[98:99], v98 offset:0\n\t"
    "v_mfma_f32_16x16x32_bf16 v[116:119], v[72:75], v[140:143], v[116:119]\n\t"
    "s_add_u32 s85, s85, s95\n\t"
    "buffer_load_dwordx4 v[108:111], v127, %12, s85 offen offset:0 sc1\n\t"
    "v_add_u32_sdwa v126, s92, v112 dst_sel:DWORD dst_unused:UNUSED_PAD src0_sel:DWORD src1_sel:WORD_1\n\t"
    "v_add_u32_sdwa v112, s92, v112 dst_sel:DWORD dst_unused:UNUSED_PAD src0_sel:DWORD src1_sel:WORD_0\n\t"
    "v_mfma_f32_16x16x32_bf16 v[116:119], v[76:79], v[144:147], v[116:119]\n\t"
    "v_cvt_pk_bf16_f32 v32, v32, v33\n\t"
    "v_cvt_pk_bf16_f32 v33, v34, v35\n\t"
    "v_cvt_pk_bf16_f32 v80, v80, v81\n\t"
    "v_cvt_pk_bf16_f32 v81, v82, v83\n\t"
    "ds_write_b64 v125, v[80:81] offset:32640\n\t"
    "v_mov_b64 v[56:57], 0\n\t"
    "v_mov_b64 v[58:59], 0\n\t"
    "v_mov_b64 v[60:61], 0\n\t"
    "v_mov_b64 v[62:63], 0\n\t"
    "s_waitcnt lgkmcnt(6)\n\t"
    "v_smfmac_f32_16x16x64_bf16 v[48:51], v[120:123], v[84:91], v124\n\t"
    "ds_read_b64 v[84:85], v112 offset:0\n\t"
    "v_mfma_f32_16x16x32_bf16 v[80:83], v[64:67], v[212:215], 0\n\t"
    "ds_read_b64 v[86:87], v126 offset:0\n\t"
    "v_mfma_f32_16x16x32_bf16 v[80:83], v[68:71], v[216:219], v[80:83]\n\t"
    "v_add_u32_sdwa v90, s92, v113 dst_sel:DWORD dst_unused:UNUSED_PAD src0_sel:DWORD src1_sel:WORD_1\n\t"
    "v_add_u32_sdwa v113, s92, v113 dst_sel:DWORD dst_unused:UNUSED_PAD src0_sel:DWORD src1_sel:WORD_0\n\t"
    "ds_read_b64 v[88:89], v113 offset:0\n\t"
    "v_mfma_f32_16x16x32_bf16 v[80:83], v[72:75], v[148:151], v[80:83]\n\t"
    "ds_read_b64 v[90:91], v90 offset:0\n\t"
    "v_mfma_f32_16x16x32_bf16 v[80:83], v[76:79], v[152:155], v[80:83]\n\t"
    "v_add_u32_sdwa v126, s92, v114 dst_sel:DWORD dst_unused:UNUSED_PAD src0_sel:DWORD src1_sel:WORD_1\n\t"
    "v_add_u32_sdwa v114, s92, v114 dst_sel:DWORD dst_unused:UNUSED_PAD src0_sel:DWORD src1_sel:WORD_0\n\t"
    "v_cvt_pk_bf16_f32 v116, v116, v117\n\t"
    "v_cvt_pk_bf16_f32 v117, v118, v119\n\t"
    "ds_write_b64 v125, v[116:117] offset:32768\n\t"
    "s_nop 2\n\t"
    "v_cvt_pk_bf16_f32 v80, v80, v81\n\t"
    "v_cvt_pk_bf16_f32 v81, v82, v83\n\t"
    "ds_write_b64 v125, v[80:81] offset:32872\n\t"
    "v_cvt_pk_bf16_f32 v34, v36, v37\n\t"
    "v_cvt_pk_bf16_f32 v35, v38, v39\n\t"
    "v_mov_b64 v[36:37], 0\n\t"
    "v_mov_b64 v[38:39], 0\n\t"
    "v_cvt_pk_bf16_f32 v40, v40, v41\n\t"
    "v_cvt_pk_bf16_f32 v41, v42, v43\n\t"
    "v_cvt_pk_bf16_f32 v42, v44, v45\n\t"
    "v_cvt_pk_bf16_f32 v43, v46, v47\n\t"
    "v_mov_b64 v[44:45], 0\n\t"
    "v_mov_b64 v[46:47], 0\n\t"
    "s_waitcnt vmcnt(3) lgkmcnt(7)\n\t"
    "v_smfmac_f32_16x16x64_bf16 v[52:55], v[120:123], v[92:99], v124\n\t"
    "ds_read_b64 v[92:93], v114 offset:0\n\t"
    "ds_read_b64 v[94:95], v126 offset:0\n\t"
    "v_add_u32_sdwa v98, s92, v115 dst_sel:DWORD dst_unused:UNUSED_PAD src0_sel:DWORD src1_sel:WORD_1\n\t"
    "v_add_u32_sdwa v115, s92, v115 dst_sel:DWORD dst_unused:UNUSED_PAD src0_sel:DWORD src1_sel:WORD_0\n\t"
    "ds_read_b64 v[96:97], v115 offset:0\n\t"
    "ds_read_b64 v[98:99], v98 offset:0\n\t"
    "buffer_load_dwordx4 v[112:115], v127, %12, s85 offen offset:1024 sc1\n\t"
    "s_sub_u32 s92, 64000, s92\n\t"
    "v_add_u32_sdwa v126, s92, v100 dst_sel:DWORD dst_unused:UNUSED_PAD src0_sel:DWORD src1_sel:WORD_1\n\t"
    "v_add_u32_sdwa v100, s92, v100 dst_sel:DWORD dst_unused:UNUSED_PAD src0_sel:DWORD src1_sel:WORD_0\n\t"
    "v_cvt_pk_bf16_f32 v48, v48, v49\n\t"
    "v_cvt_pk_bf16_f32 v49, v50, v51\n\t"
    "s_waitcnt lgkmcnt(4)\n\t"
    "s_waitcnt lgkmcnt(0)\n\t"
    "s_waitcnt vmcnt(3)\n\t"
    "s_barrier\n\t"
    "v_add_u32 v125, s93, v125\n\t"
    "s_sub_u32 s93, 0, s93\n\t"
    "v_smfmac_f32_16x16x64_bf16 v[56:59], v[120:123], v[84:91], v124\n\t"
    "ds_read_b64 v[84:85], v100 offset:0\n\t"
    "ds_read_b64 v[86:87], v126 offset:0\n\t"
    "v_add_u32_sdwa v90, s92, v101 dst_sel:DWORD dst_unused:UNUSED_PAD src0_sel:DWORD src1_sel:WORD_1\n\t"
    "v_add_u32_sdwa v101, s92, v101 dst_sel:DWORD dst_unused:UNUSED_PAD src0_sel:DWORD src1_sel:WORD_0\n\t"
    "ds_read_b64 v[88:89], v101 offset:0\n\t"
    "ds_read_b64 v[90:91], v90 offset:0\n\t"
    "v_add_u32_sdwa v126, s92, v102 dst_sel:DWORD dst_unused:UNUSED_PAD src0_sel:DWORD src1_sel:WORD_1\n\t"
    "v_add_u32_sdwa v102, s92, v102 dst_sel:DWORD dst_unused:UNUSED_PAD src0_sel:DWORD src1_sel:WORD_0\n\t"
    "v_cvt_pk_bf16_f32 v50, v52, v53\n\t"
    "v_cvt_pk_bf16_f32 v51, v54, v55\n\t"
    "v_mov_b64 v[52:53], 0\n\t"
    "v_mov_b64 v[54:55], 0\n\t"
    "s_waitcnt vmcnt(2)\n\t"
    "v_smfmac_f32_16x16x64_bf16 v[60:63], v[120:123], v[92:99], v124\n\t"
    "ds_read_b64 v[92:93], v102 offset:0\n\t"
    "ds_read_b64 v[94:95], v126 offset:0\n\t"
    "v_add_u32_sdwa v98, s92, v103 dst_sel:DWORD dst_unused:UNUSED_PAD src0_sel:DWORD src1_sel:WORD_1\n\t"
    "v_add_u32_sdwa v103, s92, v103 dst_sel:DWORD dst_unused:UNUSED_PAD src0_sel:DWORD src1_sel:WORD_0\n\t"
    "ds_read_b64 v[96:97], v103 offset:0\n\t"
    "ds_read_b64 v[98:99], v98 offset:0\n\t"
    "s_add_u32 s86, s86, s95\n\t"
    "buffer_load_dwordx4 v[100:103], v127, %12, s86 offen offset:0 sc1\n\t"
    "v_add_u32_sdwa v126, s92, v104 dst_sel:DWORD dst_unused:UNUSED_PAD src0_sel:DWORD src1_sel:WORD_1\n\t"
    "v_add_u32_sdwa v104, s92, v104 dst_sel:DWORD dst_unused:UNUSED_PAD src0_sel:DWORD src1_sel:WORD_0\n\t"
    "v_cvt_pk_bf16_f32 v56, v56, v57\n\t"
    "v_cvt_pk_bf16_f32 v57, v58, v59\n\t"
    "v_add_u32 v79, s96, v127\n\t"
    "s_add_u32 s96, s96, 0x1000\n\t"
    "s_and_b32 s96, s96, 0x3000\n\t"
    "v_add_u32 v79, 128000, v79\n\t"
    "ds_read_b128 v[64:67], v79 offset:0\n\t"
    "ds_read_b128 v[68:71], v79 offset:1024\n\t"
    "ds_read_b128 v[72:75], v79 offset:2048\n\t"
    "ds_read_b128 v[76:79], v79 offset:3072\n\t"
    "v_mov_b64 v[116:117], 0\n\t"
    "v_mov_b64 v[118:119], 0\n\t"
    "v_cvt_pk_bf16_f32 v58, v60, v61\n\t"
    "v_cvt_pk_bf16_f32 v59, v62, v63\n\t"
    "v_mov_b64 v[60:61], 0\n\t"
    "v_mov_b64 v[62:63], 0\n\t"
    "s_cmp_eq_u32 %16, 0\n\t"
    "s_cselect_b32 s48, 2, -1\n\t"
    "s_mov_b32 s66, 4\n\t"
    "s_branch L_loop_%=\n\t"
    ".p2align 9\n\t"
    "s_nop 0\n\t"
    "s_nop 0\n\t"
    "s_nop 0\n\t"
    "s_nop 0\n\t"
    "s_nop 0\n\t"
    "s_nop 0\n\t"
    "s_nop 0\n\t"
    "s_nop 0\n\t"
    "s_nop 0\n\t"
    "s_nop 0\n\t"
    "s_nop 0\n\t"
    "s_nop 0\n\t"
    "s_nop 0\n\t"
    "s_nop 0\n\t"
    "s_nop 0\n\t"
    "s_nop 0\n\t"
    "s_nop 0\n\t"
    "s_nop 0\n\t"
    "s_nop 0\n\t"
    "s_nop 0\n\t"
    "s_nop 0\n\t"
    "s_nop 0\n\t"
    "s_nop 0\n\t"
    "s_nop 0\n\t"
    "s_nop 0\n\t"
    "s_nop 0\n\t"
    "s_nop 0\n\t"
    "s_nop 0\n\t"
    "s_nop 0\n\t"
    "s_nop 0\n\t"
    "s_nop 0\n\t"
    "s_nop 0\n\t"
    "s_nop 0\n\t"
    "L_loop_%=:\n\t"
    "s_waitcnt lgkmcnt(8)\n\t"
    "v_smfmac_f32_16x16x64_bf16 v[116:119], v[120:123], v[84:91], v124\n\t"
    "ds_read_b64 v[84:85], v104 offset:0\n\t"
    "ds_read_b64 v[86:87], v126 offset:0\n\t"
    "v_add_u32_sdwa v90, s92, v105 dst_sel:DWORD dst_unused:UNUSED_PAD src0_sel:DWORD src1_sel:WORD_1\n\t"
    "v_add_u32_sdwa v105, s92, v105 dst_sel:DWORD dst_unused:UNUSED_PAD src0_sel:DWORD src1_sel:WORD_0\n\t"
    "ds_read_b64 v[88:89], v105 offset:0\n\t"
    "ds_read_b64 v[90:91], v90 offset:0\n\t"
    "v_add_u32_sdwa v126, s92, v106 dst_sel:DWORD dst_unused:UNUSED_PAD src0_sel:DWORD src1_sel:WORD_1\n\t"
    "v_add_u32_sdwa v106, s92, v106 dst_sel:DWORD dst_unused:UNUSED_PAD src0_sel:DWORD src1_sel:WORD_0\n\t"
    "s_waitcnt vmcnt(2) lgkmcnt(8)\n\t"
    "v_smfmac_f32_16x16x64_bf16 v[116:119], v[120:123], v[92:99], v124\n\t"
    "ds_read_b64 v[92:93], v106 offset:0\n\t"
    "ds_read_b64 v[94:95], v126 offset:0\n\t"
    "v_add_u32_sdwa v98, s92, v107 dst_sel:DWORD dst_unused:UNUSED_PAD src0_sel:DWORD src1_sel:WORD_1\n\t"
    "v_add_u32_sdwa v107, s92, v107 dst_sel:DWORD dst_unused:UNUSED_PAD src0_sel:DWORD src1_sel:WORD_0\n\t"
    "ds_read_b64 v[96:97], v107 offset:0\n\t"
    "ds_read_b64 v[98:99], v98 offset:0\n\t"
    "buffer_load_dwordx4 v[104:107], v127, %12, s86 offen offset:1024 sc1\n\t"
    "v_add_u32_sdwa v126, s92, v108 dst_sel:DWORD dst_unused:UNUSED_PAD src0_sel:DWORD src1_sel:WORD_1\n\t"
    "v_cvt_pk_bf16_f32 v4, v116, v117\n\t"
    "v_cvt_pk_bf16_f32 v5, v118, v119\n\t"
    "v_and_b32 v119, s67, v7\n\t"
    "v_lshlrev_b32 v118, 16, v7\n\t"
    "v_and_b32 v117, s67, v6\n\t"
    "v_lshlrev_b32 v116, 16, v6\n\t"
    "v_add_u32_sdwa v108, s92, v108 dst_sel:DWORD dst_unused:UNUSED_PAD src0_sel:DWORD src1_sel:WORD_0\n\t"
    "s_waitcnt lgkmcnt(4)\n\t"
    "v_smfmac_f32_16x16x64_bf16 v[116:119], v[120:123], v[84:91], v124\n\t"
    "ds_read_b64 v[84:85], v108 offset:0\n\t"
    "v_mfma_f32_16x16x32_bf16 v[80:83], v[64:67], v[164:167], 0\n\t"
    "ds_read_b64 v[86:87], v126 offset:0\n\t"
    "v_mfma_f32_16x16x32_bf16 v[80:83], v[68:71], v[168:171], v[80:83]\n\t"
    "v_add_u32_sdwa v90, s92, v109 dst_sel:DWORD dst_unused:UNUSED_PAD src0_sel:DWORD src1_sel:WORD_1\n\t"
    "v_add_u32_sdwa v109, s92, v109 dst_sel:DWORD dst_unused:UNUSED_PAD src0_sel:DWORD src1_sel:WORD_0\n\t"
    "ds_read_b64 v[88:89], v109 offset:0\n\t"
    "v_mfma_f32_16x16x32_bf16 v[80:83], v[72:75], v[220:223], v[80:83]\n\t"
    "ds_read_b64 v[90:91], v90 offset:0\n\t"
    "v_mfma_f32_16x16x32_bf16 v[80:83], v[76:79], v[224:227], v[80:83]\n\t"
    "v_add_u32_sdwa v126, s92, v110 dst_sel:DWORD dst_unused:UNUSED_PAD src0_sel:DWORD src1_sel:WORD_1\n\t"
    "v_add_u32_sdwa v110, s92, v110 dst_sel:DWORD dst_unused:UNUSED_PAD src0_sel:DWORD src1_sel:WORD_0\n\t"
    "s_waitcnt vmcnt(2) lgkmcnt(4)\n\t"
    "v_smfmac_f32_16x16x64_bf16 v[116:119], v[120:123], v[92:99], v124\n\t"
    "ds_read_b64 v[92:93], v110 offset:0\n\t"
    "ds_read_b64 v[94:95], v126 offset:0\n\t"
    "v_add_u32_sdwa v98, s92, v111 dst_sel:DWORD dst_unused:UNUSED_PAD src0_sel:DWORD src1_sel:WORD_1\n\t"
    "v_add_u32_sdwa v111, s92, v111 dst_sel:DWORD dst_unused:UNUSED_PAD src0_sel:DWORD src1_sel:WORD_0\n\t"
    "ds_read_b64 v[96:97], v111 offset:0\n\t"
    "ds_read_b64 v[98:99], v98 offset:0\n\t"
    "s_add_u32 s87, s87, s95\n\t"
    "buffer_load_dwordx4 v[108:111], v127, %12, s87 offen offset:0 sc1\n\t"
    "v_add_u32_sdwa v126, s92, v112 dst_sel:DWORD dst_unused:UNUSED_PAD src0_sel:DWORD src1_sel:WORD_1\n\t"
    "v_cvt_pk_bf16_f32 v6, v116, v117\n\t"
    "v_cvt_pk_bf16_f32 v7, v118, v119\n\t"
    "v_and_b32 v119, s67, v13\n\t"
    "v_lshlrev_b32 v118, 16, v13\n\t"
    "v_and_b32 v117, s67, v12\n\t"
    "v_lshlrev_b32 v116, 16, v12\n\t"
    "v_add_u32_sdwa v112, s92, v112 dst_sel:DWORD dst_unused:UNUSED_PAD src0_sel:DWORD src1_sel:WORD_0\n\t"
    "s_waitcnt lgkmcnt(4)\n\t"
    "v_smfmac_f32_16x16x64_bf16 v[116:119], v[120:123], v[84:91], v124\n\t"
    "ds_read_b64 v[84:85], v112 offset:0\n\t"
    "ds_read_b64 v[86:87], v126 offset:0\n\t"
    "v_cvt_pk_bf16_f32 v80, v80, v81\n\t"
    "v_cvt_pk_bf16_f32 v81, v82, v83\n\t"
    "ds_write_b64 v125, v[80:81] offset:0\n\t"
    "v_mfma_f32_16x16x32_bf16 v[80:83], v[64:67], v[172:175], 0\n\t"
    "v_add_u32_sdwa v90, s92, v113 dst_sel:DWORD dst_unused:UNUSED_PAD src0_sel:DWORD src1_sel:WORD_1\n\t"
    "v_add_u32_sdwa v113, s92, v113 dst_sel:DWORD dst_unused:UNUSED_PAD src0_sel:DWORD src1_sel:WORD_0\n\t"
    "ds_read_b64 v[88:89], v113 offset:0\n\t"
    "v_mfma_f32_16x16x32_bf16 v[80:83], v[68:71], v[176:179], v[80:83]\n\t"
    "ds_read_b64 v[90:91], v90 offset:0\n\t"
    "v_mfma_f32_16x16x32_bf16 v[80:83], v[72:75], v[228:231], v[80:83]\n\t"
    "v_mfma_f32_16x16x32_bf16 v[80:83], v[76:79], v[232:235], v[80:83]\n\t"
    "v_add_u32_sdwa v126, s92, v114 dst_sel:DWORD dst_unused:UNUSED_PAD src0_sel:DWORD src1_sel:WORD_1\n\t"
    "v_add_u32_sdwa v114, s92, v114 dst_sel:DWORD dst_unused:UNUSED_PAD src0_sel:DWORD src1_sel:WORD_0\n\t"
    "s_waitcnt vmcnt(2) lgkmcnt(5)\n\t"
    "v_smfmac_f32_16x16x64_bf16 v[116:119], v[120:123], v[92:99], v124\n\t"
    "ds_read_b64 v[92:93], v114 offset:0\n\t"
    "ds_read_b64 v[94:95], v126 offset:0\n\t"
    "v_add_u32_sdwa v98, s92, v115 dst_sel:DWORD dst_unused:UNUSED_PAD src0_sel:DWORD src1_sel:WORD_1\n\t"
    "v_add_u32_sdwa v115, s92, v115 dst_sel:DWORD dst_unused:UNUSED_PAD src0_sel:DWORD src1_sel:WORD_0\n\t"
    "ds_read_b64 v[96:97], v115 offset:0\n\t"
    "ds_read_b64 v[98:99], v98 offset:0\n\t"
    "buffer_load_dwordx4 v[112:115], v127, %12, s87 offen offset:1024 sc1\n\t"
    "v_add_u32_sdwa v126, s92, v100 dst_sel:DWORD dst_unused:UNUSED_PAD src0_sel:DWORD src1_sel:WORD_1\n\t"
    "v_cvt_pk_bf16_f32 v12, v116, v117\n\t"
    "v_cvt_pk_bf16_f32 v13, v118, v119\n\t"
    "v_and_b32 v119, s67, v15\n\t"
    "v_lshlrev_b32 v118, 16, v15\n\t"
    "v_and_b32 v117, s67, v14\n\t"
    "v_lshlrev_b32 v116, 16, v14\n\t"
    "v_add_u32_sdwa v100, s92, v100 dst_sel:DWORD dst_unused:UNUSED_PAD src0_sel:DWORD src1_sel:WORD_0\n\t"
    "s_waitcnt lgkmcnt(4)\n\t"
    "v_smfmac_f32_16x16x64_bf16 v[116:119], v[120:123], v[84:91], v124\n\t"
    "ds_read_b64 v[84:85], v100 offset:0\n\t"
    "ds_read_b64 v[86:87], v126 offset:0\n\t"
    "v_cvt_pk_bf16_f32 v80, v80, v81\n\t"
    "v_cvt_pk_bf16_f32 v81, v82, v83\n\t"
    "ds_write_b64 v125, v[80:81] offset:128\n\t"
    "v_mfma_f32_16x16x32_bf16 v[80:83], v[64:67], v[180:183], 0\n\t"
    "v_add_u32_sdwa v90, s92, v101 dst_sel:DWORD dst_unused:UNUSED_PAD src0_sel:DWORD src1_sel:WORD_1\n\t"
    "v_add_u32_sdwa v101, s92, v101 dst_sel:DWORD dst_unused:UNUSED_PAD src0_sel:DWORD src1_sel:WORD_0\n\t"
    "ds_read_b64 v[88:89], v101 offset:0\n\t"
    "v_mfma_f32_16x16x32_bf16 v[80:83], v[68:71], v[184:187], v[80:83]\n\t"
    "ds_read_b64 v[90:91], v90 offset:0\n\t"
    "v_mfma_f32_16x16x32_bf16 v[80:83], v[72:75], v[236:239], v[80:83]\n\t"
    "v_mfma_f32_16x16x32_bf16 v[80:83], v[76:79], v[240:243], v[80:83]\n\t"
    "v_add_u32_sdwa v126, s92, v102 dst_sel:DWORD dst_unused:UNUSED_PAD src0_sel:DWORD src1_sel:WORD_1\n\t"
    "v_add_u32_sdwa v102, s92, v102 dst_sel:DWORD dst_unused:UNUSED_PAD src0_sel:DWORD src1_sel:WORD_0\n\t"
    "s_waitcnt vmcnt(2) lgkmcnt(5)\n\t"
    "v_smfmac_f32_16x16x64_bf16 v[116:119], v[120:123], v[92:99], v124\n\t"
    "ds_read_b64 v[92:93], v102 offset:0\n\t"
    "ds_read_b64 v[94:95], v126 offset:0\n\t"
    "v_add_u32_sdwa v98, s92, v103 dst_sel:DWORD dst_unused:UNUSED_PAD src0_sel:DWORD src1_sel:WORD_1\n\t"
    "v_add_u32_sdwa v103, s92, v103 dst_sel:DWORD dst_unused:UNUSED_PAD src0_sel:DWORD src1_sel:WORD_0\n\t"
    "ds_read_b64 v[96:97], v103 offset:0\n\t"
    "ds_read_b64 v[98:99], v98 offset:0\n\t"
    "s_add_u32 s88, s88, s95\n\t"
    "buffer_load_dwordx4 v[100:103], v127, %12, s88 offen offset:0 sc1\n\t"
    "v_add_u32_sdwa v126, s92, v104 dst_sel:DWORD dst_unused:UNUSED_PAD src0_sel:DWORD src1_sel:WORD_1\n\t"
    "v_cvt_pk_bf16_f32 v14, v116, v117\n\t"
    "v_cvt_pk_bf16_f32 v15, v118, v119\n\t"
    "v_and_b32 v119, s67, v21\n\t"
    "v_lshlrev_b32 v118, 16, v21\n\t"
    "v_and_b32 v117, s67, v20\n\t"
    "v_lshlrev_b32 v116, 16, v20\n\t"
    "v_add_u32_sdwa v104, s92, v104 dst_sel:DWORD dst_unused:UNUSED_PAD src0_sel:DWORD src1_sel:WORD_0\n\t"
    "s_waitcnt lgkmcnt(4)\n\t"
    "v_smfmac_f32_16x16x64_bf16 v[116:119], v[120:123], v[84:91], v124\n\t"
    "ds_read_b64 v[84:85], v104 offset:0\n\t"
    "ds_read_b64 v[86:87], v126 offset:0\n\t"
    "v_cvt_pk_bf16_f32 v80, v80, v81\n\t"
    "v_cvt_pk_bf16_f32 v81, v82, v83\n\t"
    "ds_write_b64 v125, v[80:81] offset:256\n\t"
    "v_add_u32_sdwa v90, s92, v105 dst_sel:DWORD dst_unused:UNUSED_PAD src0_sel:DWORD src1_sel:WORD_1\n\t"
    "v_add_u32_sdwa v105, s92, v105 dst_sel:DWORD dst_unused:UNUSED_PAD src0_sel:DWORD src1_sel:WORD_0\n\t"
    "ds_read_b64 v[88:89], v105 offset:0\n\t"
    "ds_read_b64 v[90:91], v90 offset:0\n\t"
    "v_add_u32_sdwa v126, s92, v106 dst_sel:DWORD dst_unused:UNUSED_PAD src0_sel:DWORD src1_sel:WORD_1\n\t"
    "v_add_u32_sdwa v106, s92, v106 dst_sel:DWORD dst_unused:UNUSED_PAD src0_sel:DWORD src1_sel:WORD_0\n\t"
    "s_waitcnt vmcnt(2) lgkmcnt(5)\n\t"
    "v_smfmac_f32_16x16x64_bf16 v[116:119], v[120:123], v[92:99], v124\n\t"
    "ds_read_b64 v[92:93], v106 offset:0\n\t"
    "ds_read_b64 v[94:95], v126 offset:0\n\t"
    "v_add_u32_sdwa v98, s92, v107 dst_sel:DWORD dst_unused:UNUSED_PAD src0_sel:DWORD src1_sel:WORD_1\n\t"
    "v_add_u32_sdwa v107, s92, v107 dst_sel:DWORD dst_unused:UNUSED_PAD src0_sel:DWORD src1_sel:WORD_0\n\t"
    "ds_read_b64 v[96:97], v107 offset:0\n\t"
    "ds_read_b64 v[98:99], v98 offset:0\n\t"
    "buffer_load_dwordx4 v[104:107], v127, %12, s88 offen offset:1024 sc1\n\t"
    "v_add_u32_sdwa v126, s92, v108 dst_sel:DWORD dst_unused:UNUSED_PAD src0_sel:DWORD src1_sel:WORD_1\n\t"
    "v_cvt_pk_bf16_f32 v20, v116, v117\n\t"
    "v_cvt_pk_bf16_f32 v21, v118, v119\n\t"
    "v_and_b32 v119, s67, v23\n\t"
    "v_lshlrev_b32 v118, 16, v23\n\t"
    "v_and_b32 v117, s67, v22\n\t"
    "v_lshlrev_b32 v116, 16, v22\n\t"
    "v_add_u32_sdwa v108, s92, v108 dst_sel:DWORD dst_unused:UNUSED_PAD src0_sel:DWORD src1_sel:WORD_0\n\t"
    "s_waitcnt lgkmcnt(4)\n\t"
    "v_smfmac_f32_16x16x64_bf16 v[116:119], v[120:123], v[84:91], v124\n\t"
    "ds_read_b64 v[84:85], v108 offset:0\n\t"
    "v_mfma_f32_16x16x32_bf16 v[80:83], v[64:67], v[188:191], 0\n\t"
    "ds_read_b64 v[86:87], v126 offset:0\n\t"
    "v_mfma_f32_16x16x32_bf16 v[80:83], v[68:71], v[192:195], v[80:83]\n\t"
    "v_add_u32_sdwa v90, s92, v109 dst_sel:DWORD dst_unused:UNUSED_PAD src0_sel:DWORD src1_sel:WORD_1\n\t"
    "v_add_u32_sdwa v109, s92, v109 dst_sel:DWORD dst_unused:UNUSED_PAD src0_sel:DWORD src1_sel:WORD_0\n\t"
    "ds_read_b64 v[88:89], v109 offset:0\n\t"
    "v_mfma_f32_16x16x32_bf16 v[80:83], v[72:75], v[244:247], v[80:83]\n\t"
    "ds_read_b64 v[90:91], v90 offset:0\n\t"
    "v_mfma_f32_16x16x32_bf16 v[80:83], v[76:79], v[248:251], v[80:83]\n\t"
    "v_add_u32_sdwa v126, s92, v110 dst_sel:DWORD dst_unused:UNUSED_PAD src0_sel:DWORD src1_sel:WORD_1\n\t"
    "v_add_u32_sdwa v110, s92, v110 dst_sel:DWORD dst_unused:UNUSED_PAD src0_sel:DWORD src1_sel:WORD_0\n\t"
    "s_waitcnt vmcnt(2) lgkmcnt(4)\n\t"
    "v_smfmac_f32_16x16x64_bf16 v[116:119], v[120:123], v[92:99], v124\n\t"
    "ds_read_b64 v[92:93], v110 offset:0\n\t"
    "ds_read_b64 v[94:95], v126 offset:0\n\t"
    "v_add_u32_sdwa v98, s92, v111 dst_sel:DWORD dst_unused:UNUSED_PAD src0_sel:DWORD src1_sel:WORD_1\n\t"
    "v_add_u32_sdwa v111, s92, v111 dst_sel:DWORD dst_unused:UNUSED_PAD src0_sel:DWORD src1_sel:WORD_0\n\t"
    "ds_read_b64 v[96:97], v111 offset:0\n\t"
    "ds_read_b64 v[98:99], v98 offset:0\n\t"
    "s_add_u32 s89, s89, s95\n\t"
    "buffer_load_dwordx4 v[108:111], v127, %12, s89 offen offset:0 sc1\n\t"
    "v_add_u32_sdwa v126, s92, v112 dst_sel:DWORD dst_unused:UNUSED_PAD src0_sel:DWORD src1_sel:WORD_1\n\t"
    "v_cvt_pk_bf16_f32 v22, v116, v117\n\t"
    "v_cvt_pk_bf16_f32 v23, v118, v119\n\t"
    "v_and_b32 v119, s67, v29\n\t"
    "v_lshlrev_b32 v118, 16, v29\n\t"
    "v_and_b32 v117, s67, v28\n\t"
    "v_lshlrev_b32 v116, 16, v28\n\t"
    "v_add_u32_sdwa v112, s92, v112 dst_sel:DWORD dst_unused:UNUSED_PAD src0_sel:DWORD src1_sel:WORD_0\n\t"
    "s_waitcnt lgkmcnt(4)\n\t"
    "v_smfmac_f32_16x16x64_bf16 v[116:119], v[120:123], v[84:91], v124\n\t"
    "ds_read_b64 v[84:85], v112 offset:0\n\t"
    "ds_read_b64 v[86:87], v126 offset:0\n\t"
    "v_cvt_pk_bf16_f32 v80, v80, v81\n\t"
    "v_cvt_pk_bf16_f32 v81, v82, v83\n\t"
    "ds_write_b64 v125, v[80:81] offset:384\n\t"
    "v_add_u32_sdwa v90, s92, v113 dst_sel:DWORD dst_unused:UNUSED_PAD src0_sel:DWORD src1_sel:WORD_1\n\t"
    "v_add_u32_sdwa v113, s92, v113 dst_sel:DWORD dst_unused:UNUSED_PAD src0_sel:DWORD src1_sel:WORD_0\n\t"
    "ds_read_b64 v[88:89], v113 offset:0\n\t"
    "ds_read_b64 v[90:91], v90 offset:0\n\t"
    "v_add_u32_sdwa v126, s92, v114 dst_sel:DWORD dst_unused:UNUSED_PAD src0_sel:DWORD src1_sel:WORD_1\n\t"
    "v_add_u32_sdwa v114, s92, v114 dst_sel:DWORD dst_unused:UNUSED_PAD src0_sel:DWORD src1_sel:WORD_0\n\t"
    "s_waitcnt vmcnt(2) lgkmcnt(5)\n\t"
    "v_smfmac_f32_16x16x64_bf16 v[116:119], v[120:123], v[92:99], v124\n\t"
    "ds_read_b64 v[92:93], v114 offset:0\n\t"
    "ds_read_b64 v[94:95], v126 offset:0\n\t"
    "v_add_u32_sdwa v98, s92, v115 dst_sel:DWORD dst_unused:UNUSED_PAD src0_sel:DWORD src1_sel:WORD_1\n\t"
    "v_add_u32_sdwa v115, s92, v115 dst_sel:DWORD dst_unused:UNUSED_PAD src0_sel:DWORD src1_sel:WORD_0\n\t"
    "ds_read_b64 v[96:97], v115 offset:0\n\t"
    "ds_read_b64 v[98:99], v98 offset:0\n\t"
    "buffer_load_dwordx4 v[112:115], v127, %12, s89 offen offset:1024 sc1\n\t"
    "v_add_u32_sdwa v126, s92, v100 dst_sel:DWORD dst_unused:UNUSED_PAD src0_sel:DWORD src1_sel:WORD_1\n\t"
    "v_cvt_pk_bf16_f32 v28, v116, v117\n\t"
    "v_cvt_pk_bf16_f32 v29, v118, v119\n\t"
    "v_and_b32 v119, s67, v31\n\t"
    "v_lshlrev_b32 v118, 16, v31\n\t"
    "v_and_b32 v117, s67, v30\n\t"
    "v_lshlrev_b32 v116, 16, v30\n\t"
    "v_add_u32_sdwa v100, s92, v100 dst_sel:DWORD dst_unused:UNUSED_PAD src0_sel:DWORD src1_sel:WORD_0\n\t"
    "s_waitcnt lgkmcnt(4)\n\t"
    "v_smfmac_f32_16x16x64_bf16 v[116:119], v[120:123], v[84:91], v124\n\t"
    "ds_read_b64 v[84:85], v100 offset:0\n\t"
    "v_mfma_f32_16x16x32_bf16 v[80:83], v[64:67], v[196:199], 0\n\t"
    "ds_read_b64 v[86:87], v126 offset:0\n\t"
    "v_mfma_f32_16x16x32_bf16 v[80:83], v[68:71], v[200:203], v[80:83]\n\t"
    "v_add_u32_sdwa v90, s92, v101 dst_sel:DWORD dst_unused:UNUSED_PAD src0_sel:DWORD src1_sel:WORD_1\n\t"
    "v_add_u32_sdwa v101, s92, v101 dst_sel:DWORD dst_unused:UNUSED_PAD src0_sel:DWORD src1_sel:WORD_0\n\t"
    "ds_read_b64 v[88:89], v101 offset:0\n\t"
    "v_mfma_f32_16x16x32_bf16 v[80:83], v[72:75], v[252:255], v[80:83]\n\t"
    "ds_read_b64 v[90:91], v90 offset:0\n\t"
    "v_mfma_f32_16x16x32_bf16 v[80:83], v[76:79], v[128:131], v[80:83]\n\t"
    "v_add_u32_sdwa v126, s92, v102 dst_sel:DWORD dst_unused:UNUSED_PAD src0_sel:DWORD src1_sel:WORD_1\n\t"
    "v_add_u32_sdwa v102, s92, v102 dst_sel:DWORD dst_unused:UNUSED_PAD src0_sel:DWORD src1_sel:WORD_0\n\t"
    "s_xor_b32 s50, s50, 0x2000\n\t"
    "s_add_u32 m0, s50, 128000\n\t"
    "s_add_u32 s63, s63, 0x4000\n\t"
    "buffer_load_dwordx4 v127, %12, s63 offen sc1 lds\n\t"
    "s_waitcnt vmcnt(3) lgkmcnt(4)\n\t"
    "v_smfmac_f32_16x16x64_bf16 v[116:119], v[120:123], v[92:99], v124\n\t"
    "ds_read_b64 v[92:93], v102 offset:0\n\t"
    "ds_read_b64 v[94:95], v126 offset:0\n\t"
    "v_add_u32_sdwa v98, s92, v103 dst_sel:DWORD dst_unused:UNUSED_PAD src0_sel:DWORD src1_sel:WORD_1\n\t"
    "v_add_u32_sdwa v103, s92, v103 dst_sel:DWORD dst_unused:UNUSED_PAD src0_sel:DWORD src1_sel:WORD_0\n\t"
    "ds_read_b64 v[96:97], v103 offset:0\n\t"
    "ds_read_b64 v[98:99], v98 offset:0\n\t"
    "s_add_u32 s90, s90, s95\n\t"
    "buffer_load_dwordx4 v[100:103], v127, %12, s90 offen offset:0 sc1\n\t"
    "v_add_u32_sdwa v126, s92, v104 dst_sel:DWORD dst_unused:UNUSED_PAD src0_sel:DWORD src1_sel:WORD_1\n\t"
    "v_cvt_pk_bf16_f32 v30, v116, v117\n\t"
    "v_cvt_pk_bf16_f32 v31, v118, v119\n\t"
    "v_and_b32 v119, s67, v37\n\t"
    "v_lshlrev_b32 v118, 16, v37\n\t"
    "v_and_b32 v117, s67, v36\n\t"
    "v_lshlrev_b32 v116, 16, v36\n\t"
    "v_add_u32_sdwa v104, s92, v104 dst_sel:DWORD dst_unused:UNUSED_PAD src0_sel:DWORD src1_sel:WORD_0\n\t"
    "s_waitcnt lgkmcnt(4)\n\t"
    "v_smfmac_f32_16x16x64_bf16 v[116:119], v[120:123], v[84:91], v124\n\t"
    "ds_read_b64 v[84:85], v104 offset:0\n\t"
    "ds_read_b64 v[86:87], v126 offset:0\n\t"
    "v_cvt_pk_bf16_f32 v80, v80, v81\n\t"
    "v_cvt_pk_bf16_f32 v81, v82, v83\n\t"
    "ds_write_b64 v125, v[80:81] offset:512\n\t"
    "v_add_u32_sdwa v90, s92, v105 dst_sel:DWORD dst_unused:UNUSED_PAD src0_sel:DWORD src1_sel:WORD_1\n\t"
    "v_add_u32_sdwa v105, s92, v105 dst_sel:DWORD dst_unused:UNUSED_PAD src0_sel:DWORD src1_sel:WORD_0\n\t"
    "ds_read_b64 v[88:89], v105 offset:0\n\t"
    "ds_read_b64 v[90:91], v90 offset:0\n\t"
    "v_add_u32_sdwa v126, s92, v106 dst_sel:DWORD dst_unused:UNUSED_PAD src0_sel:DWORD src1_sel:WORD_1\n\t"
    "v_add_u32_sdwa v106, s92, v106 dst_sel:DWORD dst_unused:UNUSED_PAD src0_sel:DWORD src1_sel:WORD_0\n\t"
    "s_waitcnt vmcnt(3) lgkmcnt(5)\n\t"
    "v_smfmac_f32_16x16x64_bf16 v[116:119], v[120:123], v[92:99], v124\n\t"
    "ds_read_b64 v[92:93], v106 offset:0\n\t"
    "ds_read_b64 v[94:95], v126 offset:0\n\t"
    "v_add_u32_sdwa v98, s92, v107 dst_sel:DWORD dst_unused:UNUSED_PAD src0_sel:DWORD src1_sel:WORD_1\n\t"
    "v_add_u32_sdwa v107, s92, v107 dst_sel:DWORD dst_unused:UNUSED_PAD src0_sel:DWORD src1_sel:WORD_0\n\t"
    "ds_read_b64 v[96:97], v107 offset:0\n\t"
    "ds_read_b64 v[98:99], v98 offset:0\n\t"
    "buffer_load_dwordx4 v[104:107], v127, %12, s90 offen offset:1024 sc1\n\t"
    "v_add_u32_sdwa v126, s92, v108 dst_sel:DWORD dst_unused:UNUSED_PAD src0_sel:DWORD src1_sel:WORD_1\n\t"
    "v_cvt_pk_bf16_f32 v36, v116, v117\n\t"
    "v_cvt_pk_bf16_f32 v37, v118, v119\n\t"
    "v_and_b32 v119, s67, v39\n\t"
    "v_lshlrev_b32 v118, 16, v39\n\t"
    "v_and_b32 v117, s67, v38\n\t"
    "v_lshlrev_b32 v116, 16, v38\n\t"
    "v_add_u32_sdwa v108, s92, v108 dst_sel:DWORD dst_unused:UNUSED_PAD src0_sel:DWORD src1_sel:WORD_0\n\t"
    "s_waitcnt lgkmcnt(4)\n\t"
    "v_smfmac_f32_16x16x64_bf16 v[116:119], v[120:123], v[84:91], v124\n\t"
    "ds_read_b64 v[84:85], v108 offset:0\n\t"
    "v_mfma_f32_16x16x32_bf16 v[80:83], v[64:67], v[204:207], 0\n\t"
    "ds_read_b64 v[86:87], v126 offset:0\n\t"
    "v_mfma_f32_16x16x32_bf16 v[80:83], v[68:71], v[208:211], v[80:83]\n\t"
    "v_add_u32_sdwa v90, s92, v109 dst_sel:DWORD dst_unused:UNUSED_PAD src0_sel:DWORD src1_sel:WORD_1\n\t"
    "v_add_u32_sdwa v109, s92, v109 dst_sel:DWORD dst_unused:UNUSED_PAD src0_sel:DWORD src1_sel:WORD_0\n\t"
    "ds_read_b64 v[88:89], v109 offset:0\n\t"
    "v_mfma_f32_16x16x32_bf16 v[80:83], v[72:75], v[132:135], v[80:83]\n\t"
    "ds_read_b64 v[90:91], v90 offset:0\n\t"
    "v_mfma_f32_16x16x32_bf16 v[80:83], v[76:79], v[136:139], v[80:83]\n\t"
    "v_add_u32_sdwa v126, s92, v110 dst_sel:DWORD dst_unused:UNUSED_PAD src0_sel:DWORD src1_sel:WORD_1\n\t"
    "v_add_u32_sdwa v110, s92, v110 dst_sel:DWORD dst_unused:UNUSED_PAD src0_sel:DWORD src1_sel:WORD_0\n\t"
    "s_cmp_lt_u32 %16, 4\n\t"
    "s_cbranch_scc1 L_tp19_%=\n\t"
    "s_setprio 0\n\t"
    "L_tp19_%=:\n\t"
    "s_waitcnt vmcnt(3) lgkmcnt(4)\n\t"
    "v_smfmac_f32_16x16x64_bf16 v[116:119], v[120:123], v[92:99], v124\n\t"
    "ds_read_b64 v[92:93], v110 offset:0\n\t"
    "ds_read_b64 v[94:95], v126 offset:0\n\t"
    "v_add_u32_sdwa v98, s92, v111 dst_sel:DWORD dst_unused:UNUSED_PAD src0_sel:DWORD src1_sel:WORD_1\n\t"
    "v_add_u32_sdwa v111, s92, v111 dst_sel:DWORD dst_unused:UNUSED_PAD src0_sel:DWORD src1_sel:WORD_0\n\t"
    "ds_read_b64 v[96:97], v111 offset:0\n\t"
    "ds_read_b64 v[98:99], v98 offset:0\n\t"
    "s_add_u32 s91, s91, s95\n\t"
    "buffer_load_dwordx4 v[108:111], v127, %12, s91 offen offset:0 sc1\n\t"
    "v_add_u32_sdwa v126, s92, v112 dst_sel:DWORD dst_unused:UNUSED_PAD src0_sel:DWORD src1_sel:WORD_1\n\t"
    "v_cvt_pk_bf16_f32 v38, v116, v117\n\t"
    "v_cvt_pk_bf16_f32 v39, v118, v119\n\t"
    "v_and_b32 v119, s67, v45\n\t"
    "v_lshlrev_b32 v118, 16, v45\n\t"
    "v_and_b32 v117, s67, v44\n\t"
    "v_lshlrev_b32 v116, 16, v44\n\t"
    "v_add_u32_sdwa v112, s92, v112 dst_sel:DWORD dst_unused:UNUSED_PAD src0_sel:DWORD src1_sel:WORD_0\n\t"
    "s_waitcnt lgkmcnt(4)\n\t"
    "v_smfmac_f32_16x16x64_bf16 v[116:119], v[120:123], v[84:91], v124\n\t"
    "ds_read_b64 v[84:85], v112 offset:0\n\t"
    "ds_read_b64 v[86:87], v126 offset:0\n\t"
    "v_cvt_pk_bf16_f32 v80, v80, v81\n\t"
    "v_cvt_pk_bf16_f32 v81, v82, v83\n\t"
    "ds_write_b64 v125, v[80:81] offset:640\n\t"
    "v_add_u32_sdwa v90, s92, v113 dst_sel:DWORD dst_unused:UNUSED_PAD src0_sel:DWORD src1_sel:WORD_1\n\t"
    "v_add_u32_sdwa v113, s92, v113 dst_sel:DWORD dst_unused:UNUSED_PAD src0_sel:DWORD src1_sel:WORD_0\n\t"
    "ds_read_b64 v[88:89], v113 offset:0\n\t"
    "ds_read_b64 v[90:91], v90 offset:0\n\t"
    "v_add_u32_sdwa v126, s92, v114 dst_sel:DWORD dst_unused:UNUSED_PAD src0_sel:DWORD src1_sel:WORD_1\n\t"
    "v_add_u32_sdwa v114, s92, v114 dst_sel:DWORD dst_unused:UNUSED_PAD src0_sel:DWORD src1_sel:WORD_0\n\t"
    "s_waitcnt vmcnt(2) lgkmcnt(5)\n\t"
    "v_smfmac_f32_16x16x64_bf16 v[116:119], v[120:123], v[92:99], v124\n\t"
    "ds_read_b64 v[92:93], v114 offset:0\n\t"
    "ds_read_b64 v[94:95], v126 offset:0\n\t"
    "v_add_u32_sdwa v98, s92, v115 dst_sel:DWORD dst_unused:UNUSED_PAD src0_sel:DWORD src1_sel:WORD_1\n\t"
    "v_add_u32_sdwa v115, s92, v115 dst_sel:DWORD dst_unused:UNUSED_PAD src0_sel:DWORD src1_sel:WORD_0\n\t"
    "ds_read_b64 v[96:97], v115 offset:0\n\t"
    "ds_read_b64 v[98:99], v98 offset:0\n\t"
    "buffer_load_dwordx4 v[112:115], v127, %12, s91 offen offset:1024 sc1\n\t"
    "v_add_u32_sdwa v126, s92, v100 dst_sel:DWORD dst_unused:UNUSED_PAD src0_sel:DWORD src1_sel:WORD_1\n\t"
    "v_cvt_pk_bf16_f32 v44, v116, v117\n\t"
    "v_cvt_pk_bf16_f32 v45, v118, v119\n\t"
    "v_and_b32 v119, s67, v47\n\t"
    "v_lshlrev_b32 v118, 16, v47\n\t"
    "v_and_b32 v117, s67, v46\n\t"
    "v_lshlrev_b32 v116, 16, v46\n\t"
    "v_add_u32_sdwa v100, s92, v100 dst_sel:DWORD dst_unused:UNUSED_PAD src0_sel:DWORD src1_sel:WORD_0\n\t"
    "s_waitcnt lgkmcnt(4)\n\t"
    "v_smfmac_f32_16x16x64_bf16 v[116:119], v[120:123], v[84:91], v124\n\t"
    "ds_read_b64 v[84:85], v100 offset:0\n\t"
    "v_mfma_f32_16x16x32_bf16 v[80:83], v[64:67], v[156:159], 0\n\t"
    "ds_read_b64 v[86:87], v126 offset:0\n\t"
    "v_mfma_f32_16x16x32_bf16 v[80:83], v[68:71], v[160:163], v[80:83]\n\t"
    "v_add_u32_sdwa v90, s92, v101 dst_sel:DWORD dst_unused:UNUSED_PAD src0_sel:DWORD src1_sel:WORD_1\n\t"
    "v_add_u32_sdwa v101, s92, v101 dst_sel:DWORD dst_unused:UNUSED_PAD src0_sel:DWORD src1_sel:WORD_0\n\t"
    "ds_read_b64 v[88:89], v101 offset:0\n\t"
    "v_mfma_f32_16x16x32_bf16 v[80:83], v[72:75], v[140:143], v[80:83]\n\t"
    "ds_read_b64 v[90:91], v90 offset:0\n\t"
    "v_mfma_f32_16x16x32_bf16 v[80:83], v[76:79], v[144:147], v[80:83]\n\t"
    "v_add_u32_sdwa v126, s92, v102 dst_sel:DWORD dst_unused:UNUSED_PAD src0_sel:DWORD src1_sel:WORD_1\n\t"
    "v_add_u32_sdwa v102, s92, v102 dst_sel:DWORD dst_unused:UNUSED_PAD src0_sel:DWORD src1_sel:WORD_0\n\t"
    "s_waitcnt vmcnt(2) lgkmcnt(4)\n\t"
    "v_smfmac_f32_16x16x64_bf16 v[116:119], v[120:123], v[92:99], v124\n\t"
    "ds_read_b64 v[92:93], v102 offset:0\n\t"
    "ds_read_b64 v[94:95], v126 offset:0\n\t"
    "v_add_u32_sdwa v98, s92, v103 dst_sel:DWORD dst_unused:UNUSED_PAD src0_sel:DWORD src1_sel:WORD_1\n\t"
    "v_add_u32_sdwa v103, s92, v103 dst_sel:DWORD dst_unused:UNUSED_PAD src0_sel:DWORD src1_sel:WORD_0\n\t"
    "ds_read_b64 v[96:97], v103 offset:0\n\t"
    "ds_read_b64 v[98:99], v98 offset:0\n\t"
    "s_add_u32 s84, s84, s94\n\t"
    "buffer_load_dwordx4 v[100:103], v127, %12, s84 offen offset:0 sc1\n\t"
    "v_add_u32_sdwa v126, s92, v104 dst_sel:DWORD dst_unused:UNUSED_PAD src0_sel:DWORD src1_sel:WORD_1\n\t"
    "v_cvt_pk_bf16_f32 v46, v116, v117\n\t"
    "v_cvt_pk_bf16_f32 v47, v118, v119\n\t"
    "v_and_b32 v119, s67, v53\n\t"
    "v_lshlrev_b32 v118, 16, v53\n\t"
    "v_and_b32 v117, s67, v52\n\t"
    "v_lshlrev_b32 v116, 16, v52\n\t"
    "v_add_u32_sdwa v104, s92, v104 dst_sel:DWORD dst_unused:UNUSED_PAD src0_sel:DWORD src1_sel:WORD_0\n\t"
    "s_waitcnt lgkmcnt(4)\n\t"
    "v_smfmac_f32_16x16x64_bf16 v[116:119], v[120:123], v[84:91], v124\n\t"
    "ds_read_b64 v[84:85], v104 offset:0\n\t"
    "ds_read_b64 v[86:87], v126 offset:0\n\t"
    "v_cvt_pk_bf16_f32 v80, v80, v81\n\t"
    "v_cvt_pk_bf16_f32 v81, v82, v83\n\t"
    "ds_write_b64 v125, v[80:81] offset:768\n\t"
    "v_add_u32_sdwa v90, s92, v105 dst_sel:DWORD dst_unused:UNUSED_PAD src0_sel:DWORD src1_sel:WORD_1\n\t"
    "v_add_u32_sdwa v105, s92, v105 dst_sel:DWORD dst_unused:UNUSED_PAD src0_sel:DWORD src1_sel:WORD_0\n\t"
    "ds_read_b64 v[88:89], v105 offset:0\n\t"
    "ds_read_b64 v[90:91], v90 offset:0\n\t"
    "v_add_u32_sdwa v126, s92, v106 dst_sel:DWORD dst_unused:UNUSED_PAD src0_sel:DWORD src1_sel:WORD_1\n\t"
    "v_add_u32_sdwa v106, s92, v106 dst_sel:DWORD dst_unused:UNUSED_PAD src0_sel:DWORD src1_sel:WORD_0\n\t"
    "s_waitcnt vmcnt(2) lgkmcnt(5)\n\t"
    "v_smfmac_f32_16x16x64_bf16 v[116:119], v[120:123], v[92:99], v124\n\t"
    "ds_read_b64 v[92:93], v106 offset:0\n\t"
    "ds_read_b64 v[94:95], v126 offset:0\n\t"
    "v_add_u32_sdwa v98, s92, v107 dst_sel:DWORD dst_unused:UNUSED_PAD src0_sel:DWORD src1_sel:WORD_1\n\t"
    "v_add_u32_sdwa v107, s92, v107 dst_sel:DWORD dst_unused:UNUSED_PAD src0_sel:DWORD src1_sel:WORD_0\n\t"
    "ds_read_b64 v[96:97], v107 offset:0\n\t"
    "ds_read_b64 v[98:99], v98 offset:0\n\t"
    "buffer_load_dwordx4 v[104:107], v127, %12, s84 offen offset:1024 sc1\n\t"
    "v_add_u32_sdwa v126, s92, v108 dst_sel:DWORD dst_unused:UNUSED_PAD src0_sel:DWORD src1_sel:WORD_1\n\t"
    "v_cvt_pk_bf16_f32 v52, v116, v117\n\t"
    "v_cvt_pk_bf16_f32 v53, v118, v119\n\t"
    "v_and_b32 v119, s67, v55\n\t"
    "v_lshlrev_b32 v118, 16, v55\n\t"
    "v_and_b32 v117, s67, v54\n\t"
    "v_lshlrev_b32 v116, 16, v54\n\t"
    "v_add_u32_sdwa v108, s92, v108 dst_sel:DWORD dst_unused:UNUSED_PAD src0_sel:DWORD src1_sel:WORD_0\n\t"
    "s_waitcnt lgkmcnt(4)\n\t"
    "v_smfmac_f32_16x16x64_bf16 v[116:119], v[120:123], v[84:91], v124\n\t"
    "ds_read_b64 v[84:85], v108 offset:0\n\t"
    "v_mfma_f32_16x16x32_bf16 v[80:83], v[64:67], v[212:215], 0\n\t"
    "ds_read_b64 v[86:87], v126 offset:0\n\t"
    "v_mfma_f32_16x16x32_bf16 v[80:83], v[68:71], v[216:219], v[80:83]\n\t"
    "v_add_u32_sdwa v90, s92, v109 dst_sel:DWORD dst_unused:UNUSED_PAD src0_sel:DWORD src1_sel:WORD_1\n\t"
    "v_add_u32_sdwa v109, s92, v109 dst_sel:DWORD dst_unused:UNUSED_PAD src0_sel:DWORD src1_sel:WORD_0\n\t"
    "ds_read_b64 v[88:89], v109 offset:0\n\t"
    "v_mfma_f32_16x16x32_bf16 v[80:83], v[72:75], v[148:151], v[80:83]\n\t"
    "ds_read_b64 v[90:91], v90 offset:0\n\t"
    "v_mfma_f32_16x16x32_bf16 v[80:83], v[76:79], v[152:155], v[80:83]\n\t"
    "v_add_u32 v79, s96, v127\n\t"
    "s_add_u32 s96, s96, 0x1000\n\t"
    "s_and_b32 s96, s96, 0x3000\n\t"
    "v_add_u32 v79, 128000, v79\n\t"
    "ds_read_b128 v[64:67], v79 offset:0\n\t"
    "ds_read_b128 v[68:71], v79 offset:1024\n\t"
    "ds_read_b128 v[72:75], v79 offset:2048\n\t"
    "ds_read_b128 v[76:79], v79 offset:3072\n\t"
    "v_add_u32_sdwa v126, s92, v110 dst_sel:DWORD dst_unused:UNUSED_PAD src0_sel:DWORD src1_sel:WORD_1\n\t"
    "v_add_u32_sdwa v110, s92, v110 dst_sel:DWORD dst_unused:UNUSED_PAD src0_sel:DWORD src1_sel:WORD_0\n\t"
    "s_waitcnt vmcnt(2) lgkmcnt(8)\n\t"
    "v_smfmac_f32_16x16x64_bf16 v[116:119], v[120:123], v[92:99], v124\n\t"
    "ds_read_b64 v[92:93], v110 offset:0\n\t"
    "ds_read_b64 v[94:95], v126 offset:0\n\t"
    "v_add_u32_sdwa v98, s92, v111 dst_sel:DWORD dst_unused:UNUSED_PAD src0_sel:DWORD src1_sel:WORD_1\n\t"
    "v_add_u32_sdwa v111, s92, v111 dst_sel:DWORD dst_unused:UNUSED_PAD src0_sel:DWORD src1_sel:WORD_0\n\t"
    "ds_read_b64 v[96:97], v111 offset:0\n\t"
    "ds_read_b64 v[98:99], v98 offset:0\n\t"
    "s_add_u32 s85, s85, s94\n\t"
    "buffer_load_dwordx4 v[108:111], v127, %12, s85 offen offset:0 sc1\n\t"
    "v_add_u32_sdwa v126, s92, v112 dst_sel:DWORD dst_unused:UNUSED_PAD src0_sel:DWORD src1_sel:WORD_1\n\t"
    "v_cvt_pk_bf16_f32 v54, v116, v117\n\t"
    "v_cvt_pk_bf16_f32 v55, v118, v119\n\t"
    "v_and_b32 v119, s67, v61\n\t"
    "v_lshlrev_b32 v118, 16, v61\n\t"
    "v_and_b32 v117, s67, v60\n\t"
    "v_lshlrev_b32 v116, 16, v60\n\t"
    "v_add_u32_sdwa v112, s92, v112 dst_sel:DWORD dst_unused:UNUSED_PAD src0_sel:DWORD src1_sel:WORD_0\n\t"
    "s_waitcnt lgkmcnt(8)\n\t"
    "v_smfmac_f32_16x16x64_bf16 v[116:119], v[120:123], v[84:91], v124\n\t"
    "ds_read_b64 v[84:85], v112 offset:0\n\t"
    "ds_read_b64 v[86:87], v126 offset:0\n\t"
    "v_cvt_pk_bf16_f32 v80, v80, v81\n\t"
    "v_cvt_pk_bf16_f32 v81, v82, v83\n\t"
    "ds_write_b64 v125, v[80:81] offset:872\n\t"
    "v_add_u32_sdwa v90, s92, v113 dst_sel:DWORD dst_unused:UNUSED_PAD src0_sel:DWORD src1_sel:WORD_1\n\t"
    "v_add_u32_sdwa v113, s92, v113 dst_sel:DWORD dst_unused:UNUSED_PAD src0_sel:DWORD src1_sel:WORD_0\n\t"
    "ds_read_b64 v[88:89], v113 offset:0\n\t"
    "ds_read_b64 v[90:91], v90 offset:0\n\t"
    "v_add_u32_sdwa v126, s92, v114 dst_sel:DWORD dst_unused:UNUSED_PAD src0_sel:DWORD src1_sel:WORD_1\n\t"
    "v_add_u32_sdwa v114, s92, v114 dst_sel:DWORD dst_unused:UNUSED_PAD src0_sel:DWORD src1_sel:WORD_0\n\t"
    "s_waitcnt vmcnt(2) lgkmcnt(5)\n\t"
    "v_smfmac_f32_16x16x64_bf16 v[116:119], v[120:123], v[92:99], v124\n\t"
    "ds_read_b64 v[92:93], v114 offset:0\n\t"
    "ds_read_b64 v[94:95], v126 offset:0\n\t"
    "v_add_u32_sdwa v98, s92, v115 dst_sel:DWORD dst_unused:UNUSED_PAD src0_sel:DWORD src1_sel:WORD_1\n\t"
    "v_add_u32_sdwa v115, s92, v115 dst_sel:DWORD dst_unused:UNUSED_PAD src0_sel:DWORD src1_sel:WORD_0\n\t"
    "ds_read_b64 v[96:97], v115 offset:0\n\t"
    "ds_read_b64 v[98:99], v98 offset:0\n\t"
    "buffer_load_dwordx4 v[112:115], v127, %12, s85 offen offset:1024 sc1\n\t"
    "v_add_u32_sdwa v126, s92, v100 dst_sel:DWORD dst_unused:UNUSED_PAD src0_sel:DWORD src1_sel:WORD_1\n\t"
    "v_cvt_pk_bf16_f32 v60, v116, v117\n\t"
    "v_cvt_pk_bf16_f32 v61, v118, v119\n\t"
    "v_and_b32 v119, s67, v63\n\t"
    "v_lshlrev_b32 v118, 16, v63\n\t"
    "v_and_b32 v117, s67, v62\n\t"
    "v_lshlrev_b32 v116, 16, v62\n\t"
    "v_add_u32_sdwa v100, s92, v100 dst_sel:DWORD dst_unused:UNUSED_PAD src0_sel:DWORD src1_sel:WORD_0\n\t"
    "s_waitcnt lgkmcnt(4)\n\t"
    "v_smfmac_f32_16x16x64_bf16 v[116:119], v[120:123], v[84:91], v124\n\t"
    "ds_read_b64 v[84:85], v100 offset:0\n\t"
    "ds_read_b64 v[86:87], v126 offset:0\n\t"
    "v_add_u32_sdwa v90, s92, v101 dst_sel:DWORD dst_unused:UNUSED_PAD src0_sel:DWORD src1_sel:WORD_1\n\t"
    "v_add_u32_sdwa v101, s92, v101 dst_sel:DWORD dst_unused:UNUSED_PAD src0_sel:DWORD src1_sel:WORD_0\n\t"
    "ds_read_b64 v[88:89], v101 offset:0\n\t"
    "ds_read_b64 v[90:91], v90 offset:0\n\t"
    "v_add_u32_sdwa v126, s92, v102 dst_sel:DWORD dst_unused:UNUSED_PAD src0_sel:DWORD src1_sel:WORD_1\n\t"
    "v_add_u32_sdwa v102, s92, v102 dst_sel:DWORD dst_unused:UNUSED_PAD src0_sel:DWORD src1_sel:WORD_0\n\t"
    "s_cmp_lt_u32 %16, 4\n\t"
    "s_cbranch_scc1 L_tp31_%=\n\t"
    "s_setprio 1\n\t"
    "L_tp31_%=:\n\t"
    "s_waitcnt vmcnt(2) lgkmcnt(4)\n\t"
    "v_smfmac_f32_16x16x64_bf16 v[116:119], v[120:123], v[92:99], v124\n\t"
    "ds_read_b64 v[92:93], v102 offset:0\n\t"
    "ds_read_b64 v[94:95], v126 offset:0\n\t"
    "v_add_u32_sdwa v98, s92, v103 dst_sel:DWORD dst_unused:UNUSED_PAD src0_sel:DWORD src1_sel:WORD_1\n\t"
    "v_add_u32_sdwa v103, s92, v103 dst_sel:DWORD dst_unused:UNUSED_PAD src0_sel:DWORD src1_sel:WORD_0\n\t"
    "ds_read_b64 v[96:97], v103 offset:0\n\t"
    "ds_read_b64 v[98:99], v98 offset:0\n\t"
    "s_add_u32 s86, s86, s94\n\t"
    "buffer_load_dwordx4 v[100:103], v127, %12, s86 offen offset:0 sc1\n\t"
    "v_add_u32_sdwa v126, s92, v104 dst_sel:DWORD dst_unused:UNUSED_PAD src0_sel:DWORD src1_sel:WORD_1\n\t"
    "v_cvt_pk_bf16_f32 v62, v116, v117\n\t"
    "v_cvt_pk_bf16_f32 v63, v118, v119\n\t"
    "v_and_b32 v119, s67, v1\n\t"
    "v_lshlrev_b32 v118, 16, v1\n\t"
    "v_and_b32 v117, s67, v0\n\t"
    "v_lshlrev_b32 v116, 16, v0\n\t"
    "v_add_u32_sdwa v104, s92, v104 dst_sel:DWORD dst_unused:UNUSED_PAD src0_sel:DWORD src1_sel:WORD_0\n\t"
    "s_waitcnt lgkmcnt(4)\n\t"
    "v_smfmac_f32_16x16x64_bf16 v[116:119], v[120:123], v[84:91], v124\n\t"
    "ds_read_b64 v[84:85], v104 offset:0\n\t"
    "v_mfma_f32_16x16x32_bf16 v[80:83], v[64:67], v[164:167], 0\n\t"
    "ds_read_b64 v[86:87], v126 offset:0\n\t"
    "v_mfma_f32_16x16x32_bf16 v[80:83], v[68:71], v[168:171], v[80:83]\n\t"
    "v_add_u32_sdwa v90, s92, v105 dst_sel:DWORD dst_unused:UNUSED_PAD src0_sel:DWORD src1_sel:WORD_1\n\t"
    "v_add_u32_sdwa v105, s92, v105 dst_sel:DWORD dst_unused:UNUSED_PAD src0_sel:DWORD src1_sel:WORD_0\n\t"
    "ds_read_b64 v[88:89], v105 offset:0\n\t"
    "v_mfma_f32_16x16x32_bf16 v[80:83], v[72:75], v[220:223], v[80:83]\n\t"
    "ds_read_b64 v[90:91], v90 offset:0\n\t"
    "v_mfma_f32_16x16x32_bf16 v[80:83], v[76:79], v[224:227], v[80:83]\n\t"
    "v_add_u32_sdwa v126, s92, v106 dst_sel:DWORD dst_unused:UNUSED_PAD src0_sel:DWORD src1_sel:WORD_1\n\t"
    "v_add_u32_sdwa v106, s92, v106 dst_sel:DWORD dst_unused:UNUSED_PAD src0_sel:DWORD src1_sel:WORD_0\n\t"
    "s_waitcnt vmcnt(2) lgkmcnt(4)\n\t"
    "v_smfmac_f32_16x16x64_bf16 v[116:119], v[120:123], v[92:99], v124\n\t"
    "ds_read_b64 v[92:93], v106 offset:0\n\t"
    "ds_read_b64 v[94:95], v126 offset:0\n\t"
    "v_add_u32_sdwa v98, s92, v107 dst_sel:DWORD dst_unused:UNUSED_PAD src0_sel:DWORD src1_sel:WORD_1\n\t"
    "v_add_u32_sdwa v107, s92, v107 dst_sel:DWORD dst_unused:UNUSED_PAD src0_sel:DWORD src1_sel:WORD_0\n\t"
    "ds_read_b64 v[96:97], v107 offset:0\n\t"
    "ds_read_b64 v[98:99], v98 offset:0\n\t"
    "buffer_load_dwordx4 v[104:107], v127, %12, s86 offen offset:1024 sc1\n\t"
    "v_add_u32_sdwa v126, s92, v108 dst_sel:DWORD dst_unused:UNUSED_PAD src0_sel:DWORD src1_sel:WORD_1\n\t"
    "v_cvt_pk_bf16_f32 v0, v116, v117\n\t"
    "v_cvt_pk_bf16_f32 v1, v118, v119\n\t"
    "v_and_b32 v119, s67, v3\n\t"
    "v_lshlrev_b32 v118, 16, v3\n\t"
    "v_and_b32 v117, s67, v2\n\t"
    "v_lshlrev_b32 v116, 16, v2\n\t"
    "v_add_u32_sdwa v108, s92, v108 dst_sel:DWORD dst_unused:UNUSED_PAD src0_sel:DWORD src1_sel:WORD_0\n\t"
    "s_waitcnt lgkmcnt(4)\n\t"
    "v_smfmac_f32_16x16x64_bf16 v[116:119], v[120:123], v[84:91], v124\n\t"
    "ds_read_b64 v[84:85], v108 offset:0\n\t"
    "ds_read_b64 v[86:87], v126 offset:0\n\t"
    "v_cvt_pk_bf16_f32 v80, v80, v81\n\t"
    "v_cvt_pk_bf16_f32 v81, v82, v83\n\t"
    "ds_write_b64 v125, v[80:81] offset:32000\n\t"
    "v_mfma_f32_16x16x32_bf16 v[80:83], v[64:67], v[172:175], 0\n\t"
    "v_add_u32_sdwa v90, s92, v109 dst_sel:DWORD dst_unused:UNUSED_PAD src0_sel:DWORD src1_sel:WORD_1\n\t"
    "v_add_u32_sdwa v109, s92, v109 dst_sel:DWORD dst_unused:UNUSED_PAD src0_sel:DWORD src1_sel:WORD_0\n\t"
    "ds_read_b64 v[88:89], v109 offset:0\n\t"
    "v_mfma_f32_16x16x32_bf16 v[80:83], v[68:71], v[176:179], v[80:83]\n\t"
    "ds_read_b64 v[90:91], v90 offset:0\n\t"
    "v_mfma_f32_16x16x32_bf16 v[80:83], v[72:75], v[228:231], v[80:83]\n\t"
    "v_mfma_f32_16x16x32_bf16 v[80:83], v[76:79], v[232:235], v[80:83]\n\t"
    "v_add_u32_sdwa v126, s92, v110 dst_sel:DWORD dst_unused:UNUSED_PAD src0_sel:DWORD src1_sel:WORD_1\n\t"
    "v_add_u32_sdwa v110, s92, v110 dst_sel:DWORD dst_unused:UNUSED_PAD src0_sel:DWORD src1_sel:WORD_0\n\t"
    "s_waitcnt vmcnt(2) lgkmcnt(5)\n\t"
    "v_smfmac_f32_16x16x64_bf16 v[116:119], v[120:123], v[92:99], v124\n\t"
    "ds_read_b64 v[92:93], v110 offset:0\n\t"
    "ds_read_b64 v[94:95], v126 offset:0\n\t"
    "v_add_u32_sdwa v98, s92, v111 dst_sel:DWORD dst_unused:UNUSED_PAD src0_sel:DWORD src1_sel:WORD_1\n\t"
    "v_add_u32_sdwa v111, s92, v111 dst_sel:DWORD dst_unused:UNUSED_PAD src0_sel:DWORD src1_sel:WORD_0\n\t"
    "ds_read_b64 v[96:97], v111 offset:0\n\t"
    "ds_read_b64 v[98:99], v98 offset:0\n\t"
    "s_add_u32 s87, s87, s94\n\t"
    "buffer_load_dwordx4 v[108:111], v127, %12, s87 offen offset:0 sc1\n\t"
    "v_add_u32_sdwa v126, s92, v112 dst_sel:DWORD dst_unused:UNUSED_PAD src0_sel:DWORD src1_sel:WORD_1\n\t"
    "v_cvt_pk_bf16_f32 v2, v116, v117\n\t"
    "v_cvt_pk_bf16_f32 v3, v118, v119\n\t"
    "v_and_b32 v119, s67, v9\n\t"
    "v_lshlrev_b32 v118, 16, v9\n\t"
    "v_and_b32 v117, s67, v8\n\t"
    "v_lshlrev_b32 v116, 16, v8\n\t"
    "v_add_u32_sdwa v112, s92, v112 dst_sel:DWORD dst_unused:UNUSED_PAD src0_sel:DWORD src1_sel:WORD_0\n\t"
    "s_waitcnt lgkmcnt(4)\n\t"
    "v_smfmac_f32_16x16x64_bf16 v[116:119], v[120:123], v[84:91], v124\n\t"
    "ds_read_b64 v[84:85], v112 offset:0\n\t"
    "ds_read_b64 v[86:87], v126 offset:0\n\t"
    "v_cvt_pk_bf16_f32 v80, v80, v81\n\t"
    "v_cvt_pk_bf16_f32 v81, v82, v83\n\t"
    "ds_write_b64 v125, v[80:81] offset:32128\n\t"
    "v_add_u32_sdwa v90, s92, v113 dst_sel:DWORD dst_unused:UNUSED_PAD src0_sel:DWORD src1_sel:WORD_1\n\t"
    "v_add_u32_sdwa v113, s92, v113 dst_sel:DWORD dst_unused:UNUSED_PAD src0_sel:DWORD src1_sel:WORD_0\n\t"
    "ds_read_b64 v[88:89], v113 offset:0\n\t"
    "ds_read_b64 v[90:91], v90 offset:0\n\t"
    "v_add_u32_sdwa v126, s92, v114 dst_sel:DWORD dst_unused:UNUSED_PAD src0_sel:DWORD src1_sel:WORD_1\n\t"
    "v_add_u32_sdwa v114, s92, v114 dst_sel:DWORD dst_unused:UNUSED_PAD src0_sel:DWORD src1_sel:WORD_0\n\t"
    "s_waitcnt vmcnt(2) lgkmcnt(5)\n\t"
    "v_smfmac_f32_16x16x64_bf16 v[116:119], v[120:123], v[92:99], v124\n\t"
    "ds_read_b64 v[92:93], v114 offset:0\n\t"
    "ds_read_b64 v[94:95], v126 offset:0\n\t"
    "v_add_u32_sdwa v98, s92, v115 dst_sel:DWORD dst_unused:UNUSED_PAD src0_sel:DWORD src1_sel:WORD_1\n\t"
    "v_add_u32_sdwa v115, s92, v115 dst_sel:DWORD dst_unused:UNUSED_PAD src0_sel:DWORD src1_sel:WORD_0\n\t"
    "ds_read_b64 v[96:97], v115 offset:0\n\t"
    "ds_read_b64 v[98:99], v98 offset:0\n\t"
    "buffer_load_dwordx4 v[112:115], v127, %12, s87 offen offset:1024 sc1\n\t"
    "v_add_u32_sdwa v126, s92, v100 dst_sel:DWORD dst_unused:UNUSED_PAD src0_sel:DWORD src1_sel:WORD_1\n\t"
    "v_cvt_pk_bf16_f32 v8, v116, v117\n\t"
    "v_cvt_pk_bf16_f32 v9, v118, v119\n\t"
    "v_and_b32 v119, s67, v11\n\t"
    "v_lshlrev_b32 v118, 16, v11\n\t"
    "v_and_b32 v117, s67, v10\n\t"
    "v_lshlrev_b32 v116, 16, v10\n\t"
    "v_add_u32_sdwa v100, s92, v100 dst_sel:DWORD dst_unused:UNUSED_PAD src0_sel:DWORD src1_sel:WORD_0\n\t"
    "s_waitcnt lgkmcnt(4)\n\t"
    "v_smfmac_f32_16x16x64_bf16 v[116:119], v[120:123], v[84:91], v124\n\t"
    "ds_read_b64 v[84:85], v100 offset:0\n\t"
    "v_mfma_f32_16x16x32_bf16 v[80:83], v[64:67], v[180:183], 0\n\t"
    "ds_read_b64 v[86:87], v126 offset:0\n\t"
    "v_mfma_f32_16x16x32_bf16 v[80:83], v[68:71], v[184:187], v[80:83]\n\t"
    "v_add_u32_sdwa v90, s92, v101 dst_sel:DWORD dst_unused:UNUSED_PAD src0_sel:DWORD src1_sel:WORD_1\n\t"
    "v_add_u32_sdwa v101, s92, v101 dst_sel:DWORD dst_unused:UNUSED_PAD src0_sel:DWORD src1_sel:WORD_0\n\t"
    "ds_read_b64 v[88:89], v101 offset:0\n\t"
    "v_mfma_f32_16x16x32_bf16 v[80:83], v[72:75], v[236:239], v[80:83]\n\t"
    "ds_read_b64 v[90:91], v90 offset:0\n\t"
    "v_mfma_f32_16x16x32_bf16 v[80:83], v[76:79], v[240:243], v[80:83]\n\t"
    "v_add_u32_sdwa v126, s92, v102 dst_sel:DWORD dst_unused:UNUSED_PAD src0_sel:DWORD src1_sel:WORD_1\n\t"
    "v_add_u32_sdwa v102, s92, v102 dst_sel:DWORD dst_unused:UNUSED_PAD src0_sel:DWORD src1_sel:WORD_0\n\t"
    "s_waitcnt vmcnt(2) lgkmcnt(4)\n\t"
    "v_smfmac_f32_16x16x64_bf16 v[116:119], v[120:123], v[92:99], v124\n\t"
    "ds_read_b64 v[92:93], v102 offset:0\n\t"
    "ds_read_b64 v[94:95], v126 offset:0\n\t"
    "v_add_u32_sdwa v98, s92, v103 dst_sel:DWORD dst_unused:UNUSED_PAD src0_sel:DWORD src1_sel:WORD_1\n\t"
    "v_add_u32_sdwa v103, s92, v103 dst_sel:DWORD dst_unused:UNUSED_PAD src0_sel:DWORD src1_sel:WORD_0\n\t"
    "ds_read_b64 v[96:97], v103 offset:0\n\t"
    "ds_read_b64 v[98:99], v98 offset:0\n\t"
    "s_add_u32 s88, s88, s94\n\t"
    "buffer_load_dwordx4 v[100:103], v127, %12, s88 offen offset:0 sc1\n\t"
    "v_add_u32_sdwa v126, s92, v104 dst_sel:DWORD dst_unused:UNUSED_PAD src0_sel:DWORD src1_sel:WORD_1\n\t"
    "v_cvt_pk_bf16_f32 v10, v116, v117\n\t"
    "v_cvt_pk_bf16_f32 v11, v118, v119\n\t"
    "v_and_b32 v119, s67, v17\n\t"
    "v_lshlrev_b32 v118, 16, v17\n\t"
    "v_and_b32 v117, s67, v16\n\t"
    "v_lshlrev_b32 v116, 16, v16\n\t"
    "v_add_u32_sdwa v104, s92, v104 dst_sel:DWORD dst_unused:UNUSED_PAD src0_sel:DWORD src1_sel:WORD_0\n\t"
    "s_waitcnt lgkmcnt(4)\n\t"
    "v_smfmac_f32_16x16x64_bf16 v[116:119], v[120:123], v[84:91], v124\n\t"
    "ds_read_b64 v[84:85], v104 offset:0\n\t"
    "ds_read_b64 v[86:87], v126 offset:0\n\t"
    "v_cvt_pk_bf16_f32 v80, v80, v81\n\t"
    "v_cvt_pk_bf16_f32 v81, v82, v83\n\t"
    "ds_write_b64 v125, v[80:81] offset:32256\n\t"
    "v_add_u32_sdwa v90, s92, v105 dst_sel:DWORD dst_unused:UNUSED_PAD src0_sel:DWORD src1_sel:WORD_1\n\t"
    "v_add_u32_sdwa v105, s92, v105 dst_sel:DWORD dst_unused:UNUSED_PAD src0_sel:DWORD src1_sel:WORD_0\n\t"
    "ds_read_b64 v[88:89], v105 offset:0\n\t"
    "ds_read_b64 v[90:91], v90 offset:0\n\t"
    "v_add_u32_sdwa v126, s92, v106 dst_sel:DWORD dst_unused:UNUSED_PAD src0_sel:DWORD src1_sel:WORD_1\n\t"
    "v_add_u32_sdwa v106, s92, v106 dst_sel:DWORD dst_unused:UNUSED_PAD src0_sel:DWORD src1_sel:WORD_0\n\t"
    "s_waitcnt vmcnt(2) lgkmcnt(5)\n\t"
    "v_smfmac_f32_16x16x64_bf16 v[116:119], v[120:123], v[92:99], v124\n\t"
    "ds_read_b64 v[92:93], v106 offset:0\n\t"
    "ds_read_b64 v[94:95], v126 offset:0\n\t"
    "v_add_u32_sdwa v98, s92, v107 dst_sel:DWORD dst_unused:UNUSED_PAD src0_sel:DWORD src1_sel:WORD_1\n\t"
    "v_add_u32_sdwa v107, s92, v107 dst_sel:DWORD dst_unused:UNUSED_PAD src0_sel:DWORD src1_sel:WORD_0\n\t"
    "ds_read_b64 v[96:97], v107 offset:0\n\t"
    "ds_read_b64 v[98:99], v98 offset:0\n\t"
    "buffer_load_dwordx4 v[104:107], v127, %12, s88 offen offset:1024 sc1\n\t"
    "v_add_u32_sdwa v126, s92, v108 dst_sel:DWORD dst_unused:UNUSED_PAD src0_sel:DWORD src1_sel:WORD_1\n\t"
    "v_cvt_pk_bf16_f32 v16, v116, v117\n\t"
    "v_cvt_pk_bf16_f32 v17, v118, v119\n\t"
    "v_and_b32 v119, s67, v19\n\t"
    "v_lshlrev_b32 v118, 16, v19\n\t"
    "v_and_b32 v117, s67, v18\n\t"
    "v_lshlrev_b32 v116, 16, v18\n\t"
    "v_add_u32_sdwa v108, s92, v108 dst_sel:DWORD dst_unused:UNUSED_PAD src0_sel:DWORD src1_sel:WORD_0\n\t"
    "s_waitcnt lgkmcnt(4)\n\t"
    "v_smfmac_f32_16x16x64_bf16 v[116:119], v[120:123], v[84:91], v124\n\t"
    "ds_read_b64 v[84:85], v108 offset:0\n\t"
    "v_mfma_f32_16x16x32_bf16 v[80:83], v[64:67], v[188:191], 0\n\t"
    "ds_read_b64 v[86:87], v126 offset:0\n\t"
    "v_mfma_f32_16x16x32_bf16 v[80:83], v[68:71], v[192:195], v[80:83]\n\t"
    "v_add_u32_sdwa v90, s92, v109 dst_sel:DWORD dst_unused:UNUSED_PAD src0_sel:DWORD src1_sel:WORD_1\n\t"
    "v_add_u32_sdwa v109, s92, v109 dst_sel:DWORD dst_unused:UNUSED_PAD src0_sel:DWORD src1_sel:WORD_0\n\t"
    "ds_read_b64 v[88:89], v109 offset:0\n\t"
    "v_mfma_f32_16x16x32_bf16 v[80:83], v[72:75], v[244:247], v[80:83]\n\t"
    "ds_read_b64 v[90:91], v90 offset:0\n\t"
    "v_mfma_f32_16x16x32_bf16 v[80:83], v[76:79], v[248:251], v[80:83]\n\t"
    "v_add_u32_sdwa v126, s92, v110 dst_sel:DWORD dst_unused:UNUSED_PAD src0_sel:DWORD src1_sel:WORD_1\n\t"
    "v_add_u32_sdwa v110, s92, v110 dst_sel:DWORD dst_unused:UNUSED_PAD src0_sel:DWORD src1_sel:WORD_0\n\t"
    "s_waitcnt vmcnt(2) lgkmcnt(4)\n\t"
    "v_smfmac_f32_16x16x64_bf16 v[116:119], v[120:123], v[92:99], v124\n\t"
    "ds_read_b64 v[92:93], v110 offset:0\n\t"
    "ds_read_b64 v[94:95], v126 offset:0\n\t"
    "v_add_u32_sdwa v98, s92, v111 dst_sel:DWORD dst_unused:UNUSED_PAD src0_sel:DWORD src1_sel:WORD_1\n\t"
    "v_add_u32_sdwa v111, s92, v111 dst_sel:DWORD dst_unused:UNUSED_PAD src0_sel:DWORD src1_sel:WORD_0\n\t"
    "ds_read_b64 v[96:97], v111 offset:0\n\t"
    "ds_read_b64 v[98:99], v98 offset:0\n\t"
    "s_add_u32 s89, s89, s94\n\t"
    "buffer_load_dwordx4 v[108:111], v127, %12, s89 offen offset:0 sc1\n\t"
    "v_add_u32_sdwa v126, s92, v112 dst_sel:DWORD dst_unused:UNUSED_PAD src0_sel:DWORD src1_sel:WORD_1\n\t"
    "v_cvt_pk_bf16_f32 v18, v116, v117\n\t"
    "v_cvt_pk_bf16_f32 v19, v118, v119\n\t"
    "v_and_b32 v119, s67, v25\n\t"
    "v_lshlrev_b32 v118, 16, v25\n\t"
    "v_and_b32 v117, s67, v24\n\t"
    "v_lshlrev_b32 v116, 16, v24\n\t"
    "v_add_u32_sdwa v112, s92, v112 dst_sel:DWORD dst_unused:UNUSED_PAD src0_sel:DWORD src1_sel:WORD_0\n\t"
    "s_waitcnt lgkmcnt(4)\n\t"
    "v_smfmac_f32_16x16x64_bf16 v[116:119], v[120:123], v[84:91], v124\n\t"
    "ds_read_b64 v[84:85], v112 offset:0\n\t"
    "ds_read_b64 v[86:87], v126 offset:0\n\t"
    "v_cvt_pk_bf16_f32 v80, v80, v81\n\t"
    "v_cvt_pk_bf16_f32 v81, v82, v83\n\t"
    "ds_write_b64 v125, v[80:81] offset:32384\n\t"
    "v_add_u32_sdwa v90, s92, v113 dst_sel:DWORD dst_unused:UNUSED_PAD src0_sel:DWORD src1_sel:WORD_1\n\t"
    "v_add_u32_sdwa v113, s92, v113 dst_sel:DWORD dst_unused:UNUSED_PAD src0_sel:DWORD src1_sel:WORD_0\n\t"
    "ds_read_b64 v[88:89], v113 offset:0\n\t"
    "ds_read_b64 v[90:91], v90 offset:0\n\t"
    "v_add_u32_sdwa v126, s92, v114 dst_sel:DWORD dst_unused:UNUSED_PAD src0_sel:DWORD src1_sel:WORD_1\n\t"
    "v_add_u32_sdwa v114, s92, v114 dst_sel:DWORD dst_unused:UNUSED_PAD src0_sel:DWORD src1_sel:WORD_0\n\t"
    "s_waitcnt vmcnt(2) lgkmcnt(5)\n\t"
    "v_smfmac_f32_16x16x64_bf16 v[116:119], v[120:123], v[92:99], v124\n\t"
    "ds_read_b64 v[92:93], v114 offset:0\n\t"
    "ds_read_b64 v[94:95], v126 offset:0\n\t"
    "v_add_u32_sdwa v98, s92, v115 dst_sel:DWORD dst_unused:UNUSED_PAD src0_sel:DWORD src1_sel:WORD_1\n\t"
    "v_add_u32_sdwa v115, s92, v115 dst_sel:DWORD dst_unused:UNUSED_PAD src0_sel:DWORD src1_sel:WORD_0\n\t"
    "ds_read_b64 v[96:97], v115 offset:0\n\t"
    "ds_read_b64 v[98:99], v98 offset:0\n\t"
    "buffer_load_dwordx4 v[112:115], v127, %12, s89 offen offset:1024 sc1\n\t"
    "v_add_u32_sdwa v126, s92, v100 dst_sel:DWORD dst_unused:UNUSED_PAD src0_sel:DWORD src1_sel:WORD_1\n\t"
    "v_cvt_pk_bf16_f32 v24, v116, v117\n\t"
    "v_cvt_pk_bf16_f32 v25, v118, v119\n\t"
    "v_and_b32 v119, s67, v27\n\t"
    "v_lshlrev_b32 v118, 16, v27\n\t"
    "v_and_b32 v117, s67, v26\n\t"
    "v_lshlrev_b32 v116, 16, v26\n\t"
    "v_add_u32_sdwa v100, s92, v100 dst_sel:DWORD dst_unused:UNUSED_PAD src0_sel:DWORD src1_sel:WORD_0\n\t"
    "s_waitcnt lgkmcnt(4)\n\t"
    "v_smfmac_f32_16x16x64_bf16 v[116:119], v[120:123], v[84:91], v124\n\t"
    "ds_read_b64 v[84:85], v100 offset:0\n\t"
    "v_mfma_f32_16x16x32_bf16 v[80:83], v[64:67], v[196:199], 0\n\t"
    "ds_read_b64 v[86:87], v126 offset:0\n\t"
    "v_mfma_f32_16x16x32_bf16 v[80:83], v[68:71], v[200:203], v[80:83]\n\t"
    "v_add_u32_sdwa v90, s92, v101 dst_sel:DWORD dst_unused:UNUSED_PAD src0_sel:DWORD src1_sel:WORD_1\n\t"
    "v_add_u32_sdwa v101, s92, v101 dst_sel:DWORD dst_unused:UNUSED_PAD src0_sel:DWORD src1_sel:WORD_0\n\t"
    "ds_read_b64 v[88:89], v101 offset:0\n\t"
    "v_mfma_f32_16x16x32_bf16 v[80:83], v[72:75], v[252:255], v[80:83]\n\t"
    "ds_read_b64 v[90:91], v90 offset:0\n\t"
    "v_mfma_f32_16x16x32_bf16 v[80:83], v[76:79], v[128:131], v[80:83]\n\t"
    "v_add_u32_sdwa v126, s92, v102 dst_sel:DWORD dst_unused:UNUSED_PAD src0_sel:DWORD src1_sel:WORD_1\n\t"
    "v_add_u32_sdwa v102, s92, v102 dst_sel:DWORD dst_unused:UNUSED_PAD src0_sel:DWORD src1_sel:WORD_0\n\t"
    "s_waitcnt vmcnt(2) lgkmcnt(4)\n\t"
    "v_smfmac_f32_16x16x64_bf16 v[116:119], v[120:123], v[92:99], v124\n\t"
    "ds_read_b64 v[92:93], v102 offset:0\n\t"
    "ds_read_b64 v[94:95], v126 offset:0\n\t"
    "v_add_u32_sdwa v98, s92, v103 dst_sel:DWORD dst_unused:UNUSED_PAD src0_sel:DWORD src1_sel:WORD_1\n\t"
    "v_add_u32_sdwa v103, s92, v103 dst_sel:DWORD dst_unused:UNUSED_PAD src0_sel:DWORD src1_sel:WORD_0\n\t"
    "ds_read_b64 v[96:97], v103 offset:0\n\t"
    "ds_read_b64 v[98:99], v98 offset:0\n\t"
    "s_add_u32 s90, s90, s94\n\t"
    "buffer_load_dwordx4 v[100:103], v127, %12, s90 offen offset:0 sc1\n\t"
    "v_add_u32_sdwa v126, s92, v104 dst_sel:DWORD dst_unused:UNUSED_PAD src0_sel:DWORD src1_sel:WORD_1\n\t"
    "v_cvt_pk_bf16_f32 v26, v116, v117\n\t"
    "v_cvt_pk_bf16_f32 v27, v118, v119\n\t"
    "v_and_b32 v119, s67, v33\n\t"
    "v_lshlrev_b32 v118, 16, v33\n\t"
    "v_and_b32 v117, s67, v32\n\t"
    "v_lshlrev_b32 v116, 16, v32\n\t"
    "v_add_u32_sdwa v104, s92, v104 dst_sel:DWORD dst_unused:UNUSED_PAD src0_sel:DWORD src1_sel:WORD_0\n\t"
    "s_waitcnt lgkmcnt(4)\n\t"
    "v_smfmac_f32_16x16x64_bf16 v[116:119], v[120:123], v[84:91], v124\n\t"
    "ds_read_b64 v[84:85], v104 offset:0\n\t"
    "ds_read_b64 v[86:87], v126 offset:0\n\t"
    "v_cvt_pk_bf16_f32 v80, v80, v81\n\t"
    "v_cvt_pk_bf16_f32 v81, v82, v83\n\t"
    "ds_write_b64 v125, v[80:81] offset:32512\n\t"
    "v_add_u32_sdwa v90, s92, v105 dst_sel:DWORD dst_unused:UNUSED_PAD src0_sel:DWORD src1_sel:WORD_1\n\t"
    "v_add_u32_sdwa v105, s92, v105 dst_sel:DWORD dst_unused:UNUSED_PAD src0_sel:DWORD src1_sel:WORD_0\n\t"
    "ds_read_b64 v[88:89], v105 offset:0\n\t"
    "ds_read_b64 v[90:91], v90 offset:0\n\t"
    "v_add_u32_sdwa v126, s92, v106 dst_sel:DWORD dst_unused:UNUSED_PAD src0_sel:DWORD src1_sel:WORD_1\n\t"
    "v_add_u32_sdwa v106, s92, v106 dst_sel:DWORD dst_unused:UNUSED_PAD src0_sel:DWORD src1_sel:WORD_0\n\t"
    "s_waitcnt vmcnt(2) lgkmcnt(5)\n\t"
    "v_smfmac_f32_16x16x64_bf16 v[116:119], v[120:123], v[92:99], v124\n\t"
    "ds_read_b64 v[92:93], v106 offset:0\n\t"
    "ds_read_b64 v[94:95], v126 offset:0\n\t"
    "v_add_u32_sdwa v98, s92, v107 dst_sel:DWORD dst_unused:UNUSED_PAD src0_sel:DWORD src1_sel:WORD_1\n\t"
    "v_add_u32_sdwa v107, s92, v107 dst_sel:DWORD dst_unused:UNUSED_PAD src0_sel:DWORD src1_sel:WORD_0\n\t"
    "ds_read_b64 v[96:97], v107 offset:0\n\t"
    "ds_read_b64 v[98:99], v98 offset:0\n\t"
    "buffer_load_dwordx4 v[104:107], v127, %12, s90 offen offset:1024 sc1\n\t"
    "v_add_u32_sdwa v126, s92, v108 dst_sel:DWORD dst_unused:UNUSED_PAD src0_sel:DWORD src1_sel:WORD_1\n\t"
    "v_cvt_pk_bf16_f32 v32, v116, v117\n\t"
    "v_cvt_pk_bf16_f32 v33, v118, v119\n\t"
    "v_and_b32 v119, s67, v35\n\t"
    "v_lshlrev_b32 v118, 16, v35\n\t"
    "v_and_b32 v117, s67, v34\n\t"
    "v_lshlrev_b32 v116, 16, v34\n\t"
    "v_add_u32_sdwa v108, s92, v108 dst_sel:DWORD dst_unused:UNUSED_PAD src0_sel:DWORD src1_sel:WORD_0\n\t"
    "s_waitcnt lgkmcnt(4)\n\t"
    "v_smfmac_f32_16x16x64_bf16 v[116:119], v[120:123], v[84:91], v124\n\t"
    "ds_read_b64 v[84:85], v108 offset:0\n\t"
    "v_mfma_f32_16x16x32_bf16 v[80:83], v[64:67], v[204:207], 0\n\t"
    "ds_read_b64 v[86:87], v126 offset:0\n\t"
    "v_mfma_f32_16x16x32_bf16 v[80:83], v[68:71], v[208:211], v[80:83]\n\t"
    "v_add_u32_sdwa v90, s92, v109 dst_sel:DWORD dst_unused:UNUSED_PAD src0_sel:DWORD src1_sel:WORD_1\n\t"
    "v_add_u32_sdwa v109, s92, v109 dst_sel:DWORD dst_unused:UNUSED_PAD src0_sel:DWORD src1_sel:WORD_0\n\t"
    "ds_read_b64 v[88:89], v109 offset:0\n\t"
    "v_mfma_f32_16x16x32_bf16 v[80:83], v[72:75], v[132:135], v[80:83]\n\t"
    "ds_read_b64 v[90:91], v90 offset:0\n\t"
    "v_mfma_f32_16x16x32_bf16 v[80:83], v[76:79], v[136:139], v[80:83]\n\t"
    "v_add_u32_sdwa v126, s92, v110 dst_sel:DWORD dst_unused:UNUSED_PAD src0_sel:DWORD src1_sel:WORD_1\n\t"
    "v_add_u32_sdwa v110, s92, v110 dst_sel:DWORD dst_unused:UNUSED_PAD src0_sel:DWORD src1_sel:WORD_0\n\t"
    "s_cmp_lt_u32 %16, 4\n\t"
    "s_cbranch_scc1 L_tp51_%=\n\t"
    "s_setprio 0\n\t"
    "L_tp51_%=:\n\t"
    "s_waitcnt vmcnt(2) lgkmcnt(4)\n\t"
    "v_smfmac_f32_16x16x64_bf16 v[116:119], v[120:123], v[92:99], v124\n\t"
    "ds_read_b64 v[92:93], v110 offset:0\n\t"
    "ds_read_b64 v[94:95], v126 offset:0\n\t"
    "v_add_u32_sdwa v98, s92, v111 dst_sel:DWORD dst_unused:UNUSED_PAD src0_sel:DWORD src1_sel:WORD_1\n\t"
    "v_add_u32_sdwa v111, s92, v111 dst_sel:DWORD dst_unused:UNUSED_PAD src0_sel:DWORD src1_sel:WORD_0\n\t"
    "ds_read_b64 v[96:97], v111 offset:0\n\t"
    "ds_read_b64 v[98:99], v98 offset:0\n\t"
    "s_add_u32 s91, s91, s94\n\t"
    "buffer_load_dwordx4 v[108:111], v127, %12, s91 offen offset:0 sc1\n\t"
    "v_add_u32_sdwa v126, s92, v112 dst_sel:DWORD dst_unused:UNUSED_PAD src0_sel:DWORD src1_sel:WORD_1\n\t"
    "v_cvt_pk_bf16_f32 v34, v116, v117\n\t"
    "v_cvt_pk_bf16_f32 v35, v118, v119\n\t"
    "v_and_b32 v119, s67, v41\n\t"
    "v_lshlrev_b32 v118, 16, v41\n\t"
    "v_and_b32 v117, s67, v40\n\t"
    "v_lshlrev_b32 v116, 16, v40\n\t"
    "v_add_u32_sdwa v112, s92, v112 dst_sel:DWORD dst_unused:UNUSED_PAD src0_sel:DWORD src1_sel:WORD_0\n\t"
    "s_waitcnt lgkmcnt(4)\n\t"
    "v_smfmac_f32_16x16x64_bf16 v[116:119], v[120:123], v[84:91], v124\n\t"
    "ds_read_b64 v[84:85], v112 offset:0\n\t"
    "ds_read_b64 v[86:87], v126 offset:0\n\t"
    "v_cvt_pk_bf16_f32 v80, v80, v81\n\t"
    "v_cvt_pk_bf16_f32 v81, v82, v83\n\t"
    "ds_write_b64 v125, v[80:81] offset:32640\n\t"
    "v_add_u32_sdwa v90, s92, v113 dst_sel:DWORD dst_unused:UNUSED_PAD src0_sel:DWORD src1_sel:WORD_1\n\t"
    "v_add_u32_sdwa v113, s92, v113 dst_sel:DWORD dst_unused:UNUSED_PAD src0_sel:DWORD src1_sel:WORD_0\n\t"
    "ds_read_b64 v[88:89], v113 offset:0\n\t"
    "ds_read_b64 v[90:91], v90 offset:0\n\t"
    "v_add_u32_sdwa v126, s92, v114 dst_sel:DWORD dst_unused:UNUSED_PAD src0_sel:DWORD src1_sel:WORD_1\n\t"
    "v_add_u32_sdwa v114, s92, v114 dst_sel:DWORD dst_unused:UNUSED_PAD src0_sel:DWORD src1_sel:WORD_0\n\t"
    "s_waitcnt vmcnt(2) lgkmcnt(5)\n\t"
    "v_smfmac_f32_16x16x64_bf16 v[116:119], v[120:123], v[92:99], v124\n\t"
    "ds_read_b64 v[92:93], v114 offset:0\n\t"
    "ds_read_b64 v[94:95], v126 offset:0\n\t"
    "v_add_u32_sdwa v98, s92, v115 dst_sel:DWORD dst_unused:UNUSED_PAD src0_sel:DWORD src1_sel:WORD_1\n\t"
    "v_add_u32_sdwa v115, s92, v115 dst_sel:DWORD dst_unused:UNUSED_PAD src0_sel:DWORD src1_sel:WORD_0\n\t"
    "ds_read_b64 v[96:97], v115 offset:0\n\t"
    "ds_read_b64 v[98:99], v98 offset:0\n\t"
    "buffer_load_dwordx4 v[112:115], v127, %12, s91 offen offset:1024 sc1\n\t"
    "v_add_u32_sdwa v126, s92, v100 dst_sel:DWORD dst_unused:UNUSED_PAD src0_sel:DWORD src1_sel:WORD_1\n\t"
    "v_cvt_pk_bf16_f32 v40, v116, v117\n\t"
    "v_cvt_pk_bf16_f32 v41, v118, v119\n\t"
    "v_and_b32 v119, s67, v43\n\t"
    "v_lshlrev_b32 v118, 16, v43\n\t"
    "v_and_b32 v117, s67, v42\n\t"
    "v_lshlrev_b32 v116, 16, v42\n\t"
    "v_add_u32_sdwa v100, s92, v100 dst_sel:DWORD dst_unused:UNUSED_PAD src0_sel:DWORD src1_sel:WORD_0\n\t"
    "s_waitcnt lgkmcnt(4)\n\t"
    "v_smfmac_f32_16x16x64_bf16 v[116:119], v[120:123], v[84:91], v124\n\t"
    "ds_read_b64 v[84:85], v100 offset:0\n\t"
    "v_mfma_f32_16x16x32_bf16 v[80:83], v[64:67], v[156:159], 0\n\t"
    "ds_read_b64 v[86:87], v126 offset:0\n\t"
    "v_mfma_f32_16x16x32_bf16 v[80:83], v[68:71], v[160:163], v[80:83]\n\t"
    "v_add_u32_sdwa v90, s92, v101 dst_sel:DWORD dst_unused:UNUSED_PAD src0_sel:DWORD src1_sel:WORD_1\n\t"
    "v_add_u32_sdwa v101, s92, v101 dst_sel:DWORD dst_unused:UNUSED_PAD src0_sel:DWORD src1_sel:WORD_0\n\t"
    "ds_read_b64 v[88:89], v101 offset:0\n\t"
    "v_mfma_f32_16x16x32_bf16 v[80:83], v[72:75], v[140:143], v[80:83]\n\t"
    "ds_read_b64 v[90:91], v90 offset:0\n\t"
    "v_mfma_f32_16x16x32_bf16 v[80:83], v[76:79], v[144:147], v[80:83]\n\t"
    "v_add_u32_sdwa v126, s92, v102 dst_sel:DWORD dst_unused:UNUSED_PAD src0_sel:DWORD src1_sel:WORD_1\n\t"
    "v_add_u32_sdwa v102, s92, v102 dst_sel:DWORD dst_unused:UNUSED_PAD src0_sel:DWORD src1_sel:WORD_0\n\t"
    "s_waitcnt vmcnt(2) lgkmcnt(4)\n\t"
    "v_smfmac_f32_16x16x64_bf16 v[116:119], v[120:123], v[92:99], v124\n\t"
    "ds_read_b64 v[92:93], v102 offset:0\n\t"
    "ds_read_b64 v[94:95], v126 offset:0\n\t"
    "v_add_u32_sdwa v98, s92, v103 dst_sel:DWORD dst_unused:UNUSED_PAD src0_sel:DWORD src1_sel:WORD_1\n\t"
    "v_add_u32_sdwa v103, s92, v103 dst_sel:DWORD dst_unused:UNUSED_PAD src0_sel:DWORD src1_sel:WORD_0\n\t"
    "ds_read_b64 v[96:97], v103 offset:0\n\t"
    "ds_read_b64 v[98:99], v98 offset:0\n\t"
    "s_add_u32 s84, s84, s95\n\t"
    "buffer_load_dwordx4 v[100:103], v127, %12, s84 offen offset:0 sc1\n\t"
    "v_add_u32_sdwa v126, s92, v104 dst_sel:DWORD dst_unused:UNUSED_PAD src0_sel:DWORD src1_sel:WORD_1\n\t"
    "v_cvt_pk_bf16_f32 v42, v116, v117\n\t"
    "v_cvt_pk_bf16_f32 v43, v118, v119\n\t"
    "v_and_b32 v119, s67, v49\n\t"
    "v_lshlrev_b32 v118, 16, v49\n\t"
    "v_and_b32 v117, s67, v48\n\t"
    "v_lshlrev_b32 v116, 16, v48\n\t"
    "v_add_u32_sdwa v104, s92, v104 dst_sel:DWORD dst_unused:UNUSED_PAD src0_sel:DWORD src1_sel:WORD_0\n\t"
    "s_waitcnt lgkmcnt(4)\n\t"
    "v_smfmac_f32_16x16x64_bf16 v[116:119], v[120:123], v[84:91], v124\n\t"
    "ds_read_b64 v[84:85], v104 offset:0\n\t"
    "ds_read_b64 v[86:87], v126 offset:0\n\t"
    "v_cvt_pk_bf16_f32 v80, v80, v81\n\t"
    "v_cvt_pk_bf16_f32 v81, v82, v83\n\t"
    "ds_write_b64 v125, v[80:81] offset:32768\n\t"
    "v_add_u32_sdwa v90, s92, v105 dst_sel:DWORD dst_unused:UNUSED_PAD src0_sel:DWORD src1_sel:WORD_1\n\t"
    "v_add_u32_sdwa v105, s92, v105 dst_sel:DWORD dst_unused:UNUSED_PAD src0_sel:DWORD src1_sel:WORD_0\n\t"
    "ds_read_b64 v[88:89], v105 offset:0\n\t"
    "ds_read_b64 v[90:91], v90 offset:0\n\t"
    "v_add_u32_sdwa v126, s92, v106 dst_sel:DWORD dst_unused:UNUSED_PAD src0_sel:DWORD src1_sel:WORD_1\n\t"
    "v_add_u32_sdwa v106, s92, v106 dst_sel:DWORD dst_unused:UNUSED_PAD src0_sel:DWORD src1_sel:WORD_0\n\t"
    "s_waitcnt vmcnt(2) lgkmcnt(5)\n\t"
    "v_smfmac_f32_16x16x64_bf16 v[116:119], v[120:123], v[92:99], v124\n\t"
    "ds_read_b64 v[92:93], v106 offset:0\n\t"
    "ds_read_b64 v[94:95], v126 offset:0\n\t"
    "v_add_u32_sdwa v98, s92, v107 dst_sel:DWORD dst_unused:UNUSED_PAD src0_sel:DWORD src1_sel:WORD_1\n\t"
    "v_add_u32_sdwa v107, s92, v107 dst_sel:DWORD dst_unused:UNUSED_PAD src0_sel:DWORD src1_sel:WORD_0\n\t"
    "ds_read_b64 v[96:97], v107 offset:0\n\t"
    "ds_read_b64 v[98:99], v98 offset:0\n\t"
    "buffer_load_dwordx4 v[104:107], v127, %12, s84 offen offset:1024 sc1\n\t"
    "v_add_u32_sdwa v126, s92, v108 dst_sel:DWORD dst_unused:UNUSED_PAD src0_sel:DWORD src1_sel:WORD_1\n\t"
    "v_cvt_pk_bf16_f32 v48, v116, v117\n\t"
    "v_cvt_pk_bf16_f32 v49, v118, v119\n\t"
    "v_and_b32 v119, s67, v51\n\t"
    "v_lshlrev_b32 v118, 16, v51\n\t"
    "v_and_b32 v117, s67, v50\n\t"
    "v_lshlrev_b32 v116, 16, v50\n\t"
    "v_add_u32_sdwa v108, s92, v108 dst_sel:DWORD dst_unused:UNUSED_PAD src0_sel:DWORD src1_sel:WORD_0\n\t"
    "s_waitcnt lgkmcnt(4)\n\t"
    "v_smfmac_f32_16x16x64_bf16 v[116:119], v[120:123], v[84:91], v124\n\t"
    "ds_read_b64 v[84:85], v108 offset:0\n\t"
    "v_mfma_f32_16x16x32_bf16 v[80:83], v[64:67], v[212:215], 0\n\t"
    "ds_read_b64 v[86:87], v126 offset:0\n\t"
    "v_mfma_f32_16x16x32_bf16 v[80:83], v[68:71], v[216:219], v[80:83]\n\t"
    "v_add_u32_sdwa v90, s92, v109 dst_sel:DWORD dst_unused:UNUSED_PAD src0_sel:DWORD src1_sel:WORD_1\n\t"
    "v_add_u32_sdwa v109, s92, v109 dst_sel:DWORD dst_unused:UNUSED_PAD src0_sel:DWORD src1_sel:WORD_0\n\t"
    "ds_read_b64 v[88:89], v109 offset:0\n\t"
    "v_mfma_f32_16x16x32_bf16 v[80:83], v[72:75], v[148:151], v[80:83]\n\t"
    "ds_read_b64 v[90:91], v90 offset:0\n\t"
    "v_mfma_f32_16x16x32_bf16 v[80:83], v[76:79], v[152:155], v[80:83]\n\t"
    "v_add_u32_sdwa v126, s92, v110 dst_sel:DWORD dst_unused:UNUSED_PAD src0_sel:DWORD src1_sel:WORD_1\n\t"
    "v_add_u32_sdwa v110, s92, v110 dst_sel:DWORD dst_unused:UNUSED_PAD src0_sel:DWORD src1_sel:WORD_0\n\t"
    "s_waitcnt vmcnt(2) lgkmcnt(4)\n\t"
    "v_smfmac_f32_16x16x64_bf16 v[116:119], v[120:123], v[92:99], v124\n\t"
    "ds_read_b64 v[92:93], v110 offset:0\n\t"
    "ds_read_b64 v[94:95], v126 offset:0\n\t"
    "v_add_u32_sdwa v98, s92, v111 dst_sel:DWORD dst_unused:UNUSED_PAD src0_sel:DWORD src1_sel:WORD_1\n\t"
    "v_add_u32_sdwa v111, s92, v111 dst_sel:DWORD dst_unused:UNUSED_PAD src0_sel:DWORD src1_sel:WORD_0\n\t"
    "ds_read_b64 v[96:97], v111 offset:0\n\t"
    "ds_read_b64 v[98:99], v98 offset:0\n\t"
    "s_add_u32 s85, s85, s95\n\t"
    "buffer_load_dwordx4 v[108:111], v127, %12, s85 offen offset:0 sc1\n\t"
    "v_add_u32_sdwa v126, s92, v112 dst_sel:DWORD dst_unused:UNUSED_PAD src0_sel:DWORD src1_sel:WORD_1\n\t"
    "v_cvt_pk_bf16_f32 v50, v116, v117\n\t"
    "v_cvt_pk_bf16_f32 v51, v118, v119\n\t"
    "v_and_b32 v119, s67, v57\n\t"
    "v_lshlrev_b32 v118, 16, v57\n\t"
    "v_and_b32 v117, s67, v56\n\t"
    "v_lshlrev_b32 v116, 16, v56\n\t"
    "v_add_u32_sdwa v112, s92, v112 dst_sel:DWORD dst_unused:UNUSED_PAD src0_sel:DWORD src1_sel:WORD_0\n\t"
    "s_waitcnt lgkmcnt(4)\n\t"
    "v_smfmac_f32_16x16x64_bf16 v[116:119], v[120:123], v[84:91], v124\n\t"
    "ds_read_b64 v[84:85], v112 offset:0\n\t"
    "ds_read_b64 v[86:87], v126 offset:0\n\t"
    "v_cvt_pk_bf16_f32 v80, v80, v81\n\t"
    "v_cvt_pk_bf16_f32 v81, v82, v83\n\t"
    "ds_write_b64 v125, v[80:81] offset:32872\n\t"
    "v_add_u32_sdwa v90, s92, v113 dst_sel:DWORD dst_unused:UNUSED_PAD src0_sel:DWORD src1_sel:WORD_1\n\t"
    "v_add_u32_sdwa v113, s92, v113 dst_sel:DWORD dst_unused:UNUSED_PAD src0_sel:DWORD src1_sel:WORD_0\n\t"
    "ds_read_b64 v[88:89], v113 offset:0\n\t"
    "ds_read_b64 v[90:91], v90 offset:0\n\t"
    "v_add_u32_sdwa v126, s92, v114 dst_sel:DWORD dst_unused:UNUSED_PAD src0_sel:DWORD src1_sel:WORD_1\n\t"
    "v_add_u32_sdwa v114, s92, v114 dst_sel:DWORD dst_unused:UNUSED_PAD src0_sel:DWORD src1_sel:WORD_0\n\t"
    "s_waitcnt vmcnt(2) lgkmcnt(5)\n\t"
    "v_smfmac_f32_16x16x64_bf16 v[116:119], v[120:123], v[92:99], v124\n\t"
    "ds_read_b64 v[92:93], v114 offset:0\n\t"
    "ds_read_b64 v[94:95], v126 offset:0\n\t"
    "v_add_u32_sdwa v98, s92, v115 dst_sel:DWORD dst_unused:UNUSED_PAD src0_sel:DWORD src1_sel:WORD_1\n\t"
    "v_add_u32_sdwa v115, s92, v115 dst_sel:DWORD dst_unused:UNUSED_PAD src0_sel:DWORD src1_sel:WORD_0\n\t"
    "ds_read_b64 v[96:97], v115 offset:0\n\t"
    "ds_read_b64 v[98:99], v98 offset:0\n\t"
    "buffer_load_dwordx4 v[112:115], v127, %12, s85 offen offset:1024 sc1\n\t"
    "s_sub_u32 s92, 64000, s92\n\t"
    "v_add_u32_sdwa v126, s92, v100 dst_sel:DWORD dst_unused:UNUSED_PAD src0_sel:DWORD src1_sel:WORD_1\n\t"
    "v_cvt_pk_bf16_f32 v56, v116, v117\n\t"
    "v_cvt_pk_bf16_f32 v57, v118, v119\n\t"
    "v_and_b32 v119, s67, v59\n\t"
    "v_lshlrev_b32 v118, 16, v59\n\t"
    "v_and_b32 v117, s67, v58\n\t"
    "v_lshlrev_b32 v116, 16, v58\n\t"
    "v_add_u32_sdwa v100, s92, v100 dst_sel:DWORD dst_unused:UNUSED_PAD src0_sel:DWORD src1_sel:WORD_0\n\t"
    "s_waitcnt lgkmcnt(0)\n\t"
    "s_barrier\n\t"
    "v_add_u32 v125, s93, v125\n\t"
    "s_sub_u32 s93, 0, s93\n\t"
    "v_smfmac_f32_16x16x64_bf16 v[116:119], v[120:123], v[84:91], v124\n\t"
    "ds_read_b64 v[84:85], v100 offset:0\n\t"
    "ds_read_b64 v[86:87], v126 offset:0\n\t"
    "v_add_u32_sdwa v90, s92, v101 dst_sel:DWORD dst_unused:UNUSED_PAD src0_sel:DWORD src1_sel:WORD_1\n\t"
    "v_add_u32_sdwa v101, s92, v101 dst_sel:DWORD dst_unused:UNUSED_PAD src0_sel:DWORD src1_sel:WORD_0\n\t"
    "ds_read_b64 v[88:89], v101 offset:0\n\t"
    "ds_read_b64 v[90:91], v90 offset:0\n\t"
    "v_add_u32_sdwa v126, s92, v102 dst_sel:DWORD dst_unused:UNUSED_PAD src0_sel:DWORD src1_sel:WORD_1\n\t"
    "v_add_u32_sdwa v102, s92, v102 dst_sel:DWORD dst_unused:UNUSED_PAD src0_sel:DWORD src1_sel:WORD_0\n\t"
    "s_cmp_lt_u32 %16, 4\n\t"
    "s_cbranch_scc1 L_tp63_%=\n\t"
    "s_setprio 1\n\t"
    "L_tp63_%=:\n\t"
    "s_waitcnt vmcnt(2)\n\t"
    "v_smfmac_f32_16x16x64_bf16 v[116:119], v[120:123], v[92:99], v124\n\t"
    "ds_read_b64 v[92:93], v102 offset:0\n\t"
    "ds_read_b64 v[94:95], v126 offset:0\n\t"
    "v_add_u32_sdwa v98, s92, v103 dst_sel:DWORD dst_unused:UNUSED_PAD src0_sel:DWORD src1_sel:WORD_1\n\t"
    "v_add_u32_sdwa v103, s92, v103 dst_sel:DWORD dst_unused:UNUSED_PAD src0_sel:DWORD src1_sel:WORD_0\n\t"
    "ds_read_b64 v[96:97], v103 offset:0\n\t"
    "ds_read_b64 v[98:99], v98 offset:0\n\t"
    "s_add_u32 s86, s86, s95\n\t"
    "buffer_load_dwordx4 v[100:103], v127, %12, s86 offen offset:0 sc1\n\t"
    "v_add_u32_sdwa v126, s92, v104 dst_sel:DWORD dst_unused:UNUSED_PAD src0_sel:DWORD src1_sel:WORD_1\n\t"
    "v_cvt_pk_bf16_f32 v58, v116, v117\n\t"
    "v_cvt_pk_bf16_f32 v59, v118, v119\n\t"
    "v_and_b32 v119, s67, v5\n\t"
    "v_lshlrev_b32 v118, 16, v5\n\t"
    "v_and_b32 v117, s67, v4\n\t"
    "v_lshlrev_b32 v116, 16, v4\n\t"
    "v_add_u32_sdwa v104, s92, v104 dst_sel:DWORD dst_unused:UNUSED_PAD src0_sel:DWORD src1_sel:WORD_0\n\t"
    "v_add_u32 v79, s96, v127\n\t"
    "s_add_u32 s96, s96, 0x1000\n\t"
    "s_and_b32 s96, s96, 0x3000\n\t"
    "v_add_u32 v79, 128000, v79\n\t"
    "ds_read_b128 v[64:67], v79 offset:0\n\t"
    "ds_read_b128 v[68:71], v79 offset:1024\n\t"
    "ds_read_b128 v[72:75], v79 offset:2048\n\t"
    "ds_read_b128 v[76:79], v79 offset:3072\n\t"
    "s_cmp_eq_u32 s66, s48\n\t"
    "s_cbranch_scc1 L_pub_%=\n\t"
    "L_pubb_%=:\n\t"
    "s_sub_u32 s66, s66, 1\n\t"
    "s_cmp_lg_u32 s66, 0\n\t"
    "s_cbranch_scc1 L_loop_%=\n\t"
    "s_mov_b32 s92, 0xffff\n\t"
    "s_waitcnt lgkmcnt(8)\n\t"
    "v_smfmac_f32_16x16x64_bf16 v[116:119], v[120:123], v[84:91], v124\n\t"
    "ds_read_b64 v[84:85], v104 offset:0\n\t"
    "ds_read_b64 v[86:87], v126 offset:0\n\t"
    "v_lshrrev_b32 v90, 16, v105\n\t"
    "v_and_b32 v105, s92, v105\n\t"
    "ds_read_b64 v[88:89], v105 offset:0\n\t"
    "ds_read_b64 v[90:91], v90 offset:0\n\t"
    "v_lshrrev_b32 v126, 16, v106\n\t"
    "v_and_b32 v106, s92, v106\n\t"
    "s_waitcnt vmcnt(2) lgkmcnt(8)\n\t"
    "v_smfmac_f32_16x16x64_bf16 v[116:119], v[120:123], v[92:99], v124\n\t"
    "ds_read_b64 v[92:93], v106 offset:0\n\t"
    "s_waitcnt lgkmcnt(5)\n\t"
    "v_mfma_f32_16x16x32_bf16 v[80:83], v[64:67], v[164:167], 0\n\t"
    "ds_read_b64 v[94:95], v126 offset:0\n\t"
    "v_mfma_f32_16x16x32_bf16 v[80:83], v[68:71], v[168:171], v[80:83]\n\t"
    "v_lshrrev_b32 v98, 16, v107\n\t"
    "v_and_b32 v107, s92, v107\n\t"
    "ds_read_b64 v[96:97], v107 offset:0\n\t"
    "v_mfma_f32_16x16x32_bf16 v[80:83], v[72:75], v[220:223], v[80:83]\n\t"
    "ds_read_b64 v[98:99], v98 offset:0\n\t"
    "v_mfma_f32_16x16x32_bf16 v[80:83], v[76:79], v[224:227], v[80:83]\n\t"
    "v_lshrrev_b32 v126, 16, v108\n\t"
    "v_cvt_pk_bf16_f32 v4, v116, v117\n\t"
    "v_cvt_pk_bf16_f32 v5, v118, v119\n\t"
    "v_and_b32 v119, s67, v7\n\t"
    "v_lshlrev_b32 v118, 16, v7\n\t"
    "v_and_b32 v117, s67, v6\n\t"
    "v_lshlrev_b32 v116, 16, v6\n\t"
    "v_and_b32 v108, s92, v108\n\t"
    "s_waitcnt lgkmcnt(4)\n\t"
    "v_smfmac_f32_16x16x64_bf16 v[116:119], v[120:123], v[84:91], v124\n\t"
    "ds_read_b64 v[84:85], v108 offset:0\n\t"
    "v_mfma_f32_16x16x32_bf16 v[104:107], v[64:67], v[172:175], 0\n\t"
    "ds_read_b64 v[86:87], v126 offset:0\n\t"
    "v_mfma_f32_16x16x32_bf16 v[104:107], v[68:71], v[176:179], v[104:107]\n\t"
    "v_lshrrev_b32 v90, 16, v109\n\t"
    "v_and_b32 v109, s92, v109\n\t"
    "ds_read_b64 v[88:89], v109 offset:0\n\t"
    "v_mfma_f32_16x16x32_bf16 v[104:107], v[72:75], v[228:231], v[104:107]\n\t"
    "ds_read_b64 v[90:91], v90 offset:0\n\t"
    "v_mfma_f32_16x16x32_bf16 v[104:107], v[76:79], v[232:235], v[104:107]\n\t"
    "v_lshrrev_b32 v126, 16, v110\n\t"
    "v_cvt_pk_bf16_f32 v80, v80, v81\n\t"
    "v_cvt_pk_bf16_f32 v81, v82, v83\n\t"
    "ds_write_b64 v125, v[80:81] offset:0\n\t"
    "v_mfma_f32_16x16x32_bf16 v[80:83], v[64:67], v[180:183], 0\n\t"
    "v_and_b32 v110, s92, v110\n\t"
    "v_mfma_f32_16x16x32_bf16 v[80:83], v[68:71], v[184:187], v[80:83]\n\t"
    "v_mfma_f32_16x16x32_bf16 v[80:83], v[72:75], v[236:239], v[80:83]\n\t"
    "s_waitcnt vmcnt(1) lgkmcnt(5)\n\t"
    "v_smfmac_f32_16x16x64_bf16 v[116:119], v[120:123], v[92:99], v124\n\t"
    "ds_read_b64 v[92:93], v110 offset:0\n\t"
    "v_mfma_f32_16x16x32_bf16 v[80:83], v[76:79], v[240:243], v[80:83]\n\t"
    "ds_read_b64 v[94:95], v126 offset:0\n\t"
    "v_cvt_pk_bf16_f32 v104, v104, v105\n\t"
    "v_cvt_pk_bf16_f32 v105, v106, v107\n\t"
    "ds_write_b64 v125, v[104:105] offset:128\n\t"
    "v_mfma_f32_16x16x32_bf16 v[104:107], v[64:67], v[188:191], 0\n\t"
    "v_lshrrev_b32 v98, 16, v111\n\t"
    "v_and_b32 v111, s92, v111\n\t"
    "ds_read_b64 v[96:97], v111 offset:0\n\t"
    "v_mfma_f32_16x16x32_bf16 v[104:107], v[68:71], v[192:195], v[104:107]\n\t"
    "ds_read_b64 v[98:99], v98 offset:0\n\t"
    "v_mfma_f32_16x16x32_bf16 v[104:107], v[72:75], v[244:247], v[104:107]\n\t"
    "buffer_load_dwordx4 v[108:111], v127, %12, s86 offen offset:1024 sc1\n\t"
    "v_lshrrev_b32 v126, 16, v112\n\t"
    "v_cvt_pk_bf16_f32 v6, v116, v117\n\t"
    "v_cvt_pk_bf16_f32 v7, v118, v119\n\t"
    "v_and_b32 v119, s67, v13\n\t"
    "v_lshlrev_b32 v118, 16, v13\n\t"
    "v_and_b32 v117, s67, v12\n\t"
    "v_lshlrev_b32 v116, 16, v12\n\t"
    "v_and_b32 v112, s92, v112\n\t"
    "s_waitcnt lgkmcnt(6)\n\t"
    "v_smfmac_f32_16x16x64_bf16 v[116:119], v[120:123], v[84:91], v124\n\t"
    "ds_read_b64 v[84:85], v112 offset:0\n\t"
    "v_mfma_f32_16x16x32_bf16 v[104:107], v[76:79], v[248:251], v[104:107]\n\t"
    "ds_read_b64 v[86:87], v126 offset:0\n\t"
    "v_cvt_pk_bf16_f32 v80, v80, v81\n\t"
    "v_cvt_pk_bf16_f32 v81, v82, v83\n\t"
    "ds_write_b64 v125, v[80:81] offset:256\n\t"
    "v_mfma_f32_16x16x32_bf16 v[80:83], v[64:67], v[196:199], 0\n\t"
    "v_lshrrev_b32 v90, 16, v113\n\t"
    "v_and_b32 v113, s92, v113\n\t"
    "ds_read_b64 v[88:89], v113 offset:0\n\t"
    "v_mfma_f32_16x16x32_bf16 v[80:83], v[68:71], v[200:203], v[80:83]\n\t"
    "ds_read_b64 v[90:91], v90 offset:0\n\t"
    "v_mfma_f32_16x16x32_bf16 v[80:83], v[72:75], v[252:255], v[80:83]\n\t"
    "v_lshrrev_b32 v126, 16, v114\n\t"
    "v_mfma_f32_16x16x32_bf16 v[80:83], v[76:79], v[128:131], v[80:83]\n\t"
    "v_and_b32 v114, s92, v114\n\t"
    "v_cvt_pk_bf16_f32 v104, v104, v105\n\t"
    "v_cvt_pk_bf16_f32 v105, v106, v107\n\t"
    "ds_write_b64 v125, v[104:105] offset:384\n\t"
    "v_mfma_f32_16x16x32_bf16 v[104:107], v[64:67], v[204:207], 0\n\t"
    "s_waitcnt vmcnt(1) lgkmcnt(6)\n\t"
    "v_smfmac_f32_16x16x64_bf16 v[116:119], v[120:123], v[92:99], v124\n\t"
    "ds_read_b64 v[92:93], v114 offset:0\n\t"
    "v_mfma_f32_16x16x32_bf16 v[104:107], v[68:71], v[208:211], v[104:107]\n\t"
    "ds_read_b64 v[94:95], v126 offset:0\n\t"
    "v_mfma_f32_16x16x32_bf16 v[104:107], v[72:75], v[132:135], v[104:107]\n\t"
    "v_lshrrev_b32 v98, 16, v115\n\t"
    "v_and_b32 v115, s92, v115\n\t"
    "ds_read_b64 v[96:97], v115 offset:0\n\t"
    "v_mfma_f32_16x16x32_bf16 v[104:107], v[76:79], v[136:139], v[104:107]\n\t"
    "ds_read_b64 v[98:99], v98 offset:0\n\t"
    "v_cvt_pk_bf16_f32 v80, v80, v81\n\t"
    "v_cvt_pk_bf16_f32 v81, v82, v83\n\t"
    "ds_write_b64 v125, v[80:81] offset:512\n\t"
    "v_mfma_f32_16x16x32_bf16 v[80:83], v[64:67], v[156:159], 0\n\t"
    "s_add_u32 s87, s87, s95\n\t"
    "buffer_load_dwordx4 v[112:115], v127, %12, s87 offen offset:0 sc1\n\t"
    "v_lshrrev_b32 v126, 16, v100\n\t"
    "v_mfma_f32_16x16x32_bf16 v[80:83], v[68:71], v[160:163], v[80:83]\n\t"
    "v_cvt_pk_bf16_f32 v12, v116, v117\n\t"
    "v_cvt_pk_bf16_f32 v13, v118, v119\n\t"
    "v_and_b32 v119, s67, v15\n\t"
    "v_lshlrev_b32 v118, 16, v15\n\t"
    "v_and_b32 v117, s67, v14\n\t"
    "v_lshlrev_b32 v116, 16, v14\n\t"
    "v_and_b32 v100, s92, v100\n\t"
    "s_waitcnt lgkmcnt(6)\n\t"
    "v_smfmac_f32_16x16x64_bf16 v[116:119], v[120:123], v[84:91], v124\n\t"
    "ds_read_b64 v[84:85], v100 offset:0\n\t"
    "v_mfma_f32_16x16x32_bf16 v[80:83], v[72:75], v[140:143], v[80:83]\n\t"
    "ds_read_b64 v[86:87], v126 offset:0\n\t"
    "v_mfma_f32_16x16x32_bf16 v[80:83], v[76:79], v[144:147], v[80:83]\n\t"
    "v_lshrrev_b32 v90, 16, v101\n\t"
    "v_and_b32 v101, s92, v101\n\t"
    "ds_read_b64 v[88:89], v101 offset:0\n\t"
    "v_cvt_pk_bf16_f32 v104, v104, v105\n\t"
    "v_cvt_pk_bf16_f32 v105, v106, v107\n\t"
    "ds_write_b64 v125, v[104:105] offset:640\n\t"
    "v_mfma_f32_16x16x32_bf16 v[104:107], v[64:67], v[212:215], 0\n\t"
    "ds_read_b64 v[90:91], v90 offset:0\n\t"
    "v_mfma_f32_16x16x32_bf16 v[104:107], v[68:71], v[216:219], v[104:107]\n\t"
    "v_lshrrev_b32 v126, 16, v102\n\t"
    "v_mfma_f32_16x16x32_bf16 v[104:107], v[72:75], v[148:151], v[104:107]\n\t"
    "v_and_b32 v102, s92, v102\n\t"
    "v_mfma_f32_16x16x32_bf16 v[104:107], v[76:79], v[152:155], v[104:107]\n\t"
    "v_add_u32 v79, s96, v127\n\t"
    "s_add_u32 s96, s96, 0x1000\n\t"
    "s_and_b32 s96, s96, 0x3000\n\t"
    "v_add_u32 v79, 128000, v79\n\t"
    "ds_read_b128 v[64:67], v79 offset:0\n\t"
    "ds_read_b128 v[68:71], v79 offset:1024\n\t"
    "ds_read_b128 v[72:75], v79 offset:2048\n\t"
    "ds_read_b128 v[76:79], v79 offset:3072\n\t"
    "v_cvt_pk_bf16_f32 v80, v80, v81\n\t"
    "v_cvt_pk_bf16_f32 v81, v82, v83\n\t"
    "ds_write_b64 v125, v[80:81] offset:768\n\t"
    "s_waitcnt vmcnt(1) lgkmcnt(11)\n\t"
    "v_smfmac_f32_16x16x64_bf16 v[116:119], v[120:123], v[92:99], v124\n\t"
    "ds_read_b64 v[92:93], v102 offset:0\n\t"
    "ds_read_b64 v[94:95], v126 offset:0\n\t"
    "v_lshrrev_b32 v98, 16, v103\n\t"
    "v_and_b32 v103, s92, v103\n\t"
    "ds_read_b64 v[96:97], v103 offset:0\n\t"
    "ds_read_b64 v[98:99], v98 offset:0\n\t"
    "buffer_load_dwordx4 v[100:103], v127, %12, s87 offen offset:1024 sc1\n\t"
    "v_lshrrev_b32 v126, 16, v108\n\t"
    "v_cvt_pk_bf16_f32 v14, v116, v117\n\t"
    "v_cvt_pk_bf16_f32 v15, v118, v119\n\t"
    "v_and_b32 v119, s67, v21\n\t"
    "v_lshlrev_b32 v118, 16, v21\n\t"
    "v_and_b32 v117, s67, v20\n\t"
    "v_lshlrev_b32 v116, 16, v20\n\t"
    "v_and_b32 v108, s92, v108\n\t"
    "s_waitcnt lgkmcnt(9)\n\t"
    "v_smfmac_f32_16x16x64_bf16 v[116:119], v[120:123], v[84:91], v124\n\t"
    "ds_read_b64 v[84:85], v108 offset:0\n\t"
    "s_waitcnt lgkmcnt(6)\n\t"
    "v_mfma_f32_16x16x32_bf16 v[80:83], v[64:67], v[164:167], 0\n\t"
    "ds_read_b64 v[86:87], v126 offset:0\n\t"
    "v_mfma_f32_16x16x32_bf16 v[80:83], v[68:71], v[168:171], v[80:83]\n\t"
    "v_lshrrev_b32 v90, 16, v109\n\t"
    "v_and_b32 v109, s92, v109\n\t"
    "ds_read_b64 v[88:89], v109 offset:0\n\t"
    "v_mfma_f32_16x16x32_bf16 v[80:83], v[72:75], v[220:223], v[80:83]\n\t"
    "ds_read_b64 v[90:91], v90 offset:0\n\t"
    "v_mfma_f32_16x16x32_bf16 v[80:83], v[76:79], v[224:227], v[80:83]\n\t"
    "v_lshrrev_b32 v126, 16, v110\n\t"
    "v_cvt_pk_bf16_f32 v104, v104, v105\n\t"
    "v_cvt_pk_bf16_f32 v105, v106, v107\n\t"
    "ds_write_b64 v125, v[104:105] offset:872\n\t"
    "v_mfma_f32_16x16x32_bf16 v[104:107], v[64:67], v[172:175], 0\n\t"
    "v_and_b32 v110, s92, v110\n\t"
    "v_mfma_f32_16x16x32_bf16 v[104:107], v[68:71], v[176:179], v[104:107]\n\t"
    "s_waitcnt vmcnt(1) lgkmcnt(5)\n\t"
    "v_smfmac_f32_16x16x64_bf16 v[116:119], v[120:123], v[92:99], v124\n\t"
    "ds_read_b64 v[92:93], v110 offset:0\n\t"
    "v_mfma_f32_16x16x32_bf16 v[104:107], v[72:75], v[228:231], v[104:107]\n\t"
    "ds_read_b64 v[94:95], v126 offset:0\n\t"
    "v_mfma_f32_16x16x32_bf16 v[104:107], v[76:79], v[232:235], v[104:107]\n\t"
    "v_lshrrev_b32 v98, 16, v111\n\t"
    "v_and_b32 v111, s92, v111\n\t"
    "ds_read_b64 v[96:97], v111 offset:0\n\t"
    "v_cvt_pk_bf16_f32 v80, v80, v81\n\t"
    "v_cvt_pk_bf16_f32 v81, v82, v83\n\t"
    "ds_write_b64 v125, v[80:81] offset:32000\n\t"
    "v_mfma_f32_16x16x32_bf16 v[80:83], v[64:67], v[180:183], 0\n\t"
    "ds_read_b64 v[98:99], v98 offset:0\n\t"
    "v_mfma_f32_16x16x32_bf16 v[80:83], v[68:71], v[184:187], v[80:83]\n\t"
    "s_add_u32 s84, s84, 0x8000\n\t"
    "buffer_load_dwordx4 v[108:111], v127, %12, s84 offen offset:0 sc1\n\t"
    "v_lshrrev_b32 v126, 16, v112\n\t"
    "v_mfma_f32_16x16x32_bf16 v[80:83], v[72:75], v[236:239], v[80:83]\n\t"
    "v_cvt_pk_bf16_f32 v20, v116, v117\n\t"
    "v_cvt_pk_bf16_f32 v21, v118, v119\n\t"
    "v_and_b32 v119, s67, v23\n\t"
    "v_lshlrev_b32 v118, 16, v23\n\t"
    "v_and_b32 v117, s67, v22\n\t"
    "v_lshlrev_b32 v116, 16, v22\n\t"
    "v_and_b32 v112, s92, v112\n\t"
    "s_waitcnt lgkmcnt(6)\n\t"
    "v_smfmac_f32_16x16x64_bf16 v[116:119], v[120:123], v[84:91], v124\n\t"
    "ds_read_b64 v[84:85], v112 offset:0\n\t"
    "v_mfma_f32_16x16x32_bf16 v[80:83], v[76:79], v[240:243], v[80:83]\n\t"
    "ds_read_b64 v[86:87], v126 offset:0\n\t"
    "v_cvt_pk_bf16_f32 v104, v104, v105\n\t"
    "v_cvt_pk_bf16_f32 v105, v106, v107\n\t"
    "ds_write_b64 v125, v[104:105] offset:32128\n\t"
    "v_mfma_f32_16x16x32_bf16 v[104:107], v[64:67], v[188:191], 0\n\t"
    "v_lshrrev_b32 v90, 16, v113\n\t"
    "v_and_b32 v113, s92, v113\n\t"
    "ds_read_b64 v[88:89], v113 offset:0\n\t"
    "v_mfma_f32_16x16x32_bf16 v[104:107], v[68:71], v[192:195], v[104:107]\n\t"
    "ds_read_b64 v[90:91], v90 offset:0\n\t"
    "v_mfma_f32_16x16x32_bf16 v[104:107], v[72:75], v[244:247], v[104:107]\n\t"
    "v_lshrrev_b32 v126, 16, v114\n\t"
    "v_mfma_f32_16x16x32_bf16 v[104:107], v[76:79], v[248:251], v[104:107]\n\t"
    "v_and_b32 v114, s92, v114\n\t"
    "v_cvt_pk_bf16_f32 v80, v80, v81\n\t"
    "v_cvt_pk_bf16_f32 v81, v82, v83\n\t"
    "ds_write_b64 v125, v[80:81] offset:32256\n\t"
    "v_mfma_f32_16x16x32_bf16 v[80:83], v[64:67], v[196:199], 0\n\t"
    "s_waitcnt vmcnt(1) lgkmcnt(6)\n\t"
    "v_smfmac_f32_16x16x64_bf16 v[116:119], v[120:123], v[92:99], v124\n\t"
    "ds_read_b64 v[92:93], v114 offset:0\n\t"
    "v_mfma_f32_16x16x32_bf16 v[80:83], v[68:71], v[200:203], v[80:83]\n\t"
    "ds_read_b64 v[94:95], v126 offset:0\n\t"
    "v_mfma_f32_16x16x32_bf16 v[80:83], v[72:75], v[252:255], v[80:83]\n\t"
    "v_lshrrev_b32 v98, 16, v115\n\t"
    "v_and_b32 v115, s92, v115\n\t"
    "ds_read_b64 v[96:97], v115 offset:0\n\t"
    "v_mfma_f32_16x16x32_bf16 v[80:83], v[76:79], v[128:131], v[80:83]\n\t"
    "ds_read_b64 v[98:99], v98 offset:0\n\t"
    "v_cvt_pk_bf16_f32 v104, v104, v105\n\t"
    "v_cvt_pk_bf16_f32 v105, v106, v107\n\t"
    "ds_write_b64 v125, v[104:105] offset:32384\n\t"
    "v_mfma_f32_16x16x32_bf16 v[104:107], v[64:67], v[204:207], 0\n\t"
    "buffer_load_dwordx4 v[112:115], v127, %12, s84 offen offset:1024 sc1\n\t"
    "v_lshrrev_b32 v126, 16, v100\n\t"
    "v_cvt_pk_bf16_f32 v22, v116, v117\n\t"
    "v_cvt_pk_bf16_f32 v23, v118, v119\n\t"
    "v_and_b32 v119, s67, v29\n\t"
    "v_lshlrev_b32 v118, 16, v29\n\t"
    "v_and_b32 v117, s67, v28\n\t"
    "v_lshlrev_b32 v116, 16, v28\n\t"
    "v_and_b32 v100, s92, v100\n\t"
    "s_waitcnt lgkmcnt(6)\n\t"
    "v_smfmac_f32_16x16x64_bf16 v[116:119], v[120:123], v[84:91], v124\n\t"
    "ds_read_b64 v[84:85], v100 offset:0\n\t"
    "v_mfma_f32_16x16x32_bf16 v[104:107], v[68:71], v[208:211], v[104:107]\n\t"
    "ds_read_b64 v[86:87], v126 offset:0\n\t"
    "v_mfma_f32_16x16x32_bf16 v[104:107], v[72:75], v[132:135], v[104:107]\n\t"
    "v_lshrrev_b32 v90, 16, v101\n\t"
    "v_and_b32 v101, s92, v101\n\t"
    "ds_read_b64 v[88:89], v101 offset:0\n\t"
    "v_mfma_f32_16x16x32_bf16 v[104:107], v[76:79], v[136:139], v[104:107]\n\t"
    "ds_read_b64 v[90:91], v90 offset:0\n\t"
    "v_cvt_pk_bf16_f32 v80, v80, v81\n\t"
    "v_cvt_pk_bf16_f32 v81, v82, v83\n\t"
    "ds_write_b64 v125, v[80:81] offset:32512\n\t"
    "v_mfma_f32_16x16x32_bf16 v[80:83], v[64:67], v[156:159], 0\n\t"
    "v_lshrrev_b32 v126, 16, v102\n\t"
    "v_mfma_f32_16x16x32_bf16 v[80:83], v[68:71], v[160:163], v[80:83]\n\t"
    "v_and_b32 v102, s92, v102\n\t"
    "v_mfma_f32_16x16x32_bf16 v[80:83], v[72:75], v[140:143], v[80:83]\n\t"
    "v_mfma_f32_16x16x32_bf16 v[80:83], v[76:79], v[144:147], v[80:83]\n\t"
    "v_cvt_pk_bf16_f32 v104, v104, v105\n\t"
    "v_cvt_pk_bf16_f32 v105, v106, v107\n\t"
    "ds_write_b64 v125, v[104:105] offset:32640\n\t"
    "s_waitcnt vmcnt(1) lgkmcnt(7)\n\t"
    "v_smfmac_f32_16x16x64_bf16 v[116:119], v[120:123], v[92:99], v124\n\t"
    "ds_read_b64 v[92:93], v102 offset:0\n\t"
    "v_mfma_f32_16x16x32_bf16 v[104:107], v[64:67], v[212:215], 0\n\t"
    "ds_read_b64 v[94:95], v126 offset:0\n\t"
    "v_mfma_f32_16x16x32_bf16 v[104:107], v[68:71], v[216:219], v[104:107]\n\t"
    "v_lshrrev_b32 v98, 16, v103\n\t"
    "v_and_b32 v103, s92, v103\n\t"
    "ds_read_b64 v[96:97], v103 offset:0\n\t"
    "v_mfma_f32_16x16x32_bf16 v[104:107], v[72:75], v[148:151], v[104:107]\n\t"
    "ds_read_b64 v[98:99], v98 offset:0\n\t"
    "v_mfma_f32_16x16x32_bf16 v[104:107], v[76:79], v[152:155], v[104:107]\n\t"
    "s_add_u32 s85, s85, 0x8000\n\t"
    "buffer_load_dwordx4 v[100:103], v127, %12, s85 offen offset:0 sc1\n\t"
    "v_lshrrev_b32 v126, 16, v108\n\t"
    "v_cvt_pk_bf16_f32 v80, v80, v81\n\t"
    "v_cvt_pk_bf16_f32 v81, v82, v83\n\t"
    "ds_write_b64 v125, v[80:81] offset:32768\n\t"
    "s_nop 1\n\t"
    "v_cvt_pk_bf16_f32 v104, v104, v105\n\t"
    "v_cvt_pk_bf16_f32 v105, v106, v107\n\t"
    "ds_write_b64 v125, v[104:105] offset:32872\n\t"
    "v_cvt_pk_bf16_f32 v28, v116, v117\n\t"
    "v_cvt_pk_bf16_f32 v29, v118, v119\n\t"
    "v_and_b32 v119, s67, v31\n\t"
    "v_lshlrev_b32 v118, 16, v31\n\t"
    "v_and_b32 v117, s67, v30\n\t"
    "v_lshlrev_b32 v116, 16, v30\n\t"
    "v_and_b32 v108, s92, v108\n\t"
    "s_waitcnt lgkmcnt(0)\n\t"
    "s_barrier\n\t"
    "v_smfmac_f32_16x16x64_bf16 v[116:119], v[120:123], v[84:91], v124\n\t"
    "ds_read_b64 v[84:85], v108 offset:64000\n\t"
    "ds_read_b64 v[86:87], v126 offset:64000\n\t"
    "v_lshrrev_b32 v90, 16, v109\n\t"
    "v_and_b32 v109, s92, v109\n\t"
    "ds_read_b64 v[88:89], v109 offset:64000\n\t"
    "ds_read_b64 v[90:91], v90 offset:64000\n\t"
    "buffer_load_dwordx4 v[104:107], v127, %12, s85 offen offset:1024 sc1\n\t"
    "v_lshrrev_b32 v126, 16, v110\n\t"
    "v_and_b32 v110, s92, v110\n\t"
    "s_waitcnt vmcnt(2)\n\t"
    "v_smfmac_f32_16x16x64_bf16 v[116:119], v[120:123], v[92:99], v124\n\t"
    "ds_read_b64 v[92:93], v110 offset:64000\n\t"
    "ds_read_b64 v[94:95], v126 offset:64000\n\t"
    "v_lshrrev_b32 v98, 16, v111\n\t"
    "v_and_b32 v111, s92, v111\n\t"
    "ds_read_b64 v[96:97], v111 offset:64000\n\t"
    "ds_read_b64 v[98:99], v98 offset:64000\n\t"
    "s_add_u32 s86, s86, 0x8000\n\t"
    "buffer_load_dwordx4 v[108:111], v127, %12, s86 offen offset:0 sc1\n\t"
    "v_lshrrev_b32 v252, 16, v112\n\t"
    "v_and_b32 v112, s92, v112\n\t"
    "v_lshrrev_b32 v253, 16, v113\n\t"
    "v_cvt_pk_bf16_f32 v30, v116, v117\n\t"
    "v_cvt_pk_bf16_f32 v31, v118, v119\n\t"
    "v_and_b32 v119, s67, v5\n\t"
    "v_lshlrev_b32 v118, 16, v5\n\t"
    "v_and_b32 v117, s67, v4\n\t"
    "v_lshlrev_b32 v116, 16, v4\n\t"
    "v_and_b32 v113, s92, v113\n\t"
    "v_mov_b32 v82, 0xbdd2d3e8\n\t"
    "v_mov_b32 v83, 0xbdd2d3e8\n\t"
    "v_mov_b32 v81, 0xc0135761\n\t"
    "v_lshrrev_b32 v125, 2, v127\n\t"
    "v_mov_b32 v80, 0xc0135761\n\t"
    "s_waitcnt lgkmcnt(4)\n\t"
    "v_smfmac_f32_16x16x64_bf16 v[116:119], v[120:123], v[84:91], v124\n\t"
    "ds_read_b64 v[84:85], v112 offset:64000\n\t"
    "ds_read_b64 v[86:87], v252 offset:64000\n\t"
    "ds_read_b64 v[88:89], v113 offset:64000\n\t"
    "ds_read_b64 v[90:91], v253 offset:64000\n\t"
    "v_lshrrev_b32 v254, 16, v114\n\t"
    "v_and_b32 v114, s92, v114\n\t"
    "v_lshrrev_b32 v255, 16, v115\n\t"
    "v_and_b32 v115, s92, v115\n\t"
    "s_lshl_b32 s65, %16, 10\n\t"
    "s_add_u32 s65, s65, 31457280\n\t"
    "s_xor_b32 s61, %14, 8\n\t"
    "s_mul_i32 s61, s61, 69632\n\t"
    "s_add_u32 s61, s61, s65\n\t"
    "s_mul_i32 s94, %14, 69632\n\t"
    "s_add_u32 s94, s94, s65\n\t"
    "s_lshl_b32 s65, %16, 9\n\t"
    "s_sub_u32 s65, 0x10000, s65\n\t"
    "s_add_u32 s93, s94, s65\n\t"
    "s_add_u32 s96, s61, s65\n\t"
    "v_mov_b32 v165, 1\n\t"
    "s_or_b32 s97, %14, %16\n\t"
    "s_cbranch_scc0 L_part0_%=\n\t"
    "L_partb0_%=:\n\t"
    "s_waitcnt vmcnt(2) lgkmcnt(4)\n\t"
    "v_smfmac_f32_16x16x64_bf16 v[116:119], v[120:123], v[92:99], v124\n\t"
    "ds_read_b64 v[92:93], v114 offset:64000\n\t"
    "ds_read_b64 v[94:95], v254 offset:64000\n\t"
    "ds_read_b64 v[96:97], v115 offset:64000\n\t"
    "ds_read_b64 v[98:99], v255 offset:64000\n\t"
    "buffer_load_dwordx4 v[172:175], v127, %12, s86 offen offset:1024 sc1\n\t"
    "v_lshrrev_b32 v252, 16, v100\n\t"
    "v_and_b32 v100, s92, v100\n\t"
    "v_lshrrev_b32 v253, 16, v101\n\t"
    "v_cvt_pk_bf16_f32 v4, v116, v117\n\t"
    "v_cvt_pk_bf16_f32 v5, v118, v119\n\t"
    "v_and_b32 v119, s67, v7\n\t"
    "v_lshlrev_b32 v118, 16, v7\n\t"
    "v_and_b32 v117, s67, v6\n\t"
    "v_lshlrev_b32 v116, 16, v6\n\t"
    "v_and_b32 v101, s92, v101\n\t"
    "v_mov_b32 v156, v120\n\t"
    "v_mov_b32 v160, 0\n\t"
    "v_mov_b32 v161, v121\n\t"
    "v_mov_b32 v157, 0\n\t"
    "v_mov_b32 v158, v122\n\t"
    "v_mov_b32 v162, 0\n\t"
    "v_mov_b32 v163, v123\n\t"
    "v_mov_b32 v159, 0\n\t"
    "s_waitcnt lgkmcnt(4)\n\t"
    "v_smfmac_f32_16x16x64_bf16 v[116:119], v[120:123], v[84:91], v124\n\t"
    "ds_read_b64 v[84:85], v100 offset:64000\n\t"
    "ds_read_b64 v[86:87], v252 offset:64000\n\t"
    "ds_read_b64 v[88:89], v101 offset:64000\n\t"
    "ds_read_b64 v[90:91], v253 offset:64000\n\t"
    "s_add_u32 s87, s87, 0x8000\n\t"
    "buffer_load_dwordx4 v[176:179], v127, %12, s87 offen offset:0 sc1\n\t"
    "v_lshrrev_b32 v254, 16, v102\n\t"
    "v_and_b32 v102, s92, v102\n\t"
    "v_lshrrev_b32 v255, 16, v103\n\t"
    "v_and_b32 v103, s92, v103\n\t"
    "s_cmp_lt_u32 %16, 4\n\t"
    "s_cbranch_scc1 L_tp163_%=\n\t"
    "s_setprio 0\n\t"
    "L_tp163_%=:\n\t"
    "s_waitcnt vmcnt(3) lgkmcnt(4)\n\t"
    "v_smfmac_f32_16x16x64_bf16 v[116:119], v[120:123], v[92:99], v124\n\t"
    "ds_read_b64 v[92:93], v102 offset:64000\n\t"
    "ds_read_b64 v[94:95], v254 offset:64000\n\t"
    "ds_read_b64 v[96:97], v103 offset:64000\n\t"
    "ds_read_b64 v[98:99], v255 offset:64000\n\t"
    "buffer_load_dwordx4 v[180:183], v127, %12, s87 offen offset:1024 sc1\n\t"
    "v_lshrrev_b32 v252, 16, v104\n\t"
    "v_and_b32 v104, s92, v104\n\t"
    "v_lshrrev_b32 v253, 16, v105\n\t"
    "v_cvt_pk_bf16_f32 v6, v116, v117\n\t"
    "v_cvt_pk_bf16_f32 v7, v118, v119\n\t"
    "v_and_b32 v119, s67, v13\n\t"
    "v_lshlrev_b32 v118, 16, v13\n\t"
    "v_and_b32 v117, s67, v12\n\t"
    "v_lshlrev_b32 v116, 16, v12\n\t"
    "v_and_b32 v105, s92, v105\n\t"
    "buffer_store_dwordx4 v[4:7], v127, %12, s61 offen sc1\n\t"
    "s_waitcnt lgkmcnt(4)\n\t"
    "v_smfmac_f32_16x16x64_bf16 v[116:119], v[120:123], v[84:91], v124\n\t"
    "ds_read_b64 v[84:85], v104 offset:64000\n\t"
    "ds_read_b64 v[86:87], v252 offset:64000\n\t"
    "ds_read_b64 v[88:89], v105 offset:64000\n\t"
    "ds_read_b64 v[90:91], v253 offset:64000\n\t"
    "s_add_u32 s88, s88, s95\n\t"
    "buffer_load_dwordx4 v[184:187], v127, %12, s88 offen offset:0 sc1\n\t"
    "v_lshrrev_b32 v254, 16, v106\n\t"
    "v_and_b32 v106, s92, v106\n\t"
    "v_lshrrev_b32 v255, 16, v107\n\t"
    "v_and_b32 v107, s92, v107\n\t"
    "s_waitcnt vmcnt(5) lgkmcnt(4)\n\t"
    "v_smfmac_f32_16x16x64_bf16 v[116:119], v[120:123], v[92:99], v124\n\t"
    "ds_read_b64 v[92:93], v106 offset:64000\n\t"
    "ds_read_b64 v[94:95], v254 offset:64000\n\t"
    "ds_read_b64 v[96:97], v107 offset:64000\n\t"
    "ds_read_b64 v[98:99], v255 offset:64000\n\t"
    "s_add_u32 s88, s88, 0x8000\n\t"
    "buffer_load_dwordx4 v[188:191], v127, %12, s88 offen offset:0 sc1\n\t"
    "v_lshrrev_b32 v252, 16, v108\n\t"
    "v_and_b32 v108, s92, v108\n\t"
    "v_lshrrev_b32 v253, 16, v109\n\t"
    "v_cvt_pk_bf16_f32 v12, v116, v117\n\t"
    "v_cvt_pk_bf16_f32 v13, v118, v119\n\t"
    "v_and_b32 v119, s67, v15\n\t"
    "v_lshlrev_b32 v118, 16, v15\n\t"
    "v_and_b32 v117, s67, v14\n\t"
    "v_lshlrev_b32 v116, 16, v14\n\t"
    "v_and_b32 v109, s92, v109\n\t"
    "s_waitcnt lgkmcnt(4)\n\t"
    "v_smfmac_f32_16x16x64_bf16 v[116:119], v[120:123], v[84:91], v124\n\t"
    "ds_read_b64 v[84:85], v108 offset:64000\n\t"
    "ds_read_b64 v[86:87], v252 offset:64000\n\t"
    "ds_read_b64 v[88:89], v109 offset:64000\n\t"
    "ds_read_b64 v[90:91], v253 offset:64000\n\t"
    "s_sub_u32 s88, s88, 0x8000\n\t"
    "buffer_load_dwordx4 v[236:239], v127, %12, s88 offen offset:1024 sc1\n\t"
    "v_lshrrev_b32 v254, 16, v110\n\t"
    "v_and_b32 v110, s92, v110\n\t"
    "v_lshrrev_b32 v255, 16, v111\n\t"
    "v_and_b32 v111, s92, v111\n\t"
    "s_waitcnt vmcnt(6) lgkmcnt(4)\n\t"
    "v_smfmac_f32_16x16x64_bf16 v[116:119], v[120:123], v[92:99], v124\n\t"
    "ds_read_b64 v[92:93], v110 offset:64000\n\t"
    "ds_read_b64 v[94:95], v254 offset:64000\n\t"
    "ds_read_b64 v[96:97], v111 offset:64000\n\t"
    "ds_read_b64 v[98:99], v255 offset:64000\n\t"
    "s_add_u32 s88, s88, 0x8000\n\t"
    "buffer_load_dwordx4 v[192:195], v127, %12, s88 offen offset:1024 sc1\n\t"
    "v_lshrrev_b32 v252, 16, v172\n\t"
    "v_and_b32 v172, s92, v172\n\t"
    "v_lshrrev_b32 v253, 16, v173\n\t"
    "v_cvt_pk_bf16_f32 v14, v116, v117\n\t"
    "v_cvt_pk_bf16_f32 v15, v118, v119\n\t"
    "v_and_b32 v119, s67, v21\n\t"
    "v_lshlrev_b32 v118, 16, v21\n\t"
    "v_and_b32 v117, s67, v20\n\t"
    "v_lshlrev_b32 v116, 16, v20\n\t"
    "v_and_b32 v173, s92, v173\n\t"
    "s_waitcnt lgkmcnt(4)\n\t"
    "v_smfmac_f32_16x16x64_bf16 v[116:119], v[120:123], v[84:91], v124\n\t"
    "ds_read_b64 v[84:85], v172 offset:64000\n\t"
    "ds_read_b64 v[86:87], v252 offset:64000\n\t"
    "ds_read_b64 v[88:89], v173 offset:64000\n\t"
    "ds_read_b64 v[90:91], v253 offset:64000\n\t"
    "v_lshrrev_b32 v254, 16, v174\n\t"
    "v_and_b32 v174, s92, v174\n\t"
    "v_lshrrev_b32 v255, 16, v175\n\t"
    "v_and_b32 v175, s92, v175\n\t"
    "s_cmp_lg_u32 s97, 0\n\t"
    "s_cbranch_scc0 L_part1_%=\n\t"
    "L_partb1_%=:\n\t"
    "s_waitcnt vmcnt(6) lgkmcnt(4)\n\t"
    "v_smfmac_f32_16x16x64_bf16 v[116:119], v[120:123], v[92:99], v124\n\t"
    "ds_read_b64 v[92:93], v174 offset:64000\n\t"
    "ds_read_b64 v[94:95], v254 offset:64000\n\t"
    "ds_read_b64 v[96:97], v175 offset:64000\n\t"
    "ds_read_b64 v[98:99], v255 offset:64000\n\t"
    "s_add_u32 s89, s89, s95\n\t"
    "buffer_load_dwordx4 v[196:199], v127, %12, s89 offen offset:0 sc1\n\t"
    "v_lshrrev_b32 v252, 16, v176\n\t"
    "v_and_b32 v176, s92, v176\n\t"
    "v_lshrrev_b32 v253, 16, v177\n\t"
    "v_cvt_pk_bf16_f32 v20, v116, v117\n\t"
    "v_cvt_pk_bf16_f32 v21, v118, v119\n\t"
    "v_and_b32 v119, s67, v23\n\t"
    "v_lshlrev_b32 v118, 16, v23\n\t"
    "v_and_b32 v117, s67, v22\n\t"
    "v_lshlrev_b32 v116, 16, v22\n\t"
    "v_and_b32 v177, s92, v177\n\t"
    "s_waitcnt lgkmcnt(4)\n\t"
    "v_smfmac_f32_16x16x64_bf16 v[116:119], v[120:123], v[84:91], v124\n\t"
    "ds_read_b64 v[84:85], v176 offset:64000\n\t"
    "ds_read_b64 v[86:87], v252 offset:64000\n\t"
    "ds_read_b64 v[88:89], v177 offset:64000\n\t"
    "ds_read_b64 v[90:91], v253 offset:64000\n\t"
    "v_lshrrev_b32 v254, 16, v178\n\t"
    "v_and_b32 v178, s92, v178\n\t"
    "v_lshrrev_b32 v255, 16, v179\n\t"
    "v_and_b32 v179, s92, v179\n\t"
    "s_waitcnt vmcnt(6) lgkmcnt(4)\n\t"
    "v_smfmac_f32_16x16x64_bf16 v[116:119], v[120:123], v[92:99], v124\n\t"
    "ds_read_b64 v[92:93], v178 offset:64000\n\t"
    "ds_read_b64 v[94:95], v254 offset:64000\n\t"
    "ds_read_b64 v[96:97], v179 offset:64000\n\t"
    "ds_read_b64 v[98:99], v255 offset:64000\n\t"
    "s_add_u32 s89, s89, 0x8000\n\t"
    "buffer_load_dwordx4 v[200:203], v127, %12, s89 offen offset:0 sc1\n\t"
    "v_lshrrev_b32 v252, 16, v180\n\t"
    "v_and_b32 v180, s92, v180\n\t"
    "v_lshrrev_b32 v253, 16, v181\n\t"
    "v_cvt_pk_bf16_f32 v22, v116, v117\n\t"
    "v_cvt_pk_bf16_f32 v23, v118, v119\n\t"
    "v_and_b32 v119, s67, v29\n\t"
    "v_lshlrev_b32 v118, 16, v29\n\t"
    "v_and_b32 v117, s67, v28\n\t"
    "v_lshlrev_b32 v116, 16, v28\n\t"
    "v_and_b32 v181, s92, v181\n\t"
    "s_add_u32 s61, s61, 0x2000\n\t"
    "buffer_store_dwordx4 v[12:15], v127, %12, s61 offen sc1\n\t"
    "s_waitcnt lgkmcnt(4)\n\t"
    "v_smfmac_f32_16x16x64_bf16 v[116:119], v[120:123], v[84:91], v124\n\t"
    "ds_read_b64 v[84:85], v180 offset:64000\n\t"
    "ds_read_b64 v[86:87], v252 offset:64000\n\t"
    "ds_read_b64 v[88:89], v181 offset:64000\n\t"
    "ds_read_b64 v[90:91], v253 offset:64000\n\t"
    "v_lshrrev_b32 v254, 16, v182\n\t"
    "v_and_b32 v182, s92, v182\n\t"
    "v_lshrrev_b32 v255, 16, v183\n\t"
    "v_and_b32 v183, s92, v183\n\t"
    "s_waitcnt vmcnt(6) lgkmcnt(4)\n\t"
    "v_smfmac_f32_16x16x64_bf16 v[116:119], v[120:123], v[92:99], v124\n\t"
    "ds_read_b64 v[92:93], v182 offset:64000\n\t"
    "ds_read_b64 v[94:95], v254 offset:64000\n\t"
    "ds_read_b64 v[96:97], v183 offset:64000\n\t"
    "ds_read_b64 v[98:99], v255 offset:64000\n\t"
    "s_sub_u32 s89, s89, 0x8000\n\t"
    "buffer_load_dwordx4 v[204:207], v127, %12, s89 offen offset:1024 sc1\n\t"
    "v_lshrrev_b32 v252, 16, v184\n\t"
    "v_and_b32 v184, s92, v184\n\t"
    "v_lshrrev_b32 v253, 16, v185\n\t"
    "v_cvt_pk_bf16_f32 v28, v116, v117\n\t"
    "v_cvt_pk_bf16_f32 v29, v118, v119\n\t"
    "v_and_b32 v119, s67, v31\n\t"
    "v_lshlrev_b32 v118, 16, v31\n\t"
    "v_and_b32 v117, s67, v30\n\t"
    "v_lshlrev_b32 v116, 16, v30\n\t"
    "v_and_b32 v185, s92, v185\n\t"
    "s_waitcnt lgkmcnt(4)\n\t"
    "v_smfmac_f32_16x16x64_bf16 v[116:119], v[120:123], v[84:91], v124\n\t"
    "ds_read_b64 v[84:85], v184 offset:0\n\t"
    "ds_read_b64 v[86:87], v252 offset:0\n\t"
    "ds_read_b64 v[88:89], v185 offset:0\n\t"
    "ds_read_b64 v[90:91], v253 offset:0\n\t"
    "v_lshrrev_b32 v254, 16, v186\n\t"
    "v_and_b32 v186, s92, v186\n\t"
    "v_lshrrev_b32 v255, 16, v187\n\t"
    "v_and_b32 v187, s92, v187\n\t"
    "s_cmp_lt_u32 %16, 4\n\t"
    "s_cbranch_scc1 L_tp175_%=\n\t"
    "s_setprio 1\n\t"
    "L_tp175_%=:\n\t"
    "s_waitcnt vmcnt(6) lgkmcnt(4)\n\t"
    "v_smfmac_f32_16x16x64_bf16 v[116:119], v[120:123], v[92:99], v124\n\t"
    "ds_read_b64 v[92:93], v186 offset:0\n\t"
    "ds_read_b64 v[94:95], v254 offset:0\n\t"
    "ds_read_b64 v[96:97], v187 offset:0\n\t"
    "ds_read_b64 v[98:99], v255 offset:0\n\t"
    "s_add_u32 s89, s89, 0x8000\n\t"
    "buffer_load_dwordx4 v[144:147], v127, %12, s89 offen offset:1024 sc1\n\t"
    "v_lshrrev_b32 v252, 16, v188\n\t"
    "v_and_b32 v188, s92, v188\n\t"
    "v_lshrrev_b32 v253, 16, v189\n\t"
    "v_cvt_pk_bf16_f32 v30, v116, v117\n\t"
    "v_cvt_pk_bf16_f32 v31, v118, v119\n\t"
    "v_and_b32 v119, s67, v37\n\t"
    "v_lshlrev_b32 v118, 16, v37\n\t"
    "v_and_b32 v117, s67, v36\n\t"
    "v_lshlrev_b32 v116, 16, v36\n\t"
    "v_and_b32 v189, s92, v189\n\t"
    "s_waitcnt lgkmcnt(4)\n\t"
    "v_smfmac_f32_16x16x64_bf16 v[116:119], v[120:123], v[84:91], v124\n\t"
    "ds_read_b64 v[84:85], v188 offset:64000\n\t"
    "ds_read_b64 v[86:87], v252 offset:64000\n\t"
    "ds_read_b64 v[88:89], v189 offset:64000\n\t"
    "ds_read_b64 v[90:91], v253 offset:64000\n\t"
    "v_lshrrev_b32 v254, 16, v190\n\t"
    "v_and_b32 v190, s92, v190\n\t"
    "v_lshrrev_b32 v255, 16, v191\n\t"
    "v_and_b32 v191, s92, v191\n\t"
    "s_waitcnt vmcnt(6) lgkmcnt(4)\n\t"
    "v_smfmac_f32_16x16x64_bf16 v[116:119], v[120:123], v[92:99], v124\n\t"
    "ds_read_b64 v[92:93], v190 offset:64000\n\t"
    "ds_read_b64 v[94:95], v254 offset:64000\n\t"
    "ds_read_b64 v[96:97], v191 offset:64000\n\t"
    "ds_read_b64 v[98:99], v255 offset:64000\n\t"
    "s_add_u32 s90, s90, s95\n\t"
    "buffer_load_dwordx4 v[148:151], v127, %12, s90 offen offset:0 sc1\n\t"
    "v_lshrrev_b32 v252, 16, v236\n\t"
    "v_and_b32 v236, s92, v236\n\t"
    "v_lshrrev_b32 v253, 16, v237\n\t"
    "v_and_b32 v237, s92, v237\n\t"
    "s_waitcnt lgkmcnt(4)\n\t"
    "v_smfmac_f32_16x16x64_bf16 v[116:119], v[120:123], v[84:91], v124\n\t"
    "ds_read_b64 v[84:85], v236 offset:0\n\t"
    "ds_read_b64 v[86:87], v252 offset:0\n\t"
    "ds_read_b64 v[88:89], v237 offset:0\n\t"
    "ds_read_b64 v[90:91], v253 offset:0\n\t"
    "v_lshrrev_b32 v254, 16, v238\n\t"
    "v_and_b32 v238, s92, v238\n\t"
    "v_lshrrev_b32 v255, 16, v239\n\t"
    "v_and_b32 v239, s92, v239\n\t"
    "s_waitcnt vmcnt(6) lgkmcnt(4)\n\t"
    "v_smfmac_f32_16x16x64_bf16 v[116:119], v[120:123], v[92:99], v124\n\t"
    "ds_read_b64 v[92:93], v238 offset:0\n\t"
    "ds_read_b64 v[94:95], v254 offset:0\n\t"
    "ds_read_b64 v[96:97], v239 offset:0\n\t"
    "ds_read_b64 v[98:99], v255 offset:0\n\t"
    "s_add_u32 s90, s90, 0x8000\n\t"
    "buffer_load_dwordx4 v[152:155], v127, %12, s90 offen offset:0 sc1\n\t"
    "v_lshrrev_b32 v252, 16, v192\n\t"
    "v_and_b32 v192, s92, v192\n\t"
    "v_lshrrev_b32 v253, 16, v193\n\t"
    "v_cvt_pk_bf16_f32 v36, v116, v117\n\t"
    "v_cvt_pk_bf16_f32 v37, v118, v119\n\t"
    "v_and_b32 v119, s67, v39\n\t"
    "v_lshlrev_b32 v118, 16, v39\n\t"
    "v_and_b32 v117, s67, v38\n\t"
    "v_lshlrev_b32 v116, 16, v38\n\t"
    "v_and_b32 v193, s92, v193\n\t"
    "s_waitcnt vmcnt(4)\n\t"
    "s_mov_b64 exec, 0xffffffff\n\t"
    "buffer_store_dword v165, v125, %12, s96 offen offset:0 sc1\n\t"
    "s_mov_b64 exec, -1\n\t"
    "s_add_u32 s61, s61, 0x2000\n\t"
    "buffer_store_dwordx4 v[20:23], v127, %12, s61 offen sc1\n\t"
    "s_waitcnt lgkmcnt(4)\n\t"
    "v_smfmac_f32_16x16x64_bf16 v[116:119], v[120:123], v[84:91], v124\n\t"
    "ds_read_b64 v[84:85], v192 offset:64000\n\t"
    "ds_read_b64 v[86:87], v252 offset:64000\n\t"
    "ds_read_b64 v[88:89], v193 offset:64000\n\t"
    "ds_read_b64 v[90:91], v253 offset:64000\n\t"
    "v_lshrrev_b32 v254, 16, v194\n\t"
    "v_and_b32 v194, s92, v194\n\t"
    "v_lshrrev_b32 v255, 16, v195\n\t"
    "v_and_b32 v195, s92, v195\n\t"
    "s_waitcnt lgkmcnt(4)\n\t"
    "v_smfmac_f32_16x16x64_bf16 v[116:119], v[120:123], v[92:99], v124\n\t"
    "ds_read_b64 v[92:93], v194 offset:64000\n\t"
    "ds_read_b64 v[94:95], v254 offset:64000\n\t"
    "ds_read_b64 v[96:97], v195 offset:64000\n\t"
    "ds_read_b64 v[98:99], v255 offset:64000\n\t"
    "s_sub_u32 s90, s90, 0x8000\n\t"
    "buffer_load_dwordx4 v[240:243], v127, %12, s90 offen offset:1024 sc1\n\t"
    "v_lshrrev_b32 v252, 16, v196\n\t"
    "v_and_b32 v196, s92, v196\n\t"
    "v_lshrrev_b32 v253, 16, v197\n\t"
    "v_and_b32 v197, s92, v197\n\t"
    "s_waitcnt lgkmcnt(4)\n\t"
    "v_smfmac_f32_16x16x64_bf16 v[116:119], v[120:123], v[84:91], v124\n\t"
    "ds_read_b64 v[84:85], v196 offset:0\n\t"
    "ds_read_b64 v[86:87], v252 offset:0\n\t"
    "ds_read_b64 v[88:89], v197 offset:0\n\t"
    "ds_read_b64 v[90:91], v253 offset:0\n\t"
    "v_lshrrev_b32 v254, 16, v198\n\t"
    "v_and_b32 v198, s92, v198\n\t"
    "v_lshrrev_b32 v255, 16, v199\n\t"
    "v_and_b32 v199, s92, v199\n\t"
    "s_waitcnt lgkmcnt(4)\n\t"
    "v_smfmac_f32_16x16x64_bf16 v[116:119], v[120:123], v[92:99], v124\n\t"
    "ds_read_b64 v[92:93], v198 offset:0\n\t"
    "ds_read_b64 v[94:95], v254 offset:0\n\t"
    "ds_read_b64 v[96:97], v199 offset:0\n\t"
    "ds_read_b64 v[98:99], v255 offset:0\n\t"
    "s_add_u32 s90, s90, 0x8000\n\t"
    "buffer_load_dwordx4 v[208:211], v127, %12, s90 offen offset:1024 sc1\n\t"
    "v_lshrrev_b32 v252, 16, v200\n\t"
    "v_and_b32 v200, s92, v200\n\t"
    "v_lshrrev_b32 v253, 16, v201\n\t"
    "v_cvt_pk_bf16_f32 v38, v116, v117\n\t"
    "v_cvt_pk_bf16_f32 v39, v118, v119\n\t"
    "v_and_b32 v119, s67, v45\n\t"
    "v_lshlrev_b32 v118, 16, v45\n\t"
    "v_and_b32 v117, s67, v44\n\t"
    "v_lshlrev_b32 v116, 16, v44\n\t"
    "v_and_b32 v201, s92, v201\n\t"
    "s_waitcnt lgkmcnt(4)\n\t"
    "v_smfmac_f32_16x16x64_bf16 v[116:119], v[120:123], v[84:91], v124\n\t"
    "ds_read_b64 v[84:85], v200 offset:64000\n\t"
    "ds_read_b64 v[86:87], v252 offset:64000\n\t"
    "ds_read_b64 v[88:89], v201 offset:64000\n\t"
    "ds_read_b64 v[90:91], v253 offset:64000\n\t"
    "v_lshrrev_b32 v254, 16, v202\n\t"
    "v_and_b32 v202, s92, v202\n\t"
    "v_lshrrev_b32 v255, 16, v203\n\t"
    "v_and_b32 v203, s92, v203\n\t"
    "s_waitcnt vmcnt(7) lgkmcnt(4)\n\t"
    "v_smfmac_f32_16x16x64_bf16 v[116:119], v[120:123], v[92:99], v124\n\t"
    "ds_read_b64 v[92:93], v202 offset:64000\n\t"
    "ds_read_b64 v[94:95], v254 offset:64000\n\t"
    "ds_read_b64 v[96:97], v203 offset:64000\n\t"
    "ds_read_b64 v[98:99], v255 offset:64000\n\t"
    "s_add_u32 s91, s91, s95\n\t"
    "buffer_load_dwordx4 v[244:247], v127, %12, s91 offen offset:0 sc1\n\t"
    "v_lshrrev_b32 v252, 16, v204\n\t"
    "v_and_b32 v204, s92, v204\n\t"
    "v_lshrrev_b32 v253, 16, v205\n\t"
    "v_and_b32 v205, s92, v205\n\t"
    "s_waitcnt lgkmcnt(4)\n\t"
    "v_smfmac_f32_16x16x64_bf16 v[116:119], v[120:123], v[84:91], v124\n\t"
    "ds_read_b64 v[84:85], v204 offset:0\n\t"
    "ds_read_b64 v[86:87], v252 offset:0\n\t"
    "ds_read_b64 v[88:89], v205 offset:0\n\t"
    "ds_read_b64 v[90:91], v253 offset:0\n\t"
    "v_lshrrev_b32 v254, 16, v206\n\t"
    "v_and_b32 v206, s92, v206\n\t"
    "v_lshrrev_b32 v255, 16, v207\n\t"
    "v_and_b32 v207, s92, v207\n\t"
    "s_waitcnt vmcnt(7) lgkmcnt(4)\n\t"
    "v_smfmac_f32_16x16x64_bf16 v[116:119], v[120:123], v[92:99], v124\n\t"
    "ds_read_b64 v[92:93], v206 offset:0\n\t"
    "ds_read_b64 v[94:95], v254 offset:0\n\t"
    "ds_read_b64 v[96:97], v207 offset:0\n\t"
    "ds_read_b64 v[98:99], v255 offset:0\n\t"
    "s_add_u32 s91, s91, 0x8000\n\t"
    "buffer_load_dwordx4 v[248:251], v127, %12, s91 offen offset:0 sc1\n\t"
    "v_lshrrev_b32 v252, 16, v144\n\t"
    "v_and_b32 v144, s92, v144\n\t"
    "v_lshrrev_b32 v253, 16, v145\n\t"
    "v_cvt_pk_bf16_f32 v44, v116, v117\n\t"
    "v_cvt_pk_bf16_f32 v45, v118, v119\n\t"
    "v_and_b32 v119, s67, v47\n\t"
    "v_lshlrev_b32 v118, 16, v47\n\t"
    "v_and_b32 v117, s67, v46\n\t"
    "v_lshlrev_b32 v116, 16, v46\n\t"
    "v_and_b32 v145, s92, v145\n\t"
    "s_add_u32 s61, s61, 0x2000\n\t"
    "buffer_store_dwordx4 v[28:31], v127, %12, s61 offen sc1\n\t"
    "s_waitcnt lgkmcnt(4)\n\t"
    "v_smfmac_f32_16x16x64_bf16 v[116:119], v[120:123], v[84:91], v124\n\t"
    "ds_read_b64 v[84:85], v144 offset:64000\n\t"
    "ds_read_b64 v[86:87], v252 offset:64000\n\t"
    "ds_read_b64 v[88:89], v145 offset:64000\n\t"
    "ds_read_b64 v[90:91], v253 offset:64000\n\t"
    "v_lshrrev_b32 v254, 16, v146\n\t"
    "v_and_b32 v146, s92, v146\n\t"
    "v_lshrrev_b32 v255, 16, v147\n\t"
    "v_and_b32 v147, s92, v147\n\t"
    "s_waitcnt vmcnt(8) lgkmcnt(4)\n\t"
    "v_smfmac_f32_16x16x64_bf16 v[116:119], v[120:123], v[92:99], v124\n\t"
    "ds_read_b64 v[92:93], v146 offset:64000\n\t"
    "ds_read_b64 v[94:95], v254 offset:64000\n\t"
    "ds_read_b64 v[96:97], v147 offset:64000\n\t"
    "ds_read_b64 v[98:99], v255 offset:64000\n\t"
    "s_sub_u32 s91, s91, 0x8000\n\t"
    "buffer_load_dwordx4 v[212:215], v127, %12, s91 offen offset:1024 sc1\n\t"
    "v_lshrrev_b32 v252, 16, v148\n\t"
    "v_and_b32 v148, s92, v148\n\t"
    "v_lshrrev_b32 v253, 16, v149\n\t"
    "v_and_b32 v149, s92, v149\n\t"
    "s_waitcnt lgkmcnt(4)\n\t"
    "v_smfmac_f32_16x16x64_bf16 v[116:119], v[120:123], v[84:91], v124\n\t"
    "ds_read_b64 v[84:85], v148 offset:0\n\t"
    "ds_read_b64 v[86:87], v252 offset:0\n\t"
    "ds_read_b64 v[88:89], v149 offset:0\n\t"
    "ds_read_b64 v[90:91], v253 offset:0\n\t"
    "v_lshrrev_b32 v254, 16, v150\n\t"
    "v_and_b32 v150, s92, v150\n\t"
    "v_lshrrev_b32 v255, 16, v151\n\t"
    "v_and_b32 v151, s92, v151\n\t"
    "s_waitcnt vmcnt(8) lgkmcnt(4)\n\t"
    "v_smfmac_f32_16x16x64_bf16 v[116:119], v[120:123], v[92:99], v124\n\t"
    "ds_read_b64 v[92:93], v150 offset:0\n\t"
    "ds_read_b64 v[94:95], v254 offset:0\n\t"
    "ds_read_b64 v[96:97], v151 offset:0\n\t"
    "ds_read_b64 v[98:99], v255 offset:0\n\t"
    "s_add_u32 s91, s91, 0x8000\n\t"
    "buffer_load_dwordx4 v[216:219], v127, %12, s91 offen offset:1024 sc1\n\t"
    "v_lshrrev_b32 v252, 16, v152\n\t"
    "v_and_b32 v152, s92, v152\n\t"
    "v_lshrrev_b32 v253, 16, v153\n\t"
    "v_cvt_pk_bf16_f32 v46, v116, v117\n\t"
    "v_cvt_pk_bf16_f32 v47, v118, v119\n\t"
    "v_and_b32 v119, s67, v53\n\t"
    "v_lshlrev_b32 v118, 16, v53\n\t"
    "v_and_b32 v117, s67, v52\n\t"
    "v_lshlrev_b32 v116, 16, v52\n\t"
    "v_and_b32 v153, s92, v153\n\t"
    "s_waitcnt lgkmcnt(4)\n\t"
    "v_smfmac_f32_16x16x64_bf16 v[116:119], v[120:123], v[84:91], v124\n\t"
    "ds_read_b64 v[84:85], v152 offset:64000\n\t"
    "ds_read_b64 v[86:87], v252 offset:64000\n\t"
    "ds_read_b64 v[88:89], v153 offset:64000\n\t"
    "ds_read_b64 v[90:91], v253 offset:64000\n\t"
    "v_lshrrev_b32 v254, 16, v154\n\t"
    "v_and_b32 v154, s92, v154\n\t"
    "v_lshrrev_b32 v255, 16, v155\n\t"
    "v_and_b32 v155, s92, v155\n\t"
    "s_waitcnt vmcnt(6) lgkmcnt(4)\n\t"
    "v_smfmac_f32_16x16x64_bf16 v[116:119], v[120:123], v[92:99], v124\n\t"
    "ds_read_b64 v[92:93], v154 offset:64000\n\t"
    "ds_read_b64 v[94:95], v254 offset:64000\n\t"
    "ds_read_b64 v[96:97], v155 offset:64000\n\t"
    "ds_read_b64 v[98:99], v255 offset:64000\n\t"
    "s_sub_u32 s84, s84, s95\n\t"
    "buffer_load_dwordx4 v[220:223], v127, %12, s84 offen offset:0 sc1\n\t"
    "v_lshrrev_b32 v252, 16, v240\n\t"
    "v_and_b32 v240, s92, v240\n\t"
    "v_lshrrev_b32 v253, 16, v241\n\t"
    "v_and_b32 v241, s92, v241\n\t"
    "s_waitcnt lgkmcnt(4)\n\t"
    "v_smfmac_f32_16x16x64_bf16 v[116:119], v[120:123], v[84:91], v124\n\t"
    "ds_read_b64 v[84:85], v240 offset:0\n\t"
    "ds_read_b64 v[86:87], v252 offset:0\n\t"
    "ds_read_b64 v[88:89], v241 offset:0\n\t"
    "ds_read_b64 v[90:91], v253 offset:0\n\t"
    "v_lshrrev_b32 v254, 16, v242\n\t"
    "v_and_b32 v242, s92, v242\n\t"
    "v_lshrrev_b32 v255, 16, v243\n\t"
    "v_and_b32 v243, s92, v243\n\t"
    "s_cmp_lt_u32 %16, 4\n\t"
    "s_cbranch_scc1 L_tp195_%=\n\t"
    "s_setprio 0\n\t"
    "L_tp195_%=:\n\t"
    "s_waitcnt vmcnt(6) lgkmcnt(4)\n\t"
    "v_smfmac_f32_16x16x64_bf16 v[116:119], v[120:123], v[92:99], v124\n\t"
    "ds_read_b64 v[92:93], v242 offset:0\n\t"
    "ds_read_b64 v[94:95], v254 offset:0\n\t"
    "ds_read_b64 v[96:97], v243 offset:0\n\t"
    "ds_read_b64 v[98:99], v255 offset:0\n\t"
    "buffer_load_dwordx4 v[224:227], v127, %12, s84 offen offset:1024 sc1\n\t"
    "v_lshrrev_b32 v252, 16, v208\n\t"
    "v_and_b32 v208, s92, v208\n\t"
    "v_lshrrev_b32 v253, 16, v209\n\t"
    "v_cvt_pk_bf16_f32 v52, v116, v117\n\t"
    "v_cvt_pk_bf16_f32 v53, v118, v119\n\t"
    "v_and_b32 v119, s67, v55\n\t"
    "v_lshlrev_b32 v118, 16, v55\n\t"
    "v_and_b32 v117, s67, v54\n\t"
    "v_lshlrev_b32 v116, 16, v54\n\t"
    "v_and_b32 v209, s92, v209\n\t"
    "s_waitcnt vmcnt(4)\n\t"
    "s_mov_b64 exec, 0xffffffff\n\t"
    "buffer_store_dword v165, v125, %12, s96 offen offset:128 sc1\n\t"
    "s_mov_b64 exec, -1\n\t"
    "s_add_u32 s61, s61, 0x2000\n\t"
    "buffer_store_dwordx4 v[36:39], v127, %12, s61 offen sc1\n\t"
    "s_waitcnt lgkmcnt(4)\n\t"
    "v_smfmac_f32_16x16x64_bf16 v[116:119], v[120:123], v[84:91], v124\n\t"
    "ds_read_b64 v[84:85], v208 offset:64000\n\t"
    "ds_read_b64 v[86:87], v252 offset:64000\n\t"
    "ds_read_b64 v[88:89], v209 offset:64000\n\t"
    "ds_read_b64 v[90:91], v253 offset:64000\n\t"
    "v_lshrrev_b32 v254, 16, v210\n\t"
    "v_and_b32 v210, s92, v210\n\t"
    "v_lshrrev_b32 v255, 16, v211\n\t"
    "v_and_b32 v211, s92, v211\n\t"
    "s_waitcnt lgkmcnt(4)\n\t"
    "v_smfmac_f32_16x16x64_bf16 v[116:119], v[120:123], v[92:99], v124\n\t"
    "ds_read_b64 v[92:93], v210 offset:64000\n\t"
    "ds_read_b64 v[94:95], v254 offset:64000\n\t"
    "ds_read_b64 v[96:97], v211 offset:64000\n\t"
    "ds_read_b64 v[98:99], v255 offset:64000\n\t"
    "s_sub_u32 s85, s85, s95\n\t"
    "buffer_load_dwordx4 v[228:231], v127, %12, s85 offen offset:0 sc1\n\t"
    "v_lshrrev_b32 v252, 16, v244\n\t"
    "v_and_b32 v244, s92, v244\n\t"
    "v_lshrrev_b32 v253, 16, v245\n\t"
    "v_and_b32 v245, s92, v245\n\t"
    "s_waitcnt lgkmcnt(4)\n\t"
    "v_smfmac_f32_16x16x64_bf16 v[116:119], v[120:123], v[84:91], v124\n\t"
    "ds_read_b64 v[84:85], v244 offset:0\n\t"
    "ds_read_b64 v[86:87], v252 offset:0\n\t"
    "ds_read_b64 v[88:89], v245 offset:0\n\t"
    "ds_read_b64 v[90:91], v253 offset:0\n\t"
    "v_lshrrev_b32 v254, 16, v246\n\t"
    "v_and_b32 v246, s92, v246\n\t"
    "v_lshrrev_b32 v255, 16, v247\n\t"
    "v_and_b32 v247, s92, v247\n\t"
    "s_waitcnt lgkmcnt(4)\n\t"
    "v_smfmac_f32_16x16x64_bf16 v[116:119], v[120:123], v[92:99], v124\n\t"
    "ds_read_b64 v[92:93], v246 offset:0\n\t"
    "ds_read_b64 v[94:95], v254 offset:0\n\t"
    "ds_read_b64 v[96:97], v247 offset:0\n\t"
    "ds_read_b64 v[98:99], v255 offset:0\n\t"
    "buffer_load_dwordx4 v[232:235], v127, %12, s85 offen offset:1024 sc1\n\t"
    "v_lshrrev_b32 v252, 16, v248\n\t"
    "v_and_b32 v248, s92, v248\n\t"
    "v_lshrrev_b32 v253, 16, v249\n\t"
    "v_cvt_pk_bf16_f32 v54, v116, v117\n\t"
    "v_cvt_pk_bf16_f32 v55, v118, v119\n\t"
    "v_and_b32 v119, s67, v61\n\t"
    "v_lshlrev_b32 v118, 16, v61\n\t"
    "v_and_b32 v117, s67, v60\n\t"
    "v_lshlrev_b32 v116, 16, v60\n\t"
    "v_and_b32 v249, s92, v249\n\t"
    "s_waitcnt lgkmcnt(4)\n\t"
    "v_smfmac_f32_16x16x64_bf16 v[116:119], v[120:123], v[84:91], v124\n\t"
    "ds_read_b64 v[84:85], v248 offset:64000\n\t"
    "ds_read_b64 v[86:87], v252 offset:64000\n\t"
    "ds_read_b64 v[88:89], v249 offset:64000\n\t"
    "ds_read_b64 v[90:91], v253 offset:64000\n\t"
    "v_lshrrev_b32 v254, 16, v250\n\t"
    "v_and_b32 v250, s92, v250\n\t"
    "v_lshrrev_b32 v255, 16, v251\n\t"
    "v_and_b32 v251, s92, v251\n\t"
    "s_waitcnt vmcnt(7) lgkmcnt(4)\n\t"
    "v_smfmac_f32_16x16x64_bf16 v[116:119], v[120:123], v[92:99], v124\n\t"
    "ds_read_b64 v[92:93], v250 offset:64000\n\t"
    "ds_read_b64 v[94:95], v254 offset:64000\n\t"
    "ds_read_b64 v[96:97], v251 offset:64000\n\t"
    "ds_read_b64 v[98:99], v255 offset:64000\n\t"
    "s_sub_u32 s86, s86, s95\n\t"
    "buffer_load_dwordx4 v[168:171], v127, %12, s86 offen offset:0 sc1\n\t"
    "v_lshrrev_b32 v252, 16, v212\n\t"
    "v_and_b32 v212, s92, v212\n\t"
    "v_lshrrev_b32 v253, 16, v213\n\t"
    "v_and_b32 v213, s92, v213\n\t"
    "s_waitcnt lgkmcnt(4)\n\t"
    "v_smfmac_f32_16x16x64_bf16 v[116:119], v[120:123], v[84:91], v124\n\t"
    "ds_read_b64 v[84:85], v212 offset:0\n\t"
    "ds_read_b64 v[86:87], v252 offset:0\n\t"
    "ds_read_b64 v[88:89], v213 offset:0\n\t"
    "ds_read_b64 v[90:91], v253 offset:0\n\t"
    "v_lshrrev_b32 v254, 16, v214\n\t"
    "v_and_b32 v214, s92, v214\n\t"
    "v_lshrrev_b32 v255, 16, v215\n\t"
    "v_and_b32 v215, s92, v215\n\t"
    "s_waitcnt vmcnt(7) lgkmcnt(4)\n\t"
    "v_smfmac_f32_16x16x64_bf16 v[116:119], v[120:123], v[92:99], v124\n\t"
    "ds_read_b64 v[92:93], v214 offset:0\n\t"
    "ds_read_b64 v[94:95], v254 offset:0\n\t"
    "ds_read_b64 v[96:97], v215 offset:0\n\t"
    "ds_read_b64 v[98:99], v255 offset:0\n\t"
    "s_add_u32 s84, s84, 0x8000\n\t"
    "buffer_load_dwordx4 v[112:115], v127, %12, s84 offen offset:0 sc1\n\t"
    "v_lshrrev_b32 v252, 16, v216\n\t"
    "v_and_b32 v216, s92, v216\n\t"
    "v_lshrrev_b32 v253, 16, v217\n\t"
    "v_cvt_pk_bf16_f32 v60, v116, v117\n\t"
    "v_cvt_pk_bf16_f32 v61, v118, v119\n\t"
    "v_and_b32 v119, s67, v63\n\t"
    "v_lshlrev_b32 v118, 16, v63\n\t"
    "v_and_b32 v117, s67, v62\n\t"
    "v_lshlrev_b32 v116, 16, v62\n\t"
    "v_and_b32 v217, s92, v217\n\t"
    "s_add_u32 s61, s61, 0x2000\n\t"
    "buffer_store_dwordx4 v[44:47], v127, %12, s61 offen sc1\n\t"
    "s_waitcnt lgkmcnt(4)\n\t"
    "v_smfmac_f32_16x16x64_bf16 v[116:119], v[120:123], v[84:91], v124\n\t"
    "ds_read_b64 v[84:85], v216 offset:64000\n\t"
    "ds_read_b64 v[86:87], v252 offset:64000\n\t"
    "ds_read_b64 v[88:89], v217 offset:64000\n\t"
    "ds_read_b64 v[90:91], v253 offset:64000\n\t"
    "v_lshrrev_b32 v254, 16, v218\n\t"
    "v_and_b32 v218, s92, v218\n\t"
    "v_lshrrev_b32 v255, 16, v219\n\t"
    "v_and_b32 v219, s92, v219\n\t"
    "s_mov_b64 exec, 1\n\t"
    "buffer_load_dword v164, off, %12, s93 offset:0 sc1\n\t"
    "s_mov_b64 exec, -1\n\t"
    "s_waitcnt vmcnt(9) lgkmcnt(4)\n\t"
    "v_smfmac_f32_16x16x64_bf16 v[116:119], v[120:123], v[92:99], v124\n\t"
    "ds_read_b64 v[92:93], v218 offset:64000\n\t"
    "ds_read_b64 v[94:95], v254 offset:64000\n\t"
    "ds_read_b64 v[96:97], v219 offset:64000\n\t"
    "ds_read_b64 v[98:99], v255 offset:64000\n\t"
    "buffer_load_dwordx4 v[100:103], v127, %12, s86 offen offset:1024 sc1\n\t"
    "v_lshrrev_b32 v252, 16, v220\n\t"
    "v_and_b32 v220, s92, v220\n\t"
    "v_lshrrev_b32 v253, 16, v221\n\t"
    "v_and_b32 v221, s92, v221\n\t"
    "s_waitcnt lgkmcnt(4)\n\t"
    "v_smfmac_f32_16x16x64_bf16 v[116:119], v[120:123], v[84:91], v124\n\t"
    "ds_read_b64 v[84:85], v220 offset:0\n\t"
    "ds_read_b64 v[86:87], v252 offset:0\n\t"
    "ds_read_b64 v[88:89], v221 offset:0\n\t"
    "ds_read_b64 v[90:91], v253 offset:0\n\t"
    "v_lshrrev_b32 v254, 16, v222\n\t"
    "v_and_b32 v222, s92, v222\n\t"
    "v_lshrrev_b32 v255, 16, v223\n\t"
    "v_and_b32 v223, s92, v223\n\t"
    "s_cmp_lt_u32 %16, 4\n\t"
    "s_cbranch_scc1 L_tp207_%=\n\t"
    "s_setprio 1\n\t"
    "L_tp207_%=:\n\t"
    "s_waitcnt vmcnt(9) lgkmcnt(4)\n\t"
    "v_smfmac_f32_16x16x64_bf16 v[116:119], v[120:123], v[92:99], v124\n\t"
    "ds_read_b64 v[92:93], v222 offset:0\n\t"
    "ds_read_b64 v[94:95], v254 offset:0\n\t"
    "ds_read_b64 v[96:97], v223 offset:0\n\t"
    "ds_read_b64 v[98:99], v255 offset:0\n\t"
    "buffer_load_dwordx4 v[104:107], v127, %12, s84 offen offset:1024 sc1\n\t"
    "v_lshrrev_b32 v252, 16, v224\n\t"
    "v_and_b32 v224, s92, v224\n\t"
    "v_lshrrev_b32 v253, 16, v225\n\t"
    "v_cvt_pk_bf16_f32 v62, v116, v117\n\t"
    "v_cvt_pk_bf16_f32 v63, v118, v119\n\t"
    "v_mov_b64 v[172:173], s[70:71]\n\t"
    "v_mov_b64 v[174:175], s[72:73]\n\t"
    "v_and_b32 v225, s92, v225\n\t"
    "s_waitcnt lgkmcnt(4)\n\t"
    "v_smfmac_f32_16x16x64_bf16 v[172:175], v[120:123], v[84:91], v124\n\t"
    "ds_read_b64 v[84:85], v224 offset:0\n\t"
    "ds_read_b64 v[86:87], v252 offset:0\n\t"
    "ds_read_b64 v[88:89], v225 offset:0\n\t"
    "ds_read_b64 v[90:91], v253 offset:0\n\t"
    "v_lshrrev_b32 v254, 16, v226\n\t"
    "v_and_b32 v226, s92, v226\n\t"
    "v_lshrrev_b32 v255, 16, v227\n\t"
    "v_and_b32 v227, s92, v227\n\t"
    "s_waitcnt vmcnt(7) lgkmcnt(4)\n\t"
    "v_smfmac_f32_16x16x64_bf16 v[172:175], v[120:123], v[92:99], v124\n\t"
    "ds_read_b64 v[92:93], v226 offset:0\n\t"
    "ds_read_b64 v[94:95], v254 offset:0\n\t"
    "ds_read_b64 v[96:97], v227 offset:0\n\t"
    "ds_read_b64 v[98:99], v255 offset:0\n\t"
    "s_sub_u32 s87, s87, s95\n\t"
    "buffer_load_dwordx4 v[108:111], v127, %12, s87 offen offset:0 sc1\n\t"
    "v_lshrrev_b32 v252, 16, v228\n\t"
    "v_and_b32 v228, s92, v228\n\t"
    "v_lshrrev_b32 v253, 16, v229\n\t"
    "v_mov_b64 v[176:177], s[70:71]\n\t"
    "v_mov_b64 v[178:179], s[72:73]\n\t"
    "v_and_b32 v229, s92, v229\n\t"
    "s_waitcnt lgkmcnt(4)\n\t"
    "v_smfmac_f32_16x16x64_bf16 v[176:179], v[120:123], v[84:91], v124\n\t"
    "ds_read_b64 v[84:85], v228 offset:0\n\t"
    "ds_read_b64 v[86:87], v252 offset:0\n\t"
    "ds_read_b64 v[88:89], v229 offset:0\n\t"
    "ds_read_b64 v[90:91], v253 offset:0\n\t"
    "v_lshrrev_b32 v254, 16, v230\n\t"
    "v_and_b32 v230, s92, v230\n\t"
    "v_lshrrev_b32 v255, 16, v231\n\t"
    "v_and_b32 v231, s92, v231\n\t"
    "s_waitcnt vmcnt(3)\n\t"
    "v_readfirstlane_b32 s65, v164\n\t"
    "s_nop 0\n\t"
    "s_cmp_eq_u32 s65, 0\n\t"
    "s_cbranch_scc1 L_xw0_%=\n\t"
    "L_xwb0_%=:\n\t"
    "buffer_load_dwordx4 v[4:7], v127, %12, s94 offen sc1\n\t"
    "s_waitcnt lgkmcnt(4)\n\t"
    "v_smfmac_f32_16x16x64_bf16 v[176:179], v[120:123], v[92:99], v124\n\t"
    "ds_read_b64 v[92:93], v230 offset:0\n\t"
    "ds_read_b64 v[94:95], v254 offset:0\n\t"
    "ds_read_b64 v[96:97], v231 offset:0\n\t"
    "ds_read_b64 v[98:99], v255 offset:0\n\t"
    "s_add_u32 s85, s85, 0x8000\n\t"
    "buffer_load_dwordx4 v[200:203], v127, %12, s85 offen offset:0 sc1\n\t"
    "v_lshrrev_b32 v252, 16, v232\n\t"
    "v_and_b32 v232, s92, v232\n\t"
    "v_lshrrev_b32 v253, 16, v233\n\t"
    "v_mov_b64 v[180:181], s[70:71]\n\t"
    "v_mov_b64 v[182:183], s[72:73]\n\t"
    "v_and_b32 v233, s92, v233\n\t"
    "s_mov_b64 exec, 0xffffffff\n\t"
    "buffer_store_dword v165, v125, %12, s96 offen offset:256 sc1\n\t"
    "s_mov_b64 exec, -1\n\t"
    "s_add_u32 s61, s61, 0x2000\n\t"
    "buffer_store_dwordx4 v[52:55], v127, %12, s61 offen sc1\n\t"
    "s_waitcnt lgkmcnt(4)\n\t"
    "v_smfmac_f32_16x16x64_bf16 v[180:183], v[120:123], v[84:91], v124\n\t"
    "ds_read_b64 v[84:85], v232 offset:0\n\t"
    "ds_read_b64 v[86:87], v252 offset:0\n\t"
    "ds_read_b64 v[88:89], v233 offset:0\n\t"
    "ds_read_b64 v[90:91], v253 offset:0\n\t"
    "v_lshrrev_b32 v254, 16, v234\n\t"
    "v_and_b32 v234, s92, v234\n\t"
    "v_lshrrev_b32 v255, 16, v235\n\t"
    "v_and_b32 v235, s92, v235\n\t"
    "s_waitcnt lgkmcnt(4)\n\t"
    "v_smfmac_f32_16x16x64_bf16 v[180:183], v[120:123], v[92:99], v124\n\t"
    "ds_read_b64 v[92:93], v234 offset:0\n\t"
    "ds_read_b64 v[94:95], v254 offset:0\n\t"
    "ds_read_b64 v[96:97], v235 offset:0\n\t"
    "ds_read_b64 v[98:99], v255 offset:0\n\t"
    "buffer_load_dwordx4 v[204:207], v127, %12, s87 offen offset:1024 sc1\n\t"
    "v_lshrrev_b32 v252, 16, v168\n\t"
    "v_and_b32 v168, s92, v168\n\t"
    "v_lshrrev_b32 v253, 16, v169\n\t"
    "v_mov_b64 v[184:185], s[70:71]\n\t"
    "v_mov_b64 v[186:187], s[72:73]\n\t"
    "v_and_b32 v169, s92, v169\n\t"
    "s_waitcnt lgkmcnt(4)\n\t"
    "v_smfmac_f32_16x16x64_bf16 v[184:187], v[120:123], v[84:91], v124\n\t"
    "ds_read_b64 v[84:85], v168 offset:0\n\t"
    "ds_read_b64 v[86:87], v252 offset:0\n\t"
    "ds_read_b64 v[88:89], v169 offset:0\n\t"
    "ds_read_b64 v[90:91], v253 offset:0\n\t"
    "v_lshrrev_b32 v254, 16, v170\n\t"
    "v_and_b32 v170, s92, v170\n\t"
    "v_lshrrev_b32 v255, 16, v171\n\t"
    "v_and_b32 v171, s92, v171\n\t"
    "s_add_u32 s94, s94, 0x2000\n\t"
    "buffer_load_dwordx4 v[12:15], v127, %12, s94 offen sc1\n\t"
    "s_waitcnt lgkmcnt(4)\n\t"
    "v_smfmac_f32_16x16x64_bf16 v[184:187], v[120:123], v[92:99], v124\n\t"
    "ds_read_b64 v[92:93], v170 offset:0\n\t"
    "ds_read_b64 v[94:95], v254 offset:0\n\t"
    "ds_read_b64 v[96:97], v171 offset:0\n\t"
    "ds_read_b64 v[98:99], v255 offset:0\n\t"
    "buffer_load_dwordx4 v[144:147], v127, %12, s85 offen offset:1024 sc1\n\t"
    "v_lshrrev_b32 v252, 16, v112\n\t"
    "v_and_b32 v112, s92, v112\n\t"
    "v_lshrrev_b32 v253, 16, v113\n\t"
    "v_mov_b64 v[188:189], s[70:71]\n\t"
    "v_mov_b64 v[190:191], s[72:73]\n\t"
    "v_and_b32 v113, s92, v113\n\t"
    "s_waitcnt lgkmcnt(4)\n\t"
    "v_smfmac_f32_16x16x64_bf16 v[188:191], v[120:123], v[84:91], v124\n\t"
    "ds_read_b64 v[84:85], v112 offset:64000\n\t"
    "ds_read_b64 v[86:87], v252 offset:64000\n\t"
    "ds_read_b64 v[88:89], v113 offset:64000\n\t"
    "ds_read_b64 v[90:91], v253 offset:64000\n\t"
    "v_lshrrev_b32 v254, 16, v114\n\t"
    "v_and_b32 v114, s92, v114\n\t"
    "v_lshrrev_b32 v255, 16, v115\n\t"
    "v_and_b32 v115, s92, v115\n\t"
    "s_waitcnt vmcnt(9) lgkmcnt(4)\n\t"
    "v_smfmac_f32_16x16x64_bf16 v[188:191], v[120:123], v[92:99], v124\n\t"
    "ds_read_b64 v[92:93], v114 offset:64000\n\t"
    "ds_read_b64 v[94:95], v254 offset:64000\n\t"
    "ds_read_b64 v[96:97], v115 offset:64000\n\t"
    "ds_read_b64 v[98:99], v255 offset:64000\n\t"
    "s_sub_u32 s88, s88, s95\n\t"
    "buffer_load_dwordx4 v[148:151], v127, %12, s88 offen offset:0 sc1\n\t"
    "v_lshrrev_b32 v252, 16, v100\n\t"
    "v_and_b32 v100, s92, v100\n\t"
    "v_lshrrev_b32 v253, 16, v101\n\t"
    "v_and_b32 v101, s92, v101\n\t"
    "s_waitcnt lgkmcnt(4)\n\t"
    "v_smfmac_f32_16x16x64_bf16 v[172:175], v[120:123], v[84:91], v124\n\t"
    "ds_read_b64 v[84:85], v100 offset:0\n\t"
    "ds_read_b64 v[86:87], v252 offset:0\n\t"
    "ds_read_b64 v[88:89], v101 offset:0\n\t"
    "ds_read_b64 v[90:91], v253 offset:0\n\t"
    "v_lshrrev_b32 v254, 16, v102\n\t"
    "v_and_b32 v102, s92, v102\n\t"
    "v_lshrrev_b32 v255, 16, v103\n\t"
    "v_and_b32 v103, s92, v103\n\t"
    "s_waitcnt vmcnt(7)\n\t"
    "v_smfmac_f32_16x16x64_bf16 v[172:175], v[156:159], v[0:7], v124\n\t"
    "s_waitcnt lgkmcnt(4)\n\t"
    "v_smfmac_f32_16x16x64_bf16 v[172:175], v[120:123], v[92:99], v124\n\t"
    "ds_read_b64 v[92:93], v102 offset:0\n\t"
    "ds_read_b64 v[94:95], v254 offset:0\n\t"
    "ds_read_b64 v[96:97], v103 offset:0\n\t"
    "ds_read_b64 v[98:99], v255 offset:0\n\t"
    "s_add_u32 s86, s86, 0x8000\n\t"
    "buffer_load_dwordx4 v[152:155], v127, %12, s86 offen offset:0 sc1\n\t"
    "v_lshrrev_b32 v252, 16, v104\n\t"
    "v_and_b32 v104, s92, v104\n\t"
    "v_lshrrev_b32 v253, 16, v105\n\t"
    "v_mov_b64 v[192:193], s[70:71]\n\t"
    "v_mov_b64 v[194:195], s[72:73]\n\t"
    "v_and_b32 v105, s92, v105\n\t"
    "s_add_u32 s61, s61, 0x2000\n\t"
    "buffer_store_dwordx4 v[60:63], v127, %12, s61 offen sc1\n\t"
    "v_smfmac_f32_16x16x64_bf16 v[176:179], v[160:163], v[0:7], v124\n\t"
    "v_pk_mul_f32 v[64:65], v[172:173], v[172:173]\n\t"
    "v_pk_mul_f32 v[66:67], v[174:175], v[174:175]\n\t"
    "v_pk_fma_f32 v[64:65], v[64:65], v[82:83], v[80:81]\n\t"
    "v_pk_fma_f32 v[66:67], v[66:67], v[82:83], v[80:81]\n\t"
    "v_pk_mul_f32 v[64:65], v[64:65], v[172:173]\n\t"
    "s_waitcnt lgkmcnt(4)\n\t"
    "v_smfmac_f32_16x16x64_bf16 v[192:195], v[120:123], v[84:91], v124\n\t"
    "ds_read_b64 v[84:85], v104 offset:64000\n\t"
    "ds_read_b64 v[86:87], v252 offset:64000\n\t"
    "ds_read_b64 v[88:89], v105 offset:64000\n\t"
    "ds_read_b64 v[90:91], v253 offset:64000\n\t"
    "v_lshrrev_b32 v254, 16, v106\n\t"
    "v_and_b32 v106, s92, v106\n\t"
    "v_lshrrev_b32 v255, 16, v107\n\t"
    "v_and_b32 v107, s92, v107\n\t"
    "s_mov_b64 exec, 1\n\t"
    "buffer_load_dword v164, off, %12, s93 offset:128 sc1\n\t"
    "s_mov_b64 exec, -1\n\t"
    "v_pk_mul_f32 v[66:67], v[66:67], v[174:175]\n\t"
    "v_exp_f32 v68, v64\n\t"
    "v_exp_f32 v69, v65\n\t"
    "v_exp_f32 v70, v66\n\t"
    "v_exp_f32 v71, v67\n\t"
    "s_waitcnt lgkmcnt(4)\n\t"
    "v_smfmac_f32_16x16x64_bf16 v[192:195], v[120:123], v[92:99], v124\n\t"
    "ds_read_b64 v[92:93], v106 offset:64000\n\t"
    "ds_read_b64 v[94:95], v254 offset:64000\n\t"
    "ds_read_b64 v[96:97], v107 offset:64000\n\t"
    "ds_read_b64 v[98:99], v255 offset:64000\n\t"
    "buffer_load_dwordx4 v[244:247], v127, %12, s88 offen offset:1024 sc1\n\t"
    "v_lshrrev_b32 v252, 16, v108\n\t"
    "v_and_b32 v108, s92, v108\n\t"
    "v_lshrrev_b32 v253, 16, v109\n\t"
    "v_and_b32 v109, s92, v109\n\t"
    "v_pk_fma_f32 v[68:69], v[68:69], s[40:41], s[40:41]\n\t"
    "v_pk_fma_f32 v[70:71], v[70:71], s[42:43], s[42:43]\n\t"
    "v_rcp_f32 v68, v68\n\t"
    "v_rcp_f32 v69, v69\n\t"
    "v_rcp_f32 v70, v70\n\t"
    "s_waitcnt lgkmcnt(4)\n\t"
    "v_smfmac_f32_16x16x64_bf16 v[176:179], v[120:123], v[84:91], v124\n\t"
    "ds_read_b64 v[84:85], v108 offset:0\n\t"
    "ds_read_b64 v[86:87], v252 offset:0\n\t"
    "ds_read_b64 v[88:89], v109 offset:0\n\t"
    "ds_read_b64 v[90:91], v253 offset:0\n\t"
    "v_lshrrev_b32 v254, 16, v110\n\t"
    "v_and_b32 v110, s92, v110\n\t"
    "v_lshrrev_b32 v255, 16, v111\n\t"
    "v_and_b32 v111, s92, v111\n\t"
    "s_waitcnt vmcnt(6)\n\t"
    "v_smfmac_f32_16x16x64_bf16 v[180:183], v[156:159], v[8:15], v124\n\t"
    "v_rcp_f32 v71, v71\n\t"
    "v_pk_mul_f32 v[68:69], v[68:69], v[172:173]\n\t"
    "v_pk_fma_f32 v[68:69], v[70:71], v[174:175], v[68:69]\n\t"
    "v_add_f32 v236, v68, v69\n\t"
    "s_waitcnt lgkmcnt(4)\n\t"
    "v_smfmac_f32_16x16x64_bf16 v[176:179], v[120:123], v[92:99], v124\n\t"
    "ds_read_b64 v[92:93], v110 offset:0\n\t"
    "ds_read_b64 v[94:95], v254 offset:0\n\t"
    "ds_read_b64 v[96:97], v111 offset:0\n\t"
    "ds_read_b64 v[98:99], v255 offset:0\n\t"
    "buffer_load_dwordx4 v[248:251], v127, %12, s86 offen offset:1024 sc1\n\t"
    "v_lshrrev_b32 v252, 16, v200\n\t"
    "v_and_b32 v200, s92, v200\n\t"
    "v_lshrrev_b32 v253, 16, v201\n\t"
    "v_mov_b64 v[196:197], s[70:71]\n\t"
    "v_mov_b64 v[198:199], s[72:73]\n\t"
    "v_and_b32 v201, s92, v201\n\t"
    "v_smfmac_f32_16x16x64_bf16 v[184:187], v[160:163], v[8:15], v124\n\t"
    "v_pk_mul_f32 v[72:73], v[176:177], v[176:177]\n\t"
    "v_pk_mul_f32 v[74:75], v[178:179], v[178:179]\n\t"
    "v_pk_fma_f32 v[72:73], v[72:73], v[82:83], v[80:81]\n\t"
    "v_pk_fma_f32 v[74:75], v[74:75], v[82:83], v[80:81]\n\t"
    "v_pk_mul_f32 v[72:73], v[72:73], v[176:177]\n\t"
    "s_waitcnt lgkmcnt(4)\n\t"
    "v_smfmac_f32_16x16x64_bf16 v[196:199], v[120:123], v[84:91], v124\n\t"
    "ds_read_b64 v[84:85], v200 offset:64000\n\t"
    "ds_read_b64 v[86:87], v252 offset:64000\n\t"
    "ds_read_b64 v[88:89], v201 offset:64000\n\t"
    "ds_read_b64 v[90:91], v253 offset:64000\n\t"
    "v_lshrrev_b32 v254, 16, v202\n\t"
    "v_and_b32 v202, s92, v202\n\t"
    "v_lshrrev_b32 v255, 16, v203\n\t"
    "v_and_b32 v203, s92, v203\n\t"
    "v_pk_mul_f32 v[74:75], v[74:75], v[178:179]\n\t"
    "v_exp_f32 v76, v72\n\t"
    "v_exp_f32 v77, v73\n\t"
    "v_exp_f32 v78, v74\n\t"
    "v_exp_f32 v79, v75\n\t"
    "s_waitcnt lgkmcnt(4)\n\t"
    "v_smfmac_f32_16x16x64_bf16 v[196:199], v[120:123], v[92:99], v124\n\t"
    "ds_read_b64 v[92:93], v202 offset:64000\n\t"
    "ds_read_b64 v[94:95], v254 offset:64000\n\t"
    "ds_read_b64 v[96:97], v203 offset:64000\n\t"
    "ds_read_b64 v[98:99], v255 offset:64000\n\t"
    "s_sub_u32 s89, s89, s95\n\t"
    "buffer_load_dwordx4 v[212:215], v127, %12, s89 offen offset:0 sc1\n\t"
    "v_lshrrev_b32 v252, 16, v204\n\t"
    "v_and_b32 v204, s92, v204\n\t"
    "v_lshrrev_b32 v253, 16, v205\n\t"
    "v_and_b32 v205, s92, v205\n\t"
    "v_pk_fma_f32 v[76:77], v[76:77], s[40:41], s[40:41]\n\t"
    "v_pk_fma_f32 v[78:79], v[78:79], s[42:43], s[42:43]\n\t"
    "v_rcp_f32 v76, v76\n\t"
    "v_rcp_f32 v77, v77\n\t"
    "v_rcp_f32 v78, v78\n\t"
    "s_waitcnt lgkmcnt(4)\n\t"
    "v_smfmac_f32_16x16x64_bf16 v[180:183], v[120:123], v[84:91], v124\n\t"
    "ds_read_b64 v[84:85], v204 offset:0\n\t"
    "ds_read_b64 v[86:87], v252 offset:0\n\t"
    "ds_read_b64 v[88:89], v205 offset:0\n\t"
    "ds_read_b64 v[90:91], v253 offset:0\n\t"
    "v_lshrrev_b32 v254, 16, v206\n\t"
    "v_and_b32 v206, s92, v206\n\t"
    "v_lshrrev_b32 v255, 16, v207\n\t"
    "v_and_b32 v207, s92, v207\n\t"
    "s_waitcnt vmcnt(3)\n\t"
    "v_readfirstlane_b32 s65, v164\n\t"
    "s_nop 0\n\t"
    "s_cmp_eq_u32 s65, 0\n\t"
    "s_cbranch_scc1 L_xw1_%=\n\t"
    "L_xwb1_%=:\n\t"
    "s_add_u32 s94, s94, 0x2000\n\t"
    "buffer_load_dwordx4 v[20:23], v127, %12, s94 offen sc1\n\t"
    "v_rcp_f32 v79, v79\n\t"
    "v_pk_mul_f32 v[76:77], v[76:77], v[176:177]\n\t"
    "v_pk_fma_f32 v[76:77], v[78:79], v[178:179], v[76:77]\n\t"
    "v_add_f32 v237, v76, v77\n\t"
    "s_cmp_lt_u32 %16, 4\n\t"
    "s_cbranch_scc1 L_tp227_%=\n\t"
    "s_setprio 0\n\t"
    "L_tp227_%=:\n\t"
    "s_waitcnt lgkmcnt(4)\n\t"
    "v_smfmac_f32_16x16x64_bf16 v[180:183], v[120:123], v[92:99], v124\n\t"
    "ds_read_b64 v[92:93], v206 offset:0\n\t"
    "ds_read_b64 v[94:95], v254 offset:0\n\t"
    "ds_read_b64 v[96:97], v207 offset:0\n\t"
    "ds_read_b64 v[98:99], v255 offset:0\n\t"
    "s_add_u32 s87, s87, 0x8000\n\t"
    "buffer_load_dwordx4 v[216:219], v127, %12, s87 offen offset:0 sc1\n\t"
    "v_lshrrev_b32 v252, 16, v144\n\t"
    "v_and_b32 v144, s92, v144\n\t"
    "v_lshrrev_b32 v253, 16, v145\n\t"
    "v_mov_b64 v[200:201], s[70:71]\n\t"
    "v_mov_b64 v[202:203], s[72:73]\n\t"
    "v_and_b32 v145, s92, v145\n\t"
    "s_mov_b64 exec, 0xffffffff\n\t"
    "buffer_store_dword v165, v125, %12, s96 offen offset:384 sc1\n\t"
    "s_mov_b64 exec, -1\n\t"
    "v_pk_mul_f32 v[64:65], v[180:181], v[180:181]\n\t"
    "v_pk_mul_f32 v[66:67], v[182:183], v[182:183]\n\t"
    "v_pk_fma_f32 v[64:65], v[64:65], v[82:83], v[80:81]\n\t"
    "v_pk_fma_f32 v[66:67], v[66:67], v[82:83], v[80:81]\n\t"
    "v_pk_mul_f32 v[64:65], v[64:65], v[180:181]\n\t"
    "s_waitcnt lgkmcnt(4)\n\t"
    "v_smfmac_f32_16x16x64_bf16 v[200:203], v[120:123], v[84:91], v124\n\t"
    "ds_read_b64 v[84:85], v144 offset:64000\n\t"
    "ds_read_b64 v[86:87], v252 offset:64000\n\t"
    "ds_read_b64 v[88:89], v145 offset:64000\n\t"
    "ds_read_b64 v[90:91], v253 offset:64000\n\t"
    "v_lshrrev_b32 v254, 16, v146\n\t"
    "v_and_b32 v146, s92, v146\n\t"
    "v_lshrrev_b32 v255, 16, v147\n\t"
    "v_and_b32 v147, s92, v147\n\t"
    "v_pk_mul_f32 v[66:67], v[66:67], v[182:183]\n\t"
    "v_exp_f32 v68, v64\n\t"
    "v_exp_f32 v69, v65\n\t"
    "v_exp_f32 v70, v66\n\t"
    "v_exp_f32 v71, v67\n\t"
    "s_waitcnt lgkmcnt(4)\n\t"
    "v_smfmac_f32_16x16x64_bf16 v[200:203], v[120:123], v[92:99], v124\n\t"
    "ds_read_b64 v[92:93], v146 offset:64000\n\t"
    "ds_read_b64 v[94:95], v254 offset:64000\n\t"
    "ds_read_b64 v[96:97], v147 offset:64000\n\t"
    "ds_read_b64 v[98:99], v255 offset:64000\n\t"
    "buffer_load_dwordx4 v[220:223], v127, %12, s89 offen offset:1024 sc1\n\t"
    "v_lshrrev_b32 v252, 16, v148\n\t"
    "v_and_b32 v148, s92, v148\n\t"
    "v_lshrrev_b32 v253, 16, v149\n\t"
    "v_and_b32 v149, s92, v149\n\t"
    "v_pk_fma_f32 v[68:69], v[68:69], s[40:41], s[40:41]\n\t"
    "v_pk_fma_f32 v[70:71], v[70:71], s[42:43], s[42:43]\n\t"
    "v_rcp_f32 v68, v68\n\t"
    "v_rcp_f32 v69, v69\n\t"
    "v_rcp_f32 v70, v70\n\t"
    "s_waitcnt lgkmcnt(4)\n\t"
    "v_smfmac_f32_16x16x64_bf16 v[184:187], v[120:123], v[84:91], v124\n\t"
    "ds_read_b64 v[84:85], v148 offset:0\n\t"
    "ds_read_b64 v[86:87], v252 offset:0\n\t"
    "ds_read_b64 v[88:89], v149 offset:0\n\t"
    "ds_read_b64 v[90:91], v253 offset:0\n\t"
    "v_lshrrev_b32 v254, 16, v150\n\t"
    "v_and_b32 v150, s92, v150\n\t"
    "v_lshrrev_b32 v255, 16, v151\n\t"
    "v_and_b32 v151, s92, v151\n\t"
    "s_add_u32 s94, s94, 0x2000\n\t"
    "buffer_load_dwordx4 v[28:31], v127, %12, s94 offen sc1\n\t"
    "v_rcp_f32 v71, v71\n\t"
    "v_pk_mul_f32 v[68:69], v[68:69], v[180:181]\n\t"
    "v_pk_fma_f32 v[68:69], v[70:71], v[182:183], v[68:69]\n\t"
    "v_add_f32 v238, v68, v69\n\t"
    "s_waitcnt lgkmcnt(4)\n\t"
    "v_smfmac_f32_16x16x64_bf16 v[184:187], v[120:123], v[92:99], v124\n\t"
    "ds_read_b64 v[92:93], v150 offset:0\n\t"
    "ds_read_b64 v[94:95], v254 offset:0\n\t"
    "ds_read_b64 v[96:97], v151 offset:0\n\t"
    "ds_read_b64 v[98:99], v255 offset:0\n\t"
    "buffer_load_dwordx4 v[116:119], v127, %12, s87 offen offset:1024 sc1\n\t"
    "v_lshrrev_b32 v252, 16, v152\n\t"
    "v_and_b32 v152, s92, v152\n\t"
    "v_lshrrev_b32 v253, 16, v153\n\t"
    "v_mov_b64 v[204:205], s[70:71]\n\t"
    "v_mov_b64 v[206:207], s[72:73]\n\t"
    "v_and_b32 v153, s92, v153\n\t"
    "v_pk_mul_f32 v[72:73], v[184:185], v[184:185]\n\t"
    "v_pk_mul_f32 v[74:75], v[186:187], v[186:187]\n\t"
    "v_pk_fma_f32 v[72:73], v[72:73], v[82:83], v[80:81]\n\t"
    "v_pk_fma_f32 v[74:75], v[74:75], v[82:83], v[80:81]\n\t"
    "v_pk_mul_f32 v[72:73], v[72:73], v[184:185]\n\t"
    "s_waitcnt lgkmcnt(4)\n\t"
    "v_smfmac_f32_16x16x64_bf16 v[204:207], v[120:123], v[84:91], v124\n\t"
    "ds_read_b64 v[84:85], v152 offset:64000\n\t"
    "ds_read_b64 v[86:87], v252 offset:64000\n\t"
    "ds_read_b64 v[88:89], v153 offset:64000\n\t"
    "ds_read_b64 v[90:91], v253 offset:64000\n\t"
    "v_lshrrev_b32 v254, 16, v154\n\t"
    "v_and_b32 v154, s92, v154\n\t"
    "v_lshrrev_b32 v255, 16, v155\n\t"
    "v_and_b32 v155, s92, v155\n\t"
    "v_pk_mul_f32 v[74:75], v[74:75], v[186:187]\n\t"
    "v_exp_f32 v76, v72\n\t"
    "v_exp_f32 v77, v73\n\t"
    "v_exp_f32 v78, v74\n\t"
    "v_exp_f32 v79, v75\n\t"
    "s_waitcnt vmcnt(8) lgkmcnt(4)\n\t"
    "v_smfmac_f32_16x16x64_bf16 v[204:207], v[120:123], v[92:99], v124\n\t"
    "ds_read_b64 v[92:93], v154 offset:64000\n\t"
    "ds_read_b64 v[94:95], v254 offset:64000\n\t"
    "ds_read_b64 v[96:97], v155 offset:64000\n\t"
    "ds_read_b64 v[98:99], v255 offset:64000\n\t"
    "s_sub_u32 s90, s90, s95\n\t"
    "buffer_load_dwordx4 v[224:227], v127, %12, s90 offen offset:0 sc1\n\t"
    "v_lshrrev_b32 v252, 16, v244\n\t"
    "v_and_b32 v244, s92, v244\n\t"
    "v_lshrrev_b32 v253, 16, v245\n\t"
    "v_and_b32 v245, s92, v245\n\t"
    "v_pk_fma_f32 v[76:77], v[76:77], s[40:41], s[40:41]\n\t"
    "v_pk_fma_f32 v[78:79], v[78:79], s[42:43], s[42:43]\n\t"
    "v_rcp_f32 v76, v76\n\t"
    "v_rcp_f32 v77, v77\n\t"
    "v_rcp_f32 v78, v78\n\t"
    "s_waitcnt lgkmcnt(4)\n\t"
    "v_smfmac_f32_16x16x64_bf16 v[188:191], v[120:123], v[84:91], v124\n\t"
    "ds_read_b64 v[84:85], v244 offset:0\n\t"
    "ds_read_b64 v[86:87], v252 offset:0\n\t"
    "ds_read_b64 v[88:89], v245 offset:0\n\t"
    "ds_read_b64 v[90:91], v253 offset:0\n\t"
    "v_lshrrev_b32 v254, 16, v246\n\t"
    "v_and_b32 v246, s92, v246\n\t"
    "v_lshrrev_b32 v255, 16, v247\n\t"
    "v_and_b32 v247, s92, v247\n\t"
    "s_waitcnt vmcnt(6)\n\t"
    "v_smfmac_f32_16x16x64_bf16 v[188:191], v[156:159], v[16:23], v124\n\t"
    "v_rcp_f32 v79, v79\n\t"
    "v_pk_mul_f32 v[76:77], v[76:77], v[184:185]\n\t"
    "v_pk_fma_f32 v[76:77], v[78:79], v[186:187], v[76:77]\n\t"
    "v_add_f32 v239, v76, v77\n\t"
    "v_lshrrev_b32 v147, 4, v127\n\t"
    "v_add_u32 v148, 140288, v127\n\t"
    "s_lshl_b32 s65, %14, 12\n\t"
    "s_add_u32 s65, s65, 16777216\n\t"
    "buffer_load_dwordx4 v[0:3], v127, %12, s65 offen offset:0 sc1\n\t"
    "s_waitcnt lgkmcnt(4)\n\t"
    "v_smfmac_f32_16x16x64_bf16 v[188:191], v[120:123], v[92:99], v124\n\t"
    "ds_read_b64 v[92:93], v246 offset:0\n\t"
    "ds_read_b64 v[94:95], v254 offset:0\n\t"
    "ds_read_b64 v[96:97], v247 offset:0\n\t"
    "ds_read_b64 v[98:99], v255 offset:0\n\t"
    "s_add_u32 s88, s88, 0x8000\n\t"
    "buffer_load_dwordx4 v[228:231], v127, %12, s88 offen offset:0 sc1\n\t"
    "v_lshrrev_b32 v252, 16, v248\n\t"
    "v_and_b32 v248, s92, v248\n\t"
    "v_lshrrev_b32 v253, 16, v249\n\t"
    "v_mov_b64 v[208:209], s[70:71]\n\t"
    "v_mov_b64 v[210:211], s[72:73]\n\t"
    "v_and_b32 v249, s92, v249\n\t"
    "v_smfmac_f32_16x16x64_bf16 v[192:195], v[160:163], v[16:23], v124\n\t"
    "v_pk_mul_f32 v[64:65], v[188:189], v[188:189]\n\t"
    "v_pk_mul_f32 v[66:67], v[190:191], v[190:191]\n\t"
    "v_pk_fma_f32 v[64:65], v[64:65], v[82:83], v[80:81]\n\t"
    "v_pk_fma_f32 v[66:67], v[66:67], v[82:83], v[80:81]\n\t"
    "v_pk_mul_f32 v[64:65], v[64:65], v[188:189]\n\t"
    "s_lshl_b32 s65, %14, 12\n\t"
    "s_add_u32 s65, s65, 16777216\n\t"
    "buffer_load_dwordx4 v[4:7], v127, %12, s65 offen offset:1024 sc1\n\t"
    "s_waitcnt lgkmcnt(4)\n\t"
    "v_smfmac_f32_16x16x64_bf16 v[208:211], v[120:123], v[84:91], v124\n\t"
    "ds_read_b64 v[84:85], v248 offset:64000\n\t"
    "ds_read_b64 v[86:87], v252 offset:64000\n\t"
    "ds_read_b64 v[88:89], v249 offset:64000\n\t"
    "ds_read_b64 v[90:91], v253 offset:64000\n\t"
    "v_lshrrev_b32 v254, 16, v250\n\t"
    "v_and_b32 v250, s92, v250\n\t"
    "v_lshrrev_b32 v255, 16, v251\n\t"
    "v_and_b32 v251, s92, v251\n\t"
    "s_mov_b64 exec, 1\n\t"
    "buffer_load_dword v164, off, %12, s93 offset:256 sc1\n\t"
    "s_mov_b64 exec, -1\n\t"
    "v_pk_mul_f32 v[66:67], v[66:67], v[190:191]\n\t"
    "v_exp_f32 v68, v64\n\t"
    "v_exp_f32 v69, v65\n\t"
    "v_exp_f32 v70, v66\n\t"
    "v_exp_f32 v71, v67\n\t"
    "s_lshl_b32 s65, %14, 12\n\t"
    "s_add_u32 s65, s65, 16777216\n\t"
    "buffer_load_dwordx4 v[8:11], v127, %12, s65 offen offset:2048 sc1\n\t"
    "s_waitcnt lgkmcnt(4)\n\t"
    "v_smfmac_f32_16x16x64_bf16 v[208:211], v[120:123], v[92:99], v124\n\t"
    "ds_read_b64 v[92:93], v250 offset:64000\n\t"
    "ds_read_b64 v[94:95], v254 offset:64000\n\t"
    "ds_read_b64 v[96:97], v251 offset:64000\n\t"
    "ds_read_b64 v[98:99], v255 offset:64000\n\t"
    "buffer_load_dwordx4 v[232:235], v127, %12, s90 offen offset:1024 sc1\n\t"
    "v_lshrrev_b32 v252, 16, v212\n\t"
    "v_and_b32 v212, s92, v212\n\t"
    "v_lshrrev_b32 v253, 16, v213\n\t"
    "v_and_b32 v213, s92, v213\n\t"
    "v_pk_fma_f32 v[68:69], v[68:69], s[40:41], s[40:41]\n\t"
    "v_pk_fma_f32 v[70:71], v[70:71], s[42:43], s[42:43]\n\t"
    "v_rcp_f32 v68, v68\n\t"
    "v_rcp_f32 v69, v69\n\t"
    "v_rcp_f32 v70, v70\n\t"
    "s_lshl_b32 s65, %14, 12\n\t"
    "s_add_u32 s65, s65, 16777216\n\t"
    "buffer_load_dwordx4 v[12:15], v127, %12, s65 offen offset:3072 sc1\n\t"
    "s_waitcnt lgkmcnt(4)\n\t"
    "v_smfmac_f32_16x16x64_bf16 v[192:195], v[120:123], v[84:91], v124\n\t"
    "ds_read_b64 v[84:85], v212 offset:0\n\t"
    "ds_read_b64 v[86:87], v252 offset:0\n\t"
    "ds_read_b64 v[88:89], v213 offset:0\n\t"
    "ds_read_b64 v[90:91], v253 offset:0\n\t"
    "v_lshrrev_b32 v254, 16, v214\n\t"
    "v_and_b32 v214, s92, v214\n\t"
    "v_lshrrev_b32 v255, 16, v215\n\t"
    "v_and_b32 v215, s92, v215\n\t"
    "s_waitcnt vmcnt(9)\n\t"
    "v_smfmac_f32_16x16x64_bf16 v[196:199], v[156:159], v[24:31], v124\n\t"
    "v_rcp_f32 v71, v71\n\t"
    "v_pk_mul_f32 v[68:69], v[68:69], v[188:189]\n\t"
    "v_pk_fma_f32 v[68:69], v[70:71], v[190:191], v[68:69]\n\t"
    "v_add_f32 v240, v68, v69\n\t"
    "ds_read_b128 v[16:19], v148 offset:4096\n\t"
    "s_cmp_lt_u32 %16, 4\n\t"
    "s_cbranch_scc1 L_tp239_%=\n\t"
    "s_setprio 1\n\t"
    "L_tp239_%=:\n\t"
    "s_waitcnt lgkmcnt(5)\n\t"
    "v_smfmac_f32_16x16x64_bf16 v[192:195], v[120:123], v[92:99], v124\n\t"
    "ds_read_b64 v[92:93], v214 offset:0\n\t"
    "ds_read_b64 v[94:95], v254 offset:0\n\t"
    "ds_read_b64 v[96:97], v215 offset:0\n\t"
    "ds_read_b64 v[98:99], v255 offset:0\n\t"
    "buffer_load_dwordx4 v[168:171], v127, %12, s88 offen offset:1024 sc1\n\t"
    "v_lshrrev_b32 v252, 16, v216\n\t"
    "v_and_b32 v216, s92, v216\n\t"
    "v_lshrrev_b32 v253, 16, v217\n\t"
    "v_mov_b64 v[172:173], s[70:71]\n\t"
    "v_mov_b64 v[174:175], s[72:73]\n\t"
    "v_and_b32 v217, s92, v217\n\t"
    "v_smfmac_f32_16x16x64_bf16 v[200:203], v[160:163], v[24:31], v124\n\t"
    "v_pk_mul_f32 v[72:73], v[192:193], v[192:193]\n\t"
    "v_pk_mul_f32 v[74:75], v[194:195], v[194:195]\n\t"
    "v_pk_fma_f32 v[72:73], v[72:73], v[82:83], v[80:81]\n\t"
    "v_pk_fma_f32 v[74:75], v[74:75], v[82:83], v[80:81]\n\t"
    "v_pk_mul_f32 v[72:73], v[72:73], v[192:193]\n\t"
    "ds_read_b128 v[20:23], v148 offset:5120\n\t"
    "s_waitcnt lgkmcnt(6)\n\t"
    "v_smfmac_f32_16x16x64_bf16 v[172:175], v[120:123], v[84:91], v124\n\t"
    "ds_read_b64 v[84:85], v216 offset:64000\n\t"
    "ds_read_b64 v[86:87], v252 offset:64000\n\t"
    "ds_read_b64 v[88:89], v217 offset:64000\n\t"
    "ds_read_b64 v[90:91], v253 offset:64000\n\t"
    "v_lshrrev_b32 v254, 16, v218\n\t"
    "v_and_b32 v218, s92, v218\n\t"
    "v_lshrrev_b32 v255, 16, v219\n\t"
    "v_and_b32 v219, s92, v219\n\t"
    "v_pk_mul_f32 v[74:75], v[74:75], v[194:195]\n\t"
    "v_exp_f32 v76, v72\n\t"
    "v_exp_f32 v77, v73\n\t"
    "v_exp_f32 v78, v74\n\t"
    "v_exp_f32 v79, v75\n\t"
    "ds_read_b128 v[24:27], v148 offset:6144\n\t"
    "s_waitcnt lgkmcnt(6)\n\t"
    "v_smfmac_f32_16x16x64_bf16 v[172:175], v[120:123], v[92:99], v124\n\t"
    "ds_read_b64 v[92:93], v218 offset:64000\n\t"
    "ds_read_b64 v[94:95], v254 offset:64000\n\t"
    "ds_read_b64 v[96:97], v219 offset:64000\n\t"
    "ds_read_b64 v[98:99], v255 offset:64000\n\t"
    "s_sub_u32 s91, s91, s95\n\t"
    "buffer_load_dwordx4 v[112:115], v127, %12, s91 offen offset:0 sc1\n\t"
    "v_lshrrev_b32 v252, 16, v220\n\t"
    "v_and_b32 v220, s92, v220\n\t"
    "v_lshrrev_b32 v253, 16, v221\n\t"
    "v_and_b32 v221, s92, v221\n\t"
    "v_pk_fma_f32 v[76:77], v[76:77], s[40:41], s[40:41]\n\t"
    "v_pk_fma_f32 v[78:79], v[78:79], s[42:43], s[42:43]\n\t"
    "v_rcp_f32 v76, v76\n\t"
    "v_rcp_f32 v77, v77\n\t"
    "v_rcp_f32 v78, v78\n\t"
    "ds_read_b128 v[28:31], v148 offset:7168\n\t"
    "s_waitcnt lgkmcnt(6)\n\t"
    "v_smfmac_f32_16x16x64_bf16 v[196:199], v[120:123], v[84:91], v124\n\t"
    "ds_read_b64 v[84:85], v220 offset:0\n\t"
    "ds_read_b64 v[86:87], v252 offset:0\n\t"
    "ds_read_b64 v[88:89], v221 offset:0\n\t"
    "ds_read_b64 v[90:91], v253 offset:0\n\t"
    "v_lshrrev_b32 v254, 16, v222\n\t"
    "v_and_b32 v222, s92, v222\n\t"
    "v_lshrrev_b32 v255, 16, v223\n\t"
    "v_and_b32 v223, s92, v223\n\t"
    "s_waitcnt vmcnt(5)\n\t"
    "v_readfirstlane_b32 s65, v164\n\t"
    "s_nop 0\n\t"
    "s_cmp_eq_u32 s65, 0\n\t"
    "s_cbranch_scc1 L_xw2_%=\n\t"
    "L_xwb2_%=:\n\t"
    "s_add_u32 s94, s94, 0x2000\n\t"
    "buffer_load_dwordx4 v[36:39], v127, %12, s94 offen sc1\n\t"
    "v_rcp_f32 v79, v79\n\t"
    "v_pk_mul_f32 v[76:77], v[76:77], v[192:193]\n\t"
    "v_pk_fma_f32 v[76:77], v[78:79], v[194:195], v[76:77]\n\t"
    "v_add_f32 v241, v76, v77\n\t"
    "s_waitcnt lgkmcnt(5)\n\t"
    "v_smfmac_f32_16x16x64_bf16 v[196:199], v[120:123], v[92:99], v124\n\t"
    "ds_read_b64 v[92:93], v222 offset:0\n\t"
    "ds_read_b64 v[94:95], v254 offset:0\n\t"
    "ds_read_b64 v[96:97], v223 offset:0\n\t"
    "ds_read_b64 v[98:99], v255 offset:0\n\t"
    "s_add_u32 s89, s89, 0x8000\n\t"
    "buffer_load_dwordx4 v[100:103], v127, %12, s89 offen offset:0 sc1\n\t"
    "v_lshrrev_b32 v252, 16, v116\n\t"
    "v_and_b32 v116, s92, v116\n\t"
    "v_lshrrev_b32 v253, 16, v117\n\t"
    "v_mov_b64 v[176:177], s[70:71]\n\t"
    "v_mov_b64 v[178:179], s[72:73]\n\t"
    "v_and_b32 v117, s92, v117\n\t"
    "v_pk_mul_f32 v[64:65], v[196:197], v[196:197]\n\t"
    "v_pk_mul_f32 v[66:67], v[198:199], v[198:199]\n\t"
    "v_pk_fma_f32 v[64:65], v[64:65], v[82:83], v[80:81]\n\t"
    "v_pk_fma_f32 v[66:67], v[66:67], v[82:83], v[80:81]\n\t"
    "v_pk_mul_f32 v[64:65], v[64:65], v[196:197]\n\t"
    "s_waitcnt lgkmcnt(4)\n\t"
    "v_smfmac_f32_16x16x64_bf16 v[176:179], v[120:123], v[84:91], v124\n\t"
    "ds_read_b64 v[84:85], v116 offset:64000\n\t"
    "ds_read_b64 v[86:87], v252 offset:64000\n\t"
    "ds_read_b64 v[88:89], v117 offset:64000\n\t"
    "ds_read_b64 v[90:91], v253 offset:64000\n\t"
    "v_lshrrev_b32 v254, 16, v118\n\t"
    "v_and_b32 v118, s92, v118\n\t"
    "v_lshrrev_b32 v255, 16, v119\n\t"
    "v_and_b32 v119, s92, v119\n\t"
    "v_pk_mul_f32 v[66:67], v[66:67], v[198:199]\n\t"
    "v_exp_f32 v68, v64\n\t"
    "v_exp_f32 v69, v65\n\t"
    "v_exp_f32 v70, v66\n\t"
    "v_exp_f32 v71, v67\n\t"
    "s_waitcnt lgkmcnt(4)\n\t"
    "v_smfmac_f32_16x16x64_bf16 v[176:179], v[120:123], v[92:99], v124\n\t"
    "ds_read_b64 v[92:93], v118 offset:64000\n\t"
    "ds_read_b64 v[94:95], v254 offset:64000\n\t"
    "ds_read_b64 v[96:97], v119 offset:64000\n\t"
    "ds_read_b64 v[98:99], v255 offset:64000\n\t"
    "buffer_load_dwordx4 v[104:107], v127, %12, s91 offen offset:1024 sc1\n\t"
    "v_lshrrev_b32 v252, 16, v224\n\t"
    "v_and_b32 v224, s92, v224\n\t"
    "v_lshrrev_b32 v253, 16, v225\n\t"
    "v_and_b32 v225, s92, v225\n\t"
    "v_pk_fma_f32 v[68:69], v[68:69], s[40:41], s[40:41]\n\t"
    "v_pk_fma_f32 v[70:71], v[70:71], s[42:43], s[42:43]\n\t"
    "v_rcp_f32 v68, v68\n\t"
    "v_rcp_f32 v69, v69\n\t"
    "v_rcp_f32 v70, v70\n\t"
    "s_waitcnt lgkmcnt(4)\n\t"
    "v_smfmac_f32_16x16x64_bf16 v[200:203], v[120:123], v[84:91], v124\n\t"
    "ds_read_b64 v[84:85], v224 offset:0\n\t"
    "ds_read_b64 v[86:87], v252 offset:0\n\t"
    "ds_read_b64 v[88:89], v225 offset:0\n\t"
    "ds_read_b64 v[90:91], v253 offset:0\n\t"
    "v_lshrrev_b32 v254, 16, v226\n\t"
    "v_and_b32 v226, s92, v226\n\t"
    "v_lshrrev_b32 v255, 16, v227\n\t"
    "v_and_b32 v227, s92, v227\n\t"
    "s_add_u32 s94, s94, 0x2000\n\t"
    "buffer_load_dwordx4 v[44:47], v127, %12, s94 offen sc1\n\t"
    "v_rcp_f32 v71, v71\n\t"
    "v_pk_mul_f32 v[68:69], v[68:69], v[196:197]\n\t"
    "v_pk_fma_f32 v[68:69], v[70:71], v[198:199], v[68:69]\n\t"
    "v_add_f32 v242, v68, v69\n\t"
    "s_lshl_b32 s46, %14, 2\n\t"
    "s_add_u32 s46, s46, 49294336\n\t"
    "s_mov_b64 exec, 1\n\t"
    "buffer_load_dword v144, off, %12, s46 sc1\n\t"
    "s_mov_b64 exec, -1\n\t"
    "s_waitcnt lgkmcnt(4)\n\t"
    "v_smfmac_f32_16x16x64_bf16 v[200:203], v[120:123], v[92:99], v124\n\t"
    "ds_read_b64 v[92:93], v226 offset:0\n\t"
    "ds_read_b64 v[94:95], v254 offset:0\n\t"
    "ds_read_b64 v[96:97], v227 offset:0\n\t"
    "ds_read_b64 v[98:99], v255 offset:0\n\t"
    "buffer_load_dwordx4 v[108:111], v127, %12, s89 offen offset:1024 sc1\n\t"
    "v_lshrrev_b32 v252, 16, v228\n\t"
    "v_and_b32 v228, s92, v228\n\t"
    "v_lshrrev_b32 v253, 16, v229\n\t"
    "v_mov_b64 v[180:181], s[70:71]\n\t"
    "v_mov_b64 v[182:183], s[72:73]\n\t"
    "v_and_b32 v229, s92, v229\n\t"
    "v_pk_mul_f32 v[72:73], v[200:201], v[200:201]\n\t"
    "v_pk_mul_f32 v[74:75], v[202:203], v[202:203]\n\t"
    "v_pk_fma_f32 v[72:73], v[72:73], v[82:83], v[80:81]\n\t"
    "v_pk_fma_f32 v[74:75], v[74:75], v[82:83], v[80:81]\n\t"
    "v_pk_mul_f32 v[72:73], v[72:73], v[200:201]\n\t"
    "s_waitcnt lgkmcnt(4)\n\t"
    "v_smfmac_f32_16x16x64_bf16 v[180:183], v[120:123], v[84:91], v124\n\t"
    "ds_read_b64 v[84:85], v228 offset:64000\n\t"
    "ds_read_b64 v[86:87], v252 offset:64000\n\t"
    "ds_read_b64 v[88:89], v229 offset:64000\n\t"
    "ds_read_b64 v[90:91], v253 offset:64000\n\t"
    "v_lshrrev_b32 v254, 16, v230\n\t"
    "v_and_b32 v230, s92, v230\n\t"
    "v_lshrrev_b32 v255, 16, v231\n\t"
    "v_and_b32 v231, s92, v231\n\t"
    "v_pk_mul_f32 v[74:75], v[74:75], v[202:203]\n\t"
    "v_exp_f32 v76, v72\n\t"
    "v_exp_f32 v77, v73\n\t"
    "v_exp_f32 v78, v74\n\t"
    "v_exp_f32 v79, v75\n\t"
    "s_waitcnt vmcnt(9) lgkmcnt(4)\n\t"
    "v_smfmac_f32_16x16x64_bf16 v[180:183], v[120:123], v[92:99], v124\n\t"
    "ds_read_b64 v[92:93], v230 offset:64000\n\t"
    "ds_read_b64 v[94:95], v254 offset:64000\n\t"
    "ds_read_b64 v[96:97], v231 offset:64000\n\t"
    "ds_read_b64 v[98:99], v255 offset:64000\n\t"
    "s_add_u32 s90, s90, 0x8000\n\t"
    "buffer_load_dwordx4 v[248:251], v127, %12, s90 offen offset:0 sc1\n\t"
    "v_lshrrev_b32 v252, 16, v232\n\t"
    "v_and_b32 v232, s92, v232\n\t"
    "v_lshrrev_b32 v253, 16, v233\n\t"
    "v_and_b32 v233, s92, v233\n\t"
    "v_pk_fma_f32 v[76:77], v[76:77], s[40:41], s[40:41]\n\t"
    "v_pk_fma_f32 v[78:79], v[78:79], s[42:43], s[42:43]\n\t"
    "v_rcp_f32 v76, v76\n\t"
    "v_rcp_f32 v77, v77\n\t"
    "v_rcp_f32 v78, v78\n\t"
    "s_waitcnt lgkmcnt(4)\n\t"
    "v_smfmac_f32_16x16x64_bf16 v[204:207], v[120:123], v[84:91], v124\n\t"
    "ds_read_b64 v[84:85], v232 offset:0\n\t"
    "ds_read_b64 v[86:87], v252 offset:0\n\t"
    "ds_read_b64 v[88:89], v233 offset:0\n\t"
    "ds_read_b64 v[90:91], v253 offset:0\n\t"
    "v_lshrrev_b32 v254, 16, v234\n\t"
    "v_and_b32 v234, s92, v234\n\t"
    "v_lshrrev_b32 v255, 16, v235\n\t"
    "v_and_b32 v235, s92, v235\n\t"
    "s_mov_b64 exec, 1\n\t"
    "buffer_load_dword v164, off, %12, s93 offset:384 sc1\n\t"
    "s_mov_b64 exec, -1\n\t"
    "s_waitcnt vmcnt(7)\n\t"
    "v_smfmac_f32_16x16x64_bf16 v[204:207], v[156:159], v[32:39], v124\n\t"
    "v_rcp_f32 v79, v79\n\t"
    "v_pk_mul_f32 v[76:77], v[76:77], v[200:201]\n\t"
    "v_pk_fma_f32 v[76:77], v[78:79], v[202:203], v[76:77]\n\t"
    "v_add_f32 v243, v76, v77\n\t"
    "s_waitcnt lgkmcnt(4)\n\t"
    "v_smfmac_f32_16x16x64_bf16 v[204:207], v[120:123], v[92:99], v124\n\t"
    "ds_read_b64 v[92:93], v234 offset:0\n\t"
    "ds_read_b64 v[94:95], v254 offset:0\n\t"
    "ds_read_b64 v[96:97], v235 offset:0\n\t"
    "ds_read_b64 v[98:99], v255 offset:0\n\t"
    "buffer_load_dwordx4 v[212:215], v127, %12, s90 offen offset:1024 sc1\n\t"
    "v_lshrrev_b32 v252, 16, v168\n\t"
    "v_and_b32 v168, s92, v168\n\t"
    "v_lshrrev_b32 v253, 16, v169\n\t"
    "v_mov_b64 v[184:185], s[70:71]\n\t"
    "v_mov_b64 v[186:187], s[72:73]\n\t"
    "v_and_b32 v169, s92, v169\n\t"
    "v_smfmac_f32_16x16x64_bf16 v[208:211], v[160:163], v[32:39], v124\n\t"
    "v_pk_mul_f32 v[64:65], v[204:205], v[204:205]\n\t"
    "v_pk_mul_f32 v[66:67], v[206:207], v[206:207]\n\t"
    "v_pk_fma_f32 v[64:65], v[64:65], v[82:83], v[80:81]\n\t"
    "v_pk_fma_f32 v[66:67], v[66:67], v[82:83], v[80:81]\n\t"
    "v_pk_mul_f32 v[64:65], v[64:65], v[204:205]\n\t"
    "ds_read_b128 v[32:35], v148 offset:8192\n\t"
    "s_waitcnt lgkmcnt(5)\n\t"
    "v_smfmac_f32_16x16x64_bf16 v[184:187], v[120:123], v[84:91], v124\n\t"
    "ds_read_b64 v[84:85], v168 offset:64000\n\t"
    "ds_read_b64 v[86:87], v252 offset:64000\n\t"
    "ds_read_b64 v[88:89], v169 offset:64000\n\t"
    "ds_read_b64 v[90:91], v253 offset:64000\n\t"
    "v_lshrrev_b32 v254, 16, v170\n\t"
    "v_and_b32 v170, s92, v170\n\t"
    "v_lshrrev_b32 v255, 16, v171\n\t"
    "v_and_b32 v171, s92, v171\n\t"
    "v_pk_mul_f32 v[66:67], v[66:67], v[206:207]\n\t"
    "v_exp_f32 v68, v64\n\t"
    "v_exp_f32 v69, v65\n\t"
    "v_exp_f32 v70, v66\n\t"
    "v_exp_f32 v71, v67\n\t"
    "ds_read_b128 v[36:39], v148 offset:9216\n\t"
    "s_waitcnt lgkmcnt(6)\n\t"
    "v_smfmac_f32_16x16x64_bf16 v[184:187], v[120:123], v[92:99], v124\n\t"
    "ds_read_b64 v[92:93], v170 offset:64000\n\t"
    "ds_read_b64 v[94:95], v254 offset:64000\n\t"
    "ds_read_b64 v[96:97], v171 offset:64000\n\t"
    "ds_read_b64 v[98:99], v255 offset:64000\n\t"
    "s_add_u32 s91, s91, 0x8000\n\t"
    "buffer_load_dwordx4 v[216:219], v127, %12, s91 offen offset:0 sc1\n\t"
    "v_lshrrev_b32 v252, 16, v112\n\t"
    "v_and_b32 v112, s92, v112\n\t"
    "v_lshrrev_b32 v253, 16, v113\n\t"
    "v_and_b32 v113, s92, v113\n\t"
    "v_pk_fma_f32 v[68:69], v[68:69], s[40:41], s[40:41]\n\t"
    "v_pk_fma_f32 v[70:71], v[70:71], s[42:43], s[42:43]\n\t"
    "v_rcp_f32 v68, v68\n\t"
    "v_rcp_f32 v69, v69\n\t"
    "v_rcp_f32 v70, v70\n\t"
    "s_waitcnt lgkmcnt(5)\n\t"
    "v_smfmac_f32_16x16x64_bf16 v[208:211], v[120:123], v[84:91], v124\n\t"
    "ds_read_b64 v[84:85], v112 offset:0\n\t"
    "ds_read_b64 v[86:87], v252 offset:0\n\t"
    "ds_read_b64 v[88:89], v113 offset:0\n\t"
    "ds_read_b64 v[90:91], v253 offset:0\n\t"
    "v_lshrrev_b32 v254, 16, v114\n\t"
    "v_and_b32 v114, s92, v114\n\t"
    "v_lshrrev_b32 v255, 16, v115\n\t"
    "v_and_b32 v115, s92, v115\n\t"
    "s_waitcnt vmcnt(6)\n\t"
    "v_smfmac_f32_16x16x64_bf16 v[172:175], v[156:159], v[40:47], v124\n\t"
    "v_rcp_f32 v71, v71\n\t"
    "v_pk_mul_f32 v[68:69], v[68:69], v[204:205]\n\t"
    "v_pk_fma_f32 v[68:69], v[70:71], v[206:207], v[68:69]\n\t"
    "v_add_f32 v244, v68, v69\n\t"
    "s_waitcnt vmcnt(5)\n\t"
    "v_readfirstlane_b32 s38, v144\n\t"
    "s_nop 0\n\t"
    "s_cmp_eq_u32 s38, 0\n\t"
    "s_cbranch_scc1 L_rw_%=\n\t"
    "L_rok_%=:\n\t"
    "s_and_b32 s46, %16, 3\n\t"
    "s_mul_i32 s49, s46, 1040\n\t"
    "s_add_u32 s49, s49, 2064\n\t"
    "s_and_b32 s37, s38, 0xffff\n\t"
    "s_lshr_b32 s44, s38, 16\n\t"
    "s_sub_u32 s44, s44, s37\n\t"
    "s_lshl_b32 s37, s37, 2\n\t"
    "s_lshl_b32 s38, s44, 2\n\t"
    "s_mul_i32 s51, s46, s44\n\t"
    "s_add_u32 s48, s51, s44\n\t"
    "s_lshl_b32 s48, s48, 16\n\t"
    "s_add_u32 s48, s48, s51\n\t"
    "s_lshr_b32 s47, %16, 2\n\t"
    "s_add_u32 s48, s48, s47\n\t"
    "s_add_u32 s48, s48, 0x200\n\t"
    "s_mul_i32 s46, s37, 400\n\t"
    "s_add_u32 s96, %18, s46\n\t"
    "s_addc_u32 s97, %19, 0\n\t"
    "s_sub_u32 s98, 6553600, s46\n\t"
    "s_mov_b32 s99, 0x20000\n\t"
    "v_bfe_u32 v154, v147, 2, 2\n\t"
    "v_and_b32 v155, 3, v147\n\t"
    "v_mul_u32_u24 v154, 25, v154\n\t"
    "v_mad_u32_u24 v154, v155, 6, v154\n\t"
    "v_lshlrev_b32 v153, 2, v154\n\t"
    "s_and_b32 s53, s48, 0xff\n\t"
    "s_bfe_u32 s55, s48, 0x80008\n\t"
    "s_add_u32 s54, s53, s55\n\t"
    "s_add_u32 s55, s54, s55\n\t"
    "s_lshr_b32 s64, s48, 16\n\t"
    "s_mul_i32 s66, s53, 400\n\t"
    "buffer_load_dwordx3 v[128:130], v153, s[96:99], s66 offen\n\t"
    "buffer_load_dwordx4 v[132:135], v153, s[96:99], s66 offen offset:12\n\t"
    "s_mul_i32 s66, s54, 400\n\t"
    "buffer_load_dwordx3 v[136:138], v153, s[96:99], s66 offen\n\t"
    "buffer_load_dwordx4 v[140:143], v153, s[96:99], s66 offen offset:12\n\t"
    "v_cmp_eq_u32 s[62:63], 0, v155\n\t"
    "v_and_b32 v151, 48, v147\n\t"
    "v_or_b32 v155, 0x100000, v151\n\t"
    "v_cndmask_b32 v152, v155, v151, s[62:63]\n\t"
    "s_waitcnt lgkmcnt(4)\n\t"
    "v_smfmac_f32_16x16x64_bf16 v[208:211], v[120:123], v[92:99], v124\n\t"
    "ds_read_b64 v[92:93], v114 offset:0\n\t"
    "ds_read_b64 v[94:95], v254 offset:0\n\t"
    "ds_read_b64 v[96:97], v115 offset:0\n\t"
    "ds_read_b64 v[98:99], v255 offset:0\n\t"
    "buffer_load_dwordx4 v[220:223], v127, %12, s91 offen offset:1024 sc1\n\t"
    "v_lshrrev_b32 v252, 16, v100\n\t"
    "v_and_b32 v100, s92, v100\n\t"
    "v_lshrrev_b32 v253, 16, v101\n\t"
    "v_mov_b64 v[188:189], s[70:71]\n\t"
    "v_mov_b64 v[190:191], s[72:73]\n\t"
    "v_and_b32 v101, s92, v101\n\t"
    "v_smfmac_f32_16x16x64_bf16 v[176:179], v[160:163], v[40:47], v124\n\t"
    "v_pk_mul_f32 v[72:73], v[208:209], v[208:209]\n\t"
    "v_pk_mul_f32 v[74:75], v[210:211], v[210:211]\n\t"
    "v_pk_fma_f32 v[72:73], v[72:73], v[82:83], v[80:81]\n\t"
    "v_pk_fma_f32 v[74:75], v[74:75], v[82:83], v[80:81]\n\t"
    "v_pk_mul_f32 v[72:73], v[72:73], v[208:209]\n\t"
    "ds_read_b128 v[40:43], v148 offset:10240\n\t"
    "s_setprio 0\n\t"
    "s_waitcnt lgkmcnt(5)\n\t"
    "v_smfmac_f32_16x16x64_bf16 v[188:191], v[120:123], v[84:91], v124\n\t"
    "ds_read_b64 v[84:85], v100 offset:64000\n\t"
    "ds_read_b64 v[86:87], v252 offset:64000\n\t"
    "ds_read_b64 v[88:89], v101 offset:64000\n\t"
    "ds_read_b64 v[90:91], v253 offset:64000\n\t"
    "v_lshrrev_b32 v254, 16, v102\n\t"
    "v_and_b32 v102, s92, v102\n\t"
    "v_lshrrev_b32 v255, 16, v103\n\t"
    "v_and_b32 v103, s92, v103\n\t"
    "s_waitcnt vmcnt(7)\n\t"
    "v_readfirstlane_b32 s65, v164\n\t"
    "s_nop 0\n\t"
    "s_cmp_eq_u32 s65, 0\n\t"
    "s_cbranch_scc1 L_xw3_%=\n\t"
    "L_xwb3_%=:\n\t"
    "s_add_u32 s94, s94, 0x2000\n\t"
    "buffer_load_dwordx4 v[52:55], v127, %12, s94 offen sc1\n\t"
    "v_pk_mul_f32 v[74:75], v[74:75], v[210:211]\n\t"
    "v_exp_f32 v76, v72\n\t"
    "v_exp_f32 v77, v73\n\t"
    "v_exp_f32 v78, v74\n\t"
    "v_exp_f32 v79, v75\n\t"
    "ds_read_b128 v[44:47], v148 offset:11264\n\t"
    "s_waitcnt lgkmcnt(6)\n\t"
    "v_smfmac_f32_16x16x64_bf16 v[188:191], v[120:123], v[92:99], v124\n\t"
    "ds_read_b64 v[92:93], v102 offset:64000\n\t"
    "ds_read_b64 v[94:95], v254 offset:64000\n\t"
    "ds_read_b64 v[96:97], v103 offset:64000\n\t"
    "ds_read_b64 v[98:99], v255 offset:64000\n\t"
    "v_lshrrev_b32 v252, 16, v104\n\t"
    "v_and_b32 v104, s92, v104\n\t"
    "v_lshrrev_b32 v253, 16, v105\n\t"
    "v_and_b32 v105, s92, v105\n\t"
    "v_pk_fma_f32 v[76:77], v[76:77], s[40:41], s[40:41]\n\t"
    "v_pk_fma_f32 v[78:79], v[78:79], s[42:43], s[42:43]\n\t"
    "v_rcp_f32 v76, v76\n\t"
    "v_rcp_f32 v77, v77\n\t"
    "v_rcp_f32 v78, v78\n\t"
    "s_waitcnt lgkmcnt(5)\n\t"
    "v_smfmac_f32_16x16x64_bf16 v[172:175], v[120:123], v[84:91], v124\n\t"
    "ds_read_b64 v[84:85], v104 offset:0\n\t"
    "ds_read_b64 v[86:87], v252 offset:0\n\t"
    "ds_read_b64 v[88:89], v105 offset:0\n\t"
    "ds_read_b64 v[90:91], v253 offset:0\n\t"
    "v_lshrrev_b32 v254, 16, v106\n\t"
    "v_and_b32 v106, s92, v106\n\t"
    "v_lshrrev_b32 v255, 16, v107\n\t"
    "v_and_b32 v107, s92, v107\n\t"
    "v_rcp_f32 v79, v79\n\t"
    "v_pk_mul_f32 v[76:77], v[76:77], v[208:209]\n\t"
    "v_pk_fma_f32 v[76:77], v[78:79], v[210:211], v[76:77]\n\t"
    "v_add_f32 v245, v76, v77\n\t"
    "s_waitcnt lgkmcnt(4)\n\t"
    "v_smfmac_f32_16x16x64_bf16 v[172:175], v[120:123], v[92:99], v124\n\t"
    "ds_read_b64 v[92:93], v106 offset:0\n\t"
    "ds_read_b64 v[94:95], v254 offset:0\n\t"
    "ds_read_b64 v[96:97], v107 offset:0\n\t"
    "ds_read_b64 v[98:99], v255 offset:0\n\t"
    "v_lshrrev_b32 v252, 16, v108\n\t"
    "v_and_b32 v108, s92, v108\n\t"
    "v_lshrrev_b32 v253, 16, v109\n\t"
    "v_mov_b64 v[192:193], s[70:71]\n\t"
    "v_mov_b64 v[194:195], s[72:73]\n\t"
    "v_and_b32 v109, s92, v109\n\t"
    "v_pk_mul_f32 v[64:65], v[172:173], v[172:173]\n\t"
    "v_pk_mul_f32 v[66:67], v[174:175], v[174:175]\n\t"
    "v_pk_fma_f32 v[64:65], v[64:65], v[82:83], v[80:81]\n\t"
    "v_pk_fma_f32 v[66:67], v[66:67], v[82:83], v[80:81]\n\t"
    "v_pk_mul_f32 v[64:65], v[64:65], v[172:173]\n\t"
    "s_waitcnt lgkmcnt(4)\n\t"
    "v_smfmac_f32_16x16x64_bf16 v[192:195], v[120:123], v[84:91], v124\n\t"
    "ds_read_b64 v[84:85], v108 offset:64000\n\t"
    "ds_read_b64 v[86:87], v252 offset:64000\n\t"
    "ds_read_b64 v[88:89], v109 offset:64000\n\t"
    "ds_read_b64 v[90:91], v253 offset:64000\n\t"
    "v_lshrrev_b32 v254, 16, v110\n\t"
    "v_and_b32 v110, s92, v110\n\t"
    "v_lshrrev_b32 v255, 16, v111\n\t"
    "v_and_b32 v111, s92, v111\n\t"
    "s_add_u32 s94, s94, 0x2000\n\t"
    "buffer_load_dwordx4 v[60:63], v127, %12, s94 offen sc1\n\t"
    "v_pk_mul_f32 v[66:67], v[66:67], v[174:175]\n\t"
    "v_exp_f32 v68, v64\n\t"
    "v_exp_f32 v69, v65\n\t"
    "v_exp_f32 v70, v66\n\t"
    "v_exp_f32 v71, v67\n\t"
    "v_lshrrev_b32 v166, 7, v127\n\t"
    "v_and_b32 v167, 0x70, v127\n\t"
    "v_lshl_add_u32 v126, v166, 8, v167\n\t"
    "v_add_u32 v126, 128, v126\n\t"
    "s_bfe_u32 s46, %14, 0x30003\n\t"
    "s_min_u32 s46, s46, 6\n\t"
    "s_lshl_b32 s69, s46, 3\n\t"
    "s_add_u32 s69, s69, %16\n\t"
    "s_waitcnt lgkmcnt(4)\n\t"
    "v_smfmac_f32_16x16x64_bf16 v[192:195], v[120:123], v[92:99], v124\n\t"
    "ds_read_b64 v[92:93], v110 offset:64000\n\t"
    "ds_read_b64 v[94:95], v254 offset:64000\n\t"
    "ds_read_b64 v[96:97], v111 offset:64000\n\t"
    "ds_read_b64 v[98:99], v255 offset:64000\n\t"
    "v_lshrrev_b32 v252, 16, v248\n\t"
    "v_and_b32 v248, s92, v248\n\t"
    "v_lshrrev_b32 v253, 16, v249\n\t"
    "v_and_b32 v249, s92, v249\n\t"
    "v_pk_fma_f32 v[68:69], v[68:69], s[40:41], s[40:41]\n\t"
    "v_pk_fma_f32 v[70:71], v[70:71], s[42:43], s[42:43]\n\t"
    "v_rcp_f32 v68, v68\n\t"
    "v_rcp_f32 v69, v69\n\t"
    "v_rcp_f32 v70, v70\n\t"
    "s_barrier\n\t"
    "s_waitcnt lgkmcnt(4)\n\t"
    "v_smfmac_f32_16x16x64_bf16 v[176:179], v[120:123], v[84:91], v124\n\t"
    "ds_read_b64 v[84:85], v248 offset:64000\n\t"
    "ds_read_b64 v[86:87], v252 offset:64000\n\t"
    "ds_read_b64 v[88:89], v249 offset:64000\n\t"
    "ds_read_b64 v[90:91], v253 offset:64000\n\t"
    "v_lshrrev_b32 v254, 16, v250\n\t"
    "v_and_b32 v250, s92, v250\n\t"
    "v_lshrrev_b32 v255, 16, v251\n\t"
    "v_and_b32 v251, s92, v251\n\t"
    "v_rcp_f32 v71, v71\n\t"
    "v_pk_mul_f32 v[68:69], v[68:69], v[172:173]\n\t"
    "v_pk_fma_f32 v[68:69], v[70:71], v[174:175], v[68:69]\n\t"
    "v_add_f32 v246, v68, v69\n\t"
    "s_mul_i32 s44, s69, 1040\n\t"
    "s_add_u32 m0, s44, 0\n\t"
    "s_lshl_b32 s44, s69, 11\n\t"
    "buffer_load_dwordx4 v126, %15, s44 offen lds\n\t"
    "s_add_u32 s69, s69, 8\n\t"
    "s_sub_u32 s44, s69, 61\n\t"
    "s_cmp_ge_u32 s69, 61\n\t"
    "s_cselect_b32 s69, s44, s69\n\t"
    "s_waitcnt vmcnt(9) lgkmcnt(4)\n\t"
    "v_smfmac_f32_16x16x64_bf16 v[176:179], v[120:123], v[92:99], v124\n\t"
    "ds_read_b64 v[92:93], v250 offset:64000\n\t"
    "ds_read_b64 v[94:95], v254 offset:64000\n\t"
    "ds_read_b64 v[96:97], v251 offset:64000\n\t"
    "ds_read_b64 v[98:99], v255 offset:64000\n\t"
    "v_lshrrev_b32 v252, 16, v212\n\t"
    "v_and_b32 v212, s92, v212\n\t"
    "v_lshrrev_b32 v253, 16, v213\n\t"
    "v_and_b32 v213, s92, v213\n\t"
    "s_mul_i32 s44, s69, 1040\n\t"
    "s_add_u32 m0, s44, 0\n\t"
    "s_lshl_b32 s44, s69, 11\n\t"
    "buffer_load_dwordx4 v126, %15, s44 offen lds\n\t"
    "s_add_u32 s69, s69, 8\n\t"
    "s_sub_u32 s44, s69, 61\n\t"
    "s_cmp_ge_u32 s69, 61\n\t"
    "s_cselect_b32 s69, s44, s69\n\t"
    "v_lshlrev_b32 v146, 2, v147\n\t"
    "s_lshl_b32 s50, s37, 8\n\t"
    "s_add_u32 s50, s50, 27262976\n\t"
    "s_lshl_b32 s46, s53, 8\n\t"
    "s_add_u32 s46, s46, s50\n\t"
    "buffer_load_dword v144, v146, %12, s46 offen sc1\n\t"
    "buffer_load_dword v145, v146, %12, s46 offen offset:512 sc1\n\t"
    "v_mov_b32 v149, s49\n\t"
    "s_add_u32 s46, s51, 6\n\t"
    "v_mov_b32 v155, s46\n\t"
    "ds_write_b32 v149, v155\n\t"
    "s_mov_b32 s61, 0\n\t"
    "v_mov_b32 v149, 1024\n\t"
    "v_mov_b32 v150, 0\n\t"
    "ds_write_b32 v149, v150\n\t"
    "s_mov_b32 s45, 1\n\t"
    "s_waitcnt lgkmcnt(6)\n\t"
    "v_smfmac_f32_16x16x64_bf16 v[180:183], v[120:123], v[84:91], v124\n\t"
    "ds_read_b64 v[84:85], v212 offset:64000\n\t"
    "ds_read_b64 v[86:87], v252 offset:64000\n\t"
    "ds_read_b64 v[88:89], v213 offset:64000\n\t"
    "ds_read_b64 v[90:91], v253 offset:64000\n\t"
    "v_lshrrev_b32 v254, 16, v214\n\t"
    "v_and_b32 v214, s92, v214\n\t"
    "v_lshrrev_b32 v255, 16, v215\n\t"
    "v_and_b32 v215, s92, v215\n\t"
    "s_waitcnt vmcnt(5)\n\t"
    "v_smfmac_f32_16x16x64_bf16 v[180:183], v[156:159], v[48:55], v124\n\t"
    "v_pk_mul_f32 v[72:73], v[176:177], v[176:177]\n\t"
    "v_pk_mul_f32 v[74:75], v[178:179], v[178:179]\n\t"
    "v_pk_fma_f32 v[72:73], v[72:73], v[82:83], v[80:81]\n\t"
    "v_pk_fma_f32 v[74:75], v[74:75], v[82:83], v[80:81]\n\t"
    "v_pk_mul_f32 v[72:73], v[72:73], v[176:177]\n\t"
    "v_pk_mul_f32 v[74:75], v[74:75], v[178:179]\n\t"
    "v_exp_f32 v76, v72\n\t"
    "s_mul_i32 s44, s69, 1040\n\t"
    "s_add_u32 m0, s44, 0\n\t"
    "s_lshl_b32 s44, s69, 11\n\t"
    "buffer_load_dwordx4 v126, %15, s44 offen lds\n\t"
    "s_add_u32 s69, s69, 8\n\t"
    "s_sub_u32 s44, s69, 61\n\t"
    "s_cmp_ge_u32 s69, 61\n\t"
    "s_cselect_b32 s69, s44, s69\n\t"
    "s_waitcnt lgkmcnt(6)\n\t"
    "v_smfmac_f32_16x16x64_bf16 v[180:183], v[120:123], v[92:99], v124\n\t"
    "ds_read_b64 v[92:93], v214 offset:64000\n\t"
    "ds_read_b64 v[94:95], v254 offset:64000\n\t"
    "ds_read_b64 v[96:97], v215 offset:64000\n\t"
    "ds_read_b64 v[98:99], v255 offset:64000\n\t"
    "v_lshrrev_b32 v252, 16, v216\n\t"
    "v_and_b32 v216, s92, v216\n\t"
    "v_lshrrev_b32 v253, 16, v217\n\t"
    "v_and_b32 v217, s92, v217\n\t"
    "v_smfmac_f32_16x16x64_bf16 v[184:187], v[160:163], v[48:55], v124\n\t"
    "v_exp_f32 v77, v73\n\t"
    "v_exp_f32 v78, v74\n\t"
    "v_exp_f32 v79, v75\n\t"
    "v_pk_fma_f32 v[76:77], v[76:77], s[40:41], s[40:41]\n\t"
    "v_pk_fma_f32 v[78:79], v[78:79], s[42:43], s[42:43]\n\t"
    "v_rcp_f32 v76, v76\n\t"
    "v_rcp_f32 v77, v77\n\t"
    "s_mul_i32 s44, s69, 1040\n\t"
    "s_add_u32 m0, s44, 0\n\t"
    "s_lshl_b32 s44, s69, 11\n\t"
    "buffer_load_dwordx4 v126, %15, s44 offen lds\n\t"
    "s_add_u32 s69, s69, 8\n\t"
    "s_sub_u32 s44, s69, 61\n\t"
    "s_cmp_ge_u32 s69, 61\n\t"
    "s_cselect_b32 s69, s44, s69\n\t"
    "ds_read_b128 v[48:51], v148 offset:12288\n\t"
    "s_waitcnt lgkmcnt(5)\n\t"
    "v_smfmac_f32_16x16x64_bf16 v[184:187], v[120:123], v[84:91], v124\n\t"
    "ds_read_b64 v[84:85], v216 offset:64000\n\t"
    "ds_read_b64 v[86:87], v252 offset:64000\n\t"
    "ds_read_b64 v[88:89], v217 offset:64000\n\t"
    "ds_read_b64 v[90:91], v253 offset:64000\n\t"
    "v_lshrrev_b32 v254, 16, v218\n\t"
    "v_and_b32 v218, s92, v218\n\t"
    "v_lshrrev_b32 v255, 16, v219\n\t"
    "v_and_b32 v219, s92, v219\n\t"
    "v_rcp_f32 v78, v78\n\t"
    "v_rcp_f32 v79, v79\n\t"
    "v_pk_mul_f32 v[76:77], v[76:77], v[176:177]\n\t"
    "v_pk_fma_f32 v[76:77], v[78:79], v[178:179], v[76:77]\n\t"
    "v_add_f32 v247, v76, v77\n\t"
    "v_pk_mul_f32 v[64:65], v[180:181], v[180:181]\n\t"
    "v_pk_mul_f32 v[66:67], v[182:183], v[182:183]\n\t"
    "s_mul_i32 s44, s69, 1040\n\t"
    "s_add_u32 m0, s44, 0\n\t"
    "s_lshl_b32 s44, s69, 11\n\t"
    "buffer_load_dwordx4 v126, %15, s44 offen lds\n\t"
    "s_add_u32 s69, s69, 8\n\t"
    "s_sub_u32 s44, s69, 61\n\t"
    "s_cmp_ge_u32 s69, 61\n\t"
    "s_cselect_b32 s69, s44, s69\n\t"
    "ds_read_b128 v[52:55], v148 offset:13312\n\t"
    "s_waitcnt lgkmcnt(6)\n\t"
    "v_smfmac_f32_16x16x64_bf16 v[184:187], v[120:123], v[92:99], v124\n\t"
    "ds_read_b64 v[92:93], v218 offset:64000\n\t"
    "ds_read_b64 v[94:95], v254 offset:64000\n\t"
    "ds_read_b64 v[96:97], v219 offset:64000\n\t"
    "ds_read_b64 v[98:99], v255 offset:64000\n\t"
    "v_lshrrev_b32 v252, 16, v220\n\t"
    "v_and_b32 v220, s92, v220\n\t"
    "v_lshrrev_b32 v253, 16, v221\n\t"
    "v_and_b32 v221, s92, v221\n\t"
    "v_pk_fma_f32 v[64:65], v[64:65], v[82:83], v[80:81]\n\t"
    "v_pk_fma_f32 v[66:67], v[66:67], v[82:83], v[80:81]\n\t"
    "v_pk_mul_f32 v[64:65], v[64:65], v[180:181]\n\t"
    "v_pk_mul_f32 v[66:67], v[66:67], v[182:183]\n\t"
    "v_exp_f32 v68, v64\n\t"
    "v_exp_f32 v69, v65\n\t"
    "v_exp_f32 v70, v66\n\t"
    "s_mul_i32 s44, s69, 1040\n\t"
    "s_add_u32 m0, s44, 0\n\t"
    "s_lshl_b32 s44, s69, 11\n\t"
    "buffer_load_dwordx4 v126, %15, s44 offen lds\n\t"
    "s_add_u32 s69, s69, 8\n\t"
    "s_sub_u32 s44, s69, 61\n\t"
    "s_cmp_ge_u32 s69, 61\n\t"
    "s_cselect_b32 s69, s44, s69\n\t"
    "s_waitcnt lgkmcnt(5)\n\t"
    "v_smfmac_f32_16x16x64_bf16 v[188:191], v[120:123], v[84:91], v124\n\t"
    "ds_read_b64 v[84:85], v220 offset:64000\n\t"
    "ds_read_b64 v[86:87], v252 offset:64000\n\t"
    "ds_read_b64 v[88:89], v221 offset:64000\n\t"
    "ds_read_b64 v[90:91], v253 offset:64000\n\t"
    "v_lshrrev_b32 v254, 16, v222\n\t"
    "v_and_b32 v222, s92, v222\n\t"
    "v_lshrrev_b32 v255, 16, v223\n\t"
    "v_and_b32 v223, s92, v223\n\t"
    "s_waitcnt vmcnt(8)\n\t"
    "v_smfmac_f32_16x16x64_bf16 v[188:191], v[156:159], v[56:63], v124\n\t"
    "v_exp_f32 v71, v67\n\t"
    "v_pk_fma_f32 v[68:69], v[68:69], s[40:41], s[40:41]\n\t"
    "v_pk_fma_f32 v[70:71], v[70:71], s[42:43], s[42:43]\n\t"
    "v_rcp_f32 v68, v68\n\t"
    "v_rcp_f32 v69, v69\n\t"
    "v_rcp_f32 v70, v70\n\t"
    "v_rcp_f32 v71, v71\n\t"
    "s_mul_i32 s44, s69, 1040\n\t"
    "s_add_u32 m0, s44, 0\n\t"
    "s_lshl_b32 s44, s69, 11\n\t"
    "buffer_load_dwordx4 v126, %15, s44 offen lds\n\t"
    "s_add_u32 s69, s69, 8\n\t"
    "s_sub_u32 s44, s69, 61\n\t"
    "s_cmp_ge_u32 s69, 61\n\t"
    "s_cselect_b32 s69, s44, s69\n\t"
    "s_waitcnt lgkmcnt(4)\n\t"
    "v_smfmac_f32_16x16x64_bf16 v[188:191], v[120:123], v[92:99], v124\n\t"
    "ds_read_b64 v[92:93], v222 offset:64000\n\t"
    "ds_read_b64 v[94:95], v254 offset:64000\n\t"
    "ds_read_b64 v[96:97], v223 offset:64000\n\t"
    "ds_read_b64 v[98:99], v255 offset:64000\n\t"
    "v_smfmac_f32_16x16x64_bf16 v[192:195], v[160:163], v[56:63], v124\n\t"
    "v_pk_mul_f32 v[68:69], v[68:69], v[180:181]\n\t"
    "v_pk_fma_f32 v[68:69], v[70:71], v[182:183], v[68:69]\n\t"
    "v_add_f32 v248, v68, v69\n\t"
    "v_pk_mul_f32 v[72:73], v[184:185], v[184:185]\n\t"
    "v_pk_mul_f32 v[74:75], v[186:187], v[186:187]\n\t"
    "v_pk_fma_f32 v[72:73], v[72:73], v[82:83], v[80:81]\n\t"
    "v_pk_fma_f32 v[74:75], v[74:75], v[82:83], v[80:81]\n\t"
    "s_mul_i32 s44, s69, 1040\n\t"
    "s_add_u32 m0, s44, 0\n\t"
    "s_lshl_b32 s44, s69, 11\n\t"
    "buffer_load_dwordx4 v126, %15, s44 offen lds\n\t"
    "s_add_u32 s69, s69, 8\n\t"
    "s_sub_u32 s44, s69, 61\n\t"
    "s_cmp_ge_u32 s69, 61\n\t"
    "s_cselect_b32 s69, s44, s69\n\t"
    "ds_read_b128 v[56:59], v148 offset:14336\n\t"
    "s_waitcnt lgkmcnt(5)\n\t"
    "v_smfmac_f32_16x16x64_bf16 v[192:195], v[120:123], v[84:91], v124\n\t"
    "v_pk_mul_f32 v[72:73], v[72:73], v[184:185]\n\t"
    "v_pk_mul_f32 v[74:75], v[74:75], v[186:187]\n\t"
    "v_exp_f32 v76, v72\n\t"
    "v_exp_f32 v77, v73\n\t"
    "v_exp_f32 v78, v74\n\t"
    "v_exp_f32 v79, v75\n\t"
    "v_pk_fma_f32 v[76:77], v[76:77], s[40:41], s[40:41]\n\t"
    "ds_read_b128 v[60:63], v148 offset:15360\n\t"
    "s_waitcnt lgkmcnt(2)\n\t"
    "v_smfmac_f32_16x16x64_bf16 v[192:195], v[120:123], v[92:99], v124\n\t"
    "v_pk_fma_f32 v[78:79], v[78:79], s[42:43], s[42:43]\n\t"
    "v_rcp_f32 v76, v76\n\t"
    "v_rcp_f32 v77, v77\n\t"
    "v_rcp_f32 v78, v78\n\t"
    "v_rcp_f32 v79, v79\n\t"
    "v_pk_mul_f32 v[76:77], v[76:77], v[184:185]\n\t"
    "v_pk_fma_f32 v[76:77], v[78:79], v[186:187], v[76:77]\n\t"
    "s_barrier\n\t"
    "v_add_u32 v126, 124928, v126\n\t"
    "s_bfe_u32 s46, %14, 0x30003\n\t"
    "s_min_u32 s46, s46, 6\n\t"
    "s_lshl_b32 s69, s46, 3\n\t"
    "s_add_u32 s69, s69, %16\n\t"
    "s_mul_i32 s44, s69, 1040\n\t"
    "s_add_u32 m0, s44, 63440\n\t"
    "s_lshl_b32 s44, s69, 11\n\t"
    "buffer_load_dwordx4 v126, %15, s44 offen lds\n\t"
    "s_add_u32 s69, s69, 8\n\t"
    "s_sub_u32 s44, s69, 64\n\t"
    "s_cmp_ge_u32 s69, 64\n\t"
    "s_cselect_b32 s69, s44, s69\n\t"
    "s_mul_i32 s44, s69, 1040\n\t"
    "s_add_u32 m0, s44, 63440\n\t"
    "s_lshl_b32 s44, s69, 11\n\t"
    "buffer_load_dwordx4 v126, %15, s44 offen lds\n\t"
    "s_add_u32 s69, s69, 8\n\t"
    "s_sub_u32 s44, s69, 64\n\t"
    "s_cmp_ge_u32 s69, 64\n\t"
    "s_cselect_b32 s69, s44, s69\n\t"
    "s_mul_i32 s44, s69, 1040\n\t"
    "s_add_u32 m0, s44, 63440\n\t"
    "s_lshl_b32 s44, s69, 11\n\t"
    "buffer_load_dwordx4 v126, %15, s44 offen lds\n\t"
    "s_add_u32 s69, s69, 8\n\t"
    "s_sub_u32 s44, s69, 64\n\t"
    "s_cmp_ge_u32 s69, 64\n\t"
    "s_cselect_b32 s69, s44, s69\n\t"
    "s_mul_i32 s44, s69, 1040\n\t"
    "s_add_u32 m0, s44, 63440\n\t"
    "s_lshl_b32 s44, s69, 11\n\t"
    "buffer_load_dwordx4 v126, %15, s44 offen lds\n\t"
    "s_add_u32 s69, s69, 8\n\t"
    "s_sub_u32 s44, s69, 64\n\t"
    "s_cmp_ge_u32 s69, 64\n\t"
    "s_cselect_b32 s69, s44, s69\n\t"
    "s_mul_i32 s44, s69, 1040\n\t"
    "s_add_u32 m0, s44, 63440\n\t"
    "s_lshl_b32 s44, s69, 11\n\t"
    "buffer_load_dwordx4 v126, %15, s44 offen lds\n\t"
    "s_add_u32 s69, s69, 8\n\t"
    "s_sub_u32 s44, s69, 64\n\t"
    "s_cmp_ge_u32 s69, 64\n\t"
    "s_cselect_b32 s69, s44, s69\n\t"
    "s_mul_i32 s44, s69, 1040\n\t"
    "s_add_u32 m0, s44, 63440\n\t"
    "s_lshl_b32 s44, s69, 11\n\t"
    "buffer_load_dwordx4 v126, %15, s44 offen lds\n\t"
    "s_add_u32 s69, s69, 8\n\t"
    "s_sub_u32 s44, s69, 64\n\t"
    "s_cmp_ge_u32 s69, 64\n\t"
    "s_cselect_b32 s69, s44, s69\n\t"
    "s_mul_i32 s44, s69, 1040\n\t"
    "s_add_u32 m0, s44, 63440\n\t"
    "s_lshl_b32 s44, s69, 11\n\t"
    "buffer_load_dwordx4 v126, %15, s44 offen lds\n\t"
    "s_add_u32 s69, s69, 8\n\t"
    "s_sub_u32 s44, s69, 64\n\t"
    "s_cmp_ge_u32 s69, 64\n\t"
    "s_cselect_b32 s69, s44, s69\n\t"
    "s_mul_i32 s44, s69, 1040\n\t"
    "s_add_u32 m0, s44, 63440\n\t"
    "s_lshl_b32 s44, s69, 11\n\t"
    "buffer_load_dwordx4 v126, %15, s44 offen lds\n\t"
    "s_add_u32 s69, s69, 8\n\t"
    "s_sub_u32 s44, s69, 64\n\t"
    "s_cmp_ge_u32 s69, 64\n\t"
    "s_cselect_b32 s69, s44, s69\n\t"
    "ds_read_b128 v[84:87], v148 offset:21504\n\t"
    "ds_read_b128 v[88:91], v148 offset:22528\n\t"
    "v_add_f32 v249, v76, v77\n\t"
    "v_pk_mul_f32 v[64:65], v[188:189], v[188:189]\n\t"
    "v_pk_mul_f32 v[66:67], v[190:191], v[190:191]\n\t"
    "v_pk_fma_f32 v[64:65], v[64:65], v[82:83], v[80:81]\n\t"
    "v_pk_fma_f32 v[66:67], v[66:67], v[82:83], v[80:81]\n\t"
    "v_pk_mul_f32 v[64:65], v[64:65], v[188:189]\n\t"
    "v_pk_mul_f32 v[66:67], v[66:67], v[190:191]\n\t"
    "v_exp_f32 v68, v64\n\t"
    "v_exp_f32 v69, v65\n\t"
    "v_exp_f32 v70, v66\n\t"
    "v_exp_f32 v71, v67\n\t"
    "v_pk_fma_f32 v[68:69], v[68:69], s[40:41], s[40:41]\n\t"
    "v_pk_fma_f32 v[70:71], v[70:71], s[42:43], s[42:43]\n\t"
    "v_rcp_f32 v68, v68\n\t"
    "v_rcp_f32 v69, v69\n\t"
    "v_rcp_f32 v70, v70\n\t"
    "v_rcp_f32 v71, v71\n\t"
    "v_pk_mul_f32 v[68:69], v[68:69], v[188:189]\n\t"
    "v_pk_fma_f32 v[68:69], v[70:71], v[190:191], v[68:69]\n\t"
    "v_add_f32 v250, v68, v69\n\t"
    "v_pk_mul_f32 v[72:73], v[192:193], v[192:193]\n\t"
    "v_pk_mul_f32 v[74:75], v[194:195], v[194:195]\n\t"
    "v_pk_fma_f32 v[72:73], v[72:73], v[82:83], v[80:81]\n\t"
    "v_pk_fma_f32 v[74:75], v[74:75], v[82:83], v[80:81]\n\t"
    "v_pk_mul_f32 v[72:73], v[72:73], v[192:193]\n\t"
    "v_pk_mul_f32 v[74:75], v[74:75], v[194:195]\n\t"
    "v_exp_f32 v76, v72\n\t"
    "v_exp_f32 v77, v73\n\t"
    "v_exp_f32 v78, v74\n\t"
    "v_exp_f32 v79, v75\n\t"
    "v_pk_fma_f32 v[76:77], v[76:77], s[40:41], s[40:41]\n\t"
    "v_pk_fma_f32 v[78:79], v[78:79], s[42:43], s[42:43]\n\t"
    "v_rcp_f32 v76, v76\n\t"
    "v_rcp_f32 v77, v77\n\t"
    "v_rcp_f32 v78, v78\n\t"
    "v_rcp_f32 v79, v79\n\t"
    "v_pk_mul_f32 v[76:77], v[76:77], v[192:193]\n\t"
    "v_pk_fma_f32 v[76:77], v[78:79], v[194:195], v[76:77]\n\t"
    "v_add_f32 v251, v76, v77\n\t"
    "ds_read_b128 v[64:67], v148 offset:16384\n\t"
    "ds_read_b128 v[68:71], v148 offset:17408\n\t"
    "ds_read_b128 v[72:75], v148 offset:18432\n\t"
    "ds_read_b128 v[76:79], v148 offset:19456\n\t"
    "ds_read_b128 v[80:83], v148 offset:20480\n\t"
    "s_waitcnt vmcnt(0) lgkmcnt(0)\n\t"
    "s_mov_b32 s68, 130\n\t"
    "v_mad_u32_u24 v128, v128, s68, v152\n\t"
    "v_and_b32 v128, -16, v128\n\t"
    "v_mad_u32_u24 v129, v129, s68, v151\n\t"
    "v_and_b32 v129, -16, v129\n\t"
    "v_mad_u32_u24 v130, v130, s68, v151\n\t"
    "v_and_b32 v130, -16, v130\n\t"
    "v_mad_u32_u24 v132, v132, s68, v151\n\t"
    "v_and_b32 v132, -16, v132\n\t"
    "v_mad_u32_u24 v133, v133, s68, v151\n\t"
    "v_and_b32 v133, -16, v133\n\t"
    "v_mad_u32_u24 v134, v134, s68, v151\n\t"
    "v_and_b32 v134, -16, v134\n\t"
    "v_mad_u32_u24 v135, v135, s68, v151\n\t"
    "v_and_b32 v135, -16, v135\n\t"
    "v_lshrrev_b32 v122, 4, v147\n\t"
    "v_and_b32 v155, 15, v147\n\t"
    "v_lshlrev_b32 v155, 4, v155\n\t"
    "v_mul_u32_u24 v122, 65536, v122\n\t"
    "v_add_u32 v122, v122, v155\n\t"
    "v_mov_b32 v150, 1\n\t"
    "v_mov_b32 v123, 0\n\t"
    "v_lshlrev_b32 v149, 20, v147\n\t"
    "v_add_u32 v149, s49, v149\n\t"
    "s_mov_b32 s52, s53\n\t"
    "s_cmp_ge_u32 %16, 4\n\t"
    "s_cbranch_scc1 L_st4_%=\n\t"
    "s_bfe_u32 s65, s84, 0xc0002\n\t"
    "s_add_u32 s65, s65, s36\n\t"
    "buffer_store_dword v236, v125, %12, s65 offen offset:0 sc1\n\t"
    "buffer_store_dword v237, v125, %12, s65 offen offset:256 sc1\n\t"
    "s_bfe_u32 s65, s85, 0xc0002\n\t"
    "s_add_u32 s65, s65, s36\n\t"
    "buffer_store_dword v238, v125, %12, s65 offen offset:0 sc1\n\t"
    "buffer_store_dword v239, v125, %12, s65 offen offset:256 sc1\n\t"
    "s_bfe_u32 s65, s86, 0xc0002\n\t"
    "s_add_u32 s65, s65, s36\n\t"
    "buffer_store_dword v240, v125, %12, s65 offen offset:0 sc1\n\t"
    "buffer_store_dword v241, v125, %12, s65 offen offset:256 sc1\n\t"
    "s_bfe_u32 s65, s87, 0xc0002\n\t"
    "s_add_u32 s65, s65, s36\n\t"
    "buffer_store_dword v242, v125, %12, s65 offen offset:0 sc1\n\t"
    "buffer_store_dword v243, v125, %12, s65 offen offset:256 sc1\n\t"
    "s_bfe_u32 s65, s88, 0xc0002\n\t"
    "s_add_u32 s65, s65, s36\n\t"
    "buffer_store_dword v244, v125, %12, s65 offen offset:0 sc1\n\t"
    "buffer_store_dword v245, v125, %12, s65 offen offset:256 sc1\n\t"
    "s_bfe_u32 s65, s89, 0xc0002\n\t"
    "s_add_u32 s65, s65, s36\n\t"
    "buffer_store_dword v246, v125, %12, s65 offen offset:0 sc1\n\t"
    "buffer_store_dword v247, v125, %12, s65 offen offset:256 sc1\n\t"
    "s_bfe_u32 s65, s90, 0xc0002\n\t"
    "s_add_u32 s65, s65, s36\n\t"
    "buffer_store_dword v248, v125, %12, s65 offen offset:0 sc1\n\t"
    "buffer_store_dword v249, v125, %12, s65 offen offset:256 sc1\n\t"
    "s_bfe_u32 s65, s91, 0xc0002\n\t"
    "s_add_u32 s65, s65, s36\n\t"
    "buffer_store_dword v250, v125, %12, s65 offen offset:0 sc1\n\t"
    "buffer_store_dword v251, v125, %12, s65 offen offset:256 sc1\n\t"
    "s_sub_u32 s61, s64, 3\n\t"
    "L_st4_%=:\n\t"
    "s_barrier\n\t"
    "s_mov_b32 s68, 130\n\t"
    "ds_read_b128 v[156:159], v128 offset:0\n\t"
    "ds_read_b128 v[160:163], v128 offset:64\n\t"
    "ds_read_b128 v[164:167], v129 offset:0\n\t"
    "ds_read_b128 v[168:171], v129 offset:64\n\t"
    "ds_read_b128 v[172:175], v130 offset:0\n\t"
    "ds_read_b128 v[176:179], v130 offset:64\n\t"
    "ds_read_b128 v[180:183], v132 offset:0\n\t"
    "ds_read_b128 v[184:187], v132 offset:64\n\t"
    "ds_read_b128 v[188:191], v133 offset:0\n\t"
    "ds_read_b128 v[192:195], v133 offset:64\n\t"
    "ds_read_b128 v[196:199], v134 offset:0\n\t"
    "ds_read_b128 v[200:203], v134 offset:64\n\t"
    "ds_read_b128 v[204:207], v135 offset:0\n\t"
    "ds_read_b128 v[208:211], v135 offset:64\n\t"
    "s_setprio 1\n\t"
    "L_pnn_%=:\n\t"
    "s_mul_i32 s69, s52, 272\n\t"
    "s_add_u32 s69, s69, 130048\n\t"
    "s_mul_i32 s66, s55, 400\n\t"
    "s_waitcnt lgkmcnt(8)\n\t"
    "v_mfma_f32_16x16x32_bf16 v[92:95], v[156:159], v[156:159], 0\n\t"
    "v_add_u32 v147, s69, v146\n\t"
    "v_fmac_f32 v121, v108, v72\n\t"
    "ds_add_rtn_u32 v148, v149, v150\n\t"
    "v_mfma_f32_16x16x32_bf16 v[92:95], v[160:163], v[160:163], v[92:95]\n\t"
    "v_fmac_f32 v120, v109, v73\n\t"
    "v_fmac_f32 v121, v110, v74\n\t"
    "buffer_load_dwordx3 v[128:130], v153, s[96:99], s66 offen\n\t"
    "v_mfma_f32_16x16x32_bf16 v[96:99], v[164:167], v[164:167], 0\n\t"
    "v_fmac_f32 v120, v111, v75\n\t"
    "v_fmac_f32 v121, v112, v76\n\t"
    "v_mfma_f32_16x16x32_bf16 v[96:99], v[168:171], v[168:171], v[96:99]\n\t"
    "v_fmac_f32 v120, v113, v77\n\t"
    "v_fmac_f32 v121, v114, v78\n\t"
    "buffer_load_dwordx4 v[132:135], v153, s[96:99], s66 offen offset:12\n\t"
    "v_mfma_f32_16x16x32_bf16 v[100:103], v[164:167], v[172:175], 0\n\t"
    "v_fmac_f32 v120, v115, v79\n\t"
    "v_fmac_f32 v121, v116, v80\n\t"
    "v_mfma_f32_16x16x32_bf16 v[100:103], v[168:171], v[176:179], v[100:103]\n\t"
    "v_fmac_f32 v120, v117, v81\n\t"
    "v_fmac_f32 v121, v118, v82\n\t"
    "s_waitcnt vmcnt(3)\n\t"
    "s_cmp_ge_u32 s55, s61\n\t"
    "s_cbranch_scc1 L_req0_%=\n\t"
    "L_back0_%=:\n\t"
    "v_mfma_f32_16x16x32_bf16 v[104:107], v[172:175], v[172:175], 0\n\t"
    "v_mad_u32_u24 v136, v136, s68, v152\n\t"
    "v_and_b32 v136, -16, v136\n\t"
    "v_mfma_f32_16x16x32_bf16 v[104:107], v[176:179], v[176:179], v[104:107]\n\t"
    "v_fmac_f32 v120, v119, v83\n\t"
    "v_add_f32 v154, v120, v121\n\t"
    "ds_write_b32 v147, v154\n\t"
    "v_mfma_f32_16x16x32_bf16 v[108:111], v[156:159], v[164:167], 0\n\t"
    "v_fma_f32 v120, v92, v84, v144\n\t"
    "v_mul_f32 v121, v96, v0\n\t"
    "v_mfma_f32_16x16x32_bf16 v[108:111], v[160:163], v[168:171], v[108:111]\n\t"
    "v_fmac_f32 v120, v97, v1\n\t"
    "v_fmac_f32 v121, v98, v2\n\t"
    "s_waitcnt lgkmcnt(8)\n\t"
    "v_mfma_f32_16x16x32_bf16 v[112:115], v[164:167], v[180:183], 0\n\t"
    "v_fmac_f32 v120, v99, v3\n\t"
    "v_fmac_f32 v121, v100, v4\n\t"
    "v_mfma_f32_16x16x32_bf16 v[112:115], v[168:171], v[184:187], v[112:115]\n\t"
    "v_fmac_f32 v120, v101, v5\n\t"
    "v_fmac_f32 v121, v102, v6\n\t"
    "v_mfma_f32_16x16x32_bf16 v[116:119], v[172:175], v[180:183], 0\n\t"
    "v_fmac_f32 v120, v103, v7\n\t"
    "v_fmac_f32 v121, v104, v8\n\t"
    "v_mfma_f32_16x16x32_bf16 v[116:119], v[176:179], v[184:187], v[116:119]\n\t"
    "v_fmac_f32 v120, v105, v9\n\t"
    "v_fmac_f32 v121, v106, v10\n\t"
    "ds_read_b128 v[212:215], v136 offset:0\n\t"
    "ds_read_b128 v[216:219], v136 offset:64\n\t"
    "v_mfma_f32_16x16x32_bf16 v[92:95], v[180:183], v[180:183], 0\n\t"
    "v_fmac_f32 v120, v107, v11\n\t"
    "v_mad_u32_u24 v137, v137, s68, v151\n\t"
    "v_mfma_f32_16x16x32_bf16 v[92:95], v[184:187], v[184:187], v[92:95]\n\t"
    "v_and_b32 v137, -16, v137\n\t"
    "v_fmac_f32 v121, v108, v85\n\t"
    "v_mfma_f32_16x16x32_bf16 v[96:99], v[156:159], v[172:175], 0\n\t"
    "v_fmac_f32 v120, v112, v12\n\t"
    "v_fmac_f32 v121, v113, v13\n\t"
    "v_mfma_f32_16x16x32_bf16 v[96:99], v[160:163], v[176:179], v[96:99]\n\t"
    "v_fmac_f32 v120, v114, v14\n\t"
    "v_fmac_f32 v121, v115, v15\n\t"
    "s_lshl_b32 s51, s55, 8\n\t"
    "s_add_u32 s51, s51, s50\n\t"
    "s_waitcnt lgkmcnt(8)\n\t"
    "v_mfma_f32_16x16x32_bf16 v[100:103], v[164:167], v[188:191], 0\n\t"
    "v_mad_u32_u24 v138, v138, s68, v151\n\t"
    "v_and_b32 v138, -16, v138\n\t"
    "v_mfma_f32_16x16x32_bf16 v[100:103], v[168:171], v[192:195], v[100:103]\n\t"
    "v_fmac_f32 v120, v116, v16\n\t"
    "v_fmac_f32 v121, v117, v17\n\t"
    "buffer_load_dword v144, v146, %12, s51 offen sc1\n\t"
    "v_mfma_f32_16x16x32_bf16 v[104:107], v[172:175], v[188:191], 0\n\t"
    "v_fmac_f32 v120, v118, v18\n\t"
    "v_fmac_f32 v121, v119, v19\n\t"
    "v_mfma_f32_16x16x32_bf16 v[104:107], v[176:179], v[192:195], v[104:107]\n\t"
    "v_fmac_f32 v120, v92, v20\n\t"
    "v_fmac_f32 v121, v93, v21\n\t"
    "s_waitcnt lgkmcnt(6)\n\t"
    "v_mfma_f32_16x16x32_bf16 v[108:111], v[164:167], v[196:199], 0\n\t"
    "v_fmac_f32 v120, v94, v22\n\t"
    "v_fmac_f32 v121, v95, v23\n\t"
    "v_mfma_f32_16x16x32_bf16 v[108:111], v[168:171], v[200:203], v[108:111]\n\t"
    "v_fmac_f32 v120, v96, v86\n\t"
    "v_mad_u32_u24 v140, v140, s68, v151\n\t"
    "v_mfma_f32_16x16x32_bf16 v[112:115], v[156:159], v[180:183], 0\n\t"
    "v_fmac_f32 v121, v100, v24\n\t"
    "v_fmac_f32 v120, v101, v25\n\t"
    "v_mfma_f32_16x16x32_bf16 v[112:115], v[160:163], v[184:187], v[112:115]\n\t"
    "v_fmac_f32 v121, v102, v26\n\t"
    "v_fmac_f32 v120, v103, v27\n\t"
    "s_waitcnt lgkmcnt(3)\n\t"
    "v_mfma_f32_16x16x32_bf16 v[116:119], v[164:167], v[204:207], 0\n\t"
    "v_readfirstlane_b32 s60, v148\n\t"
    "v_and_b32 v140, -16, v140\n\t"
    "v_mfma_f32_16x16x32_bf16 v[116:119], v[168:171], v[208:211], v[116:119]\n\t"
    "v_fmac_f32 v121, v104, v28\n\t"
    "v_fmac_f32 v120, v105, v29\n\t"
    "ds_read_b128 v[164:167], v137 offset:0\n\t"
    "ds_read_b128 v[168:171], v137 offset:64\n\t"
    "v_mfma_f32_16x16x32_bf16 v[92:95], v[172:175], v[196:199], 0\n\t"
    "v_fmac_f32 v121, v106, v30\n\t"
    "v_fmac_f32 v120, v107, v31\n\t"
    "v_mfma_f32_16x16x32_bf16 v[92:95], v[176:179], v[200:203], v[92:95]\n\t"
    "v_fmac_f32 v121, v108, v32\n\t"
    "v_fmac_f32 v120, v109, v33\n\t"
    "v_mfma_f32_16x16x32_bf16 v[96:99], v[172:175], v[204:207], 0\n\t"
    "v_fmac_f32 v121, v110, v34\n\t"
    "v_fmac_f32 v120, v111, v35\n\t"
    "v_mfma_f32_16x16x32_bf16 v[96:99], v[176:179], v[208:211], v[96:99]\n\t"
    "v_fmac_f32 v121, v112, v87\n\t"
    "v_mad_u32_u24 v141, v141, s68, v151\n\t"
    "ds_read_b128 v[172:175], v138 offset:0\n\t"
    "ds_read_b128 v[176:179], v138 offset:64\n\t"
    "v_mfma_f32_16x16x32_bf16 v[100:103], v[156:159], v[188:191], 0\n\t"
    "v_fmac_f32 v120, v116, v36\n\t"
    "v_fmac_f32 v121, v117, v37\n\t"
    "v_mfma_f32_16x16x32_bf16 v[100:103], v[160:163], v[192:195], v[100:103]\n\t"
    "v_fmac_f32 v120, v118, v38\n\t"
    "v_fmac_f32 v121, v119, v39\n\t"
    "v_mfma_f32_16x16x32_bf16 v[104:107], v[180:183], v[188:191], 0\n\t"
    "v_and_b32 v141, -16, v141\n\t"
    "v_fmac_f32 v120, v92, v40\n\t"
    "v_mfma_f32_16x16x32_bf16 v[104:107], v[184:187], v[192:195], v[104:107]\n\t"
    "v_fmac_f32 v121, v93, v41\n\t"
    "v_fmac_f32 v120, v94, v42\n\t"
    "v_mfma_f32_16x16x32_bf16 v[108:111], v[180:183], v[196:199], 0\n\t"
    "v_fmac_f32 v121, v95, v43\n\t"
    "v_fmac_f32 v120, v96, v44\n\t"
    "v_mfma_f32_16x16x32_bf16 v[108:111], v[184:187], v[200:203], v[108:111]\n\t"
    "v_fmac_f32 v121, v97, v45\n\t"
    "v_fmac_f32 v120, v98, v46\n\t"
    "v_mfma_f32_16x16x32_bf16 v[112:115], v[180:183], v[204:207], 0\n\t"
    "v_fmac_f32 v121, v99, v47\n\t"
    "v_fmac_f32 v120, v100, v88\n\t"
    "v_mfma_f32_16x16x32_bf16 v[112:115], v[184:187], v[208:211], v[112:115]\n\t"
    "v_mad_u32_u24 v142, v142, s68, v151\n\t"
    "v_and_b32 v142, -16, v142\n\t"
    "ds_read_b128 v[180:183], v140 offset:0\n\t"
    "ds_read_b128 v[184:187], v140 offset:64\n\t"
    "s_cmp_eq_u32 s45, 1\n\t"
    "s_cbranch_scc1 L_sig_%=\n\t"
    "L_sigb_%=:\n\t"
    "v_mfma_f32_16x16x32_bf16 v[116:119], v[156:159], v[196:199], 0\n\t"
    "v_fmac_f32 v121, v104, v48\n\t"
    "v_fmac_f32 v120, v105, v49\n\t"
    "v_mfma_f32_16x16x32_bf16 v[116:119], v[160:163], v[200:203], v[116:119]\n\t"
    "v_fmac_f32 v121, v106, v50\n\t"
    "v_fmac_f32 v120, v107, v51\n\t"
    "v_mfma_f32_16x16x32_bf16 v[92:95], v[188:191], v[188:191], 0\n\t"
    "v_mad_u32_u24 v143, v143, s68, v151\n\t"
    "v_and_b32 v143, -16, v143\n\t"
    "v_mfma_f32_16x16x32_bf16 v[92:95], v[192:195], v[192:195], v[92:95]\n\t"
    "v_fmac_f32 v121, v108, v52\n\t"
    "v_fmac_f32 v120, v109, v53\n\t"
    "v_mfma_f32_16x16x32_bf16 v[96:99], v[188:191], v[196:199], 0\n\t"
    "v_fmac_f32 v121, v110, v54\n\t"
    "v_fmac_f32 v120, v111, v55\n\t"
    "v_mfma_f32_16x16x32_bf16 v[96:99], v[192:195], v[200:203], v[96:99]\n\t"
    "v_fmac_f32 v121, v112, v56\n\t"
    "v_fmac_f32 v120, v113, v57\n\t"
    "v_mfma_f32_16x16x32_bf16 v[100:103], v[188:191], v[204:207], 0\n\t"
    "v_fmac_f32 v121, v114, v58\n\t"
    "v_fmac_f32 v120, v115, v59\n\t"
    "v_mfma_f32_16x16x32_bf16 v[100:103], v[192:195], v[208:211], v[100:103]\n\t"
    "v_fmac_f32 v121, v116, v89\n\t"
    "ds_read_b128 v[188:191], v141 offset:0\n\t"
    "ds_read_b128 v[192:195], v141 offset:64\n\t"
    "v_mfma_f32_16x16x32_bf16 v[104:107], v[156:159], v[204:207], 0\n\t"
    "v_fmac_f32 v120, v92, v60\n\t"
    "v_fmac_f32 v121, v93, v61\n\t"
    "v_mfma_f32_16x16x32_bf16 v[104:107], v[160:163], v[208:211], v[104:107]\n\t"
    "v_fmac_f32 v120, v94, v62\n\t"
    "v_fmac_f32 v121, v95, v63\n\t"
    "v_mfma_f32_16x16x32_bf16 v[108:111], v[196:199], v[196:199], 0\n\t"
    "v_fmac_f32 v120, v96, v64\n\t"
    "v_fmac_f32 v121, v97, v65\n\t"
    "v_mfma_f32_16x16x32_bf16 v[108:111], v[200:203], v[200:203], v[108:111]\n\t"
    "v_fmac_f32 v120, v98, v66\n\t"
    "v_fmac_f32 v121, v99, v67\n\t"
    "v_mfma_f32_16x16x32_bf16 v[112:115], v[196:199], v[204:207], 0\n\t"
    "v_fmac_f32 v120, v100, v68\n\t"
    "v_fmac_f32 v121, v101, v69\n\t"
    "v_mfma_f32_16x16x32_bf16 v[112:115], v[200:203], v[208:211], v[112:115]\n\t"
    "v_fmac_f32 v120, v102, v70\n\t"
    "v_fmac_f32 v121, v103, v71\n\t"
    "ds_read_b128 v[196:199], v142 offset:0\n\t"
    "ds_read_b128 v[200:203], v142 offset:64\n\t"
    "v_mfma_f32_16x16x32_bf16 v[116:119], v[204:207], v[204:207], 0\n\t"
    "v_fmac_f32 v120, v104, v90\n\t"
    "v_mfma_f32_16x16x32_bf16 v[116:119], v[208:211], v[208:211], v[116:119]\n\t"
    "ds_read_b128 v[204:207], v143 offset:0\n\t"
    "ds_read_b128 v[208:211], v143 offset:64\n\t"
    "s_mov_b32 s52, s53\n\t"
    "s_mov_b32 s53, s54\n\t"
    "s_mov_b32 s54, s55\n\t"
    "s_mov_b32 s55, s60\n\t"
    "s_cmp_lt_u32 s54, s64\n\t"
    "s_cbranch_scc1 L_pr0_%=\n\t"
    "s_setprio 0\n\t"
    "L_pr0_%=:\n\t"
    "s_cmp_lt_u32 s53, s64\n\t"
    "s_cbranch_scc0 L_done_%=\n\t"
    "s_mul_i32 s69, s52, 272\n\t"
    "s_add_u32 s69, s69, 130048\n\t"
    "s_mul_i32 s66, s55, 400\n\t"
    "s_waitcnt lgkmcnt(8)\n\t"
    "v_mfma_f32_16x16x32_bf16 v[92:95], v[212:215], v[212:215], 0\n\t"
    "v_add_u32 v147, s69, v146\n\t"
    "v_fmac_f32 v121, v108, v72\n\t"
    "ds_add_rtn_u32 v148, v149, v150\n\t"
    "v_mfma_f32_16x16x32_bf16 v[92:95], v[216:219], v[216:219], v[92:95]\n\t"
    "v_fmac_f32 v120, v109, v73\n\t"
    "v_fmac_f32 v121, v110, v74\n\t"
    "buffer_load_dwordx3 v[136:138], v153, s[96:99], s66 offen\n\t"
    "v_mfma_f32_16x16x32_bf16 v[96:99], v[164:167], v[164:167], 0\n\t"
    "v_fmac_f32 v120, v111, v75\n\t"
    "v_fmac_f32 v121, v112, v76\n\t"
    "v_mfma_f32_16x16x32_bf16 v[96:99], v[168:171], v[168:171], v[96:99]\n\t"
    "v_fmac_f32 v120, v113, v77\n\t"
    "v_fmac_f32 v121, v114, v78\n\t"
    "buffer_load_dwordx4 v[140:143], v153, s[96:99], s66 offen offset:12\n\t"
    "v_mfma_f32_16x16x32_bf16 v[100:103], v[164:167], v[172:175], 0\n\t"
    "v_fmac_f32 v120, v115, v79\n\t"
    "v_fmac_f32 v121, v116, v80\n\t"
    "v_mfma_f32_16x16x32_bf16 v[100:103], v[168:171], v[176:179], v[100:103]\n\t"
    "v_fmac_f32 v120, v117, v81\n\t"
    "v_fmac_f32 v121, v118, v82\n\t"
    "s_waitcnt vmcnt(3)\n\t"
    "s_cmp_ge_u32 s55, s61\n\t"
    "s_cbranch_scc1 L_req1_%=\n\t"
    "L_back1_%=:\n\t"
    "v_mfma_f32_16x16x32_bf16 v[104:107], v[172:175], v[172:175], 0\n\t"
    "v_mad_u32_u24 v128, v128, s68, v152\n\t"
    "v_and_b32 v128, -16, v128\n\t"
    "v_mfma_f32_16x16x32_bf16 v[104:107], v[176:179], v[176:179], v[104:107]\n\t"
    "v_fmac_f32 v120, v119, v83\n\t"
    "v_add_f32 v154, v120, v121\n\t"
    "ds_write_b32 v147, v154\n\t"
    "v_mfma_f32_16x16x32_bf16 v[108:111], v[212:215], v[164:167], 0\n\t"
    "v_fma_f32 v120, v92, v84, v145\n\t"
    "v_mul_f32 v121, v96, v0\n\t"
    "v_mfma_f32_16x16x32_bf16 v[108:111], v[216:219], v[168:171], v[108:111]\n\t"
    "v_fmac_f32 v120, v97, v1\n\t"
    "v_fmac_f32 v121, v98, v2\n\t"
    "s_waitcnt lgkmcnt(8)\n\t"
    "v_mfma_f32_16x16x32_bf16 v[112:115], v[164:167], v[180:183], 0\n\t"
    "v_fmac_f32 v120, v99, v3\n\t"
    "v_fmac_f32 v121, v100, v4\n\t"
    "v_mfma_f32_16x16x32_bf16 v[112:115], v[168:171], v[184:187], v[112:115]\n\t"
    "v_fmac_f32 v120, v101, v5\n\t"
    "v_fmac_f32 v121, v102, v6\n\t"
    "v_mfma_f32_16x16x32_bf16 v[116:119], v[172:175], v[180:183], 0\n\t"
    "v_fmac_f32 v120, v103, v7\n\t"
    "v_fmac_f32 v121, v104, v8\n\t"
    "v_mfma_f32_16x16x32_bf16 v[116:119], v[176:179], v[184:187], v[116:119]\n\t"
    "v_fmac_f32 v120, v105, v9\n\t"
    "v_fmac_f32 v121, v106, v10\n\t"
    "ds_read_b128 v[156:159], v128 offset:0\n\t"
    "ds_read_b128 v[160:163], v128 offset:64\n\t"
    "v_mfma_f32_16x16x32_bf16 v[92:95], v[180:183], v[180:183], 0\n\t"
    "v_fmac_f32 v120, v107, v11\n\t"
    "v_mad_u32_u24 v129, v129, s68, v151\n\t"
    "v_mfma_f32_16x16x32_bf16 v[92:95], v[184:187], v[184:187], v[92:95]\n\t"
    "v_and_b32 v129, -16, v129\n\t"
    "v_fmac_f32 v121, v108, v85\n\t"
    "v_mfma_f32_16x16x32_bf16 v[96:99], v[212:215], v[172:175], 0\n\t"
    "v_fmac_f32 v120, v112, v12\n\t"
    "v_fmac_f32 v121, v113, v13\n\t"
    "v_mfma_f32_16x16x32_bf16 v[96:99], v[216:219], v[176:179], v[96:99]\n\t"
    "v_fmac_f32 v120, v114, v14\n\t"
    "v_fmac_f32 v121, v115, v15\n\t"
    "s_lshl_b32 s51, s55, 8\n\t"
    "s_add_u32 s51, s51, s50\n\t"
    "s_waitcnt lgkmcnt(8)\n\t"
    "v_mfma_f32_16x16x32_bf16 v[100:103], v[164:167], v[188:191], 0\n\t"
    "v_mad_u32_u24 v130, v130, s68, v151\n\t"
    "v_and_b32 v130, -16, v130\n\t"
    "v_mfma_f32_16x16x32_bf16 v[100:103], v[168:171], v[192:195], v[100:103]\n\t"
    "v_fmac_f32 v120, v116, v16\n\t"
    "v_fmac_f32 v121, v117, v17\n\t"
    "buffer_load_dword v145, v146, %12, s51 offen sc1\n\t"
    "v_mfma_f32_16x16x32_bf16 v[104:107], v[172:175], v[188:191], 0\n\t"
    "v_fmac_f32 v120, v118, v18\n\t"
    "v_fmac_f32 v121, v119, v19\n\t"
    "v_mfma_f32_16x16x32_bf16 v[104:107], v[176:179], v[192:195], v[104:107]\n\t"
    "v_fmac_f32 v120, v92, v20\n\t"
    "v_fmac_f32 v121, v93, v21\n\t"
    "s_waitcnt lgkmcnt(6)\n\t"
    "v_mfma_f32_16x16x32_bf16 v[108:111], v[164:167], v[196:199], 0\n\t"
    "v_fmac_f32 v120, v94, v22\n\t"
    "v_fmac_f32 v121, v95, v23\n\t"
    "v_mfma_f32_16x16x32_bf16 v[108:111], v[168:171], v[200:203], v[108:111]\n\t"
    "v_fmac_f32 v120, v96, v86\n\t"
    "v_mad_u32_u24 v132, v132, s68, v151\n\t"
    "v_mfma_f32_16x16x32_bf16 v[112:115], v[212:215], v[180:183], 0\n\t"
    "v_fmac_f32 v121, v100, v24\n\t"
    "v_fmac_f32 v120, v101, v25\n\t"
    "v_mfma_f32_16x16x32_bf16 v[112:115], v[216:219], v[184:187], v[112:115]\n\t"
    "v_fmac_f32 v121, v102, v26\n\t"
    "v_fmac_f32 v120, v103, v27\n\t"
    "s_waitcnt lgkmcnt(3)\n\t"
    "v_mfma_f32_16x16x32_bf16 v[116:119], v[164:167], v[204:207], 0\n\t"
    "v_readfirstlane_b32 s60, v148\n\t"
    "v_and_b32 v132, -16, v132\n\t"
    "v_mfma_f32_16x16x32_bf16 v[116:119], v[168:171], v[208:211], v[116:119]\n\t"
    "v_fmac_f32 v121, v104, v28\n\t"
    "v_fmac_f32 v120, v105, v29\n\t"
    "ds_read_b128 v[164:167], v129 offset:0\n\t"
    "ds_read_b128 v[168:171], v129 offset:64\n\t"
    "v_mfma_f32_16x16x32_bf16 v[92:95], v[172:175], v[196:199], 0\n\t"
    "v_fmac_f32 v121, v106, v30\n\t"
    "v_fmac_f32 v120, v107, v31\n\t"
    "v_mfma_f32_16x16x32_bf16 v[92:95], v[176:179], v[200:203], v[92:95]\n\t"
    "v_fmac_f32 v121, v108, v32\n\t"
    "v_fmac_f32 v120, v109, v33\n\t"
    "v_mfma_f32_16x16x32_bf16 v[96:99], v[172:175], v[204:207], 0\n\t"
    "v_fmac_f32 v121, v110, v34\n\t"
    "v_fmac_f32 v120, v111, v35\n\t"
    "v_mfma_f32_16x16x32_bf16 v[96:99], v[176:179], v[208:211], v[96:99]\n\t"
    "v_fmac_f32 v121, v112, v87\n\t"
    "v_mad_u32_u24 v133, v133, s68, v151\n\t"
    "ds_read_b128 v[172:175], v130 offset:0\n\t"
    "ds_read_b128 v[176:179], v130 offset:64\n\t"
    "v_mfma_f32_16x16x32_bf16 v[100:103], v[212:215], v[188:191], 0\n\t"
    "v_fmac_f32 v120, v116, v36\n\t"
    "v_fmac_f32 v121, v117, v37\n\t"
    "v_mfma_f32_16x16x32_bf16 v[100:103], v[216:219], v[192:195], v[100:103]\n\t"
    "v_fmac_f32 v120, v118, v38\n\t"
    "v_fmac_f32 v121, v119, v39\n\t"
    "v_mfma_f32_16x16x32_bf16 v[104:107], v[180:183], v[188:191], 0\n\t"
    "v_and_b32 v133, -16, v133\n\t"
    "v_fmac_f32 v120, v92, v40\n\t"
    "v_mfma_f32_16x16x32_bf16 v[104:107], v[184:187], v[192:195], v[104:107]\n\t"
    "v_fmac_f32 v121, v93, v41\n\t"
    "v_fmac_f32 v120, v94, v42\n\t"
    "v_mfma_f32_16x16x32_bf16 v[108:111], v[180:183], v[196:199], 0\n\t"
    "v_fmac_f32 v121, v95, v43\n\t"
    "v_fmac_f32 v120, v96, v44\n\t"
    "v_mfma_f32_16x16x32_bf16 v[108:111], v[184:187], v[200:203], v[108:111]\n\t"
    "v_fmac_f32 v121, v97, v45\n\t"
    "v_fmac_f32 v120, v98, v46\n\t"
    "v_mfma_f32_16x16x32_bf16 v[112:115], v[180:183], v[204:207], 0\n\t"
    "v_fmac_f32 v121, v99, v47\n\t"
    "v_fmac_f32 v120, v100, v88\n\t"
    "v_mfma_f32_16x16x32_bf16 v[112:115], v[184:187], v[208:211], v[112:115]\n\t"
    "v_mad_u32_u24 v134, v134, s68, v151\n\t"
    "v_and_b32 v134, -16, v134\n\t"
    "ds_read_b128 v[180:183], v132 offset:0\n\t"
    "ds_read_b128 v[184:187], v132 offset:64\n\t"
    "v_mfma_f32_16x16x32_bf16 v[116:119], v[212:215], v[196:199], 0\n\t"
    "v_fmac_f32 v121, v104, v48\n\t"
    "v_fmac_f32 v120, v105, v49\n\t"
    "v_mfma_f32_16x16x32_bf16 v[116:119], v[216:219], v[200:203], v[116:119]\n\t"
    "v_fmac_f32 v121, v106, v50\n\t"
    "v_fmac_f32 v120, v107, v51\n\t"
    "v_mfma_f32_16x16x32_bf16 v[92:95], v[188:191], v[188:191], 0\n\t"
    "v_mad_u32_u24 v135, v135, s68, v151\n\t"
    "v_and_b32 v135, -16, v135\n\t"
    "v_mfma_f32_16x16x32_bf16 v[92:95], v[192:195], v[192:195], v[92:95]\n\t"
    "v_fmac_f32 v121, v108, v52\n\t"
    "v_fmac_f32 v120, v109, v53\n\t"
    "v_mfma_f32_16x16x32_bf16 v[96:99], v[188:191], v[196:199], 0\n\t"
    "v_fmac_f32 v121, v110, v54\n\t"
    "v_fmac_f32 v120, v111, v55\n\t"
    "v_mfma_f32_16x16x32_bf16 v[96:99], v[192:195], v[200:203], v[96:99]\n\t"
    "v_fmac_f32 v121, v112, v56\n\t"
    "v_fmac_f32 v120, v113, v57\n\t"
    "v_mfma_f32_16x16x32_bf16 v[100:103], v[188:191], v[204:207], 0\n\t"
    "v_fmac_f32 v121, v114, v58\n\t"
    "v_fmac_f32 v120, v115, v59\n\t"
    "v_mfma_f32_16x16x32_bf16 v[100:103], v[192:195], v[208:211], v[100:103]\n\t"
    "v_fmac_f32 v121, v116, v89\n\t"
    "ds_read_b128 v[188:191], v133 offset:0\n\t"
    "ds_read_b128 v[192:195], v133 offset:64\n\t"
    "v_mfma_f32_16x16x32_bf16 v[104:107], v[212:215], v[204:207], 0\n\t"
    "v_fmac_f32 v120, v92, v60\n\t"
    "v_fmac_f32 v121, v93, v61\n\t"
    "v_mfma_f32_16x16x32_bf16 v[104:107], v[216:219], v[208:211], v[104:107]\n\t"
    "v_fmac_f32 v120, v94, v62\n\t"
    "v_fmac_f32 v121, v95, v63\n\t"
    "v_mfma_f32_16x16x32_bf16 v[108:111], v[196:199], v[196:199], 0\n\t"
    "v_fmac_f32 v120, v96, v64\n\t"
    "v_fmac_f32 v121, v97, v65\n\t"
    "v_mfma_f32_16x16x32_bf16 v[108:111], v[200:203], v[200:203], v[108:111]\n\t"
    "v_fmac_f32 v120, v98, v66\n\t"
    "v_fmac_f32 v121, v99, v67\n\t"
    "v_mfma_f32_16x16x32_bf16 v[112:115], v[196:199], v[204:207], 0\n\t"
    "v_fmac_f32 v120, v100, v68\n\t"
    "v_fmac_f32 v121, v101, v69\n\t"
    "v_mfma_f32_16x16x32_bf16 v[112:115], v[200:203], v[208:211], v[112:115]\n\t"
    "v_fmac_f32 v120, v102, v70\n\t"
    "v_fmac_f32 v121, v103, v71\n\t"
    "ds_read_b128 v[196:199], v134 offset:0\n\t"
    "ds_read_b128 v[200:203], v134 offset:64\n\t"
    "v_mfma_f32_16x16x32_bf16 v[116:119], v[204:207], v[204:207], 0\n\t"
    "v_fmac_f32 v120, v104, v90\n\t"
    "v_mfma_f32_16x16x32_bf16 v[116:119], v[208:211], v[208:211], v[116:119]\n\t"
    "ds_read_b128 v[204:207], v135 offset:0\n\t"
    "ds_read_b128 v[208:211], v135 offset:64\n\t"
    "s_mov_b32 s52, s53\n\t"
    "s_mov_b32 s53, s54\n\t"
    "s_mov_b32 s54, s55\n\t"
    "s_mov_b32 s55, s60\n\t"
    "s_cmp_lt_u32 s54, s64\n\t"
    "s_cbranch_scc1 L_pr1_%=\n\t"
    "s_setprio 0\n\t"
    "L_pr1_%=:\n\t"
    "s_cmp_lt_u32 s53, s64\n\t"
    "s_cbranch_scc1 L_pnn_%=\n\t"
    "L_done_%=:\n\t"
    "s_mul_i32 s69, s52, 272\n\t"
    "s_add_u32 s69, s69, 130048\n\t"
    "v_add_u32 v147, s69, v146\n\t"
    "v_fmac_f32 v121, v108, v72\n\t"
    "v_fmac_f32 v120, v109, v73\n\t"
    "v_fmac_f32 v121, v110, v74\n\t"
    "v_fmac_f32 v120, v111, v75\n\t"
    "v_fmac_f32 v121, v112, v76\n\t"
    "v_fmac_f32 v120, v113, v77\n\t"
    "v_fmac_f32 v121, v114, v78\n\t"
    "v_fmac_f32 v120, v115, v79\n\t"
    "v_fmac_f32 v121, v116, v80\n\t"
    "v_fmac_f32 v120, v117, v81\n\t"
    "v_fmac_f32 v121, v118, v82\n\t"
    "v_fmac_f32 v120, v119, v83\n\t"
    "v_add_f32 v154, v120, v121\n\t"
    "ds_write_b32 v147, v154\n\t"
    "v_lshrrev_b32 v147, 2, v146\n\t"
    "s_mov_b64 s[92:93], %17\n\t"
    "s_and_b32 s93, s93, 0xffff\n\t"
    "s_mov_b32 s94, 32768\n\t"
    "s_mov_b32 s95, 0x20000\n\t"
    "s_lshl_b32 s44, s37, 1\n\t"
    "v_and_b32 v155, 15, v147\n\t"
    "v_lshl_add_u32 v155, %16, 4, v155\n\t"
    "v_lshrrev_b32 v154, 4, v147\n\t"
    "v_mul_u32_u24 v122, 272, v155\n\t"
    "v_lshl_add_u32 v122, v154, 6, v122\n\t"
    "v_add_u32 v122, 130048, v122\n\t"
    "v_mul_u32_u24 v154, 2688, v154\n\t"
    "v_lshl_add_u32 v154, v155, 2, v154\n\t"
    "v_add_u32 v154, 152896, v154\n\t"
    "v_cmp_le_u32 vcc, 48, v147\n\t"
    "v_cmp_gt_u32 s[46:47], s38, v155\n\t"
    "v_lshlrev_b32 v155, 1, v155\n\t"
    "s_and_b64 vcc, vcc, s[46:47]\n\t"
    "s_waitcnt lgkmcnt(0)\n\t"
    "s_barrier\n\t"
    "s_cbranch_vccz L_fin_%=\n\t"
    "ds_read_b128 v[92:95], v122 offset:0\n\t"
    "ds_read_b128 v[96:99], v122 offset:16\n\t"
    "ds_read_b128 v[100:103], v122 offset:32\n\t"
    "ds_read_b128 v[104:107], v122 offset:48\n\t"
    "ds_read2_b32 v[108:109], v154 offset0:0 offset1:84\n\t"
    "ds_read2_b32 v[110:111], v154 offset0:168 offset1:252\n\t"
    "v_add_u32 v154, 1344, v154\n\t"
    "ds_read2_b32 v[112:113], v154 offset0:0 offset1:84\n\t"
    "ds_read2_b32 v[114:115], v154 offset0:168 offset1:252\n\t"
    "s_waitcnt lgkmcnt(6)\n\t"
    "v_add_f32 v92, v92, v96\n\t"
    "v_add_f32 v93, v93, v97\n\t"
    "v_add_f32 v94, v94, v98\n\t"
    "v_add_f32 v95, v95, v99\n\t"
    "s_waitcnt lgkmcnt(5)\n\t"
    "v_add_f32 v92, v92, v100\n\t"
    "v_add_f32 v93, v93, v101\n\t"
    "v_add_f32 v94, v94, v102\n\t"
    "v_add_f32 v95, v95, v103\n\t"
    "s_waitcnt lgkmcnt(4)\n\t"
    "v_add_f32 v92, v92, v104\n\t"
    "v_add_f32 v93, v93, v105\n\t"
    "v_add_f32 v94, v94, v106\n\t"
    "v_add_f32 v95, v95, v107\n\t"
    "s_waitcnt lgkmcnt(2)\n\t"
    "v_add_f32 v92, v92, v108\n\t"
    "v_add_f32 v93, v93, v109\n\t"
    "v_add_f32 v94, v94, v110\n\t"
    "v_add_f32 v95, v95, v111\n\t"
    "s_waitcnt lgkmcnt(0)\n\t"
    "v_add_f32 v92, v92, v112\n\t"
    "v_add_f32 v93, v93, v113\n\t"
    "v_add_f32 v94, v94, v114\n\t"
    "v_add_f32 v95, v95, v115\n\t"
    "v_add_f32 v92, v92, v93\n\t"
    "v_add_f32 v94, v94, v95\n\t"
    "v_add_f32 v92, v92, v94\n\t"
    "v_mov_b32 v93, v92\n\t"
    "s_nop 1\n\t"
    "v_permlane16_swap_b32 v93, v92\n\t"
    "s_nop 1\n\t"
    "v_add_f32 v92, v92, v93\n\t"
    "v_mov_b32 v93, v92\n\t"
    "s_nop 1\n\t"
    "v_permlane32_swap_b32 v93, v92\n\t"
    "s_nop 1\n\t"
    "v_add_f32 v92, v92, v93\n\t"
    "v_add_f32 v92, s39, v92\n\t"
    "v_cvt_pk_bf16_f32 v92, v92, v92\n\t"
    "s_mov_b64 exec, vcc\n\t"
    "buffer_store_short v92, v155, s[92:95], s44 offen\n\t"
    "s_mov_b64 exec, -1\n\t"
    "s_branch L_fin_%=\n\t"
    "L_req0_%=:\n\t"
    "s_cmp_lg_u32 s61, 0\n\t"
    "s_cbranch_scc1 L_req0b_%=\n\t"
    "s_bfe_u32 s65, s84, 0xc0002\n\t"
    "s_add_u32 s65, s65, s36\n\t"
    "buffer_store_dword v236, v125, %12, s65 offen offset:0 sc1\n\t"
    "buffer_store_dword v237, v125, %12, s65 offen offset:256 sc1\n\t"
    "s_bfe_u32 s65, s85, 0xc0002\n\t"
    "s_add_u32 s65, s65, s36\n\t"
    "buffer_store_dword v238, v125, %12, s65 offen offset:0 sc1\n\t"
    "buffer_store_dword v239, v125, %12, s65 offen offset:256 sc1\n\t"
    "s_bfe_u32 s65, s86, 0xc0002\n\t"
    "s_add_u32 s65, s65, s36\n\t"
    "buffer_store_dword v240, v125, %12, s65 offen offset:0 sc1\n\t"
    "buffer_store_dword v241, v125, %12, s65 offen offset:256 sc1\n\t"
    "s_bfe_u32 s65, s87, 0xc0002\n\t"
    "s_add_u32 s65, s65, s36\n\t"
    "buffer_store_dword v242, v125, %12, s65 offen offset:0 sc1\n\t"
    "buffer_store_dword v243, v125, %12, s65 offen offset:256 sc1\n\t"
    "s_bfe_u32 s65, s88, 0xc0002\n\t"
    "s_add_u32 s65, s65, s36\n\t"
    "buffer_store_dword v244, v125, %12, s65 offen offset:0 sc1\n\t"
    "buffer_store_dword v245, v125, %12, s65 offen offset:256 sc1\n\t"
    "s_bfe_u32 s65, s89, 0xc0002\n\t"
    "s_add_u32 s65, s65, s36\n\t"
    "buffer_store_dword v246, v125, %12, s65 offen offset:0 sc1\n\t"
    "buffer_store_dword v247, v125, %12, s65 offen offset:256 sc1\n\t"
    "s_bfe_u32 s65, s90, 0xc0002\n\t"
    "s_add_u32 s65, s65, s36\n\t"
    "buffer_store_dword v248, v125, %12, s65 offen offset:0 sc1\n\t"
    "buffer_store_dword v249, v125, %12, s65 offen offset:256 sc1\n\t"
    "s_bfe_u32 s65, s91, 0xc0002\n\t"
    "s_add_u32 s65, s65, s36\n\t"
    "buffer_store_dword v250, v125, %12, s65 offen offset:0 sc1\n\t"
    "buffer_store_dword v251, v125, %12, s65 offen offset:256 sc1\n\t"
    "s_sub_u32 s61, s64, 3\n\t"
    "s_branch L_back0_%=\n\t"
    "L_req0b_%=:\n\t"
    "s_cmp_lg_u32 s61, 1\n\t"
    "s_cbranch_scc1 L_req0c_%=\n\t"
    "v_lshrrev_b32 v154, 16, v122\n\t"
    "v_lshl_add_u32 v154, %16, 2, v154\n\t"
    "v_mul_u32_u24 v154, 336, v154\n\t"
    "v_and_b32 v122, 0xf0, v122\n\t"
    "v_cmp_gt_u32 vcc, 80, v122\n\t"
    "v_add3_u32 v122, v122, v154, s45\n\t"
    "s_mov_b32 s61, -1\n\t"
    "v_add_f32 v124, v220, v224\n\t"
    "v_add_f32 v154, v228, v232\n\t"
    "v_add_f32 v124, v124, v154\n\t"
    "v_add_f32 v125, v221, v225\n\t"
    "v_add_f32 v154, v229, v233\n\t"
    "v_add_f32 v125, v125, v154\n\t"
    "v_add_f32 v126, v222, v226\n\t"
    "v_add_f32 v154, v230, v234\n\t"
    "v_add_f32 v126, v126, v154\n\t"
    "v_add_f32 v127, v223, v227\n\t"
    "v_add_f32 v154, v231, v235\n\t"
    "v_add_f32 v127, v127, v154\n\t"
    "ds_write_b128 v122, v[124:127] offset:0\n\t"
    "s_cmp_le_u32 s38, 64\n\t"
    "s_cbranch_scc1 L_back0_%=\n\t"
    "v_add_f32 v124, v236, v240\n\t"
    "v_add_f32 v154, v244, v248\n\t"
    "v_add_f32 v124, v124, v154\n\t"
    "v_add_f32 v125, v237, v241\n\t"
    "v_add_f32 v154, v245, v249\n\t"
    "v_add_f32 v125, v125, v154\n\t"
    "v_add_f32 v126, v238, v242\n\t"
    "v_add_f32 v154, v246, v250\n\t"
    "v_add_f32 v126, v126, v154\n\t"
    "v_add_f32 v127, v239, v243\n\t"
    "v_add_f32 v154, v247, v251\n\t"
    "v_add_f32 v127, v127, v154\n\t"
    "s_and_saveexec_b64 s[46:47], vcc\n\t"
    "ds_write_b128 v122, v[124:127] offset:256\n\t"
    "s_mov_b64 exec, s[46:47]\n\t"
    "s_branch L_back0_%=\n\t"
    "L_req0c_%=:\n\t"
    "s_lshl_b32 s46, %14, 7\n\t"
    "s_add_u32 s46, s46, 49295360\n\t"
    "s_mov_b32 s47, 1048576\n\t"
    "s_mov_b64 exec, 1\n\t"
    "s_cmp_ge_u32 s55, s64\n\t"
    "s_cbranch_scc1 L_pq0_%=\n\t"
    "buffer_load_dword v123, off, %12, s46 sc1\n\t"
    "s_mov_b64 exec, -1\n\t"
    "s_branch L_back0_%=\n\t"
    "L_pq0_%=:\n\t"
    "v_cmp_eq_u32 vcc, 0, v123\n\t"
    "s_cbranch_vccz L_in0_%=\n\t"
    "L_poll0_%=:\n\t"
    "buffer_load_dword v155, off, %12, s46 sc1\n\t"
    "s_sub_u32 s47, s47, 1\n\t"
    "s_cmp_eq_u32 s47, 0\n\t"
    "s_cbranch_scc1 L_in0_%=\n\t"
    "s_waitcnt vmcnt(0)\n\t"
    "v_cmp_eq_u32 vcc, 0, v155\n\t"
    "s_cbranch_vccz L_in0_%=\n\t"
    "s_sleep 8\n\t"
    "s_branch L_poll0_%=\n\t"
    "L_in0_%=:\n\t"
    "s_mov_b64 exec, -1\n\t"
    "s_lshl_b32 s46, s37, 2\n\t"
    "s_mul_i32 s47, %16, 1048576\n\t"
    "s_add_u32 s46, s46, s47\n\t"
    "s_add_u32 s46, s46, 17825792\n\t"
    "v_and_b32 v155, 0x80, v122\n\t"
    "v_lshl_add_u32 v155, v155, 23, v122\n\t"
    "buffer_load_dwordx4 v[220:223], v122, %12, s46 offen sc1\n\t"
    "s_add_u32 s46, s46, 262144\n\t"
    "buffer_load_dwordx4 v[224:227], v122, %12, s46 offen sc1\n\t"
    "s_add_u32 s46, s46, 262144\n\t"
    "buffer_load_dwordx4 v[228:231], v122, %12, s46 offen sc1\n\t"
    "s_add_u32 s46, s46, 262144\n\t"
    "buffer_load_dwordx4 v[232:235], v122, %12, s46 offen sc1\n\t"
    "s_add_u32 s46, s46, 262144\n\t"
    "s_mov_b32 s61, 1\n\t"
    "s_cmp_le_u32 s38, 64\n\t"
    "s_cbranch_scc1 L_back0_%=\n\t"
    "s_sub_u32 s46, s46, 262144\n\t"
    "buffer_load_dwordx4 v[248:251], v155, %12, s46 offen offset:256 sc1\n\t"
    "s_sub_u32 s46, s46, 262144\n\t"
    "buffer_load_dwordx4 v[244:247], v155, %12, s46 offen offset:256 sc1\n\t"
    "s_sub_u32 s46, s46, 262144\n\t"
    "buffer_load_dwordx4 v[240:243], v155, %12, s46 offen offset:256 sc1\n\t"
    "s_sub_u32 s46, s46, 262144\n\t"
    "buffer_load_dwordx4 v[236:239], v155, %12, s46 offen offset:256 sc1\n\t"
    "s_branch L_back0_%=\n\t"
    "L_req1_%=:\n\t"
    "s_cmp_lg_u32 s61, 1\n\t"
    "s_cbranch_scc1 L_req1c_%=\n\t"
    "v_lshrrev_b32 v154, 16, v122\n\t"
    "v_lshl_add_u32 v154, %16, 2, v154\n\t"
    "v_mul_u32_u24 v154, 336, v154\n\t"
    "v_and_b32 v122, 0xf0, v122\n\t"
    "v_cmp_gt_u32 vcc, 80, v122\n\t"
    "v_add3_u32 v122, v122, v154, s45\n\t"
    "s_mov_b32 s61, -1\n\t"
    "v_add_f32 v124, v220, v224\n\t"
    "v_add_f32 v154, v228, v232\n\t"
    "v_add_f32 v124, v124, v154\n\t"
    "v_add_f32 v125, v221, v225\n\t"
    "v_add_f32 v154, v229, v233\n\t"
    "v_add_f32 v125, v125, v154\n\t"
    "v_add_f32 v126, v222, v226\n\t"
    "v_add_f32 v154, v230, v234\n\t"
    "v_add_f32 v126, v126, v154\n\t"
    "v_add_f32 v127, v223, v227\n\t"
    "v_add_f32 v154, v231, v235\n\t"
    "v_add_f32 v127, v127, v154\n\t"
    "ds_write_b128 v122, v[124:127] offset:0\n\t"
    "s_cmp_le_u32 s38, 64\n\t"
    "s_cbranch_scc1 L_back1_%=\n\t"
    "v_add_f32 v124, v236, v240\n\t"
    "v_add_f32 v154, v244, v248\n\t"
    "v_add_f32 v124, v124, v154\n\t"
    "v_add_f32 v125, v237, v241\n\t"
    "v_add_f32 v154, v245, v249\n\t"
    "v_add_f32 v125, v125, v154\n\t"
    "v_add_f32 v126, v238, v242\n\t"
    "v_add_f32 v154, v246, v250\n\t"
    "v_add_f32 v126, v126, v154\n\t"
    "v_add_f32 v127, v239, v243\n\t"
    "v_add_f32 v154, v247, v251\n\t"
    "v_add_f32 v127, v127, v154\n\t"
    "s_and_saveexec_b64 s[46:47], vcc\n\t"
    "ds_write_b128 v122, v[124:127] offset:256\n\t"
    "s_mov_b64 exec, s[46:47]\n\t"
    "s_branch L_back1_%=\n\t"
    "L_req1c_%=:\n\t"
    "s_lshl_b32 s46, %14, 7\n\t"
    "s_add_u32 s46, s46, 49295360\n\t"
    "s_mov_b32 s47, 1048576\n\t"
    "s_mov_b64 exec, 1\n\t"
    "s_cmp_ge_u32 s55, s64\n\t"
    "s_cbranch_scc1 L_pq1_%=\n\t"
    "buffer_load_dword v123, off, %12, s46 sc1\n\t"
    "s_mov_b64 exec, -1\n\t"
    "s_branch L_back1_%=\n\t"
    "L_pq1_%=:\n\t"
    "v_cmp_eq_u32 vcc, 0, v123\n\t"
    "s_cbranch_vccz L_in1_%=\n\t"
    "L_poll1_%=:\n\t"
    "buffer_load_dword v155, off, %12, s46 sc1\n\t"
    "s_sub_u32 s47, s47, 1\n\t"
    "s_cmp_eq_u32 s47, 0\n\t"
    "s_cbranch_scc1 L_in1_%=\n\t"
    "s_waitcnt vmcnt(0)\n\t"
    "v_cmp_eq_u32 vcc, 0, v155\n\t"
    "s_cbranch_vccz L_in1_%=\n\t"
    "s_sleep 8\n\t"
    "s_branch L_poll1_%=\n\t"
    "L_in1_%=:\n\t"
    "s_mov_b64 exec, -1\n\t"
    "s_lshl_b32 s46, s37, 2\n\t"
    "s_mul_i32 s47, %16, 1048576\n\t"
    "s_add_u32 s46, s46, s47\n\t"
    "s_add_u32 s46, s46, 17825792\n\t"
    "v_and_b32 v155, 0x80, v122\n\t"
    "v_lshl_add_u32 v155, v155, 23, v122\n\t"
    "buffer_load_dwordx4 v[220:223], v122, %12, s46 offen sc1\n\t"
    "s_add_u32 s46, s46, 262144\n\t"
    "buffer_load_dwordx4 v[224:227], v122, %12, s46 offen sc1\n\t"
    "s_add_u32 s46, s46, 262144\n\t"
    "buffer_load_dwordx4 v[228:231], v122, %12, s46 offen sc1\n\t"
    "s_add_u32 s46, s46, 262144\n\t"
    "buffer_load_dwordx4 v[232:235], v122, %12, s46 offen sc1\n\t"
    "s_add_u32 s46, s46, 262144\n\t"
    "s_mov_b32 s61, 1\n\t"
    "s_cmp_le_u32 s38, 64\n\t"
    "s_cbranch_scc1 L_back1_%=\n\t"
    "s_sub_u32 s46, s46, 262144\n\t"
    "buffer_load_dwordx4 v[248:251], v155, %12, s46 offen offset:256 sc1\n\t"
    "s_sub_u32 s46, s46, 262144\n\t"
    "buffer_load_dwordx4 v[244:247], v155, %12, s46 offen offset:256 sc1\n\t"
    "s_sub_u32 s46, s46, 262144\n\t"
    "buffer_load_dwordx4 v[240:243], v155, %12, s46 offen offset:256 sc1\n\t"
    "s_sub_u32 s46, s46, 262144\n\t"
    "buffer_load_dwordx4 v[236:239], v155, %12, s46 offen offset:256 sc1\n\t"
    "s_branch L_back1_%=\n\t"
    "L_sig_%=:\n\t"
    "v_subrev_u32 v155, s49, v149\n\t"
    "v_add_u32 v155, 1024, v155\n\t"
    "s_waitcnt vmcnt(0)\n\t"
    "ds_add_rtn_u32 v148, v155, v150\n\t"
    "s_mov_b32 s45, 152896\n\t"
    "s_mov_b32 s46, 49283072\n\t"
    "v_mov_b32 v155, 1\n\t"
    "s_waitcnt lgkmcnt(0)\n\t"
    "v_readfirstlane_b32 s47, v148\n\t"
    "s_nop 0\n\t"
    "s_cmp_lg_u32 s47, 7\n\t"
    "s_cbranch_scc1 L_sigb_%=\n\t"
    "s_mov_b64 exec, 1\n\t"
    "buffer_atomic_add v155, off, %12, s46 sc0 sc1\n\t"
    "s_mov_b64 exec, -1\n\t"
    "s_waitcnt vmcnt(0)\n\t"
    "v_readfirstlane_b32 s47, v155\n\t"
    "v_lshlrev_b32 v155, 5, v146\n\t"
    "s_cmp_lg_u32 s47, 255\n\t"
    "s_cbranch_scc1 L_sigb_%=\n\t"
    "s_mov_b32 s46, 49295360\n\t"
    "buffer_store_dword v150, v155, %12, s46 offen sc1\n\t"
    "s_add_u32 s46, s46, 0x2000\n\t"
    "buffer_store_dword v150, v155, %12, s46 offen sc1\n\t"
    "s_add_u32 s46, s46, 0x2000\n\t"
    "buffer_store_dword v150, v155, %12, s46 offen sc1\n\t"
    "s_add_u32 s46, s46, 0x2000\n\t"
    "buffer_store_dword v150, v155, %12, s46 offen sc1\n\t"
    "s_add_u32 s46, s46, 0x2000\n\t"
    "s_branch L_sigb_%=\n\t"
    "L_fin_%=:\n\t"
    "s_branch L_pend_%=\n\t"
    "L_pub_%=:\n\t"
    "s_memrealtime s[46:47]\n\t"
    "s_lshl_b32 s49, %14, 2\n\t"
    "s_add_u32 s49, s49, 49293312\n\t"
    "s_waitcnt lgkmcnt(0)\n\t"
    "s_sub_u32 s47, s46, s44\n\t"
    "s_mul_i32 s47, s47, 320\n\t"
    "s_lshr_b32 s47, s47, 8\n\t"
    "s_add_u32 s46, s46, s47\n\t"
    "s_or_b32 s46, s46, 1\n\t"
    "v_mov_b32 v83, s46\n\t"
    "s_mov_b64 exec, 1\n\t"
    "buffer_store_dword v83, off, %12, s49 sc1\n\t"
    "s_mov_b64 exec, -1\n\t"
    "s_branch L_pubb_%=\n\t"
    "L_part0_%=:\n\t"
    "s_mov_b32 s47, 0\n\t"
    "s_call_b64 s[98:99], L_bld_%=\n\t"
    "s_branch L_partb0_%=\n\t"
    "L_part1_%=:\n\t"
    "s_mov_b32 s47, 1048576\n\t"
    "s_waitcnt vmcnt(8)\n\t"
    "s_call_b64 s[98:99], L_bchk_%=\n\t"
    "s_branch L_partb1_%=\n\t"
    "L_bchk_%=:\n\t"
    "v_min_u32 v132, v128, v129\n\t"
    "v_min3_u32 v132, v132, v130, v131\n\t"
    "v_cmp_eq_u32 vcc, 0, v132\n\t"
    "s_cbranch_vccz L_bok_%=\n\t"
    "L_bld_%=:\n\t"
    "s_mov_b32 s46, 49293312\n\t"
    "buffer_load_dwordx4 v[128:131], v127, %12, s46 offen sc1\n\t"
    "s_cmp_eq_u32 s47, 0\n\t"
    "s_cbranch_scc1 L_bret_%=\n\t"
    "s_sub_u32 s47, s47, 1\n\t"
    "s_sleep 16\n\t"
    "s_waitcnt vmcnt(0)\n\t"
    "s_branch L_bchk_%=\n\t"
    "L_bok_%=:\n\t"
    "v_readfirstlane_b32 s46, v128\n\t"
    "v_mov_b32 v138, -4096\n\t"
    "v_mov_b32 v139, 4096\n\t"
    "v_subrev_u32 v128, s46, v128\n\t"
    "v_subrev_u32 v129, s46, v129\n\t"
    "v_subrev_u32 v130, s46, v130\n\t"
    "v_subrev_u32 v131, s46, v131\n\t"
    "v_writelane_b32 v128, 50, 0\n\t"
    "v_add3_u32 v132, v128, v129, v130\n\t"
    "v_add_u32 v132, v132, v131\n\t"
    "s_nop 1\n\t"
    "v_add_u32_dpp v132, v132, v132 row_shr:1 row_mask:0xf bank_mask:0xf\n\t"
    "s_nop 1\n\t"
    "v_add_u32_dpp v132, v132, v132 row_shr:2 row_mask:0xf bank_mask:0xf\n\t"
    "s_nop 1\n\t"
    "v_add_u32_dpp v132, v132, v132 row_shr:4 row_mask:0xf bank_mask:0xf\n\t"
    "s_nop 1\n\t"
    "v_add_u32_dpp v132, v132, v132 row_shr:8 row_mask:0xf bank_mask:0xf\n\t"
    "s_nop 1\n\t"
    "v_add_u32_dpp v132, v132, v132 row_bcast:15 row_mask:0xa bank_mask:0xf\n\t"
    "s_nop 1\n\t"
    "v_add_u32_dpp v132, v132, v132 row_bcast:31 row_mask:0xc bank_mask:0xf\n\t"
    "s_nop 0\n\t"
    "v_readlane_b32 s46, v132, 63\n\t"
    "s_movk_i32 s47, 20\n\t"
    "v_mov_b32 v133, -8192\n\t"
    "v_mov_b32 v134, 5120\n\t"
    "s_ashr_i32 s46, s46, 8\n\t"
    "v_sub_u32 v128, s46, v128\n\t"
    "v_med3_i32 v128, v128, v138, v139\n\t"
    "v_mul_i32_i24 v128, s47, v128\n\t"
    "v_med3_i32 v128, v128, v133, v134\n\t"
    "v_and_b32 v140, 0x3ff, v128\n\t"
    "v_ashrrev_i32 v128, 10, v128\n\t"
    "v_sub_u32 v129, s46, v129\n\t"
    "v_med3_i32 v129, v129, v138, v139\n\t"
    "v_mul_i32_i24 v129, s47, v129\n\t"
    "v_med3_i32 v129, v129, v133, v134\n\t"
    "v_and_b32 v141, 0x3ff, v129\n\t"
    "v_ashrrev_i32 v129, 10, v129\n\t"
    "v_sub_u32 v130, s46, v130\n\t"
    "v_med3_i32 v130, v130, v138, v139\n\t"
    "v_mul_i32_i24 v130, s47, v130\n\t"
    "v_med3_i32 v130, v130, v133, v134\n\t"
    "v_and_b32 v142, 0x3ff, v130\n\t"
    "v_ashrrev_i32 v130, 10, v130\n\t"
    "v_sub_u32 v131, s46, v131\n\t"
    "v_med3_i32 v131, v131, v138, v139\n\t"
    "v_mul_i32_i24 v131, s47, v131\n\t"
    "v_med3_i32 v131, v131, v133, v134\n\t"
    "v_and_b32 v143, 0x3ff, v131\n\t"
    "v_ashrrev_i32 v131, 10, v131\n\t"
    "v_add3_u32 v132, v128, v129, v130\n\t"
    "v_add_u32 v132, v132, v131\n\t"
    "s_nop 1\n\t"
    "v_add_u32_dpp v132, v132, v132 row_shr:1 row_mask:0xf bank_mask:0xf\n\t"
    "s_nop 1\n\t"
    "v_add_u32_dpp v132, v132, v132 row_shr:2 row_mask:0xf bank_mask:0xf\n\t"
    "s_nop 1\n\t"
    "v_add_u32_dpp v132, v132, v132 row_shr:4 row_mask:0xf bank_mask:0xf\n\t"
    "s_nop 1\n\t"
    "v_add_u32_dpp v132, v132, v132 row_shr:8 row_mask:0xf bank_mask:0xf\n\t"
    "s_nop 1\n\t"
    "v_add_u32_dpp v132, v132, v132 row_bcast:15 row_mask:0xa bank_mask:0xf\n\t"
    "s_nop 1\n\t"
    "v_add_u32_dpp v132, v132, v132 row_bcast:31 row_mask:0xc bank_mask:0xf\n\t"
    "s_nop 0\n\t"
    "v_readlane_b32 s46, v132, 63\n\t"
    "s_movk_i32 s47, 0x400\n\t"
    "s_sub_u32 s48, s47, 512\n\t"
    "s_mov_b32 s49, s46\n\t"
    "v_cmp_le_u32 vcc, s48, v140\n\t"
    "s_bcnt1_i32_b64 s44, vcc\n\t"
    "s_add_u32 s49, s49, s44\n\t"
    "v_cmp_le_u32 vcc, s48, v141\n\t"
    "s_bcnt1_i32_b64 s44, vcc\n\t"
    "s_add_u32 s49, s49, s44\n\t"
    "v_cmp_le_u32 vcc, s48, v142\n\t"
    "s_bcnt1_i32_b64 s44, vcc\n\t"
    "s_add_u32 s49, s49, s44\n\t"
    "v_cmp_le_u32 vcc, s48, v143\n\t"
    "s_bcnt1_i32_b64 s44, vcc\n\t"
    "s_add_u32 s49, s49, s44\n\t"
    "s_cmp_lt_i32 s49, 0\n\t"
    "s_cselect_b32 s47, s48, s47\n\t"
    "s_sub_u32 s48, s47, 256\n\t"
    "s_mov_b32 s49, s46\n\t"
    "v_cmp_le_u32 vcc, s48, v140\n\t"
    "s_bcnt1_i32_b64 s44, vcc\n\t"
    "s_add_u32 s49, s49, s44\n\t"
    "v_cmp_le_u32 vcc, s48, v141\n\t"
    "s_bcnt1_i32_b64 s44, vcc\n\t"
    "s_add_u32 s49, s49, s44\n\t"
    "v_cmp_le_u32 vcc, s48, v142\n\t"
    "s_bcnt1_i32_b64 s44, vcc\n\t"
    "s_add_u32 s49, s49, s44\n\t"
    "v_cmp_le_u32 vcc, s48, v143\n\t"
    "s_bcnt1_i32_b64 s44, vcc\n\t"
    "s_add_u32 s49, s49, s44\n\t"
    "s_cmp_lt_i32 s49, 0\n\t"
    "s_cselect_b32 s47, s48, s47\n\t"
    "s_sub_u32 s48, s47, 128\n\t"
    "s_mov_b32 s49, s46\n\t"
    "v_cmp_le_u32 vcc, s48, v140\n\t"
    "s_bcnt1_i32_b64 s44, vcc\n\t"
    "s_add_u32 s49, s49, s44\n\t"
    "v_cmp_le_u32 vcc, s48, v141\n\t"
    "s_bcnt1_i32_b64 s44, vcc\n\t"
    "s_add_u32 s49, s49, s44\n\t"
    "v_cmp_le_u32 vcc, s48, v142\n\t"
    "s_bcnt1_i32_b64 s44, vcc\n\t"
    "s_add_u32 s49, s49, s44\n\t"
    "v_cmp_le_u32 vcc, s48, v143\n\t"
    "s_bcnt1_i32_b64 s44, vcc\n\t"
    "s_add_u32 s49, s49, s44\n\t"
    "s_cmp_lt_i32 s49, 0\n\t"
    "s_cselect_b32 s47, s48, s47\n\t"
    "s_sub_u32 s48, s47, 64\n\t"
    "s_mov_b32 s49, s46\n\t"
    "v_cmp_le_u32 vcc, s48, v140\n\t"
    "s_bcnt1_i32_b64 s44, vcc\n\t"
    "s_add_u32 s49, s49, s44\n\t"
    "v_cmp_le_u32 vcc, s48, v141\n\t"
    "s_bcnt1_i32_b64 s44, vcc\n\t"
    "s_add_u32 s49, s49, s44\n\t"
    "v_cmp_le_u32 vcc, s48, v142\n\t"
    "s_bcnt1_i32_b64 s44, vcc\n\t"
    "s_add_u32 s49, s49, s44\n\t"
    "v_cmp_le_u32 vcc, s48, v143\n\t"
    "s_bcnt1_i32_b64 s44, vcc\n\t"
    "s_add_u32 s49, s49, s44\n\t"
    "s_cmp_lt_i32 s49, 0\n\t"
    "s_cselect_b32 s47, s48, s47\n\t"
    "s_sub_u32 s48, s47, 32\n\t"
    "s_mov_b32 s49, s46\n\t"
    "v_cmp_le_u32 vcc, s48, v140\n\t"
    "s_bcnt1_i32_b64 s44, vcc\n\t"
    "s_add_u32 s49, s49, s44\n\t"
    "v_cmp_le_u32 vcc, s48, v141\n\t"
    "s_bcnt1_i32_b64 s44, vcc\n\t"
    "s_add_u32 s49, s49, s44\n\t"
    "v_cmp_le_u32 vcc, s48, v142\n\t"
    "s_bcnt1_i32_b64 s44, vcc\n\t"
    "s_add_u32 s49, s49, s44\n\t"
    "v_cmp_le_u32 vcc, s48, v143\n\t"
    "s_bcnt1_i32_b64 s44, vcc\n\t"
    "s_add_u32 s49, s49, s44\n\t"
    "s_cmp_lt_i32 s49, 0\n\t"
    "s_cselect_b32 s47, s48, s47\n\t"
    "s_sub_u32 s47, s47, 32\n\t"
    "s_max_u32 s47, s47, 1\n\t"
    "v_cmp_le_u32 vcc, s47, v140\n\t"
    "s_nop 1\n\t"
    "v_addc_co_u32 v128, vcc, 0, v128, vcc\n\t"
    "v_cmp_le_u32 vcc, s47, v141\n\t"
    "s_nop 1\n\t"
    "v_addc_co_u32 v129, vcc, 0, v129, vcc\n\t"
    "v_cmp_le_u32 vcc, s47, v142\n\t"
    "s_nop 1\n\t"
    "v_addc_co_u32 v130, vcc, 0, v130, vcc\n\t"
    "v_cmp_le_u32 vcc, s47, v143\n\t"
    "s_nop 1\n\t"
    "v_addc_co_u32 v131, vcc, 0, v131, vcc\n\t"
    "v_add3_u32 v132, v128, v129, v130\n\t"
    "v_add_u32 v132, v132, v131\n\t"
    "s_nop 1\n\t"
    "v_add_u32_dpp v132, v132, v132 row_shr:1 row_mask:0xf bank_mask:0xf\n\t"
    "s_nop 1\n\t"
    "v_add_u32_dpp v132, v132, v132 row_shr:2 row_mask:0xf bank_mask:0xf\n\t"
    "s_nop 1\n\t"
    "v_add_u32_dpp v132, v132, v132 row_shr:4 row_mask:0xf bank_mask:0xf\n\t"
    "s_nop 1\n\t"
    "v_add_u32_dpp v132, v132, v132 row_shr:8 row_mask:0xf bank_mask:0xf\n\t"
    "s_nop 1\n\t"
    "v_add_u32_dpp v132, v132, v132 row_bcast:15 row_mask:0xa bank_mask:0xf\n\t"
    "s_nop 1\n\t"
    "v_add_u32_dpp v132, v132, v132 row_bcast:31 row_mask:0xc bank_mask:0xf\n\t"
    "s_nop 0\n\t"
    "v_readlane_b32 s46, v132, 63\n\t"
    "v_lshrrev_b32 v135, 2, v127\n\t"
    "s_cmp_le_u32 s46, 256\n\t"
    "s_cselect_b32 s47, -1, 0\n\t"
    "v_add_u32 v133, 4, v135\n\t"
    "v_mul_u32_u24 v134, s46, v133\n\t"
    "v_lshrrev_b32 v134, 8, v134\n\t"
    "v_sub_u32 v134, v132, v134\n\t"
    "v_and_b32 v134, s47, v134\n\t"
    "v_lshl_add_u32 v136, v133, 4, v134\n\t"
    "v_sub_u32 v132, v132, v131\n\t"
    "v_add_u32 v133, 3, v135\n\t"
    "v_mul_u32_u24 v134, s46, v133\n\t"
    "v_lshrrev_b32 v134, 8, v134\n\t"
    "v_sub_u32 v134, v132, v134\n\t"
    "v_and_b32 v134, s47, v134\n\t"
    "v_lshl_add_u32 v137, v133, 4, v134\n\t"
    "v_lshl_or_b32 v143, v136, 16, v137\n\t"
    "v_sub_u32 v132, v132, v130\n\t"
    "v_add_u32 v133, 2, v135\n\t"
    "v_mul_u32_u24 v134, s46, v133\n\t"
    "v_lshrrev_b32 v134, 8, v134\n\t"
    "v_sub_u32 v134, v132, v134\n\t"
    "v_and_b32 v134, s47, v134\n\t"
    "v_lshl_add_u32 v136, v133, 4, v134\n\t"
    "v_lshl_or_b32 v142, v137, 16, v136\n\t"
    "v_sub_u32 v132, v132, v129\n\t"
    "v_add_u32 v133, 1, v135\n\t"
    "v_mul_u32_u24 v134, s46, v133\n\t"
    "v_lshrrev_b32 v134, 8, v134\n\t"
    "v_sub_u32 v134, v132, v134\n\t"
    "v_and_b32 v134, s47, v134\n\t"
    "v_lshl_add_u32 v137, v133, 4, v134\n\t"
    "v_lshl_or_b32 v141, v136, 16, v137\n\t"
    "v_sub_u32 v132, v132, v128\n\t"
    "v_add_u32 v133, 0, v135\n\t"
    "v_mul_u32_u24 v134, s46, v133\n\t"
    "v_lshrrev_b32 v134, 8, v134\n\t"
    "v_sub_u32 v134, v132, v134\n\t"
    "v_and_b32 v134, s47, v134\n\t"
    "v_lshl_add_u32 v136, v133, 4, v134\n\t"
    "v_lshl_or_b32 v140, v137, 16, v136\n\t"
    "s_mov_b32 s46, 49294336\n\t"
    "buffer_store_dwordx4 v[140:143], v127, %12, s46 offen sc1\n\t"
    "s_mov_b32 s97, 1\n\t"
    "L_bret_%=:\n\t"
    "s_setpc_b64 s[98:99]\n\t"
    "L_rw_%=:\n\t"
    "s_mov_b32 s47, 1048576\n\t"
    "L_rw2_%=:\n\t"
    "s_sleep 16\n\t"
    "s_lshl_b32 s46, %14, 2\n\t"
    "s_add_u32 s46, s46, 49294336\n\t"
    "s_mov_b64 exec, 1\n\t"
    "buffer_load_dword v144, off, %12, s46 sc1\n\t"
    "s_mov_b64 exec, -1\n\t"
    "s_waitcnt vmcnt(0)\n\t"
    "v_readfirstlane_b32 s38, v144\n\t"
    "s_sub_u32 s47, s47, 1\n\t"
    "s_cmp_eq_u32 s47, 0\n\t"
    "s_cselect_b32 s38, 0x100000, s38\n\t"
    "s_cmp_eq_u32 s38, 0\n\t"
    "s_cbranch_scc1 L_rw2_%=\n\t"
    "s_branch L_rok_%=\n\t"
    "L_xw0_%=:\n\t"
    "s_mov_b32 s68, 1048576\n\t"
    "L_xv0_%=:\n\t"
    "s_sleep 4\n\t"
    "s_mov_b64 exec, 1\n\t"
    "buffer_load_dword v164, off, %12, s93 offset:0 sc1\n\t"
    "s_mov_b64 exec, -1\n\t"
    "s_sub_u32 s68, s68, 1\n\t"
    "s_cmp_eq_u32 s68, 0\n\t"
    "s_cbranch_scc1 L_xwb0_%=\n\t"
    "s_waitcnt vmcnt(0)\n\t"
    "v_readfirstlane_b32 s65, v164\n\t"
    "s_nop 0\n\t"
    "s_cmp_eq_u32 s65, 0\n\t"
    "s_cbranch_scc1 L_xv0_%=\n\t"
    "s_branch L_xwb0_%=\n\t"
    "L_xw1_%=:\n\t"
    "s_mov_b32 s68, 1048576\n\t"
    "L_xv1_%=:\n\t"
    "s_sleep 4\n\t"
    "s_mov_b64 exec, 1\n\t"
    "buffer_load_dword v164, off, %12, s93 offset:128 sc1\n\t"
    "s_mov_b64 exec, -1\n\t"
    "s_sub_u32 s68, s68, 1\n\t"
    "s_cmp_eq_u32 s68, 0\n\t"
    "s_cbranch_scc1 L_xwb1_%=\n\t"
    "s_waitcnt vmcnt(0)\n\t"
    "v_readfirstlane_b32 s65, v164\n\t"
    "s_nop 0\n\t"
    "s_cmp_eq_u32 s65, 0\n\t"
    "s_cbranch_scc1 L_xv1_%=\n\t"
    "s_branch L_xwb1_%=\n\t"
    "L_xw2_%=:\n\t"
    "s_mov_b32 s68, 1048576\n\t"
    "L_xv2_%=:\n\t"
    "s_sleep 4\n\t"
    "s_mov_b64 exec, 1\n\t"
    "buffer_load_dword v164, off, %12, s93 offset:256 sc1\n\t"
    "s_mov_b64 exec, -1\n\t"
    "s_sub_u32 s68, s68, 1\n\t"
    "s_cmp_eq_u32 s68, 0\n\t"
    "s_cbranch_scc1 L_xwb2_%=\n\t"
    "s_waitcnt vmcnt(0)\n\t"
    "v_readfirstlane_b32 s65, v164\n\t"
    "s_nop 0\n\t"
    "s_cmp_eq_u32 s65, 0\n\t"
    "s_cbranch_scc1 L_xv2_%=\n\t"
    "s_branch L_xwb2_%=\n\t"
    "L_xw3_%=:\n\t"
    "s_mov_b32 s68, 1048576\n\t"
    "L_xv3_%=:\n\t"
    "s_sleep 4\n\t"
    "s_mov_b64 exec, 1\n\t"
    "buffer_load_dword v164, off, %12, s93 offset:384 sc1\n\t"
    "s_mov_b64 exec, -1\n\t"
    "s_sub_u32 s68, s68, 1\n\t"
    "s_cmp_eq_u32 s68, 0\n\t"
    "s_cbranch_scc1 L_xwb3_%=\n\t"
    "s_waitcnt vmcnt(0)\n\t"
    "v_readfirstlane_b32 s65, v164\n\t"
    "s_nop 0\n\t"
    "s_cmp_eq_u32 s65, 0\n\t"
    "s_cbranch_scc1 L_xv3_%=\n\t"
    "s_branch L_xwb3_%=\n\t"
    "L_mp2_%=:\n\t"
    "v_lshrrev_b32 v57, 4, v127\n\t"
    "v_add_u32 v57, 32, v57\n\t"
    "s_and_b32 s65, %14, 7\n\t"
    "s_add_u32 s65, s65, 24\n\t"
    "v_writelane_b32 v57, s65, 16\n\t"
    "v_mov_b32 v58, 16\n\t"
    "v_lshlrev_b32 v57, 7, v57\n\t"
    "v_writelane_b32 v58, 32, 16\n\t"
    "s_mov_b32 s65, 49283072\n\t"
    "v_mov_b32 v56, 32\n\t"
    "s_mov_b32 s66, 1048576\n\t"
    "s_mov_b32 s65, 49283072\n\t"
    "s_mov_b64 exec, 0x1ffff\n\t"
    "buffer_load_dword v56, v57, %12, s65 offen sc1\n\t"
    "s_mov_b64 exec, -1\n\t"
    "s_branch L_mp2b_%=\n\t"
    "L_mp3_%=:\n\t"
    "v_cmp_gt_u32 vcc, v58, v56\n\t"
    "s_cbranch_vccz L_mp3b_%=\n\t"
    "s_sub_u32 s66, s66, 1\n\t"
    "s_cmp_eq_u32 s66, 0\n\t"
    "s_cbranch_scc1 L_mp3b_%=\n\t"
    "s_sleep 8\n\t"
    "s_mov_b32 s65, 49283072\n\t"
    "s_mov_b64 exec, 0x1ffff\n\t"
    "buffer_load_dword v56, v57, %12, s65 offen sc1\n\t"
    "s_mov_b64 exec, -1\n\t"
    "s_waitcnt vmcnt(0)\n\t"
    "s_branch L_mp3_%=\n\t"
    "L_pend_%=:\n\t"
    ".Ldcp_end:\n\t"
    :: "s"(s_idx), "s"(p_wp), "s"(p_w1), "n"(0), "n"(0), "n"(0), "s"(pb1), "s"(pw2), "s"(pb2), "s"(b2s),
       "n"(0),
       "s"((unsigned long long)ws & 0xFFFFFFFFFFFFull), "s"(s_ws), "n"(0), "s"(bid), "s"(s_tab), "s"(w), "s"(p_out),
       "s"((unsigned)(unsigned long long)idx), "s"((unsigned)((unsigned long long)idx >> 32) & 0xFFFFu)
    : "memory", "vcc", "scc", "m0", "s36", "s37", "s38", "s39", "s40", "s41", "s42", "s43", "s44", "s45", "s46", "s47", "s48", "s49", "s50", "s51", "s52", "s53", "s54", "s55", "s56", "s57", "s58", "s59", "s60", "s61", "s62", "s63", "s64", "s65", "s66", "s67", "s68", "s69", "s70", "s71", "s72", "s73", "s74", "s75", "s76", "s77", "s78", "s79", "s80", "s81", "s82", "s83", "s84", "s85", "s86", "s87", "s88", "s89", "s90", "s91", "s92", "s93", "s94", "s95", "s96", "s97", "s98", "s99", "v0", "v1", "v2", "v3", "v4", "v5", "v6", "v7", "v8", "v9", "v10", "v11", "v12", "v13", "v14", "v15", "v16", "v17", "v18", "v19", "v20", "v21", "v22", "v23", "v24", "v25", "v26", "v27", "v28", "v29", "v30", "v31", "v32", "v33", "v34", "v35", "v36", "v37", "v38", "v39", "v40", "v41", "v42", "v43", "v44", "v45", "v46", "v47", "v48", "v49", "v50", "v51", "v52", "v53", "v54", "v55", "v56", "v57", "v58", "v59", "v60", "v61", "v62", "v63", "v64", "v65", "v66", "v67", "v68", "v69", "v70", "v71", "v72", "v73", "v74", "v75", "v76", "v77", "v78", "v79", "v80", "v81", "v82", "v83", "v84", "v85", "v86", "v87", "v88", "v89", "v90", "v91", "v92", "v93", "v94", "v95", "v96", "v97", "v98", "v99", "v100", "v101", "v102", "v103", "v104", "v105", "v106", "v107", "v108", "v109", "v110", "v111", "v112", "v113", "v114", "v115", "v116", "v117", "v118", "v119", "v120", "v121", "v122", "v123", "v124", "v125", "v126", "v127", "v128", "v129", "v130", "v131", "v132", "v133", "v134", "v135", "v136", "v137", "v138", "v139", "v140", "v141", "v142", "v143", "v144", "v145", "v146", "v147", "v148", "v149", "v150", "v151", "v152", "v153", "v154", "v155", "v156", "v157", "v158", "v159", "v160", "v161", "v162", "v163", "v164", "v165", "v166", "v167", "v168", "v169", "v170", "v171", "v172", "v173", "v174", "v175", "v176", "v177", "v178", "v179", "v180", "v181", "v182", "v183", "v184", "v185", "v186", "v187", "v188", "v189", "v190", "v191", "v192", "v193", "v194", "v195", "v196", "v197", "v198", "v199", "v200", "v201", "v202", "v203", "v204", "v205", "v206", "v207", "v208", "v209", "v210", "v211", "v212", "v213", "v214", "v215", "v216", "v217", "v218", "v219", "v220", "v221", "v222", "v223", "v224", "v225", "v226", "v227", "v228", "v229", "v230", "v231", "v232", "v233", "v234", "v235", "v236", "v237", "v238", "v239", "v240", "v241", "v242", "v243", "v244", "v245", "v246", "v247", "v248", "v249", "v250", "v251", "v252", "v253", "v254", "v255");
}

extern "C" void kernel_launch(void* const* d_in, const int* in_sizes, int n_in, void* d_out, int out_size, void* d_ws,
                              size_t ws_size, hipStream_t stream) {
  const int* idx = (const int*)d_in[0];
  const unsigned short* table = (const unsigned short*)d_in[1];
  const unsigned short* w_p = (const unsigned short*)d_in[2];
  const unsigned short* w1 = (const unsigned short*)d_in[3];
  const unsigned short* b1 = (const unsigned short*)d_in[4];
  const unsigned short* w2 = (const unsigned short*)d_in[5];
  const unsigned short* b2 = (const unsigned short*)d_in[6];
  char* ws = (char*)d_ws;
  dcp_all<<<dim3(256), dim3(512), 0, stream>>>(idx, table, w_p, w1, b1, w2, b2, ws, (__hip_bfloat16*)d_out);
}
```

```cpp
#include <hip/hip_runtime.h>
#include <hip/hip_bf16.h>
#include <stdint.h>

typedef __attribute__((ext_vector_type(2))) unsigned u32x2;

constexpr int BATCH = 16384, NF = 100, DIM = 128, VOC = 1000, HID = 512;
constexpr size_t NWIN = 13;
constexpr size_t WS_IDX = 0;
constexpr size_t IDX_STREAM = 212992;
constexpr size_t WS_W1F = 3407872;
constexpr size_t W1F_SLAB = 102400;
constexpr size_t WS_TF = 16515072;
constexpr size_t WS_WT = 16777216;
constexpr size_t WS_PART = 17825792;
constexpr size_t WS_P0 = 27262976;
constexpr size_t WS_XCH = 31457280;
constexpr size_t XCH_PITCH = 69632;
constexpr size_t WS_CNT = 49283072;
constexpr size_t WS_CH = 49291264;
constexpr size_t WS_RP = 49292288;
constexpr size_t WS_TS = 49293312;
constexpr size_t WS_RNG = 49294336;
constexpr size_t WS_RDY = 49295360;
constexpr size_t WS_DBG = 49328128;
constexpr size_t WS_END = 49721344;

__device__ __forceinline__ __amdgpu_buffer_rsrc_t mk_srd(const void* p, unsigned bytes) {
  return __builtin_amdgcn_make_buffer_rsrc((void*)p, (short)0, (int)bytes, 0x00020000);
}

__global__ __launch_bounds__(512, 2) void dcp_all(const int* __restrict__ idx, const unsigned short* __restrict__ table,
                                                  const unsigned short* __restrict__ w_p, const unsigned short* __restrict__ w1,
                                                  const unsigned short* __restrict__ b1, const unsigned short* __restrict__ w2,
                                                  const unsigned short* __restrict__ b2, char* __restrict__ ws,
                                                  __hip_bfloat16* __restrict__ out) {
  __shared__ __attribute__((aligned(1024))) char smem[163840];
  const int w = __builtin_amdgcn_readfirstlane(threadIdx.x >> 6) + (int)(uintptr_t)smem;
  const int bid = blockIdx.x;
  asm volatile(
    "s_getpc_b64 s[36:37]\n\t"
    ".Ldcp_pc:\n\t"
    "s_bfe_u32 s38, %0, 0x40003\n\t"
    "s_lshl_b32 s38, s38, 13\n\t"
    "v_and_b32 v122, 63, %1\n\t"
    "v_lshlrev_b32 v122, 7, v122\n\t"
    "v_add_u32 v122, s38, v122\n\t"
    "v_min_u32 v122, .Ldcp_end-.Ldcp_pc-4, v122\n\t"
    "s_cmp_lg_u32 %2, 3\n\t"
    "s_cbranch_scc1 L_nopf_%=\n\t"
    "global_load_dword v123, v122, s[36:37]\n\t"
    "L_nopf_%=:\n\t"
    :: "s"(bid), "v"(threadIdx.x), "s"(w) : "memory", "scc", "s36", "s37", "s38", "s74", "s75", "v122", "v123");
  const int xcd = bid & 7, loc = bid >> 3;
  const int cg = xcd * 16 + (loc >> 1);
  const __amdgpu_buffer_rsrc_t s_idx = mk_srd(idx + (size_t)bid * 64 * NF, (unsigned)((BATCH - bid * 64) * NF * 4));
  const __amdgpu_buffer_rsrc_t s_tab = mk_srd(table, VOC * DIM * 2);
  const __amdgpu_buffer_rsrc_t s_ws = mk_srd(ws, (unsigned)WS_END);
  const unsigned short* pb1 = b1 + 4 * cg;
  const unsigned short* pw2 = w2 + 4 * cg;
  const unsigned long long pb2 = (unsigned long long)b2 & ~3ull;
  const unsigned b2s = ((unsigned)(unsigned long long)b2 & 2u) * 8u;
  const unsigned long long p_wp = (unsigned long long)w_p & 0xFFFFFFFFFFFFull, p_w1 = (unsigned long long)w1 & 0xFFFFFFFFFFFFull;
  const unsigned long long p_out = (unsigned long long)out & 0xFFFFFFFFFFFFull;
  asm volatile(
    "v_mbcnt_lo_u32_b32 v147, -1, 0\n\t"
    "v_mbcnt_hi_u32_b32 v147, -1, v147\n\t"
    "s_mov_b64 s[92:93], %1\n\t"
    "s_mov_b32 s94, 20000\n\t"
    "s_mov_b32 s95, 0x20000\n\t"
    "s_lshr_b32 s46, %14, 3\n\t"
    "s_lshl_b32 s51, s46, 2\n\t"
    "v_lshlrev_b32 v122, 4, v147\n\t"
    "s_cmp_ge_u32 %16, 2\n\t"
    "s_cbranch_scc1 L_aimg_%=\n\t"
    "s_lshl_b32 s44, %16, 10\n\t"
    "s_add_u32 m0, s44, 140288\n\t"
    "s_nop 0\n\t"
    "buffer_load_dwordx4 v122, s[92:95], s44 offen lds\n\t"
    "s_add_u32 s44, s44, 0x800\n\t"
    "s_add_u32 m0, s44, 140288\n\t"
    "s_nop 0\n\t"
    "buffer_load_dwordx4 v122, s[92:95], s44 offen lds\n\t"
    "s_add_u32 s44, s44, 0x800\n\t"
    "s_add_u32 m0, s44, 140288\n\t"
    "s_nop 0\n\t"
    "buffer_load_dwordx4 v122, s[92:95], s44 offen lds\n\t"
    "s_add_u32 s44, s44, 0x800\n\t"
    "s_add_u32 m0, s44, 140288\n\t"
    "s_nop 0\n\t"
    "buffer_load_dwordx4 v122, s[92:95], s44 offen lds\n\t"
    "s_add_u32 s44, s44, 0x800\n\t"
    "s_add_u32 m0, s44, 140288\n\t"
    "s_nop 0\n\t"
    "buffer_load_dwordx4 v122, s[92:95], s44 offen lds\n\t"
    "s_add_u32 s44, s44, 0x800\n\t"
    "s_add_u32 m0, s44, 140288\n\t"
    "s_nop 0\n\t"
    "buffer_load_dwordx4 v122, s[92:95], s44 offen lds\n\t"
    "s_add_u32 s44, s44, 0x800\n\t"
    "s_add_u32 m0, s44, 140288\n\t"
    "s_nop 0\n\t"
    "buffer_load_dwordx4 v122, s[92:95], s44 offen lds\n\t"
    "s_add_u32 s44, s44, 0x800\n\t"
    "s_add_u32 m0, s44, 140288\n\t"
    "s_nop 0\n\t"
    "buffer_load_dwordx4 v122, s[92:95], s44 offen lds\n\t"
    "s_add_u32 s44, s44, 0x800\n\t"
    "s_add_u32 m0, s44, 140288\n\t"
    "s_nop 0\n\t"
    "buffer_load_dwordx4 v122, s[92:95], s44 offen lds\n\t"
    "s_add_u32 s44, s44, 0x800\n\t"
    "s_add_u32 m0, s44, 140288\n\t"
    "s_nop 0\n\t"
    "buffer_load_dwordx4 v122, s[92:95], s44 offen lds\n\t"
    "s_add_u32 s44, s44, 0x800\n\t"
    "s_mov_b32 s98, 13107200\n\t"
    "s_mov_b32 s99, 0x20000\n\t"
    "s_cmp_eq_u32 %16, 0\n\t"
    "s_cbranch_scc1 L_aside_%=\n\t"
    "s_branch L_ahelp_%=\n\t"
    "L_aimg_%=:\n\t"
    "s_cmp_lg_u32 %16, 2\n\t"
    "s_cbranch_scc1 L_anoinit_%=\n\t"
    "v_mov_b32 v0, 0\n\t"
    "v_mov_b32 v1, 0\n\t"
    "v_mov_b32 v2, 0\n\t"
    "v_mov_b32 v3, 0\n\t"
    "v_lshlrev_b32 v8, 4, v147\n\t"
    "v_add_u32 v8, 130048, v8\n\t"
    "v_mov_b32 v9, 1024\n\t"
    "ds_write_b128 v9, v[0:3]\n\t"
    "ds_write_b128 v8, v[0:3] offset:0\n\t"
    "ds_write_b128 v8, v[0:3] offset:1024\n\t"
    "ds_write_b128 v8, v[0:3] offset:2048\n\t"
    "ds_write_b32 v9, v0 offset:2084\n\t"
    "ds_write_b32 v9, v0 offset:2088\n\t"
    "ds_write_b32 v9, v0 offset:1044\n\t"
    "ds_write_b32 v9, v0 offset:1052\n\t"
    "v_mov_b32 v4, 3\n\t"
    "ds_write_b32 v9, v4 offset:1040\n\t"
    "v_mov_b32 v5, 16\n\t"
    "ds_write_b32 v9, v5 offset:2080\n\t"
    "v_mov_b32 v6, 32\n\t"
    "ds_write_b32 v9, v6 offset:3120\n\t"
    "v_mov_b32 v7, 51\n\t"
    "ds_write_b32 v9, v7 offset:4160\n\t"
    "L_anoinit_%=:\n\t"
    "s_and_b32 s46, %16, 3\n\t"
    "s_mul_i32 s49, s46, 1040\n\t"
    "s_add_u32 s49, s49, 2064\n\t"
    "s_lshl_b32 s46, s46, 3\n\t"
    "s_lshr_b32 s47, 1076697610, s46\n\t"
    "s_and_b32 s47, s47, 0xff\n\t"
    "s_lshl_b32 s48, s47, 16\n\t"
    "s_lshr_b32 s47, 756681216, s46\n\t"
    "s_and_b32 s47, s47, 0xff\n\t"
    "s_add_u32 s48, s48, s47\n\t"
    "s_lshr_b32 s47, %16, 2\n\t"
    "s_add_u32 s47, s47, 0x200\n\t"
    "s_cmp_eq_u32 s46, 0\n\t"
    "s_cselect_b32 s47, 0x100, s47\n\t"
    "s_add_u32 s48, s48, s47\n\t"
    "v_lshrrev_b32 v126, 3, v147\n\t"
    "v_and_b32 v154, 7, v147\n\t"
    "v_lshlrev_b32 v126, 8, v126\n\t"
    "v_lshl_add_u32 v126, v154, 4, v126\n\t"
    "v_lshlrev_b32 v117, 20, v147\n\t"
    "v_add_u32 v117, 1024, v117\n\t"
    "v_mov_b32 v118, 1\n\t"
    "s_and_b32 s46, %16, 6\n\t"
    "s_cmp_eq_u32 s46, 4\n\t"
    "s_cbranch_scc1 L_adma_%=\n\t"
    "s_lshr_b32 s61, %16, 2\n\t"
    "s_lshl_b32 s61, s61, 1\n\t"
    "s_and_b32 s46, %16, 1\n\t"
    "s_add_u32 s61, s61, s46\n\t"
    "s_add_u32 s46, s61, 102\n\t"
    "s_add_u32 s69, s51, s46\n\t"
    "s_sub_u32 s44, s69, 125\n\t"
    "s_cmp_ge_u32 s69, 125\n\t"
    "s_cselect_b32 s69, s44, s69\n\t"
    "s_mul_i32 m0, s69, 1040\n\t"
    "s_lshl_b32 s44, s69, 11\n\t"
    "buffer_load_dwordx4 v126, %15, s44 offen lds\n\t"
    "s_add_u32 s69, s69, 4\n\t"
    "s_sub_u32 s44, s69, 125\n\t"
    "s_cmp_ge_u32 s69, 125\n\t"
    "s_cselect_b32 s69, s44, s69\n\t"
    "s_mul_i32 m0, s69, 1040\n\t"
    "s_lshl_b32 s44, s69, 11\n\t"
    "buffer_load_dwordx4 v126, %15, s44 offen lds\n\t"
    "s_add_u32 s69, s69, 4\n\t"
    "s_sub_u32 s44, s69, 125\n\t"
    "s_cmp_ge_u32 s69, 125\n\t"
    "s_cselect_b32 s69, s44, s69\n\t"
    "s_waitcnt lgkmcnt(0)\n\t"
    "s_barrier\n\t"
    "s_load_dwordx2 s[72:73], %6, 0\n\t"
    "s_load_dwordx2 s[88:89], %7, 0\n\t"
    "s_load_dword s91, %8, 0\n\t"
    "s_mul_i32 m0, s69, 1040\n\t"
    "s_lshl_b32 s44, s69, 11\n\t"
    "buffer_load_dwordx4 v126, %15, s44 offen lds\n\t"
    "s_add_u32 s69, s69, 4\n\t"
    "s_sub_u32 s44, s69, 125\n\t"
    "s_cmp_ge_u32 s69, 125\n\t"
    "s_cselect_b32 s69, s44, s69\n\t"
    "s_mul_i32 m0, s69, 1040\n\t"
    "s_lshl_b32 s44, s69, 11\n\t"
    "buffer_load_dwordx4 v126, %15, s44 offen lds\n\t"
    "s_add_u32 s69, s69, 4\n\t"
    "s_sub_u32 s44, s69, 125\n\t"
    "s_cmp_ge_u32 s69, 125\n\t"
    "s_cselect_b32 s69, s44, s69\n\t"
    "s_mul_i32 m0, s69, 1040\n\t"
    "s_lshl_b32 s44, s69, 11\n\t"
    "buffer_load_dwordx4 v126, %15, s44 offen lds\n\t"
    "s_add_u32 s69, s69, 4\n\t"
    "s_sub_u32 s44, s69, 125\n\t"
    "s_cmp_ge_u32 s69, 125\n\t"
    "s_cselect_b32 s69, s44, s69\n\t"
    "s_mul_i32 m0, s69, 1040\n\t"
    "s_lshl_b32 s44, s69, 11\n\t"
    "buffer_load_dwordx4 v126, %15, s44 offen lds\n\t"
    "s_add_u32 s69, s69, 4\n\t"
    "s_sub_u32 s44, s69, 125\n\t"
    "s_cmp_ge_u32 s69, 125\n\t"
    "s_cselect_b32 s69, s44, s69\n\t"
    "v_bfe_u32 v154, v147, 2, 2\n\t"
    "v_and_b32 v155, 3, v147\n\t"
    "v_mul_u32_u24 v154, 25, v154\n\t"
    "v_mad_u32_u24 v154, v155, 6, v154\n\t"
    "v_lshlrev_b32 v153, 2, v154\n\t"
    "s_and_b32 s53, s48, 0xff\n\t"
    "s_bfe_u32 s55, s48, 0x80008\n\t"
    "s_add_u32 s54, s53, s55\n\t"
    "s_add_u32 s55, s54, s55\n\t"
    "s_lshr_b32 s64, s48, 16\n\t"
    "v_cmp_eq_u32 s[62:63], 0, v155\n\t"
    "v_and_b32 v151, 48, v147\n\t"
    "v_or_b32 v155, 0x100000, v151\n\t"
    "v_cndmask_b32 v152, v155, v151, s[62:63]\n\t"
    "v_lshrrev_b32 v94, 4, v147\n\t"
    "v_mul_u32_u24 v92, 0x1388, v94\n\t"
    "v_lshl_add_u32 v92, v154, 1, v92\n\t"
    "v_add_u32 v92, 140288, v92\n\t"
    "v_mul_u32_u24 v93, 200, v154\n\t"
    "v_mad_u32_u24 v93, v94, 50, v93\n\t"
    "v_add_u32 v93, 140288, v93\n\t"
    "v_add_u32 v95, 140288, v122\n\t"
    "v_mov_b32 v0, 0\n\t"
    "v_mov_b32 v28, 0\n\t"
    "v_mov_b32 v1, 0\n\t"
    "v_mov_b32 v29, 0\n\t"
    "v_mov_b32 v2, 0\n\t"
    "v_mov_b32 v30, 0\n\t"
    "v_mov_b32 v3, 0\n\t"
    "v_mov_b32 v31, 0\n\t"
    "v_mov_b32 v4, 0\n\t"
    "v_mov_b32 v32, 0\n\t"
    "v_mov_b32 v5, 0\n\t"
    "v_mov_b32 v33, 0\n\t"
    "v_mov_b32 v6, 0\n\t"
    "v_mov_b32 v34, 0\n\t"
    "v_mov_b32 v7, 0\n\t"
    "v_mov_b32 v35, 0\n\t"
    "v_mov_b32 v8, 0\n\t"
    "v_mov_b32 v36, 0\n\t"
    "v_mov_b32 v9, 0\n\t"
    "v_mov_b32 v37, 0\n\t"
    "v_mov_b32 v10, 0\n\t"
    "v_mov_b32 v38, 0\n\t"
    "v_mov_b32 v11, 0\n\t"
    "v_mov_b32 v39, 0\n\t"
    "v_mov_b32 v12, 0\n\t"
    "v_mov_b32 v40, 0\n\t"
    "v_mov_b32 v13, 0\n\t"
    "v_mov_b32 v41, 0\n\t"
    "v_mov_b32 v14, 0\n\t"
    "v_mov_b32 v42, 0\n\t"
    "v_mov_b32 v15, 0\n\t"
    "v_mov_b32 v43, 0\n\t"
    "v_mov_b32 v16, 0\n\t"
    "v_mov_b32 v44, 0\n\t"
    "v_mov_b32 v17, 0\n\t"
    "v_mov_b32 v45, 0\n\t"
    "v_mov_b32 v18, 0\n\t"
    "v_mov_b32 v46, 0\n\t"
    "v_mov_b32 v19, 0\n\t"
    "v_mov_b32 v47, 0\n\t"
    "v_mov_b32 v20, 0\n\t"
    "v_mov_b32 v48, 0\n\t"
    "v_mov_b32 v21, 0\n\t"
    "v_mov_b32 v49, 0\n\t"
    "v_mov_b32 v22, 0\n\t"
    "v_mov_b32 v50, 0\n\t"
    "v_mov_b32 v23, 0\n\t"
    "v_mov_b32 v51, 0\n\t"
    "s_cmp_eq_u32 s61, 1\n\t"
    "s_cbranch_scc1 L_afold1_%=\n\t"
    "s_cmp_eq_u32 s61, 2\n\t"
    "s_cbranch_scc1 L_afold2_%=\n\t"
    "s_cmp_eq_u32 s61, 3\n\t"
    "s_cbranch_scc1 L_afold3_%=\n\t"
    "L_afold0_%=:\n\t"
    "L_asp0_%=:\n\t"
    "ds_read_b32 v119, v117 offset:1044\n\t"
    "s_waitcnt lgkmcnt(0)\n\t"
    "v_readfirstlane_b32 s46, v119\n\t"
    "s_nop 0\n\t"
    "s_cmp_ge_u32 s46, 2\n\t"
    "s_cbranch_scc1 L_ago0_%=\n\t"
    "s_sleep 1\n\t"
    "s_branch L_asp0_%=\n\t"
    "L_ago0_%=:\n\t"
    "ds_read_u16_d16_hi v0, v92 offset:208\n\t"
    "ds_read_u16_d16_hi v28, v93 offset:802\n\t"
    "ds_read_u16_d16_hi v1, v92 offset:1408\n\t"
    "ds_read_u16_d16_hi v29, v93 offset:814\n\t"
    "ds_read_u16_d16_hi v2, v92 offset:2608\n\t"
    "ds_read_u16_d16_hi v30, v93 offset:826\n\t"
    "ds_read_u16_d16_hi v3, v92 offset:3808\n\t"
    "ds_read_u16_d16_hi v31, v93 offset:838\n\t"
    "ds_read_u16_d16_hi v4, v92 offset:410\n\t"
    "ds_read_u16_d16_hi v32, v93 offset:1004\n\t"
    "ds_read_u16_d16_hi v5, v92 offset:1610\n\t"
    "ds_read_u16_d16_hi v33, v93 offset:1016\n\t"
    "ds_read_u16_d16_hi v6, v92 offset:2810\n\t"
    "ds_read_u16_d16_hi v34, v93 offset:1028\n\t"
    "ds_read_u16_d16_hi v7, v92 offset:4010\n\t"
    "ds_read_u16_d16_hi v35, v93 offset:1040\n\t"
    "ds_read_u16_d16_hi v8, v92 offset:612\n\t"
    "ds_read_u16_d16_hi v36, v93 offset:1206\n\t"
    "ds_read_u16_d16_hi v9, v92 offset:1812\n\t"
    "ds_read_u16_d16_hi v37, v93 offset:1218\n\t"
    "ds_read_u16_d16_hi v10, v92 offset:3012\n\t"
    "ds_read_u16_d16_hi v38, v93 offset:1230\n\t"
    "ds_read_u16_d16_hi v11, v92 offset:4212\n\t"
    "ds_read_u16_d16_hi v39, v93 offset:1242\n\t"
    "ds_read_u16_d16_hi v12, v92 offset:0\n\t"
    "ds_read_u16_d16_hi v13, v92 offset:2\n\t"
    "ds_read_u16_d16_hi v41, v93 offset:200\n\t"
    "ds_read_u16_d16_hi v14, v92 offset:4\n\t"
    "ds_read_u16_d16_hi v42, v93 offset:400\n\t"
    "ds_read_u16_d16_hi v15, v92 offset:6\n\t"
    "ds_read_u16_d16_hi v43, v93 offset:600\n\t"
    "ds_read_u16_d16_hi v16, v92 offset:8\n\t"
    "ds_read_u16_d16_hi v44, v93 offset:800\n\t"
    "ds_read_u16_d16_hi v17, v92 offset:10\n\t"
    "ds_read_u16_d16_hi v45, v93 offset:1000\n\t"
    "ds_read_u16_d16_hi v18, v92 offset:12\n\t"
    "ds_read_u16_d16_hi v46, v93 offset:1200\n\t"
    "ds_add_u32 v117, v118 offset:0\n\t"
    "s_waitcnt lgkmcnt(15)\n\t"
    "v_add_f32 v0, v0, v28\n\t"
    "v_add_f32 v1, v1, v29\n\t"
    "v_add_f32 v2, v2, v30\n\t"
    "v_add_f32 v3, v3, v31\n\t"
    "v_add_f32 v4, v4, v32\n\t"
    "v_add_f32 v5, v5, v33\n\t"
    "v_add_f32 v6, v6, v34\n\t"
    "v_add_f32 v7, v7, v35\n\t"
    "s_waitcnt lgkmcnt(14)\n\t"
    "v_add_f32 v8, v8, v36\n\t"
    "v_add_f32 v9, v9, v37\n\t"
    "v_add_f32 v10, v10, v38\n\t"
    "v_add_f32 v11, v11, v39\n\t"
    "s_waitcnt lgkmcnt(7)\n\t"
    "v_cndmask_b32 v12, 0, v12, s[62:63]\n\t"
    "v_add_f32 v13, v13, v41\n\t"
    "v_add_f32 v14, v14, v42\n\t"
    "v_add_f32 v15, v15, v43\n\t"
    "s_waitcnt lgkmcnt(1)\n\t"
    "v_add_f32 v16, v16, v44\n\t"
    "v_add_f32 v17, v17, v45\n\t"
    "v_add_f32 v18, v18, v46\n\t"
    "L_asp1_%=:\n\t"
    "ds_read_b32 v119, v117 offset:0\n\t"
    "s_waitcnt lgkmcnt(0)\n\t"
    "v_readfirstlane_b32 s46, v119\n\t"
    "s_nop 0\n\t"
    "s_cmp_ge_u32 s46, 4\n\t"
    "s_cbranch_scc1 L_ago1_%=\n\t"
    "s_sleep 1\n\t"
    "s_branch L_asp1_%=\n\t"
    "L_ago1_%=:\n\t"
    "ds_write_b128 v95, v[0:3] offset:6144\n\t"
    "ds_write_b128 v95, v[4:7] offset:10240\n\t"
    "ds_write_b128 v95, v[8:11] offset:14336\n\t"
    "ds_write_b128 v95, v[12:15] offset:21504\n\t"
    "ds_write_b128 v95, v[16:19] offset:22528\n\t"
    "ds_add_u32 v117, v118 offset:4\n\t"
    "s_branch L_ab0_%=\n\t"
    "L_afold1_%=:\n\t"
    "L_asp2_%=:\n\t"
    "ds_read_b32 v119, v117 offset:1044\n\t"
    "s_waitcnt lgkmcnt(0)\n\t"
    "v_readfirstlane_b32 s46, v119\n\t"
    "s_nop 0\n\t"
    "s_cmp_ge_u32 s46, 2\n\t"
    "s_cbranch_scc1 L_ago2_%=\n\t"
    "s_sleep 1\n\t"
    "s_branch L_asp2_%=\n\t"
    "L_ago2_%=:\n\t"
    "ds_read_u16_d16_hi v0, v92 offset:204\n\t"
    "ds_read_u16_d16_hi v28, v93 offset:402\n\t"
    "ds_read_u16_d16_hi v1, v92 offset:1404\n\t"
    "ds_read_u16_d16_hi v29, v93 offset:414\n\t"
    "ds_read_u16_d16_hi v2, v92 offset:2604\n\t"
    "ds_read_u16_d16_hi v30, v93 offset:426\n\t"
    "ds_read_u16_d16_hi v3, v92 offset:3804\n\t"
    "ds_read_u16_d16_hi v31, v93 offset:438\n\t"
    "ds_read_u16_d16_hi v4, v92 offset:408\n\t"
    "ds_read_u16_d16_hi v32, v93 offset:804\n\t"
    "ds_read_u16_d16_hi v5, v92 offset:1608\n\t"
    "ds_read_u16_d16_hi v33, v93 offset:816\n\t"
    "ds_read_u16_d16_hi v6, v92 offset:2808\n\t"
    "ds_read_u16_d16_hi v34, v93 offset:828\n\t"
    "ds_read_u16_d16_hi v7, v92 offset:4008\n\t"
    "ds_read_u16_d16_hi v35, v93 offset:840\n\t"
    "ds_read_u16_d16_hi v8, v92 offset:412\n\t"
    "ds_read_u16_d16_hi v36, v93 offset:1204\n\t"
    "ds_read_u16_d16_hi v9, v92 offset:1612\n\t"
    "ds_read_u16_d16_hi v37, v93 offset:1216\n\t"
    "ds_read_u16_d16_hi v10, v92 offset:2812\n\t"
    "ds_read_u16_d16_hi v38, v93 offset:1228\n\t"
    "ds_read_u16_d16_hi v11, v92 offset:4012\n\t"
    "ds_read_u16_d16_hi v39, v93 offset:1240\n\t"
    "ds_read_u16_d16_hi v12, v92 offset:810\n\t"
    "ds_read_u16_d16_hi v40, v93 offset:1008\n\t"
    "ds_read_u16_d16_hi v13, v92 offset:2010\n\t"
    "ds_read_u16_d16_hi v41, v93 offset:1020\n\t"
    "ds_read_u16_d16_hi v14, v92 offset:3210\n\t"
    "ds_read_u16_d16_hi v42, v93 offset:1032\n\t"
    "ds_read_u16_d16_hi v15, v92 offset:4410\n\t"
    "ds_read_u16_d16_hi v43, v93 offset:1044\n\t"
    "ds_read_u16_d16_hi v16, v92 offset:202\n\t"
    "ds_read_u16_d16_hi v17, v92 offset:1402\n\t"
    "ds_read_u16_d16_hi v18, v92 offset:2602\n\t"
    "ds_read_u16_d16_hi v19, v92 offset:3802\n\t"
    "ds_read_u16_d16_hi v20, v92 offset:808\n\t"
    "ds_read_u16_d16_hi v21, v92 offset:2008\n\t"
    "ds_read_u16_d16_hi v22, v92 offset:3208\n\t"
    "ds_read_u16_d16_hi v23, v92 offset:4408\n\t"
    "ds_add_u32 v117, v118 offset:0\n\t"
    "s_waitcnt lgkmcnt(15)\n\t"
    "v_add_f32 v0, v0, v28\n\t"
    "v_add_f32 v1, v1, v29\n\t"
    "v_add_f32 v2, v2, v30\n\t"
    "v_add_f32 v3, v3, v31\n\t"
    "v_add_f32 v4, v4, v32\n\t"
    "v_add_f32 v5, v5, v33\n\t"
    "v_add_f32 v6, v6, v34\n\t"
    "v_add_f32 v7, v7, v35\n\t"
    "v_add_f32 v8, v8, v36\n\t"
    "v_add_f32 v9, v9, v37\n\t"
    "v_add_f32 v10, v10, v38\n\t"
    "v_add_f32 v11, v11, v39\n\t"
    "s_waitcnt lgkmcnt(9)\n\t"
    "v_add_f32 v12, v12, v40\n\t"
    "v_add_f32 v13, v13, v41\n\t"
    "v_add_f32 v14, v14, v42\n\t"
    "v_add_f32 v15, v15, v43\n\t"
    "s_waitcnt lgkmcnt(5)\n\t"
    "s_waitcnt lgkmcnt(1)\n\t"
    "L_asp3_%=:\n\t"
    "ds_read_b32 v119, v117 offset:0\n\t"
    "s_waitcnt lgkmcnt(0)\n\t"
    "v_readfirstlane_b32 s46, v119\n\t"
    "s_nop 0\n\t"
    "s_cmp_ge_u32 s46, 4\n\t"
    "s_cbranch_scc1 L_ago3_%=\n\t"
    "s_sleep 1\n\t"
    "s_branch L_asp3_%=\n\t"
    "L_ago3_%=:\n\t"
    "ds_write_b128 v95, v[0:3] offset:1024\n\t"
    "ds_write_b128 v95, v[4:7] offset:7168\n\t"
    "ds_write_b128 v95, v[8:11] offset:11264\n\t"
    "ds_write_b128 v95, v[12:15] offset:16384\n\t"
    "ds_write_b128 v95, v[16:19] offset:0\n\t"
    "ds_write_b128 v95, v[20:23] offset:15360\n\t"
    "ds_add_u32 v117, v118 offset:4\n\t"
    "s_branch L_ab0_%=\n\t"
    "L_afold2_%=:\n\t"
    "L_asp4_%=:\n\t"
    "ds_read_b32 v119, v117 offset:1044\n\t"
    "s_waitcnt lgkmcnt(0)\n\t"
    "v_readfirstlane_b32 s46, v119\n\t"
    "s_nop 0\n\t"
    "s_cmp_ge_u32 s46, 2\n\t"
    "s_cbranch_scc1 L_ago4_%=\n\t"
    "s_sleep 1\n\t"
    "s_branch L_asp4_%=\n\t"
    "L_ago4_%=:\n\t"
    "ds_read_u16_d16_hi v0, v92 offset:206\n\t"
    "ds_read_u16_d16_hi v28, v93 offset:602\n\t"
    "ds_read_u16_d16_hi v1, v92 offset:1406\n\t"
    "ds_read_u16_d16_hi v29, v93 offset:614\n\t"
    "ds_read_u16_d16_hi v2, v92 offset:2606\n\t"
    "ds_read_u16_d16_hi v30, v93 offset:626\n\t"
    "ds_read_u16_d16_hi v3, v92 offset:3806\n\t"
    "ds_read_u16_d16_hi v31, v93 offset:638\n\t"
    "ds_read_u16_d16_hi v4, v92 offset:210\n\t"
    "ds_read_u16_d16_hi v32, v93 offset:1002\n\t"
    "ds_read_u16_d16_hi v5, v92 offset:1410\n\t"
    "ds_read_u16_d16_hi v33, v93 offset:1014\n\t"
    "ds_read_u16_d16_hi v6, v92 offset:2610\n\t"
    "ds_read_u16_d16_hi v34, v93 offset:1026\n\t"
    "ds_read_u16_d16_hi v7, v92 offset:3810\n\t"
    "ds_read_u16_d16_hi v35, v93 offset:1038\n\t"
    "ds_read_u16_d16_hi v8, v92 offset:608\n\t"
    "ds_read_u16_d16_hi v36, v93 offset:806\n\t"
    "ds_read_u16_d16_hi v9, v92 offset:1808\n\t"
    "ds_read_u16_d16_hi v37, v93 offset:818\n\t"
    "ds_read_u16_d16_hi v10, v92 offset:3008\n\t"
    "ds_read_u16_d16_hi v38, v93 offset:830\n\t"
    "ds_read_u16_d16_hi v11, v92 offset:4208\n\t"
    "ds_read_u16_d16_hi v39, v93 offset:842\n\t"
    "ds_read_u16_d16_hi v12, v92 offset:812\n\t"
    "ds_read_u16_d16_hi v40, v93 offset:1208\n\t"
    "ds_read_u16_d16_hi v13, v92 offset:2012\n\t"
    "ds_read_u16_d16_hi v41, v93 offset:1220\n\t"
    "ds_read_u16_d16_hi v14, v92 offset:3212\n\t"
    "ds_read_u16_d16_hi v42, v93 offset:1232\n\t"
    "ds_read_u16_d16_hi v15, v92 offset:4412\n\t"
    "ds_read_u16_d16_hi v43, v93 offset:1244\n\t"
    "ds_read_u16_d16_hi v16, v92 offset:404\n\t"
    "ds_read_u16_d16_hi v17, v92 offset:1604\n\t"
    "ds_read_u16_d16_hi v18, v92 offset:2804\n\t"
    "ds_read_u16_d16_hi v19, v92 offset:4004\n\t"
    "ds_read_u16_d16_hi v20, v92 offset:1010\n\t"
    "ds_read_u16_d16_hi v21, v92 offset:2210\n\t"
    "ds_read_u16_d16_hi v22, v92 offset:3410\n\t"
    "ds_read_u16_d16_hi v23, v92 offset:4610\n\t"
    "ds_add_u32 v117, v118 offset:0\n\t"
    "s_waitcnt lgkmcnt(15)\n\t"
    "v_add_f32 v0, v0, v28\n\t"
    "v_add_f32 v1, v1, v29\n\t"
    "v_add_f32 v2, v2, v30\n\t"
    "v_add_f32 v3, v3, v31\n\t"
    "v_add_f32 v4, v4, v32\n\t"
    "v_add_f32 v5, v5, v33\n\t"
    "v_add_f32 v6, v6, v34\n\t"
    "v_add_f32 v7, v7, v35\n\t"
    "v_add_f32 v8, v8, v36\n\t"
    "v_add_f32 v9, v9, v37\n\t"
    "v_add_f32 v10, v10, v38\n\t"
    "v_add_f32 v11, v11, v39\n\t"
    "s_waitcnt lgkmcnt(9)\n\t"
    "v_add_f32 v12, v12, v40\n\t"
    "v_add_f32 v13, v13, v41\n\t"
    "v_add_f32 v14, v14, v42\n\t"
    "v_add_f32 v15, v15, v43\n\t"
    "s_waitcnt lgkmcnt(5)\n\t"
    "s_waitcnt lgkmcnt(1)\n\t"
    "L_asp5_%=:\n\t"
    "ds_read_b32 v119, v117 offset:0\n\t"
    "s_waitcnt lgkmcnt(0)\n\t"
    "v_readfirstlane_b32 s46, v119\n\t"
    "s_nop 0\n\t"
    "s_cmp_ge_u32 s46, 4\n\t"
    "s_cbranch_scc1 L_ago5_%=\n\t"
    "s_sleep 1\n\t"
    "s_branch L_asp5_%=\n\t"
    "L_ago5_%=:\n\t"
    "ds_write_b128 v95, v[0:3] offset:3072\n\t"
    "ds_write_b128 v95, v[4:7] offset:8192\n\t"
    "ds_write_b128 v95, v[8:11] offset:12288\n\t"
    "ds_write_b128 v95, v[12:15] offset:17408\n\t"
    "ds_write_b128 v95, v[16:19] offset:2048\n\t"
    "ds_write_b128 v95, v[20:23] offset:18432\n\t"
    "ds_add_u32 v117, v118 offset:4\n\t"
    "s_branch L_ab0_%=\n\t"
    "L_afold3_%=:\n\t"
    "L_asp6_%=:\n\t"
    "ds_read_b32 v119, v117 offset:1044\n\t"
    "s_waitcnt lgkmcnt(0)\n\t"
    "v_readfirstlane_b32 s46, v119\n\t"
    "s_nop 0\n\t"
    "s_cmp_ge_u32 s46, 2\n\t"
    "s_cbranch_scc1 L_ago6_%=\n\t"
    "s_sleep 1\n\t"
    "s_branch L_asp6_%=\n\t"
    "L_ago6_%=:\n\t"
    "ds_read_u16_d16_hi v0, v92 offset:406\n\t"
    "ds_read_u16_d16_hi v28, v93 offset:604\n\t"
    "ds_read_u16_d16_hi v1, v92 offset:1606\n\t"
    "ds_read_u16_d16_hi v29, v93 offset:616\n\t"
    "ds_read_u16_d16_hi v2, v92 offset:2806\n\t"
    "ds_read_u16_d16_hi v30, v93 offset:628\n\t"
    "ds_read_u16_d16_hi v3, v92 offset:4006\n\t"
    "ds_read_u16_d16_hi v31, v93 offset:640\n\t"
    "ds_read_u16_d16_hi v4, v92 offset:212\n\t"
    "ds_read_u16_d16_hi v32, v93 offset:1202\n\t"
    "ds_read_u16_d16_hi v5, v92 offset:1412\n\t"
    "ds_read_u16_d16_hi v33, v93 offset:1214\n\t"
    "ds_read_u16_d16_hi v6, v92 offset:2612\n\t"
    "ds_read_u16_d16_hi v34, v93 offset:1226\n\t"
    "ds_read_u16_d16_hi v7, v92 offset:3812\n\t"
    "ds_read_u16_d16_hi v35, v93 offset:1238\n\t"
    "ds_read_u16_d16_hi v8, v92 offset:610\n\t"
    "ds_read_u16_d16_hi v36, v93 offset:1006\n\t"
    "ds_read_u16_d16_hi v9, v92 offset:1810\n\t"
    "ds_read_u16_d16_hi v37, v93 offset:1018\n\t"
    "ds_read_u16_d16_hi v10, v92 offset:3010\n\t"
    "ds_read_u16_d16_hi v38, v93 offset:1030\n\t"
    "ds_read_u16_d16_hi v11, v92 offset:4210\n\t"
    "ds_read_u16_d16_hi v39, v93 offset:1042\n\t"
    "ds_read_u16_d16_hi v12, v92 offset:1012\n\t"
    "ds_read_u16_d16_hi v40, v93 offset:1210\n\t"
    "ds_read_u16_d16_hi v13, v92 offset:2212\n\t"
    "ds_read_u16_d16_hi v41, v93 offset:1222\n\t"
    "ds_read_u16_d16_hi v14, v92 offset:3412\n\t"
    "ds_read_u16_d16_hi v42, v93 offset:1234\n\t"
    "ds_read_u16_d16_hi v15, v92 offset:4612\n\t"
    "ds_read_u16_d16_hi v43, v93 offset:1246\n\t"
    "ds_read_u16_d16_hi v16, v92 offset:606\n\t"
    "ds_read_u16_d16_hi v17, v92 offset:1806\n\t"
    "ds_read_u16_d16_hi v18, v92 offset:3006\n\t"
    "ds_read_u16_d16_hi v19, v92 offset:4206\n\t"
    "ds_read_u16_d16_hi v20, v92 offset:1212\n\t"
    "ds_read_u16_d16_hi v21, v92 offset:2412\n\t"
    "ds_read_u16_d16_hi v22, v92 offset:3612\n\t"
    "ds_read_u16_d16_hi v23, v92 offset:4812\n\t"
    "ds_add_u32 v117, v118 offset:0\n\t"
    "s_waitcnt lgkmcnt(15)\n\t"
    "v_add_f32 v0, v0, v28\n\t"
    "v_add_f32 v1, v1, v29\n\t"
    "v_add_f32 v2, v2, v30\n\t"
    "v_add_f32 v3, v3, v31\n\t"
    "v_add_f32 v4, v4, v32\n\t"
    "v_add_f32 v5, v5, v33\n\t"
    "v_add_f32 v6, v6, v34\n\t"
    "v_add_f32 v7, v7, v35\n\t"
    "v_add_f32 v8, v8, v36\n\t"
    "v_add_f32 v9, v9, v37\n\t"
    "v_add_f32 v10, v10, v38\n\t"
    "v_add_f32 v11, v11, v39\n\t"
    "s_waitcnt lgkmcnt(9)\n\t"
    "v_add_f32 v12, v12, v40\n\t"
    "v_add_f32 v13, v13, v41\n\t"
    "v_add_f32 v14, v14, v42\n\t"
    "v_add_f32 v15, v15, v43\n\t"
    "s_waitcnt lgkmcnt(5)\n\t"
    "s_waitcnt lgkmcnt(1)\n\t"
    "L_asp7_%=:\n\t"
    "ds_read_b32 v119, v117 offset:0\n\t"
    "s_waitcnt lgkmcnt(0)\n\t"
    "v_readfirstlane_b32 s46, v119\n\t"
    "s_nop 0\n\t"
    "s_cmp_ge_u32 s46, 4\n\t"
    "s_cbranch_scc1 L_ago7_%=\n\t"
    "s_sleep 1\n\t"
    "s_branch L_asp7_%=\n\t"
    "L_ago7_%=:\n\t"
    "ds_write_b128 v95, v[0:3] offset:4096\n\t"
    "ds_write_b128 v95, v[4:7] offset:9216\n\t"
    "ds_write_b128 v95, v[8:11] offset:13312\n\t"
    "ds_write_b128 v95, v[12:15] offset:19456\n\t"
    "ds_write_b128 v95, v[16:19] offset:5120\n\t"
    "ds_write_b128 v95, v[20:23] offset:20480\n\t"
    "ds_add_u32 v117, v118 offset:4\n\t"
    "L_ab0_%=:\n\t"
    "s_branch L_ab_%=\n\t"
    "L_adma_%=:\n\t"
    "s_sub_u32 s46, %16, 4\n\t"
    "s_add_u32 s69, s51, s46\n\t"
    "s_sub_u32 s44, s69, 125\n\t"
    "s_cmp_ge_u32 s69, 125\n\t"
    "s_cselect_b32 s69, s44, s69\n\t"
    "s_mul_i32 m0, s69, 1040\n\t"
    "s_lshl_b32 s44, s69, 11\n\t"
    "buffer_load_dwordx4 v126, %15, s44 offen lds\n\t"
    "s_add_u32 s69, s69, 2\n\t"
    "s_sub_u32 s44, s69, 125\n\t"
    "s_cmp_ge_u32 s69, 125\n\t"
    "s_cselect_b32 s69, s44, s69\n\t"
    "s_mul_i32 m0, s69, 1040\n\t"
    "s_lshl_b32 s44, s69, 11\n\t"
    "buffer_load_dwordx4 v126, %15, s44 offen lds\n\t"
    "s_add_u32 s69, s69, 2\n\t"
    "s_sub_u32 s44, s69, 125\n\t"
    "s_cmp_ge_u32 s69, 125\n\t"
    "s_cselect_b32 s69, s44, s69\n\t"
    "s_barrier\n\t"
    "s_load_dwordx2 s[72:73], %6, 0\n\t"
    "s_load_dwordx2 s[88:89], %7, 0\n\t"
    "s_load_dword s91, %8, 0\n\t"
    "s_mul_i32 m0, s69, 1040\n\t"
    "s_lshl_b32 s44, s69, 11\n\t"
    "buffer_load_dwordx4 v126, %15, s44 offen lds\n\t"
    "s_add_u32 s69, s69, 2\n\t"
    "s_sub_u32 s44, s69, 125\n\t"
    "s_cmp_ge_u32 s69, 125\n\t"
    "s_cselect_b32 s69, s44, s69\n\t"
    "s_mul_i32 m0, s69, 1040\n\t"
    "s_lshl_b32 s44, s69, 11\n\t"
    "buffer_load_dwordx4 v126, %15, s44 offen lds\n\t"
    "s_add_u32 s69, s69, 2\n\t"
    "s_sub_u32 s44, s69, 125\n\t"
    "s_cmp_ge_u32 s69, 125\n\t"
    "s_cselect_b32 s69, s44, s69\n\t"
    "s_mul_i32 m0, s69, 1040\n\t"
    "s_lshl_b32 s44, s69, 11\n\t"
    "buffer_load_dwordx4 v126, %15, s44 offen lds\n\t"
    "s_add_u32 s69, s69, 2\n\t"
    "s_sub_u32 s44, s69, 125\n\t"
    "s_cmp_ge_u32 s69, 125\n\t"
    "s_cselect_b32 s69, s44, s69\n\t"
    "s_mul_i32 m0, s69, 1040\n\t"
    "s_lshl_b32 s44, s69, 11\n\t"
    "buffer_load_dwordx4 v126, %15, s44 offen lds\n\t"
    "s_add_u32 s69, s69, 2\n\t"
    "s_sub_u32 s44, s69, 125\n\t"
    "s_cmp_ge_u32 s69, 125\n\t"
    "s_cselect_b32 s69, s44, s69\n\t"
    "s_mul_i32 m0, s69, 1040\n\t"
    "s_lshl_b32 s44, s69, 11\n\t"
    "buffer_load_dwordx4 v126, %15, s44 offen lds\n\t"
    "s_add_u32 s69, s69, 2\n\t"
    "s_sub_u32 s44, s69, 125\n\t"
    "s_cmp_ge_u32 s69, 125\n\t"
    "s_cselect_b32 s69, s44, s69\n\t"
    "s_mul_i32 m0, s69, 1040\n\t"
    "s_lshl_b32 s44, s69, 11\n\t"
    "buffer_load_dwordx4 v126, %15, s44 offen lds\n\t"
    "s_add_u32 s69, s69, 2\n\t"
    "s_sub_u32 s44, s69, 125\n\t"
    "s_cmp_ge_u32 s69, 125\n\t"
    "s_cselect_b32 s69, s44, s69\n\t"
    "s_mul_i32 m0, s69, 1040\n\t"
    "s_lshl_b32 s44, s69, 11\n\t"
    "buffer_load_dwordx4 v126, %15, s44 offen lds\n\t"
    "s_add_u32 s69, s69, 2\n\t"
    "s_sub_u32 s44, s69, 125\n\t"
    "s_cmp_ge_u32 s69, 125\n\t"
    "s_cselect_b32 s69, s44, s69\n\t"
    "s_mul_i32 m0, s69, 1040\n\t"
    "s_lshl_b32 s44, s69, 11\n\t"
    "buffer_load_dwordx4 v126, %15, s44 offen lds\n\t"
    "s_add_u32 s69, s69, 2\n\t"
    "s_sub_u32 s44, s69, 125\n\t"
    "s_cmp_ge_u32 s69, 125\n\t"
    "s_cselect_b32 s69, s44, s69\n\t"
    "s_mul_i32 m0, s69, 1040\n\t"
    "s_lshl_b32 s44, s69, 11\n\t"
    "buffer_load_dwordx4 v126, %15, s44 offen lds\n\t"
    "s_add_u32 s69, s69, 2\n\t"
    "s_sub_u32 s44, s69, 125\n\t"
    "s_cmp_ge_u32 s69, 125\n\t"
    "s_cselect_b32 s69, s44, s69\n\t"
    "s_mul_i32 m0, s69, 1040\n\t"
    "s_lshl_b32 s44, s69, 11\n\t"
    "buffer_load_dwordx4 v126, %15, s44 offen lds\n\t"
    "s_add_u32 s69, s69, 2\n\t"
    "s_sub_u32 s44, s69, 125\n\t"
    "s_cmp_ge_u32 s69, 125\n\t"
    "s_cselect_b32 s69, s44, s69\n\t"
    "v_bfe_u32 v154, v147, 2, 2\n\t"
    "v_and_b32 v155, 3, v147\n\t"
    "v_mul_u32_u24 v154, 25, v154\n\t"
    "v_mad_u32_u24 v154, v155, 6, v154\n\t"
    "v_lshlrev_b32 v153, 2, v154\n\t"
    "s_and_b32 s53, s48, 0xff\n\t"
    "s_bfe_u32 s55, s48, 0x80008\n\t"
    "s_add_u32 s54, s53, s55\n\t"
    "s_add_u32 s55, s54, s55\n\t"
    "s_lshr_b32 s64, s48, 16\n\t"
    "v_cmp_eq_u32 s[62:63], 0, v155\n\t"
    "v_and_b32 v151, 48, v147\n\t"
    "v_or_b32 v155, 0x100000, v151\n\t"
    "v_cndmask_b32 v152, v155, v151, s[62:63]\n\t"
    "v_add_u32 v95, 140288, v122\n\t"
    "s_mul_i32 m0, s69, 1040\n\t"
    "s_lshl_b32 s44, s69, 11\n\t"
    "buffer_load_dwordx4 v126, %15, s44 offen lds\n\t"
    "s_add_u32 s69, s69, 2\n\t"
    "s_sub_u32 s44, s69, 125\n\t"
    "s_cmp_ge_u32 s69, 125\n\t"
    "s_cselect_b32 s69, s44, s69\n\t"
    "s_mul_i32 m0, s69, 1040\n\t"
    "s_lshl_b32 s44, s69, 11\n\t"
    "buffer_load_dwordx4 v126, %15, s44 offen lds\n\t"
    "s_add_u32 s69, s69, 2\n\t"
    "s_sub_u32 s44, s69, 125\n\t"
    "s_cmp_ge_u32 s69, 125\n\t"
    "s_cselect_b32 s69, s44, s69\n\t"
    "s_mul_i32 m0, s69, 1040\n\t"
    "s_lshl_b32 s44, s69, 11\n\t"
    "buffer_load_dwordx4 v126, %15, s44 offen lds\n\t"
    "s_add_u32 s69, s69, 2\n\t"
    "s_sub_u32 s44, s69, 125\n\t"
    "s_cmp_ge_u32 s69, 125\n\t"
    "s_cselect_b32 s69, s44, s69\n\t"
    "s_mul_i32 m0, s69, 1040\n\t"
    "s_lshl_b32 s44, s69, 11\n\t"
    "buffer_load_dwordx4 v126, %15, s44 offen lds\n\t"
    "s_add_u32 s69, s69, 2\n\t"
    "s_sub_u32 s44, s69, 125\n\t"
    "s_cmp_ge_u32 s69, 125\n\t"
    "s_cselect_b32 s69, s44, s69\n\t"
    "s_mul_i32 m0, s69, 1040\n\t"
    "s_lshl_b32 s44, s69, 11\n\t"
    "buffer_load_dwordx4 v126, %15, s44 offen lds\n\t"
    "s_add_u32 s69, s69, 2\n\t"
    "s_sub_u32 s44, s69, 125\n\t"
    "s_cmp_ge_u32 s69, 125\n\t"
    "s_cselect_b32 s69, s44, s69\n\t"
    "s_mul_i32 m0, s69, 1040\n\t"
    "s_lshl_b32 s44, s69, 11\n\t"
    "buffer_load_dwordx4 v126, %15, s44 offen lds\n\t"
    "s_add_u32 s69, s69, 2\n\t"
    "s_sub_u32 s44, s69, 125\n\t"
    "s_cmp_ge_u32 s69, 125\n\t"
    "s_cselect_b32 s69, s44, s69\n\t"
    "s_mul_i32 m0, s69, 1040\n\t"
    "s_lshl_b32 s44, s69, 11\n\t"
    "buffer_load_dwordx4 v126, %15, s44 offen lds\n\t"
    "s_add_u32 s69, s69, 2\n\t"
    "s_sub_u32 s44, s69, 125\n\t"
    "s_cmp_ge_u32 s69, 125\n\t"
    "s_cselect_b32 s69, s44, s69\n\t"
    "s_mul_i32 m0, s69, 1040\n\t"
    "s_lshl_b32 s44, s69, 11\n\t"
    "buffer_load_dwordx4 v126, %15, s44 offen lds\n\t"
    "s_add_u32 s69, s69, 2\n\t"
    "s_sub_u32 s44, s69, 125\n\t"
    "s_cmp_ge_u32 s69, 125\n\t"
    "s_cselect_b32 s69, s44, s69\n\t"
    "s_mul_i32 m0, s69, 1040\n\t"
    "s_lshl_b32 s44, s69, 11\n\t"
    "buffer_load_dwordx4 v126, %15, s44 offen lds\n\t"
    "s_add_u32 s69, s69, 2\n\t"
    "s_sub_u32 s44, s69, 125\n\t"
    "s_cmp_ge_u32 s69, 125\n\t"
    "s_cselect_b32 s69, s44, s69\n\t"
    "s_mul_i32 m0, s69, 1040\n\t"
    "s_lshl_b32 s44, s69, 11\n\t"
    "buffer_load_dwordx4 v126, %15, s44 offen lds\n\t"
    "s_add_u32 s69, s69, 2\n\t"
    "s_sub_u32 s44, s69, 125\n\t"
    "s_cmp_ge_u32 s69, 125\n\t"
    "s_cselect_b32 s69, s44, s69\n\t"
    "s_mul_i32 m0, s69, 1040\n\t"
    "s_lshl_b32 s44, s69, 11\n\t"
    "buffer_load_dwordx4 v126, %15, s44 offen lds\n\t"
    "s_add_u32 s69, s69, 2\n\t"
    "s_sub_u32 s44, s69, 125\n\t"
    "s_cmp_ge_u32 s69, 125\n\t"
    "s_cselect_b32 s69, s44, s69\n\t"
    "s_mul_i32 m0, s69, 1040\n\t"
    "s_lshl_b32 s44, s69, 11\n\t"
    "buffer_load_dwordx4 v126, %15, s44 offen lds\n\t"
    "s_add_u32 s69, s69, 2\n\t"
    "s_sub_u32 s44, s69, 125\n\t"
    "s_cmp_ge_u32 s69, 125\n\t"
    "s_cselect_b32 s69, s44, s69\n\t"
    "s_mul_i32 m0, s69, 1040\n\t"
    "s_lshl_b32 s44, s69, 11\n\t"
    "buffer_load_dwordx4 v126, %15, s44 offen lds\n\t"
    "s_add_u32 s69, s69, 2\n\t"
    "s_sub_u32 s44, s69, 125\n\t"
    "s_cmp_ge_u32 s69, 125\n\t"
    "s_cselect_b32 s69, s44, s69\n\t"
    "s_mul_i32 m0, s69, 1040\n\t"
    "s_lshl_b32 s44, s69, 11\n\t"
    "buffer_load_dwordx4 v126, %15, s44 offen lds\n\t"
    "s_add_u32 s69, s69, 2\n\t"
    "s_sub_u32 s44, s69, 125\n\t"
    "s_cmp_ge_u32 s69, 125\n\t"
    "s_cselect_b32 s69, s44, s69\n\t"
    "s_mul_i32 m0, s69, 1040\n\t"
    "s_lshl_b32 s44, s69, 11\n\t"
    "buffer_load_dwordx4 v126, %15, s44 offen lds\n\t"
    "s_add_u32 s69, s69, 2\n\t"
    "s_sub_u32 s44, s69, 125\n\t"
    "s_cmp_ge_u32 s69, 125\n\t"
    "s_cselect_b32 s69, s44, s69\n\t"
    "s_mul_i32 m0, s69, 1040\n\t"
    "s_lshl_b32 s44, s69, 11\n\t"
    "buffer_load_dwordx4 v126, %15, s44 offen lds\n\t"
    "s_add_u32 s69, s69, 2\n\t"
    "s_sub_u32 s44, s69, 125\n\t"
    "s_cmp_ge_u32 s69, 125\n\t"
    "s_cselect_b32 s69, s44, s69\n\t"
    "s_mul_i32 m0, s69, 1040\n\t"
    "s_lshl_b32 s44, s69, 11\n\t"
    "buffer_load_dwordx4 v126, %15, s44 offen lds\n\t"
    "s_add_u32 s69, s69, 2\n\t"
    "s_sub_u32 s44, s69, 125\n\t"
    "s_cmp_ge_u32 s69, 125\n\t"
    "s_cselect_b32 s69, s44, s69\n\t"
    "s_mul_i32 m0, s69, 1040\n\t"
    "s_lshl_b32 s44, s69, 11\n\t"
    "buffer_load_dwordx4 v126, %15, s44 offen lds\n\t"
    "s_add_u32 s69, s69, 2\n\t"
    "s_sub_u32 s44, s69, 125\n\t"
    "s_cmp_ge_u32 s69, 125\n\t"
    "s_cselect_b32 s69, s44, s69\n\t"
    "s_mul_i32 m0, s69, 1040\n\t"
    "s_lshl_b32 s44, s69, 11\n\t"
    "buffer_load_dwordx4 v126, %15, s44 offen lds\n\t"
    "s_add_u32 s69, s69, 2\n\t"
    "s_sub_u32 s44, s69, 125\n\t"
    "s_cmp_ge_u32 s69, 125\n\t"
    "s_cselect_b32 s69, s44, s69\n\t"
    "s_mul_i32 m0, s69, 1040\n\t"
    "s_lshl_b32 s44, s69, 11\n\t"
    "buffer_load_dwordx4 v126, %15, s44 offen lds\n\t"
    "s_add_u32 s69, s69, 2\n\t"
    "s_sub_u32 s44, s69, 125\n\t"
    "s_cmp_ge_u32 s69, 125\n\t"
    "s_cselect_b32 s69, s44, s69\n\t"
    "s_mul_i32 m0, s69, 1040\n\t"
    "s_lshl_b32 s44, s69, 11\n\t"
    "buffer_load_dwordx4 v126, %15, s44 offen lds\n\t"
    "s_add_u32 s69, s69, 2\n\t"
    "s_sub_u32 s44, s69, 125\n\t"
    "s_cmp_ge_u32 s69, 125\n\t"
    "s_cselect_b32 s69, s44, s69\n\t"
    "s_mul_i32 m0, s69, 1040\n\t"
    "s_lshl_b32 s44, s69, 11\n\t"
    "buffer_load_dwordx4 v126, %15, s44 offen lds\n\t"
    "s_add_u32 s69, s69, 2\n\t"
    "s_sub_u32 s44, s69, 125\n\t"
    "s_cmp_ge_u32 s69, 125\n\t"
    "s_cselect_b32 s69, s44, s69\n\t"
    "s_mul_i32 m0, s69, 1040\n\t"
    "s_lshl_b32 s44, s69, 11\n\t"
    "buffer_load_dwordx4 v126, %15, s44 offen lds\n\t"
    "s_add_u32 s69, s69, 2\n\t"
    "s_sub_u32 s44, s69, 125\n\t"
    "s_cmp_ge_u32 s69, 125\n\t"
    "s_cselect_b32 s69, s44, s69\n\t"
    "s_mul_i32 m0, s69, 1040\n\t"
    "s_lshl_b32 s44, s69, 11\n\t"
    "buffer_load_dwordx4 v126, %15, s44 offen lds\n\t"
    "s_add_u32 s69, s69, 2\n\t"
    "s_sub_u32 s44, s69, 125\n\t"
    "s_cmp_ge_u32 s69, 125\n\t"
    "s_cselect_b32 s69, s44, s69\n\t"
    "s_mul_i32 m0, s69, 1040\n\t"
    "s_lshl_b32 s44, s69, 11\n\t"
    "buffer_load_dwordx4 v126, %15, s44 offen lds\n\t"
    "s_add_u32 s69, s69, 2\n\t"
    "s_sub_u32 s44, s69, 125\n\t"
    "s_cmp_ge_u32 s69, 125\n\t"
    "s_cselect_b32 s69, s44, s69\n\t"
    "s_mul_i32 m0, s69, 1040\n\t"
    "s_lshl_b32 s44, s69, 11\n\t"
    "buffer_load_dwordx4 v126, %15, s44 offen lds\n\t"
    "s_add_u32 s69, s69, 2\n\t"
    "s_sub_u32 s44, s69, 125\n\t"
    "s_cmp_ge_u32 s69, 125\n\t"
    "s_cselect_b32 s69, s44, s69\n\t"
    "s_mul_i32 m0, s69, 1040\n\t"
    "s_lshl_b32 s44, s69, 11\n\t"
    "buffer_load_dwordx4 v126, %15, s44 offen lds\n\t"
    "s_add_u32 s69, s69, 2\n\t"
    "s_sub_u32 s44, s69, 125\n\t"
    "s_cmp_ge_u32 s69, 125\n\t"
    "s_cselect_b32 s69, s44, s69\n\t"
    "s_mul_i32 m0, s69, 1040\n\t"
    "s_lshl_b32 s44, s69, 11\n\t"
    "buffer_load_dwordx4 v126, %15, s44 offen lds\n\t"
    "s_add_u32 s69, s69, 2\n\t"
    "s_sub_u32 s44, s69, 125\n\t"
    "s_cmp_ge_u32 s69, 125\n\t"
    "s_cselect_b32 s69, s44, s69\n\t"
    "s_mul_i32 m0, s69, 1040\n\t"
    "s_lshl_b32 s44, s69, 11\n\t"
    "buffer_load_dwordx4 v126, %15, s44 offen lds\n\t"
    "s_add_u32 s69, s69, 2\n\t"
    "s_sub_u32 s44, s69, 125\n\t"
    "s_cmp_ge_u32 s69, 125\n\t"
    "s_cselect_b32 s69, s44, s69\n\t"
    "s_mul_i32 m0, s69, 1040\n\t"
    "s_lshl_b32 s44, s69, 11\n\t"
    "buffer_load_dwordx4 v126, %15, s44 offen lds\n\t"
    "s_add_u32 s69, s69, 2\n\t"
    "s_sub_u32 s44, s69, 125\n\t"
    "s_cmp_ge_u32 s69, 125\n\t"
    "s_cselect_b32 s69, s44, s69\n\t"
    "s_mul_i32 m0, s69, 1040\n\t"
    "s_lshl_b32 s44, s69, 11\n\t"
    "buffer_load_dwordx4 v126, %15, s44 offen lds\n\t"
    "s_add_u32 s69, s69, 2\n\t"
    "s_sub_u32 s44, s69, 125\n\t"
    "s_cmp_ge_u32 s69, 125\n\t"
    "s_cselect_b32 s69, s44, s69\n\t"
    "s_mul_i32 m0, s69, 1040\n\t"
    "s_lshl_b32 s44, s69, 11\n\t"
    "buffer_load_dwordx4 v126, %15, s44 offen lds\n\t"
    "s_add_u32 s69, s69, 2\n\t"
    "s_sub_u32 s44, s69, 125\n\t"
    "s_cmp_ge_u32 s69, 125\n\t"
    "s_cselect_b32 s69, s44, s69\n\t"
    "s_mul_i32 m0, s69, 1040\n\t"
    "s_lshl_b32 s44, s69, 11\n\t"
    "buffer_load_dwordx4 v126, %15, s44 offen lds\n\t"
    "s_add_u32 s69, s69, 2\n\t"
    "s_sub_u32 s44, s69, 125\n\t"
    "s_cmp_ge_u32 s69, 125\n\t"
    "s_cselect_b32 s69, s44, s69\n\t"
    "s_mul_i32 m0, s69, 1040\n\t"
    "s_lshl_b32 s44, s69, 11\n\t"
    "buffer_load_dwordx4 v126, %15, s44 offen lds\n\t"
    "s_add_u32 s69, s69, 2\n\t"
    "s_sub_u32 s44, s69, 125\n\t"
    "s_cmp_ge_u32 s69, 125\n\t"
    "s_cselect_b32 s69, s44, s69\n\t"
    "s_mul_i32 m0, s69, 1040\n\t"
    "s_lshl_b32 s44, s69, 11\n\t"
    "buffer_load_dwordx4 v126, %15, s44 offen lds\n\t"
    "s_add_u32 s69, s69, 2\n\t"
    "s_sub_u32 s44, s69, 125\n\t"
    "s_cmp_ge_u32 s69, 125\n\t"
    "s_cselect_b32 s69, s44, s69\n\t"
    "s_mul_i32 m0, s69, 1040\n\t"
    "s_lshl_b32 s44, s69, 11\n\t"
    "buffer_load_dwordx4 v126, %15, s44 offen lds\n\t"
    "s_add_u32 s69, s69, 2\n\t"
    "s_sub_u32 s44, s69, 125\n\t"
    "s_cmp_ge_u32 s69, 125\n\t"
    "s_cselect_b32 s69, s44, s69\n\t"
    "s_mul_i32 m0, s69, 1040\n\t"
    "s_lshl_b32 s44, s69, 11\n\t"
    "buffer_load_dwordx4 v126, %15, s44 offen lds\n\t"
    "s_add_u32 s69, s69, 2\n\t"
    "s_sub_u32 s44, s69, 125\n\t"
    "s_cmp_ge_u32 s69, 125\n\t"
    "s_cselect_b32 s69, s44, s69\n\t"
    "s_mul_i32 m0, s69, 1040\n\t"
    "s_lshl_b32 s44, s69, 11\n\t"
    "buffer_load_dwordx4 v126, %15, s44 offen lds\n\t"
    "s_add_u32 s69, s69, 2\n\t"
    "s_sub_u32 s44, s69, 125\n\t"
    "s_cmp_ge_u32 s69, 125\n\t"
    "s_cselect_b32 s69, s44, s69\n\t"
    "s_mul_i32 m0, s69, 1040\n\t"
    "s_lshl_b32 s44, s69, 11\n\t"
    "buffer_load_dwordx4 v126, %15, s44 offen lds\n\t"
    "s_add_u32 s69, s69, 2\n\t"
    "s_sub_u32 s44, s69, 125\n\t"
    "s_cmp_ge_u32 s69, 125\n\t"
    "s_cselect_b32 s69, s44, s69\n\t"
    "L_ab_%=:\n\t"
    "s_mul_i32 s66, s53, 400\n\t"
    "buffer_load_dwordx3 v[128:130], v153, %0, s66 offen\n\t"
    "buffer_load_dwordx4 v[132:135], v153, %0, s66 offen offset:12\n\t"
    "s_mul_i32 s66, s54, 400\n\t"
    "buffer_load_dwordx3 v[136:138], v153, %0, s66 offen\n\t"
    "buffer_load_dwordx4 v[140:143], v153, %0, s66 offen offset:12\n\t"
    "s_call_b64 s[98:99], L_setup_%=\n\t"
    "L_asp8_%=:\n\t"
    "ds_read_b32 v119, v117 offset:4\n\t"
    "s_waitcnt lgkmcnt(0)\n\t"
    "v_readfirstlane_b32 s46, v119\n\t"
    "s_nop 0\n\t"
    "s_cmp_ge_u32 s46, 4\n\t"
    "s_cbranch_scc1 L_ago8_%=\n\t"
    "s_sleep 1\n\t"
    "s_branch L_asp8_%=\n\t"
    "L_ago8_%=:\n\t"
    "ds_read_b128 v[0:3], v95 offset:0\n\t"
    "ds_read_b128 v[4:7], v95 offset:1024\n\t"
    "ds_read_b128 v[8:11], v95 offset:2048\n\t"
    "ds_read_b128 v[12:15], v95 offset:3072\n\t"
    "ds_read_b128 v[16:19], v95 offset:4096\n\t"
    "ds_read_b128 v[20:23], v95 offset:5120\n\t"
    "ds_read_b128 v[24:27], v95 offset:6144\n\t"
    "ds_read_b128 v[28:31], v95 offset:7168\n\t"
    "ds_read_b128 v[32:35], v95 offset:8192\n\t"
    "ds_read_b128 v[36:39], v95 offset:9216\n\t"
    "ds_read_b128 v[40:43], v95 offset:10240\n\t"
    "ds_read_b128 v[44:47], v95 offset:11264\n\t"
    "ds_read_b128 v[48:51], v95 offset:12288\n\t"
    "ds_read_b128 v[52:55], v95 offset:13312\n\t"
    "ds_read_b128 v[56:59], v95 offset:14336\n\t"
    "ds_read_b128 v[60:63], v95 offset:15360\n\t"
    "ds_read_b128 v[64:67], v95 offset:16384\n\t"
    "ds_read_b128 v[68:71], v95 offset:17408\n\t"
    "ds_read_b128 v[72:75], v95 offset:18432\n\t"
    "ds_read_b128 v[76:79], v95 offset:19456\n\t"
    "ds_read_b128 v[80:83], v95 offset:20480\n\t"
    "ds_read_b128 v[84:87], v95 offset:21504\n\t"
    "ds_read_b128 v[88:91], v95 offset:22528\n\t"
    "s_waitcnt vmcnt(4)\n\t"
    "ds_add_u32 v117, v118 offset:8\n\t"
    "s_waitcnt vmcnt(0)\n\t"
    "s_branch L_awd_%=\n\t"
    "s_waitcnt vmcnt(4)\n\t"
    "ds_add_u32 v117, v118 offset:8\n\t"
    "s_branch L_await_%=\n\t"
    "L_setup_%=:\n\t"
    "v_mbcnt_lo_u32_b32 v100, -1, 0\n\t"
    "v_mbcnt_hi_u32_b32 v100, -1, v100\n\t"
    "s_lshl_b32 s61, %16, 15\n\t"
    "s_add_u32 s61, s61, 16515072\n\t"
    "v_lshlrev_b32 v127, 4, v100\n\t"
    "s_lshr_b32 s37, %14, 4\n\t"
    "s_and_b32 s39, %14, 7\n\t"
    "v_and_b32 v101, 15, v100\n\t"
    "s_lshl_b32 s39, s39, 4\n\t"
    "s_add_u32 s39, s39, s37\n\t"
    "s_mul_i32 s65, %16, 125\n\t"
    "v_add_u32 v155, s65, v101\n\t"
    "s_mul_i32 s63, s39, 102400\n\t"
    "s_add_u32 s63, s63, 3407872\n\t"
    "v_lshrrev_b32 v102, 2, v101\n\t"
    "s_and_b32 s37, s37, 7\n\t"
    "s_lshl_b32 s37, s37, 11\n\t"
    "v_lshrrev_b32 v103, 3, v100\n\t"
    "s_mul_i32 s36, s39, 65536\n\t"
    "s_bfe_u32 s65, %14, 0x10003\n\t"
    "v_and_b32 v103, 2, v103\n\t"
    "s_lshl_b32 s38, s65, 15\n\t"
    "s_add_u32 s36, s36, s38\n\t"
    "v_and_b32 v104, 1, v100\n\t"
    "s_lshl_b32 s38, %16, 12\n\t"
    "s_add_u32 s36, s36, s38\n\t"
    "v_lshlrev_b32 v104, 4, v104\n\t"
    "s_add_u32 s36, s36, 17825792\n\t"
    "v_mov_b32 v101, 0x3f80\n\t"
    "v_lshlrev_b32 v104, v104, v101\n\t"
    "v_cmp_eq_u32 vcc, v102, v103\n\t"
    "s_waitcnt lgkmcnt(0)\n\t"
    "s_lshr_b32 s39, s91, %9\n\t"
    "s_lshl_b32 s39, s39, 16\n\t"
    "v_cndmask_b32 v120, 0, v104, vcc\n\t"
    "v_cndmask_b32 v121, 0, v104, vcc\n\t"
    "v_add_u32 v103, 1, v103\n\t"
    "v_cmp_eq_u32 vcc, v102, v103\n\t"
    "s_lshl_b32 s40, s88, 16\n\t"
    "s_and_b32 s41, s88, 0xffff0000\n\t"
    "v_cndmask_b32 v122, 0, v104, vcc\n\t"
    "v_cndmask_b32 v123, 0, v104, vcc\n\t"
    "v_and_b32 v101, 2, v100\n\t"
    "v_mov_b32 v102, 0x44444444\n\t"
    "v_mov_b32 v103, 0xeeeeeeee\n\t"
    "v_cmp_eq_u32 vcc, 0, v101\n\t"
    "s_lshl_b32 s42, s89, 16\n\t"
    "s_and_b32 s43, s89, 0xffff0000\n\t"
    "v_cndmask_b32 v124, v103, v102, vcc\n\t"
    "v_lshrrev_b32 v101, 4, v100\n\t"
    "v_mul_u32_u24 v101, 8000, v101\n\t"
    "v_lshl_add_u32 v125, v155, 3, v101\n\t"
    "v_add_u32 v125, 64000, v125\n\t"
    "s_lshl_b32 s70, s72, 16\n\t"
    "s_and_b32 s71, s72, 0xffff0000\n\t"
    "s_lshl_b32 s72, s73, 16\n\t"
    "s_and_b32 s73, s73, 0xffff0000\n\t"
    "s_mov_b32 s67, 0xffff0000\n\t"
    "s_and_b32 s65, %14, 8\n\t"
    "s_add_u32 s38, s65, %16\n\t"
    "s_mul_i32 s38, s38, 212992\n\t"
    "s_add_u32 s38, s38, 196608\n\t"
    "s_add_u32 s84, s37, s38\n\t"
    "s_add_u32 s37, s37, 0x800\n\t"
    "s_and_b32 s37, s37, 0x3800\n\t"
    "s_add_u32 s85, s37, s38\n\t"
    "s_add_u32 s37, s37, 0x800\n\t"
    "s_and_b32 s37, s37, 0x3800\n\t"
    "s_add_u32 s86, s37, s38\n\t"
    "s_add_u32 s37, s37, 0x800\n\t"
    "s_and_b32 s37, s37, 0x3800\n\t"
    "s_add_u32 s87, s37, s38\n\t"
    "s_add_u32 s37, s37, 0x800\n\t"
    "s_and_b32 s37, s37, 0x3800\n\t"
    "s_add_u32 s88, s37, s38\n\t"
    "s_add_u32 s37, s37, 0x800\n\t"
    "s_and_b32 s37, s37, 0x3800\n\t"
    "s_add_u32 s89, s37, s38\n\t"
    "s_add_u32 s37, s37, 0x800\n\t"
    "s_and_b32 s37, s37, 0x3800\n\t"
    "s_add_u32 s90, s37, s38\n\t"
    "s_add_u32 s37, s37, 0x800\n\t"
    "s_and_b32 s37, s37, 0x3800\n\t"
    "s_add_u32 s91, s37, s38\n\t"
    "s_mul_i32 s94, s65, 425984\n\t"
    "s_sub_u32 s94, s94, 1703936\n\t"
    "s_sub_u32 s95, 0x8000, s94\n\t"
    "s_lshl_b32 s97, s65, 11\n\t"
    "s_sub_u32 s97, s97, 229376\n\t"
    "s_mov_b32 s92, 64000\n\t"
    "s_mov_b32 s93, -64000\n\t"
    "s_mov_b32 s96, 0x2000\n\t"
    "s_mov_b32 s62, 2\n\t"
    "v_rcp_f32 v101, s40\n\t"
    "v_rcp_f32 v102, s41\n\t"
    "v_rcp_f32 v103, s42\n\t"
    "v_rcp_f32 v104, s43\n\t"
    "v_mov_b32 v100, 0x7f7fffff\n\t"
    "v_med3_f32 v101, v101, -v100, v100\n\t"
    "v_med3_f32 v102, v102, -v100, v100\n\t"
    "v_med3_f32 v103, v103, -v100, v100\n\t"
    "v_med3_f32 v104, v104, -v100, v100\n\t"
    "v_readfirstlane_b32 s40, v101\n\t"
    "v_readfirstlane_b32 s41, v102\n\t"
    "v_readfirstlane_b32 s42, v103\n\t"
    "v_readfirstlane_b32 s43, v104\n\t"
    "s_setpc_b64 s[98:99]\n\t"
    "L_aside_%=:\n\t"
    "s_setprio 3\n\t"
    "v_cmp_gt_u32 vcc, 48, v147\n\t"
    "v_mov_b32 v133, 0x40000000\n\t"
    "v_lshrrev_b32 v134, 3, v147\n\t"
    "v_bfe_u32 v135, v147, 2, 1\n\t"
    "v_and_b32 v130, 3, v147\n\t"
    "v_lshl_add_u32 v132, v135, 5, v130\n\t"
    "v_mul_u32_u24 v64, 400, v132\n\t"
    "v_lshl_add_u32 v64, v134, 5, v64\n\t"
    "s_movk_i32 s50, 0\n\t"
    "buffer_load_dwordx4 v[0:3], v64, %0, s50 offen offset:16\n\t"
    "buffer_load_dwordx4 v[4:7], v64, %0, s50 offen offset:32\n\t"
    "v_lshlrev_b32 v66, 14, v134\n\t"
    "v_lshl_add_u32 v66, v132, 4, v66\n\t"
    "v_lshlrev_b32 v68, 8, v134\n\t"
    "v_lshl_add_u32 v68, v135, 2, v68\n\t"
    "v_add_u32 v68, 130048, v68\n\t"
    "v_cndmask_b32 v68, v133, v68, vcc\n\t"
    "v_cndmask_b32 v66, v133, v66, vcc\n\t"
    "v_lshrrev_b32 v134, 2, v147\n\t"
    "v_bfe_u32 v135, v147, 1, 1\n\t"
    "v_and_b32 v130, 1, v147\n\t"
    "v_lshl_add_u32 v132, v135, 5, v130\n\t"
    "v_mul_u32_u24 v65, 400, v132\n\t"
    "v_lshl_add_u32 v65, v134, 5, v65\n\t"
    "v_lshlrev_b32 v67, 14, v134\n\t"
    "v_lshl_add_u32 v67, v132, 4, v67\n\t"
    "v_lshlrev_b32 v69, 8, v134\n\t"
    "v_lshl_add_u32 v69, v135, 2, v69\n\t"
    "v_add_u32 v69, 130048, v69\n\t"
    "v_cndmask_b32 v69, v133, v69, vcc\n\t"
    "v_cndmask_b32 v67, v133, v67, vcc\n\t"
    "s_barrier\n\t"
    "s_load_dwordx2 s[72:73], %6, 0\n\t"
    "s_load_dwordx2 s[88:89], %7, 0\n\t"
    "s_load_dword s91, %8, 0\n\t"
    "v_lshlrev_b32 v128, 4, v147\n\t"
    "v_add_u32 v128, 133120, v128\n\t"
    "v_and_b32 v130, 1, v147\n\t"
    "v_cndmask_b32 v128, v133, v128, vcc\n\t"
    "v_mul_u32_u24 v127, 7, v130\n\t"
    "v_cmp_eq_u32 vcc, 1, v130\n\t"
    "v_mov_b32 v126, 0x07060100\n\t"
    "v_mov_b32 v133, 0x05040100\n\t"
    "v_mov_b32 v136, 1\n\t"
    "v_cndmask_b32 v126, v126, v133, vcc\n\t"
    "s_mov_b32 s36, 0\n\t"
    "s_mov_b32 s37, 8000\n\t"
    "s_mov_b32 s38, 16000\n\t"
    "s_mov_b32 s39, 24000\n\t"
    "s_mov_b32 s40, 32000\n\t"
    "s_mov_b32 s41, 40000\n\t"
    "s_mov_b32 s42, 48000\n\t"
    "s_mov_b32 s43, 56000\n\t"
    "s_movk_i32 s44, 0xf8\n\t"
    "s_mov_b32 s45, 0x01010000\n\t"
    "s_mov_b32 s46, 0x020c000c\n\t"
    "s_lshr_b32 s49, %14, 4\n\t"
    "s_mul_i32 s49, s49, 212992\n\t"
    "s_and_b32 s50, %14, 15\n\t"
    "s_lshl_b32 s50, s50, 10\n\t"
    "s_add_u32 s49, s49, s50\n\t"
    "v_lshrrev_b32 v134, 4, v147\n\t"
    "v_bfe_u32 v135, v147, 2, 2\n\t"
    "v_and_b32 v130, 3, v147\n\t"
    "v_lshlrev_b32 v137, 17, v134\n\t"
    "v_lshl_add_u32 v137, v135, 10, v137\n\t"
    "v_lshl_add_u32 v137, v130, 4, v137\n\t"
    "v_add_u32 v138, 0x1000, v137\n\t"
    "v_mul_u32_u24 v141, 0x210, v134\n\t"
    "v_lshl_add_u32 v141, v135, 6, v141\n\t"
    "v_lshl_add_u32 v141, v130, 4, v141\n\t"
    "v_add_u32 v141, 134656, v141\n\t"
    "v_mul_u32_u24 v142, 0x210, v130\n\t"
    "v_lshl_add_u32 v142, v135, 6, v142\n\t"
    "v_lshl_add_u32 v142, v134, 3, v142\n\t"
    "v_add_u32 v142, 134656, v142\n\t"
    "s_mov_b32 s50, 102400\n\t"
    "v_mul_u32_u24 v139, s50, v134\n\t"
    "v_and_b32 v132, 15, v147\n\t"
    "v_lshl_add_u32 v139, v132, 4, v139\n\t"
    "v_add_u32 v140, 409600, v139\n\t"
    "s_lshr_b32 s50, %14, 4\n\t"
    "s_lshl_b32 s47, s50, 13\n\t"
    "s_lshl_b32 s48, s50, 8\n\t"
    "s_and_b32 s50, %14, 3\n\t"
    "s_mul_i32 s51, s50, 256\n\t"
    "s_add_u32 s47, s47, s51\n\t"
    "s_mul_i32 s51, s50, 3276800\n\t"
    "s_add_u32 s48, s48, s51\n\t"
    "s_bfe_u32 s50, %14, 131074\n\t"
    "s_lshl_b32 s51, s50, 19\n\t"
    "s_add_u32 s47, s47, s51\n\t"
    "s_lshl_b32 s51, s50, 12\n\t"
    "s_add_u32 s48, s48, s51\n\t"
    "s_add_u32 s48, s48, 3407872\n\t"
    "s_waitcnt vmcnt(2)\n\t"
    "v_lshlrev_b32 v134, 20, v147\n\t"
    "v_add_u32 v134, 2068, v134\n\t"
    "ds_add_u32 v134, v136\n\t"
    "v_mov_b32 v110, 65664\n\t"
    "v_mov_b32 v111, 131136\n\t"
    "v_mov_b32 v112, 262176\n\t"
    "v_mov_b32 v113, 524304\n\t"
    "v_mov_b32 v114, 1048584\n\t"
    "v_mov_b32 v115, 2097156\n\t"
    "v_mov_b32 v116, 4194306\n\t"
    "v_mov_b32 v117, 8388609\n\t"
    "s_waitcnt vmcnt(0)\n\t"
    "v_lshl_add_u32 v76, v0, 3, s36\n\t"
    "v_lshl_add_u32 v77, v1, 3, s37\n\t"
    "v_lshl_add_u32 v78, v2, 3, s38\n\t"
    "v_lshl_add_u32 v79, v3, 3, s39\n\t"
    "v_lshl_add_u32 v80, v4, 3, s40\n\t"
    "v_lshl_add_u32 v81, v5, 3, s41\n\t"
    "v_lshl_add_u32 v82, v6, 3, s42\n\t"
    "v_lshl_add_u32 v83, v7, 3, s43\n\t"
    "v_and_or_b32 v92, v76, s44, v68\n\t"
    "ds_or_rtn_b32 v118, v92, v110\n\t"
    "v_and_or_b32 v93, v77, s44, v68\n\t"
    "ds_or_rtn_b32 v119, v93, v111\n\t"
    "v_and_or_b32 v94, v78, s44, v68\n\t"
    "ds_or_rtn_b32 v120, v94, v112\n\t"
    "v_and_or_b32 v95, v79, s44, v68\n\t"
    "ds_or_rtn_b32 v121, v95, v113\n\t"
    "v_and_or_b32 v96, v80, s44, v68\n\t"
    "ds_or_rtn_b32 v122, v96, v114\n\t"
    "v_and_or_b32 v97, v81, s44, v68\n\t"
    "ds_or_rtn_b32 v123, v97, v115\n\t"
    "v_and_or_b32 v98, v82, s44, v68\n\t"
    "ds_or_rtn_b32 v124, v98, v116\n\t"
    "v_and_or_b32 v99, v83, s44, v68\n\t"
    "ds_or_rtn_b32 v125, v99, v117\n\t"
    "v_lshl_or_b32 v164, v77, 16, v76\n\t"
    "v_lshl_or_b32 v165, v79, 16, v78\n\t"
    "v_lshl_or_b32 v166, v81, 16, v80\n\t"
    "v_lshl_or_b32 v167, v83, 16, v82\n\t"
    "s_movk_i32 s50, 0\n\t"
    "buffer_load_dwordx4 v[8:11], v64, %0, s50 offen offset:1616\n\t"
    "buffer_load_dwordx4 v[12:15], v64, %0, s50 offen offset:1632\n\t"
    "s_movk_i32 s50, 3200\n\t"
    "buffer_load_dwordx4 v[16:19], v64, %0, s50 offen offset:16\n\t"
    "buffer_load_dwordx4 v[20:23], v64, %0, s50 offen offset:32\n\t"
    "s_waitcnt vmcnt(2)\n\t"
    "v_lshl_add_u32 v84, v8, 3, s36\n\t"
    "v_lshl_add_u32 v85, v9, 3, s37\n\t"
    "v_lshl_add_u32 v86, v10, 3, s38\n\t"
    "v_lshl_add_u32 v87, v11, 3, s39\n\t"
    "v_lshl_add_u32 v88, v12, 3, s40\n\t"
    "v_lshl_add_u32 v89, v13, 3, s41\n\t"
    "v_lshl_add_u32 v90, v14, 3, s42\n\t"
    "v_lshl_add_u32 v91, v15, 3, s43\n\t"
    "s_waitcnt lgkmcnt(4)\n\t"
    "v_and_b32 v118, v118, v110\n\t"
    "v_perm_b32 v118, v118, v118, s46\n\t"
    "ds_or_b32 v92, v118\n\t"
    "v_and_b32 v119, v119, v111\n\t"
    "v_perm_b32 v119, v119, v119, s46\n\t"
    "ds_or_b32 v93, v119\n\t"
    "v_and_b32 v120, v120, v112\n\t"
    "v_perm_b32 v120, v120, v120, s46\n\t"
    "ds_or_b32 v94, v120\n\t"
    "v_and_b32 v121, v121, v113\n\t"
    "v_perm_b32 v121, v121, v121, s46\n\t"
    "ds_or_b32 v95, v121\n\t"
    "s_waitcnt lgkmcnt(4)\n\t"
    "v_and_b32 v122, v122, v114\n\t"
    "v_perm_b32 v122, v122, v122, s46\n\t"
    "ds_or_b32 v96, v122\n\t"
    "v_and_b32 v123, v123, v115\n\t"
    "v_perm_b32 v123, v123, v123, s46\n\t"
    "ds_or_b32 v97, v123\n\t"
    "v_and_b32 v124, v124, v116\n\t"
    "v_perm_b32 v124, v124, v124, s46\n\t"
    "ds_or_b32 v98, v124\n\t"
    "v_and_b32 v125, v125, v117\n\t"
    "v_perm_b32 v125, v125, v125, s46\n\t"
    "ds_or_b32 v99, v125\n\t"
    "buffer_store_dwordx4 v[164:167], v66, %12, s49 offen offset:0 sc1\n\t"
    "v_and_or_b32 v100, v84, s44, v68\n\t"
    "ds_read_b32 v100, v100\n\t"
    "v_and_or_b32 v101, v85, s44, v68\n\t"
    "ds_read_b32 v101, v101\n\t"
    "v_and_or_b32 v102, v86, s44, v68\n\t"
    "ds_read_b32 v102, v102\n\t"
    "v_and_or_b32 v103, v87, s44, v68\n\t"
    "ds_read_b32 v103, v103\n\t"
    "v_and_or_b32 v104, v88, s44, v68\n\t"
    "ds_read_b32 v104, v104\n\t"
    "v_and_or_b32 v105, v89, s44, v68\n\t"
    "ds_read_b32 v105, v105\n\t"
    "v_and_or_b32 v106, v90, s44, v68\n\t"
    "ds_read_b32 v106, v106\n\t"
    "v_and_or_b32 v107, v91, s44, v68\n\t"
    "ds_read_b32 v107, v107\n\t"
    "s_waitcnt vmcnt(1)\n\t"
    "v_lshl_add_u32 v76, v16, 3, s36\n\t"
    "v_lshl_add_u32 v77, v17, 3, s37\n\t"
    "v_lshl_add_u32 v78, v18, 3, s38\n\t"
    "v_lshl_add_u32 v79, v19, 3, s39\n\t"
    "v_lshl_add_u32 v80, v20, 3, s40\n\t"
    "v_lshl_add_u32 v81, v21, 3, s41\n\t"
    "v_lshl_add_u32 v82, v22, 3, s42\n\t"
    "v_lshl_add_u32 v83, v23, 3, s43\n\t"
    "s_movk_i32 s50, 3200\n\t"
    "buffer_load_dwordx4 v[24:27], v64, %0, s50 offen offset:1616\n\t"
    "buffer_load_dwordx4 v[28:31], v64, %0, s50 offen offset:1632\n\t"
    "s_movk_i32 s50, 6400\n\t"
    "buffer_load_dwordx4 v[32:35], v64, %0, s50 offen offset:16\n\t"
    "buffer_load_dwordx4 v[36:39], v64, %0, s50 offen offset:32\n\t"
    "v_mov_b32 v129, 0xffff0000\n\t"
    "s_waitcnt lgkmcnt(4)\n\t"
    "v_perm_b32 v100, v100, v84, v126\n\t"
    "v_perm_b32 v101, v101, v85, v126\n\t"
    "v_perm_b32 v102, v102, v86, v126\n\t"
    "v_perm_b32 v103, v103, v87, v126\n\t"
    "s_waitcnt lgkmcnt(0)\n\t"
    "v_perm_b32 v104, v104, v88, v126\n\t"
    "v_perm_b32 v105, v105, v89, v126\n\t"
    "v_perm_b32 v106, v106, v90, v126\n\t"
    "v_perm_b32 v107, v107, v91, v126\n\t"
    "v_max_u32 v108, v100, v101\n\t"
    "v_min_u32 v101, v100, v101\n\t"
    "v_max_u32 v100, v102, v103\n\t"
    "v_min_u32 v103, v102, v103\n\t"
    "v_max_u32 v102, v104, v105\n\t"
    "v_min_u32 v105, v104, v105\n\t"
    "v_max_u32 v104, v106, v107\n\t"
    "v_min_u32 v107, v106, v107\n\t"
    "v_max_u32 v106, v108, v100\n\t"
    "v_min_u32 v100, v108, v100\n\t"
    "v_max_u32 v108, v101, v103\n\t"
    "v_min_u32 v103, v101, v103\n\t"
    "v_max_u32 v101, v102, v104\n\t"
    "v_min_u32 v104, v102, v104\n\t"
    "v_max_u32 v102, v105, v107\n\t"
    "v_min_u32 v107, v105, v107\n\t"
    "v_bfi_b32 v130, v106, 0, v129\n\t"
    "v_cmp_eq_u32 vcc, 0, v130\n\t"
    "v_and_or_b32 v92, v106, s44, v68\n\t"
    "s_nop 0\n\t"
    "v_cndmask_b32 v130, v130, v129, vcc\n\t"
    "v_ffbl_b32 v131, v130\n\t"
    "v_and_b32 v134, 7, v131\n\t"
    "v_lshlrev_b32 v134, v134, s45\n\t"
    "v_xor_b32 v129, v129, v134\n\t"
    "v_xor_b32 v132, v131, v127\n\t"
    "v_bfi_b32 v130, v101, 0, v129\n\t"
    "v_cmp_eq_u32 vcc, 0, v130\n\t"
    "v_and_or_b32 v93, v101, s44, v68\n\t"
    "v_xor_b32 v133, 23, v132\n\t"
    "v_lshlrev_b32 v110, v132, v136\n\t"
    "v_and_b32 v132, 7, v132\n\t"
    "v_lshl_or_b32 v110, v136, v133, v110\n\t"
    "v_lshl_add_u32 v132, v132, 1, v128\n\t"
    "ds_or_rtn_b32 v118, v92, v110\n\t"
    "ds_write_b16 v132, v106\n\t"
    "v_cndmask_b32 v130, v130, v129, vcc\n\t"
    "v_ffbl_b32 v131, v130\n\t"
    "v_and_b32 v134, 7, v131\n\t"
    "v_lshlrev_b32 v134, v134, s45\n\t"
    "v_xor_b32 v129, v129, v134\n\t"
    "v_xor_b32 v132, v131, v127\n\t"
    "v_bfi_b32 v130, v108, 0, v129\n\t"
    "v_cmp_eq_u32 vcc, 0, v130\n\t"
    "v_and_or_b32 v94, v108, s44, v68\n\t"
    "v_xor_b32 v133, 23, v132\n\t"
    "v_lshlrev_b32 v111, v132, v136\n\t"
    "v_and_b32 v132, 7, v132\n\t"
    "v_lshl_or_b32 v111, v136, v133, v111\n\t"
    "v_lshl_add_u32 v132, v132, 1, v128\n\t"
    "ds_or_rtn_b32 v119, v93, v111\n\t"
    "ds_write_b16 v132, v101\n\t"
    "v_cndmask_b32 v130, v130, v129, vcc\n\t"
    "v_ffbl_b32 v131, v130\n\t"
    "v_and_b32 v134, 7, v131\n\t"
    "v_lshlrev_b32 v134, v134, s45\n\t"
    "v_xor_b32 v129, v129, v134\n\t"
    "v_xor_b32 v132, v131, v127\n\t"
    "v_bfi_b32 v130, v100, 0, v129\n\t"
    "v_cmp_eq_u32 vcc, 0, v130\n\t"
    "v_and_or_b32 v95, v100, s44, v68\n\t"
    "v_xor_b32 v133, 23, v132\n\t"
    "v_lshlrev_b32 v112, v132, v136\n\t"
    "v_and_b32 v132, 7, v132\n\t"
    "v_lshl_or_b32 v112, v136, v133, v112\n\t"
    "v_lshl_add_u32 v132, v132, 1, v128\n\t"
    "ds_or_rtn_b32 v120, v94, v112\n\t"
    "ds_write_b16 v132, v108\n\t"
    "v_cndmask_b32 v130, v130, v129, vcc\n\t"
    "v_ffbl_b32 v131, v130\n\t"
    "v_and_b32 v134, 7, v131\n\t"
    "v_lshlrev_b32 v134, v134, s45\n\t"
    "v_xor_b32 v129, v129, v134\n\t"
    "v_xor_b32 v132, v131, v127\n\t"
    "v_bfi_b32 v130, v102, 0, v129\n\t"
    "v_cmp_eq_u32 vcc, 0, v130\n\t"
    "v_and_or_b32 v96, v102, s44, v68\n\t"
    "v_xor_b32 v133, 23, v132\n\t"
    "v_lshlrev_b32 v113, v132, v136\n\t"
    "v_and_b32 v132, 7, v132\n\t"
    "v_lshl_or_b32 v113, v136, v133, v113\n\t"
    "v_lshl_add_u32 v132, v132, 1, v128\n\t"
    "ds_or_rtn_b32 v121, v95, v113\n\t"
    "ds_write_b16 v132, v100\n\t"
    "v_cndmask_b32 v130, v130, v129, vcc\n\t"
    "v_ffbl_b32 v131, v130\n\t"
    "v_and_b32 v134, 7, v131\n\t"
    "v_lshlrev_b32 v134, v134, s45\n\t"
    "v_xor_b32 v129, v129, v134\n\t"
    "v_xor_b32 v132, v131, v127\n\t"
    "s_waitcnt lgkmcnt(7)\n\t"
    "v_and_b32 v118, v118, v110\n\t"
    "v_perm_b32 v118, v118, v118, s46\n\t"
    "ds_or_b32 v92, v118\n\t"
    "v_bfi_b32 v130, v104, 0, v129\n\t"
    "v_cmp_eq_u32 vcc, 0, v130\n\t"
    "v_and_or_b32 v97, v104, s44, v68\n\t"
    "v_xor_b32 v133, 23, v132\n\t"
    "v_lshlrev_b32 v114, v132, v136\n\t"
    "v_and_b32 v132, 7, v132\n\t"
    "v_lshl_or_b32 v114, v136, v133, v114\n\t"
    "v_lshl_add_u32 v132, v132, 1, v128\n\t"
    "ds_or_rtn_b32 v122, v96, v114\n\t"
    "ds_write_b16 v132, v102\n\t"
    "v_cndmask_b32 v130, v130, v129, vcc\n\t"
    "v_ffbl_b32 v131, v130\n\t"
    "v_and_b32 v134, 7, v131\n\t"
    "v_lshlrev_b32 v134, v134, s45\n\t"
    "v_xor_b32 v129, v129, v134\n\t"
    "v_xor_b32 v132, v131, v127\n\t"
    "s_waitcnt lgkmcnt(8)\n\t"
    "v_and_b32 v119, v119, v111\n\t"
    "v_perm_b32 v119, v119, v119, s46\n\t"
    "ds_or_b32 v93, v119\n\t"
    "v_bfi_b32 v130, v103, 0, v129\n\t"
    "v_cmp_eq_u32 vcc, 0, v130\n\t"
    "v_and_or_b32 v98, v103, s44, v68\n\t"
    "v_xor_b32 v133, 23, v132\n\t"
    "v_lshlrev_b32 v115, v132, v136\n\t"
    "v_and_b32 v132, 7, v132\n\t"
    "v_lshl_or_b32 v115, v136, v133, v115\n\t"
    "v_lshl_add_u32 v132, v132, 1, v128\n\t"
    "ds_or_rtn_b32 v123, v97, v115\n\t"
    "ds_write_b16 v132, v104\n\t"
    "v_cndmask_b32 v130, v130, v129, vcc\n\t"
    "v_ffbl_b32 v131, v130\n\t"
    "v_and_b32 v134, 7, v131\n\t"
    "v_lshlrev_b32 v134, v134, s45\n\t"
    "v_xor_b32 v129, v129, v134\n\t"
    "v_xor_b32 v132, v131, v127\n\t"
    "s_waitcnt lgkmcnt(9)\n\t"
    "v_and_b32 v120, v120, v112\n\t"
    "v_perm_b32 v120, v120, v120, s46\n\t"
    "ds_or_b32 v94, v120\n\t"
    "v_bfi_b32 v130, v107, 0, v129\n\t"
    "v_cmp_eq_u32 vcc, 0, v130\n\t"
    "v_and_or_b32 v99, v107, s44, v68\n\t"
    "v_xor_b32 v133, 23, v132\n\t"
    "v_lshlrev_b32 v116, v132, v136\n\t"
    "v_and_b32 v132, 7, v132\n\t"
    "v_lshl_or_b32 v116, v136, v133, v116\n\t"
    "v_lshl_add_u32 v132, v132, 1, v128\n\t"
    "ds_or_rtn_b32 v124, v98, v116\n\t"
    "ds_write_b16 v132, v103\n\t"
    "v_cndmask_b32 v130, v130, v129, vcc\n\t"
    "v_ffbl_b32 v131, v130\n\t"
    "v_xor_b32 v132, v131, v127\n\t"
    "s_waitcnt lgkmcnt(10)\n\t"
    "v_and_b32 v121, v121, v113\n\t"
    "v_perm_b32 v121, v121, v121, s46\n\t"
    "ds_or_b32 v95, v121\n\t"
    "v_xor_b32 v133, 23, v132\n\t"
    "v_lshlrev_b32 v117, v132, v136\n\t"
    "v_and_b32 v132, 7, v132\n\t"
    "v_lshl_or_b32 v117, v136, v133, v117\n\t"
    "v_lshl_add_u32 v132, v132, 1, v128\n\t"
    "ds_or_rtn_b32 v125, v99, v117\n\t"
    "ds_write_b16 v132, v107\n\t"
    "s_waitcnt lgkmcnt(10)\n\t"
    "v_and_b32 v122, v122, v114\n\t"
    "v_perm_b32 v122, v122, v122, s46\n\t"
    "ds_or_b32 v96, v122\n\t"
    "ds_read_b128 v[72:75], v128\n\t"
    "v_and_or_b32 v100, v76, s44, v68\n\t"
    "ds_read_b32 v100, v100\n\t"
    "v_and_or_b32 v101, v77, s44, v68\n\t"
    "ds_read_b32 v101, v101\n\t"
    "v_and_or_b32 v102, v78, s44, v68\n\t"
    "ds_read_b32 v102, v102\n\t"
    "v_and_or_b32 v103, v79, s44, v68\n\t"
    "ds_read_b32 v103, v103\n\t"
    "v_and_or_b32 v104, v80, s44, v68\n\t"
    "ds_read_b32 v104, v104\n\t"
    "v_and_or_b32 v105, v81, s44, v68\n\t"
    "ds_read_b32 v105, v105\n\t"
    "v_and_or_b32 v106, v82, s44, v68\n\t"
    "ds_read_b32 v106, v106\n\t"
    "v_and_or_b32 v107, v83, s44, v68\n\t"
    "ds_read_b32 v107, v107\n\t"
    "s_waitcnt vmcnt(2)\n\t"
    "v_lshl_add_u32 v84, v24, 3, s36\n\t"
    "v_lshl_add_u32 v85, v25, 3, s37\n\t"
    "v_lshl_add_u32 v86, v26, 3, s38\n\t"
    "v_lshl_add_u32 v87, v27, 3, s39\n\t"
    "v_lshl_add_u32 v88, v28, 3, s40\n\t"
    "v_lshl_add_u32 v89, v29, 3, s41\n\t"
    "v_lshl_add_u32 v90, v30, 3, s42\n\t"
    "v_lshl_add_u32 v91, v31, 3, s43\n\t"
    "s_movk_i32 s50, 6400\n\t"
    "buffer_load_dwordx4 v[40:43], v65, %0, s50 offen offset:1616\n\t"
    "buffer_load_dwordx4 v[44:47], v65, %0, s50 offen offset:1632\n\t"
    "s_movk_i32 s50, 6400\n\t"
    "buffer_load_dwordx4 v[48:51], v65, %0, s50 offen offset:2416\n\t"
    "buffer_load_dwordx4 v[52:55], v65, %0, s50 offen offset:2432\n\t"
    "s_movk_i32 s50, 9600\n\t"
    "buffer_load_dwordx4 v[56:59], v65, %0, s50 offen offset:16\n\t"
    "buffer_load_dwordx4 v[60:63], v65, %0, s50 offen offset:32\n\t"
    "s_movk_i32 s50, 9600\n\t"
    "buffer_load_dwordx4 v[0:3], v65, %0, s50 offen offset:816\n\t"
    "buffer_load_dwordx4 v[4:7], v65, %0, s50 offen offset:832\n\t"
    "s_movk_i32 s50, 9600\n\t"
    "buffer_load_dwordx4 v[8:11], v65, %0, s50 offen offset:1616\n\t"
    "buffer_load_dwordx4 v[12:15], v65, %0, s50 offen offset:1632\n\t"
    "v_mov_b32 v134, 1032\n\t"
    "L_asvsp8_%=:\n\t"
    "ds_read_b32 v130, v134\n\t"
    "s_waitcnt lgkmcnt(0)\n\t"
    "v_readfirstlane_b32 s50, v130\n\t"
    "s_nop 0\n\t"
    "s_cmp_lt_u32 s50, 6\n\t"
    "s_cbranch_scc1 L_asvsp8_%=\n\t"
    "s_mov_b64 s[96:97], %2\n\t"
    "s_and_b32 s97, s97, 0xffff\n\t"
    "buffer_load_dwordx4 v[156:159], v137, s[96:99], s47 offen offset:0\n\t"
    "buffer_load_dwordx4 v[160:163], v138, s[96:99], s47 offen offset:0\n\t"
    "buffer_load_dwordx4 v[164:167], v137, s[96:99], s47 offen offset:64\n\t"
    "buffer_load_dwordx4 v[168:171], v138, s[96:99], s47 offen offset:64\n\t"
    "buffer_load_dwordx4 v[172:175], v137, s[96:99], s47 offen offset:128\n\t"
    "buffer_load_dwordx4 v[176:179], v138, s[96:99], s47 offen offset:128\n\t"
    "buffer_load_dwordx4 v[180:183], v137, s[96:99], s47 offen offset:192\n\t"
    "buffer_load_dwordx4 v[184:187], v138, s[96:99], s47 offen offset:192\n\t"
    "s_add_u32 s47, s47, 2097152\n\t"
    "buffer_load_dwordx4 v[188:191], v137, s[96:99], s47 offen offset:0\n\t"
    "buffer_load_dwordx4 v[192:195], v138, s[96:99], s47 offen offset:0\n\t"
    "buffer_load_dwordx4 v[196:199], v137, s[96:99], s47 offen offset:64\n\t"
    "buffer_load_dwordx4 v[200:203], v138, s[96:99], s47 offen offset:64\n\t"
    "buffer_load_dwordx4 v[204:207], v137, s[96:99], s47 offen offset:128\n\t"
    "buffer_load_dwordx4 v[208:211], v138, s[96:99], s47 offen offset:128\n\t"
    "buffer_load_dwordx4 v[212:215], v137, s[96:99], s47 offen offset:192\n\t"
    "buffer_load_dwordx4 v[216:219], v138, s[96:99], s47 offen offset:192\n\t"
    "s_add_u32 s47, s47, 2097152\n\t"
    "v_and_b32 v123, v123, v115\n\t"
    "v_perm_b32 v123, v123, v123, s46\n\t"
    "ds_or_b32 v97, v123\n\t"
    "v_and_b32 v124, v124, v116\n\t"
    "v_perm_b32 v124, v124, v124, s46\n\t"
    "ds_or_b32 v98, v124\n\t"
    "v_and_b32 v125, v125, v117\n\t"
    "v_perm_b32 v125, v125, v125, s46\n\t"
    "ds_or_b32 v99, v125\n\t"
    "buffer_store_dwordx4 v[72:75], v66, %12, s49 offen offset:64 sc1\n\t"
    "v_mov_b32 v129, 0xffff0000\n\t"
    "v_perm_b32 v100, v100, v76, v126\n\t"
    "v_perm_b32 v101, v101, v77, v126\n\t"
    "v_perm_b32 v102, v102, v78, v126\n\t"
    "v_perm_b32 v103, v103, v79, v126\n\t"
    "v_perm_b32 v104, v104, v80, v126\n\t"
    "v_perm_b32 v105, v105, v81, v126\n\t"
    "v_perm_b32 v106, v106, v82, v126\n\t"
    "v_perm_b32 v107, v107, v83, v126\n\t"
    "v_max_u32 v108, v100, v101\n\t"
    "v_min_u32 v101, v100, v101\n\t"
    "v_max_u32 v100, v102, v103\n\t"
    "v_min_u32 v103, v102, v103\n\t"
    "v_max_u32 v102, v104, v105\n\t"
    "v_min_u32 v105, v104, v105\n\t"
    "v_max_u32 v104, v106, v107\n\t"
    "v_min_u32 v107, v106, v107\n\t"
    "v_max_u32 v106, v108, v100\n\t"
    "v_min_u32 v100, v108, v100\n\t"
    "v_max_u32 v108, v101, v103\n\t"
    "v_min_u32 v103, v101, v103\n\t"
    "v_max_u32 v101, v102, v104\n\t"
    "v_min_u32 v104, v102, v104\n\t"
    "v_max_u32 v102, v105, v107\n\t"
    "v_min_u32 v107, v105, v107\n\t"
    "v_bfi_b32 v130, v106, 0, v129\n\t"
    "v_cmp_eq_u32 vcc, 0, v130\n\t"
    "v_and_or_b32 v92, v106, s44, v68\n\t"
    "s_nop 0\n\t"
    "v_cndmask_b32 v130, v130, v129, vcc\n\t"
    "v_ffbl_b32 v131, v130\n\t"
    "v_and_b32 v134, 7, v131\n\t"
    "v_lshlrev_b32 v134, v134, s45\n\t"
    "v_xor_b32 v129, v129, v134\n\t"
    "v_xor_b32 v132, v131, v127\n\t"
    "v_bfi_b32 v130, v101, 0, v129\n\t"
    "v_cmp_eq_u32 vcc, 0, v130\n\t"
    "v_and_or_b32 v93, v101, s44, v68\n\t"
    "v_xor_b32 v133, 23, v132\n\t"
    "v_lshlrev_b32 v110, v132, v136\n\t"
    "v_and_b32 v132, 7, v132\n\t"
    "v_lshl_or_b32 v110, v136, v133, v110\n\t"
    "v_lshl_add_u32 v132, v132, 1, v128\n\t"
    "ds_or_rtn_b32 v118, v92, v110\n\t"
    "ds_write_b16 v132, v106\n\t"
    "v_cndmask_b32 v130, v130, v129, vcc\n\t"
    "v_ffbl_b32 v131, v130\n\t"
    "v_and_b32 v134, 7, v131\n\t"
    "v_lshlrev_b32 v134, v134, s45\n\t"
    "v_xor_b32 v129, v129, v134\n\t"
    "v_xor_b32 v132, v131, v127\n\t"
    "v_bfi_b32 v130, v108, 0, v129\n\t"
    "v_cmp_eq_u32 vcc, 0, v130\n\t"
    "v_and_or_b32 v94, v108, s44, v68\n\t"
    "v_xor_b32 v133, 23, v132\n\t"
    "v_lshlrev_b32 v111, v132, v136\n\t"
    "v_and_b32 v132, 7, v132\n\t"
    "v_lshl_or_b32 v111, v136, v133, v111\n\t"
    "v_lshl_add_u32 v132, v132, 1, v128\n\t"
    "ds_or_rtn_b32 v119, v93, v111\n\t"
    "ds_write_b16 v132, v101\n\t"
    "v_cndmask_b32 v130, v130, v129, vcc\n\t"
    "v_ffbl_b32 v131, v130\n\t"
    "v_and_b32 v134, 7, v131\n\t"
    "v_lshlrev_b32 v134, v134, s45\n\t"
    "v_xor_b32 v129, v129, v134\n\t"
    "v_xor_b32 v132, v131, v127\n\t"
    "v_bfi_b32 v130, v100, 0, v129\n\t"
    "v_cmp_eq_u32 vcc, 0, v130\n\t"
    "v_and_or_b32 v95, v100, s44, v68\n\t"
    "v_xor_b32 v133, 23, v132\n\t"
    "v_lshlrev_b32 v112, v132, v136\n\t"
    "v_and_b32 v132, 7, v132\n\t"
    "v_lshl_or_b32 v112, v136, v133, v112\n\t"
    "v_lshl_add_u32 v132, v132, 1, v128\n\t"
    "ds_or_rtn_b32 v120, v94, v112\n\t"
    "ds_write_b16 v132, v108\n\t"
    "v_cndmask_b32 v130, v130, v129, vcc\n\t"
    "v_ffbl_b32 v131, v130\n\t"
    "v_and_b32 v134, 7, v131\n\t"
    "v_lshlrev_b32 v134, v134, s45\n\t"
    "v_xor_b32 v129, v129, v134\n\t"
    "v_xor_b32 v132, v131, v127\n\t"
    "v_bfi_b32 v130, v102, 0, v129\n\t"
    "v_cmp_eq_u32 vcc, 0, v130\n\t"
    "v_and_or_b32 v96, v102, s44, v68\n\t"
    "v_xor_b32 v133, 23, v132\n\t"
    "v_lshlrev_b32 v113, v132, v136\n\t"
    "v_and_b32 v132, 7, v132\n\t"
    "v_lshl_or_b32 v113, v136, v133, v113\n\t"
    "v_lshl_add_u32 v132, v132, 1, v128\n\t"
    "ds_or_rtn_b32 v121, v95, v113\n\t"
    "ds_write_b16 v132, v100\n\t"
    "v_cndmask_b32 v130, v130, v129, vcc\n\t"
    "v_ffbl_b32 v131, v130\n\t"
    "v_and_b32 v134, 7, v131\n\t"
    "v_lshlrev_b32 v134, v134, s45\n\t"
    "v_xor_b32 v129, v129, v134\n\t"
    "v_xor_b32 v132, v131, v127\n\t"
    "s_waitcnt lgkmcnt(7)\n\t"
    "v_and_b32 v118, v118, v110\n\t"
    "v_perm_b32 v118, v118, v118, s46\n\t"
    "ds_or_b32 v92, v118\n\t"
    "v_bfi_b32 v130, v104, 0, v129\n\t"
    "v_cmp_eq_u32 vcc, 0, v130\n\t"
    "v_and_or_b32 v97, v104, s44, v68\n\t"
    "v_xor_b32 v133, 23, v132\n\t"
    "v_lshlrev_b32 v114, v132, v136\n\t"
    "v_and_b32 v132, 7, v132\n\t"
    "v_lshl_or_b32 v114, v136, v133, v114\n\t"
    "v_lshl_add_u32 v132, v132, 1, v128\n\t"
    "ds_or_rtn_b32 v122, v96, v114\n\t"
    "ds_write_b16 v132, v102\n\t"
    "v_cndmask_b32 v130, v130, v129, vcc\n\t"
    "v_ffbl_b32 v131, v130\n\t"
    "v_and_b32 v134, 7, v131\n\t"
    "v_lshlrev_b32 v134, v134, s45\n\t"
    "v_xor_b32 v129, v129, v134\n\t"
    "v_xor_b32 v132, v131, v127\n\t"
    "s_waitcnt lgkmcnt(8)\n\t"
    "v_and_b32 v119, v119, v111\n\t"
    "v_perm_b32 v119, v119, v119, s46\n\t"
    "ds_or_b32 v93, v119\n\t"
    "v_bfi_b32 v130, v103, 0, v129\n\t"
    "v_cmp_eq_u32 vcc, 0, v130\n\t"
    "v_and_or_b32 v98, v103, s44, v68\n\t"
    "v_xor_b32 v133, 23, v132\n\t"
    "v_lshlrev_b32 v115, v132, v136\n\t"
    "v_and_b32 v132, 7, v132\n\t"
    "v_lshl_or_b32 v115, v136, v133, v115\n\t"
    "v_lshl_add_u32 v132, v132, 1, v128\n\t"
    "ds_or_rtn_b32 v123, v97, v115\n\t"
    "ds_write_b16 v132, v104\n\t"
    "v_cndmask_b32 v130, v130, v129, vcc\n\t"
    "v_ffbl_b32 v131, v130\n\t"
    "v_and_b32 v134, 7, v131\n\t"
    "v_lshlrev_b32 v134, v134, s45\n\t"
    "v_xor_b32 v129, v129, v134\n\t"
    "v_xor_b32 v132, v131, v127\n\t"
    "s_waitcnt lgkmcnt(9)\n\t"
    "v_and_b32 v120, v120, v112\n\t"
    "v_perm_b32 v120, v120, v120, s46\n\t"
    "ds_or_b32 v94, v120\n\t"
    "v_bfi_b32 v130, v107, 0, v129\n\t"
    "v_cmp_eq_u32 vcc, 0, v130\n\t"
    "v_and_or_b32 v99, v107, s44, v68\n\t"
    "v_xor_b32 v133, 23, v132\n\t"
    "v_lshlrev_b32 v116, v132, v136\n\t"
    "v_and_b32 v132, 7, v132\n\t"
    "v_lshl_or_b32 v116, v136, v133, v116\n\t"
    "v_lshl_add_u32 v132, v132, 1, v128\n\t"
    "ds_or_rtn_b32 v124, v98, v116\n\t"
    "ds_write_b16 v132, v103\n\t"
    "v_cndmask_b32 v130, v130, v129, vcc\n\t"
    "v_ffbl_b32 v131, v130\n\t"
    "v_xor_b32 v132, v131, v127\n\t"
    "s_waitcnt lgkmcnt(10)\n\t"
    "v_and_b32 v121, v121, v113\n\t"
    "v_perm_b32 v121, v121, v121, s46\n\t"
    "ds_or_b32 v95, v121\n\t"
    "v_xor_b32 v133, 23, v132\n\t"
    "v_lshlrev_b32 v117, v132, v136\n\t"
    "v_and_b32 v132, 7, v132\n\t"
    "v_lshl_or_b32 v117, v136, v133, v117\n\t"
    "v_lshl_add_u32 v132, v132, 1, v128\n\t"
    "ds_or_rtn_b32 v125, v99, v117\n\t"
    "ds_write_b16 v132, v107\n\t"
    "s_waitcnt lgkmcnt(10)\n\t"
    "v_and_b32 v122, v122, v114\n\t"
    "v_perm_b32 v122, v122, v122, s46\n\t"
    "ds_or_b32 v96, v122\n\t"
    "ds_read_b128 v[72:75], v128\n\t"
    "v_and_or_b32 v100, v84, s44, v68\n\t"
    "ds_read_b32 v100, v100\n\t"
    "v_and_or_b32 v101, v85, s44, v68\n\t"
    "ds_read_b32 v101, v101\n\t"
    "v_and_or_b32 v102, v86, s44, v68\n\t"
    "ds_read_b32 v102, v102\n\t"
    "v_and_or_b32 v103, v87, s44, v68\n\t"
    "ds_read_b32 v103, v103\n\t"
    "v_and_or_b32 v104, v88, s44, v68\n\t"
    "ds_read_b32 v104, v104\n\t"
    "v_and_or_b32 v105, v89, s44, v68\n\t"
    "ds_read_b32 v105, v105\n\t"
    "v_and_or_b32 v106, v90, s44, v68\n\t"
    "ds_read_b32 v106, v106\n\t"
    "v_and_or_b32 v107, v91, s44, v68\n\t"
    "ds_read_b32 v107, v107\n\t"
    "s_waitcnt vmcnt(27)\n\t"
    "v_lshl_add_u32 v76, v32, 3, s36\n\t"
    "v_lshl_add_u32 v77, v33, 3, s37\n\t"
    "v_lshl_add_u32 v78, v34, 3, s38\n\t"
    "v_lshl_add_u32 v79, v35, 3, s39\n\t"
    "v_lshl_add_u32 v80, v36, 3, s40\n\t"
    "v_lshl_add_u32 v81, v37, 3, s41\n\t"
    "v_lshl_add_u32 v82, v38, 3, s42\n\t"
    "v_lshl_add_u32 v83, v39, 3, s43\n\t"
    "s_movk_i32 s50, 9600\n\t"
    "buffer_load_dwordx4 v[16:19], v65, %0, s50 offen offset:2416\n\t"
    "buffer_load_dwordx4 v[20:23], v65, %0, s50 offen offset:2432\n\t"
    "buffer_load_dwordx4 v[220:223], v137, s[96:99], s47 offen offset:0\n\t"
    "buffer_load_dwordx4 v[224:227], v138, s[96:99], s47 offen offset:0\n\t"
    "buffer_load_dwordx4 v[228:231], v137, s[96:99], s47 offen offset:64\n\t"
    "buffer_load_dwordx4 v[232:235], v138, s[96:99], s47 offen offset:64\n\t"
    "buffer_load_dwordx4 v[236:239], v137, s[96:99], s47 offen offset:128\n\t"
    "buffer_load_dwordx4 v[240:243], v138, s[96:99], s47 offen offset:128\n\t"
    "buffer_load_dwordx4 v[244:247], v137, s[96:99], s47 offen offset:192\n\t"
    "buffer_load_dwordx4 v[248:251], v138, s[96:99], s47 offen offset:192\n\t"
    "s_add_u32 s47, s47, 2097152\n\t"
    "buffer_load_dwordx4 v[24:27], v137, s[96:99], s47 offen offset:0\n\t"
    "buffer_load_dwordx4 v[28:31], v138, s[96:99], s47 offen offset:0\n\t"
    "buffer_load_dwordx4 v[32:35], v137, s[96:99], s47 offen offset:64\n\t"
    "buffer_load_dwordx4 v[36:39], v138, s[96:99], s47 offen offset:64\n\t"
    "s_waitcnt lgkmcnt(11)\n\t"
    "v_and_b32 v123, v123, v115\n\t"
    "v_perm_b32 v123, v123, v123, s46\n\t"
    "ds_or_b32 v97, v123\n\t"
    "v_and_b32 v124, v124, v116\n\t"
    "v_perm_b32 v124, v124, v124, s46\n\t"
    "ds_or_b32 v98, v124\n\t"
    "v_and_b32 v125, v125, v117\n\t"
    "v_perm_b32 v125, v125, v125, s46\n\t"
    "ds_or_b32 v99, v125\n\t"
    "s_waitcnt lgkmcnt(11)\n\t"
    "buffer_store_dwordx4 v[72:75], v66, %12, s49 offen offset:128 sc1\n\t"
    "v_mov_b32 v129, 0xffff0000\n\t"
    "s_waitcnt lgkmcnt(7)\n\t"
    "v_perm_b32 v100, v100, v84, v126\n\t"
    "v_perm_b32 v101, v101, v85, v126\n\t"
    "v_perm_b32 v102, v102, v86, v126\n\t"
    "v_perm_b32 v103, v103, v87, v126\n\t"
    "s_waitcnt lgkmcnt(3)\n\t"
    "v_perm_b32 v104, v104, v88, v126\n\t"
    "v_perm_b32 v105, v105, v89, v126\n\t"
    "v_perm_b32 v106, v106, v90, v126\n\t"
    "v_perm_b32 v107, v107, v91, v126\n\t"
    "v_max_u32 v108, v100, v101\n\t"
    "v_min_u32 v101, v100, v101\n\t"
    "v_max_u32 v100, v102, v103\n\t"
    "v_min_u32 v103, v102, v103\n\t"
    "v_max_u32 v102, v104, v105\n\t"
    "v_min_u32 v105, v104, v105\n\t"
    "v_max_u32 v104, v106, v107\n\t"
    "v_min_u32 v107, v106, v107\n\t"
    "v_max_u32 v106, v108, v100\n\t"
    "v_min_u32 v100, v108, v100\n\t"
    "v_max_u32 v108, v101, v103\n\t"
    "v_min_u32 v103, v101, v103\n\t"
    "v_max_u32 v101, v102, v104\n\t"
    "v_min_u32 v104, v102, v104\n\t"
    "v_max_u32 v102, v105, v107\n\t"
    "v_min_u32 v107, v105, v107\n\t"
    "v_bfi_b32 v130, v106, 0, v129\n\t"
    "v_cmp_eq_u32 vcc, 0, v130\n\t"
    "v_and_or_b32 v92, v106, s44, v68\n\t"
    "s_nop 0\n\t"
    "v_cndmask_b32 v130, v130, v129, vcc\n\t"
    "v_ffbl_b32 v131, v130\n\t"
    "v_and_b32 v134, 7, v131\n\t"
    "v_lshlrev_b32 v134, v134, s45\n\t"
    "v_xor_b32 v129, v129, v134\n\t"
    "v_xor_b32 v132, v131, v127\n\t"
    "v_bfi_b32 v130, v101, 0, v129\n\t"
    "v_cmp_eq_u32 vcc, 0, v130\n\t"
    "v_and_or_b32 v93, v101, s44, v68\n\t"
    "v_xor_b32 v133, 23, v132\n\t"
    "v_lshlrev_b32 v110, v132, v136\n\t"
    "v_and_b32 v132, 7, v132\n\t"
    "v_lshl_or_b32 v110, v136, v133, v110\n\t"
    "v_lshl_add_u32 v132, v132, 1, v128\n\t"
    "ds_or_rtn_b32 v118, v92, v110\n\t"
    "ds_write_b16 v132, v106\n\t"
    "v_cndmask_b32 v130, v130, v129, vcc\n\t"
    "v_ffbl_b32 v131, v130\n\t"
    "v_and_b32 v134, 7, v131\n\t"
    "v_lshlrev_b32 v134, v134, s45\n\t"
    "v_xor_b32 v129, v129, v134\n\t"
    "v_xor_b32 v132, v131, v127\n\t"
    "v_bfi_b32 v130, v108, 0, v129\n\t"
    "v_cmp_eq_u32 vcc, 0, v130\n\t"
    "v_and_or_b32 v94, v108, s44, v68\n\t"
    "v_xor_b32 v133, 23, v132\n\t"
    "v_lshlrev_b32 v111, v132, v136\n\t"
    "v_and_b32 v132, 7, v132\n\t"
    "v_lshl_or_b32 v111, v136, v133, v111\n\t"
    "v_lshl_add_u32 v132, v132, 1, v128\n\t"
    "ds_or_rtn_b32 v119, v93, v111\n\t"
    "ds_write_b16 v132, v101\n\t"
    "v_cndmask_b32 v130, v130, v129, vcc\n\t"
    "v_ffbl_b32 v131, v130\n\t"
    "v_and_b32 v134, 7, v131\n\t"
    "v_lshlrev_b32 v134, v134, s45\n\t"
    "v_xor_b32 v129, v129, v134\n\t"
    "v_xor_b32 v132, v131, v127\n\t"
    "v_bfi_b32 v130, v100, 0, v129\n\t"
    "v_cmp_eq_u32 vcc, 0, v130\n\t"
    "v_and_or_b32 v95, v100, s44, v68\n\t"
    "v_xor_b32 v133, 23, v132\n\t"
    "v_lshlrev_b32 v112, v132, v136\n\t"
    "v_and_b32 v132, 7, v132\n\t"
    "v_lshl_or_b32 v112, v136, v133, v112\n\t"
    "v_lshl_add_u32 v132, v132, 1, v128\n\t"
    "ds_or_rtn_b32 v120, v94, v112\n\t"
    "ds_write_b16 v132, v108\n\t"
    "v_cndmask_b32 v130, v130, v129, vcc\n\t"
    "v_ffbl_b32 v131, v130\n\t"
    "v_and_b32 v134, 7, v131\n\t"
    "v_lshlrev_b32 v134, v134, s45\n\t"
    "v_xor_b32 v129, v129, v134\n\t"
    "v_xor_b32 v132, v131, v127\n\t"
    "v_bfi_b32 v130, v102, 0, v129\n\t"
    "v_cmp_eq_u32 vcc, 0, v130\n\t"
    "v_and_or_b32 v96, v102, s44, v68\n\t"
    "v_xor_b32 v133, 23, v132\n\t"
    "v_lshlrev_b32 v113, v132, v136\n\t"
    "v_and_b32 v132, 7, v132\n\t"
    "v_lshl_or_b32 v113, v136, v133, v113\n\t"
    "v_lshl_add_u32 v132, v132, 1, v128\n\t"
    "ds_or_rtn_b32 v121, v95, v113\n\t"
    "ds_write_b16 v132, v100\n\t"
    "v_cndmask_b32 v130, v130, v129, vcc\n\t"
    "v_ffbl_b32 v131, v130\n\t"
    "v_and_b32 v134, 7, v131\n\t"
    "v_lshlrev_b32 v134, v134, s45\n\t"
    "v_xor_b32 v129, v129, v134\n\t"
    "v_xor_b32 v132, v131, v127\n\t"
    "s_waitcnt lgkmcnt(7)\n\t"
    "v_and_b32 v118, v118, v110\n\t"
    "v_perm_b32 v118, v118, v118, s46\n\t"
    "ds_or_b32 v92, v118\n\t"
    "v_bfi_b32 v130, v104, 0, v129\n\t"
    "v_cmp_eq_u32 vcc, 0, v130\n\t"
    "v_and_or_b32 v97, v104, s44, v68\n\t"
    "v_xor_b32 v133, 23, v132\n\t"
    "v_lshlrev_b32 v114, v132, v136\n\t"
    "v_and_b32 v132, 7, v132\n\t"
    "v_lshl_or_b32 v114, v136, v133, v114\n\t"
    "v_lshl_add_u32 v132, v132, 1, v128\n\t"
    "ds_or_rtn_b32 v122, v96, v114\n\t"
    "ds_write_b16 v132, v102\n\t"
    "v_cndmask_b32 v130, v130, v129, vcc\n\t"
    "v_ffbl_b32 v131, v130\n\t"
    "v_and_b32 v134, 7, v131\n\t"
    "v_lshlrev_b32 v134, v134, s45\n\t"
    "v_xor_b32 v129, v129, v134\n\t"
    "v_xor_b32 v132, v131, v127\n\t"
    "s_waitcnt lgkmcnt(8)\n\t"
    "v_and_b32 v119, v119, v111\n\t"
    "v_perm_b32 v119, v119, v119, s46\n\t"
    "ds_or_b32 v93, v119\n\t"
    "v_bfi_b32 v130, v103, 0, v129\n\t"
    "v_cmp_eq_u32 vcc, 0, v130\n\t"
    "v_and_or_b32 v98, v103, s44, v68\n\t"
    "v_xor_b32 v133, 23, v132\n\t"
    "v_lshlrev_b32 v115, v132, v136\n\t"
    "v_and_b32 v132, 7, v132\n\t"
    "v_lshl_or_b32 v115, v136, v133, v115\n\t"
    "v_lshl_add_u32 v132, v132, 1, v128\n\t"
    "ds_or_rtn_b32 v123, v97, v115\n\t"
    "ds_write_b16 v132, v104\n\t"
    "v_cndmask_b32 v130, v130, v129, vcc\n\t"
    "v_ffbl_b32 v131, v130\n\t"
    "v_and_b32 v134, 7, v131\n\t"
    "v_lshlrev_b32 v134, v134, s45\n\t"
    "v_xor_b32 v129, v129, v134\n\t"
    "v_xor_b32 v132, v131, v127\n\t"
    "s_waitcnt lgkmcnt(9)\n\t"
    "v_and_b32 v120, v120, v112\n\t"
    "v_perm_b32 v120, v120, v120, s46\n\t"
    "ds_or_b32 v94, v120\n\t"
    "v_bfi_b32 v130, v107, 0, v129\n\t"
    "v_cmp_eq_u32 vcc, 0, v130\n\t"
    "v_and_or_b32 v99, v107, s44, v68\n\t"
    "v_xor_b32 v133, 23, v132\n\t"
    "v_lshlrev_b32 v116, v132, v136\n\t"
    "v_and_b32 v132, 7, v132\n\t"
    "v_lshl_or_b32 v116, v136, v133, v116\n\t"
    "v_lshl_add_u32 v132, v132, 1, v128\n\t"
    "ds_or_rtn_b32 v124, v98, v116\n\t"
    "ds_write_b16 v132, v103\n\t"
    "v_cndmask_b32 v130, v130, v129, vcc\n\t"
    "v_ffbl_b32 v131, v130\n\t"
    "v_xor_b32 v132, v131, v127\n\t"
    "s_waitcnt lgkmcnt(10)\n\t"
    "v_and_b32 v121, v121, v113\n\t"
    "v_perm_b32 v121, v121, v121, s46\n\t"
    "ds_or_b32 v95, v121\n\t"
    "v_xor_b32 v133, 23, v132\n\t"
    "v_lshlrev_b32 v117, v132, v136\n\t"
    "v_and_b32 v132, 7, v132\n\t"
    "v_lshl_or_b32 v117, v136, v133, v117\n\t"
    "v_lshl_add_u32 v132, v132, 1, v128\n\t"
    "ds_or_rtn_b32 v125, v99, v117\n\t"
    "ds_write_b16 v132, v107\n\t"
    "s_waitcnt lgkmcnt(10)\n\t"
    "v_and_b32 v122, v122, v114\n\t"
    "v_perm_b32 v122, v122, v122, s46\n\t"
    "ds_or_b32 v96, v122\n\t"
    "ds_read_b128 v[72:75], v128\n\t"
    "v_and_or_b32 v100, v76, s44, v68\n\t"
    "ds_read_b32 v100, v100\n\t"
    "v_and_or_b32 v101, v77, s44, v68\n\t"
    "ds_read_b32 v101, v101\n\t"
    "v_and_or_b32 v102, v78, s44, v68\n\t"
    "ds_read_b32 v102, v102\n\t"
    "v_and_or_b32 v103, v79, s44, v68\n\t"
    "ds_read_b32 v103, v103\n\t"
    "v_and_or_b32 v104, v80, s44, v68\n\t"
    "ds_read_b32 v104, v104\n\t"
    "v_and_or_b32 v105, v81, s44, v68\n\t"
    "ds_read_b32 v105, v105\n\t"
    "v_and_or_b32 v106, v82, s44, v68\n\t"
    "ds_read_b32 v106, v106\n\t"
    "v_and_or_b32 v107, v83, s44, v68\n\t"
    "ds_read_b32 v107, v107\n\t"
    "s_waitcnt vmcnt(40)\n\t"
    "v_lshl_add_u32 v84, v40, 3, s36\n\t"
    "v_lshl_add_u32 v85, v41, 3, s37\n\t"
    "v_lshl_add_u32 v86, v42, 3, s38\n\t"
    "v_lshl_add_u32 v87, v43, 3, s39\n\t"
    "v_lshl_add_u32 v88, v44, 3, s40\n\t"
    "v_lshl_add_u32 v89, v45, 3, s41\n\t"
    "v_lshl_add_u32 v90, v46, 3, s42\n\t"
    "v_lshl_add_u32 v91, v47, 3, s43\n\t"
    "buffer_load_dwordx4 v[40:43], v137, s[96:99], s47 offen offset:128\n\t"
    "buffer_load_dwordx4 v[44:47], v138, s[96:99], s47 offen offset:128\n\t"
    "s_waitcnt lgkmcnt(11)\n\t"
    "v_and_b32 v123, v123, v115\n\t"
    "v_perm_b32 v123, v123, v123, s46\n\t"
    "ds_or_b32 v97, v123\n\t"
    "v_and_b32 v124, v124, v116\n\t"
    "v_perm_b32 v124, v124, v124, s46\n\t"
    "ds_or_b32 v98, v124\n\t"
    "v_and_b32 v125, v125, v117\n\t"
    "v_perm_b32 v125, v125, v125, s46\n\t"
    "ds_or_b32 v99, v125\n\t"
    "s_waitcnt lgkmcnt(11)\n\t"
    "buffer_store_dwordx4 v[72:75], v66, %12, s49 offen offset:192 sc1\n\t"
    "v_mov_b32 v129, 0xffff0000\n\t"
    "s_waitcnt lgkmcnt(7)\n\t"
    "v_perm_b32 v100, v100, v76, v126\n\t"
    "v_perm_b32 v101, v101, v77, v126\n\t"
    "v_perm_b32 v102, v102, v78, v126\n\t"
    "v_perm_b32 v103, v103, v79, v126\n\t"
    "s_waitcnt lgkmcnt(3)\n\t"
    "v_perm_b32 v104, v104, v80, v126\n\t"
    "v_perm_b32 v105, v105, v81, v126\n\t"
    "v_perm_b32 v106, v106, v82, v126\n\t"
    "v_perm_b32 v107, v107, v83, v126\n\t"
    "s_waitcnt vmcnt(33)\n\t"
    "ds_write_b128 v141, v[156:159] offset:0\n\t"
    "ds_write_b128 v141, v[160:163] offset:256\n\t"
    "ds_read_b64_tr_b16 v[156:157], v142 offset:0\n\t"
    "ds_read_b64_tr_b16 v[158:159], v142 offset:256\n\t"
    "ds_read_b64_tr_b16 v[160:161], v142 offset:32\n\t"
    "ds_read_b64_tr_b16 v[162:163], v142 offset:288\n\t"
    "s_waitcnt vmcnt(31)\n\t"
    "ds_write_b128 v141, v[164:167] offset:2112\n\t"
    "ds_write_b128 v141, v[168:171] offset:2368\n\t"
    "ds_read_b64_tr_b16 v[164:165], v142 offset:2112\n\t"
    "ds_read_b64_tr_b16 v[166:167], v142 offset:2368\n\t"
    "ds_read_b64_tr_b16 v[168:169], v142 offset:2144\n\t"
    "ds_read_b64_tr_b16 v[170:171], v142 offset:2400\n\t"
    "v_max_u32 v108, v100, v101\n\t"
    "v_min_u32 v101, v100, v101\n\t"
    "v_max_u32 v100, v102, v103\n\t"
    "v_min_u32 v103, v102, v103\n\t"
    "v_max_u32 v102, v104, v105\n\t"
    "v_min_u32 v105, v104, v105\n\t"
    "v_max_u32 v104, v106, v107\n\t"
    "v_min_u32 v107, v106, v107\n\t"
    "v_max_u32 v106, v108, v100\n\t"
    "v_min_u32 v100, v108, v100\n\t"
    "v_max_u32 v108, v101, v103\n\t"
    "v_min_u32 v103, v101, v103\n\t"
    "v_max_u32 v101, v102, v104\n\t"
    "v_min_u32 v104, v102, v104\n\t"
    "v_max_u32 v102, v105, v107\n\t"
    "v_min_u32 v107, v105, v107\n\t"
    "v_bfi_b32 v130, v106, 0, v129\n\t"
    "v_cmp_eq_u32 vcc, 0, v130\n\t"
    "v_and_or_b32 v92, v106, s44, v68\n\t"
    "s_nop 0\n\t"
    "v_cndmask_b32 v130, v130, v129, vcc\n\t"
    "v_ffbl_b32 v131, v130\n\t"
    "v_and_b32 v134, 7, v131\n\t"
    "v_lshlrev_b32 v134, v134, s45\n\t"
    "v_xor_b32 v129, v129, v134\n\t"
    "v_xor_b32 v132, v131, v127\n\t"
    "v_bfi_b32 v130, v101, 0, v129\n\t"
    "v_cmp_eq_u32 vcc, 0, v130\n\t"
    "v_and_or_b32 v93, v101, s44, v68\n\t"
    "v_xor_b32 v133, 23, v132\n\t"
    "v_lshlrev_b32 v110, v132, v136\n\t"
    "v_and_b32 v132, 7, v132\n\t"
    "v_lshl_or_b32 v110, v136, v133, v110\n\t"
    "v_lshl_add_u32 v132, v132, 1, v128\n\t"
    "ds_or_rtn_b32 v118, v92, v110\n\t"
    "ds_write_b16 v132, v106\n\t"
    "v_cndmask_b32 v130, v130, v129, vcc\n\t"
    "v_ffbl_b32 v131, v130\n\t"
    "v_and_b32 v134, 7, v131\n\t"
    "v_lshlrev_b32 v134, v134, s45\n\t"
    "v_xor_b32 v129, v129, v134\n\t"
    "v_xor_b32 v132, v131, v127\n\t"
    "s_waitcnt lgkmcnt(8)\n\t"
    "buffer_store_dwordx4 v[156:159], v139, %12, s48 offen sc1\n\t"
    "buffer_store_dwordx4 v[160:163], v140, %12, s48 offen sc1\n\t"
    "s_add_u32 s48, s48, 819200\n\t"
    "s_waitcnt lgkmcnt(2)\n\t"
    "buffer_store_dwordx4 v[164:167], v139, %12, s48 offen sc1\n\t"
    "buffer_store_dwordx4 v[168:171], v140, %12, s48 offen sc1\n\t"
    "s_add_u32 s48, s48, 819200\n\t"
    "s_waitcnt vmcnt(33)\n\t"
    "ds_write_b128 v141, v[172:175] offset:0\n\t"
    "ds_write_b128 v141, v[176:179] offset:256\n\t"
    "ds_read_b64_tr_b16 v[172:173], v142 offset:0\n\t"
    "ds_read_b64_tr_b16 v[174:175], v142 offset:256\n\t"
    "ds_read_b64_tr_b16 v[176:177], v142 offset:32\n\t"
    "ds_read_b64_tr_b16 v[178:179], v142 offset:288\n\t"
    "s_waitcnt vmcnt(31)\n\t"
    "ds_write_b128 v141, v[180:183] offset:2112\n\t"
    "ds_write_b128 v141, v[184:187] offset:2368\n\t"
    "ds_read_b64_tr_b16 v[180:181], v142 offset:2112\n\t"
    "ds_read_b64_tr_b16 v[182:183], v142 offset:2368\n\t"
    "ds_read_b64_tr_b16 v[184:185], v142 offset:2144\n\t"
    "ds_read_b64_tr_b16 v[186:187], v142 offset:2400\n\t"
    "v_bfi_b32 v130, v108, 0, v129\n\t"
    "v_cmp_eq_u32 vcc, 0, v130\n\t"
    "v_and_or_b32 v94, v108, s44, v68\n\t"
    "v_xor_b32 v133, 23, v132\n\t"
    "v_lshlrev_b32 v111, v132, v136\n\t"
    "v_and_b32 v132, 7, v132\n\t"
    "v_lshl_or_b32 v111, v136, v133, v111\n\t"
    "v_lshl_add_u32 v132, v132, 1, v128\n\t"
    "ds_or_rtn_b32 v119, v93, v111\n\t"
    "ds_write_b16 v132, v101\n\t"
    "v_cndmask_b32 v130, v130, v129, vcc\n\t"
    "v_ffbl_b32 v131, v130\n\t"
    "v_and_b32 v134, 7, v131\n\t"
    "v_lshlrev_b32 v134, v134, s45\n\t"
    "v_xor_b32 v129, v129, v134\n\t"
    "v_xor_b32 v132, v131, v127\n\t"
    "v_bfi_b32 v130, v100, 0, v129\n\t"
    "v_cmp_eq_u32 vcc, 0, v130\n\t"
    "v_and_or_b32 v95, v100, s44, v68\n\t"
    "v_xor_b32 v133, 23, v132\n\t"
    "v_lshlrev_b32 v112, v132, v136\n\t"
    "v_and_b32 v132, 7, v132\n\t"
    "v_lshl_or_b32 v112, v136, v133, v112\n\t"
    "v_lshl_add_u32 v132, v132, 1, v128\n\t"
    "ds_or_rtn_b32 v120, v94, v112\n\t"
    "ds_write_b16 v132, v108\n\t"
    "v_cndmask_b32 v130, v130, v129, vcc\n\t"
    "v_ffbl_b32 v131, v130\n\t"
    "v_and_b32 v134, 7, v131\n\t"
    "v_lshlrev_b32 v134, v134, s45\n\t"
    "v_xor_b32 v129, v129, v134\n\t"
    "v_xor_b32 v132, v131, v127\n\t"
    "s_waitcnt lgkmcnt(10)\n\t"
    "buffer_store_dwordx4 v[172:175], v139, %12, s48 offen sc1\n\t"
    "buffer_store_dwordx4 v[176:179], v140, %12, s48 offen sc1\n\t"
    "s_add_u32 s48, s48, 819200\n\t"
    "s_waitcnt lgkmcnt(4)\n\t"
    "buffer_store_dwordx4 v[180:183], v139, %12, s48 offen sc1\n\t"
    "buffer_store_dwordx4 v[184:187], v140, %12, s48 offen sc1\n\t"
    "s_add_u32 s48, s48, -2441216\n\t"
    "s_waitcnt vmcnt(33)\n\t"
    "ds_write_b128 v141, v[188:191] offset:0\n\t"
    "ds_write_b128 v141, v[192:195] offset:256\n\t"
    "ds_read_b64_tr_b16 v[188:189], v142 offset:0\n\t"
    "ds_read_b64_tr_b16 v[190:191], v142 offset:256\n\t"
    "ds_read_b64_tr_b16 v[192:193], v142 offset:32\n\t"
    "ds_read_b64_tr_b16 v[194:195], v142 offset:288\n\t"
    "s_waitcnt vmcnt(31)\n\t"
    "ds_write_b128 v141, v[196:199] offset:2112\n\t"
    "ds_write_b128 v141, v[200:203] offset:2368\n\t"
    "ds_read_b64_tr_b16 v[196:197], v142 offset:2112\n\t"
    "ds_read_b64_tr_b16 v[198:199], v142 offset:2368\n\t"
    "ds_read_b64_tr_b16 v[200:201], v142 offset:2144\n\t"
    "ds_read_b64_tr_b16 v[202:203], v142 offset:2400\n\t"
    "v_bfi_b32 v130, v102, 0, v129\n\t"
    "v_cmp_eq_u32 vcc, 0, v130\n\t"
    "v_and_or_b32 v96, v102, s44, v68\n\t"
    "v_xor_b32 v133, 23, v132\n\t"
    "v_lshlrev_b32 v113, v132, v136\n\t"
    "v_and_b32 v132, 7, v132\n\t"
    "v_lshl_or_b32 v113, v136, v133, v113\n\t"
    "v_lshl_add_u32 v132, v132, 1, v128\n\t"
    "ds_or_rtn_b32 v121, v95, v113\n\t"
    "ds_write_b16 v132, v100\n\t"
    "v_cndmask_b32 v130, v130, v129, vcc\n\t"
    "v_ffbl_b32 v131, v130\n\t"
    "v_and_b32 v134, 7, v131\n\t"
    "v_lshlrev_b32 v134, v134, s45\n\t"
    "v_xor_b32 v129, v129, v134\n\t"
    "v_xor_b32 v132, v131, v127\n\t"
    "v_and_b32 v118, v118, v110\n\t"
    "v_perm_b32 v118, v118, v118, s46\n\t"
    "ds_or_b32 v92, v118\n\t"
    "s_lshl_b32 s52, %14, 2\n\t"
    "s_add_u32 s52, s52, 49291264\n\t"
    "buffer_load_dword v71, off, %12, s52 offset:1024 sc1\n\t"
    "v_bfi_b32 v130, v104, 0, v129\n\t"
    "v_cmp_eq_u32 vcc, 0, v130\n\t"
    "v_and_or_b32 v97, v104, s44, v68\n\t"
    "v_xor_b32 v133, 23, v132\n\t"
    "v_lshlrev_b32 v114, v132, v136\n\t"
    "v_and_b32 v132, 7, v132\n\t"
    "v_lshl_or_b32 v114, v136, v133, v114\n\t"
    "v_lshl_add_u32 v132, v132, 1, v128\n\t"
    "ds_or_rtn_b32 v122, v96, v114\n\t"
    "ds_write_b16 v132, v102\n\t"
    "v_cndmask_b32 v130, v130, v129, vcc\n\t"
    "v_ffbl_b32 v131, v130\n\t"
    "v_and_b32 v134, 7, v131\n\t"
    "v_lshlrev_b32 v134, v134, s45\n\t"
    "v_xor_b32 v129, v129, v134\n\t"
    "v_xor_b32 v132, v131, v127\n\t"
    "s_waitcnt lgkmcnt(15)\n\t"
    "v_and_b32 v119, v119, v111\n\t"
    "v_perm_b32 v119, v119, v119, s46\n\t"
    "ds_or_b32 v93, v119\n\t"
    "s_waitcnt lgkmcnt(12)\n\t"
    "buffer_store_dwordx4 v[188:191], v139, %12, s48 offen sc1\n\t"
    "buffer_store_dwordx4 v[192:195], v140, %12, s48 offen sc1\n\t"
    "s_add_u32 s48, s48, 819200\n\t"
    "s_waitcnt lgkmcnt(6)\n\t"
    "buffer_store_dwordx4 v[196:199], v139, %12, s48 offen sc1\n\t"
    "buffer_store_dwordx4 v[200:203], v140, %12, s48 offen sc1\n\t"
    "s_add_u32 s48, s48, 819200\n\t"
    "s_waitcnt vmcnt(34)\n\t"
    "ds_write_b128 v141, v[204:207] offset:0\n\t"
    "ds_write_b128 v141, v[208:211] offset:256\n\t"
    "ds_read_b64_tr_b16 v[204:205], v142 offset:0\n\t"
    "ds_read_b64_tr_b16 v[206:207], v142 offset:256\n\t"
    "ds_read_b64_tr_b16 v[208:209], v142 offset:32\n\t"
    "ds_read_b64_tr_b16 v[210:211], v142 offset:288\n\t"
    "s_waitcnt vmcnt(32)\n\t"
    "ds_write_b128 v141, v[212:215] offset:2112\n\t"
    "ds_write_b128 v141, v[216:219] offset:2368\n\t"
    "ds_read_b64_tr_b16 v[212:213], v142 offset:2112\n\t"
    "ds_read_b64_tr_b16 v[214:215], v142 offset:2368\n\t"
    "ds_read_b64_tr_b16 v[216:217], v142 offset:2144\n\t"
    "ds_read_b64_tr_b16 v[218:219], v142 offset:2400\n\t"
    "v_bfi_b32 v130, v103, 0, v129\n\t"
    "v_cmp_eq_u32 vcc, 0, v130\n\t"
    "v_and_or_b32 v98, v103, s44, v68\n\t"
    "v_xor_b32 v133, 23, v132\n\t"
    "v_lshlrev_b32 v115, v132, v136\n\t"
    "v_and_b32 v132, 7, v132\n\t"
    "v_lshl_or_b32 v115, v136, v133, v115\n\t"
    "v_lshl_add_u32 v132, v132, 1, v128\n\t"
    "ds_or_rtn_b32 v123, v97, v115\n\t"
    "ds_write_b16 v132, v104\n\t"
    "v_cndmask_b32 v130, v130, v129, vcc\n\t"
    "v_ffbl_b32 v131, v130\n\t"
    "v_and_b32 v134, 7, v131\n\t"
    "v_lshlrev_b32 v134, v134, s45\n\t"
    "v_xor_b32 v129, v129, v134\n\t"
    "v_xor_b32 v132, v131, v127\n\t"
    "v_and_b32 v120, v120, v112\n\t"
    "v_perm_b32 v120, v120, v120, s46\n\t"
    "ds_or_b32 v94, v120\n\t"
    "v_bfi_b32 v130, v107, 0, v129\n\t"
    "v_cmp_eq_u32 vcc, 0, v130\n\t"
    "v_and_or_b32 v99, v107, s44, v68\n\t"
    "v_xor_b32 v133, 23, v132\n\t"
    "v_lshlrev_b32 v116, v132, v136\n\t"
    "v_and_b32 v132, 7, v132\n\t"
    "v_lshl_or_b32 v116, v136, v133, v116\n\t"
    "v_lshl_add_u32 v132, v132, 1, v128\n\t"
    "ds_or_rtn_b32 v124, v98, v116\n\t"
    "ds_write_b16 v132, v103\n\t"
    "v_cndmask_b32 v130, v130, v129, vcc\n\t"
    "v_ffbl_b32 v131, v130\n\t"
    "v_xor_b32 v132, v131, v127\n\t"
    "s_waitcnt lgkmcnt(15)\n\t"
    "v_and_b32 v121, v121, v113\n\t"
    "v_perm_b32 v121, v121, v121, s46\n\t"
    "ds_or_b32 v95, v121\n\t"
    "v_xor_b32 v133, 23, v132\n\t"
    "v_lshlrev_b32 v117, v132, v136\n\t"
    "v_and_b32 v132, 7, v132\n\t"
    "v_lshl_or_b32 v117, v136, v133, v117\n\t"
    "v_lshl_add_u32 v132, v132, 1, v128\n\t"
    "ds_or_rtn_b32 v125, v99, v117\n\t"
    "ds_write_b16 v132, v107\n\t"
    "v_and_b32 v122, v122, v114\n\t"
    "v_perm_b32 v122, v122, v122, s46\n\t"
    "ds_or_b32 v96, v122\n\t"
    "ds_read_b128 v[72:75], v128\n\t"
    "s_waitcnt lgkmcnt(15)\n\t"
    "buffer_store_dwordx4 v[204:207], v139, %12, s48 offen sc1\n\t"
    "buffer_store_dwordx4 v[208:211], v140, %12, s48 offen sc1\n\t"
    "s_add_u32 s48, s48, 819200\n\t"
    "s_waitcnt lgkmcnt(10)\n\t"
    "buffer_store_dwordx4 v[212:215], v139, %12, s48 offen sc1\n\t"
    "buffer_store_dwordx4 v[216:219], v140, %12, s48 offen sc1\n\t"
    "s_add_u32 s48, s48, -2441216\n\t"
    "v_mov_b32 v134, 1036\n\t"
    "L_asvsp12_%=:\n\t"
    "ds_read_b32 v130, v134\n\t"
    "s_waitcnt lgkmcnt(0)\n\t"
    "v_readfirstlane_b32 s50, v130\n\t"
    "s_nop 0\n\t"
    "s_cmp_lt_u32 s50, 1\n\t"
    "s_cbranch_scc1 L_asvsp12_%=\n\t"
    "v_and_or_b32 v100, v84, s44, v69\n\t"
    "ds_read_b32 v100, v100\n\t"
    "v_and_or_b32 v101, v85, s44, v69\n\t"
    "ds_read_b32 v101, v101\n\t"
    "v_and_or_b32 v102, v86, s44, v69\n\t"
    "ds_read_b32 v102, v102\n\t"
    "v_and_or_b32 v103, v87, s44, v69\n\t"
    "ds_read_b32 v103, v103\n\t"
    "v_and_or_b32 v104, v88, s44, v69\n\t"
    "ds_read_b32 v104, v104\n\t"
    "v_and_or_b32 v105, v89, s44, v69\n\t"
    "ds_read_b32 v105, v105\n\t"
    "v_and_or_b32 v106, v90, s44, v69\n\t"
    "ds_read_b32 v106, v106\n\t"
    "v_and_or_b32 v107, v91, s44, v69\n\t"
    "ds_read_b32 v107, v107\n\t"
    "v_lshl_add_u32 v76, v48, 3, s36\n\t"
    "v_lshl_add_u32 v77, v49, 3, s37\n\t"
    "v_lshl_add_u32 v78, v50, 3, s38\n\t"
    "v_lshl_add_u32 v79, v51, 3, s39\n\t"
    "v_lshl_add_u32 v80, v52, 3, s40\n\t"
    "v_lshl_add_u32 v81, v53, 3, s41\n\t"
    "v_lshl_add_u32 v82, v54, 3, s42\n\t"
    "v_lshl_add_u32 v83, v55, 3, s43\n\t"
    "v_mov_b32 v134, 2076\n\t"
    "L_asvsp1052_%=:\n\t"
    "ds_read_b32 v130, v134\n\t"
    "s_waitcnt lgkmcnt(0)\n\t"
    "v_readfirstlane_b32 s50, v130\n\t"
    "s_nop 0\n\t"
    "s_cmp_lt_u32 s50, 1\n\t"
    "s_cbranch_scc1 L_asvsp1052_%=\n\t"
    "v_mov_b32 v70, 2072\n\t"
    "ds_read_b32 v70, v70\n\t"
    "s_waitcnt vmcnt(8)\n\t"
    "s_waitcnt vmcnt(0)\n\t"
    "s_waitcnt lgkmcnt(0)\n\t"
    "v_cmp_ne_u32 vcc, v71, v70\n\t"
    "s_cbranch_vccz L_ahsok_%=\n\t"
    "s_mov_b32 s51, 1048576\n\t"
    "L_ahs_%=:\n\t"
    "buffer_load_dword v71, off, %12, s52 offset:1024 sc1\n\t"
    "s_sub_u32 s51, s51, 1\n\t"
    "s_waitcnt vmcnt(0)\n\t"
    "s_cmp_eq_u32 s51, 0\n\t"
    "s_cbranch_scc1 L_ahsok_%=\n\t"
    "v_cmp_ne_u32 vcc, v71, v70\n\t"
    "s_cbranch_vccnz L_ahs_%=\n\t"
    "L_ahsok_%=:\n\t"
    "s_lshr_b32 s50, %14, 5\n\t"
    "s_lshl_b32 s50, s50, 7\n\t"
    "s_add_u32 s50, s50, 49284096\n\t"
    "s_mov_b64 exec, 1\n\t"
    "buffer_atomic_add v136, off, %12, s50 sc1\n\t"
    "s_mov_b64 exec, -1\n\t"
    "buffer_load_dwordx4 v[156:159], v137, s[96:99], s47 offen offset:192\n\t"
    "buffer_load_dwordx4 v[160:163], v138, s[96:99], s47 offen offset:192\n\t"
    "s_add_u32 s47, s47, 2097152\n\t"
    "buffer_load_dwordx4 v[164:167], v137, s[96:99], s47 offen offset:0\n\t"
    "buffer_load_dwordx4 v[168:171], v138, s[96:99], s47 offen offset:0\n\t"
    "buffer_load_dwordx4 v[172:175], v137, s[96:99], s47 offen offset:64\n\t"
    "buffer_load_dwordx4 v[176:179], v138, s[96:99], s47 offen offset:64\n\t"
    "buffer_load_dwordx4 v[180:183], v137, s[96:99], s47 offen offset:128\n\t"
    "buffer_load_dwordx4 v[184:187], v138, s[96:99], s47 offen offset:128\n\t"
    "buffer_load_dwordx4 v[188:191], v137, s[96:99], s47 offen offset:192\n\t"
    "buffer_load_dwordx4 v[192:195], v138, s[96:99], s47 offen offset:192\n\t"
    "s_add_u32 s47, s47, 2097152\n\t"
    "buffer_load_dwordx4 v[196:199], v137, s[96:99], s47 offen offset:0\n\t"
    "buffer_load_dwordx4 v[200:203], v138, s[96:99], s47 offen offset:0\n\t"
    "v_and_b32 v123, v123, v115\n\t"
    "v_perm_b32 v123, v123, v123, s46\n\t"
    "ds_or_b32 v97, v123\n\t"
    "v_and_b32 v124, v124, v116\n\t"
    "v_perm_b32 v124, v124, v124, s46\n\t"
    "ds_or_b32 v98, v124\n\t"
    "v_and_b32 v125, v125, v117\n\t"
    "v_perm_b32 v125, v125, v125, s46\n\t"
    "ds_or_b32 v99, v125\n\t"
    "buffer_store_dwordx4 v[72:75], v66, %12, s49 offen offset:256 sc1\n\t"
    "v_mov_b32 v129, 0xffff0000\n\t"
    "v_perm_b32 v100, v100, v84, v126\n\t"
    "v_perm_b32 v101, v101, v85, v126\n\t"
    "v_perm_b32 v102, v102, v86, v126\n\t"
    "v_perm_b32 v103, v103, v87, v126\n\t"
    "v_perm_b32 v104, v104, v88, v126\n\t"
    "v_perm_b32 v105, v105, v89, v126\n\t"
    "v_perm_b32 v106, v106, v90, v126\n\t"
    "v_perm_b32 v107, v107, v91, v126\n\t"
    "ds_write_b128 v141, v[220:223] offset:0\n\t"
    "ds_write_b128 v141, v[224:227] offset:256\n\t"
    "ds_read_b64_tr_b16 v[220:221], v142 offset:0\n\t"
    "ds_read_b64_tr_b16 v[222:223], v142 offset:256\n\t"
    "ds_read_b64_tr_b16 v[224:225], v142 offset:32\n\t"
    "ds_read_b64_tr_b16 v[226:227], v142 offset:288\n\t"
    "ds_write_b128 v141, v[228:231] offset:2112\n\t"
    "ds_write_b128 v141, v[232:235] offset:2368\n\t"
    "ds_read_b64_tr_b16 v[228:229], v142 offset:2112\n\t"
    "ds_read_b64_tr_b16 v[230:231], v142 offset:2368\n\t"
    "ds_read_b64_tr_b16 v[232:233], v142 offset:2144\n\t"
    "ds_read_b64_tr_b16 v[234:235], v142 offset:2400\n\t"
    "v_max_u32 v108, v100, v101\n\t"
    "v_min_u32 v101, v100, v101\n\t"
    "v_max_u32 v100, v102, v103\n\t"
    "v_min_u32 v103, v102, v103\n\t"
    "v_max_u32 v102, v104, v105\n\t"
    "v_min_u32 v105, v104, v105\n\t"
    "v_max_u32 v104, v106, v107\n\t"
    "v_min_u32 v107, v106, v107\n\t"
    "v_max_u32 v106, v108, v100\n\t"
    "v_min_u32 v100, v108, v100\n\t"
    "v_max_u32 v108, v101, v103\n\t"
    "v_min_u32 v103, v101, v103\n\t"
    "v_max_u32 v101, v102, v104\n\t"
    "v_min_u32 v104, v102, v104\n\t"
    "v_max_u32 v102, v105, v107\n\t"
    "v_min_u32 v107, v105, v107\n\t"
    "v_bfi_b32 v130, v106, 0, v129\n\t"
    "v_cmp_eq_u32 vcc, 0, v130\n\t"
    "v_and_or_b32 v92, v106, s44, v69\n\t"
    "s_nop 0\n\t"
    "v_cndmask_b32 v130, v130, v129, vcc\n\t"
    "v_ffbl_b32 v131, v130\n\t"
    "v_and_b32 v134, 7, v131\n\t"
    "v_lshlrev_b32 v134, v134, s45\n\t"
    "v_xor_b32 v129, v129, v134\n\t"
    "v_xor_b32 v132, v131, v127\n\t"
    "v_bfi_b32 v130, v101, 0, v129\n\t"
    "v_cmp_eq_u32 vcc, 0, v130\n\t"
    "v_and_or_b32 v93, v101, s44, v69\n\t"
    "v_xor_b32 v133, 23, v132\n\t"
    "v_lshlrev_b32 v110, v132, v136\n\t"
    "v_and_b32 v132, 7, v132\n\t"
    "v_lshl_or_b32 v110, v136, v133, v110\n\t"
    "v_lshl_add_u32 v132, v132, 1, v128\n\t"
    "ds_or_rtn_b32 v118, v92, v110\n\t"
    "ds_write_b16 v132, v106\n\t"
    "v_cndmask_b32 v130, v130, v129, vcc\n\t"
    "v_ffbl_b32 v131, v130\n\t"
    "v_and_b32 v134, 7, v131\n\t"
    "v_lshlrev_b32 v134, v134, s45\n\t"
    "v_xor_b32 v129, v129, v134\n\t"
    "v_xor_b32 v132, v131, v127\n\t"
    "v_bfi_b32 v130, v108, 0, v129\n\t"
    "v_cmp_eq_u32 vcc, 0, v130\n\t"
    "v_and_or_b32 v94, v108, s44, v69\n\t"
    "v_xor_b32 v133, 23, v132\n\t"
    "v_lshlrev_b32 v111, v132, v136\n\t"
    "v_and_b32 v132, 7, v132\n\t"
    "v_lshl_or_b32 v111, v136, v133, v111\n\t"
    "v_lshl_add_u32 v132, v132, 1, v128\n\t"
    "ds_or_rtn_b32 v119, v93, v111\n\t"
    "ds_write_b16 v132, v101\n\t"
    "v_cndmask_b32 v130, v130, v129, vcc\n\t"
    "v_ffbl_b32 v131, v130\n\t"
    "v_and_b32 v134, 7, v131\n\t"
    "v_lshlrev_b32 v134, v134, s45\n\t"
    "v_xor_b32 v129, v129, v134\n\t"
    "v_xor_b32 v132, v131, v127\n\t"
    "s_waitcnt lgkmcnt(10)\n\t"
    "buffer_store_dwordx4 v[220:223], v139, %12, s48 offen sc1\n\t"
    "buffer_store_dwordx4 v[224:227], v140, %12, s48 offen sc1\n\t"
    "s_add_u32 s48, s48, 819200\n\t"
    "s_waitcnt lgkmcnt(4)\n\t"
    "buffer_store_dwordx4 v[228:231], v139, %12, s48 offen sc1\n\t"
    "buffer_store_dwordx4 v[232:235], v140, %12, s48 offen sc1\n\t"
    "s_add_u32 s48, s48, 819200\n\t"
    "ds_write_b128 v141, v[236:239] offset:0\n\t"
    "ds_write_b128 v141, v[240:243] offset:256\n\t"
    "ds_read_b64_tr_b16 v[236:237], v142 offset:0\n\t"
    "ds_read_b64_tr_b16 v[238:239], v142 offset:256\n\t"
    "ds_read_b64_tr_b16 v[240:241], v142 offset:32\n\t"
    "ds_read_b64_tr_b16 v[242:243], v142 offset:288\n\t"
    "ds_write_b128 v141, v[244:247] offset:2112\n\t"
    "ds_write_b128 v141, v[248:251] offset:2368\n\t"
    "ds_read_b64_tr_b16 v[244:245], v142 offset:2112\n\t"
    "ds_read_b64_tr_b16 v[246:247], v142 offset:2368\n\t"
    "ds_read_b64_tr_b16 v[248:249], v142 offset:2144\n\t"
    "ds_read_b64_tr_b16 v[250:251], v142 offset:2400\n\t"
    "v_bfi_b32 v130, v100, 0, v129\n\t"
    "v_cmp_eq_u32 vcc, 0, v130\n\t"
    "v_and_or_b32 v95, v100, s44, v69\n\t"
    "v_xor_b32 v133, 23, v132\n\t"
    "v_lshlrev_b32 v112, v132, v136\n\t"
    "v_and_b32 v132, 7, v132\n\t"
    "v_lshl_or_b32 v112, v136, v133, v112\n\t"
    "v_lshl_add_u32 v132, v132, 1, v128\n\t"
    "ds_or_rtn_b32 v120, v94, v112\n\t"
    "ds_write_b16 v132, v108\n\t"
    "v_cndmask_b32 v130, v130, v129, vcc\n\t"
    "v_ffbl_b32 v131, v130\n\t"
    "v_and_b32 v134, 7, v131\n\t"
    "v_lshlrev_b32 v134, v134, s45\n\t"
    "v_xor_b32 v129, v129, v134\n\t"
    "v_xor_b32 v132, v131, v127\n\t"
    "v_bfi_b32 v130, v102, 0, v129\n\t"
    "v_cmp_eq_u32 vcc, 0, v130\n\t"
    "v_and_or_b32 v96, v102, s44, v69\n\t"
    "v_xor_b32 v133, 23, v132\n\t"
    "v_lshlrev_b32 v113, v132, v136\n\t"
    "v_and_b32 v132, 7, v132\n\t"
    "v_lshl_or_b32 v113, v136, v133, v113\n\t"
    "v_lshl_add_u32 v132, v132, 1, v128\n\t"
    "ds_or_rtn_b32 v121, v95, v113\n\t"
    "ds_write_b16 v132, v100\n\t"
    "v_cndmask_b32 v130, v130, v129, vcc\n\t"
    "v_ffbl_b32 v131, v130\n\t"
    "v_and_b32 v134, 7, v131\n\t"
    "v_lshlrev_b32 v134, v134, s45\n\t"
    "v_xor_b32 v129, v129, v134\n\t"
    "v_xor_b32 v132, v131, v127\n\t"
    "s_waitcnt lgkmcnt(15)\n\t"
    "v_and_b32 v118, v118, v110\n\t"
    "v_perm_b32 v118, v118, v118, s46\n\t"
    "ds_or_b32 v92, v118\n\t"
    "v_bfi_b32 v130, v104, 0, v129\n\t"
    "v_cmp_eq_u32 vcc, 0, v130\n\t"
    "v_and_or_b32 v97, v104, s44, v69\n\t"
    "v_xor_b32 v133, 23, v132\n\t"
    "v_lshlrev_b32 v114, v132, v136\n\t"
    "v_and_b32 v132, 7, v132\n\t"
    "v_lshl_or_b32 v114, v136, v133, v114\n\t"
    "v_lshl_add_u32 v132, v132, 1, v128\n\t"
    "ds_or_rtn_b32 v122, v96, v114\n\t"
    "ds_write_b16 v132, v102\n\t"
    "v_cndmask_b32 v130, v130, v129, vcc\n\t"
    "v_ffbl_b32 v131, v130\n\t"
    "v_and_b32 v134, 7, v131\n\t"
    "v_lshlrev_b32 v134, v134, s45\n\t"
    "v_xor_b32 v129, v129, v134\n\t"
    "v_xor_b32 v132, v131, v127\n\t"
    "v_and_b32 v119, v119, v111\n\t"
    "v_perm_b32 v119, v119, v119, s46\n\t"
    "ds_or_b32 v93, v119\n\t"
    "s_waitcnt lgkmcnt(14)\n\t"
    "buffer_store_dwordx4 v[236:239], v139, %12, s48 offen sc1\n\t"
    "buffer_store_dwordx4 v[240:243], v140, %12, s48 offen sc1\n\t"
    "s_add_u32 s48, s48, 819200\n\t"
    "s_waitcnt lgkmcnt(8)\n\t"
    "buffer_store_dwordx4 v[244:247], v139, %12, s48 offen sc1\n\t"
    "buffer_store_dwordx4 v[248:251], v140, %12, s48 offen sc1\n\t"
    "s_add_u32 s48, s48, -2441216\n\t"
    "ds_write_b128 v141, v[24:27] offset:0\n\t"
    "ds_write_b128 v141, v[28:31] offset:256\n\t"
    "ds_read_b64_tr_b16 v[24:25], v142 offset:0\n\t"
    "ds_read_b64_tr_b16 v[26:27], v142 offset:256\n\t"
    "ds_read_b64_tr_b16 v[28:29], v142 offset:32\n\t"
    "ds_read_b64_tr_b16 v[30:31], v142 offset:288\n\t"
    "ds_write_b128 v141, v[32:35] offset:2112\n\t"
    "ds_write_b128 v141, v[36:39] offset:2368\n\t"
    "ds_read_b64_tr_b16 v[32:33], v142 offset:2112\n\t"
    "ds_read_b64_tr_b16 v[34:35], v142 offset:2368\n\t"
    "ds_read_b64_tr_b16 v[36:37], v142 offset:2144\n\t"
    "ds_read_b64_tr_b16 v[38:39], v142 offset:2400\n\t"
    "v_bfi_b32 v130, v103, 0, v129\n\t"
    "v_cmp_eq_u32 vcc, 0, v130\n\t"
    "v_and_or_b32 v98, v103, s44, v69\n\t"
    "v_xor_b32 v133, 23, v132\n\t"
    "v_lshlrev_b32 v115, v132, v136\n\t"
    "v_and_b32 v132, 7, v132\n\t"
    "v_lshl_or_b32 v115, v136, v133, v115\n\t"
    "v_lshl_add_u32 v132, v132, 1, v128\n\t"
    "ds_or_rtn_b32 v123, v97, v115\n\t"
    "ds_write_b16 v132, v104\n\t"
    "v_cndmask_b32 v130, v130, v129, vcc\n\t"
    "v_ffbl_b32 v131, v130\n\t"
    "v_and_b32 v134, 7, v131\n\t"
    "v_lshlrev_b32 v134, v134, s45\n\t"
    "v_xor_b32 v129, v129, v134\n\t"
    "v_xor_b32 v132, v131, v127\n\t"
    "s_waitcnt lgkmcnt(15)\n\t"
    "v_and_b32 v120, v120, v112\n\t"
    "v_perm_b32 v120, v120, v120, s46\n\t"
    "ds_or_b32 v94, v120\n\t"
    "v_bfi_b32 v130, v107, 0, v129\n\t"
    "v_cmp_eq_u32 vcc, 0, v130\n\t"
    "v_and_or_b32 v99, v107, s44, v69\n\t"
    "v_xor_b32 v133, 23, v132\n\t"
    "v_lshlrev_b32 v116, v132, v136\n\t"
    "v_and_b32 v132, 7, v132\n\t"
    "v_lshl_or_b32 v116, v136, v133, v116\n\t"
    "v_lshl_add_u32 v132, v132, 1, v128\n\t"
    "ds_or_rtn_b32 v124, v98, v116\n\t"
    "ds_write_b16 v132, v103\n\t"
    "v_cndmask_b32 v130, v130, v129, vcc\n\t"
    "v_ffbl_b32 v131, v130\n\t"
    "v_xor_b32 v132, v131, v127\n\t"
    "v_and_b32 v121, v121, v113\n\t"
    "v_perm_b32 v121, v121, v121, s46\n\t"
    "ds_or_b32 v95, v121\n\t"
    "v_xor_b32 v133, 23, v132\n\t"
    "v_lshlrev_b32 v117, v132, v136\n\t"
    "v_and_b32 v132, 7, v132\n\t"
    "v_lshl_or_b32 v117, v136, v133, v117\n\t"
    "v_lshl_add_u32 v132, v132, 1, v128\n\t"
    "ds_or_rtn_b32 v125, v99, v117\n\t"
    "ds_write_b16 v132, v107\n\t"
    "v_and_b32 v122, v122, v114\n\t"
    "v_perm_b32 v122, v122, v122, s46\n\t"
    "ds_or_b32 v96, v122\n\t"
    "ds_read_b128 v[72:75], v128\n\t"
    "v_and_or_b32 v100, v76, s44, v69\n\t"
    "ds_read_b32 v100, v100\n\t"
    "v_and_or_b32 v101, v77, s44, v69\n\t"
    "ds_read_b32 v101, v101\n\t"
    "v_and_or_b32 v102, v78, s44, v69\n\t"
    "ds_read_b32 v102, v102\n\t"
    "v_and_or_b32 v103, v79, s44, v69\n\t"
    "ds_read_b32 v103, v103\n\t"
    "v_and_or_b32 v104, v80, s44, v69\n\t"
    "ds_read_b32 v104, v104\n\t"
    "v_and_or_b32 v105, v81, s44, v69\n\t"
    "ds_read_b32 v105, v105\n\t"
    "v_and_or_b32 v106, v82, s44, v69\n\t"
    "ds_read_b32 v106, v106\n\t"
    "v_and_or_b32 v107, v83, s44, v69\n\t"
    "ds_read_b32 v107, v107\n\t"
    "v_lshl_add_u32 v84, v56, 3, s36\n\t"
    "v_lshl_add_u32 v85, v57, 3, s37\n\t"
    "v_lshl_add_u32 v86, v58, 3, s38\n\t"
    "v_lshl_add_u32 v87, v59, 3, s39\n\t"
    "v_lshl_add_u32 v88, v60, 3, s40\n\t"
    "v_lshl_add_u32 v89, v61, 3, s41\n\t"
    "v_lshl_add_u32 v90, v62, 3, s42\n\t"
    "v_lshl_add_u32 v91, v63, 3, s43\n\t"
    "s_waitcnt lgkmcnt(15)\n\t"
    "buffer_store_dwordx4 v[24:27], v139, %12, s48 offen sc1\n\t"
    "buffer_store_dwordx4 v[28:31], v140, %12, s48 offen sc1\n\t"
    "s_add_u32 s48, s48, 819200\n\t"
    "buffer_store_dwordx4 v[32:35], v139, %12, s48 offen sc1\n\t"
    "buffer_store_dwordx4 v[36:39], v140, %12, s48 offen sc1\n\t"
    "s_add_u32 s48, s48, 819200\n\t"
    "buffer_load_dwordx4 v[204:207], v137, s[96:99], s47 offen offset:64\n\t"
    "buffer_load_dwordx4 v[208:211], v138, s[96:99], s47 offen offset:64\n\t"
    "buffer_load_dwordx4 v[212:215], v137, s[96:99], s47 offen offset:128\n\t"
    "buffer_load_dwordx4 v[216:219], v138, s[96:99], s47 offen offset:128\n\t"
    "buffer_load_dwordx4 v[48:51], v137, s[96:99], s47 offen offset:192\n\t"
    "buffer_load_dwordx4 v[52:55], v138, s[96:99], s47 offen offset:192\n\t"
    "s_add_u32 s47, s47, 2097152\n\t"
    "buffer_load_dwordx4 v[220:223], v137, s[96:99], s47 offen offset:0\n\t"
    "buffer_load_dwordx4 v[224:227], v138, s[96:99], s47 offen offset:0\n\t"
    "buffer_load_dwordx4 v[228:231], v137, s[96:99], s47 offen offset:64\n\t"
    "buffer_load_dwordx4 v[232:235], v138, s[96:99], s47 offen offset:64\n\t"
    "buffer_load_dwordx4 v[236:239], v137, s[96:99], s47 offen offset:128\n\t"
    "buffer_load_dwordx4 v[240:243], v138, s[96:99], s47 offen offset:128\n\t"
    "s_waitcnt lgkmcnt(11)\n\t"
    "v_and_b32 v123, v123, v115\n\t"
    "v_perm_b32 v123, v123, v123, s46\n\t"
    "ds_or_b32 v97, v123\n\t"
    "v_and_b32 v124, v124, v116\n\t"
    "v_perm_b32 v124, v124, v124, s46\n\t"
    "ds_or_b32 v98, v124\n\t"
    "v_and_b32 v125, v125, v117\n\t"
    "v_perm_b32 v125, v125, v125, s46\n\t"
    "ds_or_b32 v99, v125\n\t"
    "s_waitcnt lgkmcnt(11)\n\t"
    "buffer_store_dwordx4 v[72:75], v67, %12, s49 offen offset:320 sc1\n\t"
    "v_mov_b32 v129, 0xffff0000\n\t"
    "s_waitcnt lgkmcnt(7)\n\t"
    "v_perm_b32 v100, v100, v76, v126\n\t"
    "v_perm_b32 v101, v101, v77, v126\n\t"
    "v_perm_b32 v102, v102, v78, v126\n\t"
    "v_perm_b32 v103, v103, v79, v126\n\t"
    "s_waitcnt lgkmcnt(3)\n\t"
    "v_perm_b32 v104, v104, v80, v126\n\t"
    "v_perm_b32 v105, v105, v81, v126\n\t"
    "v_perm_b32 v106, v106, v82, v126\n\t"
    "v_perm_b32 v107, v107, v83, v126\n\t"
    "ds_write_b128 v141, v[40:43] offset:0\n\t"
    "ds_write_b128 v141, v[44:47] offset:256\n\t"
    "ds_read_b64_tr_b16 v[40:41], v142 offset:0\n\t"
    "ds_read_b64_tr_b16 v[42:43], v142 offset:256\n\t"
    "ds_read_b64_tr_b16 v[44:45], v142 offset:32\n\t"
    "ds_read_b64_tr_b16 v[46:47], v142 offset:288\n\t"
    "v_max_u32 v108, v100, v101\n\t"
    "v_min_u32 v101, v100, v101\n\t"
    "v_max_u32 v100, v102, v103\n\t"
    "v_min_u32 v103, v102, v103\n\t"
    "v_max_u32 v102, v104, v105\n\t"
    "v_min_u32 v105, v104, v105\n\t"
    "v_max_u32 v104, v106, v107\n\t"
    "v_min_u32 v107, v106, v107\n\t"
    "v_max_u32 v106, v108, v100\n\t"
    "v_min_u32 v100, v108, v100\n\t"
    "v_max_u32 v108, v101, v103\n\t"
    "v_min_u32 v103, v101, v103\n\t"
    "v_max_u32 v101, v102, v104\n\t"
    "v_min_u32 v104, v102, v104\n\t"
    "v_max_u32 v102, v105, v107\n\t"
    "v_min_u32 v107, v105, v107\n\t"
    "v_bfi_b32 v130, v106, 0, v129\n\t"
    "v_cmp_eq_u32 vcc, 0, v130\n\t"
    "v_and_or_b32 v92, v106, s44, v69\n\t"
    "s_nop 0\n\t"
    "v_cndmask_b32 v130, v130, v129, vcc\n\t"
    "v_ffbl_b32 v131, v130\n\t"
    "v_and_b32 v134, 7, v131\n\t"
    "v_lshlrev_b32 v134, v134, s45\n\t"
    "v_xor_b32 v129, v129, v134\n\t"
    "v_xor_b32 v132, v131, v127\n\t"
    "v_bfi_b32 v130, v101, 0, v129\n\t"
    "v_cmp_eq_u32 vcc, 0, v130\n\t"
    "v_and_or_b32 v93, v101, s44, v69\n\t"
    "v_xor_b32 v133, 23, v132\n\t"
    "v_lshlrev_b32 v110, v132, v136\n\t"
    "v_and_b32 v132, 7, v132\n\t"
    "v_lshl_or_b32 v110, v136, v133, v110\n\t"
    "v_lshl_add_u32 v132, v132, 1, v128\n\t"
    "ds_or_rtn_b32 v118, v92, v110\n\t"
    "ds_write_b16 v132, v106\n\t"
    "v_cndmask_b32 v130, v130, v129, vcc\n\t"
    "v_ffbl_b32 v131, v130\n\t"
    "v_and_b32 v134, 7, v131\n\t"
    "v_lshlrev_b32 v134, v134, s45\n\t"
    "v_xor_b32 v129, v129, v134\n\t"
    "v_xor_b32 v132, v131, v127\n\t"
    "v_bfi_b32 v130, v108, 0, v129\n\t"
    "v_cmp_eq_u32 vcc, 0, v130\n\t"
    "v_and_or_b32 v94, v108, s44, v69\n\t"
    "v_xor_b32 v133, 23, v132\n\t"
    "v_lshlrev_b32 v111, v132, v136\n\t"
    "v_and_b32 v132, 7, v132\n\t"
    "v_lshl_or_b32 v111, v136, v133, v111\n\t"
    "v_lshl_add_u32 v132, v132, 1, v128\n\t"
    "ds_or_rtn_b32 v119, v93, v111\n\t"
    "ds_write_b16 v132, v101\n\t"
    "v_cndmask_b32 v130, v130, v129, vcc\n\t"
    "v_ffbl_b32 v131, v130\n\t"
    "v_and_b32 v134, 7, v131\n\t"
    "v_lshlrev_b32 v134, v134, s45\n\t"
    "v_xor_b32 v129, v129, v134\n\t"
    "v_xor_b32 v132, v131, v127\n\t"
    "s_waitcnt lgkmcnt(4)\n\t"
    "buffer_store_dwordx4 v[40:43], v139, %12, s48 offen sc1\n\t"
    "buffer_store_dwordx4 v[44:47], v140, %12, s48 offen sc1\n\t"
    "s_add_u32 s48, s48, 819200\n\t"
    "v_bfi_b32 v130, v100, 0, v129\n\t"
    "v_cmp_eq_u32 vcc, 0, v130\n\t"
    "v_and_or_b32 v95, v100, s44, v69\n\t"
    "v_xor_b32 v133, 23, v132\n\t"
    "v_lshlrev_b32 v112, v132, v136\n\t"
    "v_and_b32 v132, 7, v132\n\t"
    "v_lshl_or_b32 v112, v136, v133, v112\n\t"
    "v_lshl_add_u32 v132, v132, 1, v128\n\t"
    "ds_or_rtn_b32 v120, v94, v112\n\t"
    "ds_write_b16 v132, v108\n\t"
    "v_cndmask_b32 v130, v130, v129, vcc\n\t"
    "v_ffbl_b32 v131, v130\n\t"
    "v_and_b32 v134, 7, v131\n\t"
    "v_lshlrev_b32 v134, v134, s45\n\t"
    "v_xor_b32 v129, v129, v134\n\t"
    "v_xor_b32 v132, v131, v127\n\t"
    "v_bfi_b32 v130, v102, 0, v129\n\t"
    "v_cmp_eq_u32 vcc, 0, v130\n\t"
    "v_and_or_b32 v96, v102, s44, v69\n\t"
    "v_xor_b32 v133, 23, v132\n\t"
    "v_lshlrev_b32 v113, v132, v136\n\t"
    "v_and_b32 v132, 7, v132\n\t"
    "v_lshl_or_b32 v113, v136, v133, v113\n\t"
    "v_lshl_add_u32 v132, v132, 1, v128\n\t"
    "ds_or_rtn_b32 v121, v95, v113\n\t"
    "ds_write_b16 v132, v100\n\t"
    "v_cndmask_b32 v130, v130, v129, vcc\n\t"
    "v_ffbl_b32 v131, v130\n\t"
    "v_and_b32 v134, 7, v131\n\t"
    "v_lshlrev_b32 v134, v134, s45\n\t"
    "v_xor_b32 v129, v129, v134\n\t"
    "v_xor_b32 v132, v131, v127\n\t"
    "s_waitcnt lgkmcnt(7)\n\t"
    "v_and_b32 v118, v118, v110\n\t"
    "v_perm_b32 v118, v118, v118, s46\n\t"
    "ds_or_b32 v92, v118\n\t"
    "v_bfi_b32 v130, v104, 0, v129\n\t"
    "v_cmp_eq_u32 vcc, 0, v130\n\t"
    "v_and_or_b32 v97, v104, s44, v69\n\t"
    "v_xor_b32 v133, 23, v132\n\t"
    "v_lshlrev_b32 v114, v132, v136\n\t"
    "v_and_b32 v132, 7, v132\n\t"
    "v_lshl_or_b32 v114, v136, v133, v114\n\t"
    "v_lshl_add_u32 v132, v132, 1, v128\n\t"
    "ds_or_rtn_b32 v122, v96, v114\n\t"
    "ds_write_b16 v132, v102\n\t"
    "v_cndmask_b32 v130, v130, v129, vcc\n\t"
    "v_ffbl_b32 v131, v130\n\t"
    "v_and_b32 v134, 7, v131\n\t"
    "v_lshlrev_b32 v134, v134, s45\n\t"
    "v_xor_b32 v129, v129, v134\n\t"
    "v_xor_b32 v132, v131, v127\n\t"
    "s_waitcnt lgkmcnt(8)\n\t"
    "v_and_b32 v119, v119, v111\n\t"
    "v_perm_b32 v119, v119, v119, s46\n\t"
    "ds_or_b32 v93, v119\n\t"
    "v_bfi_b32 v130, v103, 0, v129\n\t"
    "v_cmp_eq_u32 vcc, 0, v130\n\t"
    "v_and_or_b32 v98, v103, s44, v69\n\t"
    "v_xor_b32 v133, 23, v132\n\t"
    "v_lshlrev_b32 v115, v132, v136\n\t"
    "v_and_b32 v132, 7, v132\n\t"
    "v_lshl_or_b32 v115, v136, v133, v115\n\t"
    "v_lshl_add_u32 v132, v132, 1, v128\n\t"
    "ds_or_rtn_b32 v123, v97, v115\n\t"
    "ds_write_b16 v132, v104\n\t"
    "v_cndmask_b32 v130, v130, v129, vcc\n\t"
    "v_ffbl_b32 v131, v130\n\t"
    "v_and_b32 v134, 7, v131\n\t"
    "v_lshlrev_b32 v134, v134, s45\n\t"
    "v_xor_b32 v129, v129, v134\n\t"
    "v_xor_b32 v132, v131, v127\n\t"
    "s_waitcnt lgkmcnt(9)\n\t"
    "v_and_b32 v120, v120, v112\n\t"
    "v_perm_b32 v120, v120, v120, s46\n\t"
    "ds_or_b32 v94, v120\n\t"
    "v_bfi_b32 v130, v107, 0, v129\n\t"
    "v_cmp_eq_u32 vcc, 0, v130\n\t"
    "v_and_or_b32 v99, v107, s44, v69\n\t"
    "v_xor_b32 v133, 23, v132\n\t"
    "v_lshlrev_b32 v116, v132, v136\n\t"
    "v_and_b32 v132, 7, v132\n\t"
    "v_lshl_or_b32 v116, v136, v133, v116\n\t"
    "v_lshl_add_u32 v132, v132, 1, v128\n\t"
    "ds_or_rtn_b32 v124, v98, v116\n\t"
    "ds_write_b16 v132, v103\n\t"
    "v_cndmask_b32 v130, v130, v129, vcc\n\t"
    "v_ffbl_b32 v131, v130\n\t"
    "v_xor_b32 v132, v131, v127\n\t"
    "s_waitcnt lgkmcnt(10)\n\t"
    "v_and_b32 v121, v121, v113\n\t"
    "v_perm_b32 v121, v121, v121, s46\n\t"
    "ds_or_b32 v95, v121\n\t"
    "v_xor_b32 v133, 23, v132\n\t"
    "v_lshlrev_b32 v117, v132, v136\n\t"
    "v_and_b32 v132, 7, v132\n\t"
    "v_lshl_or_b32 v117, v136, v133, v117\n\t"
    "v_lshl_add_u32 v132, v132, 1, v128\n\t"
    "ds_or_rtn_b32 v125, v99, v117\n\t"
    "ds_write_b16 v132, v107\n\t"
    "s_waitcnt lgkmcnt(10)\n\t"
    "v_and_b32 v122, v122, v114\n\t"
    "v_perm_b32 v122, v122, v122, s46\n\t"
    "ds_or_b32 v96, v122\n\t"
    "ds_read_b128 v[72:75], v128\n\t"
    "v_and_or_b32 v100, v84, s44, v69\n\t"
    "ds_read_b32 v100, v100\n\t"
    "v_and_or_b32 v101, v85, s44, v69\n\t"
    "ds_read_b32 v101, v101\n\t"
    "v_and_or_b32 v102, v86, s44, v69\n\t"
    "ds_read_b32 v102, v102\n\t"
    "v_and_or_b32 v103, v87, s44, v69\n\t"
    "ds_read_b32 v103, v103\n\t"
    "v_and_or_b32 v104, v88, s44, v69\n\t"
    "ds_read_b32 v104, v104\n\t"
    "v_and_or_b32 v105, v89, s44, v69\n\t"
    "ds_read_b32 v105, v105\n\t"
    "v_and_or_b32 v106, v90, s44, v69\n\t"
    "ds_read_b32 v106, v106\n\t"
    "v_and_or_b32 v107, v91, s44, v69\n\t"
    "ds_read_b32 v107, v107\n\t"
    "v_lshl_add_u32 v76, v0, 3, s36\n\t"
    "v_lshl_add_u32 v77, v1, 3, s37\n\t"
    "v_lshl_add_u32 v78, v2, 3, s38\n\t"
    "v_lshl_add_u32 v79, v3, 3, s39\n\t"
    "v_lshl_add_u32 v80, v4, 3, s40\n\t"
    "v_lshl_add_u32 v81, v5, 3, s41\n\t"
    "v_lshl_add_u32 v82, v6, 3, s42\n\t"
    "v_lshl_add_u32 v83, v7, 3, s43\n\t"
    "v_mbcnt_lo_u32_b32 v70, -1, 0\n\t"
    "v_mbcnt_hi_u32_b32 v70, -1, v70\n\t"
    "v_add_u32 v70, 8, v70\n\t"
    "v_mov_b32 v148, 32\n\t"
    "v_lshlrev_b32 v70, 7, v70\n\t"
    "s_mov_b32 s52, 49283072\n\t"
    "s_mov_b64 exec, 0xff\n\t"
    "buffer_load_dword v71, v70, %12, s52 offen sc1\n\t"
    "s_mov_b64 exec, -1\n\t"
    "s_mov_b32 s54, 0x40000000\n\t"
    "s_mov_b32 s55, 0\n\t"
    "buffer_load_dwordx4 v[244:247], v137, s[96:99], s47 offen offset:192\n\t"
    "buffer_load_dwordx4 v[248:251], v138, s[96:99], s47 offen offset:192\n\t"
    "s_add_u32 s47, s47, 2097152\n\t"
    "s_waitcnt lgkmcnt(11)\n\t"
    "v_and_b32 v123, v123, v115\n\t"
    "v_perm_b32 v123, v123, v123, s46\n\t"
    "ds_or_b32 v97, v123\n\t"
    "v_and_b32 v124, v124, v116\n\t"
    "v_perm_b32 v124, v124, v124, s46\n\t"
    "ds_or_b32 v98, v124\n\t"
    "v_and_b32 v125, v125, v117\n\t"
    "v_perm_b32 v125, v125, v125, s46\n\t"
    "ds_or_b32 v99, v125\n\t"
    "s_waitcnt lgkmcnt(11)\n\t"
    "buffer_store_dwordx4 v[72:75], v67, %12, s49 offen offset:352 sc1\n\t"
    "v_mov_b32 v129, 0xffff0000\n\t"
    "s_waitcnt lgkmcnt(7)\n\t"
    "v_perm_b32 v100, v100, v84, v126\n\t"
    "v_perm_b32 v101, v101, v85, v126\n\t"
    "v_perm_b32 v102, v102, v86, v126\n\t"
    "v_perm_b32 v103, v103, v87, v126\n\t"
    "s_waitcnt lgkmcnt(3)\n\t"
    "v_perm_b32 v104, v104, v88, v126\n\t"
    "v_perm_b32 v105, v105, v89, v126\n\t"
    "v_perm_b32 v106, v106, v90, v126\n\t"
    "v_perm_b32 v107, v107, v91, v126\n\t"
    "s_waitcnt vmcnt(42)\n\t"
    "ds_write_b128 v141, v[156:159] offset:2112\n\t"
    "ds_write_b128 v141, v[160:163] offset:2368\n\t"
    "ds_read_b64_tr_b16 v[156:157], v142 offset:2112\n\t"
    "ds_read_b64_tr_b16 v[158:159], v142 offset:2368\n\t"
    "ds_read_b64_tr_b16 v[160:161], v142 offset:2144\n\t"
    "ds_read_b64_tr_b16 v[162:163], v142 offset:2400\n\t"
    "s_waitcnt vmcnt(40)\n\t"
    "ds_write_b128 v141, v[164:167] offset:0\n\t"
    "ds_write_b128 v141, v[168:171] offset:256\n\t"
    "ds_read_b64_tr_b16 v[164:165], v142 offset:0\n\t"
    "ds_read_b64_tr_b16 v[166:167], v142 offset:256\n\t"
    "ds_read_b64_tr_b16 v[168:169], v142 offset:32\n\t"
    "ds_read_b64_tr_b16 v[170:171], v142 offset:288\n\t"
    "v_max_u32 v108, v100, v101\n\t"
    "v_min_u32 v101, v100, v101\n\t"
    "v_max_u32 v100, v102, v103\n\t"
    "v_min_u32 v103, v102, v103\n\t"
    "v_max_u32 v102, v104, v105\n\t"
    "v_min_u32 v105, v104, v105\n\t"
    "v_max_u32 v104, v106, v107\n\t"
    "v_min_u32 v107, v106, v107\n\t"
    "v_max_u32 v106, v108, v100\n\t"
    "v_min_u32 v100, v108, v100\n\t"
    "v_max_u32 v108, v101, v103\n\t"
    "v_min_u32 v103, v101, v103\n\t"
    "v_max_u32 v101, v102, v104\n\t"
    "v_min_u32 v104, v102, v104\n\t"
    "v_max_u32 v102, v105, v107\n\t"
    "v_min_u32 v107, v105, v107\n\t"
    "v_bfi_b32 v130, v106, 0, v129\n\t"
    "v_cmp_eq_u32 vcc, 0, v130\n\t"
    "v_and_or_b32 v92, v106, s44, v69\n\t"
    "s_nop 0\n\t"
    "v_cndmask_b32 v130, v130, v129, vcc\n\t"
    "v_ffbl_b32 v131, v130\n\t"
    "v_and_b32 v134, 7, v131\n\t"
    "v_lshlrev_b32 v134, v134, s45\n\t"
    "v_xor_b32 v129, v129, v134\n\t"
    "v_xor_b32 v132, v131, v127\n\t"
    "v_bfi_b32 v130, v101, 0, v129\n\t"
    "v_cmp_eq_u32 vcc, 0, v130\n\t"
    "v_and_or_b32 v93, v101, s44, v69\n\t"
    "v_xor_b32 v133, 23, v132\n\t"
    "v_lshlrev_b32 v110, v132, v136\n\t"
    "v_and_b32 v132, 7, v132\n\t"
    "v_lshl_or_b32 v110, v136, v133, v110\n\t"
    "v_lshl_add_u32 v132, v132, 1, v128\n\t"
    "ds_or_b32 v92, v110\n\t"
    "ds_write_b16 v132, v106\n\t"
    "v_cndmask_b32 v130, v130, v129, vcc\n\t"
    "v_ffbl_b32 v131, v130\n\t"
    "v_and_b32 v134, 7, v131\n\t"
    "v_lshlrev_b32 v134, v134, s45\n\t"
    "v_xor_b32 v129, v129, v134\n\t"
    "v_xor_b32 v132, v131, v127\n\t"
    "v_bfi_b32 v130, v108, 0, v129\n\t"
    "v_cmp_eq_u32 vcc, 0, v130\n\t"
    "v_and_or_b32 v94, v108, s44, v69\n\t"
    "v_xor_b32 v133, 23, v132\n\t"
    "v_lshlrev_b32 v111, v132, v136\n\t"
    "v_and_b32 v132, 7, v132\n\t"
    "v_lshl_or_b32 v111, v136, v133, v111\n\t"
    "v_lshl_add_u32 v132, v132, 1, v128\n\t"
    "ds_or_b32 v93, v111\n\t"
    "ds_write_b16 v132, v101\n\t"
    "v_cndmask_b32 v130, v130, v129, vcc\n\t"
    "v_ffbl_b32 v131, v130\n\t"
    "v_and_b32 v134, 7, v131\n\t"
    "v_lshlrev_b32 v134, v134, s45\n\t"
    "v_xor_b32 v129, v129, v134\n\t"
    "v_xor_b32 v132, v131, v127\n\t"
    "s_waitcnt lgkmcnt(10)\n\t"
    "buffer_store_dwordx4 v[156:159], v139, %12, s48 offen sc1\n\t"
    "buffer_store_dwordx4 v[160:163], v140, %12, s48 offen sc1\n\t"
    "s_add_u32 s48, s48, -2441216\n\t"
    "s_waitcnt lgkmcnt(4)\n\t"
    "buffer_store_dwordx4 v[164:167], v139, %12, s48 offen sc1\n\t"
    "buffer_store_dwordx4 v[168:171], v140, %12, s48 offen sc1\n\t"
    "s_add_u32 s48, s48, 819200\n\t"
    "s_waitcnt vmcnt(42)\n\t"
    "ds_write_b128 v141, v[172:175] offset:2112\n\t"
    "ds_write_b128 v141, v[176:179] offset:2368\n\t"
    "ds_read_b64_tr_b16 v[172:173], v142 offset:2112\n\t"
    "ds_read_b64_tr_b16 v[174:175], v142 offset:2368\n\t"
    "ds_read_b64_tr_b16 v[176:177], v142 offset:2144\n\t"
    "ds_read_b64_tr_b16 v[178:179], v142 offset:2400\n\t"
    "s_waitcnt vmcnt(40)\n\t"
    "ds_write_b128 v141, v[180:183] offset:0\n\t"
    "ds_write_b128 v141, v[184:187] offset:256\n\t"
    "ds_read_b64_tr_b16 v[180:181], v142 offset:0\n\t"
    "ds_read_b64_tr_b16 v[182:183], v142 offset:256\n\t"
    "ds_read_b64_tr_b16 v[184:185], v142 offset:32\n\t"
    "ds_read_b64_tr_b16 v[186:187], v142 offset:288\n\t"
    "v_bfi_b32 v130, v100, 0, v129\n\t"
    "v_cmp_eq_u32 vcc, 0, v130\n\t"
    "v_and_or_b32 v95, v100, s44, v69\n\t"
    "v_xor_b32 v133, 23, v132\n\t"
    "v_lshlrev_b32 v112, v132, v136\n\t"
    "v_and_b32 v132, 7, v132\n\t"
    "v_lshl_or_b32 v112, v136, v133, v112\n\t"
    "v_lshl_add_u32 v132, v132, 1, v128\n\t"
    "ds_or_b32 v94, v112\n\t"
    "ds_write_b16 v132, v108\n\t"
    "v_cndmask_b32 v130, v130, v129, vcc\n\t"
    "v_ffbl_b32 v131, v130\n\t"
    "v_and_b32 v134, 7, v131\n\t"
    "v_lshlrev_b32 v134, v134, s45\n\t"
    "v_xor_b32 v129, v129, v134\n\t"
    "v_xor_b32 v132, v131, v127\n\t"
    "v_bfi_b32 v130, v102, 0, v129\n\t"
    "v_cmp_eq_u32 vcc, 0, v130\n\t"
    "v_and_or_b32 v96, v102, s44, v69\n\t"
    "v_xor_b32 v133, 23, v132\n\t"
    "v_lshlrev_b32 v113, v132, v136\n\t"
    "v_and_b32 v132, 7, v132\n\t"
    "v_lshl_or_b32 v113, v136, v133, v113\n\t"
    "v_lshl_add_u32 v132, v132, 1, v128\n\t"
    "ds_or_b32 v95, v113\n\t"
    "ds_write_b16 v132, v100\n\t"
    "v_cndmask_b32 v130, v130, v129, vcc\n\t"
    "v_ffbl_b32 v131, v130\n\t"
    "v_and_b32 v134, 7, v131\n\t"
    "v_lshlrev_b32 v134, v134, s45\n\t"
    "v_xor_b32 v129, v129, v134\n\t"
    "v_xor_b32 v132, v131, v127\n\t"
    "s_waitcnt vmcnt(7)\n\t"
    "s_call_b64 s[54:55], L_af1w_%=\n\t"
    "s_mov_b64 s[54:55], 0\n\t"
    "L_af1n15_%=:\n\t"
    "v_bfi_b32 v130, v104, 0, v129\n\t"
    "v_cmp_eq_u32 vcc, 0, v130\n\t"
    "v_and_or_b32 v97, v104, s44, v69\n\t"
    "v_xor_b32 v133, 23, v132\n\t"
    "v_lshlrev_b32 v114, v132, v136\n\t"
    "v_and_b32 v132, 7, v132\n\t"
    "v_lshl_or_b32 v114, v136, v133, v114\n\t"
    "v_lshl_add_u32 v132, v132, 1, v128\n\t"
    "ds_or_b32 v96, v114\n\t"
    "ds_write_b16 v132, v102\n\t"
    "v_cndmask_b32 v130, v130, v129, vcc\n\t"
    "v_ffbl_b32 v131, v130\n\t"
    "v_and_b32 v134, 7, v131\n\t"
    "v_lshlrev_b32 v134, v134, s45\n\t"
    "v_xor_b32 v129, v129, v134\n\t"
    "v_xor_b32 v132, v131, v127\n\t"
    "s_waitcnt lgkmcnt(12)\n\t"
    "buffer_store_dwordx4 v[172:175], v139, %12, s48 offen sc1\n\t"
    "buffer_store_dwordx4 v[176:179], v140, %12, s48 offen sc1\n\t"
    "s_add_u32 s48, s48, 819200\n\t"
    "s_waitcnt lgkmcnt(6)\n\t"
    "buffer_store_dwordx4 v[180:183], v139, %12, s48 offen sc1\n\t"
    "buffer_store_dwordx4 v[184:187], v140, %12, s48 offen sc1\n\t"
    "s_add_u32 s48, s48, 819200\n\t"
    "ds_write_b128 v141, v[188:191] offset:2112\n\t"
    "ds_write_b128 v141, v[192:195] offset:2368\n\t"
    "ds_read_b64_tr_b16 v[188:189], v142 offset:2112\n\t"
    "ds_read_b64_tr_b16 v[190:191], v142 offset:2368\n\t"
    "ds_read_b64_tr_b16 v[192:193], v142 offset:2144\n\t"
    "ds_read_b64_tr_b16 v[194:195], v142 offset:2400\n\t"
    "ds_write_b128 v141, v[196:199] offset:0\n\t"
    "ds_write_b128 v141, v[200:203] offset:256\n\t"
    "ds_read_b64_tr_b16 v[196:197], v142 offset:0\n\t"
    "ds_read_b64_tr_b16 v[198:199], v142 offset:256\n\t"
    "ds_read_b64_tr_b16 v[200:201], v142 offset:32\n\t"
    "ds_read_b64_tr_b16 v[202:203], v142 offset:288\n\t"
    "v_bfi_b32 v130, v103, 0, v129\n\t"
    "v_cmp_eq_u32 vcc, 0, v130\n\t"
    "v_and_or_b32 v98, v103, s44, v69\n\t"
    "v_xor_b32 v133, 23, v132\n\t"
    "v_lshlrev_b32 v115, v132, v136\n\t"
    "v_and_b32 v132, 7, v132\n\t"
    "v_lshl_or_b32 v115, v136, v133, v115\n\t"
    "v_lshl_add_u32 v132, v132, 1, v128\n\t"
    "ds_or_b32 v97, v115\n\t"
    "ds_write_b16 v132, v104\n\t"
    "v_cndmask_b32 v130, v130, v129, vcc\n\t"
    "v_ffbl_b32 v131, v130\n\t"
    "v_and_b32 v134, 7, v131\n\t"
    "v_lshlrev_b32 v134, v134, s45\n\t"
    "v_xor_b32 v129, v129, v134\n\t"
    "v_xor_b32 v132, v131, v127\n\t"
    "v_bfi_b32 v130, v107, 0, v129\n\t"
    "v_cmp_eq_u32 vcc, 0, v130\n\t"
    "v_and_or_b32 v99, v107, s44, v69\n\t"
    "v_xor_b32 v133, 23, v132\n\t"
    "v_lshlrev_b32 v116, v132, v136\n\t"
    "v_and_b32 v132, 7, v132\n\t"
    "v_lshl_or_b32 v116, v136, v133, v116\n\t"
    "v_lshl_add_u32 v132, v132, 1, v128\n\t"
    "ds_or_b32 v98, v116\n\t"
    "ds_write_b16 v132, v103\n\t"
    "v_cndmask_b32 v130, v130, v129, vcc\n\t"
    "v_ffbl_b32 v131, v130\n\t"
    "v_xor_b32 v132, v131, v127\n\t"
    "v_xor_b32 v133, 23, v132\n\t"
    "v_lshlrev_b32 v117, v132, v136\n\t"
    "v_and_b32 v132, 7, v132\n\t"
    "v_lshl_or_b32 v117, v136, v133, v117\n\t"
    "v_lshl_add_u32 v132, v132, 1, v128\n\t"
    "ds_or_b32 v99, v117\n\t"
    "ds_write_b16 v132, v107\n\t"
    "ds_read_b128 v[72:75], v128\n\t"
    "v_and_or_b32 v100, v76, s44, v69\n\t"
    "ds_read_b32 v100, v100\n\t"
    "v_and_or_b32 v101, v77, s44, v69\n\t"
    "ds_read_b32 v101, v101\n\t"
    "v_and_or_b32 v102, v78, s44, v69\n\t"
    "ds_read_b32 v102, v102\n\t"
    "v_and_or_b32 v103, v79, s44, v69\n\t"
    "ds_read_b32 v103, v103\n\t"
    "v_and_or_b32 v104, v80, s44, v69\n\t"
    "ds_read_b32 v104, v104\n\t"
    "v_and_or_b32 v105, v81, s44, v69\n\t"
    "ds_read_b32 v105, v105\n\t"
    "v_and_or_b32 v106, v82, s44, v69\n\t"
    "ds_read_b32 v106, v106\n\t"
    "v_and_or_b32 v107, v83, s44, v69\n\t"
    "ds_read_b32 v107, v107\n\t"
    "v_lshl_add_u32 v84, v8, 3, s36\n\t"
    "v_lshl_add_u32 v85, v9, 3, s37\n\t"
    "v_lshl_add_u32 v86, v10, 3, s38\n\t"
    "v_lshl_add_u32 v87, v11, 3, s39\n\t"
    "v_lshl_add_u32 v88, v12, 3, s40\n\t"
    "v_lshl_add_u32 v89, v13, 3, s41\n\t"
    "v_lshl_add_u32 v90, v14, 3, s42\n\t"
    "v_lshl_add_u32 v91, v15, 3, s43\n\t"
    "s_waitcnt lgkmcnt(15)\n\t"
    "buffer_store_dwordx4 v[188:191], v139, %12, s48 offen sc1\n\t"
    "buffer_store_dwordx4 v[192:195], v140, %12, s48 offen sc1\n\t"
    "s_add_u32 s48, s48, -2441216\n\t"
    "buffer_store_dwordx4 v[196:199], v139, %12, s48 offen sc1\n\t"
    "buffer_store_dwordx4 v[200:203], v140, %12, s48 offen sc1\n\t"
    "s_add_u32 s48, s48, 819200\n\t"
    "s_waitcnt lgkmcnt(8)\n\t"
    "buffer_store_dwordx4 v[72:75], v67, %12, s49 offen offset:384 sc1\n\t"
    "v_mov_b32 v129, 0xffff0000\n\t"
    "s_waitcnt lgkmcnt(4)\n\t"
    "v_perm_b32 v100, v100, v76, v126\n\t"
    "v_perm_b32 v101, v101, v77, v126\n\t"
    "v_perm_b32 v102, v102, v78, v126\n\t"
    "v_perm_b32 v103, v103, v79, v126\n\t"
    "s_waitcnt lgkmcnt(0)\n\t"
    "v_perm_b32 v104, v104, v80, v126\n\t"
    "v_perm_b32 v105, v105, v81, v126\n\t"
    "v_perm_b32 v106, v106, v82, v126\n\t"
    "v_perm_b32 v107, v107, v83, v126\n\t"
    "ds_write_b128 v141, v[204:207] offset:2112\n\t"
    "ds_write_b128 v141, v[208:211] offset:2368\n\t"
    "ds_read_b64_tr_b16 v[204:205], v142 offset:2112\n\t"
    "ds_read_b64_tr_b16 v[206:207], v142 offset:2368\n\t"
    "ds_read_b64_tr_b16 v[208:209], v142 offset:2144\n\t"
    "ds_read_b64_tr_b16 v[210:211], v142 offset:2400\n\t"
    "ds_write_b128 v141, v[212:215] offset:0\n\t"
    "ds_write_b128 v141, v[216:219] offset:256\n\t"
    "ds_read_b64_tr_b16 v[212:213], v142 offset:0\n\t"
    "ds_read_b64_tr_b16 v[214:215], v142 offset:256\n\t"
    "ds_read_b64_tr_b16 v[216:217], v142 offset:32\n\t"
    "ds_read_b64_tr_b16 v[218:219], v142 offset:288\n\t"
    "v_max_u32 v108, v100, v101\n\t"
    "v_min_u32 v101, v100, v101\n\t"
    "v_max_u32 v100, v102, v103\n\t"
    "v_min_u32 v103, v102, v103\n\t"
    "v_max_u32 v102, v104, v105\n\t"
    "v_min_u32 v105, v104, v105\n\t"
    "v_max_u32 v104, v106, v107\n\t"
    "v_min_u32 v107, v106, v107\n\t"
    "v_max_u32 v106, v108, v100\n\t"
    "v_min_u32 v100, v108, v100\n\t"
    "v_max_u32 v108, v101, v103\n\t"
    "v_min_u32 v103, v101, v103\n\t"
    "v_max_u32 v101, v102, v104\n\t"
    "v_min_u32 v104, v102, v104\n\t"
    "v_max_u32 v102, v105, v107\n\t"
    "v_min_u32 v107, v105, v107\n\t"
    "v_bfi_b32 v130, v106, 0, v129\n\t"
    "v_cmp_eq_u32 vcc, 0, v130\n\t"
    "v_and_or_b32 v92, v106, s44, v69\n\t"
    "s_nop 0\n\t"
    "v_cndmask_b32 v130, v130, v129, vcc\n\t"
    "v_ffbl_b32 v131, v130\n\t"
    "v_and_b32 v134, 7, v131\n\t"
    "v_lshlrev_b32 v134, v134, s45\n\t"
    "v_xor_b32 v129, v129, v134\n\t"
    "v_xor_b32 v132, v131, v127\n\t"
    "v_bfi_b32 v130, v101, 0, v129\n\t"
    "v_cmp_eq_u32 vcc, 0, v130\n\t"
    "v_and_or_b32 v93, v101, s44, v69\n\t"
    "v_xor_b32 v133, 23, v132\n\t"
    "v_lshlrev_b32 v110, v132, v136\n\t"
    "v_and_b32 v132, 7, v132\n\t"
    "v_lshl_or_b32 v110, v136, v133, v110\n\t"
    "v_lshl_add_u32 v132, v132, 1, v128\n\t"
    "ds_or_b32 v92, v110\n\t"
    "ds_write_b16 v132, v106\n\t"
    "v_cndmask_b32 v130, v130, v129, vcc\n\t"
    "v_ffbl_b32 v131, v130\n\t"
    "v_and_b32 v134, 7, v131\n\t"
    "v_lshlrev_b32 v134, v134, s45\n\t"
    "v_xor_b32 v129, v129, v134\n\t"
    "v_xor_b32 v132, v131, v127\n\t"
    "v_bfi_b32 v130, v108, 0, v129\n\t"
    "v_cmp_eq_u32 vcc, 0, v130\n\t"
    "v_and_or_b32 v94, v108, s44, v69\n\t"
    "v_xor_b32 v133, 23, v132\n\t"
    "v_lshlrev_b32 v111, v132, v136\n\t"
    "v_and_b32 v132, 7, v132\n\t"
    "v_lshl_or_b32 v111, v136, v133, v111\n\t"
    "v_lshl_add_u32 v132, v132, 1, v128\n\t"
    "ds_or_b32 v93, v111\n\t"
    "ds_write_b16 v132, v101\n\t"
    "v_cndmask_b32 v130, v130, v129, vcc\n\t"
    "v_ffbl_b32 v131, v130\n\t"
    "v_and_b32 v134, 7, v131\n\t"
    "v_lshlrev_b32 v134, v134, s45\n\t"
    "v_xor_b32 v129, v129, v134\n\t"
    "v_xor_b32 v132, v131, v127\n\t"
    "s_waitcnt lgkmcnt(10)\n\t"
    "buffer_store_dwordx4 v[204:207], v139, %12, s48 offen sc1\n\t"
    "buffer_store_dwordx4 v[208:211], v140, %12, s48 offen sc1\n\t"
    "s_add_u32 s48, s48, 819200\n\t"
    "s_waitcnt lgkmcnt(4)\n\t"
    "buffer_store_dwordx4 v[212:215], v139, %12, s48 offen sc1\n\t"
    "buffer_store_dwordx4 v[216:219], v140, %12, s48 offen sc1\n\t"
    "s_add_u32 s48, s48, 819200\n\t"
    "ds_write_b128 v141, v[48:51] offset:2112\n\t"
    "ds_write_b128 v141, v[52:55] offset:2368\n\t"
    "ds_read_b64_tr_b16 v[48:49], v142 offset:2112\n\t"
    "ds_read_b64_tr_b16 v[50:51], v142 offset:2368\n\t"
    "ds_read_b64_tr_b16 v[52:53], v142 offset:2144\n\t"
    "ds_read_b64_tr_b16 v[54:55], v142 offset:2400\n\t"
    "ds_write_b128 v141, v[220:223] offset:0\n\t"
    "ds_write_b128 v141, v[224:227] offset:256\n\t"
    "ds_read_b64_tr_b16 v[220:221], v142 offset:0\n\t"
    "ds_read_b64_tr_b16 v[222:223], v142 offset:256\n\t"
    "ds_read_b64_tr_b16 v[224:225], v142 offset:32\n\t"
    "ds_read_b64_tr_b16 v[226:227], v142 offset:288\n\t"
    "v_bfi_b32 v130, v100, 0, v129\n\t"
    "v_cmp_eq_u32 vcc, 0, v130\n\t"
    "v_and_or_b32 v95, v100, s44, v69\n\t"
    "v_xor_b32 v133, 23, v132\n\t"
    "v_lshlrev_b32 v112, v132, v136\n\t"
    "v_and_b32 v132, 7, v132\n\t"
    "v_lshl_or_b32 v112, v136, v133, v112\n\t"
    "v_lshl_add_u32 v132, v132, 1, v128\n\t"
    "ds_or_b32 v94, v112\n\t"
    "ds_write_b16 v132, v108\n\t"
    "v_cndmask_b32 v130, v130, v129, vcc\n\t"
    "v_ffbl_b32 v131, v130\n\t"
    "v_and_b32 v134, 7, v131\n\t"
    "v_lshlrev_b32 v134, v134, s45\n\t"
    "v_xor_b32 v129, v129, v134\n\t"
    "v_xor_b32 v132, v131, v127\n\t"
    "v_bfi_b32 v130, v102, 0, v129\n\t"
    "v_cmp_eq_u32 vcc, 0, v130\n\t"
    "v_and_or_b32 v96, v102, s44, v69\n\t"
    "v_xor_b32 v133, 23, v132\n\t"
    "v_lshlrev_b32 v113, v132, v136\n\t"
    "v_and_b32 v132, 7, v132\n\t"
    "v_lshl_or_b32 v113, v136, v133, v113\n\t"
    "v_lshl_add_u32 v132, v132, 1, v128\n\t"
    "ds_or_b32 v95, v113\n\t"
    "ds_write_b16 v132, v100\n\t"
    "v_cndmask_b32 v130, v130, v129, vcc\n\t"
    "v_ffbl_b32 v131, v130\n\t"
    "v_and_b32 v134, 7, v131\n\t"
    "v_lshlrev_b32 v134, v134, s45\n\t"
    "v_xor_b32 v129, v129, v134\n\t"
    "v_xor_b32 v132, v131, v127\n\t"
    "v_bfi_b32 v130, v104, 0, v129\n\t"
    "v_cmp_eq_u32 vcc, 0, v130\n\t"
    "v_and_or_b32 v97, v104, s44, v69\n\t"
    "v_xor_b32 v133, 23, v132\n\t"
    "v_lshlrev_b32 v114, v132, v136\n\t"
    "v_and_b32 v132, 7, v132\n\t"
    "v_lshl_or_b32 v114, v136, v133, v114\n\t"
    "v_lshl_add_u32 v132, v132, 1, v128\n\t"
    "ds_or_b32 v96, v114\n\t"
    "ds_write_b16 v132, v102\n\t"
    "v_cndmask_b32 v130, v130, v129, vcc\n\t"
    "v_ffbl_b32 v131, v130\n\t"
    "v_and_b32 v134, 7, v131\n\t"
    "v_lshlrev_b32 v134, v134, s45\n\t"
    "v_xor_b32 v129, v129, v134\n\t"
    "v_xor_b32 v132, v131, v127\n\t"
    "s_waitcnt lgkmcnt(12)\n\t"
    "buffer_store_dwordx4 v[48:51], v139, %12, s48 offen sc1\n\t"
    "buffer_store_dwordx4 v[52:55], v140, %12, s48 offen sc1\n\t"
    "s_add_u32 s48, s48, -2441216\n\t"
    "s_waitcnt lgkmcnt(6)\n\t"
    "s_and_b32 s53, %14, 12\n\t"
    "s_cselect_b32 s48, 0x40000000, s48\n\t"
    "buffer_store_dwordx4 v[220:223], v139, %12, s48 offen sc1\n\t"
    "buffer_store_dwordx4 v[224:227], v140, %12, s48 offen sc1\n\t"
    "s_add_u32 s48, s48, 819200\n\t"
    "ds_write_b128 v141, v[228:231] offset:2112\n\t"
    "ds_write_b128 v141, v[232:235] offset:2368\n\t"
    "ds_read_b64_tr_b16 v[228:229], v142 offset:2112\n\t"
    "ds_read_b64_tr_b16 v[230:231], v142 offset:2368\n\t"
    "ds_read_b64_tr_b16 v[232:233], v142 offset:2144\n\t"
    "ds_read_b64_tr_b16 v[234:235], v142 offset:2400\n\t"
    "ds_write_b128 v141, v[236:239] offset:0\n\t"
    "ds_write_b128 v141, v[240:243] offset:256\n\t"
    "ds_read_b64_tr_b16 v[236:237], v142 offset:0\n\t"
    "ds_read_b64_tr_b16 v[238:239], v142 offset:256\n\t"
    "ds_read_b64_tr_b16 v[240:241], v142 offset:32\n\t"
    "ds_read_b64_tr_b16 v[242:243], v142 offset:288\n\t"
    "v_bfi_b32 v130, v103, 0, v129\n\t"
    "v_cmp_eq_u32 vcc, 0, v130\n\t"
    "v_and_or_b32 v98, v103, s44, v69\n\t"
    "v_xor_b32 v133, 23, v132\n\t"
    "v_lshlrev_b32 v115, v132, v136\n\t"
    "v_and_b32 v132, 7, v132\n\t"
    "v_lshl_or_b32 v115, v136, v133, v115\n\t"
    "v_lshl_add_u32 v132, v132, 1, v128\n\t"
    "ds_or_b32 v97, v115\n\t"
    "ds_write_b16 v132, v104\n\t"
    "v_cndmask_b32 v130, v130, v129, vcc\n\t"
    "v_ffbl_b32 v131, v130\n\t"
    "v_and_b32 v134, 7, v131\n\t"
    "v_lshlrev_b32 v134, v134, s45\n\t"
    "v_xor_b32 v129, v129, v134\n\t"
    "v_xor_b32 v132, v131, v127\n\t"
    "v_bfi_b32 v130, v107, 0, v129\n\t"
    "v_cmp_eq_u32 vcc, 0, v130\n\t"
    "v_and_or_b32 v99, v107, s44, v69\n\t"
    "v_xor_b32 v133, 23, v132\n\t"
    "v_lshlrev_b32 v116, v132, v136\n\t"
    "v_and_b32 v132, 7, v132\n\t"
    "v_lshl_or_b32 v116, v136, v133, v116\n\t"
    "v_lshl_add_u32 v132, v132, 1, v128\n\t"
    "ds_or_b32 v98, v116\n\t"
    "ds_write_b16 v132, v103\n\t"
    "v_cndmask_b32 v130, v130, v129, vcc\n\t"
    "v_ffbl_b32 v131, v130\n\t"
    "v_xor_b32 v132, v131, v127\n\t"
    "v_xor_b32 v133, 23, v132\n\t"
    "v_lshlrev_b32 v117, v132, v136\n\t"
    "v_and_b32 v132, 7, v132\n\t"
    "v_lshl_or_b32 v117, v136, v133, v117\n\t"
    "v_lshl_add_u32 v132, v132, 1, v128\n\t"
    "ds_or_b32 v99, v117\n\t"
    "ds_write_b16 v132, v107\n\t"
    "ds_read_b128 v[72:75], v128\n\t"
    "v_and_or_b32 v100, v84, s44, v69\n\t"
    "ds_read_b32 v100, v100\n\t"
    "v_and_or_b32 v101, v85, s44, v69\n\t"
    "ds_read_b32 v101, v101\n\t"
    "v_and_or_b32 v102, v86, s44, v69\n\t"
    "ds_read_b32 v102, v102\n\t"
    "v_and_or_b32 v103, v87, s44, v69\n\t"
    "ds_read_b32 v103, v103\n\t"
    "v_and_or_b32 v104, v88, s44, v69\n\t"
    "ds_read_b32 v104, v104\n\t"
    "v_and_or_b32 v105, v89, s44, v69\n\t"
    "ds_read_b32 v105, v105\n\t"
    "v_and_or_b32 v106, v90, s44, v69\n\t"
    "ds_read_b32 v106, v106\n\t"
    "v_and_or_b32 v107, v91, s44, v69\n\t"
    "ds_read_b32 v107, v107\n\t"
    "v_lshl_add_u32 v76, v16, 3, s36\n\t"
    "v_lshl_add_u32 v77, v17, 3, s37\n\t"
    "v_lshl_add_u32 v78, v18, 3, s38\n\t"
    "v_lshl_add_u32 v79, v19, 3, s39\n\t"
    "v_lshl_add_u32 v80, v20, 3, s40\n\t"
    "v_lshl_add_u32 v81, v21, 3, s41\n\t"
    "v_lshl_add_u32 v82, v22, 3, s42\n\t"
    "v_lshl_add_u32 v83, v23, 3, s43\n\t"
    "s_waitcnt lgkmcnt(15)\n\t"
    "buffer_store_dwordx4 v[228:231], v139, %12, s48 offen sc1\n\t"
    "buffer_store_dwordx4 v[232:235], v140, %12, s48 offen sc1\n\t"
    "s_add_u32 s48, s48, 819200\n\t"
    "buffer_store_dwordx4 v[236:239], v139, %12, s48 offen sc1\n\t"
    "buffer_store_dwordx4 v[240:243], v140, %12, s48 offen sc1\n\t"
    "s_add_u32 s48, s48, 819200\n\t"
    "s_waitcnt lgkmcnt(8)\n\t"
    "buffer_store_dwordx4 v[72:75], v67, %12, s49 offen offset:416 sc1\n\t"
    "v_mov_b32 v129, 0xffff0000\n\t"
    "s_waitcnt lgkmcnt(4)\n\t"
    "v_perm_b32 v100, v100, v84, v126\n\t"
    "v_perm_b32 v101, v101, v85, v126\n\t"
    "v_perm_b32 v102, v102, v86, v126\n\t"
    "v_perm_b32 v103, v103, v87, v126\n\t"
    "s_waitcnt lgkmcnt(0)\n\t"
    "v_perm_b32 v104, v104, v88, v126\n\t"
    "v_perm_b32 v105, v105, v89, v126\n\t"
    "v_perm_b32 v106, v106, v90, v126\n\t"
    "v_perm_b32 v107, v107, v91, v126\n\t"
    "s_waitcnt vmcnt(31)\n\t"
    "ds_write_b128 v141, v[244:247] offset:2112\n\t"
    "ds_write_b128 v141, v[248:251] offset:2368\n\t"
    "ds_read_b64_tr_b16 v[244:245], v142 offset:2112\n\t"
    "ds_read_b64_tr_b16 v[246:247], v142 offset:2368\n\t"
    "ds_read_b64_tr_b16 v[248:249], v142 offset:2144\n\t"
    "ds_read_b64_tr_b16 v[250:251], v142 offset:2400\n\t"
    "v_max_u32 v108, v100, v101\n\t"
    "v_min_u32 v101, v100, v101\n\t"
    "v_max_u32 v100, v102, v103\n\t"
    "v_min_u32 v103, v102, v103\n\t"
    "v_max_u32 v102, v104, v105\n\t"
    "v_min_u32 v105, v104, v105\n\t"
    "v_max_u32 v104, v106, v107\n\t"
    "v_min_u32 v107, v106, v107\n\t"
    "v_max_u32 v106, v108, v100\n\t"
    "v_min_u32 v100, v108, v100\n\t"
    "v_max_u32 v108, v101, v103\n\t"
    "v_min_u32 v103, v101, v103\n\t"
    "v_max_u32 v101, v102, v104\n\t"
    "v_min_u32 v104, v102, v104\n\t"
    "v_max_u32 v102, v105, v107\n\t"
    "v_min_u32 v107, v105, v107\n\t"
    "v_bfi_b32 v130, v106, 0, v129\n\t"
    "v_cmp_eq_u32 vcc, 0, v130\n\t"
    "v_and_or_b32 v92, v106, s44, v69\n\t"
    "s_nop 0\n\t"
    "v_cndmask_b32 v130, v130, v129, vcc\n\t"
    "v_ffbl_b32 v131, v130\n\t"
    "v_and_b32 v134, 7, v131\n\t"
    "v_lshlrev_b32 v134, v134, s45\n\t"
    "v_xor_b32 v129, v129, v134\n\t"
    "v_xor_b32 v132, v131, v127\n\t"
    "v_bfi_b32 v130, v101, 0, v129\n\t"
    "v_cmp_eq_u32 vcc, 0, v130\n\t"
    "v_and_or_b32 v93, v101, s44, v69\n\t"
    "v_xor_b32 v133, 23, v132\n\t"
    "v_lshlrev_b32 v110, v132, v136\n\t"
    "v_and_b32 v132, 7, v132\n\t"
    "v_lshl_or_b32 v110, v136, v133, v110\n\t"
    "v_lshl_add_u32 v132, v132, 1, v128\n\t"
    "ds_or_b32 v92, v110\n\t"
    "ds_write_b16 v132, v106\n\t"
    "v_cndmask_b32 v130, v130, v129, vcc\n\t"
    "v_ffbl_b32 v131, v130\n\t"
    "v_and_b32 v134, 7, v131\n\t"
    "v_lshlrev_b32 v134, v134, s45\n\t"
    "v_xor_b32 v129, v129, v134\n\t"
    "v_xor_b32 v132, v131, v127\n\t"
    "v_bfi_b32 v130, v108, 0, v129\n\t"
    "v_cmp_eq_u32 vcc, 0, v130\n\t"
    "v_and_or_b32 v94, v108, s44, v69\n\t"
    "v_xor_b32 v133, 23, v132\n\t"
    "v_lshlrev_b32 v111, v132, v136\n\t"
    "v_and_b32 v132, 7, v132\n\t"
    "v_lshl_or_b32 v111, v136, v133, v111\n\t"
    "v_lshl_add_u32 v132, v132, 1, v128\n\t"
    "ds_or_b32 v93, v111\n\t"
    "ds_write_b16 v132, v101\n\t"
    "v_cndmask_b32 v130, v130, v129, vcc\n\t"
    "v_ffbl_b32 v131, v130\n\t"
    "v_and_b32 v134, 7, v131\n\t"
    "v_lshlrev_b32 v134, v134, s45\n\t"
    "v_xor_b32 v129, v129, v134\n\t"
    "v_xor_b32 v132, v131, v127\n\t"
    "s_waitcnt lgkmcnt(4)\n\t"
    "buffer_store_dwordx4 v[244:247], v139, %12, s48 offen sc1\n\t"
    "buffer_store_dwordx4 v[248:251], v140, %12, s48 offen sc1\n\t"
    "s_add_u32 s48, s48, -2441216\n\t"
    "v_bfi_b32 v130, v100, 0, v129\n\t"
    "v_cmp_eq_u32 vcc, 0, v130\n\t"
    "v_and_or_b32 v95, v100, s44, v69\n\t"
    "v_xor_b32 v133, 23, v132\n\t"
    "v_lshlrev_b32 v112, v132, v136\n\t"
    "v_and_b32 v132, 7, v132\n\t"
    "v_lshl_or_b32 v112, v136, v133, v112\n\t"
    "v_lshl_add_u32 v132, v132, 1, v128\n\t"
    "ds_or_b32 v94, v112\n\t"
    "ds_write_b16 v132, v108\n\t"
    "v_cndmask_b32 v130, v130, v129, vcc\n\t"
    "v_ffbl_b32 v131, v130\n\t"
    "v_and_b32 v134, 7, v131\n\t"
    "v_lshlrev_b32 v134, v134, s45\n\t"
    "v_xor_b32 v129, v129, v134\n\t"
    "v_xor_b32 v132, v131, v127\n\t"
    "v_bfi_b32 v130, v102, 0, v129\n\t"
    "v_cmp_eq_u32 vcc, 0, v130\n\t"
    "v_and_or_b32 v96, v102, s44, v69\n\t"
    "v_xor_b32 v133, 23, v132\n\t"
    "v_lshlrev_b32 v113, v132, v136\n\t"
    "v_and_b32 v132, 7, v132\n\t"
    "v_lshl_or_b32 v113, v136, v133, v113\n\t"
    "v_lshl_add_u32 v132, v132, 1, v128\n\t"
    "ds_or_b32 v95, v113\n\t"
    "ds_write_b16 v132, v100\n\t"
    "v_cndmask_b32 v130, v130, v129, vcc\n\t"
    "v_ffbl_b32 v131, v130\n\t"
    "v_and_b32 v134, 7, v131\n\t"
    "v_lshlrev_b32 v134, v134, s45\n\t"
    "v_xor_b32 v129, v129, v134\n\t"
    "v_xor_b32 v132, v131, v127\n\t"
    "v_bfi_b32 v130, v104, 0, v129\n\t"
    "v_cmp_eq_u32 vcc, 0, v130\n\t"
    "v_and_or_b32 v97, v104, s44, v69\n\t"
    "v_xor_b32 v133, 23, v132\n\t"
    "v_lshlrev_b32 v114, v132, v136\n\t"
    "v_and_b32 v132, 7, v132\n\t"
    "v_lshl_or_b32 v114, v136, v133, v114\n\t"
    "v_lshl_add_u32 v132, v132, 1, v128\n\t"
    "ds_or_b32 v96, v114\n\t"
    "ds_write_b16 v132, v102\n\t"
    "v_cndmask_b32 v130, v130, v129, vcc\n\t"
    "v_ffbl_b32 v131, v130\n\t"
    "v_and_b32 v134, 7, v131\n\t"
    "v_lshlrev_b32 v134, v134, s45\n\t"
    "v_xor_b32 v129, v129, v134\n\t"
    "v_xor_b32 v132, v131, v127\n\t"
    "v_bfi_b32 v130, v103, 0, v129\n\t"
    "v_cmp_eq_u32 vcc, 0, v130\n\t"
    "v_and_or_b32 v98, v103, s44, v69\n\t"
    "v_xor_b32 v133, 23, v132\n\t"
    "v_lshlrev_b32 v115, v132, v136\n\t"
    "v_and_b32 v132, 7, v132\n\t"
    "v_lshl_or_b32 v115, v136, v133, v115\n\t"
    "v_lshl_add_u32 v132, v132, 1, v128\n\t"
    "ds_or_b32 v97, v115\n\t"
    "ds_write_b16 v132, v104\n\t"
    "v_cndmask_b32 v130, v130, v129, vcc\n\t"
    "v_ffbl_b32 v131, v130\n\t"
    "v_and_b32 v134, 7, v131\n\t"
    "v_lshlrev_b32 v134, v134, s45\n\t"
    "v_xor_b32 v129, v129, v134\n\t"
    "v_xor_b32 v132, v131, v127\n\t"
    "v_bfi_b32 v130, v107, 0, v129\n\t"
    "v_cmp_eq_u32 vcc, 0, v130\n\t"
    "v_and_or_b32 v99, v107, s44, v69\n\t"
    "v_xor_b32 v133, 23, v132\n\t"
    "v_lshlrev_b32 v116, v132, v136\n\t"
    "v_and_b32 v132, 7, v132\n\t"
    "v_lshl_or_b32 v116, v136, v133, v116\n\t"
    "v_lshl_add_u32 v132, v132, 1, v128\n\t"
    "ds_or_b32 v98, v116\n\t"
    "ds_write_b16 v132, v103\n\t"
    "v_cndmask_b32 v130, v130, v129, vcc\n\t"
    "v_ffbl_b32 v131, v130\n\t"
    "v_xor_b32 v132, v131, v127\n\t"
    "v_xor_b32 v133, 23, v132\n\t"
    "v_lshlrev_b32 v117, v132, v136\n\t"
    "v_and_b32 v132, 7, v132\n\t"
    "v_lshl_or_b32 v117, v136, v133, v117\n\t"
    "v_lshl_add_u32 v132, v132, 1, v128\n\t"
    "ds_or_b32 v99, v117\n\t"
    "ds_write_b16 v132, v107\n\t"
    "ds_read_b128 v[72:75], v128\n\t"
    "v_and_or_b32 v100, v76, s44, v69\n\t"
    "ds_read_b32 v100, v100\n\t"
    "v_and_or_b32 v101, v77, s44, v69\n\t"
    "ds_read_b32 v101, v101\n\t"
    "v_and_or_b32 v102, v78, s44, v69\n\t"
    "ds_read_b32 v102, v102\n\t"
    "v_and_or_b32 v103, v79, s44, v69\n\t"
    "ds_read_b32 v103, v103\n\t"
    "v_and_or_b32 v104, v80, s44, v69\n\t"
    "ds_read_b32 v104, v104\n\t"
    "v_and_or_b32 v105, v81, s44, v69\n\t"
    "ds_read_b32 v105, v105\n\t"
    "v_and_or_b32 v106, v82, s44, v69\n\t"
    "ds_read_b32 v106, v106\n\t"
    "v_and_or_b32 v107, v83, s44, v69\n\t"
    "ds_read_b32 v107, v107\n\t"
    "s_waitcnt lgkmcnt(8)\n\t"
    "buffer_store_dwordx4 v[72:75], v67, %12, s49 offen offset:448 sc1\n\t"
    "v_mov_b32 v129, 0xffff0000\n\t"
    "s_waitcnt lgkmcnt(4)\n\t"
    "v_perm_b32 v100, v100, v76, v126\n\t"
    "v_perm_b32 v101, v101, v77, v126\n\t"
    "v_perm_b32 v102, v102, v78, v126\n\t"
    "v_perm_b32 v103, v103, v79, v126\n\t"
    "s_waitcnt lgkmcnt(0)\n\t"
    "v_perm_b32 v104, v104, v80, v126\n\t"
    "v_perm_b32 v105, v105, v81, v126\n\t"
    "v_perm_b32 v106, v106, v82, v126\n\t"
    "v_perm_b32 v107, v107, v83, v126\n\t"
    "v_max_u32 v108, v100, v101\n\t"
    "v_min_u32 v101, v100, v101\n\t"
    "v_max_u32 v100, v102, v103\n\t"
    "v_min_u32 v103, v102, v103\n\t"
    "v_max_u32 v102, v104, v105\n\t"
    "v_min_u32 v105, v104, v105\n\t"
    "v_max_u32 v104, v106, v107\n\t"
    "v_min_u32 v107, v106, v107\n\t"
    "v_max_u32 v106, v108, v100\n\t"
    "v_min_u32 v100, v108, v100\n\t"
    "v_max_u32 v108, v101, v103\n\t"
    "v_min_u32 v103, v101, v103\n\t"
    "v_max_u32 v101, v102, v104\n\t"
    "v_min_u32 v104, v102, v104\n\t"
    "v_max_u32 v102, v105, v107\n\t"
    "v_min_u32 v107, v105, v107\n\t"
    "v_bfi_b32 v130, v106, 0, v129\n\t"
    "v_cmp_eq_u32 vcc, 0, v130\n\t"
    "v_and_or_b32 v92, v106, s44, v69\n\t"
    "s_nop 0\n\t"
    "v_cndmask_b32 v130, v130, v129, vcc\n\t"
    "v_ffbl_b32 v131, v130\n\t"
    "v_and_b32 v134, 7, v131\n\t"
    "v_lshlrev_b32 v134, v134, s45\n\t"
    "v_xor_b32 v129, v129, v134\n\t"
    "v_xor_b32 v132, v131, v127\n\t"
    "v_bfi_b32 v130, v101, 0, v129\n\t"
    "v_cmp_eq_u32 vcc, 0, v130\n\t"
    "v_and_b32 v132, 7, v132\n\t"
    "v_lshl_add_u32 v132, v132, 1, v128\n\t"
    "ds_write_b16 v132, v106\n\t"
    "v_cndmask_b32 v130, v130, v129, vcc\n\t"
    "v_ffbl_b32 v131, v130\n\t"
    "v_and_b32 v134, 7, v131\n\t"
    "v_lshlrev_b32 v134, v134, s45\n\t"
    "v_xor_b32 v129, v129, v134\n\t"
    "v_xor_b32 v132, v131, v127\n\t"
    "v_bfi_b32 v130, v108, 0, v129\n\t"
    "v_cmp_eq_u32 vcc, 0, v130\n\t"
    "v_and_b32 v132, 7, v132\n\t"
    "v_lshl_add_u32 v132, v132, 1, v128\n\t"
    "ds_write_b16 v132, v101\n\t"
    "v_cndmask_b32 v130, v130, v129, vcc\n\t"
    "v_ffbl_b32 v131, v130\n\t"
    "v_and_b32 v134, 7, v131\n\t"
    "v_lshlrev_b32 v134, v134, s45\n\t"
    "v_xor_b32 v129, v129, v134\n\t"
    "v_xor_b32 v132, v131, v127\n\t"
    "v_bfi_b32 v130, v100, 0, v129\n\t"
    "v_cmp_eq_u32 vcc, 0, v130\n\t"
    "v_and_b32 v132, 7, v132\n\t"
    "v_lshl_add_u32 v132, v132, 1, v128\n\t"
    "ds_write_b16 v132, v108\n\t"
    "v_cndmask_b32 v130, v130, v129, vcc\n\t"
    "v_ffbl_b32 v131, v130\n\t"
    "v_and_b32 v134, 7, v131\n\t"
    "v_lshlrev_b32 v134, v134, s45\n\t"
    "v_xor_b32 v129, v129, v134\n\t"
    "v_xor_b32 v132, v131, v127\n\t"
    "v_bfi_b32 v130, v102, 0, v129\n\t"
    "v_cmp_eq_u32 vcc, 0, v130\n\t"
    "v_and_b32 v132, 7, v132\n\t"
    "v_lshl_add_u32 v132, v132, 1, v128\n\t"
    "ds_write_b16 v132, v100\n\t"
    "v_cndmask_b32 v130, v130, v129, vcc\n\t"
    "v_ffbl_b32 v131, v130\n\t"
    "v_and_b32 v134, 7, v131\n\t"
    "v_lshlrev_b32 v134, v134, s45\n\t"
    "v_xor_b32 v129, v129, v134\n\t"
    "v_xor_b32 v132, v131, v127\n\t"
    "v_bfi_b32 v130, v104, 0, v129\n\t"
    "v_cmp_eq_u32 vcc, 0, v130\n\t"
    "v_and_b32 v132, 7, v132\n\t"
    "v_lshl_add_u32 v132, v132, 1, v128\n\t"
    "ds_write_b16 v132, v102\n\t"
    "v_cndmask_b32 v130, v130, v129, vcc\n\t"
    "v_ffbl_b32 v131, v130\n\t"
    "v_and_b32 v134, 7, v131\n\t"
    "v_lshlrev_b32 v134, v134, s45\n\t"
    "v_xor_b32 v129, v129, v134\n\t"
    "v_xor_b32 v132, v131, v127\n\t"
    "v_bfi_b32 v130, v103, 0, v129\n\t"
    "v_cmp_eq_u32 vcc, 0, v130\n\t"
    "v_and_b32 v132, 7, v132\n\t"
    "v_lshl_add_u32 v132, v132, 1, v128\n\t"
    "ds_write_b16 v132, v104\n\t"
    "v_cndmask_b32 v130, v130, v129, vcc\n\t"
    "v_ffbl_b32 v131, v130\n\t"
    "v_and_b32 v134, 7, v131\n\t"
    "v_lshlrev_b32 v134, v134, s45\n\t"
    "v_xor_b32 v129, v129, v134\n\t"
    "v_xor_b32 v132, v131, v127\n\t"
    "v_bfi_b32 v130, v107, 0, v129\n\t"
    "v_cmp_eq_u32 vcc, 0, v130\n\t"
    "v_and_b32 v132, 7, v132\n\t"
    "v_lshl_add_u32 v132, v132, 1, v128\n\t"
    "ds_write_b16 v132, v103\n\t"
    "v_cndmask_b32 v130, v130, v129, vcc\n\t"
    "v_ffbl_b32 v131, v130\n\t"
    "v_xor_b32 v132, v131, v127\n\t"
    "v_and_b32 v132, 7, v132\n\t"
    "v_lshl_add_u32 v132, v132, 1, v128\n\t"
    "ds_write_b16 v132, v107\n\t"
    "ds_read_b128 v[72:75], v128\n\t"
    "s_waitcnt lgkmcnt(0)\n\t"
    "buffer_store_dwordx4 v[72:75], v67, %12, s49 offen offset:480 sc1\n\t"
    "s_setprio 0\n\t"
    "v_mov_b32 v134, 1036\n\t"
    "L_asvsp12d_%=:\n\t"
    "ds_read_b32 v130, v134\n\t"
    "s_waitcnt lgkmcnt(0)\n\t"
    "v_readfirstlane_b32 s50, v130\n\t"
    "s_nop 0\n\t"
    "s_cmp_lt_u32 s50, 4\n\t"
    "s_cbranch_scc1 L_asvsp12d_%=\n\t"
    "s_call_b64 s[98:99], L_setup_%=\n\t"
    "v_lshrrev_b32 v105, 4, v127\n\t"
    "v_and_b32 v105, 48, v105\n\t"
    "s_movk_i32 s65, 130\n\t"
    "v_mad_u32_u24 v104, v155, s65, v105\n\t"
    "v_add_u32 v105, 14170, v104\n\t"
    "v_and_b32 v104, -16, v104\n\t"
    "v_and_b32 v105, -16, v105\n\t"
    "ds_read_b128 v[164:167], v104 offset:0\n\t"
    "ds_read_b128 v[168:171], v104 offset:64\n\t"
    "ds_read_b128 v[172:175], v104 offset:2080\n\t"
    "ds_read_b128 v[176:179], v104 offset:2144\n\t"
    "ds_read_b128 v[180:183], v104 offset:4160\n\t"
    "ds_read_b128 v[184:187], v104 offset:4224\n\t"
    "ds_read_b128 v[188:191], v104 offset:6240\n\t"
    "ds_read_b128 v[192:195], v104 offset:6304\n\t"
    "ds_read_b128 v[196:199], v104 offset:8320\n\t"
    "ds_read_b128 v[200:203], v104 offset:8384\n\t"
    "ds_read_b128 v[204:207], v104 offset:10400\n\t"
    "ds_read_b128 v[208:211], v104 offset:10464\n\t"
    "s_cmp_lt_u32 s62, 2\n\t"
    "s_cbranch_scc1 L_mel0_%=\n\t"
    "buffer_load_dwordx4 v[220:223], v127, %12, s61 offen offset:2048 sc1\n\t"
    "buffer_load_dwordx4 v[224:227], v127, %12, s61 offen offset:3072 sc1\n\t"
    "s_add_u32 s61, s61, 0x1000\n\t"
    "buffer_load_dwordx4 v[228:231], v127, %12, s61 offen offset:2048 sc1\n\t"
    "buffer_load_dwordx4 v[232:235], v127, %12, s61 offen offset:3072 sc1\n\t"
    "s_add_u32 s61, s61, 0x1000\n\t"
    "buffer_load_dwordx4 v[236:239], v127, %12, s61 offen offset:2048 sc1\n\t"
    "buffer_load_dwordx4 v[240:243], v127, %12, s61 offen offset:3072 sc1\n\t"
    "s_add_u32 s61, s61, 0x1000\n\t"
    "buffer_load_dwordx4 v[244:247], v127, %12, s61 offen offset:2048 sc1\n\t"
    "buffer_load_dwordx4 v[248:251], v127, %12, s61 offen offset:3072 sc1\n\t"
    "s_add_u32 s61, s61, 0x1000\n\t"
    "buffer_load_dwordx4 v[252:255], v127, %12, s61 offen offset:2048 sc1\n\t"
    "s_mov_b32 s62, 1\n\t"
    "L_mel0_%=:\n\t"
    "buffer_load_dwordx4 v[0:3], v127, %12, s63 offen offset:0 sc1\n\t"
    "buffer_load_dwordx4 v[4:7], v127, %12, s63 offen offset:1024 sc1\n\t"
    "buffer_load_dwordx4 v[8:11], v127, %12, s63 offen offset:2048 sc1\n\t"
    "buffer_load_dwordx4 v[12:15], v127, %12, s63 offen offset:3072 sc1\n\t"
    "s_add_u32 s63, s63, 0x1000\n\t"
    "s_mov_b32 s62, 0\n\t"
    "s_and_b32 s65, %14, 8\n\t"
    "s_lshl_b32 s65, s65, 10\n\t"
    "s_add_u32 s65, s65, s63\n\t"
    "s_mov_b32 m0, 136192\n\t"
    "s_add_u32 s65, s65, 0\n\t"
    "buffer_load_dwordx4 v127, %12, s65 offen offset:0 sc1 lds\n\t"
    "buffer_load_dwordx4 v127, %12, s65 offen offset:1024 sc1 lds\n\t"
    "buffer_load_dwordx4 v127, %12, s65 offen offset:2048 sc1 lds\n\t"
    "buffer_load_dwordx4 v127, %12, s65 offen offset:3072 sc1 lds\n\t"
    "s_mov_b32 m0, 140288\n\t"
    "s_add_u32 s65, s65, 4096\n\t"
    "buffer_load_dwordx4 v127, %12, s65 offen offset:0 sc1 lds\n\t"
    "buffer_load_dwordx4 v127, %12, s65 offen offset:1024 sc1 lds\n\t"
    "buffer_load_dwordx4 v127, %12, s65 offen offset:2048 sc1 lds\n\t"
    "buffer_load_dwordx4 v127, %12, s65 offen offset:3072 sc1 lds\n\t"
    "s_waitcnt vmcnt(21)\n\t"
    "s_lshr_b32 s50, %14, 6\n\t"
    "s_lshl_b32 s50, s50, 7\n\t"
    "s_add_u32 s50, s50, 49287168\n\t"
    "s_mov_b64 exec, 1\n\t"
    "buffer_atomic_add v136, off, %12, s50 sc1\n\t"
    "s_mov_b64 exec, -1\n\t"
    "s_branch L_af1x_%=\n\t"
    "L_af1w_%=:\n\t"
    "s_mov_b32 s51, 1048576\n\t"
    "s_mov_b64 exec, 0xff\n\t"
    "L_af1_%=:\n\t"
    "v_cmp_gt_u32 vcc, v148, v71\n\t"
    "s_cbranch_vccz L_af1ok_%=\n\t"
    "s_sleep 8\n\t"
    "buffer_load_dword v71, v70, %12, s52 offen sc1\n\t"
    "s_sub_u32 s51, s51, 1\n\t"
    "s_waitcnt vmcnt(0)\n\t"
    "s_cmp_lg_u32 s51, 0\n\t"
    "s_cbranch_scc1 L_af1_%=\n\t"
    "L_af1ok_%=:\n\t"
    "s_mov_b64 exec, -1\n\t"
    "v_mov_b32 v134, 3112\n\t"
    "ds_write_b32 v134, v136\n\t"
    "v_mbcnt_lo_u32_b32 v70, -1, 0\n\t"
    "v_mbcnt_hi_u32_b32 v70, -1, v70\n\t"
    "s_and_b32 s50, %14, 7\n\t"
    "s_lshl_b32 s50, s50, 4\n\t"
    "s_lshr_b32 s51, %14, 4\n\t"
    "s_add_u32 s50, s50, s51\n\t"
    "s_mul_i32 s50, s50, 102400\n\t"
    "s_add_u32 s50, s50, 3407872\n\t"
    "v_lshlrev_b32 v148, 7, v70\n\t"
    "buffer_load_dword v71, v148, %12, s50 offen sc1\n\t"
    "s_and_b32 s51, %14, 8\n\t"
    "s_lshl_b32 s51, s51, 10\n\t"
    "s_add_u32 s50, s50, s51\n\t"
    "s_add_u32 s50, s50, 0x1000\n\t"
    "v_lshrrev_b32 v149, 4, v70\n\t"
    "v_lshl_add_u32 v149, v149, 11, v148\n\t"
    "buffer_load_dword v71, v148, %12, s50 offen sc1\n\t"
    "s_bfe_u32 s50, %14, 0x40003\n\t"
    "s_lshl_b32 s50, s50, 14\n\t"
    "s_add_u32 s50, s50, 16517120\n\t"
    "v_lshrrev_b32 v150, 3, v70\n\t"
    "v_lshl_add_u32 v150, v150, 10, v148\n\t"
    "buffer_load_dword v71, v149, %12, s50 offen sc1\n\t"
    "s_and_b32 s50, %14, 8\n\t"
    "s_bfe_u32 s51, %14, 0x30004\n\t"
    "s_add_u32 s50, s50, s51\n\t"
    "s_mul_i32 s50, s50, 212992\n\t"
    "s_add_u32 s50, s50, 196608\n\t"
    "buffer_load_dword v71, v150, %12, s50 offen sc1\n\t"
    "s_setpc_b64 s[54:55]\n\t"
    "L_af1x_%=:\n\t"
    "s_mov_b32 s69, 0x40000000\n\t"
    "s_branch L_aend2_%=\n\t"
    "L_ahelp_%=:\n\t"
    "s_setprio 3\n\t"
    "v_cmp_gt_u32 vcc, 48, v147\n\t"
    "v_mov_b32 v133, 0x40000000\n\t"
    "v_lshrrev_b32 v134, 3, v147\n\t"
    "v_add_u32 v134, 6, v134\n\t"
    "v_bfe_u32 v135, v147, 2, 1\n\t"
    "v_and_b32 v130, 3, v147\n\t"
    "v_lshl_add_u32 v132, v135, 5, v130\n\t"
    "v_mul_u32_u24 v64, 400, v132\n\t"
    "v_lshl_add_u32 v64, v134, 5, v64\n\t"
    "s_movk_i32 s50, 0\n\t"
    "buffer_load_dwordx4 v[0:3], v64, %0, s50 offen offset:16\n\t"
    "buffer_load_dwordx4 v[4:7], v64, %0, s50 offen offset:32\n\t"
    "v_lshlrev_b32 v66, 14, v134\n\t"
    "v_lshl_add_u32 v66, v132, 4, v66\n\t"
    "v_lshlrev_b32 v68, 8, v134\n\t"
    "v_lshl_add_u32 v68, v135, 2, v68\n\t"
    "v_add_u32 v68, 130048, v68\n\t"
    "v_cndmask_b32 v68, v133, v68, vcc\n\t"
    "v_cndmask_b32 v66, v133, v66, vcc\n\t"
    "s_barrier\n\t"
    "s_load_dwordx2 s[72:73], %6, 0\n\t"
    "s_load_dwordx2 s[88:89], %7, 0\n\t"
    "s_load_dword s91, %8, 0\n\t"
    "v_lshlrev_b32 v128, 4, v147\n\t"
    "v_add_u32 v128, 133888, v128\n\t"
    "v_and_b32 v130, 1, v147\n\t"
    "v_cndmask_b32 v128, v133, v128, vcc\n\t"
    "v_mul_u32_u24 v127, 7, v130\n\t"
    "v_cmp_eq_u32 vcc, 1, v130\n\t"
    "v_mov_b32 v126, 0x07060100\n\t"
    "v_mov_b32 v133, 0x05040100\n\t"
    "v_mov_b32 v136, 1\n\t"
    "v_cndmask_b32 v126, v126, v133, vcc\n\t"
    "s_mov_b32 s36, 0\n\t"
    "s_mov_b32 s37, 8000\n\t"
    "s_mov_b32 s38, 16000\n\t"
    "s_mov_b32 s39, 24000\n\t"
    "s_mov_b32 s40, 32000\n\t"
    "s_mov_b32 s41, 40000\n\t"
    "s_mov_b32 s42, 48000\n\t"
    "s_mov_b32 s43, 56000\n\t"
    "s_movk_i32 s44, 0xf8\n\t"
    "s_mov_b32 s45, 0x01010000\n\t"
    "s_mov_b32 s46, 0x020c000c\n\t"
    "s_lshr_b32 s49, %14, 4\n\t"
    "s_mul_i32 s49, s49, 212992\n\t"
    "s_and_b32 s50, %14, 15\n\t"
    "s_lshl_b32 s50, s50, 10\n\t"
    "s_add_u32 s49, s49, s50\n\t"
    "s_lshl_b32 s52, %14, 2\n\t"
    "s_add_u32 s52, s52, 49291264\n\t"
    "buffer_load_dword v70, off, %12, s52 offset:1024 sc1\n\t"
    "s_cmp_eq_u32 %14, 0\n\t"
    "s_cselect_b32 s47, 49291264, 0x40000000\n\t"
    "s_cselect_b32 s48, 49283072, 0x40000000\n\t"
    "v_lshlrev_b32 v152, 4, v147\n\t"
    "v_lshlrev_b32 v132, 7, v147\n\t"
    "v_mov_b32 v71, 0\n\t"
    "buffer_store_dword v71, v132, %12, s48 offen sc1\n\t"
    "v_mov_b32 v148, 0\n\t"
    "v_mov_b32 v149, 0\n\t"
    "v_mov_b32 v150, 0\n\t"
    "v_mov_b32 v151, 0\n\t"
    "buffer_store_dwordx4 v[148:151], v152, %12, s47 offen offset:2048 sc1\n\t"
    "buffer_store_dwordx4 v[148:151], v152, %12, s47 offen offset:3072 sc1\n\t"
    "s_add_u32 s50, s48, 0x1000\n\t"
    "s_waitcnt vmcnt(6)\n\t"
    "v_lshlrev_b32 v134, 20, v147\n\t"
    "v_add_u32 v134, 2068, v134\n\t"
    "ds_add_u32 v134, v136\n\t"
    "v_mov_b32 v110, 65664\n\t"
    "v_mov_b32 v111, 131136\n\t"
    "v_mov_b32 v112, 262176\n\t"
    "v_mov_b32 v113, 524304\n\t"
    "v_mov_b32 v114, 1048584\n\t"
    "v_mov_b32 v115, 2097156\n\t"
    "v_mov_b32 v116, 4194306\n\t"
    "v_mov_b32 v117, 8388609\n\t"
    "s_waitcnt vmcnt(4)\n\t"
    "v_lshl_add_u32 v76, v0, 3, s36\n\t"
    "v_lshl_add_u32 v77, v1, 3, s37\n\t"
    "v_lshl_add_u32 v78, v2, 3, s38\n\t"
    "v_lshl_add_u32 v79, v3, 3, s39\n\t"
    "v_lshl_add_u32 v80, v4, 3, s40\n\t"
    "v_lshl_add_u32 v81, v5, 3, s41\n\t"
    "v_lshl_add_u32 v82, v6, 3, s42\n\t"
    "v_lshl_add_u32 v83, v7, 3, s43\n\t"
    "v_and_or_b32 v92, v76, s44, v68\n\t"
    "ds_or_rtn_b32 v118, v92, v110\n\t"
    "v_and_or_b32 v93, v77, s44, v68\n\t"
    "ds_or_rtn_b32 v119, v93, v111\n\t"
    "v_and_or_b32 v94, v78, s44, v68\n\t"
    "ds_or_rtn_b32 v120, v94, v112\n\t"
    "v_and_or_b32 v95, v79, s44, v68\n\t"
    "ds_or_rtn_b32 v121, v95, v113\n\t"
    "v_and_or_b32 v96, v80, s44, v68\n\t"
    "ds_or_rtn_b32 v122, v96, v114\n\t"
    "v_and_or_b32 v97, v81, s44, v68\n\t"
    "ds_or_rtn_b32 v123, v97, v115\n\t"
    "v_and_or_b32 v98, v82, s44, v68\n\t"
    "ds_or_rtn_b32 v124, v98, v116\n\t"
    "v_and_or_b32 v99, v83, s44, v68\n\t"
    "ds_or_rtn_b32 v125, v99, v117\n\t"
    "v_lshl_or_b32 v164, v77, 16, v76\n\t"
    "v_lshl_or_b32 v165, v79, 16, v78\n\t"
    "v_lshl_or_b32 v166, v81, 16, v80\n\t"
    "v_lshl_or_b32 v167, v83, 16, v82\n\t"
    "s_movk_i32 s50, 0\n\t"
    "buffer_load_dwordx4 v[8:11], v64, %0, s50 offen offset:1616\n\t"
    "buffer_load_dwordx4 v[12:15], v64, %0, s50 offen offset:1632\n\t"
    "s_movk_i32 s50, 3200\n\t"
    "buffer_load_dwordx4 v[16:19], v64, %0, s50 offen offset:16\n\t"
    "buffer_load_dwordx4 v[20:23], v64, %0, s50 offen offset:32\n\t"
    "s_waitcnt vmcnt(7)\n\t"
    "v_add_u32 v70, 1, v70\n\t"
    "buffer_store_dword v70, off, %12, s52 sc1\n\t"
    "v_mov_b32 v71, 2072\n\t"
    "ds_write_b32 v71, v70\n\t"
    "s_waitcnt vmcnt(3)\n\t"
    "v_lshl_add_u32 v84, v8, 3, s36\n\t"
    "v_lshl_add_u32 v85, v9, 3, s37\n\t"
    "v_lshl_add_u32 v86, v10, 3, s38\n\t"
    "v_lshl_add_u32 v87, v11, 3, s39\n\t"
    "v_lshl_add_u32 v88, v12, 3, s40\n\t"
    "v_lshl_add_u32 v89, v13, 3, s41\n\t"
    "v_lshl_add_u32 v90, v14, 3, s42\n\t"
    "v_lshl_add_u32 v91, v15, 3, s43\n\t"
    "s_waitcnt lgkmcnt(5)\n\t"
    "v_and_b32 v118, v118, v110\n\t"
    "v_perm_b32 v118, v118, v118, s46\n\t"
    "ds_or_b32 v92, v118\n\t"
    "v_and_b32 v119, v119, v111\n\t"
    "v_perm_b32 v119, v119, v119, s46\n\t"
    "ds_or_b32 v93, v119\n\t"
    "v_and_b32 v120, v120, v112\n\t"
    "v_perm_b32 v120, v120, v120, s46\n\t"
    "ds_or_b32 v94, v120\n\t"
    "v_and_b32 v121, v121, v113\n\t"
    "v_perm_b32 v121, v121, v121, s46\n\t"
    "ds_or_b32 v95, v121\n\t"
    "s_waitcnt lgkmcnt(5)\n\t"
    "v_and_b32 v122, v122, v114\n\t"
    "v_perm_b32 v122, v122, v122, s46\n\t"
    "ds_or_b32 v96, v122\n\t"
    "v_and_b32 v123, v123, v115\n\t"
    "v_perm_b32 v123, v123, v123, s46\n\t"
    "ds_or_b32 v97, v123\n\t"
    "v_and_b32 v124, v124, v116\n\t"
    "v_perm_b32 v124, v124, v124, s46\n\t"
    "ds_or_b32 v98, v124\n\t"
    "v_and_b32 v125, v125, v117\n\t"
    "v_perm_b32 v125, v125, v125, s46\n\t"
    "ds_or_b32 v99, v125\n\t"
    "buffer_store_dwordx4 v[164:167], v66, %12, s49 offen offset:0 sc1\n\t"
    "v_and_or_b32 v100, v84, s44, v68\n\t"
    "ds_read_b32 v100, v100\n\t"
    "v_and_or_b32 v101, v85, s44, v68\n\t"
    "ds_read_b32 v101, v101\n\t"
    "v_and_or_b32 v102, v86, s44, v68\n\t"
    "ds_read_b32 v102, v102\n\t"
    "v_and_or_b32 v103, v87, s44, v68\n\t"
    "ds_read_b32 v103, v103\n\t"
    "v_and_or_b32 v104, v88, s44, v68\n\t"
    "ds_read_b32 v104, v104\n\t"
    "v_and_or_b32 v105, v89, s44, v68\n\t"
    "ds_read_b32 v105, v105\n\t"
    "v_and_or_b32 v106, v90, s44, v68\n\t"
    "ds_read_b32 v106, v106\n\t"
    "v_and_or_b32 v107, v91, s44, v68\n\t"
    "ds_read_b32 v107, v107\n\t"
    "s_waitcnt vmcnt(2)\n\t"
    "v_lshl_add_u32 v76, v16, 3, s36\n\t"
    "v_lshl_add_u32 v77, v17, 3, s37\n\t"
    "v_lshl_add_u32 v78, v18, 3, s38\n\t"
    "v_lshl_add_u32 v79, v19, 3, s39\n\t"
    "v_lshl_add_u32 v80, v20, 3, s40\n\t"
    "v_lshl_add_u32 v81, v21, 3, s41\n\t"
    "v_lshl_add_u32 v82, v22, 3, s42\n\t"
    "v_lshl_add_u32 v83, v23, 3, s43\n\t"
    "s_movk_i32 s50, 3200\n\t"
    "buffer_load_dwordx4 v[24:27], v64, %0, s50 offen offset:1616\n\t"
    "buffer_load_dwordx4 v[28:31], v64, %0, s50 offen offset:1632\n\t"
    "s_movk_i32 s50, 6400\n\t"
    "buffer_load_dwordx4 v[32:35], v64, %0, s50 offen offset:16\n\t"
    "buffer_load_dwordx4 v[36:39], v64, %0, s50 offen offset:32\n\t"
    "s_cmp_lg_u32 %14, 0\n\t"
    "s_cbranch_scc1 L_ahe184_%=\n\t"
    "buffer_load_dwordx4 v[148:151], v152, %12, s47 offen sc1\n\t"
    "L_ahe184_%=:\n\t"
    "v_mov_b32 v129, 0xffff0000\n\t"
    "s_waitcnt lgkmcnt(4)\n\t"
    "v_perm_b32 v100, v100, v84, v126\n\t"
    "v_perm_b32 v101, v101, v85, v126\n\t"
    "v_perm_b32 v102, v102, v86, v126\n\t"
    "v_perm_b32 v103, v103, v87, v126\n\t"
    "s_waitcnt lgkmcnt(0)\n\t"
    "v_perm_b32 v104, v104, v88, v126\n\t"
    "v_perm_b32 v105, v105, v89, v126\n\t"
    "v_perm_b32 v106, v106, v90, v126\n\t"
    "v_perm_b32 v107, v107, v91, v126\n\t"
    "v_max_u32 v108, v100, v101\n\t"
    "v_min_u32 v101, v100, v101\n\t"
    "v_max_u32 v100, v102, v103\n\t"
    "v_min_u32 v103, v102, v103\n\t"
    "v_max_u32 v102, v104, v105\n\t"
    "v_min_u32 v105, v104, v105\n\t"
    "v_max_u32 v104, v106, v107\n\t"
    "v_min_u32 v107, v106, v107\n\t"
    "v_max_u32 v106, v108, v100\n\t"
    "v_min_u32 v100, v108, v100\n\t"
    "v_max_u32 v108, v101, v103\n\t"
    "v_min_u32 v103, v101, v103\n\t"
    "v_max_u32 v101, v102, v104\n\t"
    "v_min_u32 v104, v102, v104\n\t"
    "v_max_u32 v102, v105, v107\n\t"
    "v_min_u32 v107, v105, v107\n\t"
    "v_bfi_b32 v130, v106, 0, v129\n\t"
    "v_cmp_eq_u32 vcc, 0, v130\n\t"
    "v_and_or_b32 v92, v106, s44, v68\n\t"
    "s_nop 0\n\t"
    "v_cndmask_b32 v130, v130, v129, vcc\n\t"
    "v_ffbl_b32 v131, v130\n\t"
    "v_and_b32 v134, 7, v131\n\t"
    "v_lshlrev_b32 v134, v134, s45\n\t"
    "v_xor_b32 v129, v129, v134\n\t"
    "v_xor_b32 v132, v131, v127\n\t"
    "v_bfi_b32 v130, v101, 0, v129\n\t"
    "v_cmp_eq_u32 vcc, 0, v130\n\t"
    "v_and_or_b32 v93, v101, s44, v68\n\t"
    "v_xor_b32 v133, 23, v132\n\t"
    "v_lshlrev_b32 v110, v132, v136\n\t"
    "v_and_b32 v132, 7, v132\n\t"
    "v_lshl_or_b32 v110, v136, v133, v110\n\t"
    "v_lshl_add_u32 v132, v132, 1, v128\n\t"
    "ds_or_rtn_b32 v118, v92, v110\n\t"
    "ds_write_b16 v132, v106\n\t"
    "v_cndmask_b32 v130, v130, v129, vcc\n\t"
    "v_ffbl_b32 v131, v130\n\t"
    "v_and_b32 v134, 7, v131\n\t"
    "v_lshlrev_b32 v134, v134, s45\n\t"
    "v_xor_b32 v129, v129, v134\n\t"
    "v_xor_b32 v132, v131, v127\n\t"
    "v_bfi_b32 v130, v108, 0, v129\n\t"
    "v_cmp_eq_u32 vcc, 0, v130\n\t"
    "v_and_or_b32 v94, v108, s44, v68\n\t"
    "v_xor_b32 v133, 23, v132\n\t"
    "v_lshlrev_b32 v111, v132, v136\n\t"
    "v_and_b32 v132, 7, v132\n\t"
    "v_lshl_or_b32 v111, v136, v133, v111\n\t"
    "v_lshl_add_u32 v132, v132, 1, v128\n\t"
    "ds_or_rtn_b32 v119, v93, v111\n\t"
    "ds_write_b16 v132, v101\n\t"
    "v_cndmask_b32 v130, v130, v129, vcc\n\t"
    "v_ffbl_b32 v131, v130\n\t"
    "v_and_b32 v134, 7, v131\n\t"
    "v_lshlrev_b32 v134, v134, s45\n\t"
    "v_xor_b32 v129, v129, v134\n\t"
    "v_xor_b32 v132, v131, v127\n\t"
    "v_bfi_b32 v130, v100, 0, v129\n\t"
    "v_cmp_eq_u32 vcc, 0, v130\n\t"
    "v_and_or_b32 v95, v100, s44, v68\n\t"
    "v_xor_b32 v133, 23, v132\n\t"
    "v_lshlrev_b32 v112, v132, v136\n\t"
    "v_and_b32 v132, 7, v132\n\t"
    "v_lshl_or_b32 v112, v136, v133, v112\n\t"
    "v_lshl_add_u32 v132, v132, 1, v128\n\t"
    "ds_or_rtn_b32 v120, v94, v112\n\t"
    "ds_write_b16 v132, v108\n\t"
    "v_cndmask_b32 v130, v130, v129, vcc\n\t"
    "v_ffbl_b32 v131, v130\n\t"
    "v_and_b32 v134, 7, v131\n\t"
    "v_lshlrev_b32 v134, v134, s45\n\t"
    "v_xor_b32 v129, v129, v134\n\t"
    "v_xor_b32 v132, v131, v127\n\t"
    "v_bfi_b32 v130, v102, 0, v129\n\t"
    "v_cmp_eq_u32 vcc, 0, v130\n\t"
    "v_and_or_b32 v96, v102, s44, v68\n\t"
    "v_xor_b32 v133, 23, v132\n\t"
    "v_lshlrev_b32 v113, v132, v136\n\t"
    "v_and_b32 v132, 7, v132\n\t"
    "v_lshl_or_b32 v113, v136, v133, v113\n\t"
    "v_lshl_add_u32 v132, v132, 1, v128\n\t"
    "ds_or_rtn_b32 v121, v95, v113\n\t"
    "ds_write_b16 v132, v100\n\t"
    "v_cndmask_b32 v130, v130, v129, vcc\n\t"
    "v_ffbl_b32 v131, v130\n\t"
    "v_and_b32 v134, 7, v131\n\t"
    "v_lshlrev_b32 v134, v134, s45\n\t"
    "v_xor_b32 v129, v129, v134\n\t"
    "v_xor_b32 v132, v131, v127\n\t"
    "s_waitcnt lgkmcnt(7)\n\t"
    "v_and_b32 v118, v118, v110\n\t"
    "v_perm_b32 v118, v118, v118, s46\n\t"
    "ds_or_b32 v92, v118\n\t"
    "v_bfi_b32 v130, v104, 0, v129\n\t"
    "v_cmp_eq_u32 vcc, 0, v130\n\t"
    "v_and_or_b32 v97, v104, s44, v68\n\t"
    "v_xor_b32 v133, 23, v132\n\t"
    "v_lshlrev_b32 v114, v132, v136\n\t"
    "v_and_b32 v132, 7, v132\n\t"
    "v_lshl_or_b32 v114, v136, v133, v114\n\t"
    "v_lshl_add_u32 v132, v132, 1, v128\n\t"
    "ds_or_rtn_b32 v122, v96, v114\n\t"
    "ds_write_b16 v132, v102\n\t"
    "v_cndmask_b32 v130, v130, v129, vcc\n\t"
    "v_ffbl_b32 v131, v130\n\t"
    "v_and_b32 v134, 7, v131\n\t"
    "v_lshlrev_b32 v134, v134, s45\n\t"
    "v_xor_b32 v129, v129, v134\n\t"
    "v_xor_b32 v132, v131, v127\n\t"
    "s_waitcnt lgkmcnt(8)\n\t"
    "v_and_b32 v119, v119, v111\n\t"
    "v_perm_b32 v119, v119, v119, s46\n\t"
    "ds_or_b32 v93, v119\n\t"
    "v_bfi_b32 v130, v103, 0, v129\n\t"
    "v_cmp_eq_u32 vcc, 0, v130\n\t"
    "v_and_or_b32 v98, v103, s44, v68\n\t"
    "v_xor_b32 v133, 23, v132\n\t"
    "v_lshlrev_b32 v115, v132, v136\n\t"
    "v_and_b32 v132, 7, v132\n\t"
    "v_lshl_or_b32 v115, v136, v133, v115\n\t"
    "v_lshl_add_u32 v132, v132, 1, v128\n\t"
    "ds_or_rtn_b32 v123, v97, v115\n\t"
    "ds_write_b16 v132, v104\n\t"
    "v_cndmask_b32 v130, v130, v129, vcc\n\t"
    "v_ffbl_b32 v131, v130\n\t"
    "v_and_b32 v134, 7, v131\n\t"
    "v_lshlrev_b32 v134, v134, s45\n\t"
    "v_xor_b32 v129, v129, v134\n\t"
    "v_xor_b32 v132, v131, v127\n\t"
    "s_waitcnt lgkmcnt(9)\n\t"
    "v_and_b32 v120, v120, v112\n\t"
    "v_perm_b32 v120, v120, v120, s46\n\t"
    "ds_or_b32 v94, v120\n\t"
    "v_bfi_b32 v130, v107, 0, v129\n\t"
    "v_cmp_eq_u32 vcc, 0, v130\n\t"
    "v_and_or_b32 v99, v107, s44, v68\n\t"
    "v_xor_b32 v133, 23, v132\n\t"
    "v_lshlrev_b32 v116, v132, v136\n\t"
    "v_and_b32 v132, 7, v132\n\t"
    "v_lshl_or_b32 v116, v136, v133, v116\n\t"
    "v_lshl_add_u32 v132, v132, 1, v128\n\t"
    "ds_or_rtn_b32 v124, v98, v116\n\t"
    "ds_write_b16 v132, v103\n\t"
    "v_cndmask_b32 v130, v130, v129, vcc\n\t"
    "v_ffbl_b32 v131, v130\n\t"
    "v_xor_b32 v132, v131, v127\n\t"
    "s_waitcnt lgkmcnt(10)\n\t"
    "v_and_b32 v121, v121, v113\n\t"
    "v_perm_b32 v121, v121, v121, s46\n\t"
    "ds_or_b32 v95, v121\n\t"
    "v_xor_b32 v133, 23, v132\n\t"
    "v_lshlrev_b32 v117, v132, v136\n\t"
    "v_and_b32 v132, 7, v132\n\t"
    "v_lshl_or_b32 v117, v136, v133, v117\n\t"
    "v_lshl_add_u32 v132, v132, 1, v128\n\t"
    "ds_or_rtn_b32 v125, v99, v117\n\t"
    "ds_write_b16 v132, v107\n\t"
    "s_waitcnt lgkmcnt(10)\n\t"
    "v_and_b32 v122, v122, v114\n\t"
    "v_perm_b32 v122, v122, v122, s46\n\t"
    "ds_or_b32 v96, v122\n\t"
    "ds_read_b128 v[72:75], v128\n\t"
    "v_and_or_b32 v100, v76, s44, v68\n\t"
    "ds_read_b32 v100, v100\n\t"
    "v_and_or_b32 v101, v77, s44, v68\n\t"
    "ds_read_b32 v101, v101\n\t"
    "v_and_or_b32 v102, v78, s44, v68\n\t"
    "ds_read_b32 v102, v102\n\t"
    "v_and_or_b32 v103, v79, s44, v68\n\t"
    "ds_read_b32 v103, v103\n\t"
    "v_and_or_b32 v104, v80, s44, v68\n\t"
    "ds_read_b32 v104, v104\n\t"
    "v_and_or_b32 v105, v81, s44, v68\n\t"
    "ds_read_b32 v105, v105\n\t"
    "v_and_or_b32 v106, v82, s44, v68\n\t"
    "ds_read_b32 v106, v106\n\t"
    "v_and_or_b32 v107, v83, s44, v68\n\t"
    "ds_read_b32 v107, v107\n\t"
    "s_cmp_lg_u32 %14, 0\n\t"
    "s_cbranch_scc1 L_ahe378_%=\n\t"
    "s_waitcnt vmcnt(0)\n\t"
    "buffer_store_dwordx4 v[148:151], v152, %12, s47 offen offset:1024 sc1\n\t"
    "L_ahe378_%=:\n\t"
    "s_waitcnt vmcnt(2)\n\t"
    "v_lshl_add_u32 v84, v24, 3, s36\n\t"
    "v_lshl_add_u32 v85, v25, 3, s37\n\t"
    "v_lshl_add_u32 v86, v26, 3, s38\n\t"
    "v_lshl_add_u32 v87, v27, 3, s39\n\t"
    "v_lshl_add_u32 v88, v28, 3, s40\n\t"
    "v_lshl_add_u32 v89, v29, 3, s41\n\t"
    "v_lshl_add_u32 v90, v30, 3, s42\n\t"
    "v_lshl_add_u32 v91, v31, 3, s43\n\t"
    "v_mul_u32_u24 v58, 400, v147\n\t"
    "buffer_load_dwordx4 v[48:51], v58, %0, 0 offen\n\t"
    "s_lshr_b32 s50, %14, 5\n\t"
    "s_mul_i32 s50, s50, 125\n\t"
    "s_bfe_u32 s51, %14, 0x30002\n\t"
    "s_lshl_b32 s51, s51, 4\n\t"
    "s_min_u32 s51, s51, 109\n\t"
    "s_add_u32 s50, s50, s51\n\t"
    "s_lshl_b32 s50, s50, 8\n\t"
    "s_and_b32 s51, %14, 3\n\t"
    "s_lshl_b32 s51, s51, 6\n\t"
    "s_add_u32 s50, s50, s51\n\t"
    "s_bitcmp1_b32 %14, 1\n\t"
    "s_cselect_b32 s50, s50, 0x40000000\n\t"
    "v_and_b32 v59, 48, v147\n\t"
    "v_and_b32 v60, 15, v147\n\t"
    "v_lshl_add_u32 v59, v60, 8, v59\n\t"
    "buffer_load_dwordx4 v[52:55], v59, %15, s50 offen\n\t"
    "s_cmp_lg_u32 %14, 0\n\t"
    "s_cbranch_scc1 L_ahe410_%=\n\t"
    "buffer_load_dwordx4 v[148:151], v152, %12, s47 offen sc1\n\t"
    "L_ahe410_%=:\n\t"
    "s_waitcnt lgkmcnt(11)\n\t"
    "v_and_b32 v123, v123, v115\n\t"
    "v_perm_b32 v123, v123, v123, s46\n\t"
    "ds_or_b32 v97, v123\n\t"
    "v_and_b32 v124, v124, v116\n\t"
    "v_perm_b32 v124, v124, v124, s46\n\t"
    "ds_or_b32 v98, v124\n\t"
    "v_and_b32 v125, v125, v117\n\t"
    "v_perm_b32 v125, v125, v125, s46\n\t"
    "ds_or_b32 v99, v125\n\t"
    "s_waitcnt lgkmcnt(11)\n\t"
    "buffer_store_dwordx4 v[72:75], v66, %12, s49 offen offset:64 sc1\n\t"
    "v_mov_b32 v129, 0xffff0000\n\t"
    "s_waitcnt lgkmcnt(7)\n\t"
    "v_perm_b32 v100, v100, v76, v126\n\t"
    "v_perm_b32 v101, v101, v77, v126\n\t"
    "v_perm_b32 v102, v102, v78, v126\n\t"
    "v_perm_b32 v103, v103, v79, v126\n\t"
    "s_waitcnt lgkmcnt(3)\n\t"
    "v_perm_b32 v104, v104, v80, v126\n\t"
    "v_perm_b32 v105, v105, v81, v126\n\t"
    "v_perm_b32 v106, v106, v82, v126\n\t"
    "v_perm_b32 v107, v107, v83, v126\n\t"
    "v_max_u32 v108, v100, v101\n\t"
    "v_min_u32 v101, v100, v101\n\t"
    "v_max_u32 v100, v102, v103\n\t"
    "v_min_u32 v103, v102, v103\n\t"
    "v_max_u32 v102, v104, v105\n\t"
    "v_min_u32 v105, v104, v105\n\t"
    "v_max_u32 v104, v106, v107\n\t"
    "v_min_u32 v107, v106, v107\n\t"
    "v_max_u32 v106, v108, v100\n\t"
    "v_min_u32 v100, v108, v100\n\t"
    "v_max_u32 v108, v101, v103\n\t"
    "v_min_u32 v103, v101, v103\n\t"
    "v_max_u32 v101, v102, v104\n\t"
    "v_min_u32 v104, v102, v104\n\t"
    "v_max_u32 v102, v105, v107\n\t"
    "v_min_u32 v107, v105, v107\n\t"
    "v_bfi_b32 v130, v106, 0, v129\n\t"
    "v_cmp_eq_u32 vcc, 0, v130\n\t"
    "v_and_or_b32 v92, v106, s44, v68\n\t"
    "s_nop 0\n\t"
    "v_cndmask_b32 v130, v130, v129, vcc\n\t"
    "v_ffbl_b32 v131, v130\n\t"
    "v_and_b32 v134, 7, v131\n\t"
    "v_lshlrev_b32 v134, v134, s45\n\t"
    "v_xor_b32 v129, v129, v134\n\t"
    "v_xor_b32 v132, v131, v127\n\t"
    "v_bfi_b32 v130, v101, 0, v129\n\t"
    "v_cmp_eq_u32 vcc, 0, v130\n\t"
    "v_and_or_b32 v93, v101, s44, v68\n\t"
    "v_xor_b32 v133, 23, v132\n\t"
    "v_lshlrev_b32 v110, v132, v136\n\t"
    "v_and_b32 v132, 7, v132\n\t"
    "v_lshl_or_b32 v110, v136, v133, v110\n\t"
    "v_lshl_add_u32 v132, v132, 1, v128\n\t"
    "ds_or_rtn_b32 v118, v92, v110\n\t"
    "ds_write_b16 v132, v106\n\t"
    "v_cndmask_b32 v130, v130, v129, vcc\n\t"
    "v_ffbl_b32 v131, v130\n\t"
    "v_and_b32 v134, 7, v131\n\t"
    "v_lshlrev_b32 v134, v134, s45\n\t"
    "v_xor_b32 v129, v129, v134\n\t"
    "v_xor_b32 v132, v131, v127\n\t"
    "v_bfi_b32 v130, v108, 0, v129\n\t"
    "v_cmp_eq_u32 vcc, 0, v130\n\t"
    "v_and_or_b32 v94, v108, s44, v68\n\t"
    "v_xor_b32 v133, 23, v132\n\t"
    "v_lshlrev_b32 v111, v132, v136\n\t"
    "v_and_b32 v132, 7, v132\n\t"
    "v_lshl_or_b32 v111, v136, v133, v111\n\t"
    "v_lshl_add_u32 v132, v132, 1, v128\n\t"
    "ds_or_rtn_b32 v119, v93, v111\n\t"
    "ds_write_b16 v132, v101\n\t"
    "v_cndmask_b32 v130, v130, v129, vcc\n\t"
    "v_ffbl_b32 v131, v130\n\t"
    "v_and_b32 v134, 7, v131\n\t"
    "v_lshlrev_b32 v134, v134, s45\n\t"
    "v_xor_b32 v129, v129, v134\n\t"
    "v_xor_b32 v132, v131, v127\n\t"
    "v_bfi_b32 v130, v100, 0, v129\n\t"
    "v_cmp_eq_u32 vcc, 0, v130\n\t"
    "v_and_or_b32 v95, v100, s44, v68\n\t"
    "v_xor_b32 v133, 23, v132\n\t"
    "v_lshlrev_b32 v112, v132, v136\n\t"
    "v_and_b32 v132, 7, v132\n\t"
    "v_lshl_or_b32 v112, v136, v133, v112\n\t"
    "v_lshl_add_u32 v132, v132, 1, v128\n\t"
    "ds_or_rtn_b32 v120, v94, v112\n\t"
    "ds_write_b16 v132, v108\n\t"
    "v_cndmask_b32 v130, v130, v129, vcc\n\t"
    "v_ffbl_b32 v131, v130\n\t"
    "v_and_b32 v134, 7, v131\n\t"
    "v_lshlrev_b32 v134, v134, s45\n\t"
    "v_xor_b32 v129, v129, v134\n\t"
    "v_xor_b32 v132, v131, v127\n\t"
    "v_bfi_b32 v130, v102, 0, v129\n\t"
    "v_cmp_eq_u32 vcc, 0, v130\n\t"
    "v_and_or_b32 v96, v102, s44, v68\n\t"
    "v_xor_b32 v133, 23, v132\n\t"
    "v_lshlrev_b32 v113, v132, v136\n\t"
    "v_and_b32 v132, 7, v132\n\t"
    "v_lshl_or_b32 v113, v136, v133, v113\n\t"
    "v_lshl_add_u32 v132, v132, 1, v128\n\t"
    "ds_or_rtn_b32 v121, v95, v113\n\t"
    "ds_write_b16 v132, v100\n\t"
    "v_cndmask_b32 v130, v130, v129, vcc\n\t"
    "v_ffbl_b32 v131, v130\n\t"
    "v_and_b32 v134, 7, v131\n\t"
    "v_lshlrev_b32 v134, v134, s45\n\t"
    "v_xor_b32 v129, v129, v134\n\t"
    "v_xor_b32 v132, v131, v127\n\t"
    "s_waitcnt lgkmcnt(7)\n\t"
    "v_and_b32 v118, v118, v110\n\t"
    "v_perm_b32 v118, v118, v118, s46\n\t"
    "ds_or_b32 v92, v118\n\t"
    "v_bfi_b32 v130, v104, 0, v129\n\t"
    "v_cmp_eq_u32 vcc, 0, v130\n\t"
    "v_and_or_b32 v97, v104, s44, v68\n\t"
    "v_xor_b32 v133, 23, v132\n\t"
    "v_lshlrev_b32 v114, v132, v136\n\t"
    "v_and_b32 v132, 7, v132\n\t"
    "v_lshl_or_b32 v114, v136, v133, v114\n\t"
    "v_lshl_add_u32 v132, v132, 1, v128\n\t"
    "ds_or_rtn_b32 v122, v96, v114\n\t"
    "ds_write_b16 v132, v102\n\t"
    "v_cndmask_b32 v130, v130, v129, vcc\n\t"
    "v_ffbl_b32 v131, v130\n\t"
    "v_and_b32 v134, 7, v131\n\t"
    "v_lshlrev_b32 v134, v134, s45\n\t"
    "v_xor_b32 v129, v129, v134\n\t"
    "v_xor_b32 v132, v131, v127\n\t"
    "s_waitcnt lgkmcnt(8)\n\t"
    "v_and_b32 v119, v119, v111\n\t"
    "v_perm_b32 v119, v119, v119, s46\n\t"
    "ds_or_b32 v93, v119\n\t"
    "v_bfi_b32 v130, v103, 0, v129\n\t"
    "v_cmp_eq_u32 vcc, 0, v130\n\t"
    "v_and_or_b32 v98, v103, s44, v68\n\t"
    "v_xor_b32 v133, 23, v132\n\t"
    "v_lshlrev_b32 v115, v132, v136\n\t"
    "v_and_b32 v132, 7, v132\n\t"
    "v_lshl_or_b32 v115, v136, v133, v115\n\t"
    "v_lshl_add_u32 v132, v132, 1, v128\n\t"
    "ds_or_rtn_b32 v123, v97, v115\n\t"
    "ds_write_b16 v132, v104\n\t"
    "v_cndmask_b32 v130, v130, v129, vcc\n\t"
    "v_ffbl_b32 v131, v130\n\t"
    "v_and_b32 v134, 7, v131\n\t"
    "v_lshlrev_b32 v134, v134, s45\n\t"
    "v_xor_b32 v129, v129, v134\n\t"
    "v_xor_b32 v132, v131, v127\n\t"
    "s_waitcnt lgkmcnt(9)\n\t"
    "v_and_b32 v120, v120, v112\n\t"
    "v_perm_b32 v120, v120, v120, s46\n\t"
    "ds_or_b32 v94, v120\n\t"
    "v_bfi_b32 v130, v107, 0, v129\n\t"
    "v_cmp_eq_u32 vcc, 0, v130\n\t"
    "v_and_or_b32 v99, v107, s44, v68\n\t"
    "v_xor_b32 v133, 23, v132\n\t"
    "v_lshlrev_b32 v116, v132, v136\n\t"
    "v_and_b32 v132, 7, v132\n\t"
    "v_lshl_or_b32 v116, v136, v133, v116\n\t"
    "v_lshl_add_u32 v132, v132, 1, v128\n\t"
    "ds_or_rtn_b32 v124, v98, v116\n\t"
    "ds_write_b16 v132, v103\n\t"
    "v_cndmask_b32 v130, v130, v129, vcc\n\t"
    "v_ffbl_b32 v131, v130\n\t"
    "v_xor_b32 v132, v131, v127\n\t"
    "s_waitcnt lgkmcnt(10)\n\t"
    "v_and_b32 v121, v121, v113\n\t"
    "v_perm_b32 v121, v121, v121, s46\n\t"
    "ds_or_b32 v95, v121\n\t"
    "v_xor_b32 v133, 23, v132\n\t"
    "v_lshlrev_b32 v117, v132, v136\n\t"
    "v_and_b32 v132, 7, v132\n\t"
    "v_lshl_or_b32 v117, v136, v133, v117\n\t"
    "v_lshl_add_u32 v132, v132, 1, v128\n\t"
    "ds_or_rtn_b32 v125, v99, v117\n\t"
    "ds_write_b16 v132, v107\n\t"
    "s_waitcnt lgkmcnt(10)\n\t"
    "v_and_b32 v122, v122, v114\n\t"
    "v_perm_b32 v122, v122, v122, s46\n\t"
    "ds_or_b32 v96, v122\n\t"
    "ds_read_b128 v[72:75], v128\n\t"
    "v_and_or_b32 v100, v84, s44, v68\n\t"
    "ds_read_b32 v100, v100\n\t"
    "v_and_or_b32 v101, v85, s44, v68\n\t"
    "ds_read_b32 v101, v101\n\t"
    "v_and_or_b32 v102, v86, s44, v68\n\t"
    "ds_read_b32 v102, v102\n\t"
    "v_and_or_b32 v103, v87, s44, v68\n\t"
    "ds_read_b32 v103, v103\n\t"
    "v_and_or_b32 v104, v88, s44, v68\n\t"
    "ds_read_b32 v104, v104\n\t"
    "v_and_or_b32 v105, v89, s44, v68\n\t"
    "ds_read_b32 v105, v105\n\t"
    "v_and_or_b32 v106, v90, s44, v68\n\t"
    "ds_read_b32 v106, v106\n\t"
    "v_and_or_b32 v107, v91, s44, v68\n\t"
    "ds_read_b32 v107, v107\n\t"
    "s_cmp_lg_u32 %14, 0\n\t"
    "s_cbranch_scc1 L_ahe616_%=\n\t"
    "s_waitcnt vmcnt(0)\n\t"
    "buffer_store_dwordx4 v[148:151], v152, %12, s47 offen offset:1024 sc1\n\t"
    "L_ahe616_%=:\n\t"
    "s_waitcnt vmcnt(3)\n\t"
    "v_lshl_add_u32 v76, v32, 3, s36\n\t"
    "v_lshl_add_u32 v77, v33, 3, s37\n\t"
    "v_lshl_add_u32 v78, v34, 3, s38\n\t"
    "v_lshl_add_u32 v79, v35, 3, s39\n\t"
    "v_lshl_add_u32 v80, v36, 3, s40\n\t"
    "v_lshl_add_u32 v81, v37, 3, s41\n\t"
    "v_lshl_add_u32 v82, v38, 3, s42\n\t"
    "v_lshl_add_u32 v83, v39, 3, s43\n\t"
    "s_waitcnt vmcnt(1)\n\t"
    "v_lshl_add_u32 v48, v48, 3, s36\n\t"
    "v_lshl_add_u32 v49, v49, 3, s37\n\t"
    "v_lshl_add_u32 v50, v50, 3, s38\n\t"
    "v_lshl_add_u32 v51, v51, 3, s39\n\t"
    "v_lshl_or_b32 v56, v49, 16, v48\n\t"
    "v_lshl_or_b32 v57, v51, 16, v50\n\t"
    "s_and_b32 s51, %14, 1\n\t"
    "s_mul_i32 s51, s51, 1016\n\t"
    "s_sub_u32 s50, s49, s51\n\t"
    "s_add_u32 s50, s50, 196608\n\t"
    "buffer_store_dwordx2 v[56:57], v152, %12, s50 offen sc1\n\t"
    "s_lshl_b32 s50, %14, 10\n\t"
    "s_add_u32 s50, s50, 16515072\n\t"
    "s_bitcmp1_b32 %14, 1\n\t"
    "s_cselect_b32 s50, s50, 0x40000000\n\t"
    "buffer_store_dwordx4 v[52:55], v152, %12, s50 offen sc1\n\t"
    "s_mul_i32 s50, %14, 69632\n\t"
    "s_add_u32 s50, s50, 31522816\n\t"
    "v_mov_b32 v60, 0\n\t"
    "v_lshlrev_b32 v61, 3, v152\n\t"
    "s_mov_b64 exec, 0xffffffff\n\t"
    "buffer_store_dword v60, v61, %12, s50 offen sc1\n\t"
    "s_lshl_b32 s50, %14, 7\n\t"
    "s_add_u32 s50, s50, 49295360\n\t"
    "v_lshrrev_b32 v62, 2, v152\n\t"
    "buffer_store_dword v60, v62, %12, s50 offen sc1\n\t"
    "s_mov_b64 exec, -1\n\t"
    "s_cmp_lg_u32 %14, 0\n\t"
    "s_cbranch_scc1 L_ahe658_%=\n\t"
    "buffer_load_dwordx4 v[148:151], v152, %12, s47 offen sc1\n\t"
    "L_ahe658_%=:\n\t"
    "s_waitcnt lgkmcnt(11)\n\t"
    "v_and_b32 v123, v123, v115\n\t"
    "v_perm_b32 v123, v123, v123, s46\n\t"
    "ds_or_b32 v97, v123\n\t"
    "v_and_b32 v124, v124, v116\n\t"
    "v_perm_b32 v124, v124, v124, s46\n\t"
    "ds_or_b32 v98, v124\n\t"
    "v_and_b32 v125, v125, v117\n\t"
    "v_perm_b32 v125, v125, v125, s46\n\t"
    "ds_or_b32 v99, v125\n\t"
    "s_waitcnt lgkmcnt(11)\n\t"
    "buffer_store_dwordx4 v[72:75], v66, %12, s49 offen offset:128 sc1\n\t"
    "v_mov_b32 v129, 0xffff0000\n\t"
    "s_waitcnt lgkmcnt(7)\n\t"
    "v_perm_b32 v100, v100, v84, v126\n\t"
    "v_perm_b32 v101, v101, v85, v126\n\t"
    "v_perm_b32 v102, v102, v86, v126\n\t"
    "v_perm_b32 v103, v103, v87, v126\n\t"
    "s_waitcnt lgkmcnt(3)\n\t"
    "v_perm_b32 v104, v104, v88, v126\n\t"
    "v_perm_b32 v105, v105, v89, v126\n\t"
    "v_perm_b32 v106, v106, v90, v126\n\t"
    "v_perm_b32 v107, v107, v91, v126\n\t"
    "v_max_u32 v108, v100, v101\n\t"
    "v_min_u32 v101, v100, v101\n\t"
    "v_max_u32 v100, v102, v103\n\t"
    "v_min_u32 v103, v102, v103\n\t"
    "v_max_u32 v102, v104, v105\n\t"
    "v_min_u32 v105, v104, v105\n\t"
    "v_max_u32 v104, v106, v107\n\t"
    "v_min_u32 v107, v106, v107\n\t"
    "v_max_u32 v106, v108, v100\n\t"
    "v_min_u32 v100, v108, v100\n\t"
    "v_max_u32 v108, v101, v103\n\t"
    "v_min_u32 v103, v101, v103\n\t"
    "v_max_u32 v101, v102, v104\n\t"
    "v_min_u32 v104, v102, v104\n\t"
    "v_max_u32 v102, v105, v107\n\t"
    "v_min_u32 v107, v105, v107\n\t"
    "v_bfi_b32 v130, v106, 0, v129\n\t"
    "v_cmp_eq_u32 vcc, 0, v130\n\t"
    "v_and_or_b32 v92, v106, s44, v68\n\t"
    "s_nop 0\n\t"
    "v_cndmask_b32 v130, v130, v129, vcc\n\t"
    "v_ffbl_b32 v131, v130\n\t"
    "v_and_b32 v134, 7, v131\n\t"
    "v_lshlrev_b32 v134, v134, s45\n\t"
    "v_xor_b32 v129, v129, v134\n\t"
    "v_xor_b32 v132, v131, v127\n\t"
    "v_bfi_b32 v130, v101, 0, v129\n\t"
    "v_cmp_eq_u32 vcc, 0, v130\n\t"
    "v_and_or_b32 v93, v101, s44, v68\n\t"
    "v_xor_b32 v133, 23, v132\n\t"
    "v_lshlrev_b32 v110, v132, v136\n\t"
    "v_and_b32 v132, 7, v132\n\t"
    "v_lshl_or_b32 v110, v136, v133, v110\n\t"
    "v_lshl_add_u32 v132, v132, 1, v128\n\t"
    "ds_or_rtn_b32 v118, v92, v110\n\t"
    "ds_write_b16 v132, v106\n\t"
    "v_cndmask_b32 v130, v130, v129, vcc\n\t"
    "v_ffbl_b32 v131, v130\n\t"
    "v_and_b32 v134, 7, v131\n\t"
    "v_lshlrev_b32 v134, v134, s45\n\t"
    "v_xor_b32 v129, v129, v134\n\t"
    "v_xor_b32 v132, v131, v127\n\t"
    "v_bfi_b32 v130, v108, 0, v129\n\t"
    "v_cmp_eq_u32 vcc, 0, v130\n\t"
    "v_and_or_b32 v94, v108, s44, v68\n\t"
    "v_xor_b32 v133, 23, v132\n\t"
    "v_lshlrev_b32 v111, v132, v136\n\t"
    "v_and_b32 v132, 7, v132\n\t"
    "v_lshl_or_b32 v111, v136, v133, v111\n\t"
    "v_lshl_add_u32 v132, v132, 1, v128\n\t"
    "ds_or_rtn_b32 v119, v93, v111\n\t"
    "ds_write_b16 v132, v101\n\t"
    "v_cndmask_b32 v130, v130, v129, vcc\n\t"
    "v_ffbl_b32 v131, v130\n\t"
    "v_and_b32 v134, 7, v131\n\t"
    "v_lshlrev_b32 v134, v134, s45\n\t"
    "v_xor_b32 v129, v129, v134\n\t"
    "v_xor_b32 v132, v131, v127\n\t"
    "v_bfi_b32 v130, v100, 0, v129\n\t"
    "v_cmp_eq_u32 vcc, 0, v130\n\t"
    "v_and_or_b32 v95, v100, s44, v68\n\t"
    "v_xor_b32 v133, 23, v132\n\t"
    "v_lshlrev_b32 v112, v132, v136\n\t"
    "v_and_b32 v132, 7, v132\n\t"
    "v_lshl_or_b32 v112, v136, v133, v112\n\t"
    "v_lshl_add_u32 v132, v132, 1, v128\n\t"
    "ds_or_rtn_b32 v120, v94, v112\n\t"
    "ds_write_b16 v132, v108\n\t"
    "v_cndmask_b32 v130, v130, v129, vcc\n\t"
    "v_ffbl_b32 v131, v130\n\t"
    "v_and_b32 v134, 7, v131\n\t"
    "v_lshlrev_b32 v134, v134, s45\n\t"
    "v_xor_b32 v129, v129, v134\n\t"
    "v_xor_b32 v132, v131, v127\n\t"
    "v_bfi_b32 v130, v102, 0, v129\n\t"
    "v_cmp_eq_u32 vcc, 0, v130\n\t"
    "v_and_or_b32 v96, v102, s44, v68\n\t"
    "v_xor_b32 v133, 23, v132\n\t"
    "v_lshlrev_b32 v113, v132, v136\n\t"
    "v_and_b32 v132, 7, v132\n\t"
    "v_lshl_or_b32 v113, v136, v133, v113\n\t"
    "v_lshl_add_u32 v132, v132, 1, v128\n\t"
    "ds_or_rtn_b32 v121, v95, v113\n\t"
    "ds_write_b16 v132, v100\n\t"
    "v_cndmask_b32 v130, v130, v129, vcc\n\t"
    "v_ffbl_b32 v131, v130\n\t"
    "v_and_b32 v134, 7, v131\n\t"
    "v_lshlrev_b32 v134, v134, s45\n\t"
    "v_xor_b32 v129, v129, v134\n\t"
    "v_xor_b32 v132, v131, v127\n\t"
    "s_waitcnt lgkmcnt(7)\n\t"
    "v_and_b32 v118, v118, v110\n\t"
    "v_perm_b32 v118, v118, v118, s46\n\t"
    "ds_or_b32 v92, v118\n\t"
    "v_bfi_b32 v130, v104, 0, v129\n\t"
    "v_cmp_eq_u32 vcc, 0, v130\n\t"
    "v_and_or_b32 v97, v104, s44, v68\n\t"
    "v_xor_b32 v133, 23, v132\n\t"
    "v_lshlrev_b32 v114, v132, v136\n\t"
    "v_and_b32 v132, 7, v132\n\t"
    "v_lshl_or_b32 v114, v136, v133, v114\n\t"
    "v_lshl_add_u32 v132, v132, 1, v128\n\t"
    "ds_or_rtn_b32 v122, v96, v114\n\t"
    "ds_write_b16 v132, v102\n\t"
    "v_cndmask_b32 v130, v130, v129, vcc\n\t"
    "v_ffbl_b32 v131, v130\n\t"
    "v_and_b32 v134, 7, v131\n\t"
    "v_lshlrev_b32 v134, v134, s45\n\t"
    "v_xor_b32 v129, v129, v134\n\t"
    "v_xor_b32 v132, v131, v127\n\t"
    "s_waitcnt lgkmcnt(8)\n\t"
    "v_and_b32 v119, v119, v111\n\t"
    "v_perm_b32 v119, v119, v119, s46\n\t"
    "ds_or_b32 v93, v119\n\t"
    "v_bfi_b32 v130, v103, 0, v129\n\t"
    "v_cmp_eq_u32 vcc, 0, v130\n\t"
    "v_and_or_b32 v98, v103, s44, v68\n\t"
    "v_xor_b32 v133, 23, v132\n\t"
    "v_lshlrev_b32 v115, v132, v136\n\t"
    "v_and_b32 v132, 7, v132\n\t"
    "v_lshl_or_b32 v115, v136, v133, v115\n\t"
    "v_lshl_add_u32 v132, v132, 1, v128\n\t"
    "ds_or_rtn_b32 v123, v97, v115\n\t"
    "ds_write_b16 v132, v104\n\t"
    "v_cndmask_b32 v130, v130, v129, vcc\n\t"
    "v_ffbl_b32 v131, v130\n\t"
    "v_and_b32 v134, 7, v131\n\t"
    "v_lshlrev_b32 v134, v134, s45\n\t"
    "v_xor_b32 v129, v129, v134\n\t"
    "v_xor_b32 v132, v131, v127\n\t"
    "s_waitcnt lgkmcnt(9)\n\t"
    "v_and_b32 v120, v120, v112\n\t"
    "v_perm_b32 v120, v120, v120, s46\n\t"
    "ds_or_b32 v94, v120\n\t"
    "v_bfi_b32 v130, v107, 0, v129\n\t"
    "v_cmp_eq_u32 vcc, 0, v130\n\t"
    "v_and_or_b32 v99, v107, s44, v68\n\t"
    "v_xor_b32 v133, 23, v132\n\t"
    "v_lshlrev_b32 v116, v132, v136\n\t"
    "v_and_b32 v132, 7, v132\n\t"
    "v_lshl_or_b32 v116, v136, v133, v116\n\t"
    "v_lshl_add_u32 v132, v132, 1, v128\n\t"
    "ds_or_rtn_b32 v124, v98, v116\n\t"
    "ds_write_b16 v132, v103\n\t"
    "v_cndmask_b32 v130, v130, v129, vcc\n\t"
    "v_ffbl_b32 v131, v130\n\t"
    "v_xor_b32 v132, v131, v127\n\t"
    "s_waitcnt lgkmcnt(10)\n\t"
    "v_and_b32 v121, v121, v113\n\t"
    "v_perm_b32 v121, v121, v121, s46\n\t"
    "ds_or_b32 v95, v121\n\t"
    "v_xor_b32 v133, 23, v132\n\t"
    "v_lshlrev_b32 v117, v132, v136\n\t"
    "v_and_b32 v132, 7, v132\n\t"
    "v_lshl_or_b32 v117, v136, v133, v117\n\t"
    "v_lshl_add_u32 v132, v132, 1, v128\n\t"
    "ds_or_rtn_b32 v125, v99, v117\n\t"
    "ds_write_b16 v132, v107\n\t"
    "s_waitcnt lgkmcnt(10)\n\t"
    "v_and_b32 v122, v122, v114\n\t"
    "v_perm_b32 v122, v122, v122, s46\n\t"
    "ds_or_b32 v96, v122\n\t"
    "ds_read_b128 v[72:75], v128\n\t"
    "v_and_or_b32 v100, v76, s44, v68\n\t"
    "ds_read_b32 v100, v100\n\t"
    "v_and_or_b32 v101, v77, s44, v68\n\t"
    "ds_read_b32 v101, v101\n\t"
    "v_and_or_b32 v102, v78, s44, v68\n\t"
    "ds_read_b32 v102, v102\n\t"
    "v_and_or_b32 v103, v79, s44, v68\n\t"
    "ds_read_b32 v103, v103\n\t"
    "v_and_or_b32 v104, v80, s44, v68\n\t"
    "ds_read_b32 v104, v104\n\t"
    "v_and_or_b32 v105, v81, s44, v68\n\t"
    "ds_read_b32 v105, v105\n\t"
    "v_and_or_b32 v106, v82, s44, v68\n\t"
    "ds_read_b32 v106, v106\n\t"
    "v_and_or_b32 v107, v83, s44, v68\n\t"
    "ds_read_b32 v107, v107\n\t"
    "s_cmp_lg_u32 %14, 0\n\t"
    "s_cbranch_scc1 L_ahe864_%=\n\t"
    "s_waitcnt vmcnt(0)\n\t"
    "buffer_store_dwordx4 v[148:151], v152, %12, s47 offen offset:1024 sc1\n\t"
    "L_ahe864_%=:\n\t"
    "s_cmp_lg_u32 %14, 0\n\t"
    "s_cbranch_scc1 L_ahe869_%=\n\t"
    "buffer_load_dwordx4 v[148:151], v152, %12, s47 offen sc1\n\t"
    "L_ahe869_%=:\n\t"
    "s_waitcnt lgkmcnt(11)\n\t"
    "v_and_b32 v123, v123, v115\n\t"
    "v_perm_b32 v123, v123, v123, s46\n\t"
    "ds_or_b32 v97, v123\n\t"
    "v_and_b32 v124, v124, v116\n\t"
    "v_perm_b32 v124, v124, v124, s46\n\t"
    "ds_or_b32 v98, v124\n\t"
    "v_and_b32 v125, v125, v117\n\t"
    "v_perm_b32 v125, v125, v125, s46\n\t"
    "ds_or_b32 v99, v125\n\t"
    "s_waitcnt lgkmcnt(11)\n\t"
    "buffer_store_dwordx4 v[72:75], v66, %12, s49 offen offset:192 sc1\n\t"
    "v_mov_b32 v129, 0xffff0000\n\t"
    "s_waitcnt lgkmcnt(7)\n\t"
    "v_perm_b32 v100, v100, v76, v126\n\t"
    "v_perm_b32 v101, v101, v77, v126\n\t"
    "v_perm_b32 v102, v102, v78, v126\n\t"
    "v_perm_b32 v103, v103, v79, v126\n\t"
    "s_waitcnt lgkmcnt(3)\n\t"
    "v_perm_b32 v104, v104, v80, v126\n\t"
    "v_perm_b32 v105, v105, v81, v126\n\t"
    "v_perm_b32 v106, v106, v82, v126\n\t"
    "v_perm_b32 v107, v107, v83, v126\n\t"
    "v_max_u32 v108, v100, v101\n\t"
    "v_min_u32 v101, v100, v101\n\t"
    "v_max_u32 v100, v102, v103\n\t"
    "v_min_u32 v103, v102, v103\n\t"
    "v_max_u32 v102, v104, v105\n\t"
    "v_min_u32 v105, v104, v105\n\t"
    "v_max_u32 v104, v106, v107\n\t"
    "v_min_u32 v107, v106, v107\n\t"
    "v_max_u32 v106, v108, v100\n\t"
    "v_min_u32 v100, v108, v100\n\t"
    "v_max_u32 v108, v101, v103\n\t"
    "v_min_u32 v103, v101, v103\n\t"
    "v_max_u32 v101, v102, v104\n\t"
    "v_min_u32 v104, v102, v104\n\t"
    "v_max_u32 v102, v105, v107\n\t"
    "v_min_u32 v107, v105, v107\n\t"
    "v_bfi_b32 v130, v106, 0, v129\n\t"
    "v_cmp_eq_u32 vcc, 0, v130\n\t"
    "v_and_or_b32 v92, v106, s44, v68\n\t"
    "s_nop 0\n\t"
    "v_cndmask_b32 v130, v130, v129, vcc\n\t"
    "v_ffbl_b32 v131, v130\n\t"
    "v_and_b32 v134, 7, v131\n\t"
    "v_lshlrev_b32 v134, v134, s45\n\t"
    "v_xor_b32 v129, v129, v134\n\t"
    "v_xor_b32 v132, v131, v127\n\t"
    "v_bfi_b32 v130, v101, 0, v129\n\t"
    "v_cmp_eq_u32 vcc, 0, v130\n\t"
    "v_and_or_b32 v93, v101, s44, v68\n\t"
    "v_xor_b32 v133, 23, v132\n\t"
    "v_lshlrev_b32 v110, v132, v136\n\t"
    "v_and_b32 v132, 7, v132\n\t"
    "v_lshl_or_b32 v110, v136, v133, v110\n\t"
    "v_lshl_add_u32 v132, v132, 1, v128\n\t"
    "ds_or_rtn_b32 v118, v92, v110\n\t"
    "ds_write_b16 v132, v106\n\t"
    "v_cndmask_b32 v130, v130, v129, vcc\n\t"
    "v_ffbl_b32 v131, v130\n\t"
    "v_and_b32 v134, 7, v131\n\t"
    "v_lshlrev_b32 v134, v134, s45\n\t"
    "v_xor_b32 v129, v129, v134\n\t"
    "v_xor_b32 v132, v131, v127\n\t"
    "v_bfi_b32 v130, v108, 0, v129\n\t"
    "v_cmp_eq_u32 vcc, 0, v130\n\t"
    "v_and_or_b32 v94, v108, s44, v68\n\t"
    "v_xor_b32 v133, 23, v132\n\t"
    "v_lshlrev_b32 v111, v132, v136\n\t"
    "v_and_b32 v132, 7, v132\n\t"
    "v_lshl_or_b32 v111, v136, v133, v111\n\t"
    "v_lshl_add_u32 v132, v132, 1, v128\n\t"
    "ds_or_rtn_b32 v119, v93, v111\n\t"
    "ds_write_b16 v132, v101\n\t"
    "v_cndmask_b32 v130, v130, v129, vcc\n\t"
    "v_ffbl_b32 v131, v130\n\t"
    "v_and_b32 v134, 7, v131\n\t"
    "v_lshlrev_b32 v134, v134, s45\n\t"
    "v_xor_b32 v129, v129, v134\n\t"
    "v_xor_b32 v132, v131, v127\n\t"
    "v_bfi_b32 v130, v100, 0, v129\n\t"
    "v_cmp_eq_u32 vcc, 0, v130\n\t"
    "v_and_or_b32 v95, v100, s44, v68\n\t"
    "v_xor_b32 v133, 23, v132\n\t"
    "v_lshlrev_b32 v112, v132, v136\n\t"
    "v_and_b32 v132, 7, v132\n\t"
    "v_lshl_or_b32 v112, v136, v133, v112\n\t"
    "v_lshl_add_u32 v132, v132, 1, v128\n\t"
    "ds_or_rtn_b32 v120, v94, v112\n\t"
    "ds_write_b16 v132, v108\n\t"
    "v_cndmask_b32 v130, v130, v129, vcc\n\t"
    "v_ffbl_b32 v131, v130\n\t"
    "v_and_b32 v134, 7, v131\n\t"
    "v_lshlrev_b32 v134, v134, s45\n\t"
    "v_xor_b32 v129, v129, v134\n\t"
    "v_xor_b32 v132, v131, v127\n\t"
    "v_bfi_b32 v130, v102, 0, v129\n\t"
    "v_cmp_eq_u32 vcc, 0, v130\n\t"
    "v_and_or_b32 v96, v102, s44, v68\n\t"
    "v_xor_b32 v133, 23, v132\n\t"
    "v_lshlrev_b32 v113, v132, v136\n\t"
    "v_and_b32 v132, 7, v132\n\t"
    "v_lshl_or_b32 v113, v136, v133, v113\n\t"
    "v_lshl_add_u32 v132, v132, 1, v128\n\t"
    "ds_or_rtn_b32 v121, v95, v113\n\t"
    "ds_write_b16 v132, v100\n\t"
    "v_cndmask_b32 v130, v130, v129, vcc\n\t"
    "v_ffbl_b32 v131, v130\n\t"
    "v_and_b32 v134, 7, v131\n\t"
    "v_lshlrev_b32 v134, v134, s45\n\t"
    "v_xor_b32 v129, v129, v134\n\t"
    "v_xor_b32 v132, v131, v127\n\t"
    "s_waitcnt lgkmcnt(7)\n\t"
    "v_and_b32 v118, v118, v110\n\t"
    "v_perm_b32 v118, v118, v118, s46\n\t"
    "ds_or_b32 v92, v118\n\t"
    "s_waitcnt vmcnt(2)\n\t"
    "ds_write_b32 v71, v136 offset:4\n\t"
    "v_bfi_b32 v130, v104, 0, v129\n\t"
    "v_cmp_eq_u32 vcc, 0, v130\n\t"
    "v_and_or_b32 v97, v104, s44, v68\n\t"
    "v_xor_b32 v133, 23, v132\n\t"
    "v_lshlrev_b32 v114, v132, v136\n\t"
    "v_and_b32 v132, 7, v132\n\t"
    "v_lshl_or_b32 v114, v136, v133, v114\n\t"
    "v_lshl_add_u32 v132, v132, 1, v128\n\t"
    "ds_or_rtn_b32 v122, v96, v114\n\t"
    "ds_write_b16 v132, v102\n\t"
    "v_cndmask_b32 v130, v130, v129, vcc\n\t"
    "v_ffbl_b32 v131, v130\n\t"
    "v_and_b32 v134, 7, v131\n\t"
    "v_lshlrev_b32 v134, v134, s45\n\t"
    "v_xor_b32 v129, v129, v134\n\t"
    "v_xor_b32 v132, v131, v127\n\t"
    "s_waitcnt lgkmcnt(9)\n\t"
    "v_and_b32 v119, v119, v111\n\t"
    "v_perm_b32 v119, v119, v119, s46\n\t"
    "ds_or_b32 v93, v119\n\t"
    "v_bfi_b32 v130, v103, 0, v129\n\t"
    "v_cmp_eq_u32 vcc, 0, v130\n\t"
    "v_and_or_b32 v98, v103, s44, v68\n\t"
    "v_xor_b32 v133, 23, v132\n\t"
    "v_lshlrev_b32 v115, v132, v136\n\t"
    "v_and_b32 v132, 7, v132\n\t"
    "v_lshl_or_b32 v115, v136, v133, v115\n\t"
    "v_lshl_add_u32 v132, v132, 1, v128\n\t"
    "ds_or_rtn_b32 v123, v97, v115\n\t"
    "ds_write_b16 v132, v104\n\t"
    "v_cndmask_b32 v130, v130, v129, vcc\n\t"
    "v_ffbl_b32 v131, v130\n\t"
    "v_and_b32 v134, 7, v131\n\t"
    "v_lshlrev_b32 v134, v134, s45\n\t"
    "v_xor_b32 v129, v129, v134\n\t"
    "v_xor_b32 v132, v131, v127\n\t"
    "s_waitcnt lgkmcnt(10)\n\t"
    "v_and_b32 v120, v120, v112\n\t"
    "v_perm_b32 v120, v120, v120, s46\n\t"
    "ds_or_b32 v94, v120\n\t"
    "v_bfi_b32 v130, v107, 0, v129\n\t"
    "v_cmp_eq_u32 vcc, 0, v130\n\t"
    "v_and_or_b32 v99, v107, s44, v68\n\t"
    "v_xor_b32 v133, 23, v132\n\t"
    "v_lshlrev_b32 v116, v132, v136\n\t"
    "v_and_b32 v132, 7, v132\n\t"
    "v_lshl_or_b32 v116, v136, v133, v116\n\t"
    "v_lshl_add_u32 v132, v132, 1, v128\n\t"
    "ds_or_rtn_b32 v124, v98, v116\n\t"
    "ds_write_b16 v132, v103\n\t"
    "v_cndmask_b32 v130, v130, v129, vcc\n\t"
    "v_ffbl_b32 v131, v130\n\t"
    "v_xor_b32 v132, v131, v127\n\t"
    "s_waitcnt lgkmcnt(11)\n\t"
    "v_and_b32 v121, v121, v113\n\t"
    "v_perm_b32 v121, v121, v121, s46\n\t"
    "ds_or_b32 v95, v121\n\t"
    "v_xor_b32 v133, 23, v132\n\t"
    "v_lshlrev_b32 v117, v132, v136\n\t"
    "v_and_b32 v132, 7, v132\n\t"
    "v_lshl_or_b32 v117, v136, v133, v117\n\t"
    "v_lshl_add_u32 v132, v132, 1, v128\n\t"
    "ds_or_rtn_b32 v125, v99, v117\n\t"
    "ds_write_b16 v132, v107\n\t"
    "s_waitcnt lgkmcnt(10)\n\t"
    "v_and_b32 v122, v122, v114\n\t"
    "v_perm_b32 v122, v122, v122, s46\n\t"
    "ds_or_b32 v96, v122\n\t"
    "ds_read_b128 v[72:75], v128\n\t"
    "s_waitcnt lgkmcnt(3)\n\t"
    "v_and_b32 v123, v123, v115\n\t"
    "v_perm_b32 v123, v123, v123, s46\n\t"
    "ds_or_b32 v97, v123\n\t"
    "v_and_b32 v124, v124, v116\n\t"
    "v_perm_b32 v124, v124, v124, s46\n\t"
    "ds_or_b32 v98, v124\n\t"
    "v_and_b32 v125, v125, v117\n\t"
    "v_perm_b32 v125, v125, v125, s46\n\t"
    "ds_or_b32 v99, v125\n\t"
    "s_waitcnt lgkmcnt(3)\n\t"
    "buffer_store_dwordx4 v[72:75], v66, %12, s49 offen offset:256 sc1\n\t"
    "v_mov_b32 v134, 1036\n\t"
    "ds_write_b32 v134, v136\n\t"
    "s_cmp_lg_u32 %14, 0\n\t"
    "s_cbranch_scc1 L_ahsend_%=\n\t"
    "s_waitcnt vmcnt(0)\n\t"
    "buffer_store_dwordx4 v[148:151], v152, %12, s47 offen offset:1024 sc1\n\t"
    "s_setprio 0\n\t"
    "v_and_b32 v132, 0x70, v152\n\t"
    "v_lshlrev_b32 v132, 3, v132\n\t"
    "s_mov_b32 s51, 1048576\n\t"
    "L_ahs2_%=:\n\t"
    "s_sleep 8\n\t"
    "buffer_load_dword v71, v132, %12, s48 offen offset:1024 sc1\n\t"
    "buffer_load_dwordx4 v[148:151], v152, %12, s47 offen sc1\n\t"
    "s_sub_u32 s51, s51, 1\n\t"
    "s_cmp_eq_u32 s51, 0\n\t"
    "s_cbranch_scc1 L_ahsend_%=\n\t"
    "s_waitcnt vmcnt(0)\n\t"
    "buffer_store_dwordx4 v[148:151], v152, %12, s47 offen offset:1024 sc1\n\t"
    "v_cmp_gt_u32 vcc, 32, v71\n\t"
    "s_cbranch_vccnz L_ahs2_%=\n\t"
    "L_ahsend_%=:\n\t"
    "s_setprio 0\n\t"
    "s_and_b32 s46, %16, 3\n\t"
    "s_mul_i32 s49, s46, 1040\n\t"
    "s_add_u32 s49, s49, 2064\n\t"
    "s_lshl_b32 s46, s46, 3\n\t"
    "s_lshr_b32 s47, 1076697610, s46\n\t"
    "s_and_b32 s47, s47, 0xff\n\t"
    "s_lshl_b32 s48, s47, 16\n\t"
    "s_lshr_b32 s47, 756681216, s46\n\t"
    "s_and_b32 s47, s47, 0xff\n\t"
    "s_add_u32 s48, s48, s47\n\t"
    "s_lshr_b32 s47, %16, 2\n\t"
    "s_add_u32 s47, s47, 0x200\n\t"
    "s_cmp_eq_u32 s46, 0\n\t"
    "s_cselect_b32 s47, 0x100, s47\n\t"
    "s_add_u32 s48, s48, s47\n\t"
    "v_bfe_u32 v154, v147, 2, 2\n\t"
    "v_and_b32 v155, 3, v147\n\t"
    "v_mul_u32_u24 v154, 25, v154\n\t"
    "v_mad_u32_u24 v154, v155, 6, v154\n\t"
    "v_lshlrev_b32 v153, 2, v154\n\t"
    "s_and_b32 s53, s48, 0xff\n\t"
    "s_bfe_u32 s55, s48, 0x80008\n\t"
    "s_add_u32 s54, s53, s55\n\t"
    "s_add_u32 s55, s54, s55\n\t"
    "s_lshr_b32 s64, s48, 16\n\t"
    "s_mul_i32 s66, s53, 400\n\t"
    "buffer_load_dwordx3 v[128:130], v153, %0, s66 offen\n\t"
    "buffer_load_dwordx4 v[132:135], v153, %0, s66 offen offset:12\n\t"
    "s_mul_i32 s66, s54, 400\n\t"
    "buffer_load_dwordx3 v[136:138], v153, %0, s66 offen\n\t"
    "buffer_load_dwordx4 v[140:143], v153, %0, s66 offen offset:12\n\t"
    "v_cmp_eq_u32 s[62:63], 0, v155\n\t"
    "v_and_b32 v151, 48, v147\n\t"
    "v_or_b32 v155, 0x100000, v151\n\t"
    "v_cndmask_b32 v152, v155, v151, s[62:63]\n\t"
    "v_lshlrev_b32 v117, 20, v147\n\t"
    "v_add_u32 v117, 1024, v117\n\t"
    "v_mov_b32 v118, 1\n\t"
    "v_lshlrev_b32 v95, 4, v147\n\t"
    "v_add_u32 v95, 140288, v95\n\t"
    "s_call_b64 s[98:99], L_setup_%=\n\t"
    "L_await_%=:\n\t"
    "L_asp9_%=:\n\t"
    "ds_read_b32 v118, v117 offset:8\n\t"
    "ds_read_b32 v119, v117 offset:4\n\t"
    "s_waitcnt lgkmcnt(0)\n\t"
    "v_readfirstlane_b32 s46, v119\n\t"
    "s_nop 0\n\t"
    "s_cmp_ge_u32 s46, 4\n\t"
    "s_cbranch_scc1 L_ago9_%=\n\t"
    "s_sleep 1\n\t"
    "s_branch L_asp9_%=\n\t"
    "L_ago9_%=:\n\t"
    "v_readfirstlane_b32 s47, v118\n\t"
    "ds_read_b128 v[0:3], v95 offset:0\n\t"
    "ds_read_b128 v[4:7], v95 offset:1024\n\t"
    "ds_read_b128 v[8:11], v95 offset:2048\n\t"
    "ds_read_b128 v[12:15], v95 offset:3072\n\t"
    "ds_read_b128 v[16:19], v95 offset:4096\n\t"
    "ds_read_b128 v[20:23], v95 offset:5120\n\t"
    "ds_read_b128 v[24:27], v95 offset:6144\n\t"
    "ds_read_b128 v[28:31], v95 offset:7168\n\t"
    "ds_read_b128 v[32:35], v95 offset:8192\n\t"
    "ds_read_b128 v[36:39], v95 offset:9216\n\t"
    "ds_read_b128 v[40:43], v95 offset:10240\n\t"
    "ds_read_b128 v[44:47], v95 offset:11264\n\t"
    "ds_read_b128 v[48:51], v95 offset:12288\n\t"
    "ds_read_b128 v[52:55], v95 offset:13312\n\t"
    "ds_read_b128 v[56:59], v95 offset:14336\n\t"
    "ds_read_b128 v[60:63], v95 offset:15360\n\t"
    "ds_read_b128 v[64:67], v95 offset:16384\n\t"
    "ds_read_b128 v[68:71], v95 offset:17408\n\t"
    "ds_read_b128 v[72:75], v95 offset:18432\n\t"
    "ds_read_b128 v[76:79], v95 offset:19456\n\t"
    "ds_read_b128 v[80:83], v95 offset:20480\n\t"
    "ds_read_b128 v[84:87], v95 offset:21504\n\t"
    "ds_read_b128 v[88:91], v95 offset:22528\n\t"
    "s_waitcnt vmcnt(0)\n\t"
    "s_cmp_lg_u32 %16, 1\n\t"
    "s_cbranch_scc1 L_anh3_%=\n\t"
    "v_mov_b32 v119, 4\n\t"
    "ds_write_b32 v117, v119 offset:12\n\t"
    "s_lshl_b32 s65, %14, 12\n\t"
    "s_add_u32 s65, s65, 16777216\n\t"
    "s_waitcnt lgkmcnt(15)\n\t"
    "buffer_store_dwordx4 v[0:3], v127, %12, s65 offen offset:0 sc1\n\t"
    "buffer_store_dwordx4 v[4:7], v127, %12, s65 offen offset:1024 sc1\n\t"
    "buffer_store_dwordx4 v[8:11], v127, %12, s65 offen offset:2048 sc1\n\t"
    "buffer_store_dwordx4 v[12:15], v127, %12, s65 offen offset:3072 sc1\n\t"
    "L_anh3_%=:\n\t"
    "s_cmp_ge_u32 s47, 6\n\t"
    "s_cbranch_scc1 L_adok_%=\n\t"
    "L_awd_%=:\n\t"
    "L_asp10_%=:\n\t"
    "ds_read_b32 v119, v117 offset:8\n\t"
    "s_waitcnt lgkmcnt(0)\n\t"
    "v_readfirstlane_b32 s46, v119\n\t"
    "s_nop 0\n\t"
    "s_cmp_ge_u32 s46, 6\n\t"
    "s_cbranch_scc1 L_ago10_%=\n\t"
    "s_sleep 1\n\t"
    "s_branch L_asp10_%=\n\t"
    "L_ago10_%=:\n\t"
    "L_adok_%=:\n\t"
    "L_amain_%=:\n\t"
    "s_mov_b32 s68, 130\n\t"
    "v_mad_u32_u24 v128, v128, s68, v152\n\t"
    "v_and_b32 v128, -16, v128\n\t"
    "ds_read_b128 v[156:159], v128 offset:0\n\t"
    "ds_read_b128 v[160:163], v128 offset:64\n\t"
    "v_mad_u32_u24 v129, v129, s68, v151\n\t"
    "v_and_b32 v129, -16, v129\n\t"
    "ds_read_b128 v[164:167], v129 offset:0\n\t"
    "ds_read_b128 v[168:171], v129 offset:64\n\t"
    "v_mad_u32_u24 v130, v130, s68, v151\n\t"
    "v_and_b32 v130, -16, v130\n\t"
    "ds_read_b128 v[172:175], v130 offset:0\n\t"
    "ds_read_b128 v[176:179], v130 offset:64\n\t"
    "s_mul_i32 s66, s55, 400\n\t"
    "buffer_load_dwordx3 v[128:130], v153, %0, s66 offen\n\t"
    "v_mad_u32_u24 v132, v132, s68, v151\n\t"
    "v_and_b32 v132, -16, v132\n\t"
    "ds_read_b128 v[180:183], v132 offset:0\n\t"
    "ds_read_b128 v[184:187], v132 offset:64\n\t"
    "v_mad_u32_u24 v133, v133, s68, v151\n\t"
    "v_and_b32 v133, -16, v133\n\t"
    "ds_read_b128 v[188:191], v133 offset:0\n\t"
    "ds_read_b128 v[192:195], v133 offset:64\n\t"
    "v_mad_u32_u24 v134, v134, s68, v151\n\t"
    "v_and_b32 v134, -16, v134\n\t"
    "ds_read_b128 v[196:199], v134 offset:0\n\t"
    "ds_read_b128 v[200:203], v134 offset:64\n\t"
    "v_mad_u32_u24 v135, v135, s68, v151\n\t"
    "v_and_b32 v135, -16, v135\n\t"
    "ds_read_b128 v[204:207], v135 offset:0\n\t"
    "ds_read_b128 v[208:211], v135 offset:64\n\t"
    "v_mov_b32 v150, 1\n\t"
    "v_lshlrev_b32 v149, 20, v147\n\t"
    "v_add_u32 v149, s49, v149\n\t"
    "v_lshlrev_b32 v146, 2, v147\n\t"
    "s_mov_b32 s52, s53\n\t"
    "s_lshl_b32 s50, %14, 14\n\t"
    "s_add_u32 s50, s50, 27262976\n\t"
    "L_apnn_%=:\n\t"
    "s_mul_i32 s69, s52, 256\n\t"
    "s_add_u32 s69, s69, s50\n\t"
    "s_waitcnt lgkmcnt(8)\n\t"
    "v_mfma_f32_16x16x32_bf16 v[92:95], v[156:159], v[156:159], 0\n\t"
    "v_fmac_f32 v145, v108, v72\n\t"
    "v_fmac_f32 v144, v109, v73\n\t"
    "ds_add_rtn_u32 v148, v149, v150\n\t"
    "v_mfma_f32_16x16x32_bf16 v[92:95], v[160:163], v[160:163], v[92:95]\n\t"
    "v_fmac_f32 v145, v110, v74\n\t"
    "v_fmac_f32 v144, v111, v75\n\t"
    "buffer_load_dwordx4 v[132:135], v153, %0, s66 offen offset:12\n\t"
    "v_mfma_f32_16x16x32_bf16 v[96:99], v[164:167], v[164:167], 0\n\t"
    "v_fmac_f32 v145, v112, v76\n\t"
    "v_fmac_f32 v144, v113, v77\n\t"
    "v_mfma_f32_16x16x32_bf16 v[96:99], v[168:171], v[168:171], v[96:99]\n\t"
    "v_fmac_f32 v145, v114, v78\n\t"
    "v_fmac_f32 v144, v115, v79\n\t"
    "v_mfma_f32_16x16x32_bf16 v[100:103], v[164:167], v[172:175], 0\n\t"
    "v_fmac_f32 v145, v116, v80\n\t"
    "v_fmac_f32 v144, v117, v81\n\t"
    "v_mfma_f32_16x16x32_bf16 v[100:103], v[168:171], v[176:179], v[100:103]\n\t"
    "v_fmac_f32 v145, v118, v82\n\t"
    "v_fmac_f32 v144, v119, v83\n\t"
    "s_waitcnt vmcnt(4)\n\t"
    "s_cmp_ge_u32 s55, s64\n\t"
    "s_cbranch_scc1 L_ahk00_%=\n\t"
    "L_ahb00_%=:\n\t"
    "v_mfma_f32_16x16x32_bf16 v[104:107], v[172:175], v[172:175], 0\n\t"
    "v_mad_u32_u24 v136, v136, s68, v152\n\t"
    "v_and_b32 v136, -16, v136\n\t"
    "v_mfma_f32_16x16x32_bf16 v[104:107], v[176:179], v[176:179], v[104:107]\n\t"
    "v_add_f32 v154, v144, v145\n\t"
    "buffer_store_dword v154, v146, %12, s69 offen sc1\n\t"
    "v_mul_f32 v144, v92, v84\n\t"
    "v_mfma_f32_16x16x32_bf16 v[108:111], v[156:159], v[164:167], 0\n\t"
    "v_mul_f32 v145, v96, v0\n\t"
    "v_fmac_f32 v144, v97, v1\n\t"
    "v_mfma_f32_16x16x32_bf16 v[108:111], v[160:163], v[168:171], v[108:111]\n\t"
    "v_fmac_f32 v145, v98, v2\n\t"
    "v_fmac_f32 v144, v99, v3\n\t"
    "s_waitcnt lgkmcnt(7)\n\t"
    "v_mfma_f32_16x16x32_bf16 v[112:115], v[164:167], v[180:183], 0\n\t"
    "v_fmac_f32 v145, v100, v4\n\t"
    "v_fmac_f32 v144, v101, v5\n\t"
    "v_mfma_f32_16x16x32_bf16 v[112:115], v[168:171], v[184:187], v[112:115]\n\t"
    "v_fmac_f32 v145, v102, v6\n\t"
    "v_fmac_f32 v144, v103, v7\n\t"
    "v_mfma_f32_16x16x32_bf16 v[116:119], v[172:175], v[180:183], 0\n\t"
    "v_fmac_f32 v145, v104, v8\n\t"
    "v_fmac_f32 v144, v105, v9\n\t"
    "v_mfma_f32_16x16x32_bf16 v[116:119], v[176:179], v[184:187], v[116:119]\n\t"
    "v_fmac_f32 v145, v106, v10\n\t"
    "v_fmac_f32 v144, v107, v11\n\t"
    "ds_read_b128 v[212:215], v136 offset:0\n\t"
    "ds_read_b128 v[216:219], v136 offset:64\n\t"
    "v_mfma_f32_16x16x32_bf16 v[92:95], v[180:183], v[180:183], 0\n\t"
    "v_mad_u32_u24 v137, v137, s68, v151\n\t"
    "v_and_b32 v137, -16, v137\n\t"
    "v_mfma_f32_16x16x32_bf16 v[92:95], v[184:187], v[184:187], v[92:95]\n\t"
    "v_fmac_f32 v145, v108, v85\n\t"
    "v_mad_u32_u24 v138, v138, s68, v151\n\t"
    "v_mfma_f32_16x16x32_bf16 v[96:99], v[156:159], v[172:175], 0\n\t"
    "v_fmac_f32 v144, v112, v12\n\t"
    "v_fmac_f32 v145, v113, v13\n\t"
    "v_mfma_f32_16x16x32_bf16 v[96:99], v[160:163], v[176:179], v[96:99]\n\t"
    "v_fmac_f32 v144, v114, v14\n\t"
    "v_fmac_f32 v145, v115, v15\n\t"
    "s_waitcnt lgkmcnt(7)\n\t"
    "v_mfma_f32_16x16x32_bf16 v[100:103], v[164:167], v[188:191], 0\n\t"
    "v_and_b32 v138, -16, v138\n\t"
    "v_fmac_f32 v144, v116, v16\n\t"
    "v_mfma_f32_16x16x32_bf16 v[100:103], v[168:171], v[192:195], v[100:103]\n\t"
    "v_fmac_f32 v145, v117, v17\n\t"
    "v_fmac_f32 v144, v118, v18\n\t"
    "v_mfma_f32_16x16x32_bf16 v[104:107], v[172:175], v[188:191], 0\n\t"
    "v_fmac_f32 v145, v119, v19\n\t"
    "v_fmac_f32 v144, v92, v20\n\t"
    "v_mfma_f32_16x16x32_bf16 v[104:107], v[176:179], v[192:195], v[104:107]\n\t"
    "v_fmac_f32 v145, v93, v21\n\t"
    "v_fmac_f32 v144, v94, v22\n\t"
    "s_waitcnt lgkmcnt(5)\n\t"
    "v_mfma_f32_16x16x32_bf16 v[108:111], v[164:167], v[196:199], 0\n\t"
    "v_fmac_f32 v145, v95, v23\n\t"
    "v_fmac_f32 v144, v96, v86\n\t"
    "v_mfma_f32_16x16x32_bf16 v[108:111], v[168:171], v[200:203], v[108:111]\n\t"
    "v_mfma_f32_16x16x32_bf16 v[112:115], v[156:159], v[180:183], 0\n\t"
    "v_fmac_f32 v145, v100, v24\n\t"
    "v_fmac_f32 v144, v101, v25\n\t"
    "v_mfma_f32_16x16x32_bf16 v[112:115], v[160:163], v[184:187], v[112:115]\n\t"
    "v_fmac_f32 v145, v102, v26\n\t"
    "v_fmac_f32 v144, v103, v27\n\t"
    "s_waitcnt lgkmcnt(2)\n\t"
    "v_mfma_f32_16x16x32_bf16 v[116:119], v[164:167], v[204:207], 0\n\t"
    "v_readfirstlane_b32 s60, v148\n\t"
    "v_fmac_f32 v145, v104, v28\n\t"
    "v_mfma_f32_16x16x32_bf16 v[116:119], v[168:171], v[208:211], v[116:119]\n\t"
    "v_fmac_f32 v144, v105, v29\n\t"
    "v_fmac_f32 v145, v106, v30\n\t"
    "ds_read_b128 v[164:167], v137 offset:0\n\t"
    "ds_read_b128 v[168:171], v137 offset:64\n\t"
    "v_mfma_f32_16x16x32_bf16 v[92:95], v[172:175], v[196:199], 0\n\t"
    "v_fmac_f32 v144, v107, v31\n\t"
    "v_fmac_f32 v145, v108, v32\n\t"
    "v_mfma_f32_16x16x32_bf16 v[92:95], v[176:179], v[200:203], v[92:95]\n\t"
    "v_fmac_f32 v144, v109, v33\n\t"
    "v_fmac_f32 v145, v110, v34\n\t"
    "s_waitcnt vmcnt(4)\n\t"
    "s_cmp_ge_u32 s55, s64\n\t"
    "s_cbranch_scc1 L_ahk10_%=\n\t"
    "L_ahb10_%=:\n\t"
    "v_mfma_f32_16x16x32_bf16 v[96:99], v[172:175], v[204:207], 0\n\t"
    "v_mad_u32_u24 v140, v140, s68, v151\n\t"
    "v_and_b32 v140, -16, v140\n\t"
    "v_mfma_f32_16x16x32_bf16 v[96:99], v[176:179], v[208:211], v[96:99]\n\t"
    "v_fmac_f32 v144, v111, v35\n\t"
    "v_fmac_f32 v145, v112, v87\n\t"
    "ds_read_b128 v[172:175], v138 offset:0\n\t"
    "ds_read_b128 v[176:179], v138 offset:64\n\t"
    "s_mul_i32 s66, s60, 400\n\t"
    "v_mfma_f32_16x16x32_bf16 v[100:103], v[156:159], v[188:191], 0\n\t"
    "v_fmac_f32 v144, v116, v36\n\t"
    "v_fmac_f32 v145, v117, v37\n\t"
    "v_mfma_f32_16x16x32_bf16 v[100:103], v[160:163], v[192:195], v[100:103]\n\t"
    "v_fmac_f32 v144, v118, v38\n\t"
    "v_fmac_f32 v145, v119, v39\n\t"
    "buffer_load_dwordx3 v[136:138], v153, %0, s66 offen\n\t"
    "v_mfma_f32_16x16x32_bf16 v[104:107], v[180:183], v[188:191], 0\n\t"
    "v_mad_u32_u24 v141, v141, s68, v151\n\t"
    "v_and_b32 v141, -16, v141\n\t"
    "v_mfma_f32_16x16x32_bf16 v[104:107], v[184:187], v[192:195], v[104:107]\n\t"
    "v_fmac_f32 v144, v92, v40\n\t"
    "v_fmac_f32 v145, v93, v41\n\t"
    "v_mfma_f32_16x16x32_bf16 v[108:111], v[180:183], v[196:199], 0\n\t"
    "v_fmac_f32 v144, v94, v42\n\t"
    "v_fmac_f32 v145, v95, v43\n\t"
    "v_mfma_f32_16x16x32_bf16 v[108:111], v[184:187], v[200:203], v[108:111]\n\t"
    "v_fmac_f32 v144, v96, v44\n\t"
    "v_fmac_f32 v145, v97, v45\n\t"
    "v_mfma_f32_16x16x32_bf16 v[112:115], v[180:183], v[204:207], 0\n\t"
    "v_fmac_f32 v144, v98, v46\n\t"
    "v_fmac_f32 v145, v99, v47\n\t"
    "v_mfma_f32_16x16x32_bf16 v[112:115], v[184:187], v[208:211], v[112:115]\n\t"
    "v_fmac_f32 v144, v100, v88\n\t"
    "v_mad_u32_u24 v142, v142, s68, v151\n\t"
    "ds_read_b128 v[180:183], v140 offset:0\n\t"
    "ds_read_b128 v[184:187], v140 offset:64\n\t"
    "v_mfma_f32_16x16x32_bf16 v[116:119], v[156:159], v[196:199], 0\n\t"
    "v_and_b32 v142, -16, v142\n\t"
    "v_fmac_f32 v145, v104, v48\n\t"
    "v_mfma_f32_16x16x32_bf16 v[116:119], v[160:163], v[200:203], v[116:119]\n\t"
    "v_fmac_f32 v144, v105, v49\n\t"
    "v_fmac_f32 v145, v106, v50\n\t"
    "v_mfma_f32_16x16x32_bf16 v[92:95], v[188:191], v[188:191], 0\n\t"
    "v_fmac_f32 v144, v107, v51\n\t"
    "v_mad_u32_u24 v143, v143, s68, v151\n\t"
    "v_mfma_f32_16x16x32_bf16 v[92:95], v[192:195], v[192:195], v[92:95]\n\t"
    "v_and_b32 v143, -16, v143\n\t"
    "v_fmac_f32 v145, v108, v52\n\t"
    "v_mfma_f32_16x16x32_bf16 v[96:99], v[188:191], v[196:199], 0\n\t"
    "v_fmac_f32 v144, v109, v53\n\t"
    "v_fmac_f32 v145, v110, v54\n\t"
    "v_mfma_f32_16x16x32_bf16 v[96:99], v[192:195], v[200:203], v[96:99]\n\t"
    "v_fmac_f32 v144, v111, v55\n\t"
    "v_fmac_f32 v145, v112, v56\n\t"
    "v_mfma_f32_16x16x32_bf16 v[100:103], v[188:191], v[204:207], 0\n\t"
    "v_fmac_f32 v144, v113, v57\n\t"
    "v_fmac_f32 v145, v114, v58\n\t"
    "v_mfma_f32_16x16x32_bf16 v[100:103], v[192:195], v[208:211], v[100:103]\n\t"
    "v_fmac_f32 v144, v115, v59\n\t"
    "v_fmac_f32 v145, v116, v89\n\t"
    "ds_read_b128 v[188:191], v141 offset:0\n\t"
    "ds_read_b128 v[192:195], v141 offset:64\n\t"
    "v_mfma_f32_16x16x32_bf16 v[104:107], v[156:159], v[204:207], 0\n\t"
    "v_fmac_f32 v144, v92, v60\n\t"
    "v_fmac_f32 v145, v93, v61\n\t"
    "v_mfma_f32_16x16x32_bf16 v[104:107], v[160:163], v[208:211], v[104:107]\n\t"
    "v_fmac_f32 v144, v94, v62\n\t"
    "v_fmac_f32 v145, v95, v63\n\t"
    "v_mfma_f32_16x16x32_bf16 v[108:111], v[196:199], v[196:199], 0\n\t"
    "v_fmac_f32 v144, v96, v64\n\t"
    "v_fmac_f32 v145, v97, v65\n\t"
    "v_mfma_f32_16x16x32_bf16 v[108:111], v[200:203], v[200:203], v[108:111]\n\t"
    "v_fmac_f32 v144, v98, v66\n\t"
    "v_fmac_f32 v145, v99, v67\n\t"
    "v_mfma_f32_16x16x32_bf16 v[112:115], v[196:199], v[204:207], 0\n\t"
    "v_fmac_f32 v144, v100, v68\n\t"
    "v_fmac_f32 v145, v101, v69\n\t"
    "v_mfma_f32_16x16x32_bf16 v[112:115], v[200:203], v[208:211], v[112:115]\n\t"
    "v_fmac_f32 v144, v102, v70\n\t"
    "v_fmac_f32 v145, v103, v71\n\t"
    "ds_read_b128 v[196:199], v142 offset:0\n\t"
    "ds_read_b128 v[200:203], v142 offset:64\n\t"
    "v_mfma_f32_16x16x32_bf16 v[116:119], v[204:207], v[204:207], 0\n\t"
    "v_fmac_f32 v144, v104, v90\n\t"
    "v_mfma_f32_16x16x32_bf16 v[116:119], v[208:211], v[208:211], v[116:119]\n\t"
    "ds_read_b128 v[204:207], v143 offset:0\n\t"
    "ds_read_b128 v[208:211], v143 offset:64\n\t"
    "s_mov_b32 s52, s53\n\t"
    "s_mov_b32 s53, s54\n\t"
    "s_mov_b32 s54, s55\n\t"
    "s_mov_b32 s55, s60\n\t"
    "s_cmp_lt_u32 s53, s64\n\t"
    "s_cbranch_scc0 L_adone_%=\n\t"
    "s_mul_i32 s69, s52, 256\n\t"
    "s_add_u32 s69, s69, s50\n\t"
    "s_waitcnt lgkmcnt(8)\n\t"
    "v_mfma_f32_16x16x32_bf16 v[92:95], v[212:215], v[212:215], 0\n\t"
    "v_fmac_f32 v145, v108, v72\n\t"
    "v_fmac_f32 v144, v109, v73\n\t"
    "ds_add_rtn_u32 v148, v149, v150\n\t"
    "v_mfma_f32_16x16x32_bf16 v[92:95], v[216:219], v[216:219], v[92:95]\n\t"
    "v_fmac_f32 v145, v110, v74\n\t"
    "v_fmac_f32 v144, v111, v75\n\t"
    "buffer_load_dwordx4 v[140:143], v153, %0, s66 offen offset:12\n\t"
    "v_mfma_f32_16x16x32_bf16 v[96:99], v[164:167], v[164:167], 0\n\t"
    "v_fmac_f32 v145, v112, v76\n\t"
    "v_fmac_f32 v144, v113, v77\n\t"
    "v_mfma_f32_16x16x32_bf16 v[96:99], v[168:171], v[168:171], v[96:99]\n\t"
    "v_fmac_f32 v145, v114, v78\n\t"
    "v_fmac_f32 v144, v115, v79\n\t"
    "v_mfma_f32_16x16x32_bf16 v[100:103], v[164:167], v[172:175], 0\n\t"
    "v_fmac_f32 v145, v116, v80\n\t"
    "v_fmac_f32 v144, v117, v81\n\t"
    "v_mfma_f32_16x16x32_bf16 v[100:103], v[168:171], v[176:179], v[100:103]\n\t"
    "v_fmac_f32 v145, v118, v82\n\t"
    "v_fmac_f32 v144, v119, v83\n\t"
    "s_waitcnt vmcnt(4)\n\t"
    "s_cmp_ge_u32 s55, s64\n\t"
    "s_cbranch_scc1 L_ahk01_%=\n\t"
    "L_ahb01_%=:\n\t"
    "v_mfma_f32_16x16x32_bf16 v[104:107], v[172:175], v[172:175], 0\n\t"
    "v_mad_u32_u24 v128, v128, s68, v152\n\t"
    "v_and_b32 v128, -16, v128\n\t"
    "v_mfma_f32_16x16x32_bf16 v[104:107], v[176:179], v[176:179], v[104:107]\n\t"
    "v_add_f32 v154, v144, v145\n\t"
    "buffer_store_dword v154, v146, %12, s69 offen sc1\n\t"
    "v_mul_f32 v144, v92, v84\n\t"
    "v_mfma_f32_16x16x32_bf16 v[108:111], v[212:215], v[164:167], 0\n\t"
    "v_mul_f32 v145, v96, v0\n\t"
    "v_fmac_f32 v144, v97, v1\n\t"
    "v_mfma_f32_16x16x32_bf16 v[108:111], v[216:219], v[168:171], v[108:111]\n\t"
    "v_fmac_f32 v145, v98, v2\n\t"
    "v_fmac_f32 v144, v99, v3\n\t"
    "s_waitcnt lgkmcnt(7)\n\t"
    "v_mfma_f32_16x16x32_bf16 v[112:115], v[164:167], v[180:183], 0\n\t"
    "v_fmac_f32 v145, v100, v4\n\t"
    "v_fmac_f32 v144, v101, v5\n\t"
    "v_mfma_f32_16x16x32_bf16 v[112:115], v[168:171], v[184:187], v[112:115]\n\t"
    "v_fmac_f32 v145, v102, v6\n\t"
    "v_fmac_f32 v144, v103, v7\n\t"
    "v_mfma_f32_16x16x32_bf16 v[116:119], v[172:175], v[180:183], 0\n\t"
    "v_fmac_f32 v145, v104, v8\n\t"
    "v_fmac_f32 v144, v105, v9\n\t"
    "v_mfma_f32_16x16x32_bf16 v[116:119], v[176:179], v[184:187], v[116:119]\n\t"
    "v_fmac_f32 v145, v106, v10\n\t"
    "v_fmac_f32 v144, v107, v11\n\t"
    "ds_read_b128 v[156:159], v128 offset:0\n\t"
    "ds_read_b128 v[160:163], v128 offset:64\n\t"
    "v_mfma_f32_16x16x32_bf16 v[92:95], v[180:183], v[180:183], 0\n\t"
    "v_mad_u32_u24 v129, v129, s68, v151\n\t"
    "v_and_b32 v129, -16, v129\n\t"
    "v_mfma_f32_16x16x32_bf16 v[92:95], v[184:187], v[184:187], v[92:95]\n\t"
    "v_fmac_f32 v145, v108, v85\n\t"
    "v_mad_u32_u24 v130, v130, s68, v151\n\t"
    "v_mfma_f32_16x16x32_bf16 v[96:99], v[212:215], v[172:175], 0\n\t"
    "v_fmac_f32 v144, v112, v12\n\t"
    "v_fmac_f32 v145, v113, v13\n\t"
    "v_mfma_f32_16x16x32_bf16 v[96:99], v[216:219], v[176:179], v[96:99]\n\t"
    "v_fmac_f32 v144, v114, v14\n\t"
    "v_fmac_f32 v145, v115, v15\n\t"
    "s_waitcnt lgkmcnt(7)\n\t"
    "v_mfma_f32_16x16x32_bf16 v[100:103], v[164:167], v[188:191], 0\n\t"
    "v_and_b32 v130, -16, v130\n\t"
    "v_fmac_f32 v144, v116, v16\n\t"
    "v_mfma_f32_16x16x32_bf16 v[100:103], v[168:171], v[192:195], v[100:103]\n\t"
    "v_fmac_f32 v145, v117, v17\n\t"
    "v_fmac_f32 v144, v118, v18\n\t"
    "v_mfma_f32_16x16x32_bf16 v[104:107], v[172:175], v[188:191], 0\n\t"
    "v_fmac_f32 v145, v119, v19\n\t"
    "v_fmac_f32 v144, v92, v20\n\t"
    "v_mfma_f32_16x16x32_bf16 v[104:107], v[176:179], v[192:195], v[104:107]\n\t"
    "v_fmac_f32 v145, v93, v21\n\t"
    "v_fmac_f32 v144, v94, v22\n\t"
    "s_waitcnt lgkmcnt(5)\n\t"
    "v_mfma_f32_16x16x32_bf16 v[108:111], v[164:167], v[196:199], 0\n\t"
    "v_fmac_f32 v145, v95, v23\n\t"
    "v_fmac_f32 v144, v96, v86\n\t"
    "v_mfma_f32_16x16x32_bf16 v[108:111], v[168:171], v[200:203], v[108:111]\n\t"
    "v_mfma_f32_16x16x32_bf16 v[112:115], v[212:215], v[180:183], 0\n\t"
    "v_fmac_f32 v145, v100, v24\n\t"
    "v_fmac_f32 v144, v101, v25\n\t"
    "v_mfma_f32_16x16x32_bf16 v[112:115], v[216:219], v[184:187], v[112:115]\n\t"
    "v_fmac_f32 v145, v102, v26\n\t"
    "v_fmac_f32 v144, v103, v27\n\t"
    "s_waitcnt lgkmcnt(2)\n\t"
    "v_mfma_f32_16x16x32_bf16 v[116:119], v[164:167], v[204:207], 0\n\t"
    "v_readfirstlane_b32 s60, v148\n\t"
    "v_fmac_f32 v145, v104, v28\n\t"
    "v_mfma_f32_16x16x32_bf16 v[116:119], v[168:171], v[208:211], v[116:119]\n\t"
    "v_fmac_f32 v144, v105, v29\n\t"
    "v_fmac_f32 v145, v106, v30\n\t"
    "ds_read_b128 v[164:167], v129 offset:0\n\t"
    "ds_read_b128 v[168:171], v129 offset:64\n\t"
    "v_mfma_f32_16x16x32_bf16 v[92:95], v[172:175], v[196:199], 0\n\t"
    "v_fmac_f32 v144, v107, v31\n\t"
    "v_fmac_f32 v145, v108, v32\n\t"
    "v_mfma_f32_16x16x32_bf16 v[92:95], v[176:179], v[200:203], v[92:95]\n\t"
    "v_fmac_f32 v144, v109, v33\n\t"
    "v_fmac_f32 v145, v110, v34\n\t"
    "s_waitcnt vmcnt(4)\n\t"
    "s_cmp_ge_u32 s55, s64\n\t"
    "s_cbranch_scc1 L_ahk11_%=\n\t"
    "L_ahb11_%=:\n\t"
    "v_mfma_f32_16x16x32_bf16 v[96:99], v[172:175], v[204:207], 0\n\t"
    "v_mad_u32_u24 v132, v132, s68, v151\n\t"
    "v_and_b32 v132, -16, v132\n\t"
    "v_mfma_f32_16x16x32_bf16 v[96:99], v[176:179], v[208:211], v[96:99]\n\t"
    "v_fmac_f32 v144, v111, v35\n\t"
    "v_fmac_f32 v145, v112, v87\n\t"
    "ds_read_b128 v[172:175], v130 offset:0\n\t"
    "ds_read_b128 v[176:179], v130 offset:64\n\t"
    "s_mul_i32 s66, s60, 400\n\t"
    "v_mfma_f32_16x16x32_bf16 v[100:103], v[212:215], v[188:191], 0\n\t"
    "v_fmac_f32 v144, v116, v36\n\t"
    "v_fmac_f32 v145, v117, v37\n\t"
    "v_mfma_f32_16x16x32_bf16 v[100:103], v[216:219], v[192:195], v[100:103]\n\t"
    "v_fmac_f32 v144, v118, v38\n\t"
    "v_fmac_f32 v145, v119, v39\n\t"
    "buffer_load_dwordx3 v[128:130], v153, %0, s66 offen\n\t"
    "v_mfma_f32_16x16x32_bf16 v[104:107], v[180:183], v[188:191], 0\n\t"
    "v_mad_u32_u24 v133, v133, s68, v151\n\t"
    "v_and_b32 v133, -16, v133\n\t"
    "v_mfma_f32_16x16x32_bf16 v[104:107], v[184:187], v[192:195], v[104:107]\n\t"
    "v_fmac_f32 v144, v92, v40\n\t"
    "v_fmac_f32 v145, v93, v41\n\t"
    "v_mfma_f32_16x16x32_bf16 v[108:111], v[180:183], v[196:199], 0\n\t"
    "v_fmac_f32 v144, v94, v42\n\t"
    "v_fmac_f32 v145, v95, v43\n\t"
    "v_mfma_f32_16x16x32_bf16 v[108:111], v[184:187], v[200:203], v[108:111]\n\t"
    "v_fmac_f32 v144, v96, v44\n\t"
    "v_fmac_f32 v145, v97, v45\n\t"
    "v_mfma_f32_16x16x32_bf16 v[112:115], v[180:183], v[204:207], 0\n\t"
    "v_fmac_f32 v144, v98, v46\n\t"
    "v_fmac_f32 v145, v99, v47\n\t"
    "v_mfma_f32_16x16x32_bf16 v[112:115], v[184:187], v[208:211], v[112:115]\n\t"
    "v_fmac_f32 v144, v100, v88\n\t"
    "v_mad_u32_u24 v134, v134, s68, v151\n\t"
    "ds_read_b128 v[180:183], v132 offset:0\n\t"
    "ds_read_b128 v[184:187], v132 offset:64\n\t"
    "v_mfma_f32_16x16x32_bf16 v[116:119], v[212:215], v[196:199], 0\n\t"
    "v_and_b32 v134, -16, v134\n\t"
    "v_fmac_f32 v145, v104, v48\n\t"
    "v_mfma_f32_16x16x32_bf16 v[116:119], v[216:219], v[200:203], v[116:119]\n\t"
    "v_fmac_f32 v144, v105, v49\n\t"
    "v_fmac_f32 v145, v106, v50\n\t"
    "v_mfma_f32_16x16x32_bf16 v[92:95], v[188:191], v[188:191], 0\n\t"
    "v_fmac_f32 v144, v107, v51\n\t"
    "v_mad_u32_u24 v135, v135, s68, v151\n\t"
    "v_mfma_f32_16x16x32_bf16 v[92:95], v[192:195], v[192:195], v[92:95]\n\t"
    "v_and_b32 v135, -16, v135\n\t"
    "v_fmac_f32 v145, v108, v52\n\t"
    "v_mfma_f32_16x16x32_bf16 v[96:99], v[188:191], v[196:199], 0\n\t"
    "v_fmac_f32 v144, v109, v53\n\t"
    "v_fmac_f32 v145, v110, v54\n\t"
    "v_mfma_f32_16x16x32_bf16 v[96:99], v[192:195], v[200:203], v[96:99]\n\t"
    "v_fmac_f32 v144, v111, v55\n\t"
    "v_fmac_f32 v145, v112, v56\n\t"
    "v_mfma_f32_16x16x32_bf16 v[100:103], v[188:191], v[204:207], 0\n\t"
    "v_fmac_f32 v144, v113, v57\n\t"
    "v_fmac_f32 v145, v114, v58\n\t"
    "v_mfma_f32_16x16x32_bf16 v[100:103], v[192:195], v[208:211], v[100:103]\n\t"
    "v_fmac_f32 v144, v115, v59\n\t"
    "v_fmac_f32 v145, v116, v89\n\t"
    "ds_read_b128 v[188:191], v133 offset:0\n\t"
    "ds_read_b128 v[192:195], v133 offset:64\n\t"
    "v_mfma_f32_16x16x32_bf16 v[104:107], v[212:215], v[204:207], 0\n\t"
    "v_fmac_f32 v144, v92, v60\n\t"
    "v_fmac_f32 v145, v93, v61\n\t"
    "v_mfma_f32_16x16x32_bf16 v[104:107], v[216:219], v[208:211], v[104:107]\n\t"
    "v_fmac_f32 v144, v94, v62\n\t"
    "v_fmac_f32 v145, v95, v63\n\t"
    "v_mfma_f32_16x16x32_bf16 v[108:111], v[196:199], v[196:199], 0\n\t"
    "v_fmac_f32 v144, v96, v64\n\t"
    "v_fmac_f32 v145, v97, v65\n\t"
    "v_mfma_f32_16x16x32_bf16 v[108:111], v[200:203], v[200:203], v[108:111]\n\t"
    "v_fmac_f32 v144, v98, v66\n\t"
    "v_fmac_f32 v145, v99, v67\n\t"
    "v_mfma_f32_16x16x32_bf16 v[112:115], v[196:199], v[204:207], 0\n\t"
    "v_fmac_f32 v144, v100, v68\n\t"
    "v_fmac_f32 v145, v101, v69\n\t"
    "v_mfma_f32_16x16x32_bf16 v[112:115], v[200:203], v[208:211], v[112:115]\n\t"
    "v_fmac_f32 v144, v102, v70\n\t"
    "v_fmac_f32 v145, v103, v71\n\t"
    "ds_read_b128 v[196:199], v134 offset:0\n\t"
    "ds_read_b128 v[200:203], v134 offset:64\n\t"
    "v_mfma_f32_16x16x32_bf16 v[116:119], v[204:207], v[204:207], 0\n\t"
    "v_fmac_f32 v144, v104, v90\n\t"
    "v_mfma_f32_16x16x32_bf16 v[116:119], v[208:211], v[208:211], v[116:119]\n\t"
    "ds_read_b128 v[204:207], v135 offset:0\n\t"
    "ds_read_b128 v[208:211], v135 offset:64\n\t"
    "s_mov_b32 s52, s53\n\t"
    "s_mov_b32 s53, s54\n\t"
    "s_mov_b32 s54, s55\n\t"
    "s_mov_b32 s55, s60\n\t"
    "s_cmp_lt_u32 s53, s64\n\t"
    "s_cbranch_scc1 L_apnn_%=\n\t"
    "L_adone_%=:\n\t"
    "s_mul_i32 s69, s52, 256\n\t"
    "s_add_u32 s69, s69, s50\n\t"
    "v_fmac_f32 v145, v108, v72\n\t"
    "v_fmac_f32 v144, v109, v73\n\t"
    "v_fmac_f32 v145, v110, v74\n\t"
    "v_fmac_f32 v144, v111, v75\n\t"
    "v_fmac_f32 v145, v112, v76\n\t"
    "v_fmac_f32 v144, v113, v77\n\t"
    "v_fmac_f32 v145, v114, v78\n\t"
    "v_fmac_f32 v144, v115, v79\n\t"
    "v_fmac_f32 v145, v116, v80\n\t"
    "v_fmac_f32 v144, v117, v81\n\t"
    "v_fmac_f32 v145, v118, v82\n\t"
    "v_fmac_f32 v144, v119, v83\n\t"
    "v_add_f32 v40, v144, v145\n\t"
    "L_aend_%=:\n\t"
    "s_branch L_aend2_%=\n\t"
    "L_ahk00_%=:\n\t"
    "v_mov_b32 v147, 3112\n\t"
    "s_cmp_lt_u32 s54, s64\n\t"
    "ds_read_b32 v147, v147\n\t"
    "s_cbranch_scc1 L_ahb00_%=\n\t"
    "s_waitcnt vmcnt(3)\n\t"
    "v_add_u32 v137, 0, v155\n\t"
    "v_add_u32 v138, 16, v155\n\t"
    "v_add_u32 v140, 32, v155\n\t"
    "v_add_u32 v141, 48, v155\n\t"
    "v_add_u32 v142, 64, v155\n\t"
    "v_add_u32 v143, 80, v155\n\t"
    "s_branch L_ahb00_%=\n\t"
    "L_ahk10_%=:\n\t"
    "s_waitcnt lgkmcnt(4)\n\t"
    "v_readfirstlane_b32 s65, v147\n\t"
    "s_nop 0\n\t"
    "s_cmp_eq_u32 s65, 0\n\t"
    "s_cbranch_scc1 L_ahb10_%=\n\t"
    "s_cmp_lt_u32 s62, 2\n\t"
    "s_cbranch_scc1 L_mel1_%=\n\t"
    "buffer_load_dwordx4 v[220:223], v127, %12, s61 offen offset:2048 sc1\n\t"
    "buffer_load_dwordx4 v[224:227], v127, %12, s61 offen offset:3072 sc1\n\t"
    "s_add_u32 s61, s61, 0x1000\n\t"
    "buffer_load_dwordx4 v[228:231], v127, %12, s61 offen offset:2048 sc1\n\t"
    "buffer_load_dwordx4 v[232:235], v127, %12, s61 offen offset:3072 sc1\n\t"
    "s_add_u32 s61, s61, 0x1000\n\t"
    "buffer_load_dwordx4 v[236:239], v127, %12, s61 offen offset:2048 sc1\n\t"
    "buffer_load_dwordx4 v[240:243], v127, %12, s61 offen offset:3072 sc1\n\t"
    "s_add_u32 s61, s61, 0x1000\n\t"
    "buffer_load_dwordx4 v[244:247], v127, %12, s61 offen offset:2048 sc1\n\t"
    "buffer_load_dwordx4 v[248:251], v127, %12, s61 offen offset:3072 sc1\n\t"
    "s_add_u32 s61, s61, 0x1000\n\t"
    "buffer_load_dwordx4 v[252:255], v127, %12, s61 offen offset:2048 sc1\n\t"
    "s_mov_b32 s62, 1\n\t"
    "L_mel1_%=:\n\t"
    "s_cmp_lt_u32 s54, s64\n\t"
    "s_cbranch_scc1 L_ahb10_%=\n\t"
    "buffer_load_dwordx4 v[0:3], v127, %12, s63 offen offset:0 sc1\n\t"
    "buffer_load_dwordx4 v[4:7], v127, %12, s63 offen offset:1024 sc1\n\t"
    "buffer_load_dwordx4 v[8:11], v127, %12, s63 offen offset:2048 sc1\n\t"
    "buffer_load_dwordx4 v[12:15], v127, %12, s63 offen offset:3072 sc1\n\t"
    "s_add_u32 s63, s63, 0x1000\n\t"
    "s_mov_b32 s62, 0\n\t"
    "s_branch L_ahb10_%=\n\t"
    "L_ahk01_%=:\n\t"
    "v_mov_b32 v147, 3112\n\t"
    "s_cmp_lt_u32 s54, s64\n\t"
    "ds_read_b32 v147, v147\n\t"
    "s_cbranch_scc1 L_ahb01_%=\n\t"
    "s_waitcnt vmcnt(3)\n\t"
    "v_add_u32 v129, 0, v155\n\t"
    "v_add_u32 v130, 16, v155\n\t"
    "v_add_u32 v132, 32, v155\n\t"
    "v_add_u32 v133, 48, v155\n\t"
    "v_add_u32 v134, 64, v155\n\t"
    "v_add_u32 v135, 80, v155\n\t"
    "s_branch L_ahb01_%=\n\t"
    "L_ahk11_%=:\n\t"
    "s_waitcnt lgkmcnt(4)\n\t"
    "v_readfirstlane_b32 s65, v147\n\t"
    "s_nop 0\n\t"
    "s_cmp_eq_u32 s65, 0\n\t"
    "s_cbranch_scc1 L_ahb11_%=\n\t"
    "s_cmp_lt_u32 s62, 2\n\t"
    "s_cbranch_scc1 L_mel3_%=\n\t"
    "buffer_load_dwordx4 v[220:223], v127, %12, s61 offen offset:2048 sc1\n\t"
    "buffer_load_dwordx4 v[224:227], v127, %12, s61 offen offset:3072 sc1\n\t"
    "s_add_u32 s61, s61, 0x1000\n\t"
    "buffer_load_dwordx4 v[228:231], v127, %12, s61 offen offset:2048 sc1\n\t"
    "buffer_load_dwordx4 v[232:235], v127, %12, s61 offen offset:3072 sc1\n\t"
    "s_add_u32 s61, s61, 0x1000\n\t"
    "buffer_load_dwordx4 v[236:239], v127, %12, s61 offen offset:2048 sc1\n\t"
    "buffer_load_dwordx4 v[240:243], v127, %12, s61 offen offset:3072 sc1\n\t"
    "s_add_u32 s61, s61, 0x1000\n\t"
    "buffer_load_dwordx4 v[244:247], v127, %12, s61 offen offset:2048 sc1\n\t"
    "buffer_load_dwordx4 v[248:251], v127, %12, s61 offen offset:3072 sc1\n\t"
    "s_add_u32 s61, s61, 0x1000\n\t"
    "buffer_load_dwordx4 v[252:255], v127, %12, s61 offen offset:2048 sc1\n\t"
    "s_mov_b32 s62, 1\n\t"
    "L_mel3_%=:\n\t"
    "s_cmp_lt_u32 s54, s64\n\t"
    "s_cbranch_scc1 L_ahb11_%=\n\t"
    "buffer_load_dwordx4 v[0:3], v127, %12, s63 offen offset:0 sc1\n\t"
    "buffer_load_dwordx4 v[4:7], v127, %12, s63 offen offset:1024 sc1\n\t"
    "buffer_load_dwordx4 v[8:11], v127, %12, s63 offen offset:2048 sc1\n\t"
    "buffer_load_dwordx4 v[12:15], v127, %12, s63 offen offset:3072 sc1\n\t"
    "s_add_u32 s63, s63, 0x1000\n\t"
    "s_mov_b32 s62, 0\n\t"
    "s_branch L_ahb11_%=\n\t"
    "L_aend2_%=:\n\t"
    "s_setprio 0\n\t"
    "v_lshlrev_b32 v113, 16, v127\n\t"
    "v_add_u32 v113, 3108, v113\n\t"
    "s_cmp_eq_u32 s62, 0\n\t"
    "s_cbranch_scc1 L_mpf_%=\n\t"
    "L_ml0_%=:\n\t"
    "ds_read_b32 v114, v113 offset:4\n\t"
    "s_waitcnt lgkmcnt(0)\n\t"
    "v_readfirstlane_b32 s65, v114\n\t"
    "s_nop 0\n\t"
    "s_cmp_lg_u32 s65, 0\n\t"
    "s_cbranch_scc1 L_mm0_%=\n\t"
    "s_sleep 2\n\t"
    "s_branch L_ml0_%=\n\t"
    "L_mm0_%=:\n\t"
    "s_cmp_lt_u32 s62, 2\n\t"
    "s_cbranch_scc1 L_mel5_%=\n\t"
    "buffer_load_dwordx4 v[220:223], v127, %12, s61 offen offset:2048 sc1\n\t"
    "buffer_load_dwordx4 v[224:227], v127, %12, s61 offen offset:3072 sc1\n\t"
    "s_add_u32 s61, s61, 0x1000\n\t"
    "buffer_load_dwordx4 v[228:231], v127, %12, s61 offen offset:2048 sc1\n\t"
    "buffer_load_dwordx4 v[232:235], v127, %12, s61 offen offset:3072 sc1\n\t"
    "s_add_u32 s61, s61, 0x1000\n\t"
    "buffer_load_dwordx4 v[236:239], v127, %12, s61 offen offset:2048 sc1\n\t"
    "buffer_load_dwordx4 v[240:243], v127, %12, s61 offen offset:3072 sc1\n\t"
    "s_add_u32 s61, s61, 0x1000\n\t"
    "buffer_load_dwordx4 v[244:247], v127, %12, s61 offen offset:2048 sc1\n\t"
    "buffer_load_dwordx4 v[248:251], v127, %12, s61 offen offset:3072 sc1\n\t"
    "s_add_u32 s61, s61, 0x1000\n\t"
    "buffer_load_dwordx4 v[252:255], v127, %12, s61 offen offset:2048 sc1\n\t"
    "s_mov_b32 s62, 1\n\t"
    "L_mel5_%=:\n\t"
    "buffer_load_dwordx4 v[0:3], v127, %12, s63 offen offset:0 sc1\n\t"
    "buffer_load_dwordx4 v[4:7], v127, %12, s63 offen offset:1024 sc1\n\t"
    "buffer_load_dwordx4 v[8:11], v127, %12, s63 offen offset:2048 sc1\n\t"
    "buffer_load_dwordx4 v[12:15], v127, %12, s63 offen offset:3072 sc1\n\t"
    "s_add_u32 s63, s63, 0x1000\n\t"
    "s_mov_b32 s62, 0\n\t"
    "L_mpf_%=:\n\t"
    "v_lshrrev_b32 v105, 4, v127\n\t"
    "v_and_b32 v105, 48, v105\n\t"
    "s_movk_i32 s65, 130\n\t"
    "v_mad_u32_u24 v104, v155, s65, v105\n\t"
    "v_add_u32 v105, 14170, v104\n\t"
    "v_and_b32 v104, -16, v104\n\t"
    "v_and_b32 v105, -16, v105\n\t"
    "buffer_load_dwordx4 v[128:131], v127, %12, s61 offen offset:3072 sc1\n\t"
    "s_add_u32 s61, s61, 0x1000\n\t"
    "buffer_load_dwordx4 v[132:135], v127, %12, s61 offen offset:2048 sc1\n\t"
    "ds_read_b128 v[156:159], v104 offset:12480\n\t"
    "ds_read_b128 v[160:163], v104 offset:12544\n\t"
    "buffer_load_dwordx4 v[136:139], v127, %12, s61 offen offset:3072 sc1\n\t"
    "s_add_u32 s61, s61, 0x1000\n\t"
    "buffer_load_dwordx4 v[140:143], v127, %12, s61 offen offset:2048 sc1\n\t"
    "ds_read_b128 v[212:215], v105 offset:0\n\t"
    "ds_read_b128 v[216:219], v105 offset:64\n\t"
    "buffer_load_dwordx4 v[144:147], v127, %12, s61 offen offset:3072 sc1\n\t"
    "s_add_u32 s61, s61, 0x1000\n\t"
    "buffer_load_dwordx4 v[148:151], v127, %12, s61 offen offset:2048 sc1\n\t"
    "buffer_load_dwordx4 v[152:155], v127, %12, s61 offen offset:3072 sc1\n\t"
    "s_add_u32 s61, s61, 0x1000\n\t"
    "v_mov_b32 v114, 1\n\t"
    "ds_add_u32 v113, v114\n\t"
    "s_waitcnt lgkmcnt(5)\n\t"
    "s_waitcnt vmcnt(7)\n\t"
    "v_mfma_f32_16x16x32_bf16 v[16:19], v[0:3], v[164:167], 0\n\t"
    "v_mfma_f32_16x16x32_bf16 v[16:19], v[4:7], v[168:171], v[16:19]\n\t"
    "v_mfma_f32_16x16x32_bf16 v[16:19], v[8:11], v[220:223], v[16:19]\n\t"
    "v_mfma_f32_16x16x32_bf16 v[16:19], v[12:15], v[224:227], v[16:19]\n\t"
    "v_mfma_f32_16x16x32_bf16 v[20:23], v[0:3], v[172:175], 0\n\t"
    "v_mfma_f32_16x16x32_bf16 v[20:23], v[4:7], v[176:179], v[20:23]\n\t"
    "v_mfma_f32_16x16x32_bf16 v[20:23], v[8:11], v[228:231], v[20:23]\n\t"
    "v_mfma_f32_16x16x32_bf16 v[20:23], v[12:15], v[232:235], v[20:23]\n\t"
    "v_mfma_f32_16x16x32_bf16 v[80:83], v[0:3], v[180:183], 0\n\t"
    "v_mfma_f32_16x16x32_bf16 v[80:83], v[4:7], v[184:187], v[80:83]\n\t"
    "v_mfma_f32_16x16x32_bf16 v[80:83], v[8:11], v[236:239], v[80:83]\n\t"
    "v_mfma_f32_16x16x32_bf16 v[80:83], v[12:15], v[240:243], v[80:83]\n\t"
    "v_cvt_pk_bf16_f32 v24, v16, v17\n\t"
    "v_cvt_pk_bf16_f32 v25, v18, v19\n\t"
    "v_mfma_f32_16x16x32_bf16 v[16:19], v[0:3], v[188:191], 0\n\t"
    "v_mfma_f32_16x16x32_bf16 v[16:19], v[4:7], v[192:195], v[16:19]\n\t"
    "v_mfma_f32_16x16x32_bf16 v[16:19], v[8:11], v[244:247], v[16:19]\n\t"
    "v_mfma_f32_16x16x32_bf16 v[16:19], v[12:15], v[248:251], v[16:19]\n\t"
    "v_cvt_pk_bf16_f32 v26, v20, v21\n\t"
    "v_cvt_pk_bf16_f32 v27, v22, v23\n\t"
    "s_waitcnt vmcnt(6)\n\t"
    "v_mfma_f32_16x16x32_bf16 v[20:23], v[0:3], v[196:199], 0\n\t"
    "v_mfma_f32_16x16x32_bf16 v[20:23], v[4:7], v[200:203], v[20:23]\n\t"
    "v_mfma_f32_16x16x32_bf16 v[20:23], v[8:11], v[252:255], v[20:23]\n\t"
    "v_mfma_f32_16x16x32_bf16 v[20:23], v[12:15], v[128:131], v[20:23]\n\t"
    "v_cvt_pk_bf16_f32 v28, v80, v81\n\t"
    "v_cvt_pk_bf16_f32 v29, v82, v83\n\t"
    "s_waitcnt lgkmcnt(0)\n\t"
    "s_waitcnt vmcnt(4)\n\t"
    "v_mfma_f32_16x16x32_bf16 v[80:83], v[0:3], v[204:207], 0\n\t"
    "v_mfma_f32_16x16x32_bf16 v[80:83], v[4:7], v[208:211], v[80:83]\n\t"
    "v_mfma_f32_16x16x32_bf16 v[80:83], v[8:11], v[132:135], v[80:83]\n\t"
    "v_mfma_f32_16x16x32_bf16 v[80:83], v[12:15], v[136:139], v[80:83]\n\t"
    "v_cvt_pk_bf16_f32 v30, v16, v17\n\t"
    "v_cvt_pk_bf16_f32 v31, v18, v19\n\t"
    "s_waitcnt vmcnt(2)\n\t"
    "v_mfma_f32_16x16x32_bf16 v[16:19], v[0:3], v[156:159], 0\n\t"
    "buffer_load_dwordx4 v[100:103], v127, %12, s84 offen offset:0 sc1\n\t"
    "s_add_u32 s84, s84, s97\n\t"
    "v_mfma_f32_16x16x32_bf16 v[16:19], v[4:7], v[160:163], v[16:19]\n\t"
    "buffer_load_dwordx4 v[104:107], v127, %12, s85 offen offset:0 sc1\n\t"
    "s_add_u32 s85, s85, s97\n\t"
    "v_mfma_f32_16x16x32_bf16 v[16:19], v[8:11], v[140:143], v[16:19]\n\t"
    "v_mfma_f32_16x16x32_bf16 v[16:19], v[12:15], v[144:147], v[16:19]\n\t"
    "v_cvt_pk_bf16_f32 v32, v20, v21\n\t"
    "v_cvt_pk_bf16_f32 v33, v22, v23\n\t"
    "s_waitcnt vmcnt(2)\n\t"
    "v_mfma_f32_16x16x32_bf16 v[20:23], v[0:3], v[212:215], 0\n\t"
    "buffer_load_dwordx4 v[108:111], v127, %12, s86 offen offset:0 sc1\n\t"
    "s_add_u32 s86, s86, s97\n\t"
    "v_mfma_f32_16x16x32_bf16 v[20:23], v[4:7], v[216:219], v[20:23]\n\t"
    "buffer_load_dwordx4 v[112:115], v127, %12, s87 offen offset:0 sc1\n\t"
    "s_add_u32 s87, s87, s97\n\t"
    "v_mfma_f32_16x16x32_bf16 v[20:23], v[8:11], v[148:151], v[20:23]\n\t"
    "v_mfma_f32_16x16x32_bf16 v[20:23], v[12:15], v[152:155], v[20:23]\n\t"
    "v_cvt_pk_bf16_f32 v34, v80, v81\n\t"
    "v_cvt_pk_bf16_f32 v35, v82, v83\n\t"
    "v_cvt_pk_bf16_f32 v36, v16, v17\n\t"
    "v_cvt_pk_bf16_f32 v37, v18, v19\n\t"
    "s_nop 3\n\t"
    "v_cvt_pk_bf16_f32 v38, v20, v21\n\t"
    "v_cvt_pk_bf16_f32 v39, v22, v23\n\t"
    "v_lshrrev_b32 v41, 2, v127\n\t"
    "s_and_b32 s65, %14, 8\n\t"
    "s_lshl_b32 s65, s65, 10\n\t"
    "s_add_u32 s63, s63, s65\n\t"
    "buffer_store_dword v40, v41, %12, s69 offen sc1\n\t"
    "v_mov_b32 v82, 3108\n\t"
    "s_cmp_eq_u32 %16, 0\n\t"
    "s_cselect_b32 s47, 0x1ff, 0\n\t"
    "L_my_%=:\n\t"
    "ds_read_b32 v81, v82\n\t"
    "s_waitcnt lgkmcnt(0)\n\t"
    "v_cmp_gt_u32 vcc, 8, v81\n\t"
    "s_cbranch_vccz L_mz_%=\n\t"
    "s_sleep 2\n\t"
    "s_branch L_my_%=\n\t"
    "L_mz_%=:\n\t"
    "ds_write_b64 v125, v[24:25] offset:0\n\t"
    "v_mov_b64 v[0:1], 0\n\t"
    "ds_write_b64 v125, v[26:27] offset:128\n\t"
    "v_mov_b64 v[2:3], 0\n\t"
    "ds_write_b64 v125, v[28:29] offset:256\n\t"
    "v_mov_b64 v[4:5], 0\n\t"
    "ds_write_b64 v125, v[30:31] offset:384\n\t"
    "v_mov_b64 v[6:7], 0\n\t"
    "ds_write_b64 v125, v[32:33] offset:512\n\t"
    "ds_write_b64 v125, v[34:35] offset:640\n\t"
    "ds_write_b64 v125, v[36:37] offset:768\n\t"
    "ds_write_b64 v125, v[38:39] offset:872\n\t"
    "v_add_u32 v125, s93, v125\n\t"
    "s_sub_u32 s93, 0, s93\n\t"
    "s_waitcnt lgkmcnt(0)\n\t"
    "s_barrier\n\t"
    "s_memrealtime s[44:45]\n\t"
    "s_waitcnt lgkmcnt(0)\n\t"
    "s_waitcnt vmcnt(4)\n\t"
    "v_add_u32_sdwa v126, s92, v100 dst_sel:DWORD dst_unused:UNUSED_PAD src0_sel:DWORD src1_sel:WORD_1\n\t"
    "v_add_u32_sdwa v100, s92, v100 dst_sel:DWORD dst_unused:UNUSED_PAD src0_sel:DWORD src1_sel:WORD_0\n\t"
    "ds_read_b64 v[84:85], v100 offset:0\n\t"
    "ds_read_b64 v[86:87], v126 offset:0\n\t"
    "v_add_u32_sdwa v90, s92, v101 dst_sel:DWORD dst_unused:UNUSED_PAD src0_sel:DWORD src1_sel:WORD_1\n\t"
    "v_add_u32_sdwa v101, s92, v101 dst_sel:DWORD dst_unused:UNUSED_PAD src0_sel:DWORD src1_sel:WORD_0\n\t"
    "ds_read_b64 v[88:89], v101 offset:0\n\t"
    "ds_read_b64 v[90:91], v90 offset:0\n\t"
    "v_add_u32 v63, s96, v127\n\t"
    "s_add_u32 s96, s96, 0x1000\n\t"
    "s_and_b32 s96, s96, 0x3000\n\t"
    "v_add_u32 v63, 128000, v63\n\t"
    "ds_read_b128 v[48:51], v63 offset:0\n\t"
    "ds_read_b128 v[52:55], v63 offset:1024\n\t"
    "ds_read_b128 v[56:59], v63 offset:2048\n\t"
    "ds_read_b128 v[60:63], v63 offset:3072\n\t"
    "v_add_u32_sdwa v126, s92, v102 dst_sel:DWORD dst_unused:UNUSED_PAD src0_sel:DWORD src1_sel:WORD_1\n\t"
    "v_add_u32_sdwa v102, s92, v102 dst_sel:DWORD dst_unused:UNUSED_PAD src0_sel:DWORD src1_sel:WORD_0\n\t"
    "ds_read_b64 v[92:93], v102 offset:0\n\t"
    "ds_read_b64 v[94:95], v126 offset:0\n\t"
    "v_add_u32_sdwa v98, s92, v103 dst_sel:DWORD dst_unused:UNUSED_PAD src0_sel:DWORD src1_sel:WORD_1\n\t"
    "v_add_u32_sdwa v103, s92, v103 dst_sel:DWORD dst_unused:UNUSED_PAD src0_sel:DWORD src1_sel:WORD_0\n\t"
    "ds_read_b64 v[96:97], v103 offset:0\n\t"
    "ds_read_b64 v[98:99], v98 offset:0\n\t"
    "buffer_load_dwordx4 v[100:103], v127, %12, s88 offen offset:0 sc1\n\t"
    "s_add_u32 s88, s88, s97\n\t"
    "s_waitcnt vmcnt(4)\n\t"
    "v_add_u32_sdwa v126, s92, v104 dst_sel:DWORD dst_unused:UNUSED_PAD src0_sel:DWORD src1_sel:WORD_1\n\t"
    "v_add_u32_sdwa v104, s92, v104 dst_sel:DWORD dst_unused:UNUSED_PAD src0_sel:DWORD src1_sel:WORD_0\n\t"
    "v_mov_b64 v[8:9], 0\n\t"
    "v_mov_b64 v[10:11], 0\n\t"
    "v_mov_b64 v[12:13], 0\n\t"
    "v_mov_b64 v[14:15], 0\n\t"
    "v_mov_b64 v[16:17], 0\n\t"
    "v_mov_b64 v[18:19], 0\n\t"
    "s_waitcnt lgkmcnt(8)\n\t"
    "v_smfmac_f32_16x16x64_bf16 v[0:3], v[120:123], v[84:91], v124\n\t"
    "ds_read_b64 v[84:85], v104 offset:0\n\t"
    "s_waitcnt lgkmcnt(5)\n\t"
    "v_mfma_f32_16x16x32_bf16 v[116:119], v[48:51], v[164:167], 0\n\t"
    "ds_read_b64 v[86:87], v126 offset:0\n\t"
    "v_mfma_f32_16x16x32_bf16 v[116:119], v[52:55], v[168:171], v[116:119]\n\t"
    "v_add_u32_sdwa v90, s92, v105 dst_sel:DWORD dst_unused:UNUSED_PAD src0_sel:DWORD src1_sel:WORD_1\n\t"
    "v_add_u32_sdwa v105, s92, v105 dst_sel:DWORD dst_unused:UNUSED_PAD src0_sel:DWORD src1_sel:WORD_0\n\t"
    "ds_read_b64 v[88:89], v105 offset:0\n\t"
    "v_mfma_f32_16x16x32_bf16 v[116:119], v[56:59], v[220:223], v[116:119]\n\t"
    "ds_read_b64 v[90:91], v90 offset:0\n\t"
    "v_mfma_f32_16x16x32_bf16 v[116:119], v[60:63], v[224:227], v[116:119]\n\t"
    "v_add_u32_sdwa v126, s92, v106 dst_sel:DWORD dst_unused:UNUSED_PAD src0_sel:DWORD src1_sel:WORD_1\n\t"
    "v_add_u32_sdwa v106, s92, v106 dst_sel:DWORD dst_unused:UNUSED_PAD src0_sel:DWORD src1_sel:WORD_0\n\t"
    "v_mfma_f32_16x16x32_bf16 v[80:83], v[48:51], v[172:175], 0\n\t"
    "v_mov_b64 v[20:21], 0\n\t"
    "v_mov_b64 v[22:23], 0\n\t"
    "v_mov_b64 v[24:25], 0\n\t"
    "v_mov_b64 v[26:27], 0\n\t"
    "v_mov_b64 v[28:29], 0\n\t"
    "v_mov_b64 v[30:31], 0\n\t"
    "s_waitcnt vmcnt(3) lgkmcnt(4)\n\t"
    "v_smfmac_f32_16x16x64_bf16 v[4:7], v[120:123], v[92:99], v124\n\t"
    "ds_read_b64 v[92:93], v106 offset:0\n\t"
    "v_mfma_f32_16x16x32_bf16 v[80:83], v[52:55], v[176:179], v[80:83]\n\t"
    "ds_read_b64 v[94:95], v126 offset:0\n\t"
    "v_mfma_f32_16x16x32_bf16 v[80:83], v[56:59], v[228:231], v[80:83]\n\t"
    "v_add_u32_sdwa v98, s92, v107 dst_sel:DWORD dst_unused:UNUSED_PAD src0_sel:DWORD src1_sel:WORD_1\n\t"
    "v_add_u32_sdwa v107, s92, v107 dst_sel:DWORD dst_unused:UNUSED_PAD src0_sel:DWORD src1_sel:WORD_0\n\t"
    "ds_read_b64 v[96:97], v107 offset:0\n\t"
    "v_mfma_f32_16x16x32_bf16 v[80:83], v[60:63], v[232:235], v[80:83]\n\t"
    "ds_read_b64 v[98:99], v98 offset:0\n\t"
    "v_cvt_pk_bf16_f32 v116, v116, v117\n\t"
    "v_cvt_pk_bf16_f32 v117, v118, v119\n\t"
    "ds_write_b64 v125, v[116:117] offset:0\n\t"
    "v_mfma_f32_16x16x32_bf16 v[116:119], v[48:51], v[180:183], 0\n\t"
    "buffer_load_dwordx4 v[104:107], v127, %12, s89 offen offset:0 sc1\n\t"
    "s_add_u32 s89, s89, s97\n\t"
    "v_add_u32_sdwa v126, s92, v108 dst_sel:DWORD dst_unused:UNUSED_PAD src0_sel:DWORD src1_sel:WORD_1\n\t"
    "v_add_u32_sdwa v108, s92, v108 dst_sel:DWORD dst_unused:UNUSED_PAD src0_sel:DWORD src1_sel:WORD_0\n\t"
    "v_mfma_f32_16x16x32_bf16 v[116:119], v[52:55], v[184:187], v[116:119]\n\t"
    "v_mov_b64 v[32:33], 0\n\t"
    "v_mov_b64 v[34:35], 0\n\t"
    "v_mov_b64 v[36:37], 0\n\t"
    "v_mov_b64 v[38:39], 0\n\t"
    "v_mov_b64 v[40:41], 0\n\t"
    "v_mov_b64 v[42:43], 0\n\t"
    "v_mov_b64 v[44:45], 0\n\t"
    "v_mov_b64 v[46:47], 0\n\t"
    "s_waitcnt lgkmcnt(5)\n\t"
    "v_smfmac_f32_16x16x64_bf16 v[8:11], v[120:123], v[84:91], v124\n\t"
    "ds_read_b64 v[84:85], v108 offset:0\n\t"
    "v_mfma_f32_16x16x32_bf16 v[116:119], v[56:59], v[236:239], v[116:119]\n\t"
    "ds_read_b64 v[86:87], v126 offset:0\n\t"
    "v_mfma_f32_16x16x32_bf16 v[116:119], v[60:63], v[240:243], v[116:119]\n\t"
    "v_add_u32_sdwa v90, s92, v109 dst_sel:DWORD dst_unused:UNUSED_PAD src0_sel:DWORD src1_sel:WORD_1\n\t"
    "v_add_u32_sdwa v109, s92, v109 dst_sel:DWORD dst_unused:UNUSED_PAD src0_sel:DWORD src1_sel:WORD_0\n\t"
    "ds_read_b64 v[88:89], v109 offset:0\n\t"
    "v_cvt_pk_bf16_f32 v80, v80, v81\n\t"
    "v_cvt_pk_bf16_f32 v81, v82, v83\n\t"
    "ds_write_b64 v125, v[80:81] offset:128\n\t"
    "v_mfma_f32_16x16x32_bf16 v[80:83], v[48:51], v[188:191], 0\n\t"
    "ds_read_b64 v[90:91], v90 offset:0\n\t"
    "v_mfma_f32_16x16x32_bf16 v[80:83], v[52:55], v[192:195], v[80:83]\n\t"
    "v_add_u32_sdwa v126, s92, v110 dst_sel:DWORD dst_unused:UNUSED_PAD src0_sel:DWORD src1_sel:WORD_1\n\t"
    "v_add_u32_sdwa v110, s92, v110 dst_sel:DWORD dst_unused:UNUSED_PAD src0_sel:DWORD src1_sel:WORD_0\n\t"
    "v_mfma_f32_16x16x32_bf16 v[80:83], v[56:59], v[244:247], v[80:83]\n\t"
    "s_waitcnt vmcnt(3) lgkmcnt(6)\n\t"
    "v_smfmac_f32_16x16x64_bf16 v[12:15], v[120:123], v[92:99], v124\n\t"
    "ds_read_b64 v[92:93], v110 offset:0\n\t"
    "v_mfma_f32_16x16x32_bf16 v[80:83], v[60:63], v[248:251], v[80:83]\n\t"
    "ds_read_b64 v[94:95], v126 offset:0\n\t"
    "v_cvt_pk_bf16_f32 v116, v116, v117\n\t"
    "v_cvt_pk_bf16_f32 v117, v118, v119\n\t"
    "ds_write_b64 v125, v[116:117] offset:256\n\t"
    "v_mfma_f32_16x16x32_bf16 v[116:119], v[48:51], v[196:199], 0\n\t"
    "v_add_u32_sdwa v98, s92, v111 dst_sel:DWORD dst_unused:UNUSED_PAD src0_sel:DWORD src1_sel:WORD_1\n\t"
    "v_add_u32_sdwa v111, s92, v111 dst_sel:DWORD dst_unused:UNUSED_PAD src0_sel:DWORD src1_sel:WORD_0\n\t"
    "ds_read_b64 v[96:97], v111 offset:0\n\t"
    "v_mfma_f32_16x16x32_bf16 v[116:119], v[52:55], v[200:203], v[116:119]\n\t"
    "ds_read_b64 v[98:99], v98 offset:0\n\t"
    "v_mfma_f32_16x16x32_bf16 v[116:119], v[56:59], v[252:255], v[116:119]\n\t"
    "buffer_load_dwordx4 v[108:111], v127, %12, s90 offen offset:0 sc1\n\t"
    "s_add_u32 s90, s90, s97\n\t"
    "v_add_u32_sdwa v126, s92, v112 dst_sel:DWORD dst_unused:UNUSED_PAD src0_sel:DWORD src1_sel:WORD_1\n\t"
    "v_add_u32_sdwa v112, s92, v112 dst_sel:DWORD dst_unused:UNUSED_PAD src0_sel:DWORD src1_sel:WORD_0\n\t"
    "v_mfma_f32_16x16x32_bf16 v[116:119], v[60:63], v[128:131], v[116:119]\n\t"
    "v_cvt_pk_bf16_f32 v0, v0, v1\n\t"
    "v_cvt_pk_bf16_f32 v1, v2, v3\n\t"
    "v_cvt_pk_bf16_f32 v80, v80, v81\n\t"
    "v_cvt_pk_bf16_f32 v81, v82, v83\n\t"
    "ds_write_b64 v125, v[80:81] offset:384\n\t"
    "v_add_u32 v79, s96, v127\n\t"
    "s_add_u32 s96, s96, 0x1000\n\t"
    "s_and_b32 s96, s96, 0x3000\n\t"
    "v_add_u32 v79, 128000, v79\n\t"
    "ds_read_b128 v[64:67], v79 offset:0\n\t"
    "ds_read_b128 v[68:71], v79 offset:1024\n\t"
    "ds_read_b128 v[72:75], v79 offset:2048\n\t"
    "ds_read_b128 v[76:79], v79 offset:3072\n\t"
    "s_waitcnt lgkmcnt(10)\n\t"
    "v_smfmac_f32_16x16x64_bf16 v[16:19], v[120:123], v[84:91], v124\n\t"
    "ds_read_b64 v[84:85], v112 offset:0\n\t"
    "v_mfma_f32_16x16x32_bf16 v[80:83], v[48:51], v[204:207], 0\n\t"
    "ds_read_b64 v[86:87], v126 offset:0\n\t"
    "v_mfma_f32_16x16x32_bf16 v[80:83], v[52:55], v[208:211], v[80:83]\n\t"
    "v_add_u32_sdwa v90, s92, v113 dst_sel:DWORD dst_unused:UNUSED_PAD src0_sel:DWORD src1_sel:WORD_1\n\t"
    "v_add_u32_sdwa v113, s92, v113 dst_sel:DWORD dst_unused:UNUSED_PAD src0_sel:DWORD src1_sel:WORD_0\n\t"
    "ds_read_b64 v[88:89], v113 offset:0\n\t"
    "v_mfma_f32_16x16x32_bf16 v[80:83], v[56:59], v[132:135], v[80:83]\n\t"
    "ds_read_b64 v[90:91], v90 offset:0\n\t"
    "v_mfma_f32_16x16x32_bf16 v[80:83], v[60:63], v[136:139], v[80:83]\n\t"
    "v_add_u32_sdwa v126, s92, v114 dst_sel:DWORD dst_unused:UNUSED_PAD src0_sel:DWORD src1_sel:WORD_1\n\t"
    "v_add_u32_sdwa v114, s92, v114 dst_sel:DWORD dst_unused:UNUSED_PAD src0_sel:DWORD src1_sel:WORD_0\n\t"
    "v_cvt_pk_bf16_f32 v116, v116, v117\n\t"
    "v_cvt_pk_bf16_f32 v117, v118, v119\n\t"
    "ds_write_b64 v125, v[116:117] offset:512\n\t"
    "v_mfma_f32_16x16x32_bf16 v[116:119], v[48:51], v[156:159], 0\n\t"
    "v_cvt_pk_bf16_f32 v2, v4, v5\n\t"
    "v_cvt_pk_bf16_f32 v3, v6, v7\n\t"
    "v_mov_b64 v[4:5], 0\n\t"
    "v_mov_b64 v[6:7], 0\n\t"
    "s_waitcnt vmcnt(2) lgkmcnt(10)\n\t"
    "v_smfmac_f32_16x16x64_bf16 v[20:23], v[120:123], v[92:99], v124\n\t"
    "ds_read_b64 v[92:93], v114 offset:0\n\t"
    "v_mfma_f32_16x16x32_bf16 v[116:119], v[52:55], v[160:163], v[116:119]\n\t"
    "ds_read_b64 v[94:95], v126 offset:0\n\t"
    "v_mfma_f32_16x16x32_bf16 v[116:119], v[56:59], v[140:143], v[116:119]\n\t"
    "v_add_u32_sdwa v98, s92, v115 dst_sel:DWORD dst_unused:UNUSED_PAD src0_sel:DWORD src1_sel:WORD_1\n\t"
    "v_add_u32_sdwa v115, s92, v115 dst_sel:DWORD dst_unused:UNUSED_PAD src0_sel:DWORD src1_sel:WORD_0\n\t"
    "ds_read_b64 v[96:97], v115 offset:0\n\t"
    "v_mfma_f32_16x16x32_bf16 v[116:119], v[60:63], v[144:147], v[116:119]\n\t"
    "ds_read_b64 v[98:99], v98 offset:0\n\t"
    "v_cvt_pk_bf16_f32 v80, v80, v81\n\t"
    "v_cvt_pk_bf16_f32 v81, v82, v83\n\t"
    "ds_write_b64 v125, v[80:81] offset:640\n\t"
    "v_mfma_f32_16x16x32_bf16 v[80:83], v[48:51], v[212:215], 0\n\t"
    "buffer_load_dwordx4 v[112:115], v127, %12, s91 offen offset:0 sc1\n\t"
    "s_add_u32 s91, s91, s97\n\t"
    "v_add_u32_sdwa v126, s92, v100 dst_sel:DWORD dst_unused:UNUSED_PAD src0_sel:DWORD src1_sel:WORD_1\n\t"
    "v_add_u32_sdwa v100, s92, v100 dst_sel:DWORD dst_unused:UNUSED_PAD src0_sel:DWORD src1_sel:WORD_0\n\t"
    "v_mfma_f32_16x16x32_bf16 v[80:83], v[52:55], v[216:219], v[80:83]\n\t"
    "v_cvt_pk_bf16_f32 v8, v8, v9\n\t"
    "v_cvt_pk_bf16_f32 v9, v10, v11\n\t"
    "s_waitcnt lgkmcnt(6)\n\t"
    "v_smfmac_f32_16x16x64_bf16 v[24:27], v[120:123], v[84:91], v124\n\t"
    "ds_read_b64 v[84:85], v100 offset:0\n\t"
    "v_mfma_f32_16x16x32_bf16 v[80:83], v[56:59], v[148:151], v[80:83]\n\t"
    "ds_read_b64 v[86:87], v126 offset:0\n\t"
    "v_mfma_f32_16x16x32_bf16 v[80:83], v[60:63], v[152:155], v[80:83]\n\t"
    "v_add_u32_sdwa v90, s92, v101 dst_sel:DWORD dst_unused:UNUSED_PAD src0_sel:DWORD src1_sel:WORD_1\n\t"
    "v_add_u32_sdwa v101, s92, v101 dst_sel:DWORD dst_unused:UNUSED_PAD src0_sel:DWORD src1_sel:WORD_0\n\t"
    "ds_read_b64 v[88:89], v101 offset:0\n\t"
    "v_cvt_pk_bf16_f32 v116, v116, v117\n\t"
    "v_cvt_pk_bf16_f32 v117, v118, v119\n\t"
    "ds_write_b64 v125, v[116:117] offset:768\n\t"
    "v_mfma_f32_16x16x32_bf16 v[116:119], v[64:67], v[164:167], 0\n\t"
    "ds_read_b64 v[90:91], v90 offset:0\n\t"
    "v_mfma_f32_16x16x32_bf16 v[116:119], v[68:71], v[168:171], v[116:119]\n\t"
    "v_add_u32_sdwa v126, s92, v102 dst_sel:DWORD dst_unused:UNUSED_PAD src0_sel:DWORD src1_sel:WORD_1\n\t"
    "v_add_u32_sdwa v102, s92, v102 dst_sel:DWORD dst_unused:UNUSED_PAD src0_sel:DWORD src1_sel:WORD_0\n\t"
    "v_mfma_f32_16x16x32_bf16 v[116:119], v[72:75], v[220:223], v[116:119]\n\t"
    "v_cvt_pk_bf16_f32 v10, v12, v13\n\t"
    "v_cvt_pk_bf16_f32 v11, v14, v15\n\t"
    "v_mov_b64 v[12:13], 0\n\t"
    "v_mov_b64 v[14:15], 0\n\t"
    "s_cmp_lg_u32 s47, 0\n\t"
    "s_cbranch_scc1 L_mp2_%=\n\t"
    "L_mp2b_%=:\n\t"
    "s_waitcnt vmcnt(2) lgkmcnt(6)\n\t"
    "v_smfmac_f32_16x16x64_bf16 v[28:31], v[120:123], v[92:99], v124\n\t"
    "ds_read_b64 v[92:93], v102 offset:0\n\t"
    "v_mfma_f32_16x16x32_bf16 v[116:119], v[76:79], v[224:227], v[116:119]\n\t"
    "ds_read_b64 v[94:95], v126 offset:0\n\t"
    "v_cvt_pk_bf16_f32 v80, v80, v81\n\t"
    "v_cvt_pk_bf16_f32 v81, v82, v83\n\t"
    "ds_write_b64 v125, v[80:81] offset:872\n\t"
    "v_mfma_f32_16x16x32_bf16 v[80:83], v[64:67], v[172:175], 0\n\t"
    "v_add_u32_sdwa v98, s92, v103 dst_sel:DWORD dst_unused:UNUSED_PAD src0_sel:DWORD src1_sel:WORD_1\n\t"
    "v_add_u32_sdwa v103, s92, v103 dst_sel:DWORD dst_unused:UNUSED_PAD src0_sel:DWORD src1_sel:WORD_0\n\t"
    "ds_read_b64 v[96:97], v103 offset:0\n\t"
    "v_mfma_f32_16x16x32_bf16 v[80:83], v[68:71], v[176:179], v[80:83]\n\t"
    "ds_read_b64 v[98:99], v98 offset:0\n\t"
    "v_mfma_f32_16x16x32_bf16 v[80:83], v[72:75], v[228:231], v[80:83]\n\t"
    "v_add_u32_sdwa v126, s92, v104 dst_sel:DWORD dst_unused:UNUSED_PAD src0_sel:DWORD src1_sel:WORD_1\n\t"
    "v_add_u32_sdwa v104, s92, v104 dst_sel:DWORD dst_unused:UNUSED_PAD src0_sel:DWORD src1_sel:WORD_0\n\t"
    "v_mfma_f32_16x16x32_bf16 v[80:83], v[76:79], v[232:235], v[80:83]\n\t"
    "v_cvt_pk_bf16_f32 v16, v16, v17\n\t"
    "v_cvt_pk_bf16_f32 v17, v18, v19\n\t"
    "v_cvt_pk_bf16_f32 v116, v116, v117\n\t"
    "v_cvt_pk_bf16_f32 v117, v118, v119\n\t"
    "ds_write_b64 v125, v[116:117] offset:32000\n\t"
    "s_waitcnt lgkmcnt(6)\n\t"
    "v_smfmac_f32_16x16x64_bf16 v[32:35], v[120:123], v[84:91], v124\n\t"
    "ds_read_b64 v[84:85], v104 offset:0\n\t"
    "v_mfma_f32_16x16x32_bf16 v[116:119], v[64:67], v[180:183], 0\n\t"
    "ds_read_b64 v[86:87], v126 offset:0\n\t"
    "v_mfma_f32_16x16x32_bf16 v[116:119], v[68:71], v[184:187], v[116:119]\n\t"
    "v_add_u32_sdwa v90, s92, v105 dst_sel:DWORD dst_unused:UNUSED_PAD src0_sel:DWORD src1_sel:WORD_1\n\t"
    "v_add_u32_sdwa v105, s92, v105 dst_sel:DWORD dst_unused:UNUSED_PAD src0_sel:DWORD src1_sel:WORD_0\n\t"
    "ds_read_b64 v[88:89], v105 offset:0\n\t"
    "v_mfma_f32_16x16x32_bf16 v[116:119], v[72:75], v[236:239], v[116:119]\n\t"
    "ds_read_b64 v[90:91], v90 offset:0\n\t"
    "v_mfma_f32_16x16x32_bf16 v[116:119], v[76:79], v[240:243], v[116:119]\n\t"
    "v_add_u32_sdwa v126, s92, v106 dst_sel:DWORD dst_unused:UNUSED_PAD src0_sel:DWORD src1_sel:WORD_1\n\t"
    "v_add_u32_sdwa v106, s92, v106 dst_sel:DWORD dst_unused:UNUSED_PAD src0_sel:DWORD src1_sel:WORD_0\n\t"
    "v_cvt_pk_bf16_f32 v80, v80, v81\n\t"
    "v_cvt_pk_bf16_f32 v81, v82, v83\n\t"
    "ds_write_b64 v125, v[80:81] offset:32128\n\t"
    "v_mfma_f32_16x16x32_bf16 v[80:83], v[64:67], v[188:191], 0\n\t"
    "v_cvt_pk_bf16_f32 v18, v20, v21\n\t"
    "v_cvt_pk_bf16_f32 v19, v22, v23\n\t"
    "v_mov_b64 v[20:21], 0\n\t"
    "v_mov_b64 v[22:23], 0\n\t"
    "s_waitcnt vmcnt(0)\n\t"
    "s_cmp_lg_u32 s47, 0\n\t"
    "s_cbranch_scc1 L_mp3_%=\n\t"
    "L_mp3b_%=:\n\t"
    "s_barrier\n\t"
    "s_add_u32 s84, s84, s95\n\t"
    "buffer_load_dwordx4 v[100:103], v127, %12, s84 offen offset:0 sc1\n\t"
    "s_lshl_b32 s50, %16, 10\n\t"
    "s_add_u32 s63, s63, s50\n\t"
    "s_add_u32 s50, s50, 0x2000\n\t"
    "s_xor_b32 s50, s50, 0x2000\n\t"
    "s_add_u32 m0, s50, 128000\n\t"
    "s_add_u32 s63, s63, 0x4000\n\t"
    "buffer_load_dwordx4 v127, %12, s63 offen sc1 lds\n\t"
    "s_cmp_lt_u32 %16, 4\n\t"
    "s_cbranch_scc1 L_sp1_%=\n\t"
    "s_setprio 1\n\t"
    "L_sp1_%=:\n\t"
    "s_waitcnt lgkmcnt(6)\n\t"
    "v_smfmac_f32_16x16x64_bf16 v[36:39], v[120:123], v[92:99], v124\n\t"
    "ds_read_b64 v[92:93], v106 offset:0\n\t"
    "v_mfma_f32_16x16x32_bf16 v[80:83], v[68:71], v[192:195], v[80:83]\n\t"
    "ds_read_b64 v[94:95], v126 offset:0\n\t"
    "v_mfma_f32_16x16x32_bf16 v[80:83], v[72:75], v[244:247], v[80:83]\n\t"
    "v_add_u32_sdwa v98, s92, v107 dst_sel:DWORD dst_unused:UNUSED_PAD src0_sel:DWORD src1_sel:WORD_1\n\t"
    "v_add_u32_sdwa v107, s92, v107 dst_sel:DWORD dst_unused:UNUSED_PAD src0_sel:DWORD src1_sel:WORD_0\n\t"
    "ds_read_b64 v[96:97], v107 offset:0\n\t"
    "v_mfma_f32_16x16x32_bf16 v[80:83], v[76:79], v[248:251], v[80:83]\n\t"
    "ds_read_b64 v[98:99], v98 offset:0\n\t"
    "v_cvt_pk_bf16_f32 v116, v116, v117\n\t"
    "v_cvt_pk_bf16_f32 v117, v118, v119\n\t"
    "ds_write_b64 v125, v[116:117] offset:32256\n\t"
    "v_mfma_f32_16x16x32_bf16 v[116:119], v[64:67], v[196:199], 0\n\t"
    "buffer_load_dwordx4 v[104:107], v127, %12, s84 offen offset:1024 sc1\n\t"
    "v_add_u32_sdwa v126, s92, v108 dst_sel:DWORD dst_unused:UNUSED_PAD src0_sel:DWORD src1_sel:WORD_1\n\t"
    "v_add_u32_sdwa v108, s92, v108 dst_sel:DWORD dst_unused:UNUSED_PAD src0_sel:DWORD src1_sel:WORD_0\n\t"
    "v_mfma_f32_16x16x32_bf16 v[116:119], v[68:71], v[200:203], v[116:119]\n\t"
    "v_cvt_pk_bf16_f32 v24, v24, v25\n\t"
    "v_cvt_pk_bf16_f32 v25, v26, v27\n\t"
    "v_mov_b64 v[48:49], 0\n\t"
    "v_mov_b64 v[50:51], 0\n\t"
    "v_mov_b64 v[52:53], 0\n\t"
    "v_mov_b64 v[54:55], 0\n\t"
    "s_waitcnt lgkmcnt(6)\n\t"
    "v_smfmac_f32_16x16x64_bf16 v[40:43], v[120:123], v[84:91], v124\n\t"
    "ds_read_b64 v[84:85], v108 offset:0\n\t"
    "v_mfma_f32_16x16x32_bf16 v[116:119], v[72:75], v[252:255], v[116:119]\n\t"
    "ds_read_b64 v[86:87], v126 offset:0\n\t"
    "v_mfma_f32_16x16x32_bf16 v[116:119], v[76:79], v[128:131], v[116:119]\n\t"
    "v_add_u32_sdwa v90, s92, v109 dst_sel:DWORD dst_unused:UNUSED_PAD src0_sel:DWORD src1_sel:WORD_1\n\t"
    "v_add_u32_sdwa v109, s92, v109 dst_sel:DWORD dst_unused:UNUSED_PAD src0_sel:DWORD src1_sel:WORD_0\n\t"
    "ds_read_b64 v[88:89], v109 offset:0\n\t"
    "v_cvt_pk_bf16_f32 v80, v80, v81\n\t"
    "v_cvt_pk_bf16_f32 v81, v82, v83\n\t"
    "ds_write_b64 v125, v[80:81] offset:32384\n\t"
    "v_mfma_f32_16x16x32_bf16 v[80:83], v[64:67], v[204:207], 0\n\t"
    "ds_read_b64 v[90:91], v90 offset:0\n\t"
    "v_mfma_f32_16x16x32_bf16 v[80:83], v[68:71], v[208:211], v[80:83]\n\t"
    "v_add_u32_sdwa v126, s92, v110 dst_sel:DWORD dst_unused:UNUSED_PAD src0_sel:DWORD src1_sel:WORD_1\n\t"
    "v_add_u32_sdwa v110, s92, v110 dst_sel:DWORD dst_unused:UNUSED_PAD src0_sel:DWORD src1_sel:WORD_0\n\t"
    "v_mfma_f32_16x16x32_bf16 v[80:83], v[72:75], v[132:135], v[80:83]\n\t"
    "v_cvt_pk_bf16_f32 v26, v28, v29\n\t"
    "v_cvt_pk_bf16_f32 v27, v30, v31\n\t"
    "v_mov_b64 v[28:29], 0\n\t"
    "v_mov_b64 v[30:31], 0\n\t"
    "s_waitcnt lgkmcnt(6)\n\t"
    "v_smfmac_f32_16x16x64_bf16 v[44:47], v[120:123], v[92:99], v124\n\t"
    "ds_read_b64 v[92:93], v110 offset:0\n\t"
    "v_mfma_f32_16x16x32_bf16 v[80:83], v[76:79], v[136:139], v[80:83]\n\t"
    "ds_read_b64 v[94:95], v126 offset:0\n\t"
    "v_cvt_pk_bf16_f32 v116, v116, v117\n\t"
    "v_cvt_pk_bf16_f32 v117, v118, v119\n\t"
    "ds_write_b64 v125, v[116:117] offset:32512\n\t"
    "v_mfma_f32_16x16x32_bf16 v[116:119], v[64:67], v[156:159], 0\n\t"
    "v_add_u32_sdwa v98, s92, v111 dst_sel:DWORD dst_unused:UNUSED_PAD src0_sel:DWORD src1_sel:WORD_1\n\t"
    "v_add_u32_sdwa v111, s92, v111 dst_sel:DWORD dst_unused:UNUSED_PAD src0_sel:DWORD src1_sel:WORD_0\n\t"
    "ds_read_b64 v[96:97], v111 offset:0\n\t"
    "v_mfma_f32_16x16x32_bf16 v[116:119], v[68:71], v[160:163], v[116:119]\n\t"
    "ds_read_b64 v[98:99], v98 offset:0\n\t"
    "v_mfma_f32_16x16x32_bf16 v[116:119], v[72:75], v[140:143], v[116:119]\n\t"
    "s_add_u32 s85, s85, s95\n\t"
    "buffer_load_dwordx4 v[108:111], v127, %12, s85 offen offset:0 sc1\n\t"
    "v_add_u32_sdwa v126, s92, v112 dst_sel:DWORD dst_unused:UNUSED_PAD src0_sel:DWORD src1_sel:WORD_1\n\t"
    "v_add_u32_sdwa v112, s92, v112 dst_sel:DWORD dst_unused:UNUSED_PAD src0_sel:DWORD src1_sel:WORD_0\n\t"
    "v_mfma_f32_16x16x32_bf16 v[116:119], v[76:79], v[144:147], v[116:119]\n\t"
    "v_cvt_pk_bf16_f32 v32, v32, v33\n\t"
    "v_cvt_pk_bf16_f32 v33, v34, v35\n\t"
    "v_cvt_pk_bf16_f32 v80, v80, v81\n\t"
    "v_cvt_pk_bf16_f32 v81, v82, v83\n\t"
    "ds_write_b64 v125, v[80:81] offset:32640\n\t"
    "v_mov_b64 v[56:57], 0\n\t"
    "v_mov_b64 v[58:59], 0\n\t"
    "v_mov_b64 v[60:61], 0\n\t"
    "v_mov_b64 v[62:63], 0\n\t"
    "s_waitcnt lgkmcnt(6)\n\t"
    "v_smfmac_f32_16x16x64_bf16 v[48:51], v[120:123], v[84:91], v124\n\t"
    "ds_read_b64 v[84:85], v112 offset:0\n\t"
    "v_mfma_f32_16x16x32_bf16 v[80:83], v[64:67], v[212:215], 0\n\t"
    "ds_read_b64 v[86:87], v126 offset:0\n\t"
    "v_mfma_f32_16x16x32_bf16 v[80:83], v[68:71], v[216:219], v[80:83]\n\t"
    "v_add_u32_sdwa v90, s92, v113 dst_sel:DWORD dst_unused:UNUSED_PAD src0_sel:DWORD src1_sel:WORD_1\n\t"
    "v_add_u32_sdwa v113, s92, v113 dst_sel:DWORD dst_unused:UNUSED_PAD src0_sel:DWORD src1_sel:WORD_0\n\t"
    "ds_read_b64 v[88:89], v113 offset:0\n\t"
    "v_mfma_f32_16x16x32_bf16 v[80:83], v[72:75], v[148:151], v[80:83]\n\t"
    "ds_read_b64 v[90:91], v90 offset:0\n\t"
    "v_mfma_f32_16x16x32_bf16 v[80:83], v[76:79], v[152:155], v[80:83]\n\t"
    "v_add_u32_sdwa v126, s92, v114 dst_sel:DWORD dst_unused:UNUSED_PAD src0_sel:DWORD src1_sel:WORD_1\n\t"
    "v_add_u32_sdwa v114, s92, v114 dst_sel:DWORD dst_unused:UNUSED_PAD src0_sel:DWORD src1_sel:WORD_0\n\t"
    "v_cvt_pk_bf16_f32 v116, v116, v117\n\t"
    "v_cvt_pk_bf16_f32 v117, v118, v119\n\t"
    "ds_write_b64 v125, v[116:117] offset:32768\n\t"
    "s_nop 2\n\t"
    "v_cvt_pk_bf16_f32 v80, v80, v81\n\t"
    "v_cvt_pk_bf16_f32 v81, v82, v83\n\t"
    "ds_write_b64 v125, v[80:81] offset:32872\n\t"
    "v_cvt_pk_bf16_f32 v34, v36, v37\n\t"
    "v_cvt_pk_bf16_f32 v35, v38, v39\n\t"
    "v_mov_b64 v[36:37], 0\n\t"
    "v_mov_b64 v[38:39], 0\n\t"
    "v_cvt_pk_bf16_f32 v40, v40, v41\n\t"
    "v_cvt_pk_bf16_f32 v41, v42, v43\n\t"
    "v_cvt_pk_bf16_f32 v42, v44, v45\n\t"
    "v_cvt_pk_bf16_f32 v43, v46, v47\n\t"
    "v_mov_b64 v[44:45], 0\n\t"
    "v_mov_b64 v[46:47], 0\n\t"
    "s_waitcnt vmcnt(3) lgkmcnt(7)\n\t"
    "v_smfmac_f32_16x16x64_bf16 v[52:55], v[120:123], v[92:99], v124\n\t"
    "ds_read_b64 v[92:93], v114 offset:0\n\t"
    "ds_read_b64 v[94:95], v126 offset:0\n\t"
    "v_add_u32_sdwa v98, s92, v115 dst_sel:DWORD dst_unused:UNUSED_PAD src0_sel:DWORD src1_sel:WORD_1\n\t"
    "v_add_u32_sdwa v115, s92, v115 dst_sel:DWORD dst_unused:UNUSED_PAD src0_sel:DWORD src1_sel:WORD_0\n\t"
    "ds_read_b64 v[96:97], v115 offset:0\n\t"
    "ds_read_b64 v[98:99], v98 offset:0\n\t"
    "buffer_load_dwordx4 v[112:115], v127, %12, s85 offen offset:1024 sc1\n\t"
    "s_sub_u32 s92, 64000, s92\n\t"
    "v_add_u32_sdwa v126, s92, v100 dst_sel:DWORD dst_unused:UNUSED_PAD src0_sel:DWORD src1_sel:WORD_1\n\t"
    "v_add_u32_sdwa v100, s92, v100 dst_sel:DWORD dst_unused:UNUSED_PAD src0_sel:DWORD src1_sel:WORD_0\n\t"
    "v_cvt_pk_bf16_f32 v48, v48, v49\n\t"
    "v_cvt_pk_bf16_f32 v49, v50, v51\n\t"
    "s_waitcnt lgkmcnt(4)\n\t"
    "s_waitcnt lgkmcnt(0)\n\t"
    "s_waitcnt vmcnt(3)\n\t"
    "s_barrier\n\t"
    "v_add_u32 v125, s93, v125\n\t"
    "s_sub_u32 s93, 0, s93\n\t"
    "v_smfmac_f32_16x16x64_bf16 v[56:59], v[120:123], v[84:91], v124\n\t"
    "ds_read_b64 v[84:85], v100 offset:0\n\t"
    "ds_read_b64 v[86:87], v126 offset:0\n\t"
    "v_add_u32_sdwa v90, s92, v101 dst_sel:DWORD dst_unused:UNUSED_PAD src0_sel:DWORD src1_sel:WORD_1\n\t"
    "v_add_u32_sdwa v101, s92, v101 dst_sel:DWORD dst_unused:UNUSED_PAD src0_sel:DWORD src1_sel:WORD_0\n\t"
    "ds_read_b64 v[88:89], v101 offset:0\n\t"
    "ds_read_b64 v[90:91], v90 offset:0\n\t"
    "v_add_u32_sdwa v126, s92, v102 dst_sel:DWORD dst_unused:UNUSED_PAD src0_sel:DWORD src1_sel:WORD_1\n\t"
    "v_add_u32_sdwa v102, s92, v102 dst_sel:DWORD dst_unused:UNUSED_PAD src0_sel:DWORD src1_sel:WORD_0\n\t"
    "v_cvt_pk_bf16_f32 v50, v52, v53\n\t"
    "v_cvt_pk_bf16_f32 v51, v54, v55\n\t"
    "v_mov_b64 v[52:53], 0\n\t"
    "v_mov_b64 v[54:55], 0\n\t"
    "s_waitcnt vmcnt(2)\n\t"
    "v_smfmac_f32_16x16x64_bf16 v[60:63], v[120:123], v[92:99], v124\n\t"
    "ds_read_b64 v[92:93], v102 offset:0\n\t"
    "ds_read_b64 v[94:95], v126 offset:0\n\t"
    "v_add_u32_sdwa v98, s92, v103 dst_sel:DWORD dst_unused:UNUSED_PAD src0_sel:DWORD src1_sel:WORD_1\n\t"
    "v_add_u32_sdwa v103, s92, v103 dst_sel:DWORD dst_unused:UNUSED_PAD src0_sel:DWORD src1_sel:WORD_0\n\t"
    "ds_read_b64 v[96:97], v103 offset:0\n\t"
    "ds_read_b64 v[98:99], v98 offset:0\n\t"
    "s_add_u32 s86, s86, s95\n\t"
    "buffer_load_dwordx4 v[100:103], v127, %12, s86 offen offset:0 sc1\n\t"
    "v_add_u32_sdwa v126, s92, v104 dst_sel:DWORD dst_unused:UNUSED_PAD src0_sel:DWORD src1_sel:WORD_1\n\t"
    "v_add_u32_sdwa v104, s92, v104 dst_sel:DWORD dst_unused:UNUSED_PAD src0_sel:DWORD src1_sel:WORD_0\n\t"
    "v_cvt_pk_bf16_f32 v56, v56, v57\n\t"
    "v_cvt_pk_bf16_f32 v57, v58, v59\n\t"
    "v_add_u32 v79, s96, v127\n\t"
    "s_add_u32 s96, s96, 0x1000\n\t"
    "s_and_b32 s96, s96, 0x3000\n\t"
    "v_add_u32 v79, 128000, v79\n\t"
    "ds_read_b128 v[64:67], v79 offset:0\n\t"
    "ds_read_b128 v[68:71], v79 offset:1024\n\t"
    "ds_read_b128 v[72:75], v79 offset:2048\n\t"
    "ds_read_b128 v[76:79], v79 offset:3072\n\t"
    "v_mov_b64 v[116:117], 0\n\t"
    "v_mov_b64 v[118:119], 0\n\t"
    "v_cvt_pk_bf16_f32 v58, v60, v61\n\t"
    "v_cvt_pk_bf16_f32 v59, v62, v63\n\t"
    "v_mov_b64 v[60:61], 0\n\t"
    "v_mov_b64 v[62:63], 0\n\t"
    "s_cmp_eq_u32 %16, 0\n\t"
    "s_cselect_b32 s48, 2, -1\n\t"
    "s_mov_b32 s66, 4\n\t"
    "s_branch L_loop_%=\n\t"
    ".p2align 9\n\t"
    "s_nop 0\n\t"
    "s_nop 0\n\t"
    "s_nop 0\n\t"
    "s_nop 0\n\t"
    "s_nop 0\n\t"
    "s_nop 0\n\t"
    "s_nop 0\n\t"
    "s_nop 0\n\t"
    "s_nop 0\n\t"
    "s_nop 0\n\t"
    "s_nop 0\n\t"
    "s_nop 0\n\t"
    "s_nop 0\n\t"
    "s_nop 0\n\t"
    "s_nop 0\n\t"
    "s_nop 0\n\t"
    "s_nop 0\n\t"
    "s_nop 0\n\t"
    "s_nop 0\n\t"
    "s_nop 0\n\t"
    "s_nop 0\n\t"
    "s_nop 0\n\t"
    "s_nop 0\n\t"
    "s_nop 0\n\t"
    "s_nop 0\n\t"
    "s_nop 0\n\t"
    "s_nop 0\n\t"
    "s_nop 0\n\t"
    "s_nop 0\n\t"
    "s_nop 0\n\t"
    "s_nop 0\n\t"
    "s_nop 0\n\t"
    "s_nop 0\n\t"
    "L_loop_%=:\n\t"
    "s_waitcnt lgkmcnt(8)\n\t"
    "v_smfmac_f32_16x16x64_bf16 v[116:119], v[120:123], v[84:91], v124\n\t"
    "ds_read_b64 v[84:85], v104 offset:0\n\t"
    "ds_read_b64 v[86:87], v126 offset:0\n\t"
    "v_add_u32_sdwa v90, s92, v105 dst_sel:DWORD dst_unused:UNUSED_PAD src0_sel:DWORD src1_sel:WORD_1\n\t"
    "v_add_u32_sdwa v105, s92, v105 dst_sel:DWORD dst_unused:UNUSED_PAD src0_sel:DWORD src1_sel:WORD_0\n\t"
    "ds_read_b64 v[88:89], v105 offset:0\n\t"
    "ds_read_b64 v[90:91], v90 offset:0\n\t"
    "v_add_u32_sdwa v126, s92, v106 dst_sel:DWORD dst_unused:UNUSED_PAD src0_sel:DWORD src1_sel:WORD_1\n\t"
    "v_add_u32_sdwa v106, s92, v106 dst_sel:DWORD dst_unused:UNUSED_PAD src0_sel:DWORD src1_sel:WORD_0\n\t"
    "s_waitcnt vmcnt(2) lgkmcnt(8)\n\t"
    "v_smfmac_f32_16x16x64_bf16 v[116:119], v[120:123], v[92:99], v124\n\t"
    "ds_read_b64 v[92:93], v106 offset:0\n\t"
    "ds_read_b64 v[94:95], v126 offset:0\n\t"
    "v_add_u32_sdwa v98, s92, v107 dst_sel:DWORD dst_unused:UNUSED_PAD src0_sel:DWORD src1_sel:WORD_1\n\t"
    "v_add_u32_sdwa v107, s92, v107 dst_sel:DWORD dst_unused:UNUSED_PAD src0_sel:DWORD src1_sel:WORD_0\n\t"
    "ds_read_b64 v[96:97], v107 offset:0\n\t"
    "ds_read_b64 v[98:99], v98 offset:0\n\t"
    "buffer_load_dwordx4 v[104:107], v127, %12, s86 offen offset:1024 sc1\n\t"
    "v_add_u32_sdwa v126, s92, v108 dst_sel:DWORD dst_unused:UNUSED_PAD src0_sel:DWORD src1_sel:WORD_1\n\t"
    "v_cvt_pk_bf16_f32 v4, v116, v117\n\t"
    "v_cvt_pk_bf16_f32 v5, v118, v119\n\t"
    "v_and_b32 v119, s67, v7\n\t"
    "v_lshlrev_b32 v118, 16, v7\n\t"
    "v_and_b32 v117, s67, v6\n\t"
    "v_lshlrev_b32 v116, 16, v6\n\t"
    "v_add_u32_sdwa v108, s92, v108 dst_sel:DWORD dst_unused:UNUSED_PAD src0_sel:DWORD src1_sel:WORD_0\n\t"
    "s_waitcnt lgkmcnt(4)\n\t"
    "v_smfmac_f32_16x16x64_bf16 v[116:119], v[120:123], v[84:91], v124\n\t"
    "ds_read_b64 v[84:85], v108 offset:0\n\t"
    "v_mfma_f32_16x16x32_bf16 v[80:83], v[64:67], v[164:167], 0\n\t"
    "ds_read_b64 v[86:87], v126 offset:0\n\t"
    "v_mfma_f32_16x16x32_bf16 v[80:83], v[68:71], v[168:171], v[80:83]\n\t"
    "v_add_u32_sdwa v90, s92, v109 dst_sel:DWORD dst_unused:UNUSED_PAD src0_sel:DWORD src1_sel:WORD_1\n\t"
    "v_add_u32_sdwa v109, s92, v109 dst_sel:DWORD dst_unused:UNUSED_PAD src0_sel:DWORD src1_sel:WORD_0\n\t"
    "ds_read_b64 v[88:89], v109 offset:0\n\t"
    "v_mfma_f32_16x16x32_bf16 v[80:83], v[72:75], v[220:223], v[80:83]\n\t"
    "ds_read_b64 v[90:91], v90 offset:0\n\t"
    "v_mfma_f32_16x16x32_bf16 v[80:83], v[76:79], v[224:227], v[80:83]\n\t"
    "v_add_u32_sdwa v126, s92, v110 dst_sel:DWORD dst_unused:UNUSED_PAD src0_sel:DWORD src1_sel:WORD_1\n\t"
    "v_add_u32_sdwa v110, s92, v110 dst_sel:DWORD dst_unused:UNUSED_PAD src0_sel:DWORD src1_sel:WORD_0\n\t"
    "s_waitcnt vmcnt(2) lgkmcnt(4)\n\t"
    "v_smfmac_f32_16x16x64_bf16 v[116:119], v[120:123], v[92:99], v124\n\t"
    "ds_read_b64 v[92:93], v110 offset:0\n\t"
    "ds_read_b64 v[94:95], v126 offset:0\n\t"
    "v_add_u32_sdwa v98, s92, v111 dst_sel:DWORD dst_unused:UNUSED_PAD src0_sel:DWORD src1_sel:WORD_1\n\t"
    "v_add_u32_sdwa v111, s92, v111 dst_sel:DWORD dst_unused:UNUSED_PAD src0_sel:DWORD src1_sel:WORD_0\n\t"
    "ds_read_b64 v[96:97], v111 offset:0\n\t"
    "ds_read_b64 v[98:99], v98 offset:0\n\t"
    "s_add_u32 s87, s87, s95\n\t"
    "buffer_load_dwordx4 v[108:111], v127, %12, s87 offen offset:0 sc1\n\t"
    "v_add_u32_sdwa v126, s92, v112 dst_sel:DWORD dst_unused:UNUSED_PAD src0_sel:DWORD src1_sel:WORD_1\n\t"
    "v_cvt_pk_bf16_f32 v6, v116, v117\n\t"
    "v_cvt_pk_bf16_f32 v7, v118, v119\n\t"
    "v_and_b32 v119, s67, v13\n\t"
    "v_lshlrev_b32 v118, 16, v13\n\t"
    "v_and_b32 v117, s67, v12\n\t"
    "v_lshlrev_b32 v116, 16, v12\n\t"
    "v_add_u32_sdwa v112, s92, v112 dst_sel:DWORD dst_unused:UNUSED_PAD src0_sel:DWORD src1_sel:WORD_0\n\t"
    "s_waitcnt lgkmcnt(4)\n\t"
    "v_smfmac_f32_16x16x64_bf16 v[116:119], v[120:123], v[84:91], v124\n\t"
    "ds_read_b64 v[84:85], v112 offset:0\n\t"
    "ds_read_b64 v[86:87], v126 offset:0\n\t"
    "v_cvt_pk_bf16_f32 v80, v80, v81\n\t"
    "v_cvt_pk_bf16_f32 v81, v82, v83\n\t"
    "ds_write_b64 v125, v[80:81] offset:0\n\t"
    "v_mfma_f32_16x16x32_bf16 v[80:83], v[64:67], v[172:175], 0\n\t"
    "v_add_u32_sdwa v90, s92, v113 dst_sel:DWORD dst_unused:UNUSED_PAD src0_sel:DWORD src1_sel:WORD_1\n\t"
    "v_add_u32_sdwa v113, s92, v113 dst_sel:DWORD dst_unused:UNUSED_PAD src0_sel:DWORD src1_sel:WORD_0\n\t"
    "ds_read_b64 v[88:89], v113 offset:0\n\t"
    "v_mfma_f32_16x16x32_bf16 v[80:83], v[68:71], v[176:179], v[80:83]\n\t"
    "ds_read_b64 v[90:91], v90 offset:0\n\t"
    "v_mfma_f32_16x16x32_bf16 v[80:83], v[72:75], v[228:231], v[80:83]\n\t"
    "v_mfma_f32_16x16x32_bf16 v[80:83], v[76:79], v[232:235], v[80:83]\n\t"
    "v_add_u32_sdwa v126, s92, v114 dst_sel:DWORD dst_unused:UNUSED_PAD src0_sel:DWORD src1_sel:WORD_1\n\t"
    "v_add_u32_sdwa v114, s92, v114 dst_sel:DWORD dst_unused:UNUSED_PAD src0_sel:DWORD src1_sel:WORD_0\n\t"
    "s_waitcnt vmcnt(2) lgkmcnt(5)\n\t"
    "v_smfmac_f32_16x16x64_bf16 v[116:119], v[120:123], v[92:99], v124\n\t"
    "ds_read_b64 v[92:93], v114 offset:0\n\t"
    "ds_read_b64 v[94:95], v126 offset:0\n\t"
    "v_add_u32_sdwa v98, s92, v115 dst_sel:DWORD dst_unused:UNUSED_PAD src0_sel:DWORD src1_sel:WORD_1\n\t"
    "v_add_u32_sdwa v115, s92, v115 dst_sel:DWORD dst_unused:UNUSED_PAD src0_sel:DWORD src1_sel:WORD_0\n\t"
    "ds_read_b64 v[96:97], v115 offset:0\n\t"
    "ds_read_b64 v[98:99], v98 offset:0\n\t"
    "buffer_load_dwordx4 v[112:115], v127, %12, s87 offen offset:1024 sc1\n\t"
    "v_add_u32_sdwa v126, s92, v100 dst_sel:DWORD dst_unused:UNUSED_PAD src0_sel:DWORD src1_sel:WORD_1\n\t"
    "v_cvt_pk_bf16_f32 v12, v116, v117\n\t"
    "v_cvt_pk_bf16_f32 v13, v118, v119\n\t"
    "v_and_b32 v119, s67, v15\n\t"
    "v_lshlrev_b32 v118, 16, v15\n\t"
    "v_and_b32 v117, s67, v14\n\t"
    "v_lshlrev_b32 v116, 16, v14\n\t"
    "v_add_u32_sdwa v100, s92, v100 dst_sel:DWORD dst_unused:UNUSED_PAD src0_sel:DWORD src1_sel:WORD_0\n\t"
    "s_waitcnt lgkmcnt(4)\n\t"
    "v_smfmac_f32_16x16x64_bf16 v[116:119], v[120:123], v[84:91], v124\n\t"
    "ds_read_b64 v[84:85], v100 offset:0\n\t"
    "ds_read_b64 v[86:87], v126 offset:0\n\t"
    "v_cvt_pk_bf16_f32 v80, v80, v81\n\t"
    "v_cvt_pk_bf16_f32 v81, v82, v83\n\t"
    "ds_write_b64 v125, v[80:81] offset:128\n\t"
    "v_mfma_f32_16x16x32_bf16 v[80:83], v[64:67], v[180:183], 0\n\t"
    "v_add_u32_sdwa v90, s92, v101 dst_sel:DWORD dst_unused:UNUSED_PAD src0_sel:DWORD src1_sel:WORD_1\n\t"
    "v_add_u32_sdwa v101, s92, v101 dst_sel:DWORD dst_unused:UNUSED_PAD src0_sel:DWORD src1_sel:WORD_0\n\t"
    "ds_read_b64 v[88:89], v101 offset:0\n\t"
    "v_mfma_f32_16x16x32_bf16 v[80:83], v[68:71], v[184:187], v[80:83]\n\t"
    "ds_read_b64 v[90:91], v90 offset:0\n\t"
    "v_mfma_f32_16x16x32_bf16 v[80:83], v[72:75], v[236:239], v[80:83]\n\t"
    "v_mfma_f32_16x16x32_bf16 v[80:83], v[76:79], v[240:243], v[80:83]\n\t"
    "v_add_u32_sdwa v126, s92, v102 dst_sel:DWORD dst_unused:UNUSED_PAD src0_sel:DWORD src1_sel:WORD_1\n\t"
    "v_add_u32_sdwa v102, s92, v102 dst_sel:DWORD dst_unused:UNUSED_PAD src0_sel:DWORD src1_sel:WORD_0\n\t"
    "s_waitcnt vmcnt(2) lgkmcnt(5)\n\t"
    "v_smfmac_f32_16x16x64_bf16 v[116:119], v[120:123], v[92:99], v124\n\t"
    "ds_read_b64 v[92:93], v102 offset:0\n\t"
    "ds_read_b64 v[94:95], v126 offset:0\n\t"
    "v_add_u32_sdwa v98, s92, v103 dst_sel:DWORD dst_unused:UNUSED_PAD src0_sel:DWORD src1_sel:WORD_1\n\t"
    "v_add_u32_sdwa v103, s92, v103 dst_sel:DWORD dst_unused:UNUSED_PAD src0_sel:DWORD src1_sel:WORD_0\n\t"
    "ds_read_b64 v[96:97], v103 offset:0\n\t"
    "ds_read_b64 v[98:99], v98 offset:0\n\t"
    "s_add_u32 s88, s88, s95\n\t"
    "buffer_load_dwordx4 v[100:103], v127, %12, s88 offen offset:0 sc1\n\t"
    "v_add_u32_sdwa v126, s92, v104 dst_sel:DWORD dst_unused:UNUSED_PAD src0_sel:DWORD src1_sel:WORD_1\n\t"
    "v_cvt_pk_bf16_f32 v14, v116, v117\n\t"
    "v_cvt_pk_bf16_f32 v15, v118, v119\n\t"
    "v_and_b32 v119, s67, v21\n\t"
    "v_lshlrev_b32 v118, 16, v21\n\t"
    "v_and_b32 v117, s67, v20\n\t"
    "v_lshlrev_b32 v116, 16, v20\n\t"
    "v_add_u32_sdwa v104, s92, v104 dst_sel:DWORD dst_unused:UNUSED_PAD src0_sel:DWORD src1_sel:WORD_0\n\t"
    "s_waitcnt lgkmcnt(4)\n\t"
    "v_smfmac_f32_16x16x64_bf16 v[116:119], v[120:123], v[84:91], v124\n\t"
    "ds_read_b64 v[84:85], v104 offset:0\n\t"
    "ds_read_b64 v[86:87], v126 offset:0\n\t"
    "v_cvt_pk_bf16_f32 v80, v80, v81\n\t"
    "v_cvt_pk_bf16_f32 v81, v82, v83\n\t"
    "ds_write_b64 v125, v[80:81] offset:256\n\t"
    "v_add_u32_sdwa v90, s92, v105 dst_sel:DWORD dst_unused:UNUSED_PAD src0_sel:DWORD src1_sel:WORD_1\n\t"
    "v_add_u32_sdwa v105, s92, v105 dst_sel:DWORD dst_unused:UNUSED_PAD src0_sel:DWORD src1_sel:WORD_0\n\t"
    "ds_read_b64 v[88:89], v105 offset:0\n\t"
    "ds_read_b64 v[90:91], v90 offset:0\n\t"
    "v_add_u32_sdwa v126, s92, v106 dst_sel:DWORD dst_unused:UNUSED_PAD src0_sel:DWORD src1_sel:WORD_1\n\t"
    "v_add_u32_sdwa v106, s92, v106 dst_sel:DWORD dst_unused:UNUSED_PAD src0_sel:DWORD src1_sel:WORD_0\n\t"
    "s_waitcnt vmcnt(2) lgkmcnt(5)\n\t"
    "v_smfmac_f32_16x16x64_bf16 v[116:119], v[120:123], v[92:99], v124\n\t"
    "ds_read_b64 v[92:93], v106 offset:0\n\t"
    "ds_read_b64 v[94:95], v126 offset:0\n\t"
    "v_add_u32_sdwa v98, s92, v107 dst_sel:DWORD dst_unused:UNUSED_PAD src0_sel:DWORD src1_sel:WORD_1\n\t"
    "v_add_u32_sdwa v107, s92, v107 dst_sel:DWORD dst_unused:UNUSED_PAD src0_sel:DWORD src1_sel:WORD_0\n\t"
    "ds_read_b64 v[96:97], v107 offset:0\n\t"
    "ds_read_b64 v[98:99], v98 offset:0\n\t"
    "buffer_load_dwordx4 v[104:107], v127, %12, s88 offen offset:1024 sc1\n\t"
    "v_add_u32_sdwa v126, s92, v108 dst_sel:DWORD dst_unused:UNUSED_PAD src0_sel:DWORD src1_sel:WORD_1\n\t"
    "v_cvt_pk_bf16_f32 v20, v116, v117\n\t"
    "v_cvt_pk_bf16_f32 v21, v118, v119\n\t"
    "v_and_b32 v119, s67, v23\n\t"
    "v_lshlrev_b32 v118, 16, v23\n\t"
    "v_and_b32 v117, s67, v22\n\t"
    "v_lshlrev_b32 v116, 16, v22\n\t"
    "v_add_u32_sdwa v108, s92, v108 dst_sel:DWORD dst_unused:UNUSED_PAD src0_sel:DWORD src1_sel:WORD_0\n\t"
    "s_waitcnt lgkmcnt(4)\n\t"
    "v_smfmac_f32_16x16x64_bf16 v[116:119], v[120:123], v[84:91], v124\n\t"
    "ds_read_b64 v[84:85], v108 offset:0\n\t"
    "v_mfma_f32_16x16x32_bf16 v[80:83], v[64:67], v[188:191], 0\n\t"
    "ds_read_b64 v[86:87], v126 offset:0\n\t"
    "v_mfma_f32_16x16x32_bf16 v[80:83], v[68:71], v[192:195], v[80:83]\n\t"
    "v_add_u32_sdwa v90, s92, v109 dst_sel:DWORD dst_unused:UNUSED_PAD src0_sel:DWORD src1_sel:WORD_1\n\t"
    "v_add_u32_sdwa v109, s92, v109 dst_sel:DWORD dst_unused:UNUSED_PAD src0_sel:DWORD src1_sel:WORD_0\n\t"
    "ds_read_b64 v[88:89], v109 offset:0\n\t"
    "v_mfma_f32_16x16x32_bf16 v[80:83], v[72:75], v[244:247], v[80:83]\n\t"
    "ds_read_b64 v[90:91], v90 offset:0\n\t"
    "v_mfma_f32_16x16x32_bf16 v[80:83], v[76:79], v[248:251], v[80:83]\n\t"
    "v_add_u32_sdwa v126, s92, v110 dst_sel:DWORD dst_unused:UNUSED_PAD src0_sel:DWORD src1_sel:WORD_1\n\t"
    "v_add_u32_sdwa v110, s92, v110 dst_sel:DWORD dst_unused:UNUSED_PAD src0_sel:DWORD src1_sel:WORD_0\n\t"
    "s_waitcnt vmcnt(2) lgkmcnt(4)\n\t"
    "v_smfmac_f32_16x16x64_bf16 v[116:119], v[120:123], v[92:99], v124\n\t"
    "ds_read_b64 v[92:93], v110 offset:0\n\t"
    "ds_read_b64 v[94:95], v126 offset:0\n\t"
    "v_add_u32_sdwa v98, s92, v111 dst_sel:DWORD dst_unused:UNUSED_PAD src0_sel:DWORD src1_sel:WORD_1\n\t"
    "v_add_u32_sdwa v111, s92, v111 dst_sel:DWORD dst_unused:UNUSED_PAD src0_sel:DWORD src1_sel:WORD_0\n\t"
    "ds_read_b64 v[96:97], v111 offset:0\n\t"
    "ds_read_b64 v[98:99], v98 offset:0\n\t"
    "s_add_u32 s89, s89, s95\n\t"
    "buffer_load_dwordx4 v[108:111], v127, %12, s89 offen offset:0 sc1\n\t"
    "v_add_u32_sdwa v126, s92, v112 dst_sel:DWORD dst_unused:UNUSED_PAD src0_sel:DWORD src1_sel:WORD_1\n\t"
    "v_cvt_pk_bf16_f32 v22, v116, v117\n\t"
    "v_cvt_pk_bf16_f32 v23, v118, v119\n\t"
    "v_and_b32 v119, s67, v29\n\t"
    "v_lshlrev_b32 v118, 16, v29\n\t"
    "v_and_b32 v117, s67, v28\n\t"
    "v_lshlrev_b32 v116, 16, v28\n\t"
    "v_add_u32_sdwa v112, s92, v112 dst_sel:DWORD dst_unused:UNUSED_PAD src0_sel:DWORD src1_sel:WORD_0\n\t"
    "s_waitcnt lgkmcnt(4)\n\t"
    "v_smfmac_f32_16x16x64_bf16 v[116:119], v[120:123], v[84:91], v124\n\t"
    "ds_read_b64 v[84:85], v112 offset:0\n\t"
    "ds_read_b64 v[86:87], v126 offset:0\n\t"
    "v_cvt_pk_bf16_f32 v80, v80, v81\n\t"
    "v_cvt_pk_bf16_f32 v81, v82, v83\n\t"
    "ds_write_b64 v125, v[80:81] offset:384\n\t"
    "v_add_u32_sdwa v90, s92, v113 dst_sel:DWORD dst_unused:UNUSED_PAD src0_sel:DWORD src1_sel:WORD_1\n\t"
    "v_add_u32_sdwa v113, s92, v113 dst_sel:DWORD dst_unused:UNUSED_PAD src0_sel:DWORD src1_sel:WORD_0\n\t"
    "ds_read_b64 v[88:89], v113 offset:0\n\t"
    "ds_read_b64 v[90:91], v90 offset:0\n\t"
    "v_add_u32_sdwa v126, s92, v114 dst_sel:DWORD dst_unused:UNUSED_PAD src0_sel:DWORD src1_sel:WORD_1\n\t"
    "v_add_u32_sdwa v114, s92, v114 dst_sel:DWORD dst_unused:UNUSED_PAD src0_sel:DWORD src1_sel:WORD_0\n\t"
    "s_waitcnt vmcnt(2) lgkmcnt(5)\n\t"
    "v_smfmac_f32_16x16x64_bf16 v[116:119], v[120:123], v[92:99], v124\n\t"
    "ds_read_b64 v[92:93], v114 offset:0\n\t"
    "ds_read_b64 v[94:95], v126 offset:0\n\t"
    "v_add_u32_sdwa v98, s92, v115 dst_sel:DWORD dst_unused:UNUSED_PAD src0_sel:DWORD src1_sel:WORD_1\n\t"
    "v_add_u32_sdwa v115, s92, v115 dst_sel:DWORD dst_unused:UNUSED_PAD src0_sel:DWORD src1_sel:WORD_0\n\t"
    "ds_read_b64 v[96:97], v115 offset:0\n\t"
    "ds_read_b64 v[98:99], v98 offset:0\n\t"
    "buffer_load_dwordx4 v[112:115], v127, %12, s89 offen offset:1024 sc1\n\t"
    "v_add_u32_sdwa v126, s92, v100 dst_sel:DWORD dst_unused:UNUSED_PAD src0_sel:DWORD src1_sel:WORD_1\n\t"
    "v_cvt_pk_bf16_f32 v28, v116, v117\n\t"
    "v_cvt_pk_bf16_f32 v29, v118, v119\n\t"
    "v_and_b32 v119, s67, v31\n\t"
    "v_lshlrev_b32 v118, 16, v31\n\t"
    "v_and_b32 v117, s67, v30\n\t"
    "v_lshlrev_b32 v116, 16, v30\n\t"
    "v_add_u32_sdwa v100, s92, v100 dst_sel:DWORD dst_unused:UNUSED_PAD src0_sel:DWORD src1_sel:WORD_0\n\t"
    "s_waitcnt lgkmcnt(4)\n\t"
    "v_smfmac_f32_16x16x64_bf16 v[116:119], v[120:123], v[84:91], v124\n\t"
    "ds_read_b64 v[84:85], v100 offset:0\n\t"
    "v_mfma_f32_16x16x32_bf16 v[80:83], v[64:67], v[196:199], 0\n\t"
    "ds_read_b64 v[86:87], v126 offset:0\n\t"
    "v_mfma_f32_16x16x32_bf16 v[80:83], v[68:71], v[200:203], v[80:83]\n\t"
    "v_add_u32_sdwa v90, s92, v101 dst_sel:DWORD dst_unused:UNUSED_PAD src0_sel:DWORD src1_sel:WORD_1\n\t"
    "v_add_u32_sdwa v101, s92, v101 dst_sel:DWORD dst_unused:UNUSED_PAD src0_sel:DWORD src1_sel:WORD_0\n\t"
    "ds_read_b64 v[88:89], v101 offset:0\n\t"
    "v_mfma_f32_16x16x32_bf16 v[80:83], v[72:75], v[252:255], v[80:83]\n\t"
    "ds_read_b64 v[90:91], v90 offset:0\n\t"
    "v_mfma_f32_16x16x32_bf16 v[80:83], v[76:79], v[128:131], v[80:83]\n\t"
    "v_add_u32_sdwa v126, s92, v102 dst_sel:DWORD dst_unused:UNUSED_PAD src0_sel:DWORD src1_sel:WORD_1\n\t"
    "v_add_u32_sdwa v102, s92, v102 dst_sel:DWORD dst_unused:UNUSED_PAD src0_sel:DWORD src1_sel:WORD_0\n\t"
    "s_xor_b32 s50, s50, 0x2000\n\t"
    "s_add_u32 m0, s50, 128000\n\t"
    "s_add_u32 s63, s63, 0x4000\n\t"
    "buffer_load_dwordx4 v127, %12, s63 offen sc1 lds\n\t"
    "s_waitcnt vmcnt(3) lgkmcnt(4)\n\t"
    "v_smfmac_f32_16x16x64_bf16 v[116:119], v[120:123], v[92:99], v124\n\t"
    "ds_read_b64 v[92:93], v102 offset:0\n\t"
    "ds_read_b64 v[94:95], v126 offset:0\n\t"
    "v_add_u32_sdwa v98, s92, v103 dst_sel:DWORD dst_unused:UNUSED_PAD src0_sel:DWORD src1_sel:WORD_1\n\t"
    "v_add_u32_sdwa v103, s92, v103 dst_sel:DWORD dst_unused:UNUSED_PAD src0_sel:DWORD src1_sel:WORD_0\n\t"
    "ds_read_b64 v[96:97], v103 offset:0\n\t"
    "ds_read_b64 v[98:99], v98 offset:0\n\t"
    "s_add_u32 s90, s90, s95\n\t"
    "buffer_load_dwordx4 v[100:103], v127, %12, s90 offen offset:0 sc1\n\t"
    "v_add_u32_sdwa v126, s92, v104 dst_sel:DWORD dst_unused:UNUSED_PAD src0_sel:DWORD src1_sel:WORD_1\n\t"
    "v_cvt_pk_bf16_f32 v30, v116, v117\n\t"
    "v_cvt_pk_bf16_f32 v31, v118, v119\n\t"
    "v_and_b32 v119, s67, v37\n\t"
    "v_lshlrev_b32 v118, 16, v37\n\t"
    "v_and_b32 v117, s67, v36\n\t"
    "v_lshlrev_b32 v116, 16, v36\n\t"
    "v_add_u32_sdwa v104, s92, v104 dst_sel:DWORD dst_unused:UNUSED_PAD src0_sel:DWORD src1_sel:WORD_0\n\t"
    "s_waitcnt lgkmcnt(4)\n\t"
    "v_smfmac_f32_16x16x64_bf16 v[116:119], v[120:123], v[84:91], v124\n\t"
    "ds_read_b64 v[84:85], v104 offset:0\n\t"
    "ds_read_b64 v[86:87], v126 offset:0\n\t"
    "v_cvt_pk_bf16_f32 v80, v80, v81\n\t"
    "v_cvt_pk_bf16_f32 v81, v82, v83\n\t"
    "ds_write_b64 v125, v[80:81] offset:512\n\t"
    "v_add_u32_sdwa v90, s92, v105 dst_sel:DWORD dst_unused:UNUSED_PAD src0_sel:DWORD src1_sel:WORD_1\n\t"
    "v_add_u32_sdwa v105, s92, v105 dst_sel:DWORD dst_unused:UNUSED_PAD src0_sel:DWORD src1_sel:WORD_0\n\t"
    "ds_read_b64 v[88:89], v105 offset:0\n\t"
    "ds_read_b64 v[90:91], v90 offset:0\n\t"
    "v_add_u32_sdwa v126, s92, v106 dst_sel:DWORD dst_unused:UNUSED_PAD src0_sel:DWORD src1_sel:WORD_1\n\t"
    "v_add_u32_sdwa v106, s92, v106 dst_sel:DWORD dst_unused:UNUSED_PAD src0_sel:DWORD src1_sel:WORD_0\n\t"
    "s_waitcnt vmcnt(3) lgkmcnt(5)\n\t"
    "v_smfmac_f32_16x16x64_bf16 v[116:119], v[120:123], v[92:99], v124\n\t"
    "ds_read_b64 v[92:93], v106 offset:0\n\t"
    "ds_read_b64 v[94:95], v126 offset:0\n\t"
    "v_add_u32_sdwa v98, s92, v107 dst_sel:DWORD dst_unused:UNUSED_PAD src0_sel:DWORD src1_sel:WORD_1\n\t"
    "v_add_u32_sdwa v107, s92, v107 dst_sel:DWORD dst_unused:UNUSED_PAD src0_sel:DWORD src1_sel:WORD_0\n\t"
    "ds_read_b64 v[96:97], v107 offset:0\n\t"
    "ds_read_b64 v[98:99], v98 offset:0\n\t"
    "buffer_load_dwordx4 v[104:107], v127, %12, s90 offen offset:1024 sc1\n\t"
    "v_add_u32_sdwa v126, s92, v108 dst_sel:DWORD dst_unused:UNUSED_PAD src0_sel:DWORD src1_sel:WORD_1\n\t"
    "v_cvt_pk_bf16_f32 v36, v116, v117\n\t"
    "v_cvt_pk_bf16_f32 v37, v118, v119\n\t"
    "v_and_b32 v119, s67, v39\n\t"
    "v_lshlrev_b32 v118, 16, v39\n\t"
    "v_and_b32 v117, s67, v38\n\t"
    "v_lshlrev_b32 v116, 16, v38\n\t"
    "v_add_u32_sdwa v108, s92, v108 dst_sel:DWORD dst_unused:UNUSED_PAD src0_sel:DWORD src1_sel:WORD_0\n\t"
    "s_waitcnt lgkmcnt(4)\n\t"
    "v_smfmac_f32_16x16x64_bf16 v[116:119], v[120:123], v[84:91], v124\n\t"
    "ds_read_b64 v[84:85], v108 offset:0\n\t"
    "v_mfma_f32_16x16x32_bf16 v[80:83], v[64:67], v[204:207], 0\n\t"
    "ds_read_b64 v[86:87], v126 offset:0\n\t"
    "v_mfma_f32_16x16x32_bf16 v[80:83], v[68:71], v[208:211], v[80:83]\n\t"
    "v_add_u32_sdwa v90, s92, v109 dst_sel:DWORD dst_unused:UNUSED_PAD src0_sel:DWORD src1_sel:WORD_1\n\t"
    "v_add_u32_sdwa v109, s92, v109 dst_sel:DWORD dst_unused:UNUSED_PAD src0_sel:DWORD src1_sel:WORD_0\n\t"
    "ds_read_b64 v[88:89], v109 offset:0\n\t"
    "v_mfma_f32_16x16x32_bf16 v[80:83], v[72:75], v[132:135], v[80:83]\n\t"
    "ds_read_b64 v[90:91], v90 offset:0\n\t"
    "v_mfma_f32_16x16x32_bf16 v[80:83], v[76:79], v[136:139], v[80:83]\n\t"
    "v_add_u32_sdwa v126, s92, v110 dst_sel:DWORD dst_unused:UNUSED_PAD src0_sel:DWORD src1_sel:WORD_1\n\t"
    "v_add_u32_sdwa v110, s92, v110 dst_sel:DWORD dst_unused:UNUSED_PAD src0_sel:DWORD src1_sel:WORD_0\n\t"
    "s_cmp_lt_u32 %16, 4\n\t"
    "s_cbranch_scc1 L_tp19_%=\n\t"
    "s_setprio 0\n\t"
    "L_tp19_%=:\n\t"
    "s_waitcnt vmcnt(3) lgkmcnt(4)\n\t"
    "v_smfmac_f32_16x16x64_bf16 v[116:119], v[120:123], v[92:99], v124\n\t"
    "ds_read_b64 v[92:93], v110 offset:0\n\t"
    "ds_read_b64 v[94:95], v126 offset:0\n\t"
    "v_add_u32_sdwa v98, s92, v111 dst_sel:DWORD dst_unused:UNUSED_PAD src0_sel:DWORD src1_sel:WORD_1\n\t"
    "v_add_u32_sdwa v111, s92, v111 dst_sel:DWORD dst_unused:UNUSED_PAD src0_sel:DWORD src1_sel:WORD_0\n\t"
    "ds_read_b64 v[96:97], v111 offset:0\n\t"
    "ds_read_b64 v[98:99], v98 offset:0\n\t"
    "s_add_u32 s91, s91, s95\n\t"
    "buffer_load_dwordx4 v[108:111], v127, %12, s91 offen offset:0 sc1\n\t"
    "v_add_u32_sdwa v126, s92, v112 dst_sel:DWORD dst_unused:UNUSED_PAD src0_sel:DWORD src1_sel:WORD_1\n\t"
    "v_cvt_pk_bf16_f32 v38, v116, v117\n\t"
    "v_cvt_pk_bf16_f32 v39, v118, v119\n\t"
    "v_and_b32 v119, s67, v45\n\t"
    "v_lshlrev_b32 v118, 16, v45\n\t"
    "v_and_b32 v117, s67, v44\n\t"
    "v_lshlrev_b32 v116, 16, v44\n\t"
    "v_add_u32_sdwa v112, s92, v112 dst_sel:DWORD dst_unused:UNUSED_PAD src0_sel:DWORD src1_sel:WORD_0\n\t"
    "s_waitcnt lgkmcnt(4)\n\t"
    "v_smfmac_f32_16x16x64_bf16 v[116:119], v[120:123], v[84:91], v124\n\t"
    "ds_read_b64 v[84:85], v112 offset:0\n\t"
    "ds_read_b64 v[86:87], v126 offset:0\n\t"
    "v_cvt_pk_bf16_f32 v80, v80, v81\n\t"
    "v_cvt_pk_bf16_f32 v81, v82, v83\n\t"
    "ds_write_b64 v125, v[80:81] offset:640\n\t"
    "v_add_u32_sdwa v90, s92, v113 dst_sel:DWORD dst_unused:UNUSED_PAD src0_sel:DWORD src1_sel:WORD_1\n\t"
    "v_add_u32_sdwa v113, s92, v113 dst_sel:DWORD dst_unused:UNUSED_PAD src0_sel:DWORD src1_sel:WORD_0\n\t"
    "ds_read_b64 v[88:89], v113 offset:0\n\t"
    "ds_read_b64 v[90:91], v90 offset:0\n\t"
    "v_add_u32_sdwa v126, s92, v114 dst_sel:DWORD dst_unused:UNUSED_PAD src0_sel:DWORD src1_sel:WORD_1\n\t"
    "v_add_u32_sdwa v114, s92, v114 dst_sel:DWORD dst_unused:UNUSED_PAD src0_sel:DWORD src1_sel:WORD_0\n\t"
    "s_waitcnt vmcnt(2) lgkmcnt(5)\n\t"
    "v_smfmac_f32_16x16x64_bf16 v[116:119], v[120:123], v[92:99], v124\n\t"
    "ds_read_b64 v[92:93], v114 offset:0\n\t"
    "ds_read_b64 v[94:95], v126 offset:0\n\t"
    "v_add_u32_sdwa v98, s92, v115 dst_sel:DWORD dst_unused:UNUSED_PAD src0_sel:DWORD src1_sel:WORD_1\n\t"
    "v_add_u32_sdwa v115, s92, v115 dst_sel:DWORD dst_unused:UNUSED_PAD src0_sel:DWORD src1_sel:WORD_0\n\t"
    "ds_read_b64 v[96:97], v115 offset:0\n\t"
    "ds_read_b64 v[98:99], v98 offset:0\n\t"
    "buffer_load_dwordx4 v[112:115], v127, %12, s91 offen offset:1024 sc1\n\t"
    "v_add_u32_sdwa v126, s92, v100 dst_sel:DWORD dst_unused:UNUSED_PAD src0_sel:DWORD src1_sel:WORD_1\n\t"
    "v_cvt_pk_bf16_f32 v44, v116, v117\n\t"
    "v_cvt_pk_bf16_f32 v45, v118, v119\n\t"
    "v_and_b32 v119, s67, v47\n\t"
    "v_lshlrev_b32 v118, 16, v47\n\t"
    "v_and_b32 v117, s67, v46\n\t"
    "v_lshlrev_b32 v116, 16, v46\n\t"
    "v_add_u32_sdwa v100, s92, v100 dst_sel:DWORD dst_unused:UNUSED_PAD src0_sel:DWORD src1_sel:WORD_0\n\t"
    "s_waitcnt lgkmcnt(4)\n\t"
    "v_smfmac_f32_16x16x64_bf16 v[116:119], v[120:123], v[84:91], v124\n\t"
    "ds_read_b64 v[84:85], v100 offset:0\n\t"
    "v_mfma_f32_16x16x32_bf16 v[80:83], v[64:67], v[156:159], 0\n\t"
    "ds_read_b64 v[86:87], v126 offset:0\n\t"
    "v_mfma_f32_16x16x32_bf16 v[80:83], v[68:71], v[160:163], v[80:83]\n\t"
    "v_add_u32_sdwa v90, s92, v101 dst_sel:DWORD dst_unused:UNUSED_PAD src0_sel:DWORD src1_sel:WORD_1\n\t"
    "v_add_u32_sdwa v101, s92, v101 dst_sel:DWORD dst_unused:UNUSED_PAD src0_sel:DWORD src1_sel:WORD_0\n\t"
    "ds_read_b64 v[88:89], v101 offset:0\n\t"
    "v_mfma_f32_16x16x32_bf16 v[80:83], v[72:75], v[140:143], v[80:83]\n\t"
    "ds_read_b64 v[90:91], v90 offset:0\n\t"
    "v_mfma_f32_16x16x32_bf16 v[80:83], v[76:79], v[144:147], v[80:83]\n\t"
    "v_add_u32_sdwa v126, s92, v102 dst_sel:DWORD dst_unused:UNUSED_PAD src0_sel:DWORD src1_sel:WORD_1\n\t"
    "v_add_u32_sdwa v102, s92, v102 dst_sel:DWORD dst_unused:UNUSED_PAD src0_sel:DWORD src1_sel:WORD_0\n\t"
    "s_waitcnt vmcnt(2) lgkmcnt(4)\n\t"
    "v_smfmac_f32_16x16x64_bf16 v[116:119], v[120:123], v[92:99], v124\n\t"
    "ds_read_b64 v[92:93], v102 offset:0\n\t"
    "ds_read_b64 v[94:95], v126 offset:0\n\t"
    "v_add_u32_sdwa v98, s92, v103 dst_sel:DWORD dst_unused:UNUSED_PAD src0_sel:DWORD src1_sel:WORD_1\n\t"
    "v_add_u32_sdwa v103, s92, v103 dst_sel:DWORD dst_unused:UNUSED_PAD src0_sel:DWORD src1_sel:WORD_0\n\t"
    "ds_read_b64 v[96:97], v103 offset:0\n\t"
    "ds_read_b64 v[98:99], v98 offset:0\n\t"
    "s_add_u32 s84, s84, s94\n\t"
    "buffer_load_dwordx4 v[100:103], v127, %12, s84 offen offset:0 sc1\n\t"
    "v_add_u32_sdwa v126, s92, v104 dst_sel:DWORD dst_unused:UNUSED_PAD src0_sel:DWORD src1_sel:WORD_1\n\t"
    "v_cvt_pk_bf16_f32 v46, v116, v117\n\t"
    "v_cvt_pk_bf16_f32 v47, v118, v119\n\t"
    "v_and_b32 v119, s67, v53\n\t"
    "v_lshlrev_b32 v118, 16, v53\n\t"
    "v_and_b32 v117, s67, v52\n\t"
    "v_lshlrev_b32 v116, 16, v52\n\t"
    "v_add_u32_sdwa v104, s92, v104 dst_sel:DWORD dst_unused:UNUSED_PAD src0_sel:DWORD src1_sel:WORD_0\n\t"
    "s_waitcnt lgkmcnt(4)\n\t"
    "v_smfmac_f32_16x16x64_bf16 v[116:119], v[120:123], v[84:91], v124\n\t"
    "ds_read_b64 v[84:85], v104 offset:0\n\t"
    "ds_read_b64 v[86:87], v126 offset:0\n\t"
    "v_cvt_pk_bf16_f32 v80, v80, v81\n\t"
    "v_cvt_pk_bf16_f32 v81, v82, v83\n\t"
    "ds_write_b64 v125, v[80:81] offset:768\n\t"
    "v_add_u32_sdwa v90, s92, v105 dst_sel:DWORD dst_unused:UNUSED_PAD src0_sel:DWORD src1_sel:WORD_1\n\t"
    "v_add_u32_sdwa v105, s92, v105 dst_sel:DWORD dst_unused:UNUSED_PAD src0_sel:DWORD src1_sel:WORD_0\n\t"
    "ds_read_b64 v[88:89], v105 offset:0\n\t"
    "ds_read_b64 v[90:91], v90 offset:0\n\t"
    "v_add_u32_sdwa v126, s92, v106 dst_sel:DWORD dst_unused:UNUSED_PAD src0_sel:DWORD src1_sel:WORD_1\n\t"
    "v_add_u32_sdwa v106, s92, v106 dst_sel:DWORD dst_unused:UNUSED_PAD src0_sel:DWORD src1_sel:WORD_0\n\t"
    "s_waitcnt vmcnt(2) lgkmcnt(5)\n\t"
    "v_smfmac_f32_16x16x64_bf16 v[116:119], v[120:123], v[92:99], v124\n\t"
    "ds_read_b64 v[92:93], v106 offset:0\n\t"
    "ds_read_b64 v[94:95], v126 offset:0\n\t"
    "v_add_u32_sdwa v98, s92, v107 dst_sel:DWORD dst_unused:UNUSED_PAD src0_sel:DWORD src1_sel:WORD_1\n\t"
    "v_add_u32_sdwa v107, s92, v107 dst_sel:DWORD dst_unused:UNUSED_PAD src0_sel:DWORD src1_sel:WORD_0\n\t"
    "ds_read_b64 v[96:97], v107 offset:0\n\t"
    "ds_read_b64 v[98:99], v98 offset:0\n\t"
    "buffer_load_dwordx4 v[104:107], v127, %12, s84 offen offset:1024 sc1\n\t"
    "v_add_u32_sdwa v126, s92, v108 dst_sel:DWORD dst_unused:UNUSED_PAD src0_sel:DWORD src1_sel:WORD_1\n\t"
    "v_cvt_pk_bf16_f32 v52, v116, v117\n\t"
    "v_cvt_pk_bf16_f32 v53, v118, v119\n\t"
    "v_and_b32 v119, s67, v55\n\t"
    "v_lshlrev_b32 v118, 16, v55\n\t"
    "v_and_b32 v117, s67, v54\n\t"
    "v_lshlrev_b32 v116, 16, v54\n\t"
    "v_add_u32_sdwa v108, s92, v108 dst_sel:DWORD dst_unused:UNUSED_PAD src0_sel:DWORD src1_sel:WORD_0\n\t"
    "s_waitcnt lgkmcnt(4)\n\t"
    "v_smfmac_f32_16x16x64_bf16 v[116:119], v[120:123], v[84:91], v124\n\t"
    "ds_read_b64 v[84:85], v108 offset:0\n\t"
    "v_mfma_f32_16x16x32_bf16 v[80:83], v[64:67], v[212:215], 0\n\t"
    "ds_read_b64 v[86:87], v126 offset:0\n\t"
    "v_mfma_f32_16x16x32_bf16 v[80:83], v[68:71], v[216:219], v[80:83]\n\t"
    "v_add_u32_sdwa v90, s92, v109 dst_sel:DWORD dst_unused:UNUSED_PAD src0_sel:DWORD src1_sel:WORD_1\n\t"
    "v_add_u32_sdwa v109, s92, v109 dst_sel:DWORD dst_unused:UNUSED_PAD src0_sel:DWORD src1_sel:WORD_0\n\t"
    "ds_read_b64 v[88:89], v109 offset:0\n\t"
    "v_mfma_f32_16x16x32_bf16 v[80:83], v[72:75], v[148:151], v[80:83]\n\t"
    "ds_read_b64 v[90:91], v90 offset:0\n\t"
    "v_mfma_f32_16x16x32_bf16 v[80:83], v[76:79], v[152:155], v[80:83]\n\t"
    "v_add_u32 v79, s96, v127\n\t"
    "s_add_u32 s96, s96, 0x1000\n\t"
    "s_and_b32 s96, s96, 0x3000\n\t"
    "v_add_u32 v79, 128000, v79\n\t"
    "ds_read_b128 v[64:67], v79 offset:0\n\t"
    "ds_read_b128 v[68:71], v79 offset:1024\n\t"
    "ds_read_b128 v[72:75], v79 offset:2048\n\t"
    "ds_read_b128 v[76:79], v79 offset:3072\n\t"
    "v_add_u32_sdwa v126, s92, v110 dst_sel:DWORD dst_unused:UNUSED_PAD src0_sel:DWORD src1_sel:WORD_1\n\t"
    "v_add_u32_sdwa v110, s92, v110 dst_sel:DWORD dst_unused:UNUSED_PAD src0_sel:DWORD src1_sel:WORD_0\n\t"
    "s_waitcnt vmcnt(2) lgkmcnt(8)\n\t"
    "v_smfmac_f32_16x16x64_bf16 v[116:119], v[120:123], v[92:99], v124\n\t"
    "ds_read_b64 v[92:93], v110 offset:0\n\t"
    "ds_read_b64 v[94:95], v126 offset:0\n\t"
    "v_add_u32_sdwa v98, s92, v111 dst_sel:DWORD dst_unused:UNUSED_PAD src0_sel:DWORD src1_sel:WORD_1\n\t"
    "v_add_u32_sdwa v111, s92, v111 dst_sel:DWORD dst_unused:UNUSED_PAD src0_sel:DWORD src1_sel:WORD_0\n\t"
    "ds_read_b64 v[96:97], v111 offset:0\n\t"
    "ds_read_b64 v[98:99], v98 offset:0\n\t"
    "s_add_u32 s85, s85, s94\n\t"
    "buffer_load_dwordx4 v[108:111], v127, %12, s85 offen offset:0 sc1\n\t"
    "v_add_u32_sdwa v126, s92, v112 dst_sel:DWORD dst_unused:UNUSED_PAD src0_sel:DWORD src1_sel:WORD_1\n\t"
    "v_cvt_pk_bf16_f32 v54, v116, v117\n\t"
    "v_cvt_pk_bf16_f32 v55, v118, v119\n\t"
    "v_and_b32 v119, s67, v61\n\t"
    "v_lshlrev_b32 v118, 16, v61\n\t"
    "v_and_b32 v117, s67, v60\n\t"
    "v_lshlrev_b32 v116, 16, v60\n\t"
    "v_add_u32_sdwa v112, s92, v112 dst_sel:DWORD dst_unused:UNUSED_PAD src0_sel:DWORD src1_sel:WORD_0\n\t"
    "s_waitcnt lgkmcnt(8)\n\t"
    "v_smfmac_f32_16x16x64_bf16 v[116:119], v[120:123], v[84:91], v124\n\t"
    "ds_read_b64 v[84:85], v112 offset:0\n\t"
    "ds_read_b64 v[86:87], v126 offset:0\n\t"
    "v_cvt_pk_bf16_f32 v80, v80, v81\n\t"
    "v_cvt_pk_bf16_f32 v81, v82, v83\n\t"
    "ds_write_b64 v125, v[80:81] offset:872\n\t"
    "v_add_u32_sdwa v90, s92, v113 dst_sel:DWORD dst_unused:UNUSED_PAD src0_sel:DWORD src1_sel:WORD_1\n\t"
    "v_add_u32_sdwa v113, s92, v113 dst_sel:DWORD dst_unused:UNUSED_PAD src0_sel:DWORD src1_sel:WORD_0\n\t"
    "ds_read_b64 v[88:89], v113 offset:0\n\t"
    "ds_read_b64 v[90:91], v90 offset:0\n\t"
    "v_add_u32_sdwa v126, s92, v114 dst_sel:DWORD dst_unused:UNUSED_PAD src0_sel:DWORD src1_sel:WORD_1\n\t"
    "v_add_u32_sdwa v114, s92, v114 dst_sel:DWORD dst_unused:UNUSED_PAD src0_sel:DWORD src1_sel:WORD_0\n\t"
    "s_waitcnt vmcnt(2) lgkmcnt(5)\n\t"
    "v_smfmac_f32_16x16x64_bf16 v[116:119], v[120:123], v[92:99], v124\n\t"
    "ds_read_b64 v[92:93], v114 offset:0\n\t"
    "ds_read_b64 v[94:95], v126 offset:0\n\t"
    "v_add_u32_sdwa v98, s92, v115 dst_sel:DWORD dst_unused:UNUSED_PAD src0_sel:DWORD src1_sel:WORD_1\n\t"
    "v_add_u32_sdwa v115, s92, v115 dst_sel:DWORD dst_unused:UNUSED_PAD src0_sel:DWORD src1_sel:WORD_0\n\t"
    "ds_read_b64 v[96:97], v115 offset:0\n\t"
    "ds_read_b64 v[98:99], v98 offset:0\n\t"
    "buffer_load_dwordx4 v[112:115], v127, %12, s85 offen offset:1024 sc1\n\t"
    "v_add_u32_sdwa v126, s92, v100 dst_sel:DWORD dst_unused:UNUSED_PAD src0_sel:DWORD src1_sel:WORD_1\n\t"
    "v_cvt_pk_bf16_f32 v60, v116, v117\n\t"
    "v_cvt_pk_bf16_f32 v61, v118, v119\n\t"
    "v_and_b32 v119, s67, v63\n\t"
    "v_lshlrev_b32 v118, 16, v63\n\t"
    "v_and_b32 v117, s67, v62\n\t"
    "v_lshlrev_b32 v116, 16, v62\n\t"
    "v_add_u32_sdwa v100, s92, v100 dst_sel:DWORD dst_unused:UNUSED_PAD src0_sel:DWORD src1_sel:WORD_0\n\t"
    "s_waitcnt lgkmcnt(4)\n\t"
    "v_smfmac_f32_16x16x64_bf16 v[116:119], v[120:123], v[84:91], v124\n\t"
    "ds_read_b64 v[84:85], v100 offset:0\n\t"
    "ds_read_b64 v[86:87], v126 offset:0\n\t"
    "v_add_u32_sdwa v90, s92, v101 dst_sel:DWORD dst_unused:UNUSED_PAD src0_sel:DWORD src1_sel:WORD_1\n\t"
    "v_add_u32_sdwa v101, s92, v101 dst_sel:DWORD dst_unused:UNUSED_PAD src0_sel:DWORD src1_sel:WORD_0\n\t"
    "ds_read_b64 v[88:89], v101 offset:0\n\t"
    "ds_read_b64 v[90:91], v90 offset:0\n\t"
    "v_add_u32_sdwa v126, s92, v102 dst_sel:DWORD dst_unused:UNUSED_PAD src0_sel:DWORD src1_sel:WORD_1\n\t"
    "v_add_u32_sdwa v102, s92, v102 dst_sel:DWORD dst_unused:UNUSED_PAD src0_sel:DWORD src1_sel:WORD_0\n\t"
    "s_cmp_lt_u32 %16, 4\n\t"
    "s_cbranch_scc1 L_tp31_%=\n\t"
    "s_setprio 1\n\t"
    "L_tp31_%=:\n\t"
    "s_waitcnt vmcnt(2) lgkmcnt(4)\n\t"
    "v_smfmac_f32_16x16x64_bf16 v[116:119], v[120:123], v[92:99], v124\n\t"
    "ds_read_b64 v[92:93], v102 offset:0\n\t"
    "ds_read_b64 v[94:95], v126 offset:0\n\t"
    "v_add_u32_sdwa v98, s92, v103 dst_sel:DWORD dst_unused:UNUSED_PAD src0_sel:DWORD src1_sel:WORD_1\n\t"
    "v_add_u32_sdwa v103, s92, v103 dst_sel:DWORD dst_unused:UNUSED_PAD src0_sel:DWORD src1_sel:WORD_0\n\t"
    "ds_read_b64 v[96:97], v103 offset:0\n\t"
    "ds_read_b64 v[98:99], v98 offset:0\n\t"
    "s_add_u32 s86, s86, s94\n\t"
    "buffer_load_dwordx4 v[100:103], v127, %12, s86 offen offset:0 sc1\n\t"
    "v_add_u32_sdwa v126, s92, v104 dst_sel:DWORD dst_unused:UNUSED_PAD src0_sel:DWORD src1_sel:WORD_1\n\t"
    "v_cvt_pk_bf16_f32 v62, v116, v117\n\t"
    "v_cvt_pk_bf16_f32 v63, v118, v119\n\t"
    "v_and_b32 v119, s67, v1\n\t"
    "v_lshlrev_b32 v118, 16, v1\n\t"
    "v_and_b32 v117, s67, v0\n\t"
    "v_lshlrev_b32 v116, 16, v0\n\t"
    "v_add_u32_sdwa v104, s92, v104 dst_sel:DWORD dst_unused:UNUSED_PAD src0_sel:DWORD src1_sel:WORD_0\n\t"
    "s_waitcnt lgkmcnt(4)\n\t"
    "v_smfmac_f32_16x16x64_bf16 v[116:119], v[120:123], v[84:91], v124\n\t"
    "ds_read_b64 v[84:85], v104 offset:0\n\t"
    "v_mfma_f32_16x16x32_bf16 v[80:83], v[64:67], v[164:167], 0\n\t"
    "ds_read_b64 v[86:87], v126 offset:0\n\t"
    "v_mfma_f32_16x16x32_bf16 v[80:83], v[68:71], v[168:171], v[80:83]\n\t"
    "v_add_u32_sdwa v90, s92, v105 dst_sel:DWORD dst_unused:UNUSED_PAD src0_sel:DWORD src1_sel:WORD_1\n\t"
    "v_add_u32_sdwa v105, s92, v105 dst_sel:DWORD dst_unused:UNUSED_PAD src0_sel:DWORD src1_sel:WORD_0\n\t"
    "ds_read_b64 v[88:89], v105 offset:0\n\t"
    "v_mfma_f32_16x16x32_bf16 v[80:83], v[72:75], v[220:223], v[80:83]\n\t"
    "ds_read_b64 v[90:91], v90 offset:0\n\t"
    "v_mfma_f32_16x16x32_bf16 v[80:83], v[76:79], v[224:227], v[80:83]\n\t"
    "v_add_u32_sdwa v126, s92, v106 dst_sel:DWORD dst_unused:UNUSED_PAD src0_sel:DWORD src1_sel:WORD_1\n\t"
    "v_add_u32_sdwa v106, s92, v106 dst_sel:DWORD dst_unused:UNUSED_PAD src0_sel:DWORD src1_sel:WORD_0\n\t"
    "s_waitcnt vmcnt(2) lgkmcnt(4)\n\t"
    "v_smfmac_f32_16x16x64_bf16 v[116:119], v[120:123], v[92:99], v124\n\t"
    "ds_read_b64 v[92:93], v106 offset:0\n\t"
    "ds_read_b64 v[94:95], v126 offset:0\n\t"
    "v_add_u32_sdwa v98, s92, v107 dst_sel:DWORD dst_unused:UNUSED_PAD src0_sel:DWORD src1_sel:WORD_1\n\t"
    "v_add_u32_sdwa v107, s92, v107 dst_sel:DWORD dst_unused:UNUSED_PAD src0_sel:DWORD src1_sel:WORD_0\n\t"
    "ds_read_b64 v[96:97], v107 offset:0\n\t"
    "ds_read_b64 v[98:99], v98 offset:0\n\t"
    "buffer_load_dwordx4 v[104:107], v127, %12, s86 offen offset:1024 sc1\n\t"
    "v_add_u32_sdwa v126, s92, v108 dst_sel:DWORD dst_unused:UNUSED_PAD src0_sel:DWORD src1_sel:WORD_1\n\t"
    "v_cvt_pk_bf16_f32 v0, v116, v117\n\t"
    "v_cvt_pk_bf16_f32 v1, v118, v119\n\t"
    "v_and_b32 v119, s67, v3\n\t"
    "v_lshlrev_b32 v118, 16, v3\n\t"
    "v_and_b32 v117, s67, v2\n\t"
    "v_lshlrev_b32 v116, 16, v2\n\t"
    "v_add_u32_sdwa v108, s92, v108 dst_sel:DWORD dst_unused:UNUSED_PAD src0_sel:DWORD src1_sel:WORD_0\n\t"
    "s_waitcnt lgkmcnt(4)\n\t"
    "v_smfmac_f32_16x16x64_bf16 v[116:119], v[120:123], v[84:91], v124\n\t"
    "ds_read_b64 v[84:85], v108 offset:0\n\t"
    "ds_read_b64 v[86:87], v126 offset:0\n\t"
    "v_cvt_pk_bf16_f32 v80, v80, v81\n\t"
    "v_cvt_pk_bf16_f32 v81, v82, v83\n\t"
    "ds_write_b64 v125, v[80:81] offset:32000\n\t"
    "v_mfma_f32_16x16x32_bf16 v[80:83], v[64:67], v[172:175], 0\n\t"
    "v_add_u32_sdwa v90, s92, v109 dst_sel:DWORD dst_unused:UNUSED_PAD src0_sel:DWORD src1_sel:WORD_1\n\t"
    "v_add_u32_sdwa v109, s92, v109 dst_sel:DWORD dst_unused:UNUSED_PAD src0_sel:DWORD src1_sel:WORD_0\n\t"
    "ds_read_b64 v[88:89], v109 offset:0\n\t"
    "v_mfma_f32_16x16x32_bf16 v[80:83], v[68:71], v[176:179], v[80:83]\n\t"
    "ds_read_b64 v[90:91], v90 offset:0\n\t"
    "v_mfma_f32_16x16x32_bf16 v[80:83], v[72:75], v[228:231], v[80:83]\n\t"
    "v_mfma_f32_16x16x32_bf16 v[80:83], v[76:79], v[232:235], v[80:83]\n\t"
    "v_add_u32_sdwa v126, s92, v110 dst_sel:DWORD dst_unused:UNUSED_PAD src0_sel:DWORD src1_sel:WORD_1\n\t"
    "v_add_u32_sdwa v110, s92, v110 dst_sel:DWORD dst_unused:UNUSED_PAD src0_sel:DWORD src1_sel:WORD_0\n\t"
    "s_waitcnt vmcnt(2) lgkmcnt(5)\n\t"
    "v_smfmac_f32_16x16x64_bf16 v[116:119], v[120:123], v[92:99], v124\n\t"
    "ds_read_b64 v[92:93], v110 offset:0\n\t"
    "ds_read_b64 v[94:95], v126 offset:0\n\t"
    "v_add_u32_sdwa v98, s92, v111 dst_sel:DWORD dst_unused:UNUSED_PAD src0_sel:DWORD src1_sel:WORD_1\n\t"
    "v_add_u32_sdwa v111, s92, v111 dst_sel:DWORD dst_unused:UNUSED_PAD src0_sel:DWORD src1_sel:WORD_0\n\t"
    "ds_read_b64 v[96:97], v111 offset:0\n\t"
    "ds_read_b64 v[98:99], v98 offset:0\n\t"
    "s_add_u32 s87, s87, s94\n\t"
    "buffer_load_dwordx4 v[108:111], v127, %12, s87 offen offset:0 sc1\n\t"
    "v_add_u32_sdwa v126, s92, v112 dst_sel:DWORD dst_unused:UNUSED_PAD src0_sel:DWORD src1_sel:WORD_1\n\t"
    "v_cvt_pk_bf16_f32 v2, v116, v117\n\t"
    "v_cvt_pk_bf16_f32 v3, v118, v119\n\t"
    "v_and_b32 v119, s67, v9\n\t"
    "v_lshlrev_b32 v118, 16, v9\n\t"
    "v_and_b32 v117, s67, v8\n\t"
    "v_lshlrev_b32 v116, 16, v8\n\t"
    "v_add_u32_sdwa v112, s92, v112 dst_sel:DWORD dst_unused:UNUSED_PAD src0_sel:DWORD src1_sel:WORD_0\n\t"
    "s_waitcnt lgkmcnt(4)\n\t"
    "v_smfmac_f32_16x16x64_bf16 v[116:119], v[120:123], v[84:91], v124\n\t"
    "ds_read_b64 v[84:85], v112 offset:0\n\t"
    "ds_read_b64 v[86:87], v126 offset:0\n\t"
    "v_cvt_pk_bf16_f32 v80, v80, v81\n\t"
    "v_cvt_pk_bf16_f32 v81, v82, v83\n\t"
    "ds_write_b64 v125, v[80:81] offset:32128\n\t"
    "v_add_u32_sdwa v90, s92, v113 dst_sel:DWORD dst_unused:UNUSED_PAD src0_sel:DWORD src1_sel:WORD_1\n\t"
    "v_add_u32_sdwa v113, s92, v113 dst_sel:DWORD dst_unused:UNUSED_PAD src0_sel:DWORD src1_sel:WORD_0\n\t"
    "ds_read_b64 v[88:89], v113 offset:0\n\t"
    "ds_read_b64 v[90:91], v90 offset:0\n\t"
    "v_add_u32_sdwa v126, s92, v114 dst_sel:DWORD dst_unused:UNUSED_PAD src0_sel:DWORD src1_sel:WORD_1\n\t"
    "v_add_u32_sdwa v114, s92, v114 dst_sel:DWORD dst_unused:UNUSED_PAD src0_sel:DWORD src1_sel:WORD_0\n\t"
    "s_waitcnt vmcnt(2) lgkmcnt(5)\n\t"
    "v_smfmac_f32_16x16x64_bf16 v[116:119], v[120:123], v[92:99], v124\n\t"
    "ds_read_b64 v[92:93], v114 offset:0\n\t"
    "ds_read_b64 v[94:95], v126 offset:0\n\t"
    "v_add_u32_sdwa v98, s92, v115 dst_sel:DWORD dst_unused:UNUSED_PAD src0_sel:DWORD src1_sel:WORD_1\n\t"
    "v_add_u32_sdwa v115, s92, v115 dst_sel:DWORD dst_unused:UNUSED_PAD src0_sel:DWORD src1_sel:WORD_0\n\t"
    "ds_read_b64 v[96:97], v115 offset:0\n\t"
    "ds_read_b64 v[98:99], v98 offset:0\n\t"
    "buffer_load_dwordx4 v[112:115], v127, %12, s87 offen offset:1024 sc1\n\t"
    "v_add_u32_sdwa v126, s92, v100 dst_sel:DWORD dst_unused:UNUSED_PAD src0_sel:DWORD src1_sel:WORD_1\n\t"
    "v_cvt_pk_bf16_f32 v8, v116, v117\n\t"
    "v_cvt_pk_bf16_f32 v9, v118, v119\n\t"
    "v_and_b32 v119, s67, v11\n\t"
    "v_lshlrev_b32 v118, 16, v11\n\t"
    "v_and_b32 v117, s67, v10\n\t"
    "v_lshlrev_b32 v116, 16, v10\n\t"
    "v_add_u32_sdwa v100, s92, v100 dst_sel:DWORD dst_unused:UNUSED_PAD src0_sel:DWORD src1_sel:WORD_0\n\t"
    "s_waitcnt lgkmcnt(4)\n\t"
    "v_smfmac_f32_16x16x64_bf16 v[116:119], v[120:123], v[84:91], v124\n\t"
    "ds_read_b64 v[84:85], v100 offset:0\n\t"
    "v_mfma_f32_16x16x32_bf16 v[80:83], v[64:67], v[180:183], 0\n\t"
    "ds_read_b64 v[86:87], v126 offset:0\n\t"
    "v_mfma_f32_16x16x32_bf16 v[80:83], v[68:71], v[184:187], v[80:83]\n\t"
    "v_add_u32_sdwa v90, s92, v101 dst_sel:DWORD dst_unused:UNUSED_PAD src0_sel:DWORD src1_sel:WORD_1\n\t"
    "v_add_u32_sdwa v101, s92, v101 dst_sel:DWORD dst_unused:UNUSED_PAD src0_sel:DWORD src1_sel:WORD_0\n\t"
    "ds_read_b64 v[88:89], v101 offset:0\n\t"
    "v_mfma_f32_16x16x32_bf16 v[80:83], v[72:75], v[236:239], v[80:83]\n\t"
    "ds_read_b64 v[90:91], v90 offset:0\n\t"
    "v_mfma_f32_16x16x32_bf16 v[80:83], v[76:79], v[240:243], v[80:83]\n\t"
    "v_add_u32_sdwa v126, s92, v102 dst_sel:DWORD dst_unused:UNUSED_PAD src0_sel:DWORD src1_sel:WORD_1\n\t"
    "v_add_u32_sdwa v102, s92, v102 dst_sel:DWORD dst_unused:UNUSED_PAD src0_sel:DWORD src1_sel:WORD_0\n\t"
    "s_waitcnt vmcnt(2) lgkmcnt(4)\n\t"
    "v_smfmac_f32_16x16x64_bf16 v[116:119], v[120:123], v[92:99], v124\n\t"
    "ds_read_b64 v[92:93], v102 offset:0\n\t"
    "ds_read_b64 v[94:95], v126 offset:0\n\t"
    "v_add_u32_sdwa v98, s92, v103 dst_sel:DWORD dst_unused:UNUSED_PAD src0_sel:DWORD src1_sel:WORD_1\n\t"
    "v_add_u32_sdwa v103, s92, v103 dst_sel:DWORD dst_unused:UNUSED_PAD src0_sel:DWORD src1_sel:WORD_0\n\t"
    "ds_read_b64 v[96:97], v103 offset:0\n\t"
    "ds_read_b64 v[98:99], v98 offset:0\n\t"
    "s_add_u32 s88, s88, s94\n\t"
    "buffer_load_dwordx4 v[100:103], v127, %12, s88 offen offset:0 sc1\n\t"
    "v_add_u32_sdwa v126, s92, v104 dst_sel:DWORD dst_unused:UNUSED_PAD src0_sel:DWORD src1_sel:WORD_1\n\t"
    "v_cvt_pk_bf16_f32 v10, v116, v117\n\t"
    "v_cvt_pk_bf16_f32 v11, v118, v119\n\t"
    "v_and_b32 v119, s67, v17\n\t"
    "v_lshlrev_b32 v118, 16, v17\n\t"
    "v_and_b32 v117, s67, v16\n\t"
    "v_lshlrev_b32 v116, 16, v16\n\t"
    "v_add_u32_sdwa v104, s92, v104 dst_sel:DWORD dst_unused:UNUSED_PAD src0_sel:DWORD src1_sel:WORD_0\n\t"
    "s_waitcnt lgkmcnt(4)\n\t"
    "v_smfmac_f32_16x16x64_bf16 v[116:119], v[120:123], v[84:91], v124\n\t"
    "ds_read_b64 v[84:85], v104 offset:0\n\t"
    "ds_read_b64 v[86:87], v126 offset:0\n\t"
    "v_cvt_pk_bf16_f32 v80, v80, v81\n\t"
    "v_cvt_pk_bf16_f32 v81, v82, v83\n\t"
    "ds_write_b64 v125, v[80:81] offset:32256\n\t"
    "v_add_u32_sdwa v90, s92, v105 dst_sel:DWORD dst_unused:UNUSED_PAD src0_sel:DWORD src1_sel:WORD_1\n\t"
    "v_add_u32_sdwa v105, s92, v105 dst_sel:DWORD dst_unused:UNUSED_PAD src0_sel:DWORD src1_sel:WORD_0\n\t"
    "ds_read_b64 v[88:89], v105 offset:0\n\t"
    "ds_read_b64 v[90:91], v90 offset:0\n\t"
    "v_add_u32_sdwa v126, s92, v106 dst_sel:DWORD dst_unused:UNUSED_PAD src0_sel:DWORD src1_sel:WORD_1\n\t"
    "v_add_u32_sdwa v106, s92, v106 dst_sel:DWORD dst_unused:UNUSED_PAD src0_sel:DWORD src1_sel:WORD_0\n\t"
    "s_waitcnt vmcnt(2) lgkmcnt(5)\n\t"
    "v_smfmac_f32_16x16x64_bf16 v[116:119], v[120:123], v[92:99], v124\n\t"
    "ds_read_b64 v[92:93], v106 offset:0\n\t"
    "ds_read_b64 v[94:95], v126 offset:0\n\t"
    "v_add_u32_sdwa v98, s92, v107 dst_sel:DWORD dst_unused:UNUSED_PAD src0_sel:DWORD src1_sel:WORD_1\n\t"
    "v_add_u32_sdwa v107, s92, v107 dst_sel:DWORD dst_unused:UNUSED_PAD src0_sel:DWORD src1_sel:WORD_0\n\t"
    "ds_read_b64 v[96:97], v107 offset:0\n\t"
    "ds_read_b64 v[98:99], v98 offset:0\n\t"
    "buffer_load_dwordx4 v[104:107], v127, %12, s88 offen offset:1024 sc1\n\t"
    "v_add_u32_sdwa v126, s92, v108 dst_sel:DWORD dst_unused:UNUSED_PAD src0_sel:DWORD src1_sel:WORD_1\n\t"
    "v_cvt_pk_bf16_f32 v16, v116, v117\n\t"
    "v_cvt_pk_bf16_f32 v17, v118, v119\n\t"
    "v_and_b32 v119, s67, v19\n\t"
    "v_lshlrev_b32 v118, 16, v19\n\t"
    "v_and_b32 v117, s67, v18\n\t"
    "v_lshlrev_b32 v116, 16, v18\n\t"
    "v_add_u32_sdwa v108, s92, v108 dst_sel:DWORD dst_unused:UNUSED_PAD src0_sel:DWORD src1_sel:WORD_0\n\t"
    "s_waitcnt lgkmcnt(4)\n\t"
    "v_smfmac_f32_16x16x64_bf16 v[116:119], v[120:123], v[84:91], v124\n\t"
    "ds_read_b64 v[84:85], v108 offset:0\n\t"
    "v_mfma_f32_16x16x32_bf16 v[80:83], v[64:67], v[188:191], 0\n\t"
    "ds_read_b64 v[86:87], v126 offset:0\n\t"
    "v_mfma_f32_16x16x32_bf16 v[80:83], v[68:71], v[192:195], v[80:83]\n\t"
    "v_add_u32_sdwa v90, s92, v109 dst_sel:DWORD dst_unused:UNUSED_PAD src0_sel:DWORD src1_sel:WORD_1\n\t"
    "v_add_u32_sdwa v109, s92, v109 dst_sel:DWORD dst_unused:UNUSED_PAD src0_sel:DWORD src1_sel:WORD_0\n\t"
    "ds_read_b64 v[88:89], v109 offset:0\n\t"
    "v_mfma_f32_16x16x32_bf16 v[80:83], v[72:75], v[244:247], v[80:83]\n\t"
    "ds_read_b64 v[90:91], v90 offset:0\n\t"
    "v_mfma_f32_16x16x32_bf16 v[80:83], v[76:79], v[248:251], v[80:83]\n\t"
    "v_add_u32_sdwa v126, s92, v110 dst_sel:DWORD dst_unused:UNUSED_PAD src0_sel:DWORD src1_sel:WORD_1\n\t"
    "v_add_u32_sdwa v110, s92, v110 dst_sel:DWORD dst_unused:UNUSED_PAD src0_sel:DWORD src1_sel:WORD_0\n\t"
    "s_waitcnt vmcnt(2) lgkmcnt(4)\n\t"
    "v_smfmac_f32_16x16x64_bf16 v[116:119], v[120:123], v[92:99], v124\n\t"
    "ds_read_b64 v[92:93], v110 offset:0\n\t"
    "ds_read_b64 v[94:95], v126 offset:0\n\t"
    "v_add_u32_sdwa v98, s92, v111 dst_sel:DWORD dst_unused:UNUSED_PAD src0_sel:DWORD src1_sel:WORD_1\n\t"
    "v_add_u32_sdwa v111, s92, v111 dst_sel:DWORD dst_unused:UNUSED_PAD src0_sel:DWORD src1_sel:WORD_0\n\t"
    "ds_read_b64 v[96:97], v111 offset:0\n\t"
    "ds_read_b64 v[98:99], v98 offset:0\n\t"
    "s_add_u32 s89, s89, s94\n\t"
    "buffer_load_dwordx4 v[108:111], v127, %12, s89 offen offset:0 sc1\n\t"
    "v_add_u32_sdwa v126, s92, v112 dst_sel:DWORD dst_unused:UNUSED_PAD src0_sel:DWORD src1_sel:WORD_1\n\t"
    "v_cvt_pk_bf16_f32 v18, v116, v117\n\t"
    "v_cvt_pk_bf16_f32 v19, v118, v119\n\t"
    "v_and_b32 v119, s67, v25\n\t"
    "v_lshlrev_b32 v118, 16, v25\n\t"
    "v_and_b32 v117, s67, v24\n\t"
    "v_lshlrev_b32 v116, 16, v24\n\t"
    "v_add_u32_sdwa v112, s92, v112 dst_sel:DWORD dst_unused:UNUSED_PAD src0_sel:DWORD src1_sel:WORD_0\n\t"
    "s_waitcnt lgkmcnt(4)\n\t"
    "v_smfmac_f32_16x16x64_bf16 v[116:119], v[120:123], v[84:91], v124\n\t"
    "ds_read_b64 v[84:85], v112 offset:0\n\t"
    "ds_read_b64 v[86:87], v126 offset:0\n\t"
    "v_cvt_pk_bf16_f32 v80, v80, v81\n\t"
    "v_cvt_pk_bf16_f32 v81, v82, v83\n\t"
    "ds_write_b64 v125, v[80:81] offset:32384\n\t"
    "v_add_u32_sdwa v90, s92, v113 dst_sel:DWORD dst_unused:UNUSED_PAD src0_sel:DWORD src1_sel:WORD_1\n\t"
    "v_add_u32_sdwa v113, s92, v113 dst_sel:DWORD dst_unused:UNUSED_PAD src0_sel:DWORD src1_sel:WORD_0\n\t"
    "ds_read_b64 v[88:89], v113 offset:0\n\t"
    "ds_read_b64 v[90:91], v90 offset:0\n\t"
    "v_add_u32_sdwa v126, s92, v114 dst_sel:DWORD dst_unused:UNUSED_PAD src0_sel:DWORD src1_sel:WORD_1\n\t"
    "v_add_u32_sdwa v114, s92, v114 dst_sel:DWORD dst_unused:UNUSED_PAD src0_sel:DWORD src1_sel:WORD_0\n\t"
    "s_waitcnt vmcnt(2) lgkmcnt(5)\n\t"
    "v_smfmac_f32_16x16x64_bf16 v[116:119], v[120:123], v[92:99], v124\n\t"
    "ds_read_b64 v[92:93], v114 offset:0\n\t"
    "ds_read_b64 v[94:95], v126 offset:0\n\t"
    "v_add_u32_sdwa v98, s92, v115 dst_sel:DWORD dst_unused:UNUSED_PAD src0_sel:DWORD src1_sel:WORD_1\n\t"
    "v_add_u32_sdwa v115, s92, v115 dst_sel:DWORD dst_unused:UNUSED_PAD src0_sel:DWORD src1_sel:WORD_0\n\t"
    "ds_read_b64 v[96:97], v115 offset:0\n\t"
    "ds_read_b64 v[98:99], v98 offset:0\n\t"
    "buffer_load_dwordx4 v[112:115], v127, %12, s89 offen offset:1024 sc1\n\t"
    "v_add_u32_sdwa v126, s92, v100 dst_sel:DWORD dst_unused:UNUSED_PAD src0_sel:DWORD src1_sel:WORD_1\n\t"
    "v_cvt_pk_bf16_f32 v24, v116, v117\n\t"
    "v_cvt_pk_bf16_f32 v25, v118, v119\n\t"
    "v_and_b32 v119, s67, v27\n\t"
    "v_lshlrev_b32 v118, 16, v27\n\t"
    "v_and_b32 v117, s67, v26\n\t"
    "v_lshlrev_b32 v116, 16, v26\n\t"
    "v_add_u32_sdwa v100, s92, v100 dst_sel:DWORD dst_unused:UNUSED_PAD src0_sel:DWORD src1_sel:WORD_0\n\t"
    "s_waitcnt lgkmcnt(4)\n\t"
    "v_smfmac_f32_16x16x64_bf16 v[116:119], v[120:123], v[84:91], v124\n\t"
    "ds_read_b64 v[84:85], v100 offset:0\n\t"
    "v_mfma_f32_16x16x32_bf16 v[80:83], v[64:67], v[196:199], 0\n\t"
    "ds_read_b64 v[86:87], v126 offset:0\n\t"
    "v_mfma_f32_16x16x32_bf16 v[80:83], v[68:71], v[200:203], v[80:83]\n\t"
    "v_add_u32_sdwa v90, s92, v101 dst_sel:DWORD dst_unused:UNUSED_PAD src0_sel:DWORD src1_sel:WORD_1\n\t"
    "v_add_u32_sdwa v101, s92, v101 dst_sel:DWORD dst_unused:UNUSED_PAD src0_sel:DWORD src1_sel:WORD_0\n\t"
    "ds_read_b64 v[88:89], v101 offset:0\n\t"
    "v_mfma_f32_16x16x32_bf16 v[80:83], v[72:75], v[252:255], v[80:83]\n\t"
    "ds_read_b64 v[90:91], v90 offset:0\n\t"
    "v_mfma_f32_16x16x32_bf16 v[80:83], v[76:79], v[128:131], v[80:83]\n\t"
    "v_add_u32_sdwa v126, s92, v102 dst_sel:DWORD dst_unused:UNUSED_PAD src0_sel:DWORD src1_sel:WORD_1\n\t"
    "v_add_u32_sdwa v102, s92, v102 dst_sel:DWORD dst_unused:UNUSED_PAD src0_sel:DWORD src1_sel:WORD_0\n\t"
    "s_waitcnt vmcnt(2) lgkmcnt(4)\n\t"
    "v_smfmac_f32_16x16x64_bf16 v[116:119], v[120:123], v[92:99], v124\n\t"
    "ds_read_b64 v[92:93], v102 offset:0\n\t"
    "ds_read_b64 v[94:95], v126 offset:0\n\t"
    "v_add_u32_sdwa v98, s92, v103 dst_sel:DWORD dst_unused:UNUSED_PAD src0_sel:DWORD src1_sel:WORD_1\n\t"
    "v_add_u32_sdwa v103, s92, v103 dst_sel:DWORD dst_unused:UNUSED_PAD src0_sel:DWORD src1_sel:WORD_0\n\t"
    "ds_read_b64 v[96:97], v103 offset:0\n\t"
    "ds_read_b64 v[98:99], v98 offset:0\n\t"
    "s_add_u32 s90, s90, s94\n\t"
    "buffer_load_dwordx4 v[100:103], v127, %12, s90 offen offset:0 sc1\n\t"
    "v_add_u32_sdwa v126, s92, v104 dst_sel:DWORD dst_unused:UNUSED_PAD src0_sel:DWORD src1_sel:WORD_1\n\t"
    "v_cvt_pk_bf16_f32 v26, v116, v117\n\t"
    "v_cvt_pk_bf16_f32 v27, v118, v119\n\t"
    "v_and_b32 v119, s67, v33\n\t"
    "v_lshlrev_b32 v118, 16, v33\n\t"
    "v_and_b32 v117, s67, v32\n\t"
    "v_lshlrev_b32 v116, 16, v32\n\t"
    "v_add_u32_sdwa v104, s92, v104 dst_sel:DWORD dst_unused:UNUSED_PAD src0_sel:DWORD src1_sel:WORD_0\n\t"
    "s_waitcnt lgkmcnt(4)\n\t"
    "v_smfmac_f32_16x16x64_bf16 v[116:119], v[120:123], v[84:91], v124\n\t"
    "ds_read_b64 v[84:85], v104 offset:0\n\t"
    "ds_read_b64 v[86:87], v126 offset:0\n\t"
    "v_cvt_pk_bf16_f32 v80, v80, v81\n\t"
    "v_cvt_pk_bf16_f32 v81, v82, v83\n\t"
    "ds_write_b64 v125, v[80:81] offset:32512\n\t"
    "v_add_u32_sdwa v90, s92, v105 dst_sel:DWORD dst_unused:UNUSED_PAD src0_sel:DWORD src1_sel:WORD_1\n\t"
    "v_add_u32_sdwa v105, s92, v105 dst_sel:DWORD dst_unused:UNUSED_PAD src0_sel:DWORD src1_sel:WORD_0\n\t"
    "ds_read_b64 v[88:89], v105 offset:0\n\t"
    "ds_read_b64 v[90:91], v90 offset:0\n\t"
    "v_add_u32_sdwa v126, s92, v106 dst_sel:DWORD dst_unused:UNUSED_PAD src0_sel:DWORD src1_sel:WORD_1\n\t"
    "v_add_u32_sdwa v106, s92, v106 dst_sel:DWORD dst_unused:UNUSED_PAD src0_sel:DWORD src1_sel:WORD_0\n\t"
    "s_waitcnt vmcnt(2) lgkmcnt(5)\n\t"
    "v_smfmac_f32_16x16x64_bf16 v[116:119], v[120:123], v[92:99], v124\n\t"
    "ds_read_b64 v[92:93], v106 offset:0\n\t"
    "ds_read_b64 v[94:95], v126 offset:0\n\t"
    "v_add_u32_sdwa v98, s92, v107 dst_sel:DWORD dst_unused:UNUSED_PAD src0_sel:DWORD src1_sel:WORD_1\n\t"
    "v_add_u32_sdwa v107, s92, v107 dst_sel:DWORD dst_unused:UNUSED_PAD src0_sel:DWORD src1_sel:WORD_0\n\t"
    "ds_read_b64 v[96:97], v107 offset:0\n\t"
    "ds_read_b64 v[98:99], v98 offset:0\n\t"
    "buffer_load_dwordx4 v[104:107], v127, %12, s90 offen offset:1024 sc1\n\t"
    "v_add_u32_sdwa v126, s92, v108 dst_sel:DWORD dst_unused:UNUSED_PAD src0_sel:DWORD src1_sel:WORD_1\n\t"
    "v_cvt_pk_bf16_f32 v32, v116, v117\n\t"
    "v_cvt_pk_bf16_f32 v33, v118, v119\n\t"
    "v_and_b32 v119, s67, v35\n\t"
    "v_lshlrev_b32 v118, 16, v35\n\t"
    "v_and_b32 v117, s67, v34\n\t"
    "v_lshlrev_b32 v116, 16, v34\n\t"
    "v_add_u32_sdwa v108, s92, v108 dst_sel:DWORD dst_unused:UNUSED_PAD src0_sel:DWORD src1_sel:WORD_0\n\t"
    "s_waitcnt lgkmcnt(4)\n\t"
    "v_smfmac_f32_16x16x64_bf16 v[116:119], v[120:123], v[84:91], v124\n\t"
    "ds_read_b64 v[84:85], v108 offset:0\n\t"
    "v_mfma_f32_16x16x32_bf16 v[80:83], v[64:67], v[204:207], 0\n\t"
    "ds_read_b64 v[86:87], v126 offset:0\n\t"
    "v_mfma_f32_16x16x32_bf16 v[80:83], v[68:71], v[208:211], v[80:83]\n\t"
    "v_add_u32_sdwa v90, s92, v109 dst_sel:DWORD dst_unused:UNUSED_PAD src0_sel:DWORD src1_sel:WORD_1\n\t"
    "v_add_u32_sdwa v109, s92, v109 dst_sel:DWORD dst_unused:UNUSED_PAD src0_sel:DWORD src1_sel:WORD_0\n\t"
    "ds_read_b64 v[88:89], v109 offset:0\n\t"
    "v_mfma_f32_16x16x32_bf16 v[80:83], v[72:75], v[132:135], v[80:83]\n\t"
    "ds_read_b64 v[90:91], v90 offset:0\n\t"
    "v_mfma_f32_16x16x32_bf16 v[80:83], v[76:79], v[136:139], v[80:83]\n\t"
    "v_add_u32_sdwa v126, s92, v110 dst_sel:DWORD dst_unused:UNUSED_PAD src0_sel:DWORD src1_sel:WORD_1\n\t"
    "v_add_u32_sdwa v110, s92, v110 dst_sel:DWORD dst_unused:UNUSED_PAD src0_sel:DWORD src1_sel:WORD_0\n\t"
    "s_cmp_lt_u32 %16, 4\n\t"
    "s_cbranch_scc1 L_tp51_%=\n\t"
    "s_setprio 0\n\t"
    "L_tp51_%=:\n\t"
    "s_waitcnt vmcnt(2) lgkmcnt(4)\n\t"
    "v_smfmac_f32_16x16x64_bf16 v[116:119], v[120:123], v[92:99], v124\n\t"
    "ds_read_b64 v[92:93], v110 offset:0\n\t"
    "ds_read_b64 v[94:95], v126 offset:0\n\t"
    "v_add_u32_sdwa v98, s92, v111 dst_sel:DWORD dst_unused:UNUSED_PAD src0_sel:DWORD src1_sel:WORD_1\n\t"
    "v_add_u32_sdwa v111, s92, v111 dst_sel:DWORD dst_unused:UNUSED_PAD src0_sel:DWORD src1_sel:WORD_0\n\t"
    "ds_read_b64 v[96:97], v111 offset:0\n\t"
    "ds_read_b64 v[98:99], v98 offset:0\n\t"
    "s_add_u32 s91, s91, s94\n\t"
    "buffer_load_dwordx4 v[108:111], v127, %12, s91 offen offset:0 sc1\n\t"
    "v_add_u32_sdwa v126, s92, v112 dst_sel:DWORD dst_unused:UNUSED_PAD src0_sel:DWORD src1_sel:WORD_1\n\t"
    "v_cvt_pk_bf16_f32 v34, v116, v117\n\t"
    "v_cvt_pk_bf16_f32 v35, v118, v119\n\t"
    "v_and_b32 v119, s67, v41\n\t"
    "v_lshlrev_b32 v118, 16, v41\n\t"
    "v_and_b32 v117, s67, v40\n\t"
    "v_lshlrev_b32 v116, 16, v40\n\t"
    "v_add_u32_sdwa v112, s92, v112 dst_sel:DWORD dst_unused:UNUSED_PAD src0_sel:DWORD src1_sel:WORD_0\n\t"
    "s_waitcnt lgkmcnt(4)\n\t"
    "v_smfmac_f32_16x16x64_bf16 v[116:119], v[120:123], v[84:91], v124\n\t"
    "ds_read_b64 v[84:85], v112 offset:0\n\t"
    "ds_read_b64 v[86:87], v126 offset:0\n\t"
    "v_cvt_pk_bf16_f32 v80, v80, v81\n\t"
    "v_cvt_pk_bf16_f32 v81, v82, v83\n\t"
    "ds_write_b64 v125, v[80:81] offset:32640\n\t"
    "v_add_u32_sdwa v90, s92, v113 dst_sel:DWORD dst_unused:UNUSED_PAD src0_sel:DWORD src1_sel:WORD_1\n\t"
    "v_add_u32_sdwa v113, s92, v113 dst_sel:DWORD dst_unused:UNUSED_PAD src0_sel:DWORD src1_sel:WORD_0\n\t"
    "ds_read_b64 v[88:89], v113 offset:0\n\t"
    "ds_read_b64 v[90:91], v90 offset:0\n\t"
    "v_add_u32_sdwa v126, s92, v114 dst_sel:DWORD dst_unused:UNUSED_PAD src0_sel:DWORD src1_sel:WORD_1\n\t"
    "v_add_u32_sdwa v114, s92, v114 dst_sel:DWORD dst_unused:UNUSED_PAD src0_sel:DWORD src1_sel:WORD_0\n\t"
    "s_waitcnt vmcnt(2) lgkmcnt(5)\n\t"
    "v_smfmac_f32_16x16x64_bf16 v[116:119], v[120:123], v[92:99], v124\n\t"
    "ds_read_b64 v[92:93], v114 offset:0\n\t"
    "ds_read_b64 v[94:95], v126 offset:0\n\t"
    "v_add_u32_sdwa v98, s92, v115 dst_sel:DWORD dst_unused:UNUSED_PAD src0_sel:DWORD src1_sel:WORD_1\n\t"
    "v_add_u32_sdwa v115, s92, v115 dst_sel:DWORD dst_unused:UNUSED_PAD src0_sel:DWORD src1_sel:WORD_0\n\t"
    "ds_read_b64 v[96:97], v115 offset:0\n\t"
    "ds_read_b64 v[98:99], v98 offset:0\n\t"
    "buffer_load_dwordx4 v[112:115], v127, %12, s91 offen offset:1024 sc1\n\t"
    "v_add_u32_sdwa v126, s92, v100 dst_sel:DWORD dst_unused:UNUSED_PAD src0_sel:DWORD src1_sel:WORD_1\n\t"
    "v_cvt_pk_bf16_f32 v40, v116, v117\n\t"
    "v_cvt_pk_bf16_f32 v41, v118, v119\n\t"
    "v_and_b32 v119, s67, v43\n\t"
    "v_lshlrev_b32 v118, 16, v43\n\t"
    "v_and_b32 v117, s67, v42\n\t"
    "v_lshlrev_b32 v116, 16, v42\n\t"
    "v_add_u32_sdwa v100, s92, v100 dst_sel:DWORD dst_unused:UNUSED_PAD src0_sel:DWORD src1_sel:WORD_0\n\t"
    "s_waitcnt lgkmcnt(4)\n\t"
    "v_smfmac_f32_16x16x64_bf16 v[116:119], v[120:123], v[84:91], v124\n\t"
    "ds_read_b64 v[84:85], v100 offset:0\n\t"
    "v_mfma_f32_16x16x32_bf16 v[80:83], v[64:67], v[156:159], 0\n\t"
    "ds_read_b64 v[86:87], v126 offset:0\n\t"
    "v_mfma_f32_16x16x32_bf16 v[80:83], v[68:71], v[160:163], v[80:83]\n\t"
    "v_add_u32_sdwa v90, s92, v101 dst_sel:DWORD dst_unused:UNUSED_PAD src0_sel:DWORD src1_sel:WORD_1\n\t"
    "v_add_u32_sdwa v101, s92, v101 dst_sel:DWORD dst_unused:UNUSED_PAD src0_sel:DWORD src1_sel:WORD_0\n\t"
    "ds_read_b64 v[88:89], v101 offset:0\n\t"
    "v_mfma_f32_16x16x32_bf16 v[80:83], v[72:75], v[140:143], v[80:83]\n\t"
    "ds_read_b64 v[90:91], v90 offset:0\n\t"
    "v_mfma_f32_16x16x32_bf16 v[80:83], v[76:79], v[144:147], v[80:83]\n\t"
    "v_add_u32_sdwa v126, s92, v102 dst_sel:DWORD dst_unused:UNUSED_PAD src0_sel:DWORD src1_sel:WORD_1\n\t"
    "v_add_u32_sdwa v102, s92, v102 dst_sel:DWORD dst_unused:UNUSED_PAD src0_sel:DWORD src1_sel:WORD_0\n\t"
    "s_waitcnt vmcnt(2) lgkmcnt(4)\n\t"
    "v_smfmac_f32_16x16x64_bf16 v[116:119], v[120:123], v[92:99], v124\n\t"
    "ds_read_b64 v[92:93], v102 offset:0\n\t"
    "ds_read_b64 v[94:95], v126 offset:0\n\t"
    "v_add_u32_sdwa v98, s92, v103 dst_sel:DWORD dst_unused:UNUSED_PAD src0_sel:DWORD src1_sel:WORD_1\n\t"
    "v_add_u32_sdwa v103, s92, v103 dst_sel:DWORD dst_unused:UNUSED_PAD src0_sel:DWORD src1_sel:WORD_0\n\t"
    "ds_read_b64 v[96:97], v103 offset:0\n\t"
    "ds_read_b64 v[98:99], v98 offset:0\n\t"
    "s_add_u32 s84, s84, s95\n\t"
    "buffer_load_dwordx4 v[100:103], v127, %12, s84 offen offset:0 sc1\n\t"
    "v_add_u32_sdwa v126, s92, v104 dst_sel:DWORD dst_unused:UNUSED_PAD src0_sel:DWORD src1_sel:WORD_1\n\t"
    "v_cvt_pk_bf16_f32 v42, v116, v117\n\t"
    "v_cvt_pk_bf16_f32 v43, v118, v119\n\t"
    "v_and_b32 v119, s67, v49\n\t"
    "v_lshlrev_b32 v118, 16, v49\n\t"
    "v_and_b32 v117, s67, v48\n\t"
    "v_lshlrev_b32 v116, 16, v48\n\t"
    "v_add_u32_sdwa v104, s92, v104 dst_sel:DWORD dst_unused:UNUSED_PAD src0_sel:DWORD src1_sel:WORD_0\n\t"
    "s_waitcnt lgkmcnt(4)\n\t"
    "v_smfmac_f32_16x16x64_bf16 v[116:119], v[120:123], v[84:91], v124\n\t"
    "ds_read_b64 v[84:85], v104 offset:0\n\t"
    "ds_read_b64 v[86:87], v126 offset:0\n\t"
    "v_cvt_pk_bf16_f32 v80, v80, v81\n\t"
    "v_cvt_pk_bf16_f32 v81, v82, v83\n\t"
    "ds_write_b64 v125, v[80:81] offset:32768\n\t"
    "v_add_u32_sdwa v90, s92, v105 dst_sel:DWORD dst_unused:UNUSED_PAD src0_sel:DWORD src1_sel:WORD_1\n\t"
    "v_add_u32_sdwa v105, s92, v105 dst_sel:DWORD dst_unused:UNUSED_PAD src0_sel:DWORD src1_sel:WORD_0\n\t"
    "ds_read_b64 v[88:89], v105 offset:0\n\t"
    "ds_read_b64 v[90:91], v90 offset:0\n\t"
    "v_add_u32_sdwa v126, s92, v106 dst_sel:DWORD dst_unused:UNUSED_PAD src0_sel:DWORD src1_sel:WORD_1\n\t"
    "v_add_u32_sdwa v106, s92, v106 dst_sel:DWORD dst_unused:UNUSED_PAD src0_sel:DWORD src1_sel:WORD_0\n\t"
    "s_waitcnt vmcnt(2) lgkmcnt(5)\n\t"
    "v_smfmac_f32_16x16x64_bf16 v[116:119], v[120:123], v[92:99], v124\n\t"
    "ds_read_b64 v[92:93], v106 offset:0\n\t"
    "ds_read_b64 v[94:95], v126 offset:0\n\t"
    "v_add_u32_sdwa v98, s92, v107 dst_sel:DWORD dst_unused:UNUSED_PAD src0_sel:DWORD src1_sel:WORD_1\n\t"
    "v_add_u32_sdwa v107, s92, v107 dst_sel:DWORD dst_unused:UNUSED_PAD src0_sel:DWORD src1_sel:WORD_0\n\t"
    "ds_read_b64 v[96:97], v107 offset:0\n\t"
    "ds_read_b64 v[98:99], v98 offset:0\n\t"
    "buffer_load_dwordx4 v[104:107], v127, %12, s84 offen offset:1024 sc1\n\t"
    "v_add_u32_sdwa v126, s92, v108 dst_sel:DWORD dst_unused:UNUSED_PAD src0_sel:DWORD src1_sel:WORD_1\n\t"
    "v_cvt_pk_bf16_f32 v48, v116, v117\n\t"
    "v_cvt_pk_bf16_f32 v49, v118, v119\n\t"
    "v_and_b32 v119, s67, v51\n\t"
    "v_lshlrev_b32 v118, 16, v51\n\t"
    "v_and_b32 v117, s67, v50\n\t"
    "v_lshlrev_b32 v116, 16, v50\n\t"
    "v_add_u32_sdwa v108, s92, v108 dst_sel:DWORD dst_unused:UNUSED_PAD src0_sel:DWORD src1_sel:WORD_0\n\t"
    "s_waitcnt lgkmcnt(4)\n\t"
    "v_smfmac_f32_16x16x64_bf16 v[116:119], v[120:123], v[84:91], v124\n\t"
    "ds_read_b64 v[84:85], v108 offset:0\n\t"
    "v_mfma_f32_16x16x32_bf16 v[80:83], v[64:67], v[212:215], 0\n\t"
    "ds_read_b64 v[86:87], v126 offset:0\n\t"
    "v_mfma_f32_16x16x32_bf16 v[80:83], v[68:71], v[216:219], v[80:83]\n\t"
    "v_add_u32_sdwa v90, s92, v109 dst_sel:DWORD dst_unused:UNUSED_PAD src0_sel:DWORD src1_sel:WORD_1\n\t"
    "v_add_u32_sdwa v109, s92, v109 dst_sel:DWORD dst_unused:UNUSED_PAD src0_sel:DWORD src1_sel:WORD_0\n\t"
    "ds_read_b64 v[88:89], v109 offset:0\n\t"
    "v_mfma_f32_16x16x32_bf16 v[80:83], v[72:75], v[148:151], v[80:83]\n\t"
    "ds_read_b64 v[90:91], v90 offset:0\n\t"
    "v_mfma_f32_16x16x32_bf16 v[80:83], v[76:79], v[152:155], v[80:83]\n\t"
    "v_add_u32_sdwa v126, s92, v110 dst_sel:DWORD dst_unused:UNUSED_PAD src0_sel:DWORD src1_sel:WORD_1\n\t"
    "v_add_u32_sdwa v110, s92, v110 dst_sel:DWORD dst_unused:UNUSED_PAD src0_sel:DWORD src1_sel:WORD_0\n\t"
    "s_waitcnt vmcnt(2) lgkmcnt(4)\n\t"
    "v_smfmac_f32_16x16x64_bf16 v[116:119], v[120:123], v[92:99], v124\n\t"
    "ds_read_b64 v[92:93], v110 offset:0\n\t"
    "ds_read_b64 v[94:95], v126 offset:0\n\t"
    "v_add_u32_sdwa v98, s92, v111 dst_sel:DWORD dst_unused:UNUSED_PAD src0_sel:DWORD src1_sel:WORD_1\n\t"
    "v_add_u32_sdwa v111, s92, v111 dst_sel:DWORD dst_unused:UNUSED_PAD src0_sel:DWORD src1_sel:WORD_0\n\t"
    "ds_read_b64 v[96:97], v111 offset:0\n\t"
    "ds_read_b64 v[98:99], v98 offset:0\n\t"
    "s_add_u32 s85, s85, s95\n\t"
    "buffer_load_dwordx4 v[108:111], v127, %12, s85 offen offset:0 sc1\n\t"
    "v_add_u32_sdwa v126, s92, v112 dst_sel:DWORD dst_unused:UNUSED_PAD src0_sel:DWORD src1_sel:WORD_1\n\t"
    "v_cvt_pk_bf16_f32 v50, v116, v117\n\t"
    "v_cvt_pk_bf16_f32 v51, v118, v119\n\t"
    "v_and_b32 v119, s67, v57\n\t"
    "v_lshlrev_b32 v118, 16, v57\n\t"
    "v_and_b32 v117, s67, v56\n\t"
    "v_lshlrev_b32 v116, 16, v56\n\t"
    "v_add_u32_sdwa v112, s92, v112 dst_sel:DWORD dst_unused:UNUSED_PAD src0_sel:DWORD src1_sel:WORD_0\n\t"
    "s_waitcnt lgkmcnt(4)\n\t"
    "v_smfmac_f32_16x16x64_bf16 v[116:119], v[120:123], v[84:91], v124\n\t"
    "ds_read_b64 v[84:85], v112 offset:0\n\t"
    "ds_read_b64 v[86:87], v126 offset:0\n\t"
    "v_cvt_pk_bf16_f32 v80, v80, v81\n\t"
    "v_cvt_pk_bf16_f32 v81, v82, v83\n\t"
    "ds_write_b64 v125, v[80:81] offset:32872\n\t"
    "v_add_u32_sdwa v90, s92, v113 dst_sel:DWORD dst_unused:UNUSED_PAD src0_sel:DWORD src1_sel:WORD_1\n\t"
    "v_add_u32_sdwa v113, s92, v113 dst_sel:DWORD dst_unused:UNUSED_PAD src0_sel:DWORD src1_sel:WORD_0\n\t"
    "ds_read_b64 v[88:89], v113 offset:0\n\t"
    "ds_read_b64 v[90:91], v90 offset:0\n\t"
    "v_add_u32_sdwa v126, s92, v114 dst_sel:DWORD dst_unused:UNUSED_PAD src0_sel:DWORD src1_sel:WORD_1\n\t"
    "v_add_u32_sdwa v114, s92, v114 dst_sel:DWORD dst_unused:UNUSED_PAD src0_sel:DWORD src1_sel:WORD_0\n\t"
    "s_waitcnt vmcnt(2) lgkmcnt(5)\n\t"
    "v_smfmac_f32_16x16x64_bf16 v[116:119], v[120:123], v[92:99], v124\n\t"
    "ds_read_b64 v[92:93], v114 offset:0\n\t"
    "ds_read_b64 v[94:95], v126 offset:0\n\t"
    "v_add_u32_sdwa v98, s92, v115 dst_sel:DWORD dst_unused:UNUSED_PAD src0_sel:DWORD src1_sel:WORD_1\n\t"
    "v_add_u32_sdwa v115, s92, v115 dst_sel:DWORD dst_unused:UNUSED_PAD src0_sel:DWORD src1_sel:WORD_0\n\t"
    "ds_read_b64 v[96:97], v115 offset:0\n\t"
    "ds_read_b64 v[98:99], v98 offset:0\n\t"
    "buffer_load_dwordx4 v[112:115], v127, %12, s85 offen offset:1024 sc1\n\t"
    "s_sub_u32 s92, 64000, s92\n\t"
    "v_add_u32_sdwa v126, s92, v100 dst_sel:DWORD dst_unused:UNUSED_PAD src0_sel:DWORD src1_sel:WORD_1\n\t"
    "v_cvt_pk_bf16_f32 v56, v116, v117\n\t"
    "v_cvt_pk_bf16_f32 v57, v118, v119\n\t"
    "v_and_b32 v119, s67, v59\n\t"
    "v_lshlrev_b32 v118, 16, v59\n\t"
    "v_and_b32 v117, s67, v58\n\t"
    "v_lshlrev_b32 v116, 16, v58\n\t"
    "v_add_u32_sdwa v100, s92, v100 dst_sel:DWORD dst_unused:UNUSED_PAD src0_sel:DWORD src1_sel:WORD_0\n\t"
    "s_waitcnt lgkmcnt(0)\n\t"
    "s_barrier\n\t"
    "v_add_u32 v125, s93, v125\n\t"
    "s_sub_u32 s93, 0, s93\n\t"
    "v_smfmac_f32_16x16x64_bf16 v[116:119], v[120:123], v[84:91], v124\n\t"
    "ds_read_b64 v[84:85], v100 offset:0\n\t"
    "ds_read_b64 v[86:87], v126 offset:0\n\t"
    "v_add_u32_sdwa v90, s92, v101 dst_sel:DWORD dst_unused:UNUSED_PAD src0_sel:DWORD src1_sel:WORD_1\n\t"
    "v_add_u32_sdwa v101, s92, v101 dst_sel:DWORD dst_unused:UNUSED_PAD src0_sel:DWORD src1_sel:WORD_0\n\t"
    "ds_read_b64 v[88:89], v101 offset:0\n\t"
    "ds_read_b64 v[90:91], v90 offset:0\n\t"
    "v_add_u32_sdwa v126, s92, v102 dst_sel:DWORD dst_unused:UNUSED_PAD src0_sel:DWORD src1_sel:WORD_1\n\t"
    "v_add_u32_sdwa v102, s92, v102 dst_sel:DWORD dst_unused:UNUSED_PAD src0_sel:DWORD src1_sel:WORD_0\n\t"
    "s_cmp_lt_u32 %16, 4\n\t"
    "s_cbranch_scc1 L_tp63_%=\n\t"
    "s_setprio 1\n\t"
    "L_tp63_%=:\n\t"
    "s_waitcnt vmcnt(2)\n\t"
    "v_smfmac_f32_16x16x64_bf16 v[116:119], v[120:123], v[92:99], v124\n\t"
    "ds_read_b64 v[92:93], v102 offset:0\n\t"
    "ds_read_b64 v[94:95], v126 offset:0\n\t"
    "v_add_u32_sdwa v98, s92, v103 dst_sel:DWORD dst_unused:UNUSED_PAD src0_sel:DWORD src1_sel:WORD_1\n\t"
    "v_add_u32_sdwa v103, s92, v103 dst_sel:DWORD dst_unused:UNUSED_PAD src0_sel:DWORD src1_sel:WORD_0\n\t"
    "ds_read_b64 v[96:97], v103 offset:0\n\t"
    "ds_read_b64 v[98:99], v98 offset:0\n\t"
    "s_add_u32 s86, s86, s95\n\t"
    "buffer_load_dwordx4 v[100:103], v127, %12, s86 offen offset:0 sc1\n\t"
    "v_add_u32_sdwa v126, s92, v104 dst_sel:DWORD dst_unused:UNUSED_PAD src0_sel:DWORD src1_sel:WORD_1\n\t"
    "v_cvt_pk_bf16_f32 v58, v116, v117\n\t"
    "v_cvt_pk_bf16_f32 v59, v118, v119\n\t"
    "v_and_b32 v119, s67, v5\n\t"
    "v_lshlrev_b32 v118, 16, v5\n\t"
    "v_and_b32 v117, s67, v4\n\t"
    "v_lshlrev_b32 v116, 16, v4\n\t"
    "v_add_u32_sdwa v104, s92, v104 dst_sel:DWORD dst_unused:UNUSED_PAD src0_sel:DWORD src1_sel:WORD_0\n\t"
    "v_add_u32 v79, s96, v127\n\t"
    "s_add_u32 s96, s96, 0x1000\n\t"
    "s_and_b32 s96, s96, 0x3000\n\t"
    "v_add_u32 v79, 128000, v79\n\t"
    "ds_read_b128 v[64:67], v79 offset:0\n\t"
    "ds_read_b128 v[68:71], v79 offset:1024\n\t"
    "ds_read_b128 v[72:75], v79 offset:2048\n\t"
    "ds_read_b128 v[76:79], v79 offset:3072\n\t"
    "s_cmp_eq_u32 s66, s48\n\t"
    "s_cbranch_scc1 L_pub_%=\n\t"
    "L_pubb_%=:\n\t"
    "s_sub_u32 s66, s66, 1\n\t"
    "s_cmp_lg_u32 s66, 0\n\t"
    "s_cbranch_scc1 L_loop_%=\n\t"
    "s_mov_b32 s92, 0xffff\n\t"
    "s_waitcnt lgkmcnt(8)\n\t"
    "v_smfmac_f32_16x16x64_bf16 v[116:119], v[120:123], v[84:91], v124\n\t"
    "ds_read_b64 v[84:85], v104 offset:0\n\t"
    "ds_read_b64 v[86:87], v126 offset:0\n\t"
    "v_lshrrev_b32 v90, 16, v105\n\t"
    "v_and_b32 v105, s92, v105\n\t"
    "ds_read_b64 v[88:89], v105 offset:0\n\t"
    "ds_read_b64 v[90:91], v90 offset:0\n\t"
    "v_lshrrev_b32 v126, 16, v106\n\t"
    "v_and_b32 v106, s92, v106\n\t"
    "s_waitcnt vmcnt(2) lgkmcnt(8)\n\t"
    "v_smfmac_f32_16x16x64_bf16 v[116:119], v[120:123], v[92:99], v124\n\t"
    "ds_read_b64 v[92:93], v106 offset:0\n\t"
    "s_waitcnt lgkmcnt(5)\n\t"
    "v_mfma_f32_16x16x32_bf16 v[80:83], v[64:67], v[164:167], 0\n\t"
    "ds_read_b64 v[94:95], v126 offset:0\n\t"
    "v_mfma_f32_16x16x32_bf16 v[80:83], v[68:71], v[168:171], v[80:83]\n\t"
    "v_lshrrev_b32 v98, 16, v107\n\t"
    "v_and_b32 v107, s92, v107\n\t"
    "ds_read_b64 v[96:97], v107 offset:0\n\t"
    "v_mfma_f32_16x16x32_bf16 v[80:83], v[72:75], v[220:223], v[80:83]\n\t"
    "ds_read_b64 v[98:99], v98 offset:0\n\t"
    "v_mfma_f32_16x16x32_bf16 v[80:83], v[76:79], v[224:227], v[80:83]\n\t"
    "v_lshrrev_b32 v126, 16, v108\n\t"
    "v_cvt_pk_bf16_f32 v4, v116, v117\n\t"
    "v_cvt_pk_bf16_f32 v5, v118, v119\n\t"
    "v_and_b32 v119, s67, v7\n\t"
    "v_lshlrev_b32 v118, 16, v7\n\t"
    "v_and_b32 v117, s67, v6\n\t"
    "v_lshlrev_b32 v116, 16, v6\n\t"
    "v_and_b32 v108, s92, v108\n\t"
    "s_waitcnt lgkmcnt(4)\n\t"
    "v_smfmac_f32_16x16x64_bf16 v[116:119], v[120:123], v[84:91], v124\n\t"
    "ds_read_b64 v[84:85], v108 offset:0\n\t"
    "v_mfma_f32_16x16x32_bf16 v[104:107], v[64:67], v[172:175], 0\n\t"
    "ds_read_b64 v[86:87], v126 offset:0\n\t"
    "v_mfma_f32_16x16x32_bf16 v[104:107], v[68:71], v[176:179], v[104:107]\n\t"
    "v_lshrrev_b32 v90, 16, v109\n\t"
    "v_and_b32 v109, s92, v109\n\t"
    "ds_read_b64 v[88:89], v109 offset:0\n\t"
    "v_mfma_f32_16x16x32_bf16 v[104:107], v[72:75], v[228:231], v[104:107]\n\t"
    "ds_read_b64 v[90:91], v90 offset:0\n\t"
    "v_mfma_f32_16x16x32_bf16 v[104:107], v[76:79], v[232:235], v[104:107]\n\t"
    "v_lshrrev_b32 v126, 16, v110\n\t"
    "v_cvt_pk_bf16_f32 v80, v80, v81\n\t"
    "v_cvt_pk_bf16_f32 v81, v82, v83\n\t"
    "ds_write_b64 v125, v[80:81] offset:0\n\t"
    "v_mfma_f32_16x16x32_bf16 v[80:83], v[64:67], v[180:183], 0\n\t"
    "v_and_b32 v110, s92, v110\n\t"
    "v_mfma_f32_16x16x32_bf16 v[80:83], v[68:71], v[184:187], v[80:83]\n\t"
    "v_mfma_f32_16x16x32_bf16 v[80:83], v[72:75], v[236:239], v[80:83]\n\t"
    "s_waitcnt vmcnt(1) lgkmcnt(5)\n\t"
    "v_smfmac_f32_16x16x64_bf16 v[116:119], v[120:123], v[92:99], v124\n\t"
    "ds_read_b64 v[92:93], v110 offset:0\n\t"
    "v_mfma_f32_16x16x32_bf16 v[80:83], v[76:79], v[240:243], v[80:83]\n\t"
    "ds_read_b64 v[94:95], v126 offset:0\n\t"
    "v_cvt_pk_bf16_f32 v104, v104, v105\n\t"
    "v_cvt_pk_bf16_f32 v105, v106, v107\n\t"
    "ds_write_b64 v125, v[104:105] offset:128\n\t"
    "v_mfma_f32_16x16x32_bf16 v[104:107], v[64:67], v[188:191], 0\n\t"
    "v_lshrrev_b32 v98, 16, v111\n\t"
    "v_and_b32 v111, s92, v111\n\t"
    "ds_read_b64 v[96:97], v111 offset:0\n\t"
    "v_mfma_f32_16x16x32_bf16 v[104:107], v[68:71], v[192:195], v[104:107]\n\t"
    "ds_read_b64 v[98:99], v98 offset:0\n\t"
    "v_mfma_f32_16x16x32_bf16 v[104:107], v[72:75], v[244:247], v[104:107]\n\t"
    "buffer_load_dwordx4 v[108:111], v127, %12, s86 offen offset:1024 sc1\n\t"
    "v_lshrrev_b32 v126, 16, v112\n\t"
    "v_cvt_pk_bf16_f32 v6, v116, v117\n\t"
    "v_cvt_pk_bf16_f32 v7, v118, v119\n\t"
    "v_and_b32 v119, s67, v13\n\t"
    "v_lshlrev_b32 v118, 16, v13\n\t"
    "v_and_b32 v117, s67, v12\n\t"
    "v_lshlrev_b32 v116, 16, v12\n\t"
    "v_and_b32 v112, s92, v112\n\t"
    "s_waitcnt lgkmcnt(6)\n\t"
    "v_smfmac_f32_16x16x64_bf16 v[116:119], v[120:123], v[84:91], v124\n\t"
    "ds_read_b64 v[84:85], v112 offset:0\n\t"
    "v_mfma_f32_16x16x32_bf16 v[104:107], v[76:79], v[248:251], v[104:107]\n\t"
    "ds_read_b64 v[86:87], v126 offset:0\n\t"
    "v_cvt_pk_bf16_f32 v80, v80, v81\n\t"
    "v_cvt_pk_bf16_f32 v81, v82, v83\n\t"
    "ds_write_b64 v125, v[80:81] offset:256\n\t"
    "v_mfma_f32_16x16x32_bf16 v[80:83], v[64:67], v[196:199], 0\n\t"
    "v_lshrrev_b32 v90, 16, v113\n\t"
    "v_and_b32 v113, s92, v113\n\t"
    "ds_read_b64 v[88:89], v113 offset:0\n\t"
    "v_mfma_f32_16x16x32_bf16 v[80:83], v[68:71], v[200:203], v[80:83]\n\t"
    "ds_read_b64 v[90:91], v90 offset:0\n\t"
    "v_mfma_f32_16x16x32_bf16 v[80:83], v[72:75], v[252:255], v[80:83]\n\t"
    "v_lshrrev_b32 v126, 16, v114\n\t"
    "v_mfma_f32_16x16x32_bf16 v[80:83], v[76:79], v[128:131], v[80:83]\n\t"
    "v_and_b32 v114, s92, v114\n\t"
    "v_cvt_pk_bf16_f32 v104, v104, v105\n\t"
    "v_cvt_pk_bf16_f32 v105, v106, v107\n\t"
    "ds_write_b64 v125, v[104:105] offset:384\n\t"
    "v_mfma_f32_16x16x32_bf16 v[104:107], v[64:67], v[204:207], 0\n\t"
    "s_waitcnt vmcnt(1) lgkmcnt(6)\n\t"
    "v_smfmac_f32_16x16x64_bf16 v[116:119], v[120:123], v[92:99], v124\n\t"
    "ds_read_b64 v[92:93], v114 offset:0\n\t"
    "v_mfma_f32_16x16x32_bf16 v[104:107], v[68:71], v[208:211], v[104:107]\n\t"
    "ds_read_b64 v[94:95], v126 offset:0\n\t"
    "v_mfma_f32_16x16x32_bf16 v[104:107], v[72:75], v[132:135], v[104:107]\n\t"
    "v_lshrrev_b32 v98, 16, v115\n\t"
    "v_and_b32 v115, s92, v115\n\t"
    "ds_read_b64 v[96:97], v115 offset:0\n\t"
    "v_mfma_f32_16x16x32_bf16 v[104:107], v[76:79], v[136:139], v[104:107]\n\t"
    "ds_read_b64 v[98:99], v98 offset:0\n\t"
    "v_cvt_pk_bf16_f32 v80, v80, v81\n\t"
    "v_cvt_pk_bf16_f32 v81, v82, v83\n\t"
    "ds_write_b64 v125, v[80:81] offset:512\n\t"
    "v_mfma_f32_16x16x32_bf16 v[80:83], v[64:67], v[156:159], 0\n\t"
    "s_add_u32 s87, s87, s95\n\t"
    "buffer_load_dwordx4 v[112:115], v127, %12, s87 offen offset:0 sc1\n\t"
    "v_lshrrev_b32 v126, 16, v100\n\t"
    "v_mfma_f32_16x16x32_bf16 v[80:83], v[68:71], v[160:163], v[80:83]\n\t"
    "v_cvt_pk_bf16_f32 v12, v116, v117\n\t"
    "v_cvt_pk_bf16_f32 v13, v118, v119\n\t"
    "v_and_b32 v119, s67, v15\n\t"
    "v_lshlrev_b32 v118, 16, v15\n\t"
    "v_and_b32 v117, s67, v14\n\t"
    "v_lshlrev_b32 v116, 16, v14\n\t"
    "v_and_b32 v100, s92, v100\n\t"
    "s_waitcnt lgkmcnt(6)\n\t"
    "v_smfmac_f32_16x16x64_bf16 v[116:119], v[120:123], v[84:91], v124\n\t"
    "ds_read_b64 v[84:85], v100 offset:0\n\t"
    "v_mfma_f32_16x16x32_bf16 v[80:83], v[72:75], v[140:143], v[80:83]\n\t"
    "ds_read_b64 v[86:87], v126 offset:0\n\t"
    "v_mfma_f32_16x16x32_bf16 v[80:83], v[76:79], v[144:147], v[80:83]\n\t"
    "v_lshrrev_b32 v90, 16, v101\n\t"
    "v_and_b32 v101, s92, v101\n\t"
    "ds_read_b64 v[88:89], v101 offset:0\n\t"
    "v_cvt_pk_bf16_f32 v104, v104, v105\n\t"
    "v_cvt_pk_bf16_f32 v105, v106, v107\n\t"
    "ds_write_b64 v125, v[104:105] offset:640\n\t"
    "v_mfma_f32_16x16x32_bf16 v[104:107], v[64:67], v[212:215], 0\n\t"
    "ds_read_b64 v[90:91], v90 offset:0\n\t"
    "v_mfma_f32_16x16x32_bf16 v[104:107], v[68:71], v[216:219], v[104:107]\n\t"
    "v_lshrrev_b32 v126, 16, v102\n\t"
    "v_mfma_f32_16x16x32_bf16 v[104:107], v[72:75], v[148:151], v[104:107]\n\t"
    "v_and_b32 v102, s92, v102\n\t"
    "v_mfma_f32_16x16x32_bf16 v[104:107], v[76:79], v[152:155], v[104:107]\n\t"
    "v_add_u32 v79, s96, v127\n\t"
    "s_add_u32 s96, s96, 0x1000\n\t"
    "s_and_b32 s96, s96, 0x3000\n\t"
    "v_add_u32 v79, 128000, v79\n\t"
    "ds_read_b128 v[64:67], v79 offset:0\n\t"
    "ds_read_b128 v[68:71], v79 offset:1024\n\t"
    "ds_read_b128 v[72:75], v79 offset:2048\n\t"
    "ds_read_b128 v[76:79], v79 offset:3072\n\t"
    "v_cvt_pk_bf16_f32 v80, v80, v81\n\t"
    "v_cvt_pk_bf16_f32 v81, v82, v83\n\t"
    "ds_write_b64 v125, v[80:81] offset:768\n\t"
    "s_waitcnt vmcnt(1) lgkmcnt(11)\n\t"
    "v_smfmac_f32_16x16x64_bf16 v[116:119], v[120:123], v[92:99], v124\n\t"
    "ds_read_b64 v[92:93], v102 offset:0\n\t"
    "ds_read_b64 v[94:95], v126 offset:0\n\t"
    "v_lshrrev_b32 v98, 16, v103\n\t"
    "v_and_b32 v103, s92, v103\n\t"
    "ds_read_b64 v[96:97], v103 offset:0\n\t"
    "ds_read_b64 v[98:99], v98 offset:0\n\t"
    "buffer_load_dwordx4 v[100:103], v127, %12, s87 offen offset:1024 sc1\n\t"
    "v_lshrrev_b32 v126, 16, v108\n\t"
    "v_cvt_pk_bf16_f32 v14, v116, v117\n\t"
    "v_cvt_pk_bf16_f32 v15, v118, v119\n\t"
    "v_and_b32 v119, s67, v21\n\t"
    "v_lshlrev_b32 v118, 16, v21\n\t"
    "v_and_b32 v117, s67, v20\n\t"
    "v_lshlrev_b32 v116, 16, v20\n\t"
    "v_and_b32 v108, s92, v108\n\t"
    "s_waitcnt lgkmcnt(9)\n\t"
    "v_smfmac_f32_16x16x64_bf16 v[116:119], v[120:123], v[84:91], v124\n\t"
    "ds_read_b64 v[84:85], v108 offset:0\n\t"
    "s_waitcnt lgkmcnt(6)\n\t"
    "v_mfma_f32_16x16x32_bf16 v[80:83], v[64:67], v[164:167], 0\n\t"
    "ds_read_b64 v[86:87], v126 offset:0\n\t"
    "v_mfma_f32_16x16x32_bf16 v[80:83], v[68:71], v[168:171], v[80:83]\n\t"
    "v_lshrrev_b32 v90, 16, v109\n\t"
    "v_and_b32 v109, s92, v109\n\t"
    "ds_read_b64 v[88:89], v109 offset:0\n\t"
    "v_mfma_f32_16x16x32_bf16 v[80:83], v[72:75], v[220:223], v[80:83]\n\t"
    "ds_read_b64 v[90:91], v90 offset:0\n\t"
    "v_mfma_f32_16x16x32_bf16 v[80:83], v[76:79], v[224:227], v[80:83]\n\t"
    "v_lshrrev_b32 v126, 16, v110\n\t"
    "v_cvt_pk_bf16_f32 v104, v104, v105\n\t"
    "v_cvt_pk_bf16_f32 v105, v106, v107\n\t"
    "ds_write_b64 v125, v[104:105] offset:872\n\t"
    "v_mfma_f32_16x16x32_bf16 v[104:107], v[64:67], v[172:175], 0\n\t"
    "v_and_b32 v110, s92, v110\n\t"
    "v_mfma_f32_16x16x32_bf16 v[104:107], v[68:71], v[176:179], v[104:107]\n\t"
    "s_waitcnt vmcnt(1) lgkmcnt(5)\n\t"
    "v_smfmac_f32_16x16x64_bf16 v[116:119], v[120:123], v[92:99], v124\n\t"
    "ds_read_b64 v[92:93], v110 offset:0\n\t"
    "v_mfma_f32_16x16x32_bf16 v[104:107], v[72:75], v[228:231], v[104:107]\n\t"
    "ds_read_b64 v[94:95], v126 offset:0\n\t"
    "v_mfma_f32_16x16x32_bf16 v[104:107], v[76:79], v[232:235], v[104:107]\n\t"
    "v_lshrrev_b32 v98, 16, v111\n\t"
    "v_and_b32 v111, s92, v111\n\t"
    "ds_read_b64 v[96:97], v111 offset:0\n\t"
    "v_cvt_pk_bf16_f32 v80, v80, v81\n\t"
    "v_cvt_pk_bf16_f32 v81, v82, v83\n\t"
    "ds_write_b64 v125, v[80:81] offset:32000\n\t"
    "v_mfma_f32_16x16x32_bf16 v[80:83], v[64:67], v[180:183], 0\n\t"
    "ds_read_b64 v[98:99], v98 offset:0\n\t"
    "v_mfma_f32_16x16x32_bf16 v[80:83], v[68:71], v[184:187], v[80:83]\n\t"
    "s_add_u32 s84, s84, 0x8000\n\t"
    "buffer_load_dwordx4 v[108:111], v127, %12, s84 offen offset:0 sc1\n\t"
    "v_lshrrev_b32 v126, 16, v112\n\t"
    "v_mfma_f32_16x16x32_bf16 v[80:83], v[72:75], v[236:239], v[80:83]\n\t"
    "v_cvt_pk_bf16_f32 v20, v116, v117\n\t"
    "v_cvt_pk_bf16_f32 v21, v118, v119\n\t"
    "v_and_b32 v119, s67, v23\n\t"
    "v_lshlrev_b32 v118, 16, v23\n\t"
    "v_and_b32 v117, s67, v22\n\t"
    "v_lshlrev_b32 v116, 16, v22\n\t"
    "v_and_b32 v112, s92, v112\n\t"
    "s_waitcnt lgkmcnt(6)\n\t"
    "v_smfmac_f32_16x16x64_bf16 v[116:119], v[120:123], v[84:91], v124\n\t"
    "ds_read_b64 v[84:85], v112 offset:0\n\t"
    "v_mfma_f32_16x16x32_bf16 v[80:83], v[76:79], v[240:243], v[80:83]\n\t"
    "ds_read_b64 v[86:87], v126 offset:0\n\t"
    "v_cvt_pk_bf16_f32 v104, v104, v105\n\t"
    "v_cvt_pk_bf16_f32 v105, v106, v107\n\t"
    "ds_write_b64 v125, v[104:105] offset:32128\n\t"
    "v_mfma_f32_16x16x32_bf16 v[104:107], v[64:67], v[188:191], 0\n\t"
    "v_lshrrev_b32 v90, 16, v113\n\t"
    "v_and_b32 v113, s92, v113\n\t"
    "ds_read_b64 v[88:89], v113 offset:0\n\t"
    "v_mfma_f32_16x16x32_bf16 v[104:107], v[68:71], v[192:195], v[104:107]\n\t"
    "ds_read_b64 v[90:91], v90 offset:0\n\t"
    "v_mfma_f32_16x16x32_bf16 v[104:107], v[72:75], v[244:247], v[104:107]\n\t"
    "v_lshrrev_b32 v126, 16, v114\n\t"
    "v_mfma_f32_16x16x32_bf16 v[104:107], v[76:79], v[248:251], v[104:107]\n\t"
    "v_and_b32 v114, s92, v114\n\t"
    "v_cvt_pk_bf16_f32 v80, v80, v81\n\t"
    "v_cvt_pk_bf16_f32 v81, v82, v83\n\t"
    "ds_write_b64 v125, v[80:81] offset:32256\n\t"
    "v_mfma_f32_16x16x32_bf16 v[80:83], v[64:67], v[196:199], 0\n\t"
    "s_waitcnt vmcnt(1) lgkmcnt(6)\n\t"
    "v_smfmac_f32_16x16x64_bf16 v[116:119], v[120:123], v[92:99], v124\n\t"
    "ds_read_b64 v[92:93], v114 offset:0\n\t"
    "v_mfma_f32_16x16x32_bf16 v[80:83], v[68:71], v[200:203], v[80:83]\n\t"
    "ds_read_b64 v[94:95], v126 offset:0\n\t"
    "v_mfma_f32_16x16x32_bf16 v[80:83], v[72:75], v[252:255], v[80:83]\n\t"
    "v_lshrrev_b32 v98, 16, v115\n\t"
    "v_and_b32 v115, s92, v115\n\t"
    "ds_read_b64 v[96:97], v115 offset:0\n\t"
    "v_mfma_f32_16x16x32_bf16 v[80:83], v[76:79], v[128:131], v[80:83]\n\t"
    "ds_read_b64 v[98:99], v98 offset:0\n\t"
    "v_cvt_pk_bf16_f32 v104, v104, v105\n\t"
    "v_cvt_pk_bf16_f32 v105, v106, v107\n\t"
    "ds_write_b64 v125, v[104:105] offset:32384\n\t"
    "v_mfma_f32_16x16x32_bf16 v[104:107], v[64:67], v[204:207], 0\n\t"
    "buffer_load_dwordx4 v[112:115], v127, %12, s84 offen offset:1024 sc1\n\t"
    "v_lshrrev_b32 v126, 16, v100\n\t"
    "v_cvt_pk_bf16_f32 v22, v116, v117\n\t"
    "v_cvt_pk_bf16_f32 v23, v118, v119\n\t"
    "v_and_b32 v119, s67, v29\n\t"
    "v_lshlrev_b32 v118, 16, v29\n\t"
    "v_and_b32 v117, s67, v28\n\t"
    "v_lshlrev_b32 v116, 16, v28\n\t"
    "v_and_b32 v100, s92, v100\n\t"
    "s_waitcnt lgkmcnt(6)\n\t"
    "v_smfmac_f32_16x16x64_bf16 v[116:119], v[120:123], v[84:91], v124\n\t"
    "ds_read_b64 v[84:85], v100 offset:0\n\t"
    "v_mfma_f32_16x16x32_bf16 v[104:107], v[68:71], v[208:211], v[104:107]\n\t"
    "ds_read_b64 v[86:87], v126 offset:0\n\t"
    "v_mfma_f32_16x16x32_bf16 v[104:107], v[72:75], v[132:135], v[104:107]\n\t"
    "v_lshrrev_b32 v90, 16, v101\n\t"
    "v_and_b32 v101, s92, v101\n\t"
    "ds_read_b64 v[88:89], v101 offset:0\n\t"
    "v_mfma_f32_16x16x32_bf16 v[104:107], v[76:79], v[136:139], v[104:107]\n\t"
    "ds_read_b64 v[90:91], v90 offset:0\n\t"
    "v_cvt_pk_bf16_f32 v80, v80, v81\n\t"
    "v_cvt_pk_bf16_f32 v81, v82, v83\n\t"
    "ds_write_b64 v125, v[80:81] offset:32512\n\t"
    "v_mfma_f32_16x16x32_bf16 v[80:83], v[64:67], v[156:159], 0\n\t"
    "v_lshrrev_b32 v126, 16, v102\n\t"
    "v_mfma_f32_16x16x32_bf16 v[80:83], v[68:71], v[160:163], v[80:83]\n\t"
    "v_and_b32 v102, s92, v102\n\t"
    "v_mfma_f32_16x16x32_bf16 v[80:83], v[72:75], v[140:143], v[80:83]\n\t"
    "v_mfma_f32_16x16x32_bf16 v[80:83], v[76:79], v[144:147], v[80:83]\n\t"
    "v_cvt_pk_bf16_f32 v104, v104, v105\n\t"
    "v_cvt_pk_bf16_f32 v105, v106, v107\n\t"
    "ds_write_b64 v125, v[104:105] offset:32640\n\t"
    "s_waitcnt vmcnt(1) lgkmcnt(7)\n\t"
    "v_smfmac_f32_16x16x64_bf16 v[116:119], v[120:123], v[92:99], v124\n\t"
    "ds_read_b64 v[92:93], v102 offset:0\n\t"
    "v_mfma_f32_16x16x32_bf16 v[104:107], v[64:67], v[212:215], 0\n\t"
    "ds_read_b64 v[94:95], v126 offset:0\n\t"
    "v_mfma_f32_16x16x32_bf16 v[104:107], v[68:71], v[216:219], v[104:107]\n\t"
    "v_lshrrev_b32 v98, 16, v103\n\t"
    "v_and_b32 v103, s92, v103\n\t"
    "ds_read_b64 v[96:97], v103 offset:0\n\t"
    "v_mfma_f32_16x16x32_bf16 v[104:107], v[72:75], v[148:151], v[104:107]\n\t"
    "ds_read_b64 v[98:99], v98 offset:0\n\t"
    "v_mfma_f32_16x16x32_bf16 v[104:107], v[76:79], v[152:155], v[104:107]\n\t"
    "s_add_u32 s85, s85, 0x8000\n\t"
    "buffer_load_dwordx4 v[100:103], v127, %12, s85 offen offset:0 sc1\n\t"
    "v_lshrrev_b32 v126, 16, v108\n\t"
    "v_cvt_pk_bf16_f32 v80, v80, v81\n\t"
    "v_cvt_pk_bf16_f32 v81, v82, v83\n\t"
    "ds_write_b64 v125, v[80:81] offset:32768\n\t"
    "s_nop 1\n\t"
    "v_cvt_pk_bf16_f32 v104, v104, v105\n\t"
    "v_cvt_pk_bf16_f32 v105, v106, v107\n\t"
    "ds_write_b64 v125, v[104:105] offset:32872\n\t"
    "v_cvt_pk_bf16_f32 v28, v116, v117\n\t"
    "v_cvt_pk_bf16_f32 v29, v118, v119\n\t"
    "v_and_b32 v119, s67, v31\n\t"
    "v_lshlrev_b32 v118, 16, v31\n\t"
    "v_and_b32 v117, s67, v30\n\t"
    "v_lshlrev_b32 v116, 16, v30\n\t"
    "v_and_b32 v108, s92, v108\n\t"
    "s_waitcnt lgkmcnt(0)\n\t"
    "s_barrier\n\t"
    "v_smfmac_f32_16x16x64_bf16 v[116:119], v[120:123], v[84:91], v124\n\t"
    "ds_read_b64 v[84:85], v108 offset:64000\n\t"
    "ds_read_b64 v[86:87], v126 offset:64000\n\t"
    "v_lshrrev_b32 v90, 16, v109\n\t"
    "v_and_b32 v109, s92, v109\n\t"
    "ds_read_b64 v[88:89], v109 offset:64000\n\t"
    "ds_read_b64 v[90:91], v90 offset:64000\n\t"
    "buffer_load_dwordx4 v[104:107], v127, %12, s85 offen offset:1024 sc1\n\t"
    "v_lshrrev_b32 v126, 16, v110\n\t"
    "v_and_b32 v110, s92, v110\n\t"
    "s_waitcnt vmcnt(2)\n\t"
    "v_smfmac_f32_16x16x64_bf16 v[116:119], v[120:123], v[92:99], v124\n\t"
    "ds_read_b64 v[92:93], v110 offset:64000\n\t"
    "ds_read_b64 v[94:95], v126 offset:64000\n\t"
    "v_lshrrev_b32 v98, 16, v111\n\t"
    "v_and_b32 v111, s92, v111\n\t"
    "ds_read_b64 v[96:97], v111 offset:64000\n\t"
    "ds_read_b64 v[98:99], v98 offset:64000\n\t"
    "s_add_u32 s86, s86, 0x8000\n\t"
    "buffer_load_dwordx4 v[108:111], v127, %12, s86 offen offset:0 sc1\n\t"
    "v_lshrrev_b32 v252, 16, v112\n\t"
    "v_and_b32 v112, s92, v112\n\t"
    "v_lshrrev_b32 v253, 16, v113\n\t"
    "v_cvt_pk_bf16_f32 v30, v116, v117\n\t"
    "v_cvt_pk_bf16_f32 v31, v118, v119\n\t"
    "v_and_b32 v119, s67, v5\n\t"
    "v_lshlrev_b32 v118, 16, v5\n\t"
    "v_and_b32 v117, s67, v4\n\t"
    "v_lshlrev_b32 v116, 16, v4\n\t"
    "v_and_b32 v113, s92, v113\n\t"
    "v_mov_b32 v82, 0xbdd2d3e8\n\t"
    "v_mov_b32 v83, 0xbdd2d3e8\n\t"
    "v_mov_b32 v81, 0xc0135761\n\t"
    "v_lshrrev_b32 v125, 2, v127\n\t"
    "v_mov_b32 v80, 0xc0135761\n\t"
    "s_waitcnt lgkmcnt(4)\n\t"
    "v_smfmac_f32_16x16x64_bf16 v[116:119], v[120:123], v[84:91], v124\n\t"
    "ds_read_b64 v[84:85], v112 offset:64000\n\t"
    "ds_read_b64 v[86:87], v252 offset:64000\n\t"
    "ds_read_b64 v[88:89], v113 offset:64000\n\t"
    "ds_read_b64 v[90:91], v253 offset:64000\n\t"
    "v_lshrrev_b32 v254, 16, v114\n\t"
    "v_and_b32 v114, s92, v114\n\t"
    "v_lshrrev_b32 v255, 16, v115\n\t"
    "v_and_b32 v115, s92, v115\n\t"
    "s_lshl_b32 s65, %16, 10\n\t"
    "s_add_u32 s65, s65, 31457280\n\t"
    "s_xor_b32 s61, %14, 8\n\t"
    "s_mul_i32 s61, s61, 69632\n\t"
    "s_add_u32 s61, s61, s65\n\t"
    "s_mul_i32 s94, %14, 69632\n\t"
    "s_add_u32 s94, s94, s65\n\t"
    "s_lshl_b32 s65, %16, 9\n\t"
    "s_sub_u32 s65, 0x10000, s65\n\t"
    "s_add_u32 s93, s94, s65\n\t"
    "s_add_u32 s96, s61, s65\n\t"
    "v_mov_b32 v165, 1\n\t"
    "s_or_b32 s97, %14, %16\n\t"
    "s_cbranch_scc0 L_part0_%=\n\t"
    "L_partb0_%=:\n\t"
    "s_waitcnt vmcnt(2) lgkmcnt(4)\n\t"
    "v_smfmac_f32_16x16x64_bf16 v[116:119], v[120:123], v[92:99], v124\n\t"
    "ds_read_b64 v[92:93], v114 offset:64000\n\t"
    "ds_read_b64 v[94:95], v254 offset:64000\n\t"
    "ds_read_b64 v[96:97], v115 offset:64000\n\t"
    "ds_read_b64 v[98:99], v255 offset:64000\n\t"
    "buffer_load_dwordx4 v[172:175], v127, %12, s86 offen offset:1024 sc1\n\t"
    "v_lshrrev_b32 v252, 16, v100\n\t"
    "v_and_b32 v100, s92, v100\n\t"
    "v_lshrrev_b32 v253, 16, v101\n\t"
    "v_cvt_pk_bf16_f32 v4, v116, v117\n\t"
    "v_cvt_pk_bf16_f32 v5, v118, v119\n\t"
    "v_and_b32 v119, s67, v7\n\t"
    "v_lshlrev_b32 v118, 16, v7\n\t"
    "v_and_b32 v117, s67, v6\n\t"
    "v_lshlrev_b32 v116, 16, v6\n\t"
    "v_and_b32 v101, s92, v101\n\t"
    "v_mov_b32 v156, v120\n\t"
    "v_mov_b32 v160, 0\n\t"
    "v_mov_b32 v161, v121\n\t"
    "v_mov_b32 v157, 0\n\t"
    "v_mov_b32 v158, v122\n\t"
    "v_mov_b32 v162, 0\n\t"
    "v_mov_b32 v163, v123\n\t"
    "v_mov_b32 v159, 0\n\t"
    "s_waitcnt lgkmcnt(4)\n\t"
    "v_smfmac_f32_16x16x64_bf16 v[116:119], v[120:123], v[84:91], v124\n\t"
    "ds_read_b64 v[84:85], v100 offset:64000\n\t"
    "ds_read_b64 v[86:87], v252 offset:64000\n\t"
    "ds_read_b64 v[88:89], v101 offset:64000\n\t"
    "ds_read_b64 v[90:91], v253 offset:64000\n\t"
    "s_add_u32 s87, s87, 0x8000\n\t"
    "buffer_load_dwordx4 v[176:179], v127, %12, s87 offen offset:0 sc1\n\t"
    "v_lshrrev_b32 v254, 16, v102\n\t"
    "v_and_b32 v102, s92, v102\n\t"
    "v_lshrrev_b32 v255, 16, v103\n\t"
    "v_and_b32 v103, s92, v103\n\t"
    "s_cmp_lt_u32 %16, 4\n\t"
    "s_cbranch_scc1 L_tp163_%=\n\t"
    "s_setprio 0\n\t"
    "L_tp163_%=:\n\t"
    "s_waitcnt vmcnt(3) lgkmcnt(4)\n\t"
    "v_smfmac_f32_16x16x64_bf16 v[116:119], v[120:123], v[92:99], v124\n\t"
    "ds_read_b64 v[92:93], v102 offset:64000\n\t"
    "ds_read_b64 v[94:95], v254 offset:64000\n\t"
    "ds_read_b64 v[96:97], v103 offset:64000\n\t"
    "ds_read_b64 v[98:99], v255 offset:64000\n\t"
    "buffer_load_dwordx4 v[180:183], v127, %12, s87 offen offset:1024 sc1\n\t"
    "v_lshrrev_b32 v252, 16, v104\n\t"
    "v_and_b32 v104, s92, v104\n\t"
    "v_lshrrev_b32 v253, 16, v105\n\t"
    "v_cvt_pk_bf16_f32 v6, v116, v117\n\t"
    "v_cvt_pk_bf16_f32 v7, v118, v119\n\t"
    "v_and_b32 v119, s67, v13\n\t"
    "v_lshlrev_b32 v118, 16, v13\n\t"
    "v_and_b32 v117, s67, v12\n\t"
    "v_lshlrev_b32 v116, 16, v12\n\t"
    "v_and_b32 v105, s92, v105\n\t"
    "buffer_store_dwordx4 v[4:7], v127, %12, s61 offen sc1\n\t"
    "s_waitcnt lgkmcnt(4)\n\t"
    "v_smfmac_f32_16x16x64_bf16 v[116:119], v[120:123], v[84:91], v124\n\t"
    "ds_read_b64 v[84:85], v104 offset:64000\n\t"
    "ds_read_b64 v[86:87], v252 offset:64000\n\t"
    "ds_read_b64 v[88:89], v105 offset:64000\n\t"
    "ds_read_b64 v[90:91], v253 offset:64000\n\t"
    "s_add_u32 s88, s88, s95\n\t"
    "buffer_load_dwordx4 v[184:187], v127, %12, s88 offen offset:0 sc1\n\t"
    "v_lshrrev_b32 v254, 16, v106\n\t"
    "v_and_b32 v106, s92, v106\n\t"
    "v_lshrrev_b32 v255, 16, v107\n\t"
    "v_and_b32 v107, s92, v107\n\t"
    "s_waitcnt vmcnt(5) lgkmcnt(4)\n\t"
    "v_smfmac_f32_16x16x64_bf16 v[116:119], v[120:123], v[92:99], v124\n\t"
    "ds_read_b64 v[92:93], v106 offset:64000\n\t"
    "ds_read_b64 v[94:95], v254 offset:64000\n\t"
    "ds_read_b64 v[96:97], v107 offset:64000\n\t"
    "ds_read_b64 v[98:99], v255 offset:64000\n\t"
    "s_add_u32 s88, s88, 0x8000\n\t"
    "buffer_load_dwordx4 v[188:191], v127, %12, s88 offen offset:0 sc1\n\t"
    "v_lshrrev_b32 v252, 16, v108\n\t"
    "v_and_b32 v108, s92, v108\n\t"
    "v_lshrrev_b32 v253, 16, v109\n\t"
    "v_cvt_pk_bf16_f32 v12, v116, v117\n\t"
    "v_cvt_pk_bf16_f32 v13, v118, v119\n\t"
    "v_and_b32 v119, s67, v15\n\t"
    "v_lshlrev_b32 v118, 16, v15\n\t"
    "v_and_b32 v117, s67, v14\n\t"
    "v_lshlrev_b32 v116, 16, v14\n\t"
    "v_and_b32 v109, s92, v109\n\t"
    "s_waitcnt lgkmcnt(4)\n\t"
    "v_smfmac_f32_16x16x64_bf16 v[116:119], v[120:123], v[84:91], v124\n\t"
    "ds_read_b64 v[84:85], v108 offset:64000\n\t"
    "ds_read_b64 v[86:87], v252 offset:64000\n\t"
    "ds_read_b64 v[88:89], v109 offset:64000\n\t"
    "ds_read_b64 v[90:91], v253 offset:64000\n\t"
    "s_sub_u32 s88, s88, 0x8000\n\t"
    "buffer_load_dwordx4 v[236:239], v127, %12, s88 offen offset:1024 sc1\n\t"
    "v_lshrrev_b32 v254, 16, v110\n\t"
    "v_and_b32 v110, s92, v110\n\t"
    "v_lshrrev_b32 v255, 16, v111\n\t"
    "v_and_b32 v111, s92, v111\n\t"
    "s_waitcnt vmcnt(6) lgkmcnt(4)\n\t"
    "v_smfmac_f32_16x16x64_bf16 v[116:119], v[120:123], v[92:99], v124\n\t"
    "ds_read_b64 v[92:93], v110 offset:64000\n\t"
    "ds_read_b64 v[94:95], v254 offset:64000\n\t"
    "ds_read_b64 v[96:97], v111 offset:64000\n\t"
    "ds_read_b64 v[98:99], v255 offset:64000\n\t"
    "s_add_u32 s88, s88, 0x8000\n\t"
    "buffer_load_dwordx4 v[192:195], v127, %12, s88 offen offset:1024 sc1\n\t"
    "v_lshrrev_b32 v252, 16, v172\n\t"
    "v_and_b32 v172, s92, v172\n\t"
    "v_lshrrev_b32 v253, 16, v173\n\t"
    "v_cvt_pk_bf16_f32 v14, v116, v117\n\t"
    "v_cvt_pk_bf16_f32 v15, v118, v119\n\t"
    "v_and_b32 v119, s67, v21\n\t"
    "v_lshlrev_b32 v118, 16, v21\n\t"
    "v_and_b32 v117, s67, v20\n\t"
    "v_lshlrev_b32 v116, 16, v20\n\t"
    "v_and_b32 v173, s92, v173\n\t"
    "s_waitcnt lgkmcnt(4)\n\t"
    "v_smfmac_f32_16x16x64_bf16 v[116:119], v[120:123], v[84:91], v124\n\t"
    "ds_read_b64 v[84:85], v172 offset:64000\n\t"
    "ds_read_b64 v[86:87], v252 offset:64000\n\t"
    "ds_read_b64 v[88:89], v173 offset:64000\n\t"
    "ds_read_b64 v[90:91], v253 offset:64000\n\t"
    "v_lshrrev_b32 v254, 16, v174\n\t"
    "v_and_b32 v174, s92, v174\n\t"
    "v_lshrrev_b32 v255, 16, v175\n\t"
    "v_and_b32 v175, s92, v175\n\t"
    "s_cmp_lg_u32 s97, 0\n\t"
    "s_cbranch_scc0 L_part1_%=\n\t"
    "L_partb1_%=:\n\t"
    "s_waitcnt vmcnt(6) lgkmcnt(4)\n\t"
    "v_smfmac_f32_16x16x64_bf16 v[116:119], v[120:123], v[92:99], v124\n\t"
    "ds_read_b64 v[92:93], v174 offset:64000\n\t"
    "ds_read_b64 v[94:95], v254 offset:64000\n\t"
    "ds_read_b64 v[96:97], v175 offset:64000\n\t"
    "ds_read_b64 v[98:99], v255 offset:64000\n\t"
    "s_add_u32 s89, s89, s95\n\t"
    "buffer_load_dwordx4 v[196:199], v127, %12, s89 offen offset:0 sc1\n\t"
    "v_lshrrev_b32 v252, 16, v176\n\t"
    "v_and_b32 v176, s92, v176\n\t"
    "v_lshrrev_b32 v253, 16, v177\n\t"
    "v_cvt_pk_bf16_f32 v20, v116, v117\n\t"
    "v_cvt_pk_bf16_f32 v21, v118, v119\n\t"
    "v_and_b32 v119, s67, v23\n\t"
    "v_lshlrev_b32 v118, 16, v23\n\t"
    "v_and_b32 v117, s67, v22\n\t"
    "v_lshlrev_b32 v116, 16, v22\n\t"
    "v_and_b32 v177, s92, v177\n\t"
    "s_waitcnt lgkmcnt(4)\n\t"
    "v_smfmac_f32_16x16x64_bf16 v[116:119], v[120:123], v[84:91], v124\n\t"
    "ds_read_b64 v[84:85], v176 offset:64000\n\t"
    "ds_read_b64 v[86:87], v252 offset:64000\n\t"
    "ds_read_b64 v[88:89], v177 offset:64000\n\t"
    "ds_read_b64 v[90:91], v253 offset:64000\n\t"
    "v_lshrrev_b32 v254, 16, v178\n\t"
    "v_and_b32 v178, s92, v178\n\t"
    "v_lshrrev_b32 v255, 16, v179\n\t"
    "v_and_b32 v179, s92, v179\n\t"
    "s_waitcnt vmcnt(6) lgkmcnt(4)\n\t"
    "v_smfmac_f32_16x16x64_bf16 v[116:119], v[120:123], v[92:99], v124\n\t"
    "ds_read_b64 v[92:93], v178 offset:64000\n\t"
    "ds_read_b64 v[94:95], v254 offset:64000\n\t"
    "ds_read_b64 v[96:97], v179 offset:64000\n\t"
    "ds_read_b64 v[98:99], v255 offset:64000\n\t"
    "s_add_u32 s89, s89, 0x8000\n\t"
    "buffer_load_dwordx4 v[200:203], v127, %12, s89 offen offset:0 sc1\n\t"
    "v_lshrrev_b32 v252, 16, v180\n\t"
    "v_and_b32 v180, s92, v180\n\t"
    "v_lshrrev_b32 v253, 16, v181\n\t"
    "v_cvt_pk_bf16_f32 v22, v116, v117\n\t"
    "v_cvt_pk_bf16_f32 v23, v118, v119\n\t"
    "v_and_b32 v119, s67, v29\n\t"
    "v_lshlrev_b32 v118, 16, v29\n\t"
    "v_and_b32 v117, s67, v28\n\t"
    "v_lshlrev_b32 v116, 16, v28\n\t"
    "v_and_b32 v181, s92, v181\n\t"
    "s_add_u32 s61, s61, 0x2000\n\t"
    "buffer_store_dwordx4 v[12:15], v127, %12, s61 offen sc1\n\t"
    "s_waitcnt lgkmcnt(4)\n\t"
    "v_smfmac_f32_16x16x64_bf16 v[116:119], v[120:123], v[84:91], v124\n\t"
    "ds_read_b64 v[84:85], v180 offset:64000\n\t"
    "ds_read_b64 v[86:87], v252 offset:64000\n\t"
    "ds_read_b64 v[88:89], v181 offset:64000\n\t"
    "ds_read_b64 v[90:91], v253 offset:64000\n\t"
    "v_lshrrev_b32 v254, 16, v182\n\t"
    "v_and_b32 v182, s92, v182\n\t"
    "v_lshrrev_b32 v255, 16, v183\n\t"
    "v_and_b32 v183, s92, v183\n\t"
    "s_waitcnt vmcnt(6) lgkmcnt(4)\n\t"
    "v_smfmac_f32_16x16x64_bf16 v[116:119], v[120:123], v[92:99], v124\n\t"
    "ds_read_b64 v[92:93], v182 offset:64000\n\t"
    "ds_read_b64 v[94:95], v254 offset:64000\n\t"
    "ds_read_b64 v[96:97], v183 offset:64000\n\t"
    "ds_read_b64 v[98:99], v255 offset:64000\n\t"
    "s_sub_u32 s89, s89, 0x8000\n\t"
    "buffer_load_dwordx4 v[204:207], v127, %12, s89 offen offset:1024 sc1\n\t"
    "v_lshrrev_b32 v252, 16, v184\n\t"
    "v_and_b32 v184, s92, v184\n\t"
    "v_lshrrev_b32 v253, 16, v185\n\t"
    "v_cvt_pk_bf16_f32 v28, v116, v117\n\t"
    "v_cvt_pk_bf16_f32 v29, v118, v119\n\t"
    "v_and_b32 v119, s67, v31\n\t"
    "v_lshlrev_b32 v118, 16, v31\n\t"
    "v_and_b32 v117, s67, v30\n\t"
    "v_lshlrev_b32 v116, 16, v30\n\t"
    "v_and_b32 v185, s92, v185\n\t"
    "s_waitcnt lgkmcnt(4)\n\t"
    "v_smfmac_f32_16x16x64_bf16 v[116:119], v[120:123], v[84:91], v124\n\t"
    "ds_read_b64 v[84:85], v184 offset:0\n\t"
    "ds_read_b64 v[86:87], v252 offset:0\n\t"
    "ds_read_b64 v[88:89], v185 offset:0\n\t"
    "ds_read_b64 v[90:91], v253 offset:0\n\t"
    "v_lshrrev_b32 v254, 16, v186\n\t"
    "v_and_b32 v186, s92, v186\n\t"
    "v_lshrrev_b32 v255, 16, v187\n\t"
    "v_and_b32 v187, s92, v187\n\t"
    "s_cmp_lt_u32 %16, 4\n\t"
    "s_cbranch_scc1 L_tp175_%=\n\t"
    "s_setprio 1\n\t"
    "L_tp175_%=:\n\t"
    "s_waitcnt vmcnt(6) lgkmcnt(4)\n\t"
    "v_smfmac_f32_16x16x64_bf16 v[116:119], v[120:123], v[92:99], v124\n\t"
    "ds_read_b64 v[92:93], v186 offset:0\n\t"
    "ds_read_b64 v[94:95], v254 offset:0\n\t"
    "ds_read_b64 v[96:97], v187 offset:0\n\t"
    "ds_read_b64 v[98:99], v255 offset:0\n\t"
    "s_add_u32 s89, s89, 0x8000\n\t"
    "buffer_load_dwordx4 v[144:147], v127, %12, s89 offen offset:1024 sc1\n\t"
    "v_lshrrev_b32 v252, 16, v188\n\t"
    "v_and_b32 v188, s92, v188\n\t"
    "v_lshrrev_b32 v253, 16, v189\n\t"
    "v_cvt_pk_bf16_f32 v30, v116, v117\n\t"
    "v_cvt_pk_bf16_f32 v31, v118, v119\n\t"
    "v_and_b32 v119, s67, v37\n\t"
    "v_lshlrev_b32 v118, 16, v37\n\t"
    "v_and_b32 v117, s67, v36\n\t"
    "v_lshlrev_b32 v116, 16, v36\n\t"
    "v_and_b32 v189, s92, v189\n\t"
    "s_waitcnt lgkmcnt(4)\n\t"
    "v_smfmac_f32_16x16x64_bf16 v[116:119], v[120:123], v[84:91], v124\n\t"
    "ds_read_b64 v[84:85], v188 offset:64000\n\t"
    "ds_read_b64 v[86:87], v252 offset:64000\n\t"
    "ds_read_b64 v[88:89], v189 offset:64000\n\t"
    "ds_read_b64 v[90:91], v253 offset:64000\n\t"
    "v_lshrrev_b32 v254, 16, v190\n\t"
    "v_and_b32 v190, s92, v190\n\t"
    "v_lshrrev_b32 v255, 16, v191\n\t"
    "v_and_b32 v191, s92, v191\n\t"
    "s_waitcnt vmcnt(6) lgkmcnt(4)\n\t"
    "v_smfmac_f32_16x16x64_bf16 v[116:119], v[120:123], v[92:99], v124\n\t"
    "ds_read_b64 v[92:93], v190 offset:64000\n\t"
    "ds_read_b64 v[94:95], v254 offset:64000\n\t"
    "ds_read_b64 v[96:97], v191 offset:64000\n\t"
    "ds_read_b64 v[98:99], v255 offset:64000\n\t"
    "s_add_u32 s90, s90, s95\n\t"
    "buffer_load_dwordx4 v[148:151], v127, %12, s90 offen offset:0 sc1\n\t"
    "v_lshrrev_b32 v252, 16, v236\n\t"
    "v_and_b32 v236, s92, v236\n\t"
    "v_lshrrev_b32 v253, 16, v237\n\t"
    "v_and_b32 v237, s92, v237\n\t"
    "s_waitcnt lgkmcnt(4)\n\t"
    "v_smfmac_f32_16x16x64_bf16 v[116:119], v[120:123], v[84:91], v124\n\t"
    "ds_read_b64 v[84:85], v236 offset:0\n\t"
    "ds_read_b64 v[86:87], v252 offset:0\n\t"
    "ds_read_b64 v[88:89], v237 offset:0\n\t"
    "ds_read_b64 v[90:91], v253 offset:0\n\t"
    "v_lshrrev_b32 v254, 16, v238\n\t"
    "v_and_b32 v238, s92, v238\n\t"
    "v_lshrrev_b32 v255, 16, v239\n\t"
    "v_and_b32 v239, s92, v239\n\t"
    "s_waitcnt vmcnt(6) lgkmcnt(4)\n\t"
    "v_smfmac_f32_16x16x64_bf16 v[116:119], v[120:123], v[92:99], v124\n\t"
    "ds_read_b64 v[92:93], v238 offset:0\n\t"
    "ds_read_b64 v[94:95], v254 offset:0\n\t"
    "ds_read_b64 v[96:97], v239 offset:0\n\t"
    "ds_read_b64 v[98:99], v255 offset:0\n\t"
    "s_add_u32 s90, s90, 0x8000\n\t"
    "buffer_load_dwordx4 v[152:155], v127, %12, s90 offen offset:0 sc1\n\t"
    "v_lshrrev_b32 v252, 16, v192\n\t"
    "v_and_b32 v192, s92, v192\n\t"
    "v_lshrrev_b32 v253, 16, v193\n\t"
    "v_cvt_pk_bf16_f32 v36, v116, v117\n\t"
    "v_cvt_pk_bf16_f32 v37, v118, v119\n\t"
    "v_and_b32 v119, s67, v39\n\t"
    "v_lshlrev_b32 v118, 16, v39\n\t"
    "v_and_b32 v117, s67, v38\n\t"
    "v_lshlrev_b32 v116, 16, v38\n\t"
    "v_and_b32 v193, s92, v193\n\t"
    "s_waitcnt vmcnt(4)\n\t"
    "s_mov_b64 exec, 0xffffffff\n\t"
    "buffer_store_dword v165, v125, %12, s96 offen offset:0 sc1\n\t"
    "s_mov_b64 exec, -1\n\t"
    "s_add_u32 s61, s61, 0x2000\n\t"
    "buffer_store_dwordx4 v[20:23], v127, %12, s61 offen sc1\n\t"
    "s_waitcnt lgkmcnt(4)\n\t"
    "v_smfmac_f32_16x16x64_bf16 v[116:119], v[120:123], v[84:91], v124\n\t"
    "ds_read_b64 v[84:85], v192 offset:64000\n\t"
    "ds_read_b64 v[86:87], v252 offset:64000\n\t"
    "ds_read_b64 v[88:89], v193 offset:64000\n\t"
    "ds_read_b64 v[90:91], v253 offset:64000\n\t"
    "v_lshrrev_b32 v254, 16, v194\n\t"
    "v_and_b32 v194, s92, v194\n\t"
    "v_lshrrev_b32 v255, 16, v195\n\t"
    "v_and_b32 v195, s92, v195\n\t"
    "s_waitcnt lgkmcnt(4)\n\t"
    "v_smfmac_f32_16x16x64_bf16 v[116:119], v[120:123], v[92:99], v124\n\t"
    "ds_read_b64 v[92:93], v194 offset:64000\n\t"
    "ds_read_b64 v[94:95], v254 offset:64000\n\t"
    "ds_read_b64 v[96:97], v195 offset:64000\n\t"
    "ds_read_b64 v[98:99], v255 offset:64000\n\t"
    "s_sub_u32 s90, s90, 0x8000\n\t"
    "buffer_load_dwordx4 v[240:243], v127, %12, s90 offen offset:1024 sc1\n\t"
    "v_lshrrev_b32 v252, 16, v196\n\t"
    "v_and_b32 v196, s92, v196\n\t"
    "v_lshrrev_b32 v253, 16, v197\n\t"
    "v_and_b32 v197, s92, v197\n\t"
    "s_waitcnt lgkmcnt(4)\n\t"
    "v_smfmac_f32_16x16x64_bf16 v[116:119], v[120:123], v[84:91], v124\n\t"
    "ds_read_b64 v[84:85], v196 offset:0\n\t"
    "ds_read_b64 v[86:87], v252 offset:0\n\t"
    "ds_read_b64 v[88:89], v197 offset:0\n\t"
    "ds_read_b64 v[90:91], v253 offset:0\n\t"
    "v_lshrrev_b32 v254, 16, v198\n\t"
    "v_and_b32 v198, s92, v198\n\t"
    "v_lshrrev_b32 v255, 16, v199\n\t"
    "v_and_b32 v199, s92, v199\n\t"
    "s_waitcnt lgkmcnt(4)\n\t"
    "v_smfmac_f32_16x16x64_bf16 v[116:119], v[120:123], v[92:99], v124\n\t"
    "ds_read_b64 v[92:93], v198 offset:0\n\t"
    "ds_read_b64 v[94:95], v254 offset:0\n\t"
    "ds_read_b64 v[96:97], v199 offset:0\n\t"
    "ds_read_b64 v[98:99], v255 offset:0\n\t"
    "s_add_u32 s90, s90, 0x8000\n\t"
    "buffer_load_dwordx4 v[208:211], v127, %12, s90 offen offset:1024 sc1\n\t"
    "v_lshrrev_b32 v252, 16, v200\n\t"
    "v_and_b32 v200, s92, v200\n\t"
    "v_lshrrev_b32 v253, 16, v201\n\t"
    "v_cvt_pk_bf16_f32 v38, v116, v117\n\t"
    "v_cvt_pk_bf16_f32 v39, v118, v119\n\t"
    "v_and_b32 v119, s67, v45\n\t"
    "v_lshlrev_b32 v118, 16, v45\n\t"
    "v_and_b32 v117, s67, v44\n\t"
    "v_lshlrev_b32 v116, 16, v44\n\t"
    "v_and_b32 v201, s92, v201\n\t"
    "s_waitcnt lgkmcnt(4)\n\t"
    "v_smfmac_f32_16x16x64_bf16 v[116:119], v[120:123], v[84:91], v124\n\t"
    "ds_read_b64 v[84:85], v200 offset:64000\n\t"
    "ds_read_b64 v[86:87], v252 offset:64000\n\t"
    "ds_read_b64 v[88:89], v201 offset:64000\n\t"
    "ds_read_b64 v[90:91], v253 offset:64000\n\t"
    "v_lshrrev_b32 v254, 16, v202\n\t"
    "v_and_b32 v202, s92, v202\n\t"
    "v_lshrrev_b32 v255, 16, v203\n\t"
    "v_and_b32 v203, s92, v203\n\t"
    "s_waitcnt vmcnt(7) lgkmcnt(4)\n\t"
    "v_smfmac_f32_16x16x64_bf16 v[116:119], v[120:123], v[92:99], v124\n\t"
    "ds_read_b64 v[92:93], v202 offset:64000\n\t"
    "ds_read_b64 v[94:95], v254 offset:64000\n\t"
    "ds_read_b64 v[96:97], v203 offset:64000\n\t"
    "ds_read_b64 v[98:99], v255 offset:64000\n\t"
    "s_add_u32 s91, s91, s95\n\t"
    "buffer_load_dwordx4 v[244:247], v127, %12, s91 offen offset:0 sc1\n\t"
    "v_lshrrev_b32 v252, 16, v204\n\t"
    "v_and_b32 v204, s92, v204\n\t"
    "v_lshrrev_b32 v253, 16, v205\n\t"
    "v_and_b32 v205, s92, v205\n\t"
    "s_waitcnt lgkmcnt(4)\n\t"
    "v_smfmac_f32_16x16x64_bf16 v[116:119], v[120:123], v[84:91], v124\n\t"
    "ds_read_b64 v[84:85], v204 offset:0\n\t"
    "ds_read_b64 v[86:87], v252 offset:0\n\t"
    "ds_read_b64 v[88:89], v205 offset:0\n\t"
    "ds_read_b64 v[90:91], v253 offset:0\n\t"
    "v_lshrrev_b32 v254, 16, v206\n\t"
    "v_and_b32 v206, s92, v206\n\t"
    "v_lshrrev_b32 v255, 16, v207\n\t"
    "v_and_b32 v207, s92, v207\n\t"
    "s_waitcnt vmcnt(7) lgkmcnt(4)\n\t"
    "v_smfmac_f32_16x16x64_bf16 v[116:119], v[120:123], v[92:99], v124\n\t"
    "ds_read_b64 v[92:93], v206 offset:0\n\t"
    "ds_read_b64 v[94:95], v254 offset:0\n\t"
    "ds_read_b64 v[96:97], v207 offset:0\n\t"
    "ds_read_b64 v[98:99], v255 offset:0\n\t"
    "s_add_u32 s91, s91, 0x8000\n\t"
    "buffer_load_dwordx4 v[248:251], v127, %12, s91 offen offset:0 sc1\n\t"
    "v_lshrrev_b32 v252, 16, v144\n\t"
    "v_and_b32 v144, s92, v144\n\t"
    "v_lshrrev_b32 v253, 16, v145\n\t"
    "v_cvt_pk_bf16_f32 v44, v116, v117\n\t"
    "v_cvt_pk_bf16_f32 v45, v118, v119\n\t"
    "v_and_b32 v119, s67, v47\n\t"
    "v_lshlrev_b32 v118, 16, v47\n\t"
    "v_and_b32 v117, s67, v46\n\t"
    "v_lshlrev_b32 v116, 16, v46\n\t"
    "v_and_b32 v145, s92, v145\n\t"
    "s_add_u32 s61, s61, 0x2000\n\t"
    "buffer_store_dwordx4 v[28:31], v127, %12, s61 offen sc1\n\t"
    "s_waitcnt lgkmcnt(4)\n\t"
    "v_smfmac_f32_16x16x64_bf16 v[116:119], v[120:123], v[84:91], v124\n\t"
    "ds_read_b64 v[84:85], v144 offset:64000\n\t"
    "ds_read_b64 v[86:87], v252 offset:64000\n\t"
    "ds_read_b64 v[88:89], v145 offset:64000\n\t"
    "ds_read_b64 v[90:91], v253 offset:64000\n\t"
    "v_lshrrev_b32 v254, 16, v146\n\t"
    "v_and_b32 v146, s92, v146\n\t"
    "v_lshrrev_b32 v255, 16, v147\n\t"
    "v_and_b32 v147, s92, v147\n\t"
    "s_waitcnt vmcnt(8) lgkmcnt(4)\n\t"
    "v_smfmac_f32_16x16x64_bf16 v[116:119], v[120:123], v[92:99], v124\n\t"
    "ds_read_b64 v[92:93], v146 offset:64000\n\t"
    "ds_read_b64 v[94:95], v254 offset:64000\n\t"
    "ds_read_b64 v[96:97], v147 offset:64000\n\t"
    "ds_read_b64 v[98:99], v255 offset:64000\n\t"
    "s_sub_u32 s91, s91, 0x8000\n\t"
    "buffer_load_dwordx4 v[212:215], v127, %12, s91 offen offset:1024 sc1\n\t"
    "v_lshrrev_b32 v252, 16, v148\n\t"
    "v_and_b32 v148, s92, v148\n\t"
    "v_lshrrev_b32 v253, 16, v149\n\t"
    "v_and_b32 v149, s92, v149\n\t"
    "s_waitcnt lgkmcnt(4)\n\t"
    "v_smfmac_f32_16x16x64_bf16 v[116:119], v[120:123], v[84:91], v124\n\t"
    "ds_read_b64 v[84:85], v148 offset:0\n\t"
    "ds_read_b64 v[86:87], v252 offset:0\n\t"
    "ds_read_b64 v[88:89], v149 offset:0\n\t"
    "ds_read_b64 v[90:91], v253 offset:0\n\t"
    "v_lshrrev_b32 v254, 16, v150\n\t"
    "v_and_b32 v150, s92, v150\n\t"
    "v_lshrrev_b32 v255, 16, v151\n\t"
    "v_and_b32 v151, s92, v151\n\t"
    "s_waitcnt vmcnt(8) lgkmcnt(4)\n\t"
    "v_smfmac_f32_16x16x64_bf16 v[116:119], v[120:123], v[92:99], v124\n\t"
    "ds_read_b64 v[92:93], v150 offset:0\n\t"
    "ds_read_b64 v[94:95], v254 offset:0\n\t"
    "ds_read_b64 v[96:97], v151 offset:0\n\t"
    "ds_read_b64 v[98:99], v255 offset:0\n\t"
    "s_add_u32 s91, s91, 0x8000\n\t"
    "buffer_load_dwordx4 v[216:219], v127, %12, s91 offen offset:1024 sc1\n\t"
    "v_lshrrev_b32 v252, 16, v152\n\t"
    "v_and_b32 v152, s92, v152\n\t"
    "v_lshrrev_b32 v253, 16, v153\n\t"
    "v_cvt_pk_bf16_f32 v46, v116, v117\n\t"
    "v_cvt_pk_bf16_f32 v47, v118, v119\n\t"
    "v_and_b32 v119, s67, v53\n\t"
    "v_lshlrev_b32 v118, 16, v53\n\t"
    "v_and_b32 v117, s67, v52\n\t"
    "v_lshlrev_b32 v116, 16, v52\n\t"
    "v_and_b32 v153, s92, v153\n\t"
    "s_waitcnt lgkmcnt(4)\n\t"
    "v_smfmac_f32_16x16x64_bf16 v[116:119], v[120:123], v[84:91], v124\n\t"
    "ds_read_b64 v[84:85], v152 offset:64000\n\t"
    "ds_read_b64 v[86:87], v252 offset:64000\n\t"
    "ds_read_b64 v[88:89], v153 offset:64000\n\t"
    "ds_read_b64 v[90:91], v253 offset:64000\n\t"
    "v_lshrrev_b32 v254, 16, v154\n\t"
    "v_and_b32 v154, s92, v154\n\t"
    "v_lshrrev_b32 v255, 16, v155\n\t"
    "v_and_b32 v155, s92, v155\n\t"
    "s_waitcnt vmcnt(6) lgkmcnt(4)\n\t"
    "v_smfmac_f32_16x16x64_bf16 v[116:119], v[120:123], v[92:99], v124\n\t"
    "ds_read_b64 v[92:93], v154 offset:64000\n\t"
    "ds_read_b64 v[94:95], v254 offset:64000\n\t"
    "ds_read_b64 v[96:97], v155 offset:64000\n\t"
    "ds_read_b64 v[98:99], v255 offset:64000\n\t"
    "s_sub_u32 s84, s84, s95\n\t"
    "buffer_load_dwordx4 v[220:223], v127, %12, s84 offen offset:0 sc1\n\t"
    "v_lshrrev_b32 v252, 16, v240\n\t"
    "v_and_b32 v240, s92, v240\n\t"
    "v_lshrrev_b32 v253, 16, v241\n\t"
    "v_and_b32 v241, s92, v241\n\t"
    "s_waitcnt lgkmcnt(4)\n\t"
    "v_smfmac_f32_16x16x64_bf16 v[116:119], v[120:123], v[84:91], v124\n\t"
    "ds_read_b64 v[84:85], v240 offset:0\n\t"
    "ds_read_b64 v[86:87], v252 offset:0\n\t"
    "ds_read_b64 v[88:89], v241 offset:0\n\t"
    "ds_read_b64 v[90:91], v253 offset:0\n\t"
    "v_lshrrev_b32 v254, 16, v242\n\t"
    "v_and_b32 v242, s92, v242\n\t"
    "v_lshrrev_b32 v255, 16, v243\n\t"
    "v_and_b32 v243, s92, v243\n\t"
    "s_cmp_lt_u32 %16, 4\n\t"
    "s_cbranch_scc1 L_tp195_%=\n\t"
    "s_setprio 0\n\t"
    "L_tp195_%=:\n\t"
    "s_waitcnt vmcnt(6) lgkmcnt(4)\n\t"
    "v_smfmac_f32_16x16x64_bf16 v[116:119], v[120:123], v[92:99], v124\n\t"
    "ds_read_b64 v[92:93], v242 offset:0\n\t"
    "ds_read_b64 v[94:95], v254 offset:0\n\t"
    "ds_read_b64 v[96:97], v243 offset:0\n\t"
    "ds_read_b64 v[98:99], v255 offset:0\n\t"
    "buffer_load_dwordx4 v[224:227], v127, %12, s84 offen offset:1024 sc1\n\t"
    "v_lshrrev_b32 v252, 16, v208\n\t"
    "v_and_b32 v208, s92, v208\n\t"
    "v_lshrrev_b32 v253, 16, v209\n\t"
    "v_cvt_pk_bf16_f32 v52, v116, v117\n\t"
    "v_cvt_pk_bf16_f32 v53, v118, v119\n\t"
    "v_and_b32 v119, s67, v55\n\t"
    "v_lshlrev_b32 v118, 16, v55\n\t"
    "v_and_b32 v117, s67, v54\n\t"
    "v_lshlrev_b32 v116, 16, v54\n\t"
    "v_and_b32 v209, s92, v209\n\t"
    "s_waitcnt vmcnt(4)\n\t"
    "s_mov_b64 exec, 0xffffffff\n\t"
    "buffer_store_dword v165, v125, %12, s96 offen offset:128 sc1\n\t"
    "s_mov_b64 exec, -1\n\t"
    "s_add_u32 s61, s61, 0x2000\n\t"
    "buffer_store_dwordx4 v[36:39], v127, %12, s61 offen sc1\n\t"
    "s_waitcnt lgkmcnt(4)\n\t"
    "v_smfmac_f32_16x16x64_bf16 v[116:119], v[120:123], v[84:91], v124\n\t"
    "ds_read_b64 v[84:85], v208 offset:64000\n\t"
    "ds_read_b64 v[86:87], v252 offset:64000\n\t"
    "ds_read_b64 v[88:89], v209 offset:64000\n\t"
    "ds_read_b64 v[90:91], v253 offset:64000\n\t"
    "v_lshrrev_b32 v254, 16, v210\n\t"
    "v_and_b32 v210, s92, v210\n\t"
    "v_lshrrev_b32 v255, 16, v211\n\t"
    "v_and_b32 v211, s92, v211\n\t"
    "s_waitcnt lgkmcnt(4)\n\t"
    "v_smfmac_f32_16x16x64_bf16 v[116:119], v[120:123], v[92:99], v124\n\t"
    "ds_read_b64 v[92:93], v210 offset:64000\n\t"
    "ds_read_b64 v[94:95], v254 offset:64000\n\t"
    "ds_read_b64 v[96:97], v211 offset:64000\n\t"
    "ds_read_b64 v[98:99], v255 offset:64000\n\t"
    "s_sub_u32 s85, s85, s95\n\t"
    "buffer_load_dwordx4 v[228:231], v127, %12, s85 offen offset:0 sc1\n\t"
    "v_lshrrev_b32 v252, 16, v244\n\t"
    "v_and_b32 v244, s92, v244\n\t"
    "v_lshrrev_b32 v253, 16, v245\n\t"
    "v_and_b32 v245, s92, v245\n\t"
    "s_waitcnt lgkmcnt(4)\n\t"
    "v_smfmac_f32_16x16x64_bf16 v[116:119], v[120:123], v[84:91], v124\n\t"
    "ds_read_b64 v[84:85], v244 offset:0\n\t"
    "ds_read_b64 v[86:87], v252 offset:0\n\t"
    "ds_read_b64 v[88:89], v245 offset:0\n\t"
    "ds_read_b64 v[90:91], v253 offset:0\n\t"
    "v_lshrrev_b32 v254, 16, v246\n\t"
    "v_and_b32 v246, s92, v246\n\t"
    "v_lshrrev_b32 v255, 16, v247\n\t"
    "v_and_b32 v247, s92, v247\n\t"
    "s_waitcnt lgkmcnt(4)\n\t"
    "v_smfmac_f32_16x16x64_bf16 v[116:119], v[120:123], v[92:99], v124\n\t"
    "ds_read_b64 v[92:93], v246 offset:0\n\t"
    "ds_read_b64 v[94:95], v254 offset:0\n\t"
    "ds_read_b64 v[96:97], v247 offset:0\n\t"
    "ds_read_b64 v[98:99], v255 offset:0\n\t"
    "buffer_load_dwordx4 v[232:235], v127, %12, s85 offen offset:1024 sc1\n\t"
    "v_lshrrev_b32 v252, 16, v248\n\t"
    "v_and_b32 v248, s92, v248\n\t"
    "v_lshrrev_b32 v253, 16, v249\n\t"
    "v_cvt_pk_bf16_f32 v54, v116, v117\n\t"
    "v_cvt_pk_bf16_f32 v55, v118, v119\n\t"
    "v_and_b32 v119, s67, v61\n\t"
    "v_lshlrev_b32 v118, 16, v61\n\t"
    "v_and_b32 v117, s67, v60\n\t"
    "v_lshlrev_b32 v116, 16, v60\n\t"
    "v_and_b32 v249, s92, v249\n\t"
    "s_waitcnt lgkmcnt(4)\n\t"
    "v_smfmac_f32_16x16x64_bf16 v[116:119], v[120:123], v[84:91], v124\n\t"
    "ds_read_b64 v[84:85], v248 offset:64000\n\t"
    "ds_read_b64 v[86:87], v252 offset:64000\n\t"
    "ds_read_b64 v[88:89], v249 offset:64000\n\t"
    "ds_read_b64 v[90:91], v253 offset:64000\n\t"
    "v_lshrrev_b32 v254, 16, v250\n\t"
    "v_and_b32 v250, s92, v250\n\t"
    "v_lshrrev_b32 v255, 16, v251\n\t"
    "v_and_b32 v251, s92, v251\n\t"
    "s_waitcnt vmcnt(7) lgkmcnt(4)\n\t"
    "v_smfmac_f32_16x16x64_bf16 v[116:119], v[120:123], v[92:99], v124\n\t"
    "ds_read_b64 v[92:93], v250 offset:64000\n\t"
    "ds_read_b64 v[94:95], v254 offset:64000\n\t"
    "ds_read_b64 v[96:97], v251 offset:64000\n\t"
    "ds_read_b64 v[98:99], v255 offset:64000\n\t"
    "s_sub_u32 s86, s86, s95\n\t"
    "buffer_load_dwordx4 v[168:171], v127, %12, s86 offen offset:0 sc1\n\t"
    "v_lshrrev_b32 v252, 16, v212\n\t"
    "v_and_b32 v212, s92, v212\n\t"
    "v_lshrrev_b32 v253, 16, v213\n\t"
    "v_and_b32 v213, s92, v213\n\t"
    "s_waitcnt lgkmcnt(4)\n\t"
    "v_smfmac_f32_16x16x64_bf16 v[116:119], v[120:123], v[84:91], v124\n\t"
    "ds_read_b64 v[84:85], v212 offset:0\n\t"
    "ds_read_b64 v[86:87], v252 offset:0\n\t"
    "ds_read_b64 v[88:89], v213 offset:0\n\t"
    "ds_read_b64 v[90:91], v253 offset:0\n\t"
    "v_lshrrev_b32 v254, 16, v214\n\t"
    "v_and_b32 v214, s92, v214\n\t"
    "v_lshrrev_b32 v255, 16, v215\n\t"
    "v_and_b32 v215, s92, v215\n\t"
    "s_waitcnt vmcnt(7) lgkmcnt(4)\n\t"
    "v_smfmac_f32_16x16x64_bf16 v[116:119], v[120:123], v[92:99], v124\n\t"
    "ds_read_b64 v[92:93], v214 offset:0\n\t"
    "ds_read_b64 v[94:95], v254 offset:0\n\t"
    "ds_read_b64 v[96:97], v215 offset:0\n\t"
    "ds_read_b64 v[98:99], v255 offset:0\n\t"
    "s_add_u32 s84, s84, 0x8000\n\t"
    "buffer_load_dwordx4 v[112:115], v127, %12, s84 offen offset:0 sc1\n\t"
    "v_lshrrev_b32 v252, 16, v216\n\t"
    "v_and_b32 v216, s92, v216\n\t"
    "v_lshrrev_b32 v253, 16, v217\n\t"
    "v_cvt_pk_bf16_f32 v60, v116, v117\n\t"
    "v_cvt_pk_bf16_f32 v61, v118, v119\n\t"
    "v_and_b32 v119, s67, v63\n\t"
    "v_lshlrev_b32 v118, 16, v63\n\t"
    "v_and_b32 v117, s67, v62\n\t"
    "v_lshlrev_b32 v116, 16, v62\n\t"
    "v_and_b32 v217, s92, v217\n\t"
    "s_add_u32 s61, s61, 0x2000\n\t"
    "buffer_store_dwordx4 v[44:47], v127, %12, s61 offen sc1\n\t"
    "s_waitcnt lgkmcnt(4)\n\t"
    "v_smfmac_f32_16x16x64_bf16 v[116:119], v[120:123], v[84:91], v124\n\t"
    "ds_read_b64 v[84:85], v216 offset:64000\n\t"
    "ds_read_b64 v[86:87], v252 offset:64000\n\t"
    "ds_read_b64 v[88:89], v217 offset:64000\n\t"
    "ds_read_b64 v[90:91], v253 offset:64000\n\t"
    "v_lshrrev_b32 v254, 16, v218\n\t"
    "v_and_b32 v218, s92, v218\n\t"
    "v_lshrrev_b32 v255, 16, v219\n\t"
    "v_and_b32 v219, s92, v219\n\t"
    "s_mov_b64 exec, 1\n\t"
    "buffer_load_dword v164, off, %12, s93 offset:0 sc1\n\t"
    "s_mov_b64 exec, -1\n\t"
    "s_waitcnt vmcnt(9) lgkmcnt(4)\n\t"
    "v_smfmac_f32_16x16x64_bf16 v[116:119], v[120:123], v[92:99], v124\n\t"
    "ds_read_b64 v[92:93], v218 offset:64000\n\t"
    "ds_read_b64 v[94:95], v254 offset:64000\n\t"
    "ds_read_b64 v[96:97], v219 offset:64000\n\t"
    "ds_read_b64 v[98:99], v255 offset:64000\n\t"
    "buffer_load_dwordx4 v[100:103], v127, %12, s86 offen offset:1024 sc1\n\t"
    "v_lshrrev_b32 v252, 16, v220\n\t"
    "v_and_b32 v220, s92, v220\n\t"
    "v_lshrrev_b32 v253, 16, v221\n\t"
    "v_and_b32 v221, s92, v221\n\t"
    "s_waitcnt lgkmcnt(4)\n\t"
    "v_smfmac_f32_16x16x64_bf16 v[116:119], v[120:123], v[84:91], v124\n\t"
    "ds_read_b64 v[84:85], v220 offset:0\n\t"
    "ds_read_b64 v[86:87], v252 offset:0\n\t"
    "ds_read_b64 v[88:89], v221 offset:0\n\t"
    "ds_read_b64 v[90:91], v253 offset:0\n\t"
    "v_lshrrev_b32 v254, 16, v222\n\t"
    "v_and_b32 v222, s92, v222\n\t"
    "v_lshrrev_b32 v255, 16, v223\n\t"
    "v_and_b32 v223, s92, v223\n\t"
    "s_cmp_lt_u32 %16, 4\n\t"
    "s_cbranch_scc1 L_tp207_%=\n\t"
    "s_setprio 1\n\t"
    "L_tp207_%=:\n\t"
    "s_waitcnt vmcnt(9) lgkmcnt(4)\n\t"
    "v_smfmac_f32_16x16x64_bf16 v[116:119], v[120:123], v[92:99], v124\n\t"
    "ds_read_b64 v[92:93], v222 offset:0\n\t"
    "ds_read_b64 v[94:95], v254 offset:0\n\t"
    "ds_read_b64 v[96:97], v223 offset:0\n\t"
    "ds_read_b64 v[98:99], v255 offset:0\n\t"
    "buffer_load_dwordx4 v[104:107], v127, %12, s84 offen offset:1024 sc1\n\t"
    "v_lshrrev_b32 v252, 16, v224\n\t"
    "v_and_b32 v224, s92, v224\n\t"
    "v_lshrrev_b32 v253, 16, v225\n\t"
    "v_cvt_pk_bf16_f32 v62, v116, v117\n\t"
    "v_cvt_pk_bf16_f32 v63, v118, v119\n\t"
    "v_mov_b64 v[172:173], s[70:71]\n\t"
    "v_mov_b64 v[174:175], s[72:73]\n\t"
    "v_and_b32 v225, s92, v225\n\t"
    "s_waitcnt lgkmcnt(4)\n\t"
    "v_smfmac_f32_16x16x64_bf16 v[172:175], v[120:123], v[84:91], v124\n\t"
    "ds_read_b64 v[84:85], v224 offset:0\n\t"
    "ds_read_b64 v[86:87], v252 offset:0\n\t"
    "ds_read_b64 v[88:89], v225 offset:0\n\t"
    "ds_read_b64 v[90:91], v253 offset:0\n\t"
    "v_lshrrev_b32 v254, 16, v226\n\t"
    "v_and_b32 v226, s92, v226\n\t"
    "v_lshrrev_b32 v255, 16, v227\n\t"
    "v_and_b32 v227, s92, v227\n\t"
    "s_waitcnt vmcnt(7) lgkmcnt(4)\n\t"
    "v_smfmac_f32_16x16x64_bf16 v[172:175], v[120:123], v[92:99], v124\n\t"
    "ds_read_b64 v[92:93], v226 offset:0\n\t"
    "ds_read_b64 v[94:95], v254 offset:0\n\t"
    "ds_read_b64 v[96:97], v227 offset:0\n\t"
    "ds_read_b64 v[98:99], v255 offset:0\n\t"
    "s_sub_u32 s87, s87, s95\n\t"
    "buffer_load_dwordx4 v[108:111], v127, %12, s87 offen offset:0 sc1\n\t"
    "v_lshrrev_b32 v252, 16, v228\n\t"
    "v_and_b32 v228, s92, v228\n\t"
    "v_lshrrev_b32 v253, 16, v229\n\t"
    "v_mov_b64 v[176:177], s[70:71]\n\t"
    "v_mov_b64 v[178:179], s[72:73]\n\t"
    "v_and_b32 v229, s92, v229\n\t"
    "s_waitcnt lgkmcnt(4)\n\t"
    "v_smfmac_f32_16x16x64_bf16 v[176:179], v[120:123], v[84:91], v124\n\t"
    "ds_read_b64 v[84:85], v228 offset:0\n\t"
    "ds_read_b64 v[86:87], v252 offset:0\n\t"
    "ds_read_b64 v[88:89], v229 offset:0\n\t"
    "ds_read_b64 v[90:91], v253 offset:0\n\t"
    "v_lshrrev_b32 v254, 16, v230\n\t"
    "v_and_b32 v230, s92, v230\n\t"
    "v_lshrrev_b32 v255, 16, v231\n\t"
    "v_and_b32 v231, s92, v231\n\t"
    "s_waitcnt vmcnt(3)\n\t"
    "v_readfirstlane_b32 s65, v164\n\t"
    "s_nop 0\n\t"
    "s_cmp_eq_u32 s65, 0\n\t"
    "s_cbranch_scc1 L_xw0_%=\n\t"
    "L_xwb0_%=:\n\t"
    "buffer_load_dwordx4 v[4:7], v127, %12, s94 offen sc1\n\t"
    "s_waitcnt lgkmcnt(4)\n\t"
    "v_smfmac_f32_16x16x64_bf16 v[176:179], v[120:123], v[92:99], v124\n\t"
    "ds_read_b64 v[92:93], v230 offset:0\n\t"
    "ds_read_b64 v[94:95], v254 offset:0\n\t"
    "ds_read_b64 v[96:97], v231 offset:0\n\t"
    "ds_read_b64 v[98:99], v255 offset:0\n\t"
    "s_add_u32 s85, s85, 0x8000\n\t"
    "buffer_load_dwordx4 v[200:203], v127, %12, s85 offen offset:0 sc1\n\t"
    "v_lshrrev_b32 v252, 16, v232\n\t"
    "v_and_b32 v232, s92, v232\n\t"
    "v_lshrrev_b32 v253, 16, v233\n\t"
    "v_mov_b64 v[180:181], s[70:71]\n\t"
    "v_mov_b64 v[182:183], s[72:73]\n\t"
    "v_and_b32 v233, s92, v233\n\t"
    "s_mov_b64 exec, 0xffffffff\n\t"
    "buffer_store_dword v165, v125, %12, s96 offen offset:256 sc1\n\t"
    "s_mov_b64 exec, -1\n\t"
    "s_add_u32 s61, s61, 0x2000\n\t"
    "buffer_store_dwordx4 v[52:55], v127, %12, s61 offen sc1\n\t"
    "s_waitcnt lgkmcnt(4)\n\t"
    "v_smfmac_f32_16x16x64_bf16 v[180:183], v[120:123], v[84:91], v124\n\t"
    "ds_read_b64 v[84:85], v232 offset:0\n\t"
    "ds_read_b64 v[86:87], v252 offset:0\n\t"
    "ds_read_b64 v[88:89], v233 offset:0\n\t"
    "ds_read_b64 v[90:91], v253 offset:0\n\t"
    "v_lshrrev_b32 v254, 16, v234\n\t"
    "v_and_b32 v234, s92, v234\n\t"
    "v_lshrrev_b32 v255, 16, v235\n\t"
    "v_and_b32 v235, s92, v235\n\t"
    "s_waitcnt lgkmcnt(4)\n\t"
    "v_smfmac_f32_16x16x64_bf16 v[180:183], v[120:123], v[92:99], v124\n\t"
    "ds_read_b64 v[92:93], v234 offset:0\n\t"
    "ds_read_b64 v[94:95], v254 offset:0\n\t"
    "ds_read_b64 v[96:97], v235 offset:0\n\t"
    "ds_read_b64 v[98:99], v255 offset:0\n\t"
    "buffer_load_dwordx4 v[204:207], v127, %12, s87 offen offset:1024 sc1\n\t"
    "v_lshrrev_b32 v252, 16, v168\n\t"
    "v_and_b32 v168, s92, v168\n\t"
    "v_lshrrev_b32 v253, 16, v169\n\t"
    "v_mov_b64 v[184:185], s[70:71]\n\t"
    "v_mov_b64 v[186:187], s[72:73]\n\t"
    "v_and_b32 v169, s92, v169\n\t"
    "s_waitcnt lgkmcnt(4)\n\t"
    "v_smfmac_f32_16x16x64_bf16 v[184:187], v[120:123], v[84:91], v124\n\t"
    "ds_read_b64 v[84:85], v168 offset:0\n\t"
    "ds_read_b64 v[86:87], v252 offset:0\n\t"
    "ds_read_b64 v[88:89], v169 offset:0\n\t"
    "ds_read_b64 v[90:91], v253 offset:0\n\t"
    "v_lshrrev_b32 v254, 16, v170\n\t"
    "v_and_b32 v170, s92, v170\n\t"
    "v_lshrrev_b32 v255, 16, v171\n\t"
    "v_and_b32 v171, s92, v171\n\t"
    "s_add_u32 s94, s94, 0x2000\n\t"
    "buffer_load_dwordx4 v[12:15], v127, %12, s94 offen sc1\n\t"
    "s_waitcnt lgkmcnt(4)\n\t"
    "v_smfmac_f32_16x16x64_bf16 v[184:187], v[120:123], v[92:99], v124\n\t"
    "ds_read_b64 v[92:93], v170 offset:0\n\t"
    "ds_read_b64 v[94:95], v254 offset:0\n\t"
    "ds_read_b64 v[96:97], v171 offset:0\n\t"
    "ds_read_b64 v[98:99], v255 offset:0\n\t"
    "buffer_load_dwordx4 v[144:147], v127, %12, s85 offen offset:1024 sc1\n\t"
    "v_lshrrev_b32 v252, 16, v112\n\t"
    "v_and_b32 v112, s92, v112\n\t"
    "v_lshrrev_b32 v253, 16, v113\n\t"
    "v_mov_b64 v[188:189], s[70:71]\n\t"
    "v_mov_b64 v[190:191], s[72:73]\n\t"
    "v_and_b32 v113, s92, v113\n\t"
    "s_waitcnt lgkmcnt(4)\n\t"
    "v_smfmac_f32_16x16x64_bf16 v[188:191], v[120:123], v[84:91], v124\n\t"
    "ds_read_b64 v[84:85], v112 offset:64000\n\t"
    "ds_read_b64 v[86:87], v252 offset:64000\n\t"
    "ds_read_b64 v[88:89], v113 offset:64000\n\t"
    "ds_read_b64 v[90:91], v253 offset:64000\n\t"
    "v_lshrrev_b32 v254, 16, v114\n\t"
    "v_and_b32 v114, s92, v114\n\t"
    "v_lshrrev_b32 v255, 16, v115\n\t"
    "v_and_b32 v115, s92, v115\n\t"
    "s_waitcnt vmcnt(9) lgkmcnt(4)\n\t"
    "v_smfmac_f32_16x16x64_bf16 v[188:191], v[120:123], v[92:99], v124\n\t"
    "ds_read_b64 v[92:93], v114 offset:64000\n\t"
    "ds_read_b64 v[94:95], v254 offset:64000\n\t"
    "ds_read_b64 v[96:97], v115 offset:64000\n\t"
    "ds_read_b64 v[98:99], v255 offset:64000\n\t"
    "s_sub_u32 s88, s88, s95\n\t"
    "buffer_load_dwordx4 v[148:151], v127, %12, s88 offen offset:0 sc1\n\t"
    "v_lshrrev_b32 v252, 16, v100\n\t"
    "v_and_b32 v100, s92, v100\n\t"
    "v_lshrrev_b32 v253, 16, v101\n\t"
    "v_and_b32 v101, s92, v101\n\t"
    "s_waitcnt lgkmcnt(4)\n\t"
    "v_smfmac_f32_16x16x64_bf16 v[172:175], v[120:123], v[84:91], v124\n\t"
    "ds_read_b64 v[84:85], v100 offset:0\n\t"
    "ds_read_b64 v[86:87], v252 offset:0\n\t"
    "ds_read_b64 v[88:89], v101 offset:0\n\t"
    "ds_read_b64 v[90:91], v253 offset:0\n\t"
    "v_lshrrev_b32 v254, 16, v102\n\t"
    "v_and_b32 v102, s92, v102\n\t"
    "v_lshrrev_b32 v255, 16, v103\n\t"
    "v_and_b32 v103, s92, v103\n\t"
    "s_waitcnt vmcnt(7)\n\t"
    "v_smfmac_f32_16x16x64_bf16 v[172:175], v[156:159], v[0:7], v124\n\t"
    "s_waitcnt lgkmcnt(4)\n\t"
    "v_smfmac_f32_16x16x64_bf16 v[172:175], v[120:123], v[92:99], v124\n\t"
    "ds_read_b64 v[92:93], v102 offset:0\n\t"
    "ds_read_b64 v[94:95], v254 offset:0\n\t"
    "ds_read_b64 v[96:97], v103 offset:0\n\t"
    "ds_read_b64 v[98:99], v255 offset:0\n\t"
    "s_add_u32 s86, s86, 0x8000\n\t"
    "buffer_load_dwordx4 v[152:155], v127, %12, s86 offen offset:0 sc1\n\t"
    "v_lshrrev_b32 v252, 16, v104\n\t"
    "v_and_b32 v104, s92, v104\n\t"
    "v_lshrrev_b32 v253, 16, v105\n\t"
    "v_mov_b64 v[192:193], s[70:71]\n\t"
    "v_mov_b64 v[194:195], s[72:73]\n\t"
    "v_and_b32 v105, s92, v105\n\t"
    "s_add_u32 s61, s61, 0x2000\n\t"
    "buffer_store_dwordx4 v[60:63], v127, %12, s61 offen sc1\n\t"
    "v_smfmac_f32_16x16x64_bf16 v[176:179], v[160:163], v[0:7], v124\n\t"
    "v_pk_mul_f32 v[64:65], v[172:173], v[172:173]\n\t"
    "v_pk_mul_f32 v[66:67], v[174:175], v[174:175]\n\t"
    "v_pk_fma_f32 v[64:65], v[64:65], v[82:83], v[80:81]\n\t"
    "v_pk_fma_f32 v[66:67], v[66:67], v[82:83], v[80:81]\n\t"
    "v_pk_mul_f32 v[64:65], v[64:65], v[172:173]\n\t"
    "s_waitcnt lgkmcnt(4)\n\t"
    "v_smfmac_f32_16x16x64_bf16 v[192:195], v[120:123], v[84:91], v124\n\t"
    "ds_read_b64 v[84:85], v104 offset:64000\n\t"
    "ds_read_b64 v[86:87], v252 offset:64000\n\t"
    "ds_read_b64 v[88:89], v105 offset:64000\n\t"
    "ds_read_b64 v[90:91], v253 offset:64000\n\t"
    "v_lshrrev_b32 v254, 16, v106\n\t"
    "v_and_b32 v106, s92, v106\n\t"
    "v_lshrrev_b32 v255, 16, v107\n\t"
    "v_and_b32 v107, s92, v107\n\t"
    "s_mov_b64 exec, 1\n\t"
    "buffer_load_dword v164, off, %12, s93 offset:128 sc1\n\t"
    "s_mov_b64 exec, -1\n\t"
    "v_pk_mul_f32 v[66:67], v[66:67], v[174:175]\n\t"
    "v_exp_f32 v68, v64\n\t"
    "v_exp_f32 v69, v65\n\t"
    "v_exp_f32 v70, v66\n\t"
    "v_exp_f32 v71, v67\n\t"
    "s_waitcnt lgkmcnt(4)\n\t"
    "v_smfmac_f32_16x16x64_bf16 v[192:195], v[120:123], v[92:99], v124\n\t"
    "ds_read_b64 v[92:93], v106 offset:64000\n\t"
    "ds_read_b64 v[94:95], v254 offset:64000\n\t"
    "ds_read_b64 v[96:97], v107 offset:64000\n\t"
    "ds_read_b64 v[98:99], v255 offset:64000\n\t"
    "buffer_load_dwordx4 v[244:247], v127, %12, s88 offen offset:1024 sc1\n\t"
    "v_lshrrev_b32 v252, 16, v108\n\t"
    "v_and_b32 v108, s92, v108\n\t"
    "v_lshrrev_b32 v253, 16, v109\n\t"
    "v_and_b32 v109, s92, v109\n\t"
    "v_pk_fma_f32 v[68:69], v[68:69], s[40:41], s[40:41]\n\t"
    "v_pk_fma_f32 v[70:71], v[70:71], s[42:43], s[42:43]\n\t"
    "v_rcp_f32 v68, v68\n\t"
    "v_rcp_f32 v69, v69\n\t"
    "v_rcp_f32 v70, v70\n\t"
    "s_waitcnt lgkmcnt(4)\n\t"
    "v_smfmac_f32_16x16x64_bf16 v[176:179], v[120:123], v[84:91], v124\n\t"
    "ds_read_b64 v[84:85], v108 offset:0\n\t"
    "ds_read_b64 v[86:87], v252 offset:0\n\t"
    "ds_read_b64 v[88:89], v109 offset:0\n\t"
    "ds_read_b64 v[90:91], v253 offset:0\n\t"
    "v_lshrrev_b32 v254, 16, v110\n\t"
    "v_and_b32 v110, s92, v110\n\t"
    "v_lshrrev_b32 v255, 16, v111\n\t"
    "v_and_b32 v111, s92, v111\n\t"
    "s_waitcnt vmcnt(6)\n\t"
    "v_smfmac_f32_16x16x64_bf16 v[180:183], v[156:159], v[8:15], v124\n\t"
    "v_rcp_f32 v71, v71\n\t"
    "v_pk_mul_f32 v[68:69], v[68:69], v[172:173]\n\t"
    "v_pk_fma_f32 v[68:69], v[70:71], v[174:175], v[68:69]\n\t"
    "v_add_f32 v236, v68, v69\n\t"
    "s_waitcnt lgkmcnt(4)\n\t"
    "v_smfmac_f32_16x16x64_bf16 v[176:179], v[120:123], v[92:99], v124\n\t"
    "ds_read_b64 v[92:93], v110 offset:0\n\t"
    "ds_read_b64 v[94:95], v254 offset:0\n\t"
    "ds_read_b64 v[96:97], v111 offset:0\n\t"
    "ds_read_b64 v[98:99], v255 offset:0\n\t"
    "buffer_load_dwordx4 v[248:251], v127, %12, s86 offen offset:1024 sc1\n\t"
    "v_lshrrev_b32 v252, 16, v200\n\t"
    "v_and_b32 v200, s92, v200\n\t"
    "v_lshrrev_b32 v253, 16, v201\n\t"
    "v_mov_b64 v[196:197], s[70:71]\n\t"
    "v_mov_b64 v[198:199], s[72:73]\n\t"
    "v_and_b32 v201, s92, v201\n\t"
    "v_smfmac_f32_16x16x64_bf16 v[184:187], v[160:163], v[8:15], v124\n\t"
    "v_pk_mul_f32 v[72:73], v[176:177], v[176:177]\n\t"
    "v_pk_mul_f32 v[74:75], v[178:179], v[178:179]\n\t"
    "v_pk_fma_f32 v[72:73], v[72:73], v[82:83], v[80:81]\n\t"
    "v_pk_fma_f32 v[74:75], v[74:75], v[82:83], v[80:81]\n\t"
    "v_pk_mul_f32 v[72:73], v[72:73], v[176:177]\n\t"
    "s_waitcnt lgkmcnt(4)\n\t"
    "v_smfmac_f32_16x16x64_bf16 v[196:199], v[120:123], v[84:91], v124\n\t"
    "ds_read_b64 v[84:85], v200 offset:64000\n\t"
    "ds_read_b64 v[86:87], v252 offset:64000\n\t"
    "ds_read_b64 v[88:89], v201 offset:64000\n\t"
    "ds_read_b64 v[90:91], v253 offset:64000\n\t"
    "v_lshrrev_b32 v254, 16, v202\n\t"
    "v_and_b32 v202, s92, v202\n\t"
    "v_lshrrev_b32 v255, 16, v203\n\t"
    "v_and_b32 v203, s92, v203\n\t"
    "v_pk_mul_f32 v[74:75], v[74:75], v[178:179]\n\t"
    "v_exp_f32 v76, v72\n\t"
    "v_exp_f32 v77, v73\n\t"
    "v_exp_f32 v78, v74\n\t"
    "v_exp_f32 v79, v75\n\t"
    "s_waitcnt lgkmcnt(4)\n\t"
    "v_smfmac_f32_16x16x64_bf16 v[196:199], v[120:123], v[92:99], v124\n\t"
    "ds_read_b64 v[92:93], v202 offset:64000\n\t"
    "ds_read_b64 v[94:95], v254 offset:64000\n\t"
    "ds_read_b64 v[96:97], v203 offset:64000\n\t"
    "ds_read_b64 v[98:99], v255 offset:64000\n\t"
    "s_sub_u32 s89, s89, s95\n\t"
    "buffer_load_dwordx4 v[212:215], v127, %12, s89 offen offset:0 sc1\n\t"
    "v_lshrrev_b32 v252, 16, v204\n\t"
    "v_and_b32 v204, s92, v204\n\t"
    "v_lshrrev_b32 v253, 16, v205\n\t"
    "v_and_b32 v205, s92, v205\n\t"
    "v_pk_fma_f32 v[76:77], v[76:77], s[40:41], s[40:41]\n\t"
    "v_pk_fma_f32 v[78:79], v[78:79], s[42:43], s[42:43]\n\t"
    "v_rcp_f32 v76, v76\n\t"
    "v_rcp_f32 v77, v77\n\t"
    "v_rcp_f32 v78, v78\n\t"
    "s_waitcnt lgkmcnt(4)\n\t"
    "v_smfmac_f32_16x16x64_bf16 v[180:183], v[120:123], v[84:91], v124\n\t"
    "ds_read_b64 v[84:85], v204 offset:0\n\t"
    "ds_read_b64 v[86:87], v252 offset:0\n\t"
    "ds_read_b64 v[88:89], v205 offset:0\n\t"
    "ds_read_b64 v[90:91], v253 offset:0\n\t"
    "v_lshrrev_b32 v254, 16, v206\n\t"
    "v_and_b32 v206, s92, v206\n\t"
    "v_lshrrev_b32 v255, 16, v207\n\t"
    "v_and_b32 v207, s92, v207\n\t"
    "s_waitcnt vmcnt(3)\n\t"
    "v_readfirstlane_b32 s65, v164\n\t"
    "s_nop 0\n\t"
    "s_cmp_eq_u32 s65, 0\n\t"
    "s_cbranch_scc1 L_xw1_%=\n\t"
    "L_xwb1_%=:\n\t"
    "s_add_u32 s94, s94, 0x2000\n\t"
    "buffer_load_dwordx4 v[20:23], v127, %12, s94 offen sc1\n\t"
    "v_rcp_f32 v79, v79\n\t"
    "v_pk_mul_f32 v[76:77], v[76:77], v[176:177]\n\t"
    "v_pk_fma_f32 v[76:77], v[78:79], v[178:179], v[76:77]\n\t"
    "v_add_f32 v237, v76, v77\n\t"
    "s_cmp_lt_u32 %16, 4\n\t"
    "s_cbranch_scc1 L_tp227_%=\n\t"
    "s_setprio 0\n\t"
    "L_tp227_%=:\n\t"
    "s_waitcnt lgkmcnt(4)\n\t"
    "v_smfmac_f32_16x16x64_bf16 v[180:183], v[120:123], v[92:99], v124\n\t"
    "ds_read_b64 v[92:93], v206 offset:0\n\t"
    "ds_read_b64 v[94:95], v254 offset:0\n\t"
    "ds_read_b64 v[96:97], v207 offset:0\n\t"
    "ds_read_b64 v[98:99], v255 offset:0\n\t"
    "s_add_u32 s87, s87, 0x8000\n\t"
    "buffer_load_dwordx4 v[216:219], v127, %12, s87 offen offset:0 sc1\n\t"
    "v_lshrrev_b32 v252, 16, v144\n\t"
    "v_and_b32 v144, s92, v144\n\t"
    "v_lshrrev_b32 v253, 16, v145\n\t"
    "v_mov_b64 v[200:201], s[70:71]\n\t"
    "v_mov_b64 v[202:203], s[72:73]\n\t"
    "v_and_b32 v145, s92, v145\n\t"
    "s_mov_b64 exec, 0xffffffff\n\t"
    "buffer_store_dword v165, v125, %12, s96 offen offset:384 sc1\n\t"
    "s_mov_b64 exec, -1\n\t"
    "v_pk_mul_f32 v[64:65], v[180:181], v[180:181]\n\t"
    "v_pk_mul_f32 v[66:67], v[182:183], v[182:183]\n\t"
    "v_pk_fma_f32 v[64:65], v[64:65], v[82:83], v[80:81]\n\t"
    "v_pk_fma_f32 v[66:67], v[66:67], v[82:83], v[80:81]\n\t"
    "v_pk_mul_f32 v[64:65], v[64:65], v[180:181]\n\t"
    "s_waitcnt lgkmcnt(4)\n\t"
    "v_smfmac_f32_16x16x64_bf16 v[200:203], v[120:123], v[84:91], v124\n\t"
    "ds_read_b64 v[84:85], v144 offset:64000\n\t"
    "ds_read_b64 v[86:87], v252 offset:64000\n\t"
    "ds_read_b64 v[88:89], v145 offset:64000\n\t"
    "ds_read_b64 v[90:91], v253 offset:64000\n\t"
    "v_lshrrev_b32 v254, 16, v146\n\t"
    "v_and_b32 v146, s92, v146\n\t"
    "v_lshrrev_b32 v255, 16, v147\n\t"
    "v_and_b32 v147, s92, v147\n\t"
    "v_pk_mul_f32 v[66:67], v[66:67], v[182:183]\n\t"
    "v_exp_f32 v68, v64\n\t"
    "v_exp_f32 v69, v65\n\t"
    "v_exp_f32 v70, v66\n\t"
    "v_exp_f32 v71, v67\n\t"
    "s_waitcnt lgkmcnt(4)\n\t"
    "v_smfmac_f32_16x16x64_bf16 v[200:203], v[120:123], v[92:99], v124\n\t"
    "ds_read_b64 v[92:93], v146 offset:64000\n\t"
    "ds_read_b64 v[94:95], v254 offset:64000\n\t"
    "ds_read_b64 v[96:97], v147 offset:64000\n\t"
    "ds_read_b64 v[98:99], v255 offset:64000\n\t"
    "buffer_load_dwordx4 v[220:223], v127, %12, s89 offen offset:1024 sc1\n\t"
    "v_lshrrev_b32 v252, 16, v148\n\t"
    "v_and_b32 v148, s92, v148\n\t"
    "v_lshrrev_b32 v253, 16, v149\n\t"
    "v_and_b32 v149, s92, v149\n\t"
    "v_pk_fma_f32 v[68:69], v[68:69], s[40:41], s[40:41]\n\t"
    "v_pk_fma_f32 v[70:71], v[70:71], s[42:43], s[42:43]\n\t"
    "v_rcp_f32 v68, v68\n\t"
    "v_rcp_f32 v69, v69\n\t"
    "v_rcp_f32 v70, v70\n\t"
    "s_waitcnt lgkmcnt(4)\n\t"
    "v_smfmac_f32_16x16x64_bf16 v[184:187], v[120:123], v[84:91], v124\n\t"
    "ds_read_b64 v[84:85], v148 offset:0\n\t"
    "ds_read_b64 v[86:87], v252 offset:0\n\t"
    "ds_read_b64 v[88:89], v149 offset:0\n\t"
    "ds_read_b64 v[90:91], v253 offset:0\n\t"
    "v_lshrrev_b32 v254, 16, v150\n\t"
    "v_and_b32 v150, s92, v150\n\t"
    "v_lshrrev_b32 v255, 16, v151\n\t"
    "v_and_b32 v151, s92, v151\n\t"
    "s_add_u32 s94, s94, 0x2000\n\t"
    "buffer_load_dwordx4 v[28:31], v127, %12, s94 offen sc1\n\t"
    "v_rcp_f32 v71, v71\n\t"
    "v_pk_mul_f32 v[68:69], v[68:69], v[180:181]\n\t"
    "v_pk_fma_f32 v[68:69], v[70:71], v[182:183], v[68:69]\n\t"
    "v_add_f32 v238, v68, v69\n\t"
    "s_waitcnt lgkmcnt(4)\n\t"
    "v_smfmac_f32_16x16x64_bf16 v[184:187], v[120:123], v[92:99], v124\n\t"
    "ds_read_b64 v[92:93], v150 offset:0\n\t"
    "ds_read_b64 v[94:95], v254 offset:0\n\t"
    "ds_read_b64 v[96:97], v151 offset:0\n\t"
    "ds_read_b64 v[98:99], v255 offset:0\n\t"
    "buffer_load_dwordx4 v[116:119], v127, %12, s87 offen offset:1024 sc1\n\t"
    "v_lshrrev_b32 v252, 16, v152\n\t"
    "v_and_b32 v152, s92, v152\n\t"
    "v_lshrrev_b32 v253, 16, v153\n\t"
    "v_mov_b64 v[204:205], s[70:71]\n\t"
    "v_mov_b64 v[206:207], s[72:73]\n\t"
    "v_and_b32 v153, s92, v153\n\t"
    "v_pk_mul_f32 v[72:73], v[184:185], v[184:185]\n\t"
    "v_pk_mul_f32 v[74:75], v[186:187], v[186:187]\n\t"
    "v_pk_fma_f32 v[72:73], v[72:73], v[82:83], v[80:81]\n\t"
    "v_pk_fma_f32 v[74:75], v[74:75], v[82:83], v[80:81]\n\t"
    "v_pk_mul_f32 v[72:73], v[72:73], v[184:185]\n\t"
    "s_waitcnt lgkmcnt(4)\n\t"
    "v_smfmac_f32_16x16x64_bf16 v[204:207], v[120:123], v[84:91], v124\n\t"
    "ds_read_b64 v[84:85], v152 offset:64000\n\t"
    "ds_read_b64 v[86:87], v252 offset:64000\n\t"
    "ds_read_b64 v[88:89], v153 offset:64000\n\t"
    "ds_read_b64 v[90:91], v253 offset:64000\n\t"
    "v_lshrrev_b32 v254, 16, v154\n\t"
    "v_and_b32 v154, s92, v154\n\t"
    "v_lshrrev_b32 v255, 16, v155\n\t"
    "v_and_b32 v155, s92, v155\n\t"
    "v_pk_mul_f32 v[74:75], v[74:75], v[186:187]\n\t"
    "v_exp_f32 v76, v72\n\t"
    "v_exp_f32 v77, v73\n\t"
    "v_exp_f32 v78, v74\n\t"
    "v_exp_f32 v79, v75\n\t"
    "s_waitcnt vmcnt(8) lgkmcnt(4)\n\t"
    "v_smfmac_f32_16x16x64_bf16 v[204:207], v[120:123], v[92:99], v124\n\t"
    "ds_read_b64 v[92:93], v154 offset:64000\n\t"
    "ds_read_b64 v[94:95], v254 offset:64000\n\t"
    "ds_read_b64 v[96:97], v155 offset:64000\n\t"
    "ds_read_b64 v[98:99], v255 offset:64000\n\t"
    "s_sub_u32 s90, s90, s95\n\t"
    "buffer_load_dwordx4 v[224:227], v127, %12, s90 offen offset:0 sc1\n\t"
    "v_lshrrev_b32 v252, 16, v244\n\t"
    "v_and_b32 v244, s92, v244\n\t"
    "v_lshrrev_b32 v253, 16, v245\n\t"
    "v_and_b32 v245, s92, v245\n\t"
    "v_pk_fma_f32 v[76:77], v[76:77], s[40:41], s[40:41]\n\t"
    "v_pk_fma_f32 v[78:79], v[78:79], s[42:43], s[42:43]\n\t"
    "v_rcp_f32 v76, v76\n\t"
    "v_rcp_f32 v77, v77\n\t"
    "v_rcp_f32 v78, v78\n\t"
    "s_waitcnt lgkmcnt(4)\n\t"
    "v_smfmac_f32_16x16x64_bf16 v[188:191], v[120:123], v[84:91], v124\n\t"
    "ds_read_b64 v[84:85], v244 offset:0\n\t"
    "ds_read_b64 v[86:87], v252 offset:0\n\t"
    "ds_read_b64 v[88:89], v245 offset:0\n\t"
    "ds_read_b64 v[90:91], v253 offset:0\n\t"
    "v_lshrrev_b32 v254, 16, v246\n\t"
    "v_and_b32 v246, s92, v246\n\t"
    "v_lshrrev_b32 v255, 16, v247\n\t"
    "v_and_b32 v247, s92, v247\n\t"
    "s_waitcnt vmcnt(6)\n\t"
    "v_smfmac_f32_16x16x64_bf16 v[188:191], v[156:159], v[16:23], v124\n\t"
    "v_rcp_f32 v79, v79\n\t"
    "v_pk_mul_f32 v[76:77], v[76:77], v[184:185]\n\t"
    "v_pk_fma_f32 v[76:77], v[78:79], v[186:187], v[76:77]\n\t"
    "v_add_f32 v239, v76, v77\n\t"
    "v_lshrrev_b32 v147, 4, v127\n\t"
    "v_add_u32 v148, 140288, v127\n\t"
    "s_lshl_b32 s65, %14, 12\n\t"
    "s_add_u32 s65, s65, 16777216\n\t"
    "buffer_load_dwordx4 v[0:3], v127, %12, s65 offen offset:0 sc1\n\t"
    "s_waitcnt lgkmcnt(4)\n\t"
    "v_smfmac_f32_16x16x64_bf16 v[188:191], v[120:123], v[92:99], v124\n\t"
    "ds_read_b64 v[92:93], v246 offset:0\n\t"
    "ds_read_b64 v[94:95], v254 offset:0\n\t"
    "ds_read_b64 v[96:97], v247 offset:0\n\t"
    "ds_read_b64 v[98:99], v255 offset:0\n\t"
    "s_add_u32 s88, s88, 0x8000\n\t"
    "buffer_load_dwordx4 v[228:231], v127, %12, s88 offen offset:0 sc1\n\t"
    "v_lshrrev_b32 v252, 16, v248\n\t"
    "v_and_b32 v248, s92, v248\n\t"
    "v_lshrrev_b32 v253, 16, v249\n\t"
    "v_mov_b64 v[208:209], s[70:71]\n\t"
    "v_mov_b64 v[210:211], s[72:73]\n\t"
    "v_and_b32 v249, s92, v249\n\t"
    "v_smfmac_f32_16x16x64_bf16 v[192:195], v[160:163], v[16:23], v124\n\t"
    "v_pk_mul_f32 v[64:65], v[188:189], v[188:189]\n\t"
    "v_pk_mul_f32 v[66:67], v[190:191], v[190:191]\n\t"
    "v_pk_fma_f32 v[64:65], v[64:65], v[82:83], v[80:81]\n\t"
    "v_pk_fma_f32 v[66:67], v[66:67], v[82:83], v[80:81]\n\t"
    "v_pk_mul_f32 v[64:65], v[64:65], v[188:189]\n\t"
    "s_lshl_b32 s65, %14, 12\n\t"
    "s_add_u32 s65, s65, 16777216\n\t"
    "buffer_load_dwordx4 v[4:7], v127, %12, s65 offen offset:1024 sc1\n\t"
    "s_waitcnt lgkmcnt(4)\n\t"
    "v_smfmac_f32_16x16x64_bf16 v[208:211], v[120:123], v[84:91], v124\n\t"
    "ds_read_b64 v[84:85], v248 offset:64000\n\t"
    "ds_read_b64 v[86:87], v252 offset:64000\n\t"
    "ds_read_b64 v[88:89], v249 offset:64000\n\t"
    "ds_read_b64 v[90:91], v253 offset:64000\n\t"
    "v_lshrrev_b32 v254, 16, v250\n\t"
    "v_and_b32 v250, s92, v250\n\t"
    "v_lshrrev_b32 v255, 16, v251\n\t"
    "v_and_b32 v251, s92, v251\n\t"
    "s_mov_b64 exec, 1\n\t"
    "buffer_load_dword v164, off, %12, s93 offset:256 sc1\n\t"
    "s_mov_b64 exec, -1\n\t"
    "v_pk_mul_f32 v[66:67], v[66:67], v[190:191]\n\t"
    "v_exp_f32 v68, v64\n\t"
    "v_exp_f32 v69, v65\n\t"
    "v_exp_f32 v70, v66\n\t"
    "v_exp_f32 v71, v67\n\t"
    "s_lshl_b32 s65, %14, 12\n\t"
    "s_add_u32 s65, s65, 16777216\n\t"
    "buffer_load_dwordx4 v[8:11], v127, %12, s65 offen offset:2048 sc1\n\t"
    "s_waitcnt lgkmcnt(4)\n\t"
    "v_smfmac_f32_16x16x64_bf16 v[208:211], v[120:123], v[92:99], v124\n\t"
    "ds_read_b64 v[92:93], v250 offset:64000\n\t"
    "ds_read_b64 v[94:95], v254 offset:64000\n\t"
    "ds_read_b64 v[96:97], v251 offset:64000\n\t"
    "ds_read_b64 v[98:99], v255 offset:64000\n\t"
    "buffer_load_dwordx4 v[232:235], v127, %12, s90 offen offset:1024 sc1\n\t"
    "v_lshrrev_b32 v252, 16, v212\n\t"
    "v_and_b32 v212, s92, v212\n\t"
    "v_lshrrev_b32 v253, 16, v213\n\t"
    "v_and_b32 v213, s92, v213\n\t"
    "v_pk_fma_f32 v[68:69], v[68:69], s[40:41], s[40:41]\n\t"
    "v_pk_fma_f32 v[70:71], v[70:71], s[42:43], s[42:43]\n\t"
    "v_rcp_f32 v68, v68\n\t"
    "v_rcp_f32 v69, v69\n\t"
    "v_rcp_f32 v70, v70\n\t"
    "s_lshl_b32 s65, %14, 12\n\t"
    "s_add_u32 s65, s65, 16777216\n\t"
    "buffer_load_dwordx4 v[12:15], v127, %12, s65 offen offset:3072 sc1\n\t"
    "s_waitcnt lgkmcnt(4)\n\t"
    "v_smfmac_f32_16x16x64_bf16 v[192:195], v[120:123], v[84:91], v124\n\t"
    "ds_read_b64 v[84:85], v212 offset:0\n\t"
    "ds_read_b64 v[86:87], v252 offset:0\n\t"
    "ds_read_b64 v[88:89], v213 offset:0\n\t"
    "ds_read_b64 v[90:91], v253 offset:0\n\t"
    "v_lshrrev_b32 v254, 16, v214\n\t"
    "v_and_b32 v214, s92, v214\n\t"
    "v_lshrrev_b32 v255, 16, v215\n\t"
    "v_and_b32 v215, s92, v215\n\t"
    "s_waitcnt vmcnt(9)\n\t"
    "v_smfmac_f32_16x16x64_bf16 v[196:199], v[156:159], v[24:31], v124\n\t"
    "v_rcp_f32 v71, v71\n\t"
    "v_pk_mul_f32 v[68:69], v[68:69], v[188:189]\n\t"
    "v_pk_fma_f32 v[68:69], v[70:71], v[190:191], v[68:69]\n\t"
    "v_add_f32 v240, v68, v69\n\t"
    "ds_read_b128 v[16:19], v148 offset:4096\n\t"
    "s_cmp_lt_u32 %16, 4\n\t"
    "s_cbranch_scc1 L_tp239_%=\n\t"
    "s_setprio 1\n\t"
    "L_tp239_%=:\n\t"
    "s_waitcnt lgkmcnt(5)\n\t"
    "v_smfmac_f32_16x16x64_bf16 v[192:195], v[120:123], v[92:99], v124\n\t"
    "ds_read_b64 v[92:93], v214 offset:0\n\t"
    "ds_read_b64 v[94:95], v254 offset:0\n\t"
    "ds_read_b64 v[96:97], v215 offset:0\n\t"
    "ds_read_b64 v[98:99], v255 offset:0\n\t"
    "buffer_load_dwordx4 v[168:171], v127, %12, s88 offen offset:1024 sc1\n\t"
    "v_lshrrev_b32 v252, 16, v216\n\t"
    "v_and_b32 v216, s92, v216\n\t"
    "v_lshrrev_b32 v253, 16, v217\n\t"
    "v_mov_b64 v[172:173], s[70:71]\n\t"
    "v_mov_b64 v[174:175], s[72:73]\n\t"
    "v_and_b32 v217, s92, v217\n\t"
    "v_smfmac_f32_16x16x64_bf16 v[200:203], v[160:163], v[24:31], v124\n\t"
    "v_pk_mul_f32 v[72:73], v[192:193], v[192:193]\n\t"
    "v_pk_mul_f32 v[74:75], v[194:195], v[194:195]\n\t"
    "v_pk_fma_f32 v[72:73], v[72:73], v[82:83], v[80:81]\n\t"
    "v_pk_fma_f32 v[74:75], v[74:75], v[82:83], v[80:81]\n\t"
    "v_pk_mul_f32 v[72:73], v[72:73], v[192:193]\n\t"
    "ds_read_b128 v[20:23], v148 offset:5120\n\t"
    "s_waitcnt lgkmcnt(6)\n\t"
    "v_smfmac_f32_16x16x64_bf16 v[172:175], v[120:123], v[84:91], v124\n\t"
    "ds_read_b64 v[84:85], v216 offset:64000\n\t"
    "ds_read_b64 v[86:87], v252 offset:64000\n\t"
    "ds_read_b64 v[88:89], v217 offset:64000\n\t"
    "ds_read_b64 v[90:91], v253 offset:64000\n\t"
    "v_lshrrev_b32 v254, 16, v218\n\t"
    "v_and_b32 v218, s92, v218\n\t"
    "v_lshrrev_b32 v255, 16, v219\n\t"
    "v_and_b32 v219, s92, v219\n\t"
    "v_pk_mul_f32 v[74:75], v[74:75], v[194:195]\n\t"
    "v_exp_f32 v76, v72\n\t"
    "v_exp_f32 v77, v73\n\t"
    "v_exp_f32 v78, v74\n\t"
    "v_exp_f32 v79, v75\n\t"
    "ds_read_b128 v[24:27], v148 offset:6144\n\t"
    "s_waitcnt lgkmcnt(6)\n\t"
    "v_smfmac_f32_16x16x64_bf16 v[172:175], v[120:123], v[92:99], v124\n\t"
    "ds_read_b64 v[92:93], v218 offset:64000\n\t"
    "ds_read_b64 v[94:95], v254 offset:64000\n\t"
    "ds_read_b64 v[96:97], v219 offset:64000\n\t"
    "ds_read_b64 v[98:99], v255 offset:64000\n\t"
    "s_sub_u32 s91, s91, s95\n\t"
    "buffer_load_dwordx4 v[112:115], v127, %12, s91 offen offset:0 sc1\n\t"
    "v_lshrrev_b32 v252, 16, v220\n\t"
    "v_and_b32 v220, s92, v220\n\t"
    "v_lshrrev_b32 v253, 16, v221\n\t"
    "v_and_b32 v221, s92, v221\n\t"
    "v_pk_fma_f32 v[76:77], v[76:77], s[40:41], s[40:41]\n\t"
    "v_pk_fma_f32 v[78:79], v[78:79], s[42:43], s[42:43]\n\t"
    "v_rcp_f32 v76, v76\n\t"
    "v_rcp_f32 v77, v77\n\t"
    "v_rcp_f32 v78, v78\n\t"
    "ds_read_b128 v[28:31], v148 offset:7168\n\t"
    "s_waitcnt lgkmcnt(6)\n\t"
    "v_smfmac_f32_16x16x64_bf16 v[196:199], v[120:123], v[84:91], v124\n\t"
    "ds_read_b64 v[84:85], v220 offset:0\n\t"
    "ds_read_b64 v[86:87], v252 offset:0\n\t"
    "ds_read_b64 v[88:89], v221 offset:0\n\t"
    "ds_read_b64 v[90:91], v253 offset:0\n\t"
    "v_lshrrev_b32 v254, 16, v222\n\t"
    "v_and_b32 v222, s92, v222\n\t"
    "v_lshrrev_b32 v255, 16, v223\n\t"
    "v_and_b32 v223, s92, v223\n\t"
    "s_waitcnt vmcnt(5)\n\t"
    "v_readfirstlane_b32 s65, v164\n\t"
    "s_nop 0\n\t"
    "s_cmp_eq_u32 s65, 0\n\t"
    "s_cbranch_scc1 L_xw2_%=\n\t"
    "L_xwb2_%=:\n\t"
    "s_add_u32 s94, s94, 0x2000\n\t"
    "buffer_load_dwordx4 v[36:39], v127, %12, s94 offen sc1\n\t"
    "v_rcp_f32 v79, v79\n\t"
    "v_pk_mul_f32 v[76:77], v[76:77], v[192:193]\n\t"
    "v_pk_fma_f32 v[76:77], v[78:79], v[194:195], v[76:77]\n\t"
    "v_add_f32 v241, v76, v77\n\t"
    "s_waitcnt lgkmcnt(5)\n\t"
    "v_smfmac_f32_16x16x64_bf16 v[196:199], v[120:123], v[92:99], v124\n\t"
    "ds_read_b64 v[92:93], v222 offset:0\n\t"
    "ds_read_b64 v[94:95], v254 offset:0\n\t"
    "ds_read_b64 v[96:97], v223 offset:0\n\t"
    "ds_read_b64 v[98:99], v255 offset:0\n\t"
    "s_add_u32 s89, s89, 0x8000\n\t"
    "buffer_load_dwordx4 v[100:103], v127, %12, s89 offen offset:0 sc1\n\t"
    "v_lshrrev_b32 v252, 16, v116\n\t"
    "v_and_b32 v116, s92, v116\n\t"
    "v_lshrrev_b32 v253, 16, v117\n\t"
    "v_mov_b64 v[176:177], s[70:71]\n\t"
    "v_mov_b64 v[178:179], s[72:73]\n\t"
    "v_and_b32 v117, s92, v117\n\t"
    "v_pk_mul_f32 v[64:65], v[196:197], v[196:197]\n\t"
    "v_pk_mul_f32 v[66:67], v[198:199], v[198:199]\n\t"
    "v_pk_fma_f32 v[64:65], v[64:65], v[82:83], v[80:81]\n\t"
    "v_pk_fma_f32 v[66:67], v[66:67], v[82:83], v[80:81]\n\t"
    "v_pk_mul_f32 v[64:65], v[64:65], v[196:197]\n\t"
    "s_waitcnt lgkmcnt(4)\n\t"
    "v_smfmac_f32_16x16x64_bf16 v[176:179], v[120:123], v[84:91], v124\n\t"
    "ds_read_b64 v[84:85], v116 offset:64000\n\t"
    "ds_read_b64 v[86:87], v252 offset:64000\n\t"
    "ds_read_b64 v[88:89], v117 offset:64000\n\t"
    "ds_read_b64 v[90:91], v253 offset:64000\n\t"
    "v_lshrrev_b32 v254, 16, v118\n\t"
    "v_and_b32 v118, s92, v118\n\t"
    "v_lshrrev_b32 v255, 16, v119\n\t"
    "v_and_b32 v119, s92, v119\n\t"
    "v_pk_mul_f32 v[66:67], v[66:67], v[198:199]\n\t"
    "v_exp_f32 v68, v64\n\t"
    "v_exp_f32 v69, v65\n\t"
    "v_exp_f32 v70, v66\n\t"
    "v_exp_f32 v71, v67\n\t"
    "s_waitcnt lgkmcnt(4)\n\t"
    "v_smfmac_f32_16x16x64_bf16 v[176:179], v[120:123], v[92:99], v124\n\t"
    "ds_read_b64 v[92:93], v118 offset:64000\n\t"
    "ds_read_b64 v[94:95], v254 offset:64000\n\t"
    "ds_read_b64 v[96:97], v119 offset:64000\n\t"
    "ds_read_b64 v[98:99], v255 offset:64000\n\t"
    "buffer_load_dwordx4 v[104:107], v127, %12, s91 offen offset:1024 sc1\n\t"
    "v_lshrrev_b32 v252, 16, v224\n\t"
    "v_and_b32 v224, s92, v224\n\t"
    "v_lshrrev_b32 v253, 16, v225\n\t"
    "v_and_b32 v225, s92, v225\n\t"
    "v_pk_fma_f32 v[68:69], v[68:69], s[40:41], s[40:41]\n\t"
    "v_pk_fma_f32 v[70:71], v[70:71], s[42:43], s[42:43]\n\t"
    "v_rcp_f32 v68, v68\n\t"
    "v_rcp_f32 v69, v69\n\t"
    "v_rcp_f32 v70, v70\n\t"
    "s_waitcnt lgkmcnt(4)\n\t"
    "v_smfmac_f32_16x16x64_bf16 v[200:203], v[120:123], v[84:91], v124\n\t"
    "ds_read_b64 v[84:85], v224 offset:0\n\t"
    "ds_read_b64 v[86:87], v252 offset:0\n\t"
    "ds_read_b64 v[88:89], v225 offset:0\n\t"
    "ds_read_b64 v[90:91], v253 offset:0\n\t"
    "v_lshrrev_b32 v254, 16, v226\n\t"
    "v_and_b32 v226, s92, v226\n\t"
    "v_lshrrev_b32 v255, 16, v227\n\t"
    "v_and_b32 v227, s92, v227\n\t"
    "s_add_u32 s94, s94, 0x2000\n\t"
    "buffer_load_dwordx4 v[44:47], v127, %12, s94 offen sc1\n\t"
    "v_rcp_f32 v71, v71\n\t"
    "v_pk_mul_f32 v[68:69], v[68:69], v[196:197]\n\t"
    "v_pk_fma_f32 v[68:69], v[70:71], v[198:199], v[68:69]\n\t"
    "v_add_f32 v242, v68, v69\n\t"
    "s_lshl_b32 s46, %14, 2\n\t"
    "s_add_u32 s46, s46, 49294336\n\t"
    "s_mov_b64 exec, 1\n\t"
    "buffer_load_dword v144, off, %12, s46 sc1\n\t"
    "s_mov_b64 exec, -1\n\t"
    "s_waitcnt lgkmcnt(4)\n\t"
    "v_smfmac_f32_16x16x64_bf16 v[200:203], v[120:123], v[92:99], v124\n\t"
    "ds_read_b64 v[92:93], v226 offset:0\n\t"
    "ds_read_b64 v[94:95], v254 offset:0\n\t"
    "ds_read_b64 v[96:97], v227 offset:0\n\t"
    "ds_read_b64 v[98:99], v255 offset:0\n\t"
    "buffer_load_dwordx4 v[108:111], v127, %12, s89 offen offset:1024 sc1\n\t"
    "v_lshrrev_b32 v252, 16, v228\n\t"
    "v_and_b32 v228, s92, v228\n\t"
    "v_lshrrev_b32 v253, 16, v229\n\t"
    "v_mov_b64 v[180:181], s[70:71]\n\t"
    "v_mov_b64 v[182:183], s[72:73]\n\t"
    "v_and_b32 v229, s92, v229\n\t"
    "v_pk_mul_f32 v[72:73], v[200:201], v[200:201]\n\t"
    "v_pk_mul_f32 v[74:75], v[202:203], v[202:203]\n\t"
    "v_pk_fma_f32 v[72:73], v[72:73], v[82:83], v[80:81]\n\t"
    "v_pk_fma_f32 v[74:75], v[74:75], v[82:83], v[80:81]\n\t"
    "v_pk_mul_f32 v[72:73], v[72:73], v[200:201]\n\t"
    "s_waitcnt lgkmcnt(4)\n\t"
    "v_smfmac_f32_16x16x64_bf16 v[180:183], v[120:123], v[84:91], v124\n\t"
    "ds_read_b64 v[84:85], v228 offset:64000\n\t"
    "ds_read_b64 v[86:87], v252 offset:64000\n\t"
    "ds_read_b64 v[88:89], v229 offset:64000\n\t"
    "ds_read_b64 v[90:91], v253 offset:64000\n\t"
    "v_lshrrev_b32 v254, 16, v230\n\t"
    "v_and_b32 v230, s92, v230\n\t"
    "v_lshrrev_b32 v255, 16, v231\n\t"
    "v_and_b32 v231, s92, v231\n\t"
    "v_pk_mul_f32 v[74:75], v[74:75], v[202:203]\n\t"
    "v_exp_f32 v76, v72\n\t"
    "v_exp_f32 v77, v73\n\t"
    "v_exp_f32 v78, v74\n\t"
    "v_exp_f32 v79, v75\n\t"
    "s_waitcnt vmcnt(9) lgkmcnt(4)\n\t"
    "v_smfmac_f32_16x16x64_bf16 v[180:183], v[120:123], v[92:99], v124\n\t"
    "ds_read_b64 v[92:93], v230 offset:64000\n\t"
    "ds_read_b64 v[94:95], v254 offset:64000\n\t"
    "ds_read_b64 v[96:97], v231 offset:64000\n\t"
    "ds_read_b64 v[98:99], v255 offset:64000\n\t"
    "s_add_u32 s90, s90, 0x8000\n\t"
    "buffer_load_dwordx4 v[248:251], v127, %12, s90 offen offset:0 sc1\n\t"
    "v_lshrrev_b32 v252, 16, v232\n\t"
    "v_and_b32 v232, s92, v232\n\t"
    "v_lshrrev_b32 v253, 16, v233\n\t"
    "v_and_b32 v233, s92, v233\n\t"
    "v_pk_fma_f32 v[76:77], v[76:77], s[40:41], s[40:41]\n\t"
    "v_pk_fma_f32 v[78:79], v[78:79], s[42:43], s[42:43]\n\t"
    "v_rcp_f32 v76, v76\n\t"
    "v_rcp_f32 v77, v77\n\t"
    "v_rcp_f32 v78, v78\n\t"
    "s_waitcnt lgkmcnt(4)\n\t"
    "v_smfmac_f32_16x16x64_bf16 v[204:207], v[120:123], v[84:91], v124\n\t"
    "ds_read_b64 v[84:85], v232 offset:0\n\t"
    "ds_read_b64 v[86:87], v252 offset:0\n\t"
    "ds_read_b64 v[88:89], v233 offset:0\n\t"
    "ds_read_b64 v[90:91], v253 offset:0\n\t"
    "v_lshrrev_b32 v254, 16, v234\n\t"
    "v_and_b32 v234, s92, v234\n\t"
    "v_lshrrev_b32 v255, 16, v235\n\t"
    "v_and_b32 v235, s92, v235\n\t"
    "s_mov_b64 exec, 1\n\t"
    "buffer_load_dword v164, off, %12, s93 offset:384 sc1\n\t"
    "s_mov_b64 exec, -1\n\t"
    "s_waitcnt vmcnt(7)\n\t"
    "v_smfmac_f32_16x16x64_bf16 v[204:207], v[156:159], v[32:39], v124\n\t"
    "v_rcp_f32 v79, v79\n\t"
    "v_pk_mul_f32 v[76:77], v[76:77], v[200:201]\n\t"
    "v_pk_fma_f32 v[76:77], v[78:79], v[202:203], v[76:77]\n\t"
    "v_add_f32 v243, v76, v77\n\t"
    "s_waitcnt lgkmcnt(4)\n\t"
    "v_smfmac_f32_16x16x64_bf16 v[204:207], v[120:123], v[92:99], v124\n\t"
    "ds_read_b64 v[92:93], v234 offset:0\n\t"
    "ds_read_b64 v[94:95], v254 offset:0\n\t"
    "ds_read_b64 v[96:97], v235 offset:0\n\t"
    "ds_read_b64 v[98:99], v255 offset:0\n\t"
    "buffer_load_dwordx4 v[212:215], v127, %12, s90 offen offset:1024 sc1\n\t"
    "v_lshrrev_b32 v252, 16, v168\n\t"
    "v_and_b32 v168, s92, v168\n\t"
    "v_lshrrev_b32 v253, 16, v169\n\t"
    "v_mov_b64 v[184:185], s[70:71]\n\t"
    "v_mov_b64 v[186:187], s[72:73]\n\t"
    "v_and_b32 v169, s92, v169\n\t"
    "v_smfmac_f32_16x16x64_bf16 v[208:211], v[160:163], v[32:39], v124\n\t"
    "v_pk_mul_f32 v[64:65], v[204:205], v[204:205]\n\t"
    "v_pk_mul_f32 v[66:67], v[206:207], v[206:207]\n\t"
    "v_pk_fma_f32 v[64:65], v[64:65], v[82:83], v[80:81]\n\t"
    "v_pk_fma_f32 v[66:67], v[66:67], v[82:83], v[80:81]\n\t"
    "v_pk_mul_f32 v[64:65], v[64:65], v[204:205]\n\t"
    "ds_read_b128 v[32:35], v148 offset:8192\n\t"
    "s_waitcnt lgkmcnt(5)\n\t"
    "v_smfmac_f32_16x16x64_bf16 v[184:187], v[120:123], v[84:91], v124\n\t"
    "ds_read_b64 v[84:85], v168 offset:64000\n\t"
    "ds_read_b64 v[86:87], v252 offset:64000\n\t"
    "ds_read_b64 v[88:89], v169 offset:64000\n\t"
    "ds_read_b64 v[90:91], v253 offset:64000\n\t"
    "v_lshrrev_b32 v254, 16, v170\n\t"
    "v_and_b32 v170, s92, v170\n\t"
    "v_lshrrev_b32 v255, 16, v171\n\t"
    "v_and_b32 v171, s92, v171\n\t"
    "v_pk_mul_f32 v[66:67], v[66:67], v[206:207]\n\t"
    "v_exp_f32 v68, v64\n\t"
    "v_exp_f32 v69, v65\n\t"
    "v_exp_f32 v70, v66\n\t"
    "v_exp_f32 v71, v67\n\t"
    "ds_read_b128 v[36:39], v148 offset:9216\n\t"
    "s_waitcnt lgkmcnt(6)\n\t"
    "v_smfmac_f32_16x16x64_bf16 v[184:187], v[120:123], v[92:99], v124\n\t"
    "ds_read_b64 v[92:93], v170 offset:64000\n\t"
    "ds_read_b64 v[94:95], v254 offset:64000\n\t"
    "ds_read_b64 v[96:97], v171 offset:64000\n\t"
    "ds_read_b64 v[98:99], v255 offset:64000\n\t"
    "s_add_u32 s91, s91, 0x8000\n\t"
    "buffer_load_dwordx4 v[216:219], v127, %12, s91 offen offset:0 sc1\n\t"
    "v_lshrrev_b32 v252, 16, v112\n\t"
    "v_and_b32 v112, s92, v112\n\t"
    "v_lshrrev_b32 v253, 16, v113\n\t"
    "v_and_b32 v113, s92, v113\n\t"
    "v_pk_fma_f32 v[68:69], v[68:69], s[40:41], s[40:41]\n\t"
    "v_pk_fma_f32 v[70:71], v[70:71], s[42:43], s[42:43]\n\t"
    "v_rcp_f32 v68, v68\n\t"
    "v_rcp_f32 v69, v69\n\t"
    "v_rcp_f32 v70, v70\n\t"
    "s_waitcnt lgkmcnt(5)\n\t"
    "v_smfmac_f32_16x16x64_bf16 v[208:211], v[120:123], v[84:91], v124\n\t"
    "ds_read_b64 v[84:85], v112 offset:0\n\t"
    "ds_read_b64 v[86:87], v252 offset:0\n\t"
    "ds_read_b64 v[88:89], v113 offset:0\n\t"
    "ds_read_b64 v[90:91], v253 offset:0\n\t"
    "v_lshrrev_b32 v254, 16, v114\n\t"
    "v_and_b32 v114, s92, v114\n\t"
    "v_lshrrev_b32 v255, 16, v115\n\t"
    "v_and_b32 v115, s92, v115\n\t"
    "s_waitcnt vmcnt(6)\n\t"
    "v_smfmac_f32_16x16x64_bf16 v[172:175], v[156:159], v[40:47], v124\n\t"
    "v_rcp_f32 v71, v71\n\t"
    "v_pk_mul_f32 v[68:69], v[68:69], v[204:205]\n\t"
    "v_pk_fma_f32 v[68:69], v[70:71], v[206:207], v[68:69]\n\t"
    "v_add_f32 v244, v68, v69\n\t"
    "s_waitcnt vmcnt(5)\n\t"
    "v_readfirstlane_b32 s38, v144\n\t"
    "s_nop 0\n\t"
    "s_cmp_eq_u32 s38, 0\n\t"
    "s_cbranch_scc1 L_rw_%=\n\t"
    "L_rok_%=:\n\t"
    "s_and_b32 s46, %16, 3\n\t"
    "s_mul_i32 s49, s46, 1040\n\t"
    "s_add_u32 s49, s49, 2064\n\t"
    "s_and_b32 s37, s38, 0xffff\n\t"
    "s_lshr_b32 s44, s38, 16\n\t"
    "s_sub_u32 s44, s44, s37\n\t"
    "s_lshl_b32 s37, s37, 2\n\t"
    "s_lshl_b32 s38, s44, 2\n\t"
    "s_mul_i32 s51, s46, s44\n\t"
    "s_add_u32 s48, s51, s44\n\t"
    "s_lshl_b32 s48, s48, 16\n\t"
    "s_add_u32 s48, s48, s51\n\t"
    "s_lshr_b32 s47, %16, 2\n\t"
    "s_add_u32 s48, s48, s47\n\t"
    "s_add_u32 s48, s48, 0x200\n\t"
    "s_mul_i32 s46, s37, 400\n\t"
    "s_add_u32 s96, %18, s46\n\t"
    "s_addc_u32 s97, %19, 0\n\t"
    "s_sub_u32 s98, 6553600, s46\n\t"
    "s_mov_b32 s99, 0x20000\n\t"
    "v_bfe_u32 v154, v147, 2, 2\n\t"
    "v_and_b32 v155, 3, v147\n\t"
    "v_mul_u32_u24 v154, 25, v154\n\t"
    "v_mad_u32_u24 v154, v155, 6, v154\n\t"
    "v_lshlrev_b32 v153, 2, v154\n\t"
    "s_and_b32 s53, s48, 0xff\n\t"
    "s_bfe_u32 s55, s48, 0x80008\n\t"
    "s_add_u32 s54, s53, s55\n\t"
    "s_add_u32 s55, s54, s55\n\t"
    "s_lshr_b32 s64, s48, 16\n\t"
    "s_mul_i32 s66, s53, 400\n\t"
    "buffer_load_dwordx3 v[128:130], v153, s[96:99], s66 offen\n\t"
    "buffer_load_dwordx4 v[132:135], v153, s[96:99], s66 offen offset:12\n\t"
    "s_mul_i32 s66, s54, 400\n\t"
    "buffer_load_dwordx3 v[136:138], v153, s[96:99], s66 offen\n\t"
    "buffer_load_dwordx4 v[140:143], v153, s[96:99], s66 offen offset:12\n\t"
    "v_cmp_eq_u32 s[62:63], 0, v155\n\t"
    "v_and_b32 v151, 48, v147\n\t"
    "v_or_b32 v155, 0x100000, v151\n\t"
    "v_cndmask_b32 v152, v155, v151, s[62:63]\n\t"
    "s_waitcnt lgkmcnt(4)\n\t"
    "v_smfmac_f32_16x16x64_bf16 v[208:211], v[120:123], v[92:99], v124\n\t"
    "ds_read_b64 v[92:93], v114 offset:0\n\t"
    "ds_read_b64 v[94:95], v254 offset:0\n\t"
    "ds_read_b64 v[96:97], v115 offset:0\n\t"
    "ds_read_b64 v[98:99], v255 offset:0\n\t"
    "buffer_load_dwordx4 v[220:223], v127, %12, s91 offen offset:1024 sc1\n\t"
    "v_lshrrev_b32 v252, 16, v100\n\t"
    "v_and_b32 v100, s92, v100\n\t"
    "v_lshrrev_b32 v253, 16, v101\n\t"
    "v_mov_b64 v[188:189], s[70:71]\n\t"
    "v_mov_b64 v[190:191], s[72:73]\n\t"
    "v_and_b32 v101, s92, v101\n\t"
    "v_smfmac_f32_16x16x64_bf16 v[176:179], v[160:163], v[40:47], v124\n\t"
    "v_pk_mul_f32 v[72:73], v[208:209], v[208:209]\n\t"
    "v_pk_mul_f32 v[74:75], v[210:211], v[210:211]\n\t"
    "v_pk_fma_f32 v[72:73], v[72:73], v[82:83], v[80:81]\n\t"
    "v_pk_fma_f32 v[74:75], v[74:75], v[82:83], v[80:81]\n\t"
    "v_pk_mul_f32 v[72:73], v[72:73], v[208:209]\n\t"
    "ds_read_b128 v[40:43], v148 offset:10240\n\t"
    "s_setprio 0\n\t"
    "s_waitcnt lgkmcnt(5)\n\t"
    "v_smfmac_f32_16x16x64_bf16 v[188:191], v[120:123], v[84:91], v124\n\t"
    "ds_read_b64 v[84:85], v100 offset:64000\n\t"
    "ds_read_b64 v[86:87], v252 offset:64000\n\t"
    "ds_read_b64 v[88:89], v101 offset:64000\n\t"
    "ds_read_b64 v[90:91], v253 offset:64000\n\t"
    "v_lshrrev_b32 v254, 16, v102\n\t"
    "v_and_b32 v102, s92, v102\n\t"
    "v_lshrrev_b32 v255, 16, v103\n\t"
    "v_and_b32 v103, s92, v103\n\t"
    "s_waitcnt vmcnt(7)\n\t"
    "v_readfirstlane_b32 s65, v164\n\t"
    "s_nop 0\n\t"
    "s_cmp_eq_u32 s65, 0\n\t"
    "s_cbranch_scc1 L_xw3_%=\n\t"
    "L_xwb3_%=:\n\t"
    "s_add_u32 s94, s94, 0x2000\n\t"
    "buffer_load_dwordx4 v[52:55], v127, %12, s94 offen sc1\n\t"
    "v_pk_mul_f32 v[74:75], v[74:75], v[210:211]\n\t"
    "v_exp_f32 v76, v72\n\t"
    "v_exp_f32 v77, v73\n\t"
    "v_exp_f32 v78, v74\n\t"
    "v_exp_f32 v79, v75\n\t"
    "ds_read_b128 v[44:47], v148 offset:11264\n\t"
    "s_waitcnt lgkmcnt(6)\n\t"
    "v_smfmac_f32_16x16x64_bf16 v[188:191], v[120:123], v[92:99], v124\n\t"
    "ds_read_b64 v[92:93], v102 offset:64000\n\t"
    "ds_read_b64 v[94:95], v254 offset:64000\n\t"
    "ds_read_b64 v[96:97], v103 offset:64000\n\t"
    "ds_read_b64 v[98:99], v255 offset:64000\n\t"
    "v_lshrrev_b32 v252, 16, v104\n\t"
    "v_and_b32 v104, s92, v104\n\t"
    "v_lshrrev_b32 v253, 16, v105\n\t"
    "v_and_b32 v105, s92, v105\n\t"
    "v_pk_fma_f32 v[76:77], v[76:77], s[40:41], s[40:41]\n\t"
    "v_pk_fma_f32 v[78:79], v[78:79], s[42:43], s[42:43]\n\t"
    "v_rcp_f32 v76, v76\n\t"
    "v_rcp_f32 v77, v77\n\t"
    "v_rcp_f32 v78, v78\n\t"
    "s_waitcnt lgkmcnt(5)\n\t"
    "v_smfmac_f32_16x16x64_bf16 v[172:175], v[120:123], v[84:91], v124\n\t"
    "ds_read_b64 v[84:85], v104 offset:0\n\t"
    "ds_read_b64 v[86:87], v252 offset:0\n\t"
    "ds_read_b64 v[88:89], v105 offset:0\n\t"
    "ds_read_b64 v[90:91], v253 offset:0\n\t"
    "v_lshrrev_b32 v254, 16, v106\n\t"
    "v_and_b32 v106, s92, v106\n\t"
    "v_lshrrev_b32 v255, 16, v107\n\t"
    "v_and_b32 v107, s92, v107\n\t"
    "v_rcp_f32 v79, v79\n\t"
    "v_pk_mul_f32 v[76:77], v[76:77], v[208:209]\n\t"
    "v_pk_fma_f32 v[76:77], v[78:79], v[210:211], v[76:77]\n\t"
    "v_add_f32 v245, v76, v77\n\t"
    "s_waitcnt lgkmcnt(4)\n\t"
    "v_smfmac_f32_16x16x64_bf16 v[172:175], v[120:123], v[92:99], v124\n\t"
    "ds_read_b64 v[92:93], v106 offset:0\n\t"
    "ds_read_b64 v[94:95], v254 offset:0\n\t"
    "ds_read_b64 v[96:97], v107 offset:0\n\t"
    "ds_read_b64 v[98:99], v255 offset:0\n\t"
    "v_lshrrev_b32 v252, 16, v108\n\t"
    "v_and_b32 v108, s92, v108\n\t"
    "v_lshrrev_b32 v253, 16, v109\n\t"
    "v_mov_b64 v[192:193], s[70:71]\n\t"
    "v_mov_b64 v[194:195], s[72:73]\n\t"
    "v_and_b32 v109, s92, v109\n\t"
    "v_pk_mul_f32 v[64:65], v[172:173], v[172:173]\n\t"
    "v_pk_mul_f32 v[66:67], v[174:175], v[174:175]\n\t"
    "v_pk_fma_f32 v[64:65], v[64:65], v[82:83], v[80:81]\n\t"
    "v_pk_fma_f32 v[66:67], v[66:67], v[82:83], v[80:81]\n\t"
    "v_pk_mul_f32 v[64:65], v[64:65], v[172:173]\n\t"
    "s_waitcnt lgkmcnt(4)\n\t"
    "v_smfmac_f32_16x16x64_bf16 v[192:195], v[120:123], v[84:91], v124\n\t"
    "ds_read_b64 v[84:85], v108 offset:64000\n\t"
    "ds_read_b64 v[86:87], v252 offset:64000\n\t"
    "ds_read_b64 v[88:89], v109 offset:64000\n\t"
    "ds_read_b64 v[90:91], v253 offset:64000\n\t"
    "v_lshrrev_b32 v254, 16, v110\n\t"
    "v_and_b32 v110, s92, v110\n\t"
    "v_lshrrev_b32 v255, 16, v111\n\t"
    "v_and_b32 v111, s92, v111\n\t"
    "s_add_u32 s94, s94, 0x2000\n\t"
    "buffer_load_dwordx4 v[60:63], v127, %12, s94 offen sc1\n\t"
    "v_pk_mul_f32 v[66:67], v[66:67], v[174:175]\n\t"
    "v_exp_f32 v68, v64\n\t"
    "v_exp_f32 v69, v65\n\t"
    "v_exp_f32 v70, v66\n\t"
    "v_exp_f32 v71, v67\n\t"
    "v_lshrrev_b32 v166, 7, v127\n\t"
    "v_and_b32 v167, 0x70, v127\n\t"
    "v_lshl_add_u32 v126, v166, 8, v167\n\t"
    "v_add_u32 v126, 128, v126\n\t"
    "s_bfe_u32 s46, %14, 0x30003\n\t"
    "s_min_u32 s46, s46, 6\n\t"
    "s_lshl_b32 s69, s46, 3\n\t"
    "s_add_u32 s69, s69, %16\n\t"
    "s_waitcnt lgkmcnt(4)\n\t"
    "v_smfmac_f32_16x16x64_bf16 v[192:195], v[120:123], v[92:99], v124\n\t"
    "ds_read_b64 v[92:93], v110 offset:64000\n\t"
    "ds_read_b64 v[94:95], v254 offset:64000\n\t"
    "ds_read_b64 v[96:97], v111 offset:64000\n\t"
    "ds_read_b64 v[98:99], v255 offset:64000\n\t"
    "v_lshrrev_b32 v252, 16, v248\n\t"
    "v_and_b32 v248, s92, v248\n\t"
    "v_lshrrev_b32 v253, 16, v249\n\t"
    "v_and_b32 v249, s92, v249\n\t"
    "v_pk_fma_f32 v[68:69], v[68:69], s[40:41], s[40:41]\n\t"
    "v_pk_fma_f32 v[70:71], v[70:71], s[42:43], s[42:43]\n\t"
    "v_rcp_f32 v68, v68\n\t"
    "v_rcp_f32 v69, v69\n\t"
    "v_rcp_f32 v70, v70\n\t"
    "s_barrier\n\t"
    "s_waitcnt lgkmcnt(4)\n\t"
    "v_smfmac_f32_16x16x64_bf16 v[176:179], v[120:123], v[84:91], v124\n\t"
    "ds_read_b64 v[84:85], v248 offset:64000\n\t"
    "ds_read_b64 v[86:87], v252 offset:64000\n\t"
    "ds_read_b64 v[88:89], v249 offset:64000\n\t"
    "ds_read_b64 v[90:91], v253 offset:64000\n\t"
    "v_lshrrev_b32 v254, 16, v250\n\t"
    "v_and_b32 v250, s92, v250\n\t"
    "v_lshrrev_b32 v255, 16, v251\n\t"
    "v_and_b32 v251, s92, v251\n\t"
    "v_rcp_f32 v71, v71\n\t"
    "v_pk_mul_f32 v[68:69], v[68:69], v[172:173]\n\t"
    "v_pk_fma_f32 v[68:69], v[70:71], v[174:175], v[68:69]\n\t"
    "v_add_f32 v246, v68, v69\n\t"
    "s_mul_i32 s44, s69, 1040\n\t"
    "s_add_u32 m0, s44, 0\n\t"
    "s_lshl_b32 s44, s69, 11\n\t"
    "buffer_load_dwordx4 v126, %15, s44 offen lds\n\t"
    "s_add_u32 s69, s69, 8\n\t"
    "s_sub_u32 s44, s69, 61\n\t"
    "s_cmp_ge_u32 s69, 61\n\t"
    "s_cselect_b32 s69, s44, s69\n\t"
    "s_waitcnt vmcnt(9) lgkmcnt(4)\n\t"
    "v_smfmac_f32_16x16x64_bf16 v[176:179], v[120:123], v[92:99], v124\n\t"
    "ds_read_b64 v[92:93], v250 offset:64000\n\t"
    "ds_read_b64 v[94:95], v254 offset:64000\n\t"
    "ds_read_b64 v[96:97], v251 offset:64000\n\t"
    "ds_read_b64 v[98:99], v255 offset:64000\n\t"
    "v_lshrrev_b32 v252, 16, v212\n\t"
    "v_and_b32 v212, s92, v212\n\t"
    "v_lshrrev_b32 v253, 16, v213\n\t"
    "v_and_b32 v213, s92, v213\n\t"
    "s_mul_i32 s44, s69, 1040\n\t"
    "s_add_u32 m0, s44, 0\n\t"
    "s_lshl_b32 s44, s69, 11\n\t"
    "buffer_load_dwordx4 v126, %15, s44 offen lds\n\t"
    "s_add_u32 s69, s69, 8\n\t"
    "s_sub_u32 s44, s69, 61\n\t"
    "s_cmp_ge_u32 s69, 61\n\t"
    "s_cselect_b32 s69, s44, s69\n\t"
    "v_lshlrev_b32 v146, 2, v147\n\t"
    "s_lshl_b32 s50, s37, 8\n\t"
    "s_add_u32 s50, s50, 27262976\n\t"
    "s_lshl_b32 s46, s53, 8\n\t"
    "s_add_u32 s46, s46, s50\n\t"
    "buffer_load_dword v144, v146, %12, s46 offen sc1\n\t"
    "buffer_load_dword v145, v146, %12, s46 offen offset:512 sc1\n\t"
    "v_mov_b32 v149, s49\n\t"
    "s_add_u32 s46, s51, 6\n\t"
    "v_mov_b32 v155, s46\n\t"
    "ds_write_b32 v149, v155\n\t"
    "s_mov_b32 s61, 0\n\t"
    "v_mov_b32 v149, 1024\n\t"
    "v_mov_b32 v150, 0\n\t"
    "ds_write_b32 v149, v150\n\t"
    "s_mov_b32 s45, 1\n\t"
    "s_waitcnt lgkmcnt(6)\n\t"
    "v_smfmac_f32_16x16x64_bf16 v[180:183], v[120:123], v[84:91], v124\n\t"
    "ds_read_b64 v[84:85], v212 offset:64000\n\t"
    "ds_read_b64 v[86:87], v252 offset:64000\n\t"
    "ds_read_b64 v[88:89], v213 offset:64000\n\t"
    "ds_read_b64 v[90:91], v253 offset:64000\n\t"
    "v_lshrrev_b32 v254, 16, v214\n\t"
    "v_and_b32 v214, s92, v214\n\t"
    "v_lshrrev_b32 v255, 16, v215\n\t"
    "v_and_b32 v215, s92, v215\n\t"
    "s_waitcnt vmcnt(5)\n\t"
    "v_smfmac_f32_16x16x64_bf16 v[180:183], v[156:159], v[48:55], v124\n\t"
    "v_pk_mul_f32 v[72:73], v[176:177], v[176:177]\n\t"
    "v_pk_mul_f32 v[74:75], v[178:179], v[178:179]\n\t"
    "v_pk_fma_f32 v[72:73], v[72:73], v[82:83], v[80:81]\n\t"
    "v_pk_fma_f32 v[74:75], v[74:75], v[82:83], v[80:81]\n\t"
    "v_pk_mul_f32 v[72:73], v[72:73], v[176:177]\n\t"
    "v_pk_mul_f32 v[74:75], v[74:75], v[178:179]\n\t"
    "v_exp_f32 v76, v72\n\t"
    "s_mul_i32 s44, s69, 1040\n\t"
    "s_add_u32 m0, s44, 0\n\t"
    "s_lshl_b32 s44, s69, 11\n\t"
    "buffer_load_dwordx4 v126, %15, s44 offen lds\n\t"
    "s_add_u32 s69, s69, 8\n\t"
    "s_sub_u32 s44, s69, 61\n\t"
    "s_cmp_ge_u32 s69, 61\n\t"
    "s_cselect_b32 s69, s44, s69\n\t"
    "s_waitcnt lgkmcnt(6)\n\t"
    "v_smfmac_f32_16x16x64_bf16 v[180:183], v[120:123], v[92:99], v124\n\t"
    "ds_read_b64 v[92:93], v214 offset:64000\n\t"
    "ds_read_b64 v[94:95], v254 offset:64000\n\t"
    "ds_read_b64 v[96:97], v215 offset:64000\n\t"
    "ds_read_b64 v[98:99], v255 offset:64000\n\t"
    "v_lshrrev_b32 v252, 16, v216\n\t"
    "v_and_b32 v216, s92, v216\n\t"
    "v_lshrrev_b32 v253, 16, v217\n\t"
    "v_and_b32 v217, s92, v217\n\t"
    "v_smfmac_f32_16x16x64_bf16 v[184:187], v[160:163], v[48:55], v124\n\t"
    "v_exp_f32 v77, v73\n\t"
    "v_exp_f32 v78, v74\n\t"
    "v_exp_f32 v79, v75\n\t"
    "v_pk_fma_f32 v[76:77], v[76:77], s[40:41], s[40:41]\n\t"
    "v_pk_fma_f32 v[78:79], v[78:79], s[42:43], s[42:43]\n\t"
    "v_rcp_f32 v76, v76\n\t"
    "v_rcp_f32 v77, v77\n\t"
    "s_mul_i32 s44, s69, 1040\n\t"
    "s_add_u32 m0, s44, 0\n\t"
    "s_lshl_b32 s44, s69, 11\n\t"
    "buffer_load_dwordx4 v126, %15, s44 offen lds\n\t"
    "s_add_u32 s69, s69, 8\n\t"
    "s_sub_u32 s44, s69, 61\n\t"
    "s_cmp_ge_u32 s69, 61\n\t"
    "s_cselect_b32 s69, s44, s69\n\t"
    "ds_read_b128 v[48:51], v148 offset:12288\n\t"
    "s_waitcnt lgkmcnt(5)\n\t"
    "v_smfmac_f32_16x16x64_bf16 v[184:187], v[120:123], v[84:91], v124\n\t"
    "ds_read_b64 v[84:85], v216 offset:64000\n\t"
    "ds_read_b64 v[86:87], v252 offset:64000\n\t"
    "ds_read_b64 v[88:89], v217 offset:64000\n\t"
    "ds_read_b64 v[90:91], v253 offset:64000\n\t"
    "v_lshrrev_b32 v254, 16, v218\n\t"
    "v_and_b32 v218, s92, v218\n\t"
    "v_lshrrev_b32 v255, 16, v219\n\t"
    "v_and_b32 v219, s92, v219\n\t"
    "v_rcp_f32 v78, v78\n\t"
    "v_rcp_f32 v79, v79\n\t"
    "v_pk_mul_f32 v[76:77], v[76:77], v[176:177]\n\t"
    "v_pk_fma_f32 v[76:77], v[78:79], v[178:179], v[76:77]\n\t"
    "v_add_f32 v247, v76, v77\n\t"
    "v_pk_mul_f32 v[64:65], v[180:181], v[180:181]\n\t"
    "v_pk_mul_f32 v[66:67], v[182:183], v[182:183]\n\t"
    "s_mul_i32 s44, s69, 1040\n\t"
    "s_add_u32 m0, s44, 0\n\t"
    "s_lshl_b32 s44, s69, 11\n\t"
    "buffer_load_dwordx4 v126, %15, s44 offen lds\n\t"
    "s_add_u32 s69, s69, 8\n\t"
    "s_sub_u32 s44, s69, 61\n\t"
    "s_cmp_ge_u32 s69, 61\n\t"
    "s_cselect_b32 s69, s44, s69\n\t"
    "ds_read_b128 v[52:55], v148 offset:13312\n\t"
    "s_waitcnt lgkmcnt(6)\n\t"
    "v_smfmac_f32_16x16x64_bf16 v[184:187], v[120:123], v[92:99], v124\n\t"
    "ds_read_b64 v[92:93], v218 offset:64000\n\t"
    "ds_read_b64 v[94:95], v254 offset:64000\n\t"
    "ds_read_b64 v[96:97], v219 offset:64000\n\t"
    "ds_read_b64 v[98:99], v255 offset:64000\n\t"
    "v_lshrrev_b32 v252, 16, v220\n\t"
    "v_and_b32 v220, s92, v220\n\t"
    "v_lshrrev_b32 v253, 16, v221\n\t"
    "v_and_b32 v221, s92, v221\n\t"
    "v_pk_fma_f32 v[64:65], v[64:65], v[82:83], v[80:81]\n\t"
    "v_pk_fma_f32 v[66:67], v[66:67], v[82:83], v[80:81]\n\t"
    "v_pk_mul_f32 v[64:65], v[64:65], v[180:181]\n\t"
    "v_pk_mul_f32 v[66:67], v[66:67], v[182:183]\n\t"
    "v_exp_f32 v68, v64\n\t"
    "v_exp_f32 v69, v65\n\t"
    "v_exp_f32 v70, v66\n\t"
    "s_mul_i32 s44, s69, 1040\n\t"
    "s_add_u32 m0, s44, 0\n\t"
    "s_lshl_b32 s44, s69, 11\n\t"
    "buffer_load_dwordx4 v126, %15, s44 offen lds\n\t"
    "s_add_u32 s69, s69, 8\n\t"
    "s_sub_u32 s44, s69, 61\n\t"
    "s_cmp_ge_u32 s69, 61\n\t"
    "s_cselect_b32 s69, s44, s69\n\t"
    "s_waitcnt lgkmcnt(5)\n\t"
    "v_smfmac_f32_16x16x64_bf16 v[188:191], v[120:123], v[84:91], v124\n\t"
    "ds_read_b64 v[84:85], v220 offset:64000\n\t"
    "ds_read_b64 v[86:87], v252 offset:64000\n\t"
    "ds_read_b64 v[88:89], v221 offset:64000\n\t"
    "ds_read_b64 v[90:91], v253 offset:64000\n\t"
    "v_lshrrev_b32 v254, 16, v222\n\t"
    "v_and_b32 v222, s92, v222\n\t"
    "v_lshrrev_b32 v255, 16, v223\n\t"
    "v_and_b32 v223, s92, v223\n\t"
    "s_waitcnt vmcnt(8)\n\t"
    "v_smfmac_f32_16x16x64_bf16 v[188:191], v[156:159], v[56:63], v124\n\t"
    "v_exp_f32 v71, v67\n\t"
    "v_pk_fma_f32 v[68:69], v[68:69], s[40:41], s[40:41]\n\t"
    "v_pk_fma_f32 v[70:71], v[70:71], s[42:43], s[42:43]\n\t"
    "v_rcp_f32 v68, v68\n\t"
    "v_rcp_f32 v69, v69\n\t"
    "v_rcp_f32 v70, v70\n\t"
    "v_rcp_f32 v71, v71\n\t"
    "s_mul_i32 s44, s69, 1040\n\t"
    "s_add_u32 m0, s44, 0\n\t"
    "s_lshl_b32 s44, s69, 11\n\t"
    "buffer_load_dwordx4 v126, %15, s44 offen lds\n\t"
    "s_add_u32 s69, s69, 8\n\t"
    "s_sub_u32 s44, s69, 61\n\t"
    "s_cmp_ge_u32 s69, 61\n\t"
    "s_cselect_b32 s69, s44, s69\n\t"
    "s_waitcnt lgkmcnt(4)\n\t"
    "v_smfmac_f32_16x16x64_bf16 v[188:191], v[120:123], v[92:99], v124\n\t"
    "ds_read_b64 v[92:93], v222 offset:64000\n\t"
    "ds_read_b64 v[94:95], v254 offset:64000\n\t"
    "ds_read_b64 v[96:97], v223 offset:64000\n\t"
    "ds_read_b64 v[98:99], v255 offset:64000\n\t"
    "v_smfmac_f32_16x16x64_bf16 v[192:195], v[160:163], v[56:63], v124\n\t"
    "v_pk_mul_f32 v[68:69], v[68:69], v[180:181]\n\t"
    "v_pk_fma_f32 v[68:69], v[70:71], v[182:183], v[68:69]\n\t"
    "v_add_f32 v248, v68, v69\n\t"
    "v_pk_mul_f32 v[72:73], v[184:185], v[184:185]\n\t"
    "v_pk_mul_f32 v[74:75], v[186:187], v[186:187]\n\t"
    "v_pk_fma_f32 v[72:73], v[72:73], v[82:83], v[80:81]\n\t"
    "v_pk_fma_f32 v[74:75], v[74:75], v[82:83], v[80:81]\n\t"
    "s_mul_i32 s44, s69, 1040\n\t"
    "s_add_u32 m0, s44, 0\n\t"
    "s_lshl_b32 s44, s69, 11\n\t"
    "buffer_load_dwordx4 v126, %15, s44 offen lds\n\t"
    "s_add_u32 s69, s69, 8\n\t"
    "s_sub_u32 s44, s69, 61\n\t"
    "s_cmp_ge_u32 s69, 61\n\t"
    "s_cselect_b32 s69, s44, s69\n\t"
    "ds_read_b128 v[56:59], v148 offset:14336\n\t"
    "s_waitcnt lgkmcnt(5)\n\t"
    "v_smfmac_f32_16x16x64_bf16 v[192:195], v[120:123], v[84:91], v124\n\t"
    "v_pk_mul_f32 v[72:73], v[72:73], v[184:185]\n\t"
    "v_pk_mul_f32 v[74:75], v[74:75], v[186:187]\n\t"
    "v_exp_f32 v76, v72\n\t"
    "v_exp_f32 v77, v73\n\t"
    "v_exp_f32 v78, v74\n\t"
    "v_exp_f32 v79, v75\n\t"
    "v_pk_fma_f32 v[76:77], v[76:77], s[40:41], s[40:41]\n\t"
    "ds_read_b128 v[60:63], v148 offset:15360\n\t"
    "s_waitcnt lgkmcnt(2)\n\t"
    "v_smfmac_f32_16x16x64_bf16 v[192:195], v[120:123], v[92:99], v124\n\t"
    "v_pk_fma_f32 v[78:79], v[78:79], s[42:43], s[42:43]\n\t"
    "v_rcp_f32 v76, v76\n\t"
    "v_rcp_f32 v77, v77\n\t"
    "v_rcp_f32 v78, v78\n\t"
    "v_rcp_f32 v79, v79\n\t"
    "v_pk_mul_f32 v[76:77], v[76:77], v[184:185]\n\t"
    "v_pk_fma_f32 v[76:77], v[78:79], v[186:187], v[76:77]\n\t"
    "s_barrier\n\t"
    "v_add_u32 v126, 124928, v126\n\t"
    "s_bfe_u32 s46, %14, 0x30003\n\t"
    "s_min_u32 s46, s46, 6\n\t"
    "s_lshl_b32 s69, s46, 3\n\t"
    "s_add_u32 s69, s69, %16\n\t"
    "s_mul_i32 s44, s69, 1040\n\t"
    "s_add_u32 m0, s44, 63440\n\t"
    "s_lshl_b32 s44, s69, 11\n\t"
    "buffer_load_dwordx4 v126, %15, s44 offen lds\n\t"
    "s_add_u32 s69, s69, 8\n\t"
    "s_sub_u32 s44, s69, 64\n\t"
    "s_cmp_ge_u32 s69, 64\n\t"
    "s_cselect_b32 s69, s44, s69\n\t"
    "s_mul_i32 s44, s69, 1040\n\t"
    "s_add_u32 m0, s44, 63440\n\t"
    "s_lshl_b32 s44, s69, 11\n\t"
    "buffer_load_dwordx4 v126, %15, s44 offen lds\n\t"
    "s_add_u32 s69, s69, 8\n\t"
    "s_sub_u32 s44, s69, 64\n\t"
    "s_cmp_ge_u32 s69, 64\n\t"
    "s_cselect_b32 s69, s44, s69\n\t"
    "s_mul_i32 s44, s69, 1040\n\t"
    "s_add_u32 m0, s44, 63440\n\t"
    "s_lshl_b32 s44, s69, 11\n\t"
    "buffer_load_dwordx4 v126, %15, s44 offen lds\n\t"
    "s_add_u32 s69, s69, 8\n\t"
    "s_sub_u32 s44, s69, 64\n\t"
    "s_cmp_ge_u32 s69, 64\n\t"
    "s_cselect_b32 s69, s44, s69\n\t"
    "s_mul_i32 s44, s69, 1040\n\t"
    "s_add_u32 m0, s44, 63440\n\t"
    "s_lshl_b32 s44, s69, 11\n\t"
    "buffer_load_dwordx4 v126, %15, s44 offen lds\n\t"
    "s_add_u32 s69, s69, 8\n\t"
    "s_sub_u32 s44, s69, 64\n\t"
    "s_cmp_ge_u32 s69, 64\n\t"
    "s_cselect_b32 s69, s44, s69\n\t"
    "s_mul_i32 s44, s69, 1040\n\t"
    "s_add_u32 m0, s44, 63440\n\t"
    "s_lshl_b32 s44, s69, 11\n\t"
    "buffer_load_dwordx4 v126, %15, s44 offen lds\n\t"
    "s_add_u32 s69, s69, 8\n\t"
    "s_sub_u32 s44, s69, 64\n\t"
    "s_cmp_ge_u32 s69, 64\n\t"
    "s_cselect_b32 s69, s44, s69\n\t"
    "s_mul_i32 s44, s69, 1040\n\t"
    "s_add_u32 m0, s44, 63440\n\t"
    "s_lshl_b32 s44, s69, 11\n\t"
    "buffer_load_dwordx4 v126, %15, s44 offen lds\n\t"
    "s_add_u32 s69, s69, 8\n\t"
    "s_sub_u32 s44, s69, 64\n\t"
    "s_cmp_ge_u32 s69, 64\n\t"
    "s_cselect_b32 s69, s44, s69\n\t"
    "s_mul_i32 s44, s69, 1040\n\t"
    "s_add_u32 m0, s44, 63440\n\t"
    "s_lshl_b32 s44, s69, 11\n\t"
    "buffer_load_dwordx4 v126, %15, s44 offen lds\n\t"
    "s_add_u32 s69, s69, 8\n\t"
    "s_sub_u32 s44, s69, 64\n\t"
    "s_cmp_ge_u32 s69, 64\n\t"
    "s_cselect_b32 s69, s44, s69\n\t"
    "s_mul_i32 s44, s69, 1040\n\t"
    "s_add_u32 m0, s44, 63440\n\t"
    "s_lshl_b32 s44, s69, 11\n\t"
    "buffer_load_dwordx4 v126, %15, s44 offen lds\n\t"
    "s_add_u32 s69, s69, 8\n\t"
    "s_sub_u32 s44, s69, 64\n\t"
    "s_cmp_ge_u32 s69, 64\n\t"
    "s_cselect_b32 s69, s44, s69\n\t"
    "ds_read_b128 v[84:87], v148 offset:21504\n\t"
    "ds_read_b128 v[88:91], v148 offset:22528\n\t"
    "v_add_f32 v249, v76, v77\n\t"
    "v_pk_mul_f32 v[64:65], v[188:189], v[188:189]\n\t"
    "v_pk_mul_f32 v[66:67], v[190:191], v[190:191]\n\t"
    "v_pk_fma_f32 v[64:65], v[64:65], v[82:83], v[80:81]\n\t"
    "v_pk_fma_f32 v[66:67], v[66:67], v[82:83], v[80:81]\n\t"
    "v_pk_mul_f32 v[64:65], v[64:65], v[188:189]\n\t"
    "v_pk_mul_f32 v[66:67], v[66:67], v[190:191]\n\t"
    "v_exp_f32 v68, v64\n\t"
    "v_exp_f32 v69, v65\n\t"
    "v_exp_f32 v70, v66\n\t"
    "v_exp_f32 v71, v67\n\t"
    "v_pk_fma_f32 v[68:69], v[68:69], s[40:41], s[40:41]\n\t"
    "v_pk_fma_f32 v[70:71], v[70:71], s[42:43], s[42:43]\n\t"
    "v_rcp_f32 v68, v68\n\t"
    "v_rcp_f32 v69, v69\n\t"
    "v_rcp_f32 v70, v70\n\t"
    "v_rcp_f32 v71, v71\n\t"
    "v_pk_mul_f32 v[68:69], v[68:69], v[188:189]\n\t"
    "v_pk_fma_f32 v[68:69], v[70:71], v[190:191], v[68:69]\n\t"
    "v_add_f32 v250, v68, v69\n\t"
    "v_pk_mul_f32 v[72:73], v[192:193], v[192:193]\n\t"
    "v_pk_mul_f32 v[74:75], v[194:195], v[194:195]\n\t"
    "v_pk_fma_f32 v[72:73], v[72:73], v[82:83], v[80:81]\n\t"
    "v_pk_fma_f32 v[74:75], v[74:75], v[82:83], v[80:81]\n\t"
    "v_pk_mul_f32 v[72:73], v[72:73], v[192:193]\n\t"
    "v_pk_mul_f32 v[74:75], v[74:75], v[194:195]\n\t"
    "v_exp_f32 v76, v72\n\t"
    "v_exp_f32 v77, v73\n\t"
    "v_exp_f32 v78, v74\n\t"
    "v_exp_f32 v79, v75\n\t"
    "v_pk_fma_f32 v[76:77], v[76:77], s[40:41], s[40:41]\n\t"
    "v_pk_fma_f32 v[78:79], v[78:79], s[42:43], s[42:43]\n\t"
    "v_rcp_f32 v76, v76\n\t"
    "v_rcp_f32 v77, v77\n\t"
    "v_rcp_f32 v78, v78\n\t"
    "v_rcp_f32 v79, v79\n\t"
    "v_pk_mul_f32 v[76:77], v[76:77], v[192:193]\n\t"
    "v_pk_fma_f32 v[76:77], v[78:79], v[194:195], v[76:77]\n\t"
    "v_add_f32 v251, v76, v77\n\t"
    "ds_read_b128 v[64:67], v148 offset:16384\n\t"
    "ds_read_b128 v[68:71], v148 offset:17408\n\t"
    "ds_read_b128 v[72:75], v148 offset:18432\n\t"
    "ds_read_b128 v[76:79], v148 offset:19456\n\t"
    "ds_read_b128 v[80:83], v148 offset:20480\n\t"
    "s_waitcnt vmcnt(0) lgkmcnt(0)\n\t"
    "s_mov_b32 s68, 130\n\t"
    "v_mad_u32_u24 v128, v128, s68, v152\n\t"
    "v_and_b32 v128, -16, v128\n\t"
    "v_mad_u32_u24 v129, v129, s68, v151\n\t"
    "v_and_b32 v129, -16, v129\n\t"
    "v_mad_u32_u24 v130, v130, s68, v151\n\t"
    "v_and_b32 v130, -16, v130\n\t"
    "v_mad_u32_u24 v132, v132, s68, v151\n\t"
    "v_and_b32 v132, -16, v132\n\t"
    "v_mad_u32_u24 v133, v133, s68, v151\n\t"
    "v_and_b32 v133, -16, v133\n\t"
    "v_mad_u32_u24 v134, v134, s68, v151\n\t"
    "v_and_b32 v134, -16, v134\n\t"
    "v_mad_u32_u24 v135, v135, s68, v151\n\t"
    "v_and_b32 v135, -16, v135\n\t"
    "v_lshrrev_b32 v122, 4, v147\n\t"
    "v_and_b32 v155, 15, v147\n\t"
    "v_lshlrev_b32 v155, 4, v155\n\t"
    "v_mul_u32_u24 v122, 65536, v122\n\t"
    "v_add_u32 v122, v122, v155\n\t"
    "v_mov_b32 v150, 1\n\t"
    "v_mov_b32 v123, 0\n\t"
    "v_lshlrev_b32 v149, 20, v147\n\t"
    "v_add_u32 v149, s49, v149\n\t"
    "s_mov_b32 s52, s53\n\t"
    "s_cmp_ge_u32 %16, 4\n\t"
    "s_cbranch_scc1 L_st4_%=\n\t"
    "s_bfe_u32 s65, s84, 0xc0002\n\t"
    "s_add_u32 s65, s65, s36\n\t"
    "buffer_store_dword v236, v125, %12, s65 offen offset:0 sc1\n\t"
    "buffer_store_dword v237, v125, %12, s65 offen offset:256 sc1\n\t"
    "s_bfe_u32 s65, s85, 0xc0002\n\t"
    "s_add_u32 s65, s65, s36\n\t"
    "buffer_store_dword v238, v125, %12, s65 offen offset:0 sc1\n\t"
    "buffer_store_dword v239, v125, %12, s65 offen offset:256 sc1\n\t"
    "s_bfe_u32 s65, s86, 0xc0002\n\t"
    "s_add_u32 s65, s65, s36\n\t"
    "buffer_store_dword v240, v125, %12, s65 offen offset:0 sc1\n\t"
    "buffer_store_dword v241, v125, %12, s65 offen offset:256 sc1\n\t"
    "s_bfe_u32 s65, s87, 0xc0002\n\t"
    "s_add_u32 s65, s65, s36\n\t"
    "buffer_store_dword v242, v125, %12, s65 offen offset:0 sc1\n\t"
    "buffer_store_dword v243, v125, %12, s65 offen offset:256 sc1\n\t"
    "s_bfe_u32 s65, s88, 0xc0002\n\t"
    "s_add_u32 s65, s65, s36\n\t"
    "buffer_store_dword v244, v125, %12, s65 offen offset:0 sc1\n\t"
    "buffer_store_dword v245, v125, %12, s65 offen offset:256 sc1\n\t"
    "s_bfe_u32 s65, s89, 0xc0002\n\t"
    "s_add_u32 s65, s65, s36\n\t"
    "buffer_store_dword v246, v125, %12, s65 offen offset:0 sc1\n\t"
    "buffer_store_dword v247, v125, %12, s65 offen offset:256 sc1\n\t"
    "s_bfe_u32 s65, s90, 0xc0002\n\t"
    "s_add_u32 s65, s65, s36\n\t"
    "buffer_store_dword v248, v125, %12, s65 offen offset:0 sc1\n\t"
    "buffer_store_dword v249, v125, %12, s65 offen offset:256 sc1\n\t"
    "s_bfe_u32 s65, s91, 0xc0002\n\t"
    "s_add_u32 s65, s65, s36\n\t"
    "buffer_store_dword v250, v125, %12, s65 offen offset:0 sc1\n\t"
    "buffer_store_dword v251, v125, %12, s65 offen offset:256 sc1\n\t"
    "s_sub_u32 s61, s64, 3\n\t"
    "L_st4_%=:\n\t"
    "s_barrier\n\t"
    "s_mov_b32 s68, 130\n\t"
    "ds_read_b128 v[156:159], v128 offset:0\n\t"
    "ds_read_b128 v[160:163], v128 offset:64\n\t"
    "ds_read_b128 v[164:167], v129 offset:0\n\t"
    "ds_read_b128 v[168:171], v129 offset:64\n\t"
    "ds_read_b128 v[172:175], v130 offset:0\n\t"
    "ds_read_b128 v[176:179], v130 offset:64\n\t"
    "ds_read_b128 v[180:183], v132 offset:0\n\t"
    "ds_read_b128 v[184:187], v132 offset:64\n\t"
    "ds_read_b128 v[188:191], v133 offset:0\n\t"
    "ds_read_b128 v[192:195], v133 offset:64\n\t"
    "ds_read_b128 v[196:199], v134 offset:0\n\t"
    "ds_read_b128 v[200:203], v134 offset:64\n\t"
    "ds_read_b128 v[204:207], v135 offset:0\n\t"
    "ds_read_b128 v[208:211], v135 offset:64\n\t"
    "s_setprio 1\n\t"
    "L_pnn_%=:\n\t"
    "s_mul_i32 s69, s52, 272\n\t"
    "s_add_u32 s69, s69, 130048\n\t"
    "s_mul_i32 s66, s55, 400\n\t"
    "s_waitcnt lgkmcnt(8)\n\t"
    "v_mfma_f32_16x16x32_bf16 v[92:95], v[156:159], v[156:159], 0\n\t"
    "v_add_u32 v147, s69, v146\n\t"
    "v_fmac_f32 v121, v108, v72\n\t"
    "ds_add_rtn_u32 v148, v149, v150\n\t"
    "v_mfma_f32_16x16x32_bf16 v[92:95], v[160:163], v[160:163], v[92:95]\n\t"
    "v_fmac_f32 v120, v109, v73\n\t"
    "v_fmac_f32 v121, v110, v74\n\t"
    "buffer_load_dwordx3 v[128:130], v153, s[96:99], s66 offen\n\t"
    "v_mfma_f32_16x16x32_bf16 v[96:99], v[164:167], v[164:167], 0\n\t"
    "v_fmac_f32 v120, v111, v75\n\t"
    "v_fmac_f32 v121, v112, v76\n\t"
    "v_mfma_f32_16x16x32_bf16 v[96:99], v[168:171], v[168:171], v[96:99]\n\t"
    "v_fmac_f32 v120, v113, v77\n\t"
    "v_fmac_f32 v121, v114, v78\n\t"
    "buffer_load_dwordx4 v[132:135], v153, s[96:99], s66 offen offset:12\n\t"
    "v_mfma_f32_16x16x32_bf16 v[100:103], v[164:167], v[172:175], 0\n\t"
    "v_fmac_f32 v120, v115, v79\n\t"
    "v_fmac_f32 v121, v116, v80\n\t"
    "v_mfma_f32_16x16x32_bf16 v[100:103], v[168:171], v[176:179], v[100:103]\n\t"
    "v_fmac_f32 v120, v117, v81\n\t"
    "v_fmac_f32 v121, v118, v82\n\t"
    "s_waitcnt vmcnt(3)\n\t"
    "s_cmp_ge_u32 s55, s61\n\t"
    "s_cbranch_scc1 L_req0_%=\n\t"
    "L_back0_%=:\n\t"
    "v_mfma_f32_16x16x32_bf16 v[104:107], v[172:175], v[172:175], 0\n\t"
    "v_mad_u32_u24 v136, v136, s68, v152\n\t"
    "v_and_b32 v136, -16, v136\n\t"
    "v_mfma_f32_16x16x32_bf16 v[104:107], v[176:179], v[176:179], v[104:107]\n\t"
    "v_fmac_f32 v120, v119, v83\n\t"
    "v_add_f32 v154, v120, v121\n\t"
    "ds_write_b32 v147, v154\n\t"
    "v_mfma_f32_16x16x32_bf16 v[108:111], v[156:159], v[164:167], 0\n\t"
    "v_fma_f32 v120, v92, v84, v144\n\t"
    "v_mul_f32 v121, v96, v0\n\t"
    "v_mfma_f32_16x16x32_bf16 v[108:111], v[160:163], v[168:171], v[108:111]\n\t"
    "v_fmac_f32 v120, v97, v1\n\t"
    "v_fmac_f32 v121, v98, v2\n\t"
    "s_waitcnt lgkmcnt(8)\n\t"
    "v_mfma_f32_16x16x32_bf16 v[112:115], v[164:167], v[180:183], 0\n\t"
    "v_fmac_f32 v120, v99, v3\n\t"
    "v_fmac_f32 v121, v100, v4\n\t"
    "v_mfma_f32_16x16x32_bf16 v[112:115], v[168:171], v[184:187], v[112:115]\n\t"
    "v_fmac_f32 v120, v101, v5\n\t"
    "v_fmac_f32 v121, v102, v6\n\t"
    "v_mfma_f32_16x16x32_bf16 v[116:119], v[172:175], v[180:183], 0\n\t"
    "v_fmac_f32 v120, v103, v7\n\t"
    "v_fmac_f32 v121, v104, v8\n\t"
    "v_mfma_f32_16x16x32_bf16 v[116:119], v[176:179], v[184:187], v[116:119]\n\t"
    "v_fmac_f32 v120, v105, v9\n\t"
    "v_fmac_f32 v121, v106, v10\n\t"
    "ds_read_b128 v[212:215], v136 offset:0\n\t"
    "ds_read_b128 v[216:219], v136 offset:64\n\t"
    "v_mfma_f32_16x16x32_bf16 v[92:95], v[180:183], v[180:183], 0\n\t"
    "v_fmac_f32 v120, v107, v11\n\t"
    "v_mad_u32_u24 v137, v137, s68, v151\n\t"
    "v_mfma_f32_16x16x32_bf16 v[92:95], v[184:187], v[184:187], v[92:95]\n\t"
    "v_and_b32 v137, -16, v137\n\t"
    "v_fmac_f32 v121, v108, v85\n\t"
    "v_mfma_f32_16x16x32_bf16 v[96:99], v[156:159], v[172:175], 0\n\t"
    "v_fmac_f32 v120, v112, v12\n\t"
    "v_fmac_f32 v121, v113, v13\n\t"
    "v_mfma_f32_16x16x32_bf16 v[96:99], v[160:163], v[176:179], v[96:99]\n\t"
    "v_fmac_f32 v120, v114, v14\n\t"
    "v_fmac_f32 v121, v115, v15\n\t"
    "s_lshl_b32 s51, s55, 8\n\t"
    "s_add_u32 s51, s51, s50\n\t"
    "s_waitcnt lgkmcnt(8)\n\t"
    "v_mfma_f32_16x16x32_bf16 v[100:103], v[164:167], v[188:191], 0\n\t"
    "v_mad_u32_u24 v138, v138, s68, v151\n\t"
    "v_and_b32 v138, -16, v138\n\t"
    "v_mfma_f32_16x16x32_bf16 v[100:103], v[168:171], v[192:195], v[100:103]\n\t"
    "v_fmac_f32 v120, v116, v16\n\t"
    "v_fmac_f32 v121, v117, v17\n\t"
    "buffer_load_dword v144, v146, %12, s51 offen sc1\n\t"
    "v_mfma_f32_16x16x32_bf16 v[104:107], v[172:175], v[188:191], 0\n\t"
    "v_fmac_f32 v120, v118, v18\n\t"
    "v_fmac_f32 v121, v119, v19\n\t"
    "v_mfma_f32_16x16x32_bf16 v[104:107], v[176:179], v[192:195], v[104:107]\n\t"
    "v_fmac_f32 v120, v92, v20\n\t"
    "v_fmac_f32 v121, v93, v21\n\t"
    "s_waitcnt lgkmcnt(6)\n\t"
    "v_mfma_f32_16x16x32_bf16 v[108:111], v[164:167], v[196:199], 0\n\t"
    "v_fmac_f32 v120, v94, v22\n\t"
    "v_fmac_f32 v121, v95, v23\n\t"
    "v_mfma_f32_16x16x32_bf16 v[108:111], v[168:171], v[200:203], v[108:111]\n\t"
    "v_fmac_f32 v120, v96, v86\n\t"
    "v_mad_u32_u24 v140, v140, s68, v151\n\t"
    "v_mfma_f32_16x16x32_bf16 v[112:115], v[156:159], v[180:183], 0\n\t"
    "v_fmac_f32 v121, v100, v24\n\t"
    "v_fmac_f32 v120, v101, v25\n\t"
    "v_mfma_f32_16x16x32_bf16 v[112:115], v[160:163], v[184:187], v[112:115]\n\t"
    "v_fmac_f32 v121, v102, v26\n\t"
    "v_fmac_f32 v120, v103, v27\n\t"
    "s_waitcnt lgkmcnt(3)\n\t"
    "v_mfma_f32_16x16x32_bf16 v[116:119], v[164:167], v[204:207], 0\n\t"
    "v_readfirstlane_b32 s60, v148\n\t"
    "v_and_b32 v140, -16, v140\n\t"
    "v_mfma_f32_16x16x32_bf16 v[116:119], v[168:171], v[208:211], v[116:119]\n\t"
    "v_fmac_f32 v121, v104, v28\n\t"
    "v_fmac_f32 v120, v105, v29\n\t"
    "ds_read_b128 v[164:167], v137 offset:0\n\t"
    "ds_read_b128 v[168:171], v137 offset:64\n\t"
    "v_mfma_f32_16x16x32_bf16 v[92:95], v[172:175], v[196:199], 0\n\t"
    "v_fmac_f32 v121, v106, v30\n\t"
    "v_fmac_f32 v120, v107, v31\n\t"
    "v_mfma_f32_16x16x32_bf16 v[92:95], v[176:179], v[200:203], v[92:95]\n\t"
    "v_fmac_f32 v121, v108, v32\n\t"
    "v_fmac_f32 v120, v109, v33\n\t"
    "v_mfma_f32_16x16x32_bf16 v[96:99], v[172:175], v[204:207], 0\n\t"
    "v_fmac_f32 v121, v110, v34\n\t"
    "v_fmac_f32 v120, v111, v35\n\t"
    "v_mfma_f32_16x16x32_bf16 v[96:99], v[176:179], v[208:211], v[96:99]\n\t"
    "v_fmac_f32 v121, v112, v87\n\t"
    "v_mad_u32_u24 v141, v141, s68, v151\n\t"
    "ds_read_b128 v[172:175], v138 offset:0\n\t"
    "ds_read_b128 v[176:179], v138 offset:64\n\t"
    "v_mfma_f32_16x16x32_bf16 v[100:103], v[156:159], v[188:191], 0\n\t"
    "v_fmac_f32 v120, v116, v36\n\t"
    "v_fmac_f32 v121, v117, v37\n\t"
    "v_mfma_f32_16x16x32_bf16 v[100:103], v[160:163], v[192:195], v[100:103]\n\t"
    "v_fmac_f32 v120, v118, v38\n\t"
    "v_fmac_f32 v121, v119, v39\n\t"
    "v_mfma_f32_16x16x32_bf16 v[104:107], v[180:183], v[188:191], 0\n\t"
    "v_and_b32 v141, -16, v141\n\t"
    "v_fmac_f32 v120, v92, v40\n\t"
    "v_mfma_f32_16x16x32_bf16 v[104:107], v[184:187], v[192:195], v[104:107]\n\t"
    "v_fmac_f32 v121, v93, v41\n\t"
    "v_fmac_f32 v120, v94, v42\n\t"
    "v_mfma_f32_16x16x32_bf16 v[108:111], v[180:183], v[196:199], 0\n\t"
    "v_fmac_f32 v121, v95, v43\n\t"
    "v_fmac_f32 v120, v96, v44\n\t"
    "v_mfma_f32_16x16x32_bf16 v[108:111], v[184:187], v[200:203], v[108:111]\n\t"
    "v_fmac_f32 v121, v97, v45\n\t"
    "v_fmac_f32 v120, v98, v46\n\t"
    "v_mfma_f32_16x16x32_bf16 v[112:115], v[180:183], v[204:207], 0\n\t"
    "v_fmac_f32 v121, v99, v47\n\t"
    "v_fmac_f32 v120, v100, v88\n\t"
    "v_mfma_f32_16x16x32_bf16 v[112:115], v[184:187], v[208:211], v[112:115]\n\t"
    "v_mad_u32_u24 v142, v142, s68, v151\n\t"
    "v_and_b32 v142, -16, v142\n\t"
    "ds_read_b128 v[180:183], v140 offset:0\n\t"
    "ds_read_b128 v[184:187], v140 offset:64\n\t"
    "s_cmp_eq_u32 s45, 1\n\t"
    "s_cbranch_scc1 L_sig_%=\n\t"
    "L_sigb_%=:\n\t"
    "v_mfma_f32_16x16x32_bf16 v[116:119], v[156:159], v[196:199], 0\n\t"
    "v_fmac_f32 v121, v104, v48\n\t"
    "v_fmac_f32 v120, v105, v49\n\t"
    "v_mfma_f32_16x16x32_bf16 v[116:119], v[160:163], v[200:203], v[116:119]\n\t"
    "v_fmac_f32 v121, v106, v50\n\t"
    "v_fmac_f32 v120, v107, v51\n\t"
    "v_mfma_f32_16x16x32_bf16 v[92:95], v[188:191], v[188:191], 0\n\t"
    "v_mad_u32_u24 v143, v143, s68, v151\n\t"
    "v_and_b32 v143, -16, v143\n\t"
    "v_mfma_f32_16x16x32_bf16 v[92:95], v[192:195], v[192:195], v[92:95]\n\t"
    "v_fmac_f32 v121, v108, v52\n\t"
    "v_fmac_f32 v120, v109, v53\n\t"
    "v_mfma_f32_16x16x32_bf16 v[96:99], v[188:191], v[196:199], 0\n\t"
    "v_fmac_f32 v121, v110, v54\n\t"
    "v_fmac_f32 v120, v111, v55\n\t"
    "v_mfma_f32_16x16x32_bf16 v[96:99], v[192:195], v[200:203], v[96:99]\n\t"
    "v_fmac_f32 v121, v112, v56\n\t"
    "v_fmac_f32 v120, v113, v57\n\t"
    "v_mfma_f32_16x16x32_bf16 v[100:103], v[188:191], v[204:207], 0\n\t"
    "v_fmac_f32 v121, v114, v58\n\t"
    "v_fmac_f32 v120, v115, v59\n\t"
    "v_mfma_f32_16x16x32_bf16 v[100:103], v[192:195], v[208:211], v[100:103]\n\t"
    "v_fmac_f32 v121, v116, v89\n\t"
    "ds_read_b128 v[188:191], v141 offset:0\n\t"
    "ds_read_b128 v[192:195], v141 offset:64\n\t"
    "v_mfma_f32_16x16x32_bf16 v[104:107], v[156:159], v[204:207], 0\n\t"
    "v_fmac_f32 v120, v92, v60\n\t"
    "v_fmac_f32 v121, v93, v61\n\t"
    "v_mfma_f32_16x16x32_bf16 v[104:107], v[160:163], v[208:211], v[104:107]\n\t"
    "v_fmac_f32 v120, v94, v62\n\t"
    "v_fmac_f32 v121, v95, v63\n\t"
    "v_mfma_f32_16x16x32_bf16 v[108:111], v[196:199], v[196:199], 0\n\t"
    "v_fmac_f32 v120, v96, v64\n\t"
    "v_fmac_f32 v121, v97, v65\n\t"
    "v_mfma_f32_16x16x32_bf16 v[108:111], v[200:203], v[200:203], v[108:111]\n\t"
    "v_fmac_f32 v120, v98, v66\n\t"
    "v_fmac_f32 v121, v99, v67\n\t"
    "v_mfma_f32_16x16x32_bf16 v[112:115], v[196:199], v[204:207], 0\n\t"
    "v_fmac_f32 v120, v100, v68\n\t"
    "v_fmac_f32 v121, v101, v69\n\t"
    "v_mfma_f32_16x16x32_bf16 v[112:115], v[200:203], v[208:211], v[112:115]\n\t"
    "v_fmac_f32 v120, v102, v70\n\t"
    "v_fmac_f32 v121, v103, v71\n\t"
    "ds_read_b128 v[196:199], v142 offset:0\n\t"
    "ds_read_b128 v[200:203], v142 offset:64\n\t"
    "v_mfma_f32_16x16x32_bf16 v[116:119], v[204:207], v[204:207], 0\n\t"
    "v_fmac_f32 v120, v104, v90\n\t"
    "v_mfma_f32_16x16x32_bf16 v[116:119], v[208:211], v[208:211], v[116:119]\n\t"
    "ds_read_b128 v[204:207], v143 offset:0\n\t"
    "ds_read_b128 v[208:211], v143 offset:64\n\t"
    "s_mov_b32 s52, s53\n\t"
    "s_mov_b32 s53, s54\n\t"
    "s_mov_b32 s54, s55\n\t"
    "s_mov_b32 s55, s60\n\t"
    "s_cmp_lt_u32 s54, s64\n\t"
    "s_cbranch_scc1 L_pr0_%=\n\t"
    "s_setprio 0\n\t"
    "L_pr0_%=:\n\t"
    "s_cmp_lt_u32 s53, s64\n\t"
    "s_cbranch_scc0 L_done_%=\n\t"
    "s_mul_i32 s69, s52, 272\n\t"
    "s_add_u32 s69, s69, 130048\n\t"
    "s_mul_i32 s66, s55, 400\n\t"
    "s_waitcnt lgkmcnt(8)\n\t"
    "v_mfma_f32_16x16x32_bf16 v[92:95], v[212:215], v[212:215], 0\n\t"
    "v_add_u32 v147, s69, v146\n\t"
    "v_fmac_f32 v121, v108, v72\n\t"
    "ds_add_rtn_u32 v148, v149, v150\n\t"
    "v_mfma_f32_16x16x32_bf16 v[92:95], v[216:219], v[216:219], v[92:95]\n\t"
    "v_fmac_f32 v120, v109, v73\n\t"
    "v_fmac_f32 v121, v110, v74\n\t"
    "buffer_load_dwordx3 v[136:138], v153, s[96:99], s66 offen\n\t"
    "v_mfma_f32_16x16x32_bf16 v[96:99], v[164:167], v[164:167], 0\n\t"
    "v_fmac_f32 v120, v111, v75\n\t"
    "v_fmac_f32 v121, v112, v76\n\t"
    "v_mfma_f32_16x16x32_bf16 v[96:99], v[168:171], v[168:171], v[96:99]\n\t"
    "v_fmac_f32 v120, v113, v77\n\t"
    "v_fmac_f32 v121, v114, v78\n\t"
    "buffer_load_dwordx4 v[140:143], v153, s[96:99], s66 offen offset:12\n\t"
    "v_mfma_f32_16x16x32_bf16 v[100:103], v[164:167], v[172:175], 0\n\t"
    "v_fmac_f32 v120, v115, v79\n\t"
    "v_fmac_f32 v121, v116, v80\n\t"
    "v_mfma_f32_16x16x32_bf16 v[100:103], v[168:171], v[176:179], v[100:103]\n\t"
    "v_fmac_f32 v120, v117, v81\n\t"
    "v_fmac_f32 v121, v118, v82\n\t"
    "s_waitcnt vmcnt(3)\n\t"
    "s_cmp_ge_u32 s55, s61\n\t"
    "s_cbranch_scc1 L_req1_%=\n\t"
    "L_back1_%=:\n\t"
    "v_mfma_f32_16x16x32_bf16 v[104:107], v[172:175], v[172:175], 0\n\t"
    "v_mad_u32_u24 v128, v128, s68, v152\n\t"
    "v_and_b32 v128, -16, v128\n\t"
    "v_mfma_f32_16x16x32_bf16 v[104:107], v[176:179], v[176:179], v[104:107]\n\t"
    "v_fmac_f32 v120, v119, v83\n\t"
    "v_add_f32 v154, v120, v121\n\t"
    "ds_write_b32 v147, v154\n\t"
    "v_mfma_f32_16x16x32_bf16 v[108:111], v[212:215], v[164:167], 0\n\t"
    "v_fma_f32 v120, v92, v84, v145\n\t"
    "v_mul_f32 v121, v96, v0\n\t"
    "v_mfma_f32_16x16x32_bf16 v[108:111], v[216:219], v[168:171], v[108:111]\n\t"
    "v_fmac_f32 v120, v97, v1\n\t"
    "v_fmac_f32 v121, v98, v2\n\t"
    "s_waitcnt lgkmcnt(8)\n\t"
    "v_mfma_f32_16x16x32_bf16 v[112:115], v[164:167], v[180:183], 0\n\t"
    "v_fmac_f32 v120, v99, v3\n\t"
    "v_fmac_f32 v121, v100, v4\n\t"
    "v_mfma_f32_16x16x32_bf16 v[112:115], v[168:171], v[184:187], v[112:115]\n\t"
    "v_fmac_f32 v120, v101, v5\n\t"
    "v_fmac_f32 v121, v102, v6\n\t"
    "v_mfma_f32_16x16x32_bf16 v[116:119], v[172:175], v[180:183], 0\n\t"
    "v_fmac_f32 v120, v103, v7\n\t"
    "v_fmac_f32 v121, v104, v8\n\t"
    "v_mfma_f32_16x16x32_bf16 v[116:119], v[176:179], v[184:187], v[116:119]\n\t"
    "v_fmac_f32 v120, v105, v9\n\t"
    "v_fmac_f32 v121, v106, v10\n\t"
    "ds_read_b128 v[156:159], v128 offset:0\n\t"
    "ds_read_b128 v[160:163], v128 offset:64\n\t"
    "v_mfma_f32_16x16x32_bf16 v[92:95], v[180:183], v[180:183], 0\n\t"
    "v_fmac_f32 v120, v107, v11\n\t"
    "v_mad_u32_u24 v129, v129, s68, v151\n\t"
    "v_mfma_f32_16x16x32_bf16 v[92:95], v[184:187], v[184:187], v[92:95]\n\t"
    "v_and_b32 v129, -16, v129\n\t"
    "v_fmac_f32 v121, v108, v85\n\t"
    "v_mfma_f32_16x16x32_bf16 v[96:99], v[212:215], v[172:175], 0\n\t"
    "v_fmac_f32 v120, v112, v12\n\t"
    "v_fmac_f32 v121, v113, v13\n\t"
    "v_mfma_f32_16x16x32_bf16 v[96:99], v[216:219], v[176:179], v[96:99]\n\t"
    "v_fmac_f32 v120, v114, v14\n\t"
    "v_fmac_f32 v121, v115, v15\n\t"
    "s_lshl_b32 s51, s55, 8\n\t"
    "s_add_u32 s51, s51, s50\n\t"
    "s_waitcnt lgkmcnt(8)\n\t"
    "v_mfma_f32_16x16x32_bf16 v[100:103], v[164:167], v[188:191], 0\n\t"
    "v_mad_u32_u24 v130, v130, s68, v151\n\t"
    "v_and_b32 v130, -16, v130\n\t"
    "v_mfma_f32_16x16x32_bf16 v[100:103], v[168:171], v[192:195], v[100:103]\n\t"
    "v_fmac_f32 v120, v116, v16\n\t"
    "v_fmac_f32 v121, v117, v17\n\t"
    "buffer_load_dword v145, v146, %12, s51 offen sc1\n\t"
    "v_mfma_f32_16x16x32_bf16 v[104:107], v[172:175], v[188:191], 0\n\t"
    "v_fmac_f32 v120, v118, v18\n\t"
    "v_fmac_f32 v121, v119, v19\n\t"
    "v_mfma_f32_16x16x32_bf16 v[104:107], v[176:179], v[192:195], v[104:107]\n\t"
    "v_fmac_f32 v120, v92, v20\n\t"
    "v_fmac_f32 v121, v93, v21\n\t"
    "s_waitcnt lgkmcnt(6)\n\t"
    "v_mfma_f32_16x16x32_bf16 v[108:111], v[164:167], v[196:199], 0\n\t"
    "v_fmac_f32 v120, v94, v22\n\t"
    "v_fmac_f32 v121, v95, v23\n\t"
    "v_mfma_f32_16x16x32_bf16 v[108:111], v[168:171], v[200:203], v[108:111]\n\t"
    "v_fmac_f32 v120, v96, v86\n\t"
    "v_mad_u32_u24 v132, v132, s68, v151\n\t"
    "v_mfma_f32_16x16x32_bf16 v[112:115], v[212:215], v[180:183], 0\n\t"
    "v_fmac_f32 v121, v100, v24\n\t"
    "v_fmac_f32 v120, v101, v25\n\t"
    "v_mfma_f32_16x16x32_bf16 v[112:115], v[216:219], v[184:187], v[112:115]\n\t"
    "v_fmac_f32 v121, v102, v26\n\t"
    "v_fmac_f32 v120, v103, v27\n\t"
    "s_waitcnt lgkmcnt(3)\n\t"
    "v_mfma_f32_16x16x32_bf16 v[116:119], v[164:167], v[204:207], 0\n\t"
    "v_readfirstlane_b32 s60, v148\n\t"
    "v_and_b32 v132, -16, v132\n\t"
    "v_mfma_f32_16x16x32_bf16 v[116:119], v[168:171], v[208:211], v[116:119]\n\t"
    "v_fmac_f32 v121, v104, v28\n\t"
    "v_fmac_f32 v120, v105, v29\n\t"
    "ds_read_b128 v[164:167], v129 offset:0\n\t"
    "ds_read_b128 v[168:171], v129 offset:64\n\t"
    "v_mfma_f32_16x16x32_bf16 v[92:95], v[172:175], v[196:199], 0\n\t"
    "v_fmac_f32 v121, v106, v30\n\t"
    "v_fmac_f32 v120, v107, v31\n\t"
    "v_mfma_f32_16x16x32_bf16 v[92:95], v[176:179], v[200:203], v[92:95]\n\t"
    "v_fmac_f32 v121, v108, v32\n\t"
    "v_fmac_f32 v120, v109, v33\n\t"
    "v_mfma_f32_16x16x32_bf16 v[96:99], v[172:175], v[204:207], 0\n\t"
    "v_fmac_f32 v121, v110, v34\n\t"
    "v_fmac_f32 v120, v111, v35\n\t"
    "v_mfma_f32_16x16x32_bf16 v[96:99], v[176:179], v[208:211], v[96:99]\n\t"
    "v_fmac_f32 v121, v112, v87\n\t"
    "v_mad_u32_u24 v133, v133, s68, v151\n\t"
    "ds_read_b128 v[172:175], v130 offset:0\n\t"
    "ds_read_b128 v[176:179], v130 offset:64\n\t"
    "v_mfma_f32_16x16x32_bf16 v[100:103], v[212:215], v[188:191], 0\n\t"
    "v_fmac_f32 v120, v116, v36\n\t"
    "v_fmac_f32 v121, v117, v37\n\t"
    "v_mfma_f32_16x16x32_bf16 v[100:103], v[216:219], v[192:195], v[100:103]\n\t"
    "v_fmac_f32 v120, v118, v38\n\t"
    "v_fmac_f32 v121, v119, v39\n\t"
    "v_mfma_f32_16x16x32_bf16 v[104:107], v[180:183], v[188:191], 0\n\t"
    "v_and_b32 v133, -16, v133\n\t"
    "v_fmac_f32 v120, v92, v40\n\t"
    "v_mfma_f32_16x16x32_bf16 v[104:107], v[184:187], v[192:195], v[104:107]\n\t"
    "v_fmac_f32 v121, v93, v41\n\t"
    "v_fmac_f32 v120, v94, v42\n\t"
    "v_mfma_f32_16x16x32_bf16 v[108:111], v[180:183], v[196:199], 0\n\t"
    "v_fmac_f32 v121, v95, v43\n\t"
    "v_fmac_f32 v120, v96, v44\n\t"
    "v_mfma_f32_16x16x32_bf16 v[108:111], v[184:187], v[200:203], v[108:111]\n\t"
    "v_fmac_f32 v121, v97, v45\n\t"
    "v_fmac_f32 v120, v98, v46\n\t"
    "v_mfma_f32_16x16x32_bf16 v[112:115], v[180:183], v[204:207], 0\n\t"
    "v_fmac_f32 v121, v99, v47\n\t"
    "v_fmac_f32 v120, v100, v88\n\t"
    "v_mfma_f32_16x16x32_bf16 v[112:115], v[184:187], v[208:211], v[112:115]\n\t"
    "v_mad_u32_u24 v134, v134, s68, v151\n\t"
    "v_and_b32 v134, -16, v134\n\t"
    "ds_read_b128 v[180:183], v132 offset:0\n\t"
    "ds_read_b128 v[184:187], v132 offset:64\n\t"
    "v_mfma_f32_16x16x32_bf16 v[116:119], v[212:215], v[196:199], 0\n\t"
    "v_fmac_f32 v121, v104, v48\n\t"
    "v_fmac_f32 v120, v105, v49\n\t"
    "v_mfma_f32_16x16x32_bf16 v[116:119], v[216:219], v[200:203], v[116:119]\n\t"
    "v_fmac_f32 v121, v106, v50\n\t"
    "v_fmac_f32 v120, v107, v51\n\t"
    "v_mfma_f32_16x16x32_bf16 v[92:95], v[188:191], v[188:191], 0\n\t"
    "v_mad_u32_u24 v135, v135, s68, v151\n\t"
    "v_and_b32 v135, -16, v135\n\t"
    "v_mfma_f32_16x16x32_bf16 v[92:95], v[192:195], v[192:195], v[92:95]\n\t"
    "v_fmac_f32 v121, v108, v52\n\t"
    "v_fmac_f32 v120, v109, v53\n\t"
    "v_mfma_f32_16x16x32_bf16 v[96:99], v[188:191], v[196:199], 0\n\t"
    "v_fmac_f32 v121, v110, v54\n\t"
    "v_fmac_f32 v120, v111, v55\n\t"
    "v_mfma_f32_16x16x32_bf16 v[96:99], v[192:195], v[200:203], v[96:99]\n\t"
    "v_fmac_f32 v121, v112, v56\n\t"
    "v_fmac_f32 v120, v113, v57\n\t"
    "v_mfma_f32_16x16x32_bf16 v[100:103], v[188:191], v[204:207], 0\n\t"
    "v_fmac_f32 v121, v114, v58\n\t"
    "v_fmac_f32 v120, v115, v59\n\t"
    "v_mfma_f32_16x16x32_bf16 v[100:103], v[192:195], v[208:211], v[100:103]\n\t"
    "v_fmac_f32 v121, v116, v89\n\t"
    "ds_read_b128 v[188:191], v133 offset:0\n\t"
    "ds_read_b128 v[192:195], v133 offset:64\n\t"
    "v_mfma_f32_16x16x32_bf16 v[104:107], v[212:215], v[204:207], 0\n\t"
    "v_fmac_f32 v120, v92, v60\n\t"
    "v_fmac_f32 v121, v93, v61\n\t"
    "v_mfma_f32_16x16x32_bf16 v[104:107], v[216:219], v[208:211], v[104:107]\n\t"
    "v_fmac_f32 v120, v94, v62\n\t"
    "v_fmac_f32 v121, v95, v63\n\t"
    "v_mfma_f32_16x16x32_bf16 v[108:111], v[196:199], v[196:199], 0\n\t"
    "v_fmac_f32 v120, v96, v64\n\t"
    "v_fmac_f32 v121, v97, v65\n\t"
    "v_mfma_f32_16x16x32_bf16 v[108:111], v[200:203], v[200:203], v[108:111]\n\t"
    "v_fmac_f32 v120, v98, v66\n\t"
    "v_fmac_f32 v121, v99, v67\n\t"
    "v_mfma_f32_16x16x32_bf16 v[112:115], v[196:199], v[204:207], 0\n\t"
    "v_fmac_f32 v120, v100, v68\n\t"
    "v_fmac_f32 v121, v101, v69\n\t"
    "v_mfma_f32_16x16x32_bf16 v[112:115], v[200:203], v[208:211], v[112:115]\n\t"
    "v_fmac_f32 v120, v102, v70\n\t"
    "v_fmac_f32 v121, v103, v71\n\t"
    "ds_read_b128 v[196:199], v134 offset:0\n\t"
    "ds_read_b128 v[200:203], v134 offset:64\n\t"
    "v_mfma_f32_16x16x32_bf16 v[116:119], v[204:207], v[204:207], 0\n\t"
    "v_fmac_f32 v120, v104, v90\n\t"
    "v_mfma_f32_16x16x32_bf16 v[116:119], v[208:211], v[208:211], v[116:119]\n\t"
    "ds_read_b128 v[204:207], v135 offset:0\n\t"
    "ds_read_b128 v[208:211], v135 offset:64\n\t"
    "s_mov_b32 s52, s53\n\t"
    "s_mov_b32 s53, s54\n\t"
    "s_mov_b32 s54, s55\n\t"
    "s_mov_b32 s55, s60\n\t"
    "s_cmp_lt_u32 s54, s64\n\t"
    "s_cbranch_scc1 L_pr1_%=\n\t"
    "s_setprio 0\n\t"
    "L_pr1_%=:\n\t"
    "s_cmp_lt_u32 s53, s64\n\t"
    "s_cbranch_scc1 L_pnn_%=\n\t"
    "L_done_%=:\n\t"
    "s_mul_i32 s69, s52, 272\n\t"
    "s_add_u32 s69, s69, 130048\n\t"
    "v_add_u32 v147, s69, v146\n\t"
    "v_fmac_f32 v121, v108, v72\n\t"
    "v_fmac_f32 v120, v109, v73\n\t"
    "v_fmac_f32 v121, v110, v74\n\t"
    "v_fmac_f32 v120, v111, v75\n\t"
    "v_fmac_f32 v121, v112, v76\n\t"
    "v_fmac_f32 v120, v113, v77\n\t"
    "v_fmac_f32 v121, v114, v78\n\t"
    "v_fmac_f32 v120, v115, v79\n\t"
    "v_fmac_f32 v121, v116, v80\n\t"
    "v_fmac_f32 v120, v117, v81\n\t"
    "v_fmac_f32 v121, v118, v82\n\t"
    "v_fmac_f32 v120, v119, v83\n\t"
    "v_add_f32 v154, v120, v121\n\t"
    "ds_write_b32 v147, v154\n\t"
    "v_lshrrev_b32 v147, 2, v146\n\t"
    "s_mov_b64 s[92:93], %17\n\t"
    "s_and_b32 s93, s93, 0xffff\n\t"
    "s_mov_b32 s94, 32768\n\t"
    "s_mov_b32 s95, 0x20000\n\t"
    "s_lshl_b32 s44, s37, 1\n\t"
    "v_and_b32 v155, 15, v147\n\t"
    "v_lshl_add_u32 v155, %16, 4, v155\n\t"
    "v_lshrrev_b32 v154, 4, v147\n\t"
    "v_mul_u32_u24 v122, 272, v155\n\t"
    "v_lshl_add_u32 v122, v154, 6, v122\n\t"
    "v_add_u32 v122, 130048, v122\n\t"
    "v_mul_u32_u24 v154, 2688, v154\n\t"
    "v_lshl_add_u32 v154, v155, 2, v154\n\t"
    "v_add_u32 v154, 152896, v154\n\t"
    "v_cmp_le_u32 vcc, 48, v147\n\t"
    "v_cmp_gt_u32 s[46:47], s38, v155\n\t"
    "v_lshlrev_b32 v155, 1, v155\n\t"
    "s_and_b64 vcc, vcc, s[46:47]\n\t"
    "s_waitcnt lgkmcnt(0)\n\t"
    "s_barrier\n\t"
    "s_cbranch_vccz L_fin_%=\n\t"
    "ds_read_b128 v[92:95], v122 offset:0\n\t"
    "ds_read_b128 v[96:99], v122 offset:16\n\t"
    "ds_read_b128 v[100:103], v122 offset:32\n\t"
    "ds_read_b128 v[104:107], v122 offset:48\n\t"
    "ds_read2_b32 v[108:109], v154 offset0:0 offset1:84\n\t"
    "ds_read2_b32 v[110:111], v154 offset0:168 offset1:252\n\t"
    "v_add_u32 v154, 1344, v154\n\t"
    "ds_read2_b32 v[112:113], v154 offset0:0 offset1:84\n\t"
    "ds_read2_b32 v[114:115], v154 offset0:168 offset1:252\n\t"
    "s_waitcnt lgkmcnt(6)\n\t"
    "v_add_f32 v92, v92, v96\n\t"
    "v_add_f32 v93, v93, v97\n\t"
    "v_add_f32 v94, v94, v98\n\t"
    "v_add_f32 v95, v95, v99\n\t"
    "s_waitcnt lgkmcnt(5)\n\t"
    "v_add_f32 v92, v92, v100\n\t"
    "v_add_f32 v93, v93, v101\n\t"
    "v_add_f32 v94, v94, v102\n\t"
    "v_add_f32 v95, v95, v103\n\t"
    "s_waitcnt lgkmcnt(4)\n\t"
    "v_add_f32 v92, v92, v104\n\t"
    "v_add_f32 v93, v93, v105\n\t"
    "v_add_f32 v94, v94, v106\n\t"
    "v_add_f32 v95, v95, v107\n\t"
    "s_waitcnt lgkmcnt(2)\n\t"
    "v_add_f32 v92, v92, v108\n\t"
    "v_add_f32 v93, v93, v109\n\t"
    "v_add_f32 v94, v94, v110\n\t"
    "v_add_f32 v95, v95, v111\n\t"
    "s_waitcnt lgkmcnt(0)\n\t"
    "v_add_f32 v92, v92, v112\n\t"
    "v_add_f32 v93, v93, v113\n\t"
    "v_add_f32 v94, v94, v114\n\t"
    "v_add_f32 v95, v95, v115\n\t"
    "v_add_f32 v92, v92, v93\n\t"
    "v_add_f32 v94, v94, v95\n\t"
    "v_add_f32 v92, v92, v94\n\t"
    "v_mov_b32 v93, v92\n\t"
    "s_nop 1\n\t"
    "v_permlane16_swap_b32 v93, v92\n\t"
    "s_nop 1\n\t"
    "v_add_f32 v92, v92, v93\n\t"
    "v_mov_b32 v93, v92\n\t"
    "s_nop 1\n\t"
    "v_permlane32_swap_b32 v93, v92\n\t"
    "s_nop 1\n\t"
    "v_add_f32 v92, v92, v93\n\t"
    "v_add_f32 v92, s39, v92\n\t"
    "v_cvt_pk_bf16_f32 v92, v92, v92\n\t"
    "s_mov_b64 exec, vcc\n\t"
    "buffer_store_short v92, v155, s[92:95], s44 offen\n\t"
    "s_mov_b64 exec, -1\n\t"
    "s_branch L_fin_%=\n\t"
    "L_req0_%=:\n\t"
    "s_cmp_lg_u32 s61, 0\n\t"
    "s_cbranch_scc1 L_req0b_%=\n\t"
    "s_bfe_u32 s65, s84, 0xc0002\n\t"
    "s_add_u32 s65, s65, s36\n\t"
    "buffer_store_dword v236, v125, %12, s65 offen offset:0 sc1\n\t"
    "buffer_store_dword v237, v125, %12, s65 offen offset:256 sc1\n\t"
    "s_bfe_u32 s65, s85, 0xc0002\n\t"
    "s_add_u32 s65, s65, s36\n\t"
    "buffer_store_dword v238, v125, %12, s65 offen offset:0 sc1\n\t"
    "buffer_store_dword v239, v125, %12, s65 offen offset:256 sc1\n\t"
    "s_bfe_u32 s65, s86, 0xc0002\n\t"
    "s_add_u32 s65, s65, s36\n\t"
    "buffer_store_dword v240, v125, %12, s65 offen offset:0 sc1\n\t"
    "buffer_store_dword v241, v125, %12, s65 offen offset:256 sc1\n\t"
    "s_bfe_u32 s65, s87, 0xc0002\n\t"
    "s_add_u32 s65, s65, s36\n\t"
    "buffer_store_dword v242, v125, %12, s65 offen offset:0 sc1\n\t"
    "buffer_store_dword v243, v125, %12, s65 offen offset:256 sc1\n\t"
    "s_bfe_u32 s65, s88, 0xc0002\n\t"
    "s_add_u32 s65, s65, s36\n\t"
    "buffer_store_dword v244, v125, %12, s65 offen offset:0 sc1\n\t"
    "buffer_store_dword v245, v125, %12, s65 offen offset:256 sc1\n\t"
    "s_bfe_u32 s65, s89, 0xc0002\n\t"
    "s_add_u32 s65, s65, s36\n\t"
    "buffer_store_dword v246, v125, %12, s65 offen offset:0 sc1\n\t"
    "buffer_store_dword v247, v125, %12, s65 offen offset:256 sc1\n\t"
    "s_bfe_u32 s65, s90, 0xc0002\n\t"
    "s_add_u32 s65, s65, s36\n\t"
    "buffer_store_dword v248, v125, %12, s65 offen offset:0 sc1\n\t"
    "buffer_store_dword v249, v125, %12, s65 offen offset:256 sc1\n\t"
    "s_bfe_u32 s65, s91, 0xc0002\n\t"
    "s_add_u32 s65, s65, s36\n\t"
    "buffer_store_dword v250, v125, %12, s65 offen offset:0 sc1\n\t"
    "buffer_store_dword v251, v125, %12, s65 offen offset:256 sc1\n\t"
    "s_sub_u32 s61, s64, 3\n\t"
    "s_branch L_back0_%=\n\t"
    "L_req0b_%=:\n\t"
    "s_cmp_lg_u32 s61, 1\n\t"
    "s_cbranch_scc1 L_req0c_%=\n\t"
    "v_lshrrev_b32 v154, 16, v122\n\t"
    "v_lshl_add_u32 v154, %16, 2, v154\n\t"
    "v_mul_u32_u24 v154, 336, v154\n\t"
    "v_and_b32 v122, 0xf0, v122\n\t"
    "v_cmp_gt_u32 vcc, 80, v122\n\t"
    "v_add3_u32 v122, v122, v154, s45\n\t"
    "s_mov_b32 s61, -1\n\t"
    "v_add_f32 v124, v220, v224\n\t"
    "v_add_f32 v154, v228, v232\n\t"
    "v_add_f32 v124, v124, v154\n\t"
    "v_add_f32 v125, v221, v225\n\t"
    "v_add_f32 v154, v229, v233\n\t"
    "v_add_f32 v125, v125, v154\n\t"
    "v_add_f32 v126, v222, v226\n\t"
    "v_add_f32 v154, v230, v234\n\t"
    "v_add_f32 v126, v126, v154\n\t"
    "v_add_f32 v127, v223, v227\n\t"
    "v_add_f32 v154, v231, v235\n\t"
    "v_add_f32 v127, v127, v154\n\t"
    "ds_write_b128 v122, v[124:127] offset:0\n\t"
    "s_cmp_le_u32 s38, 64\n\t"
    "s_cbranch_scc1 L_back0_%=\n\t"
    "v_add_f32 v124, v236, v240\n\t"
    "v_add_f32 v154, v244, v248\n\t"
    "v_add_f32 v124, v124, v154\n\t"
    "v_add_f32 v125, v237, v241\n\t"
    "v_add_f32 v154, v245, v249\n\t"
    "v_add_f32 v125, v125, v154\n\t"
    "v_add_f32 v126, v238, v242\n\t"
    "v_add_f32 v154, v246, v250\n\t"
    "v_add_f32 v126, v126, v154\n\t"
    "v_add_f32 v127, v239, v243\n\t"
    "v_add_f32 v154, v247, v251\n\t"
    "v_add_f32 v127, v127, v154\n\t"
    "s_and_saveexec_b64 s[46:47], vcc\n\t"
    "ds_write_b128 v122, v[124:127] offset:256\n\t"
    "s_mov_b64 exec, s[46:47]\n\t"
    "s_branch L_back0_%=\n\t"
    "L_req0c_%=:\n\t"
    "s_lshl_b32 s46, %14, 7\n\t"
    "s_add_u32 s46, s46, 49295360\n\t"
    "s_mov_b32 s47, 1048576\n\t"
    "s_mov_b64 exec, 1\n\t"
    "s_cmp_ge_u32 s55, s64\n\t"
    "s_cbranch_scc1 L_pq0_%=\n\t"
    "buffer_load_dword v123, off, %12, s46 sc1\n\t"
    "s_mov_b64 exec, -1\n\t"
    "s_branch L_back0_%=\n\t"
    "L_pq0_%=:\n\t"
    "v_cmp_eq_u32 vcc, 0, v123\n\t"
    "s_cbranch_vccz L_in0_%=\n\t"
    "L_poll0_%=:\n\t"
    "buffer_load_dword v155, off, %12, s46 sc1\n\t"
    "s_sub_u32 s47, s47, 1\n\t"
    "s_cmp_eq_u32 s47, 0\n\t"
    "s_cbranch_scc1 L_in0_%=\n\t"
    "s_waitcnt vmcnt(0)\n\t"
    "v_cmp_eq_u32 vcc, 0, v155\n\t"
    "s_cbranch_vccz L_in0_%=\n\t"
    "s_sleep 8\n\t"
    "s_branch L_poll0_%=\n\t"
    "L_in0_%=:\n\t"
    "s_mov_b64 exec, -1\n\t"
    "s_lshl_b32 s46, s37, 2\n\t"
    "s_mul_i32 s47, %16, 1048576\n\t"
    "s_add_u32 s46, s46, s47\n\t"
    "s_add_u32 s46, s46, 17825792\n\t"
    "v_and_b32 v155, 0x80, v122\n\t"
    "v_lshl_add_u32 v155, v155, 23, v122\n\t"
    "buffer_load_dwordx4 v[220:223], v122, %12, s46 offen sc1\n\t"
    "s_add_u32 s46, s46, 262144\n\t"
    "buffer_load_dwordx4 v[224:227], v122, %12, s46 offen sc1\n\t"
    "s_add_u32 s46, s46, 262144\n\t"
    "buffer_load_dwordx4 v[228:231], v122, %12, s46 offen sc1\n\t"
    "s_add_u32 s46, s46, 262144\n\t"
    "buffer_load_dwordx4 v[232:235], v122, %12, s46 offen sc1\n\t"
    "s_add_u32 s46, s46, 262144\n\t"
    "s_mov_b32 s61, 1\n\t"
    "s_cmp_le_u32 s38, 64\n\t"
    "s_cbranch_scc1 L_back0_%=\n\t"
    "s_sub_u32 s46, s46, 262144\n\t"
    "buffer_load_dwordx4 v[248:251], v155, %12, s46 offen offset:256 sc1\n\t"
    "s_sub_u32 s46, s46, 262144\n\t"
    "buffer_load_dwordx4 v[244:247], v155, %12, s46 offen offset:256 sc1\n\t"
    "s_sub_u32 s46, s46, 262144\n\t"
    "buffer_load_dwordx4 v[240:243], v155, %12, s46 offen offset:256 sc1\n\t"
    "s_sub_u32 s46, s46, 262144\n\t"
    "buffer_load_dwordx4 v[236:239], v155, %12, s46 offen offset:256 sc1\n\t"
    "s_branch L_back0_%=\n\t"
    "L_req1_%=:\n\t"
    "s_cmp_lg_u32 s61, 1\n\t"
    "s_cbranch_scc1 L_req1c_%=\n\t"
    "v_lshrrev_b32 v154, 16, v122\n\t"
    "v_lshl_add_u32 v154, %16, 2, v154\n\t"
    "v_mul_u32_u24 v154, 336, v154\n\t"
    "v_and_b32 v122, 0xf0, v122\n\t"
    "v_cmp_gt_u32 vcc, 80, v122\n\t"
    "v_add3_u32 v122, v122, v154, s45\n\t"
    "s_mov_b32 s61, -1\n\t"
    "v_add_f32 v124, v220, v224\n\t"
    "v_add_f32 v154, v228, v232\n\t"
    "v_add_f32 v124, v124, v154\n\t"
    "v_add_f32 v125, v221, v225\n\t"
    "v_add_f32 v154, v229, v233\n\t"
    "v_add_f32 v125, v125, v154\n\t"
    "v_add_f32 v126, v222, v226\n\t"
    "v_add_f32 v154, v230, v234\n\t"
    "v_add_f32 v126, v126, v154\n\t"
    "v_add_f32 v127, v223, v227\n\t"
    "v_add_f32 v154, v231, v235\n\t"
    "v_add_f32 v127, v127, v154\n\t"
    "ds_write_b128 v122, v[124:127] offset:0\n\t"
    "s_cmp_le_u32 s38, 64\n\t"
    "s_cbranch_scc1 L_back1_%=\n\t"
    "v_add_f32 v124, v236, v240\n\t"
    "v_add_f32 v154, v244, v248\n\t"
    "v_add_f32 v124, v124, v154\n\t"
    "v_add_f32 v125, v237, v241\n\t"
    "v_add_f32 v154, v245, v249\n\t"
    "v_add_f32 v125, v125, v154\n\t"
    "v_add_f32 v126, v238, v242\n\t"
    "v_add_f32 v154, v246, v250\n\t"
    "v_add_f32 v126, v126, v154\n\t"
    "v_add_f32 v127, v239, v243\n\t"
    "v_add_f32 v154, v247, v251\n\t"
    "v_add_f32 v127, v127, v154\n\t"
    "s_and_saveexec_b64 s[46:47], vcc\n\t"
    "ds_write_b128 v122, v[124:127] offset:256\n\t"
    "s_mov_b64 exec, s[46:47]\n\t"
    "s_branch L_back1_%=\n\t"
    "L_req1c_%=:\n\t"
    "s_lshl_b32 s46, %14, 7\n\t"
    "s_add_u32 s46, s46, 49295360\n\t"
    "s_mov_b32 s47, 1048576\n\t"
    "s_mov_b64 exec, 1\n\t"
    "s_cmp_ge_u32 s55, s64\n\t"
    "s_cbranch_scc1 L_pq1_%=\n\t"
    "buffer_load_dword v123, off, %12, s46 sc1\n\t"
    "s_mov_b64 exec, -1\n\t"
    "s_branch L_back1_%=\n\t"
    "L_pq1_%=:\n\t"
    "v_cmp_eq_u32 vcc, 0, v123\n\t"
    "s_cbranch_vccz L_in1_%=\n\t"
    "L_poll1_%=:\n\t"
    "buffer_load_dword v155, off, %12, s46 sc1\n\t"
    "s_sub_u32 s47, s47, 1\n\t"
    "s_cmp_eq_u32 s47, 0\n\t"
    "s_cbranch_scc1 L_in1_%=\n\t"
    "s_waitcnt vmcnt(0)\n\t"
    "v_cmp_eq_u32 vcc, 0, v155\n\t"
    "s_cbranch_vccz L_in1_%=\n\t"
    "s_sleep 8\n\t"
    "s_branch L_poll1_%=\n\t"
    "L_in1_%=:\n\t"
    "s_mov_b64 exec, -1\n\t"
    "s_lshl_b32 s46, s37, 2\n\t"
    "s_mul_i32 s47, %16, 1048576\n\t"
    "s_add_u32 s46, s46, s47\n\t"
    "s_add_u32 s46, s46, 17825792\n\t"
    "v_and_b32 v155, 0x80, v122\n\t"
    "v_lshl_add_u32 v155, v155, 23, v122\n\t"
    "buffer_load_dwordx4 v[220:223], v122, %12, s46 offen sc1\n\t"
    "s_add_u32 s46, s46, 262144\n\t"
    "buffer_load_dwordx4 v[224:227], v122, %12, s46 offen sc1\n\t"
    "s_add_u32 s46, s46, 262144\n\t"
    "buffer_load_dwordx4 v[228:231], v122, %12, s46 offen sc1\n\t"
    "s_add_u32 s46, s46, 262144\n\t"
    "buffer_load_dwordx4 v[232:235], v122, %12, s46 offen sc1\n\t"
    "s_add_u32 s46, s46, 262144\n\t"
    "s_mov_b32 s61, 1\n\t"
    "s_cmp_le_u32 s38, 64\n\t"
    "s_cbranch_scc1 L_back1_%=\n\t"
    "s_sub_u32 s46, s46, 262144\n\t"
    "buffer_load_dwordx4 v[248:251], v155, %12, s46 offen offset:256 sc1\n\t"
    "s_sub_u32 s46, s46, 262144\n\t"
    "buffer_load_dwordx4 v[244:247], v155, %12, s46 offen offset:256 sc1\n\t"
    "s_sub_u32 s46, s46, 262144\n\t"
    "buffer_load_dwordx4 v[240:243], v155, %12, s46 offen offset:256 sc1\n\t"
    "s_sub_u32 s46, s46, 262144\n\t"
    "buffer_load_dwordx4 v[236:239], v155, %12, s46 offen offset:256 sc1\n\t"
    "s_branch L_back1_%=\n\t"
    "L_sig_%=:\n\t"
    "v_subrev_u32 v155, s49, v149\n\t"
    "v_add_u32 v155, 1024, v155\n\t"
    "s_waitcnt vmcnt(0)\n\t"
    "ds_add_rtn_u32 v148, v155, v150\n\t"
    "s_mov_b32 s45, 152896\n\t"
    "s_mov_b32 s46, 49283072\n\t"
    "v_mov_b32 v155, 1\n\t"
    "s_waitcnt lgkmcnt(0)\n\t"
    "v_readfirstlane_b32 s47, v148\n\t"
    "s_nop 0\n\t"
    "s_cmp_lg_u32 s47, 7\n\t"
    "s_cbranch_scc1 L_sigb_%=\n\t"
    "s_mov_b64 exec, 1\n\t"
    "buffer_atomic_add v155, off, %12, s46 sc0 sc1\n\t"
    "s_mov_b64 exec, -1\n\t"
    "s_waitcnt vmcnt(0)\n\t"
    "v_readfirstlane_b32 s47, v155\n\t"
    "v_lshlrev_b32 v155, 5, v146\n\t"
    "s_cmp_lg_u32 s47, 255\n\t"
    "s_cbranch_scc1 L_sigb_%=\n\t"
    "s_mov_b32 s46, 49295360\n\t"
    "buffer_store_dword v150, v155, %12, s46 offen sc1\n\t"
    "s_add_u32 s46, s46, 0x2000\n\t"
    "buffer_store_dword v150, v155, %12, s46 offen sc1\n\t"
    "s_add_u32 s46, s46, 0x2000\n\t"
    "buffer_store_dword v150, v155, %12, s46 offen sc1\n\t"
    "s_add_u32 s46, s46, 0x2000\n\t"
    "buffer_store_dword v150, v155, %12, s46 offen sc1\n\t"
    "s_add_u32 s46, s46, 0x2000\n\t"
    "s_branch L_sigb_%=\n\t"
    "L_fin_%=:\n\t"
    "s_branch L_pend_%=\n\t"
    "L_pub_%=:\n\t"
    "s_memrealtime s[46:47]\n\t"
    "s_lshl_b32 s49, %14, 2\n\t"
    "s_add_u32 s49, s49, 49293312\n\t"
    "s_waitcnt lgkmcnt(0)\n\t"
    "s_sub_u32 s47, s46, s44\n\t"
    "s_mul_i32 s47, s47, 320\n\t"
    "s_lshr_b32 s47, s47, 8\n\t"
    "s_add_u32 s46, s46, s47\n\t"
    "s_or_b32 s46, s46, 1\n\t"
    "v_mov_b32 v83, s46\n\t"
    "s_mov_b64 exec, 1\n\t"
    "buffer_store_dword v83, off, %12, s49 sc1\n\t"
    "s_mov_b64 exec, -1\n\t"
    "s_branch L_pubb_%=\n\t"
    "L_part0_%=:\n\t"
    "s_mov_b32 s47, 0\n\t"
    "s_call_b64 s[98:99], L_bld_%=\n\t"
    "s_branch L_partb0_%=\n\t"
    "L_part1_%=:\n\t"
    "s_mov_b32 s47, 1048576\n\t"
    "s_waitcnt vmcnt(8)\n\t"
    "s_call_b64 s[98:99], L_bchk_%=\n\t"
    "s_branch L_partb1_%=\n\t"
    "L_bchk_%=:\n\t"
    "v_min_u32 v132, v128, v129\n\t"
    "v_min3_u32 v132, v132, v130, v131\n\t"
    "v_cmp_eq_u32 vcc, 0, v132\n\t"
    "s_cbranch_vccz L_bok_%=\n\t"
    "L_bld_%=:\n\t"
    "s_mov_b32 s46, 49293312\n\t"
    "buffer_load_dwordx4 v[128:131], v127, %12, s46 offen sc1\n\t"
    "s_cmp_eq_u32 s47, 0\n\t"
    "s_cbranch_scc1 L_bret_%=\n\t"
    "s_sub_u32 s47, s47, 1\n\t"
    "s_sleep 16\n\t"
    "s_waitcnt vmcnt(0)\n\t"
    "s_branch L_bchk_%=\n\t"
    "L_bok_%=:\n\t"
    "v_readfirstlane_b32 s46, v128\n\t"
    "v_mov_b32 v138, -4096\n\t"
    "v_mov_b32 v139, 4096\n\t"
    "v_subrev_u32 v128, s46, v128\n\t"
    "v_subrev_u32 v129, s46, v129\n\t"
    "v_subrev_u32 v130, s46, v130\n\t"
    "v_subrev_u32 v131, s46, v131\n\t"
    "v_writelane_b32 v128, 50, 0\n\t"
    "v_add3_u32 v132, v128, v129, v130\n\t"
    "v_add_u32 v132, v132, v131\n\t"
    "s_nop 1\n\t"
    "v_add_u32_dpp v132, v132, v132 row_shr:1 row_mask:0xf bank_mask:0xf\n\t"
    "s_nop 1\n\t"
    "v_add_u32_dpp v132, v132, v132 row_shr:2 row_mask:0xf bank_mask:0xf\n\t"
    "s_nop 1\n\t"
    "v_add_u32_dpp v132, v132, v132 row_shr:4 row_mask:0xf bank_mask:0xf\n\t"
    "s_nop 1\n\t"
    "v_add_u32_dpp v132, v132, v132 row_shr:8 row_mask:0xf bank_mask:0xf\n\t"
    "s_nop 1\n\t"
    "v_add_u32_dpp v132, v132, v132 row_bcast:15 row_mask:0xa bank_mask:0xf\n\t"
    "s_nop 1\n\t"
    "v_add_u32_dpp v132, v132, v132 row_bcast:31 row_mask:0xc bank_mask:0xf\n\t"
    "s_nop 0\n\t"
    "v_readlane_b32 s46, v132, 63\n\t"
    "s_movk_i32 s47, 20\n\t"
    "v_mov_b32 v133, -8192\n\t"
    "v_mov_b32 v134, 5120\n\t"
    "s_ashr_i32 s46, s46, 8\n\t"
    "v_sub_u32 v128, s46, v128\n\t"
    "v_med3_i32 v128, v128, v138, v139\n\t"
    "v_mul_i32_i24 v128, s47, v128\n\t"
    "v_med3_i32 v128, v128, v133, v134\n\t"
    "v_and_b32 v140, 0x3ff, v128\n\t"
    "v_ashrrev_i32 v128, 10, v128\n\t"
    "v_sub_u32 v129, s46, v129\n\t"
    "v_med3_i32 v129, v129, v138, v139\n\t"
    "v_mul_i32_i24 v129, s47, v129\n\t"
    "v_med3_i32 v129, v129, v133, v134\n\t"
    "v_and_b32 v141, 0x3ff, v129\n\t"
    "v_ashrrev_i32 v129, 10, v129\n\t"
    "v_sub_u32 v130, s46, v130\n\t"
    "v_med3_i32 v130, v130, v138, v139\n\t"
    "v_mul_i32_i24 v130, s47, v130\n\t"
    "v_med3_i32 v130, v130, v133, v134\n\t"
    "v_and_b32 v142, 0x3ff, v130\n\t"
    "v_ashrrev_i32 v130, 10, v130\n\t"
    "v_sub_u32 v131, s46, v131\n\t"
    "v_med3_i32 v131, v131, v138, v139\n\t"
    "v_mul_i32_i24 v131, s47, v131\n\t"
    "v_med3_i32 v131, v131, v133, v134\n\t"
    "v_and_b32 v143, 0x3ff, v131\n\t"
    "v_ashrrev_i32 v131, 10, v131\n\t"
    "v_add3_u32 v132, v128, v129, v130\n\t"
    "v_add_u32 v132, v132, v131\n\t"
    "s_nop 1\n\t"
    "v_add_u32_dpp v132, v132, v132 row_shr:1 row_mask:0xf bank_mask:0xf\n\t"
    "s_nop 1\n\t"
    "v_add_u32_dpp v132, v132, v132 row_shr:2 row_mask:0xf bank_mask:0xf\n\t"
    "s_nop 1\n\t"
    "v_add_u32_dpp v132, v132, v132 row_shr:4 row_mask:0xf bank_mask:0xf\n\t"
    "s_nop 1\n\t"
    "v_add_u32_dpp v132, v132, v132 row_shr:8 row_mask:0xf bank_mask:0xf\n\t"
    "s_nop 1\n\t"
    "v_add_u32_dpp v132, v132, v132 row_bcast:15 row_mask:0xa bank_mask:0xf\n\t"
    "s_nop 1\n\t"
    "v_add_u32_dpp v132, v132, v132 row_bcast:31 row_mask:0xc bank_mask:0xf\n\t"
    "s_nop 0\n\t"
    "v_readlane_b32 s46, v132, 63\n\t"
    "s_movk_i32 s47, 0x400\n\t"
    "s_sub_u32 s48, s47, 512\n\t"
    "s_mov_b32 s49, s46\n\t"
    "v_cmp_le_u32 vcc, s48, v140\n\t"
    "s_bcnt1_i32_b64 s44, vcc\n\t"
    "s_add_u32 s49, s49, s44\n\t"
    "v_cmp_le_u32 vcc, s48, v141\n\t"
    "s_bcnt1_i32_b64 s44, vcc\n\t"
    "s_add_u32 s49, s49, s44\n\t"
    "v_cmp_le_u32 vcc, s48, v142\n\t"
    "s_bcnt1_i32_b64 s44, vcc\n\t"
    "s_add_u32 s49, s49, s44\n\t"
    "v_cmp_le_u32 vcc, s48, v143\n\t"
    "s_bcnt1_i32_b64 s44, vcc\n\t"
    "s_add_u32 s49, s49, s44\n\t"
    "s_cmp_lt_i32 s49, 0\n\t"
    "s_cselect_b32 s47, s48, s47\n\t"
    "s_sub_u32 s48, s47, 256\n\t"
    "s_mov_b32 s49, s46\n\t"
    "v_cmp_le_u32 vcc, s48, v140\n\t"
    "s_bcnt1_i32_b64 s44, vcc\n\t"
    "s_add_u32 s49, s49, s44\n\t"
    "v_cmp_le_u32 vcc, s48, v141\n\t"
    "s_bcnt1_i32_b64 s44, vcc\n\t"
    "s_add_u32 s49, s49, s44\n\t"
    "v_cmp_le_u32 vcc, s48, v142\n\t"
    "s_bcnt1_i32_b64 s44, vcc\n\t"
    "s_add_u32 s49, s49, s44\n\t"
    "v_cmp_le_u32 vcc, s48, v143\n\t"
    "s_bcnt1_i32_b64 s44, vcc\n\t"
    "s_add_u32 s49, s49, s44\n\t"
    "s_cmp_lt_i32 s49, 0\n\t"
    "s_cselect_b32 s47, s48, s47\n\t"
    "s_sub_u32 s48, s47, 128\n\t"
    "s_mov_b32 s49, s46\n\t"
    "v_cmp_le_u32 vcc, s48, v140\n\t"
    "s_bcnt1_i32_b64 s44, vcc\n\t"
    "s_add_u32 s49, s49, s44\n\t"
    "v_cmp_le_u32 vcc, s48, v141\n\t"
    "s_bcnt1_i32_b64 s44, vcc\n\t"
    "s_add_u32 s49, s49, s44\n\t"
    "v_cmp_le_u32 vcc, s48, v142\n\t"
    "s_bcnt1_i32_b64 s44, vcc\n\t"
    "s_add_u32 s49, s49, s44\n\t"
    "v_cmp_le_u32 vcc, s48, v143\n\t"
    "s_bcnt1_i32_b64 s44, vcc\n\t"
    "s_add_u32 s49, s49, s44\n\t"
    "s_cmp_lt_i32 s49, 0\n\t"
    "s_cselect_b32 s47, s48, s47\n\t"
    "s_sub_u32 s48, s47, 64\n\t"
    "s_mov_b32 s49, s46\n\t"
    "v_cmp_le_u32 vcc, s48, v140\n\t"
    "s_bcnt1_i32_b64 s44, vcc\n\t"
    "s_add_u32 s49, s49, s44\n\t"
    "v_cmp_le_u32 vcc, s48, v141\n\t"
    "s_bcnt1_i32_b64 s44, vcc\n\t"
    "s_add_u32 s49, s49, s44\n\t"
    "v_cmp_le_u32 vcc, s48, v142\n\t"
    "s_bcnt1_i32_b64 s44, vcc\n\t"
    "s_add_u32 s49, s49, s44\n\t"
    "v_cmp_le_u32 vcc, s48, v143\n\t"
    "s_bcnt1_i32_b64 s44, vcc\n\t"
    "s_add_u32 s49, s49, s44\n\t"
    "s_cmp_lt_i32 s49, 0\n\t"
    "s_cselect_b32 s47, s48, s47\n\t"
    "s_sub_u32 s48, s47, 32\n\t"
    "s_mov_b32 s49, s46\n\t"
    "v_cmp_le_u32 vcc, s48, v140\n\t"
    "s_bcnt1_i32_b64 s44, vcc\n\t"
    "s_add_u32 s49, s49, s44\n\t"
    "v_cmp_le_u32 vcc, s48, v141\n\t"
    "s_bcnt1_i32_b64 s44, vcc\n\t"
    "s_add_u32 s49, s49, s44\n\t"
    "v_cmp_le_u32 vcc, s48, v142\n\t"
    "s_bcnt1_i32_b64 s44, vcc\n\t"
    "s_add_u32 s49, s49, s44\n\t"
    "v_cmp_le_u32 vcc, s48, v143\n\t"
    "s_bcnt1_i32_b64 s44, vcc\n\t"
    "s_add_u32 s49, s49, s44\n\t"
    "s_cmp_lt_i32 s49, 0\n\t"
    "s_cselect_b32 s47, s48, s47\n\t"
    "s_sub_u32 s47, s47, 32\n\t"
    "s_max_u32 s47, s47, 1\n\t"
    "v_cmp_le_u32 vcc, s47, v140\n\t"
    "s_nop 1\n\t"
    "v_addc_co_u32 v128, vcc, 0, v128, vcc\n\t"
    "v_cmp_le_u32 vcc, s47, v141\n\t"
    "s_nop 1\n\t"
    "v_addc_co_u32 v129, vcc, 0, v129, vcc\n\t"
    "v_cmp_le_u32 vcc, s47, v142\n\t"
    "s_nop 1\n\t"
    "v_addc_co_u32 v130, vcc, 0, v130, vcc\n\t"
    "v_cmp_le_u32 vcc, s47, v143\n\t"
    "s_nop 1\n\t"
    "v_addc_co_u32 v131, vcc, 0, v131, vcc\n\t"
    "v_add3_u32 v132, v128, v129, v130\n\t"
    "v_add_u32 v132, v132, v131\n\t"
    "s_nop 1\n\t"
    "v_add_u32_dpp v132, v132, v132 row_shr:1 row_mask:0xf bank_mask:0xf\n\t"
    "s_nop 1\n\t"
    "v_add_u32_dpp v132, v132, v132 row_shr:2 row_mask:0xf bank_mask:0xf\n\t"
    "s_nop 1\n\t"
    "v_add_u32_dpp v132, v132, v132 row_shr:4 row_mask:0xf bank_mask:0xf\n\t"
    "s_nop 1\n\t"
    "v_add_u32_dpp v132, v132, v132 row_shr:8 row_mask:0xf bank_mask:0xf\n\t"
    "s_nop 1\n\t"
    "v_add_u32_dpp v132, v132, v132 row_bcast:15 row_mask:0xa bank_mask:0xf\n\t"
    "s_nop 1\n\t"
    "v_add_u32_dpp v132, v132, v132 row_bcast:31 row_mask:0xc bank_mask:0xf\n\t"
    "s_nop 0\n\t"
    "v_readlane_b32 s46, v132, 63\n\t"
    "v_lshrrev_b32 v135, 2, v127\n\t"
    "s_cmp_le_u32 s46, 256\n\t"
    "s_cselect_b32 s47, -1, 0\n\t"
    "v_add_u32 v133, 4, v135\n\t"
    "v_mul_u32_u24 v134, s46, v133\n\t"
    "v_lshrrev_b32 v134, 8, v134\n\t"
    "v_sub_u32 v134, v132, v134\n\t"
    "v_and_b32 v134, s47, v134\n\t"
    "v_lshl_add_u32 v136, v133, 4, v134\n\t"
    "v_sub_u32 v132, v132, v131\n\t"
    "v_add_u32 v133, 3, v135\n\t"
    "v_mul_u32_u24 v134, s46, v133\n\t"
    "v_lshrrev_b32 v134, 8, v134\n\t"
    "v_sub_u32 v134, v132, v134\n\t"
    "v_and_b32 v134, s47, v134\n\t"
    "v_lshl_add_u32 v137, v133, 4, v134\n\t"
    "v_lshl_or_b32 v143, v136, 16, v137\n\t"
    "v_sub_u32 v132, v132, v130\n\t"
    "v_add_u32 v133, 2, v135\n\t"
    "v_mul_u32_u24 v134, s46, v133\n\t"
    "v_lshrrev_b32 v134, 8, v134\n\t"
    "v_sub_u32 v134, v132, v134\n\t"
    "v_and_b32 v134, s47, v134\n\t"
    "v_lshl_add_u32 v136, v133, 4, v134\n\t"
    "v_lshl_or_b32 v142, v137, 16, v136\n\t"
    "v_sub_u32 v132, v132, v129\n\t"
    "v_add_u32 v133, 1, v135\n\t"
    "v_mul_u32_u24 v134, s46, v133\n\t"
    "v_lshrrev_b32 v134, 8, v134\n\t"
    "v_sub_u32 v134, v132, v134\n\t"
    "v_and_b32 v134, s47, v134\n\t"
    "v_lshl_add_u32 v137, v133, 4, v134\n\t"
    "v_lshl_or_b32 v141, v136, 16, v137\n\t"
    "v_sub_u32 v132, v132, v128\n\t"
    "v_add_u32 v133, 0, v135\n\t"
    "v_mul_u32_u24 v134, s46, v133\n\t"
    "v_lshrrev_b32 v134, 8, v134\n\t"
    "v_sub_u32 v134, v132, v134\n\t"
    "v_and_b32 v134, s47, v134\n\t"
    "v_lshl_add_u32 v136, v133, 4, v134\n\t"
    "v_lshl_or_b32 v140, v137, 16, v136\n\t"
    "s_mov_b32 s46, 49294336\n\t"
    "buffer_store_dwordx4 v[140:143], v127, %12, s46 offen sc1\n\t"
    "s_mov_b32 s97, 1\n\t"
    "L_bret_%=:\n\t"
    "s_setpc_b64 s[98:99]\n\t"
    "L_rw_%=:\n\t"
    "s_mov_b32 s47, 1048576\n\t"
    "L_rw2_%=:\n\t"
    "s_sleep 16\n\t"
    "s_lshl_b32 s46, %14, 2\n\t"
    "s_add_u32 s46, s46, 49294336\n\t"
    "s_mov_b64 exec, 1\n\t"
    "buffer_load_dword v144, off, %12, s46 sc1\n\t"
    "s_mov_b64 exec, -1\n\t"
    "s_waitcnt vmcnt(0)\n\t"
    "v_readfirstlane_b32 s38, v144\n\t"
    "s_sub_u32 s47, s47, 1\n\t"
    "s_cmp_eq_u32 s47, 0\n\t"
    "s_cselect_b32 s38, 0x100000, s38\n\t"
    "s_cmp_eq_u32 s38, 0\n\t"
    "s_cbranch_scc1 L_rw2_%=\n\t"
    "s_branch L_rok_%=\n\t"
    "L_xw0_%=:\n\t"
    "s_mov_b32 s68, 1048576\n\t"
    "L_xv0_%=:\n\t"
    "s_sleep 4\n\t"
    "s_mov_b64 exec, 1\n\t"
    "buffer_load_dword v164, off, %12, s93 offset:0 sc1\n\t"
    "s_mov_b64 exec, -1\n\t"
    "s_sub_u32 s68, s68, 1\n\t"
    "s_cmp_eq_u32 s68, 0\n\t"
    "s_cbranch_scc1 L_xwb0_%=\n\t"
    "s_waitcnt vmcnt(0)\n\t"
    "v_readfirstlane_b32 s65, v164\n\t"
    "s_nop 0\n\t"
    "s_cmp_eq_u32 s65, 0\n\t"
    "s_cbranch_scc1 L_xv0_%=\n\t"
    "s_branch L_xwb0_%=\n\t"
    "L_xw1_%=:\n\t"
    "s_mov_b32 s68, 1048576\n\t"
    "L_xv1_%=:\n\t"
    "s_sleep 4\n\t"
    "s_mov_b64 exec, 1\n\t"
    "buffer_load_dword v164, off, %12, s93 offset:128 sc1\n\t"
    "s_mov_b64 exec, -1\n\t"
    "s_sub_u32 s68, s68, 1\n\t"
    "s_cmp_eq_u32 s68, 0\n\t"
    "s_cbranch_scc1 L_xwb1_%=\n\t"
    "s_waitcnt vmcnt(0)\n\t"
    "v_readfirstlane_b32 s65, v164\n\t"
    "s_nop 0\n\t"
    "s_cmp_eq_u32 s65, 0\n\t"
    "s_cbranch_scc1 L_xv1_%=\n\t"
    "s_branch L_xwb1_%=\n\t"
    "L_xw2_%=:\n\t"
    "s_mov_b32 s68, 1048576\n\t"
    "L_xv2_%=:\n\t"
    "s_sleep 4\n\t"
    "s_mov_b64 exec, 1\n\t"
    "buffer_load_dword v164, off, %12, s93 offset:256 sc1\n\t"
    "s_mov_b64 exec, -1\n\t"
    "s_sub_u32 s68, s68, 1\n\t"
    "s_cmp_eq_u32 s68, 0\n\t"
    "s_cbranch_scc1 L_xwb2_%=\n\t"
    "s_waitcnt vmcnt(0)\n\t"
    "v_readfirstlane_b32 s65, v164\n\t"
    "s_nop 0\n\t"
    "s_cmp_eq_u32 s65, 0\n\t"
    "s_cbranch_scc1 L_xv2_%=\n\t"
    "s_branch L_xwb2_%=\n\t"
    "L_xw3_%=:\n\t"
    "s_mov_b32 s68, 1048576\n\t"
    "L_xv3_%=:\n\t"
    "s_sleep 4\n\t"
    "s_mov_b64 exec, 1\n\t"
    "buffer_load_dword v164, off, %12, s93 offset:384 sc1\n\t"
    "s_mov_b64 exec, -1\n\t"
    "s_sub_u32 s68, s68, 1\n\t"
    "s_cmp_eq_u32 s68, 0\n\t"
    "s_cbranch_scc1 L_xwb3_%=\n\t"
    "s_waitcnt vmcnt(0)\n\t"
    "v_readfirstlane_b32 s65, v164\n\t"
    "s_nop 0\n\t"
    "s_cmp_eq_u32 s65, 0\n\t"
    "s_cbranch_scc1 L_xv3_%=\n\t"
    "s_branch L_xwb3_%=\n\t"
    "L_mp2_%=:\n\t"
    "v_lshrrev_b32 v57, 4, v127\n\t"
    "v_add_u32 v57, 32, v57\n\t"
    "v_mov_b32 v58, 64\n\t"
    "v_lshlrev_b32 v57, 7, v57\n\t"
    "s_mov_b32 s65, 49283072\n\t"
    "v_mov_b32 v56, 256\n\t"
    "s_mov_b32 s66, 1048576\n\t"
    "s_mov_b32 s65, 49283072\n\t"
    "s_mov_b64 exec, 15\n\t"
    "buffer_load_dword v56, v57, %12, s65 offen sc1\n\t"
    "s_mov_b64 exec, -1\n\t"
    "s_branch L_mp2b_%=\n\t"
    "L_mp3_%=:\n\t"
    "v_cmp_gt_u32 vcc, v58, v56\n\t"
    "s_cbranch_vccz L_mp3b_%=\n\t"
    "s_sub_u32 s66, s66, 1\n\t"
    "s_cmp_eq_u32 s66, 0\n\t"
    "s_cbranch_scc1 L_mp3b_%=\n\t"
    "s_sleep 8\n\t"
    "s_mov_b32 s65, 49283072\n\t"
    "s_mov_b64 exec, 15\n\t"
    "buffer_load_dword v56, v57, %12, s65 offen sc1\n\t"
    "s_mov_b64 exec, -1\n\t"
    "s_waitcnt vmcnt(0)\n\t"
    "s_branch L_mp3_%=\n\t"
    "L_pend_%=:\n\t"
    ".Ldcp_end:\n\t"
    :: "s"(s_idx), "s"(p_wp), "s"(p_w1), "n"(0), "n"(0), "n"(0), "s"(pb1), "s"(pw2), "s"(pb2), "s"(b2s),
       "n"(0),
       "s"((unsigned long long)ws & 0xFFFFFFFFFFFFull), "s"(s_ws), "n"(0), "s"(bid), "s"(s_tab), "s"(w), "s"(p_out),
       "s"((unsigned)(unsigned long long)idx), "s"((unsigned)((unsigned long long)idx >> 32) & 0xFFFFu)
    : "memory", "vcc", "scc", "m0", "s36", "s37", "s38", "s39", "s40", "s41", "s42", "s43", "s44", "s45", "s46", "s47", "s48", "s49", "s50", "s51", "s52", "s53", "s54", "s55", "s56", "s57", "s58", "s59", "s60", "s61", "s62", "s63", "s64", "s65", "s66", "s67", "s68", "s69", "s70", "s71", "s72", "s73", "s74", "s75", "s76", "s77", "s78", "s79", "s80", "s81", "s82", "s83", "s84", "s85", "s86", "s87", "s88", "s89", "s90", "s91", "s92", "s93", "s94", "s95", "s96", "s97", "s98", "s99", "v0", "v1", "v2", "v3", "v4", "v5", "v6", "v7", "v8", "v9", "v10", "v11", "v12", "v13", "v14", "v15", "v16", "v17", "v18", "v19", "v20", "v21", "v22", "v23", "v24", "v25", "v26", "v27", "v28", "v29", "v30", "v31", "v32", "v33", "v34", "v35", "v36", "v37", "v38", "v39", "v40", "v41", "v42", "v43", "v44", "v45", "v46", "v47", "v48", "v49", "v50", "v51", "v52", "v53", "v54", "v55", "v56", "v57", "v58", "v59", "v60", "v61", "v62", "v63", "v64", "v65", "v66", "v67", "v68", "v69", "v70", "v71", "v72", "v73", "v74", "v75", "v76", "v77", "v78", "v79", "v80", "v81", "v82", "v83", "v84", "v85", "v86", "v87", "v88", "v89", "v90", "v91", "v92", "v93", "v94", "v95", "v96", "v97", "v98", "v99", "v100", "v101", "v102", "v103", "v104", "v105", "v106", "v107", "v108", "v109", "v110", "v111", "v112", "v113", "v114", "v115", "v116", "v117", "v118", "v119", "v120", "v121", "v122", "v123", "v124", "v125", "v126", "v127", "v128", "v129", "v130", "v131", "v132", "v133", "v134", "v135", "v136", "v137", "v138", "v139", "v140", "v141", "v142", "v143", "v144", "v145", "v146", "v147", "v148", "v149", "v150", "v151", "v152", "v153", "v154", "v155", "v156", "v157", "v158", "v159", "v160", "v161", "v162", "v163", "v164", "v165", "v166", "v167", "v168", "v169", "v170", "v171", "v172", "v173", "v174", "v175", "v176", "v177", "v178", "v179", "v180", "v181", "v182", "v183", "v184", "v185", "v186", "v187", "v188", "v189", "v190", "v191", "v192", "v193", "v194", "v195", "v196", "v197", "v198", "v199", "v200", "v201", "v202", "v203", "v204", "v205", "v206", "v207", "v208", "v209", "v210", "v211", "v212", "v213", "v214", "v215", "v216", "v217", "v218", "v219", "v220", "v221", "v222", "v223", "v224", "v225", "v226", "v227", "v228", "v229", "v230", "v231", "v232", "v233", "v234", "v235", "v236", "v237", "v238", "v239", "v240", "v241", "v242", "v243", "v244", "v245", "v246", "v247", "v248", "v249", "v250", "v251", "v252", "v253", "v254", "v255");
}

extern "C" void kernel_launch(void* const* d_in, const int* in_sizes, int n_in, void* d_out, int out_size, void* d_ws,
                              size_t ws_size, hipStream_t stream) {
  const int* idx = (const int*)d_in[0];
  const unsigned short* table = (const unsigned short*)d_in[1];
  const unsigned short* w_p = (const unsigned short*)d_in[2];
  const unsigned short* w1 = (const unsigned short*)d_in[3];
  const unsigned short* b1 = (const unsigned short*)d_in[4];
  const unsigned short* w2 = (const unsigned short*)d_in[5];
  const unsigned short* b2 = (const unsigned short*)d_in[6];
  char* ws = (char*)d_ws;
  dcp_all<<<dim3(256), dim3(512), 0, stream>>>(idx, table, w_p, w1, b1, w2, b2, ws, (__hip_bfloat16*)d_out);
}
```

```cpp
#include <hip/hip_runtime.h>
#include <hip/hip_bf16.h>
#include <stdint.h>

typedef __attribute__((ext_vector_type(2))) unsigned u32x2;

constexpr int BATCH = 16384, NF = 100, DIM = 128, VOC = 1000, HID = 512;
constexpr size_t NWIN = 13;
constexpr size_t WS_IDX = 0;
constexpr size_t IDX_STREAM = 212992;
constexpr size_t WS_W1F = 3407872;
constexpr size_t W1F_SLAB = 102400;
constexpr size_t WS_TF = 16515072;
constexpr size_t WS_WT = 16777216;
constexpr size_t WS_PART = 17825792;
constexpr size_t WS_P0 = 27262976;
constexpr size_t WS_XCH = 31457280;
constexpr size_t XCH_PITCH = 69632;
constexpr size_t WS_CNT = 49283072;
constexpr size_t WS_CH = 49291264;
constexpr size_t WS_RP = 49292288;
constexpr size_t WS_TS = 49293312;
constexpr size_t WS_RNG = 49294336;
constexpr size_t WS_RDY = 49295360;
constexpr size_t WS_DBG = 49328128;
constexpr size_t WS_END = 49721344;

__device__ __forceinline__ __amdgpu_buffer_rsrc_t mk_srd(const void* p, unsigned bytes) {
  return __builtin_amdgcn_make_buffer_rsrc((void*)p, (short)0, (int)bytes, 0x00020000);
}

__global__ __launch_bounds__(512, 2) void dcp_all(const int* __restrict__ idx, const unsigned short* __restrict__ table,
                                                  const unsigned short* __restrict__ w_p, const unsigned short* __restrict__ w1,
                                                  const unsigned short* __restrict__ b1, const unsigned short* __restrict__ w2,
                                                  const unsigned short* __restrict__ b2, char* __restrict__ ws,
                                                  __hip_bfloat16* __restrict__ out) {
  __shared__ __attribute__((aligned(1024))) char smem[163840];
  const int w = __builtin_amdgcn_readfirstlane(threadIdx.x >> 6) + (int)(uintptr_t)smem;
  const int bid = blockIdx.x;
  asm volatile(
    "s_getpc_b64 s[36:37]\n\t"
    ".Ldcp_pc:\n\t"
    "s_bfe_u32 s38, %0, 0x40003\n\t"
    "s_lshl_b32 s38, s38, 13\n\t"
    "v_and_b32 v122, 63, %1\n\t"
    "v_lshlrev_b32 v122, 7, v122\n\t"
    "v_add_u32 v122, s38, v122\n\t"
    "v_min_u32 v122, .Ldcp_end-.Ldcp_pc-4, v122\n\t"
    "s_cmp_lg_u32 %2, 3\n\t"
    "s_cbranch_scc1 L_nopf_%=\n\t"
    "global_load_dword v123, v122, s[36:37]\n\t"
    "L_nopf_%=:\n\t"
    :: "s"(bid), "v"(threadIdx.x), "s"(w) : "memory", "scc", "s36", "s37", "s38", "s74", "s75", "v122", "v123");
  const int xcd = bid & 7, loc = bid >> 3;
  const int cg = xcd * 16 + (loc >> 1);
  const __amdgpu_buffer_rsrc_t s_idx = mk_srd(idx + (size_t)bid * 64 * NF, (unsigned)((BATCH - bid * 64) * NF * 4));
  const __amdgpu_buffer_rsrc_t s_tab = mk_srd(table, VOC * DIM * 2);
  const __amdgpu_buffer_rsrc_t s_ws = mk_srd(ws, (unsigned)WS_END);
  const unsigned short* pb1 = b1 + 4 * cg;
  const unsigned short* pw2 = w2 + 4 * cg;
  const unsigned long long pb2 = (unsigned long long)b2 & ~3ull;
  const unsigned b2s = ((unsigned)(unsigned long long)b2 & 2u) * 8u;
  const unsigned long long p_wp = (unsigned long long)w_p & 0xFFFFFFFFFFFFull, p_w1 = (unsigned long long)w1 & 0xFFFFFFFFFFFFull;
  const unsigned long long p_out = (unsigned long long)out & 0xFFFFFFFFFFFFull;
  asm volatile(
    "v_mbcnt_lo_u32_b32 v147, -1, 0\n\t"
    "v_mbcnt_hi_u32_b32 v147, -1, v147\n\t"
    "s_mov_b64 s[92:93], %1\n\t"
    "s_mov_b32 s94, 20000\n\t"
    "s_mov_b32 s95, 0x20000\n\t"
    "s_lshr_b32 s46, %14, 3\n\t"
    "s_lshl_b32 s51, s46, 2\n\t"
    "v_lshlrev_b32 v122, 4, v147\n\t"
    "s_cmp_ge_u32 %16, 2\n\t"
    "s_cbranch_scc1 L_aimg_%=\n\t"
    "s_lshl_b32 s44, %16, 10\n\t"
    "s_add_u32 m0, s44, 140288\n\t"
    "s_nop 0\n\t"
    "buffer_load_dwordx4 v122, s[92:95], s44 offen lds\n\t"
    "s_add_u32 s44, s44, 0x800\n\t"
    "s_add_u32 m0, s44, 140288\n\t"
    "s_nop 0\n\t"
    "buffer_load_dwordx4 v122, s[92:95], s44 offen lds\n\t"
    "s_add_u32 s44, s44, 0x800\n\t"
    "s_add_u32 m0, s44, 140288\n\t"
    "s_nop 0\n\t"
    "buffer_load_dwordx4 v122, s[92:95], s44 offen lds\n\t"
    "s_add_u32 s44, s44, 0x800\n\t"
    "s_add_u32 m0, s44, 140288\n\t"
    "s_nop 0\n\t"
    "buffer_load_dwordx4 v122, s[92:95], s44 offen lds\n\t"
    "s_add_u32 s44, s44, 0x800\n\t"
    "s_add_u32 m0, s44, 140288\n\t"
    "s_nop 0\n\t"
    "buffer_load_dwordx4 v122, s[92:95], s44 offen lds\n\t"
    "s_add_u32 s44, s44, 0x800\n\t"
    "s_add_u32 m0, s44, 140288\n\t"
    "s_nop 0\n\t"
    "buffer_load_dwordx4 v122, s[92:95], s44 offen lds\n\t"
    "s_add_u32 s44, s44, 0x800\n\t"
    "s_add_u32 m0, s44, 140288\n\t"
    "s_nop 0\n\t"
    "buffer_load_dwordx4 v122, s[92:95], s44 offen lds\n\t"
    "s_add_u32 s44, s44, 0x800\n\t"
    "s_add_u32 m0, s44, 140288\n\t"
    "s_nop 0\n\t"
    "buffer_load_dwordx4 v122, s[92:95], s44 offen lds\n\t"
    "s_add_u32 s44, s44, 0x800\n\t"
    "s_add_u32 m0, s44, 140288\n\t"
    "s_nop 0\n\t"
    "buffer_load_dwordx4 v122, s[92:95], s44 offen lds\n\t"
    "s_add_u32 s44, s44, 0x800\n\t"
    "s_add_u32 m0, s44, 140288\n\t"
    "s_nop 0\n\t"
    "buffer_load_dwordx4 v122, s[92:95], s44 offen lds\n\t"
    "s_add_u32 s44, s44, 0x800\n\t"
    "s_mov_b32 s98, 13107200\n\t"
    "s_mov_b32 s99, 0x20000\n\t"
    "s_cmp_eq_u32 %16, 0\n\t"
    "s_cbranch_scc1 L_aside_%=\n\t"
    "s_branch L_ahelp_%=\n\t"
    "L_aimg_%=:\n\t"
    "s_cmp_lg_u32 %16, 2\n\t"
    "s_cbranch_scc1 L_anoinit_%=\n\t"
    "v_mov_b32 v0, 0\n\t"
    "v_mov_b32 v1, 0\n\t"
    "v_mov_b32 v2, 0\n\t"
    "v_mov_b32 v3, 0\n\t"
    "v_lshlrev_b32 v8, 4, v147\n\t"
    "v_add_u32 v8, 130048, v8\n\t"
    "v_mov_b32 v9, 1024\n\t"
    "ds_write_b128 v9, v[0:3]\n\t"
    "ds_write_b128 v8, v[0:3] offset:0\n\t"
    "ds_write_b128 v8, v[0:3] offset:1024\n\t"
    "ds_write_b128 v8, v[0:3] offset:2048\n\t"
    "ds_write_b32 v9, v0 offset:2084\n\t"
    "ds_write_b32 v9, v0 offset:2088\n\t"
    "ds_write_b32 v9, v0 offset:1044\n\t"
    "ds_write_b32 v9, v0 offset:1052\n\t"
    "v_mov_b32 v4, 3\n\t"
    "ds_write_b32 v9, v4 offset:1040\n\t"
    "v_mov_b32 v5, 16\n\t"
    "ds_write_b32 v9, v5 offset:2080\n\t"
    "v_mov_b32 v6, 32\n\t"
    "ds_write_b32 v9, v6 offset:3120\n\t"
    "v_mov_b32 v7, 51\n\t"
    "ds_write_b32 v9, v7 offset:4160\n\t"
    "L_anoinit_%=:\n\t"
    "s_and_b32 s46, %16, 3\n\t"
    "s_mul_i32 s49, s46, 1040\n\t"
    "s_add_u32 s49, s49, 2064\n\t"
    "s_lshl_b32 s46, s46, 3\n\t"
    "s_lshr_b32 s47, 1076697610, s46\n\t"
    "s_and_b32 s47, s47, 0xff\n\t"
    "s_lshl_b32 s48, s47, 16\n\t"
    "s_lshr_b32 s47, 756681216, s46\n\t"
    "s_and_b32 s47, s47, 0xff\n\t"
    "s_add_u32 s48, s48, s47\n\t"
    "s_lshr_b32 s47, %16, 2\n\t"
    "s_add_u32 s47, s47, 0x200\n\t"
    "s_cmp_eq_u32 s46, 0\n\t"
    "s_cselect_b32 s47, 0x100, s47\n\t"
    "s_add_u32 s48, s48, s47\n\t"
    "v_lshrrev_b32 v126, 3, v147\n\t"
    "v_and_b32 v154, 7, v147\n\t"
    "v_lshlrev_b32 v126, 8, v126\n\t"
    "v_lshl_add_u32 v126, v154, 4, v126\n\t"
    "v_lshlrev_b32 v117, 20, v147\n\t"
    "v_add_u32 v117, 1024, v117\n\t"
    "v_mov_b32 v118, 1\n\t"
    "s_and_b32 s46, %16, 6\n\t"
    "s_cmp_eq_u32 s46, 4\n\t"
    "s_cbranch_scc1 L_adma_%=\n\t"
    "s_lshr_b32 s61, %16, 2\n\t"
    "s_lshl_b32 s61, s61, 1\n\t"
    "s_and_b32 s46, %16, 1\n\t"
    "s_add_u32 s61, s61, s46\n\t"
    "s_add_u32 s46, s61, 102\n\t"
    "s_add_u32 s69, s51, s46\n\t"
    "s_sub_u32 s44, s69, 125\n\t"
    "s_cmp_ge_u32 s69, 125\n\t"
    "s_cselect_b32 s69, s44, s69\n\t"
    "s_mul_i32 m0, s69, 1040\n\t"
    "s_lshl_b32 s44, s69, 11\n\t"
    "buffer_load_dwordx4 v126, %15, s44 offen lds\n\t"
    "s_add_u32 s69, s69, 4\n\t"
    "s_sub_u32 s44, s69, 125\n\t"
    "s_cmp_ge_u32 s69, 125\n\t"
    "s_cselect_b32 s69, s44, s69\n\t"
    "s_mul_i32 m0, s69, 1040\n\t"
    "s_lshl_b32 s44, s69, 11\n\t"
    "buffer_load_dwordx4 v126, %15, s44 offen lds\n\t"
    "s_add_u32 s69, s69, 4\n\t"
    "s_sub_u32 s44, s69, 125\n\t"
    "s_cmp_ge_u32 s69, 125\n\t"
    "s_cselect_b32 s69, s44, s69\n\t"
    "s_waitcnt lgkmcnt(0)\n\t"
    "s_barrier\n\t"
    "s_load_dwordx2 s[72:73], %6, 0\n\t"
    "s_load_dwordx2 s[88:89], %7, 0\n\t"
    "s_load_dword s91, %8, 0\n\t"
    "s_mul_i32 m0, s69, 1040\n\t"
    "s_lshl_b32 s44, s69, 11\n\t"
    "buffer_load_dwordx4 v126, %15, s44 offen lds\n\t"
    "s_add_u32 s69, s69, 4\n\t"
    "s_sub_u32 s44, s69, 125\n\t"
    "s_cmp_ge_u32 s69, 125\n\t"
    "s_cselect_b32 s69, s44, s69\n\t"
    "s_mul_i32 m0, s69, 1040\n\t"
    "s_lshl_b32 s44, s69, 11\n\t"
    "buffer_load_dwordx4 v126, %15, s44 offen lds\n\t"
    "s_add_u32 s69, s69, 4\n\t"
    "s_sub_u32 s44, s69, 125\n\t"
    "s_cmp_ge_u32 s69, 125\n\t"
    "s_cselect_b32 s69, s44, s69\n\t"
    "s_mul_i32 m0, s69, 1040\n\t"
    "s_lshl_b32 s44, s69, 11\n\t"
    "buffer_load_dwordx4 v126, %15, s44 offen lds\n\t"
    "s_add_u32 s69, s69, 4\n\t"
    "s_sub_u32 s44, s69, 125\n\t"
    "s_cmp_ge_u32 s69, 125\n\t"
    "s_cselect_b32 s69, s44, s69\n\t"
    "s_mul_i32 m0, s69, 1040\n\t"
    "s_lshl_b32 s44, s69, 11\n\t"
    "buffer_load_dwordx4 v126, %15, s44 offen lds\n\t"
    "s_add_u32 s69, s69, 4\n\t"
    "s_sub_u32 s44, s69, 125\n\t"
    "s_cmp_ge_u32 s69, 125\n\t"
    "s_cselect_b32 s69, s44, s69\n\t"
    "v_bfe_u32 v154, v147, 2, 2\n\t"
    "v_and_b32 v155, 3, v147\n\t"
    "v_mul_u32_u24 v154, 25, v154\n\t"
    "v_mad_u32_u24 v154, v155, 6, v154\n\t"
    "v_lshlrev_b32 v153, 2, v154\n\t"
    "s_and_b32 s53, s48, 0xff\n\t"
    "s_bfe_u32 s55, s48, 0x80008\n\t"
    "s_add_u32 s54, s53, s55\n\t"
    "s_add_u32 s55, s54, s55\n\t"
    "s_lshr_b32 s64, s48, 16\n\t"
    "v_cmp_eq_u32 s[62:63], 0, v155\n\t"
    "v_and_b32 v151, 48, v147\n\t"
    "v_or_b32 v155, 0x100000, v151\n\t"
    "v_cndmask_b32 v152, v155, v151, s[62:63]\n\t"
    "v_lshrrev_b32 v94, 4, v147\n\t"
    "v_mul_u32_u24 v92, 0x1388, v94\n\t"
    "v_lshl_add_u32 v92, v154, 1, v92\n\t"
    "v_add_u32 v92, 140288, v92\n\t"
    "v_mul_u32_u24 v93, 200, v154\n\t"
    "v_mad_u32_u24 v93, v94, 50, v93\n\t"
    "v_add_u32 v93, 140288, v93\n\t"
    "v_add_u32 v95, 140288, v122\n\t"
    "v_mov_b32 v0, 0\n\t"
    "v_mov_b32 v28, 0\n\t"
    "v_mov_b32 v1, 0\n\t"
    "v_mov_b32 v29, 0\n\t"
    "v_mov_b32 v2, 0\n\t"
    "v_mov_b32 v30, 0\n\t"
    "v_mov_b32 v3, 0\n\t"
    "v_mov_b32 v31, 0\n\t"
    "v_mov_b32 v4, 0\n\t"
    "v_mov_b32 v32, 0\n\t"
    "v_mov_b32 v5, 0\n\t"
    "v_mov_b32 v33, 0\n\t"
    "v_mov_b32 v6, 0\n\t"
    "v_mov_b32 v34, 0\n\t"
    "v_mov_b32 v7, 0\n\t"
    "v_mov_b32 v35, 0\n\t"
    "v_mov_b32 v8, 0\n\t"
    "v_mov_b32 v36, 0\n\t"
    "v_mov_b32 v9, 0\n\t"
    "v_mov_b32 v37, 0\n\t"
    "v_mov_b32 v10, 0\n\t"
    "v_mov_b32 v38, 0\n\t"
    "v_mov_b32 v11, 0\n\t"
    "v_mov_b32 v39, 0\n\t"
    "v_mov_b32 v12, 0\n\t"
    "v_mov_b32 v40, 0\n\t"
    "v_mov_b32 v13, 0\n\t"
    "v_mov_b32 v41, 0\n\t"
    "v_mov_b32 v14, 0\n\t"
    "v_mov_b32 v42, 0\n\t"
    "v_mov_b32 v15, 0\n\t"
    "v_mov_b32 v43, 0\n\t"
    "v_mov_b32 v16, 0\n\t"
    "v_mov_b32 v44, 0\n\t"
    "v_mov_b32 v17, 0\n\t"
    "v_mov_b32 v45, 0\n\t"
    "v_mov_b32 v18, 0\n\t"
    "v_mov_b32 v46, 0\n\t"
    "v_mov_b32 v19, 0\n\t"
    "v_mov_b32 v47, 0\n\t"
    "v_mov_b32 v20, 0\n\t"
    "v_mov_b32 v48, 0\n\t"
    "v_mov_b32 v21, 0\n\t"
    "v_mov_b32 v49, 0\n\t"
    "v_mov_b32 v22, 0\n\t"
    "v_mov_b32 v50, 0\n\t"
    "v_mov_b32 v23, 0\n\t"
    "v_mov_b32 v51, 0\n\t"
    "s_cmp_eq_u32 s61, 1\n\t"
    "s_cbranch_scc1 L_afold1_%=\n\t"
    "s_cmp_eq_u32 s61, 2\n\t"
    "s_cbranch_scc1 L_afold2_%=\n\t"
    "s_cmp_eq_u32 s61, 3\n\t"
    "s_cbranch_scc1 L_afold3_%=\n\t"
    "L_afold0_%=:\n\t"
    "L_asp0_%=:\n\t"
    "ds_read_b32 v119, v117 offset:1044\n\t"
    "s_waitcnt lgkmcnt(0)\n\t"
    "v_readfirstlane_b32 s46, v119\n\t"
    "s_nop 0\n\t"
    "s_cmp_ge_u32 s46, 2\n\t"
    "s_cbranch_scc1 L_ago0_%=\n\t"
    "s_sleep 1\n\t"
    "s_branch L_asp0_%=\n\t"
    "L_ago0_%=:\n\t"
    "ds_read_u16_d16_hi v0, v92 offset:208\n\t"
    "ds_read_u16_d16_hi v28, v93 offset:802\n\t"
    "ds_read_u16_d16_hi v1, v92 offset:1408\n\t"
    "ds_read_u16_d16_hi v29, v93 offset:814\n\t"
    "ds_read_u16_d16_hi v2, v92 offset:2608\n\t"
    "ds_read_u16_d16_hi v30, v93 offset:826\n\t"
    "ds_read_u16_d16_hi v3, v92 offset:3808\n\t"
    "ds_read_u16_d16_hi v31, v93 offset:838\n\t"
    "ds_read_u16_d16_hi v4, v92 offset:410\n\t"
    "ds_read_u16_d16_hi v32, v93 offset:1004\n\t"
    "ds_read_u16_d16_hi v5, v92 offset:1610\n\t"
    "ds_read_u16_d16_hi v33, v93 offset:1016\n\t"
    "ds_read_u16_d16_hi v6, v92 offset:2810\n\t"
    "ds_read_u16_d16_hi v34, v93 offset:1028\n\t"
    "ds_read_u16_d16_hi v7, v92 offset:4010\n\t"
    "ds_read_u16_d16_hi v35, v93 offset:1040\n\t"
    "ds_read_u16_d16_hi v8, v92 offset:612\n\t"
    "ds_read_u16_d16_hi v36, v93 offset:1206\n\t"
    "ds_read_u16_d16_hi v9, v92 offset:1812\n\t"
    "ds_read_u16_d16_hi v37, v93 offset:1218\n\t"
    "ds_read_u16_d16_hi v10, v92 offset:3012\n\t"
    "ds_read_u16_d16_hi v38, v93 offset:1230\n\t"
    "ds_read_u16_d16_hi v11, v92 offset:4212\n\t"
    "ds_read_u16_d16_hi v39, v93 offset:1242\n\t"
    "ds_read_u16_d16_hi v12, v92 offset:0\n\t"
    "ds_read_u16_d16_hi v13, v92 offset:2\n\t"
    "ds_read_u16_d16_hi v41, v93 offset:200\n\t"
    "ds_read_u16_d16_hi v14, v92 offset:4\n\t"
    "ds_read_u16_d16_hi v42, v93 offset:400\n\t"
    "ds_read_u16_d16_hi v15, v92 offset:6\n\t"
    "ds_read_u16_d16_hi v43, v93 offset:600\n\t"
    "ds_read_u16_d16_hi v16, v92 offset:8\n\t"
    "ds_read_u16_d16_hi v44, v93 offset:800\n\t"
    "ds_read_u16_d16_hi v17, v92 offset:10\n\t"
    "ds_read_u16_d16_hi v45, v93 offset:1000\n\t"
    "ds_read_u16_d16_hi v18, v92 offset:12\n\t"
    "ds_read_u16_d16_hi v46, v93 offset:1200\n\t"
    "ds_add_u32 v117, v118 offset:0\n\t"
    "s_waitcnt lgkmcnt(15)\n\t"
    "v_add_f32 v0, v0, v28\n\t"
    "v_add_f32 v1, v1, v29\n\t"
    "v_add_f32 v2, v2, v30\n\t"
    "v_add_f32 v3, v3, v31\n\t"
    "v_add_f32 v4, v4, v32\n\t"
    "v_add_f32 v5, v5, v33\n\t"
    "v_add_f32 v6, v6, v34\n\t"
    "v_add_f32 v7, v7, v35\n\t"
    "s_waitcnt lgkmcnt(14)\n\t"
    "v_add_f32 v8, v8, v36\n\t"
    "v_add_f32 v9, v9, v37\n\t"
    "v_add_f32 v10, v10, v38\n\t"
    "v_add_f32 v11, v11, v39\n\t"
    "s_waitcnt lgkmcnt(7)\n\t"
    "v_cndmask_b32 v12, 0, v12, s[62:63]\n\t"
    "v_add_f32 v13, v13, v41\n\t"
    "v_add_f32 v14, v14, v42\n\t"
    "v_add_f32 v15, v15, v43\n\t"
    "s_waitcnt lgkmcnt(1)\n\t"
    "v_add_f32 v16, v16, v44\n\t"
    "v_add_f32 v17, v17, v45\n\t"
    "v_add_f32 v18, v18, v46\n\t"
    "L_asp1_%=:\n\t"
    "ds_read_b32 v119, v117 offset:0\n\t"
    "s_waitcnt lgkmcnt(0)\n\t"
    "v_readfirstlane_b32 s46, v119\n\t"
    "s_nop 0\n\t"
    "s_cmp_ge_u32 s46, 4\n\t"
    "s_cbranch_scc1 L_ago1_%=\n\t"
    "s_sleep 1\n\t"
    "s_branch L_asp1_%=\n\t"
    "L_ago1_%=:\n\t"
    "ds_write_b128 v95, v[0:3] offset:6144\n\t"
    "ds_write_b128 v95, v[4:7] offset:10240\n\t"
    "ds_write_b128 v95, v[8:11] offset:14336\n\t"
    "ds_write_b128 v95, v[12:15] offset:21504\n\t"
    "ds_write_b128 v95, v[16:19] offset:22528\n\t"
    "ds_add_u32 v117, v118 offset:4\n\t"
    "s_branch L_ab0_%=\n\t"
    "L_afold1_%=:\n\t"
    "L_asp2_%=:\n\t"
    "ds_read_b32 v119, v117 offset:1044\n\t"
    "s_waitcnt lgkmcnt(0)\n\t"
    "v_readfirstlane_b32 s46, v119\n\t"
    "s_nop 0\n\t"
    "s_cmp_ge_u32 s46, 2\n\t"
    "s_cbranch_scc1 L_ago2_%=\n\t"
    "s_sleep 1\n\t"
    "s_branch L_asp2_%=\n\t"
    "L_ago2_%=:\n\t"
    "ds_read_u16_d16_hi v0, v92 offset:204\n\t"
    "ds_read_u16_d16_hi v28, v93 offset:402\n\t"
    "ds_read_u16_d16_hi v1, v92 offset:1404\n\t"
    "ds_read_u16_d16_hi v29, v93 offset:414\n\t"
    "ds_read_u16_d16_hi v2, v92 offset:2604\n\t"
    "ds_read_u16_d16_hi v30, v93 offset:426\n\t"
    "ds_read_u16_d16_hi v3, v92 offset:3804\n\t"
    "ds_read_u16_d16_hi v31, v93 offset:438\n\t"
    "ds_read_u16_d16_hi v4, v92 offset:408\n\t"
    "ds_read_u16_d16_hi v32, v93 offset:804\n\t"
    "ds_read_u16_d16_hi v5, v92 offset:1608\n\t"
    "ds_read_u16_d16_hi v33, v93 offset:816\n\t"
    "ds_read_u16_d16_hi v6, v92 offset:2808\n\t"
    "ds_read_u16_d16_hi v34, v93 offset:828\n\t"
    "ds_read_u16_d16_hi v7, v92 offset:4008\n\t"
    "ds_read_u16_d16_hi v35, v93 offset:840\n\t"
    "ds_read_u16_d16_hi v8, v92 offset:412\n\t"
    "ds_read_u16_d16_hi v36, v93 offset:1204\n\t"
    "ds_read_u16_d16_hi v9, v92 offset:1612\n\t"
    "ds_read_u16_d16_hi v37, v93 offset:1216\n\t"
    "ds_read_u16_d16_hi v10, v92 offset:2812\n\t"
    "ds_read_u16_d16_hi v38, v93 offset:1228\n\t"
    "ds_read_u16_d16_hi v11, v92 offset:4012\n\t"
    "ds_read_u16_d16_hi v39, v93 offset:1240\n\t"
    "ds_read_u16_d16_hi v12, v92 offset:810\n\t"
    "ds_read_u16_d16_hi v40, v93 offset:1008\n\t"
    "ds_read_u16_d16_hi v13, v92 offset:2010\n\t"
    "ds_read_u16_d16_hi v41, v93 offset:1020\n\t"
    "ds_read_u16_d16_hi v14, v92 offset:3210\n\t"
    "ds_read_u16_d16_hi v42, v93 offset:1032\n\t"
    "ds_read_u16_d16_hi v15, v92 offset:4410\n\t"
    "ds_read_u16_d16_hi v43, v93 offset:1044\n\t"
    "ds_read_u16_d16_hi v16, v92 offset:202\n\t"
    "ds_read_u16_d16_hi v17, v92 offset:1402\n\t"
    "ds_read_u16_d16_hi v18, v92 offset:2602\n\t"
    "ds_read_u16_d16_hi v19, v92 offset:3802\n\t"
    "ds_read_u16_d16_hi v20, v92 offset:808\n\t"
    "ds_read_u16_d16_hi v21, v92 offset:2008\n\t"
    "ds_read_u16_d16_hi v22, v92 offset:3208\n\t"
    "ds_read_u16_d16_hi v23, v92 offset:4408\n\t"
    "ds_add_u32 v117, v118 offset:0\n\t"
    "s_waitcnt lgkmcnt(15)\n\t"
    "v_add_f32 v0, v0, v28\n\t"
    "v_add_f32 v1, v1, v29\n\t"
    "v_add_f32 v2, v2, v30\n\t"
    "v_add_f32 v3, v3, v31\n\t"
    "v_add_f32 v4, v4, v32\n\t"
    "v_add_f32 v5, v5, v33\n\t"
    "v_add_f32 v6, v6, v34\n\t"
    "v_add_f32 v7, v7, v35\n\t"
    "v_add_f32 v8, v8, v36\n\t"
    "v_add_f32 v9, v9, v37\n\t"
    "v_add_f32 v10, v10, v38\n\t"
    "v_add_f32 v11, v11, v39\n\t"
    "s_waitcnt lgkmcnt(9)\n\t"
    "v_add_f32 v12, v12, v40\n\t"
    "v_add_f32 v13, v13, v41\n\t"
    "v_add_f32 v14, v14, v42\n\t"
    "v_add_f32 v15, v15, v43\n\t"
    "s_waitcnt lgkmcnt(5)\n\t"
    "s_waitcnt lgkmcnt(1)\n\t"
    "L_asp3_%=:\n\t"
    "ds_read_b32 v119, v117 offset:0\n\t"
    "s_waitcnt lgkmcnt(0)\n\t"
    "v_readfirstlane_b32 s46, v119\n\t"
    "s_nop 0\n\t"
    "s_cmp_ge_u32 s46, 4\n\t"
    "s_cbranch_scc1 L_ago3_%=\n\t"
    "s_sleep 1\n\t"
    "s_branch L_asp3_%=\n\t"
    "L_ago3_%=:\n\t"
    "ds_write_b128 v95, v[0:3] offset:1024\n\t"
    "ds_write_b128 v95, v[4:7] offset:7168\n\t"
    "ds_write_b128 v95, v[8:11] offset:11264\n\t"
    "ds_write_b128 v95, v[12:15] offset:16384\n\t"
    "ds_write_b128 v95, v[16:19] offset:0\n\t"
    "ds_write_b128 v95, v[20:23] offset:15360\n\t"
    "ds_add_u32 v117, v118 offset:4\n\t"
    "s_branch L_ab0_%=\n\t"
    "L_afold2_%=:\n\t"
    "L_asp4_%=:\n\t"
    "ds_read_b32 v119, v117 offset:1044\n\t"
    "s_waitcnt lgkmcnt(0)\n\t"
    "v_readfirstlane_b32 s46, v119\n\t"
    "s_nop 0\n\t"
    "s_cmp_ge_u32 s46, 2\n\t"
    "s_cbranch_scc1 L_ago4_%=\n\t"
    "s_sleep 1\n\t"
    "s_branch L_asp4_%=\n\t"
    "L_ago4_%=:\n\t"
    "ds_read_u16_d16_hi v0, v92 offset:206\n\t"
    "ds_read_u16_d16_hi v28, v93 offset:602\n\t"
    "ds_read_u16_d16_hi v1, v92 offset:1406\n\t"
    "ds_read_u16_d16_hi v29, v93 offset:614\n\t"
    "ds_read_u16_d16_hi v2, v92 offset:2606\n\t"
    "ds_read_u16_d16_hi v30, v93 offset:626\n\t"
    "ds_read_u16_d16_hi v3, v92 offset:3806\n\t"
    "ds_read_u16_d16_hi v31, v93 offset:638\n\t"
    "ds_read_u16_d16_hi v4, v92 offset:210\n\t"
    "ds_read_u16_d16_hi v32, v93 offset:1002\n\t"
    "ds_read_u16_d16_hi v5, v92 offset:1410\n\t"
    "ds_read_u16_d16_hi v33, v93 offset:1014\n\t"
    "ds_read_u16_d16_hi v6, v92 offset:2610\n\t"
    "ds_read_u16_d16_hi v34, v93 offset:1026\n\t"
    "ds_read_u16_d16_hi v7, v92 offset:3810\n\t"
    "ds_read_u16_d16_hi v35, v93 offset:1038\n\t"
    "ds_read_u16_d16_hi v8, v92 offset:608\n\t"
    "ds_read_u16_d16_hi v36, v93 offset:806\n\t"
    "ds_read_u16_d16_hi v9, v92 offset:1808\n\t"
    "ds_read_u16_d16_hi v37, v93 offset:818\n\t"
    "ds_read_u16_d16_hi v10, v92 offset:3008\n\t"
    "ds_read_u16_d16_hi v38, v93 offset:830\n\t"
    "ds_read_u16_d16_hi v11, v92 offset:4208\n\t"
    "ds_read_u16_d16_hi v39, v93 offset:842\n\t"
    "ds_read_u16_d16_hi v12, v92 offset:812\n\t"
    "ds_read_u16_d16_hi v40, v93 offset:1208\n\t"
    "ds_read_u16_d16_hi v13, v92 offset:2012\n\t"
    "ds_read_u16_d16_hi v41, v93 offset:1220\n\t"
    "ds_read_u16_d16_hi v14, v92 offset:3212\n\t"
    "ds_read_u16_d16_hi v42, v93 offset:1232\n\t"
    "ds_read_u16_d16_hi v15, v92 offset:4412\n\t"
    "ds_read_u16_d16_hi v43, v93 offset:1244\n\t"
    "ds_read_u16_d16_hi v16, v92 offset:404\n\t"
    "ds_read_u16_d16_hi v17, v92 offset:1604\n\t"
    "ds_read_u16_d16_hi v18, v92 offset:2804\n\t"
    "ds_read_u16_d16_hi v19, v92 offset:4004\n\t"
    "ds_read_u16_d16_hi v20, v92 offset:1010\n\t"
    "ds_read_u16_d16_hi v21, v92 offset:2210\n\t"
    "ds_read_u16_d16_hi v22, v92 offset:3410\n\t"
    "ds_read_u16_d16_hi v23, v92 offset:4610\n\t"
    "ds_add_u32 v117, v118 offset:0\n\t"
    "s_waitcnt lgkmcnt(15)\n\t"
    "v_add_f32 v0, v0, v28\n\t"
    "v_add_f32 v1, v1, v29\n\t"
    "v_add_f32 v2, v2, v30\n\t"
    "v_add_f32 v3, v3, v31\n\t"
    "v_add_f32 v4, v4, v32\n\t"
    "v_add_f32 v5, v5, v33\n\t"
    "v_add_f32 v6, v6, v34\n\t"
    "v_add_f32 v7, v7, v35\n\t"
    "v_add_f32 v8, v8, v36\n\t"
    "v_add_f32 v9, v9, v37\n\t"
    "v_add_f32 v10, v10, v38\n\t"
    "v_add_f32 v11, v11, v39\n\t"
    "s_waitcnt lgkmcnt(9)\n\t"
    "v_add_f32 v12, v12, v40\n\t"
    "v_add_f32 v13, v13, v41\n\t"
    "v_add_f32 v14, v14, v42\n\t"
    "v_add_f32 v15, v15, v43\n\t"
    "s_waitcnt lgkmcnt(5)\n\t"
    "s_waitcnt lgkmcnt(1)\n\t"
    "L_asp5_%=:\n\t"
    "ds_read_b32 v119, v117 offset:0\n\t"
    "s_waitcnt lgkmcnt(0)\n\t"
    "v_readfirstlane_b32 s46, v119\n\t"
    "s_nop 0\n\t"
    "s_cmp_ge_u32 s46, 4\n\t"
    "s_cbranch_scc1 L_ago5_%=\n\t"
    "s_sleep 1\n\t"
    "s_branch L_asp5_%=\n\t"
    "L_ago5_%=:\n\t"
    "ds_write_b128 v95, v[0:3] offset:3072\n\t"
    "ds_write_b128 v95, v[4:7] offset:8192\n\t"
    "ds_write_b128 v95, v[8:11] offset:12288\n\t"
    "ds_write_b128 v95, v[12:15] offset:17408\n\t"
    "ds_write_b128 v95, v[16:19] offset:2048\n\t"
    "ds_write_b128 v95, v[20:23] offset:18432\n\t"
    "ds_add_u32 v117, v118 offset:4\n\t"
    "s_branch L_ab0_%=\n\t"
    "L_afold3_%=:\n\t"
    "L_asp6_%=:\n\t"
    "ds_read_b32 v119, v117 offset:1044\n\t"
    "s_waitcnt lgkmcnt(0)\n\t"
    "v_readfirstlane_b32 s46, v119\n\t"
    "s_nop 0\n\t"
    "s_cmp_ge_u32 s46, 2\n\t"
    "s_cbranch_scc1 L_ago6_%=\n\t"
    "s_sleep 1\n\t"
    "s_branch L_asp6_%=\n\t"
    "L_ago6_%=:\n\t"
    "ds_read_u16_d16_hi v0, v92 offset:406\n\t"
    "ds_read_u16_d16_hi v28, v93 offset:604\n\t"
    "ds_read_u16_d16_hi v1, v92 offset:1606\n\t"
    "ds_read_u16_d16_hi v29, v93 offset:616\n\t"
    "ds_read_u16_d16_hi v2, v92 offset:2806\n\t"
    "ds_read_u16_d16_hi v30, v93 offset:628\n\t"
    "ds_read_u16_d16_hi v3, v92 offset:4006\n\t"
    "ds_read_u16_d16_hi v31, v93 offset:640\n\t"
    "ds_read_u16_d16_hi v4, v92 offset:212\n\t"
    "ds_read_u16_d16_hi v32, v93 offset:1202\n\t"
    "ds_read_u16_d16_hi v5, v92 offset:1412\n\t"
    "ds_read_u16_d16_hi v33, v93 offset:1214\n\t"
    "ds_read_u16_d16_hi v6, v92 offset:2612\n\t"
    "ds_read_u16_d16_hi v34, v93 offset:1226\n\t"
    "ds_read_u16_d16_hi v7, v92 offset:3812\n\t"
    "ds_read_u16_d16_hi v35, v93 offset:1238\n\t"
    "ds_read_u16_d16_hi v8, v92 offset:610\n\t"
    "ds_read_u16_d16_hi v36, v93 offset:1006\n\t"
    "ds_read_u16_d16_hi v9, v92 offset:1810\n\t"
    "ds_read_u16_d16_hi v37, v93 offset:1018\n\t"
    "ds_read_u16_d16_hi v10, v92 offset:3010\n\t"
    "ds_read_u16_d16_hi v38, v93 offset:1030\n\t"
    "ds_read_u16_d16_hi v11, v92 offset:4210\n\t"
    "ds_read_u16_d16_hi v39, v93 offset:1042\n\t"
    "ds_read_u16_d16_hi v12, v92 offset:1012\n\t"
    "ds_read_u16_d16_hi v40, v93 offset:1210\n\t"
    "ds_read_u16_d16_hi v13, v92 offset:2212\n\t"
    "ds_read_u16_d16_hi v41, v93 offset:1222\n\t"
    "ds_read_u16_d16_hi v14, v92 offset:3412\n\t"
    "ds_read_u16_d16_hi v42, v93 offset:1234\n\t"
    "ds_read_u16_d16_hi v15, v92 offset:4612\n\t"
    "ds_read_u16_d16_hi v43, v93 offset:1246\n\t"
    "ds_read_u16_d16_hi v16, v92 offset:606\n\t"
    "ds_read_u16_d16_hi v17, v92 offset:1806\n\t"
    "ds_read_u16_d16_hi v18, v92 offset:3006\n\t"
    "ds_read_u16_d16_hi v19, v92 offset:4206\n\t"
    "ds_read_u16_d16_hi v20, v92 offset:1212\n\t"
    "ds_read_u16_d16_hi v21, v92 offset:2412\n\t"
    "ds_read_u16_d16_hi v22, v92 offset:3612\n\t"
    "ds_read_u16_d16_hi v23, v92 offset:4812\n\t"
    "ds_add_u32 v117, v118 offset:0\n\t"
    "s_waitcnt lgkmcnt(15)\n\t"
    "v_add_f32 v0, v0, v28\n\t"
    "v_add_f32 v1, v1, v29\n\t"
    "v_add_f32 v2, v2, v30\n\t"
    "v_add_f32 v3, v3, v31\n\t"
    "v_add_f32 v4, v4, v32\n\t"
    "v_add_f32 v5, v5, v33\n\t"
    "v_add_f32 v6, v6, v34\n\t"
    "v_add_f32 v7, v7, v35\n\t"
    "v_add_f32 v8, v8, v36\n\t"
    "v_add_f32 v9, v9, v37\n\t"
    "v_add_f32 v10, v10, v38\n\t"
    "v_add_f32 v11, v11, v39\n\t"
    "s_waitcnt lgkmcnt(9)\n\t"
    "v_add_f32 v12, v12, v40\n\t"
    "v_add_f32 v13, v13, v41\n\t"
    "v_add_f32 v14, v14, v42\n\t"
    "v_add_f32 v15, v15, v43\n\t"
    "s_waitcnt lgkmcnt(5)\n\t"
    "s_waitcnt lgkmcnt(1)\n\t"
    "L_asp7_%=:\n\t"
    "ds_read_b32 v119, v117 offset:0\n\t"
    "s_waitcnt lgkmcnt(0)\n\t"
    "v_readfirstlane_b32 s46, v119\n\t"
    "s_nop 0\n\t"
    "s_cmp_ge_u32 s46, 4\n\t"
    "s_cbranch_scc1 L_ago7_%=\n\t"
    "s_sleep 1\n\t"
    "s_branch L_asp7_%=\n\t"
    "L_ago7_%=:\n\t"
    "ds_write_b128 v95, v[0:3] offset:4096\n\t"
    "ds_write_b128 v95, v[4:7] offset:9216\n\t"
    "ds_write_b128 v95, v[8:11] offset:13312\n\t"
    "ds_write_b128 v95, v[12:15] offset:19456\n\t"
    "ds_write_b128 v95, v[16:19] offset:5120\n\t"
    "ds_write_b128 v95, v[20:23] offset:20480\n\t"
    "ds_add_u32 v117, v118 offset:4\n\t"
    "L_ab0_%=:\n\t"
    "s_branch L_ab_%=\n\t"
    "L_adma_%=:\n\t"
    "s_sub_u32 s46, %16, 4\n\t"
    "s_add_u32 s69, s51, s46\n\t"
    "s_sub_u32 s44, s69, 125\n\t"
    "s_cmp_ge_u32 s69, 125\n\t"
    "s_cselect_b32 s69, s44, s69\n\t"
    "s_mul_i32 m0, s69, 1040\n\t"
    "s_lshl_b32 s44, s69, 11\n\t"
    "buffer_load_dwordx4 v126, %15, s44 offen lds\n\t"
    "s_add_u32 s69, s69, 2\n\t"
    "s_sub_u32 s44, s69, 125\n\t"
    "s_cmp_ge_u32 s69, 125\n\t"
    "s_cselect_b32 s69, s44, s69\n\t"
    "s_mul_i32 m0, s69, 1040\n\t"
    "s_lshl_b32 s44, s69, 11\n\t"
    "buffer_load_dwordx4 v126, %15, s44 offen lds\n\t"
    "s_add_u32 s69, s69, 2\n\t"
    "s_sub_u32 s44, s69, 125\n\t"
    "s_cmp_ge_u32 s69, 125\n\t"
    "s_cselect_b32 s69, s44, s69\n\t"
    "s_barrier\n\t"
    "s_load_dwordx2 s[72:73], %6, 0\n\t"
    "s_load_dwordx2 s[88:89], %7, 0\n\t"
    "s_load_dword s91, %8, 0\n\t"
    "s_mul_i32 m0, s69, 1040\n\t"
    "s_lshl_b32 s44, s69, 11\n\t"
    "buffer_load_dwordx4 v126, %15, s44 offen lds\n\t"
    "s_add_u32 s69, s69, 2\n\t"
    "s_sub_u32 s44, s69, 125\n\t"
    "s_cmp_ge_u32 s69, 125\n\t"
    "s_cselect_b32 s69, s44, s69\n\t"
    "s_mul_i32 m0, s69, 1040\n\t"
    "s_lshl_b32 s44, s69, 11\n\t"
    "buffer_load_dwordx4 v126, %15, s44 offen lds\n\t"
    "s_add_u32 s69, s69, 2\n\t"
    "s_sub_u32 s44, s69, 125\n\t"
    "s_cmp_ge_u32 s69, 125\n\t"
    "s_cselect_b32 s69, s44, s69\n\t"
    "s_mul_i32 m0, s69, 1040\n\t"
    "s_lshl_b32 s44, s69, 11\n\t"
    "buffer_load_dwordx4 v126, %15, s44 offen lds\n\t"
    "s_add_u32 s69, s69, 2\n\t"
    "s_sub_u32 s44, s69, 125\n\t"
    "s_cmp_ge_u32 s69, 125\n\t"
    "s_cselect_b32 s69, s44, s69\n\t"
    "s_mul_i32 m0, s69, 1040\n\t"
    "s_lshl_b32 s44, s69, 11\n\t"
    "buffer_load_dwordx4 v126, %15, s44 offen lds\n\t"
    "s_add_u32 s69, s69, 2\n\t"
    "s_sub_u32 s44, s69, 125\n\t"
    "s_cmp_ge_u32 s69, 125\n\t"
    "s_cselect_b32 s69, s44, s69\n\t"
    "s_mul_i32 m0, s69, 1040\n\t"
    "s_lshl_b32 s44, s69, 11\n\t"
    "buffer_load_dwordx4 v126, %15, s44 offen lds\n\t"
    "s_add_u32 s69, s69, 2\n\t"
    "s_sub_u32 s44, s69, 125\n\t"
    "s_cmp_ge_u32 s69, 125\n\t"
    "s_cselect_b32 s69, s44, s69\n\t"
    "s_mul_i32 m0, s69, 1040\n\t"
    "s_lshl_b32 s44, s69, 11\n\t"
    "buffer_load_dwordx4 v126, %15, s44 offen lds\n\t"
    "s_add_u32 s69, s69, 2\n\t"
    "s_sub_u32 s44, s69, 125\n\t"
    "s_cmp_ge_u32 s69, 125\n\t"
    "s_cselect_b32 s69, s44, s69\n\t"
    "s_mul_i32 m0, s69, 1040\n\t"
    "s_lshl_b32 s44, s69, 11\n\t"
    "buffer_load_dwordx4 v126, %15, s44 offen lds\n\t"
    "s_add_u32 s69, s69, 2\n\t"
    "s_sub_u32 s44, s69, 125\n\t"
    "s_cmp_ge_u32 s69, 125\n\t"
    "s_cselect_b32 s69, s44, s69\n\t"
    "s_mul_i32 m0, s69, 1040\n\t"
    "s_lshl_b32 s44, s69, 11\n\t"
    "buffer_load_dwordx4 v126, %15, s44 offen lds\n\t"
    "s_add_u32 s69, s69, 2\n\t"
    "s_sub_u32 s44, s69, 125\n\t"
    "s_cmp_ge_u32 s69, 125\n\t"
    "s_cselect_b32 s69, s44, s69\n\t"
    "s_mul_i32 m0, s69, 1040\n\t"
    "s_lshl_b32 s44, s69, 11\n\t"
    "buffer_load_dwordx4 v126, %15, s44 offen lds\n\t"
    "s_add_u32 s69, s69, 2\n\t"
    "s_sub_u32 s44, s69, 125\n\t"
    "s_cmp_ge_u32 s69, 125\n\t"
    "s_cselect_b32 s69, s44, s69\n\t"
    "s_mul_i32 m0, s69, 1040\n\t"
    "s_lshl_b32 s44, s69, 11\n\t"
    "buffer_load_dwordx4 v126, %15, s44 offen lds\n\t"
    "s_add_u32 s69, s69, 2\n\t"
    "s_sub_u32 s44, s69, 125\n\t"
    "s_cmp_ge_u32 s69, 125\n\t"
    "s_cselect_b32 s69, s44, s69\n\t"
    "v_bfe_u32 v154, v147, 2, 2\n\t"
    "v_and_b32 v155, 3, v147\n\t"
    "v_mul_u32_u24 v154, 25, v154\n\t"
    "v_mad_u32_u24 v154, v155, 6, v154\n\t"
    "v_lshlrev_b32 v153, 2, v154\n\t"
    "s_and_b32 s53, s48, 0xff\n\t"
    "s_bfe_u32 s55, s48, 0x80008\n\t"
    "s_add_u32 s54, s53, s55\n\t"
    "s_add_u32 s55, s54, s55\n\t"
    "s_lshr_b32 s64, s48, 16\n\t"
    "v_cmp_eq_u32 s[62:63], 0, v155\n\t"
    "v_and_b32 v151, 48, v147\n\t"
    "v_or_b32 v155, 0x100000, v151\n\t"
    "v_cndmask_b32 v152, v155, v151, s[62:63]\n\t"
    "v_add_u32 v95, 140288, v122\n\t"
    "s_mul_i32 m0, s69, 1040\n\t"
    "s_lshl_b32 s44, s69, 11\n\t"
    "buffer_load_dwordx4 v126, %15, s44 offen lds\n\t"
    "s_add_u32 s69, s69, 2\n\t"
    "s_sub_u32 s44, s69, 125\n\t"
    "s_cmp_ge_u32 s69, 125\n\t"
    "s_cselect_b32 s69, s44, s69\n\t"
    "s_mul_i32 m0, s69, 1040\n\t"
    "s_lshl_b32 s44, s69, 11\n\t"
    "buffer_load_dwordx4 v126, %15, s44 offen lds\n\t"
    "s_add_u32 s69, s69, 2\n\t"
    "s_sub_u32 s44, s69, 125\n\t"
    "s_cmp_ge_u32 s69, 125\n\t"
    "s_cselect_b32 s69, s44, s69\n\t"
    "s_mul_i32 m0, s69, 1040\n\t"
    "s_lshl_b32 s44, s69, 11\n\t"
    "buffer_load_dwordx4 v126, %15, s44 offen lds\n\t"
    "s_add_u32 s69, s69, 2\n\t"
    "s_sub_u32 s44, s69, 125\n\t"
    "s_cmp_ge_u32 s69, 125\n\t"
    "s_cselect_b32 s69, s44, s69\n\t"
    "s_mul_i32 m0, s69, 1040\n\t"
    "s_lshl_b32 s44, s69, 11\n\t"
    "buffer_load_dwordx4 v126, %15, s44 offen lds\n\t"
    "s_add_u32 s69, s69, 2\n\t"
    "s_sub_u32 s44, s69, 125\n\t"
    "s_cmp_ge_u32 s69, 125\n\t"
    "s_cselect_b32 s69, s44, s69\n\t"
    "s_mul_i32 m0, s69, 1040\n\t"
    "s_lshl_b32 s44, s69, 11\n\t"
    "buffer_load_dwordx4 v126, %15, s44 offen lds\n\t"
    "s_add_u32 s69, s69, 2\n\t"
    "s_sub_u32 s44, s69, 125\n\t"
    "s_cmp_ge_u32 s69, 125\n\t"
    "s_cselect_b32 s69, s44, s69\n\t"
    "s_mul_i32 m0, s69, 1040\n\t"
    "s_lshl_b32 s44, s69, 11\n\t"
    "buffer_load_dwordx4 v126, %15, s44 offen lds\n\t"
    "s_add_u32 s69, s69, 2\n\t"
    "s_sub_u32 s44, s69, 125\n\t"
    "s_cmp_ge_u32 s69, 125\n\t"
    "s_cselect_b32 s69, s44, s69\n\t"
    "s_mul_i32 m0, s69, 1040\n\t"
    "s_lshl_b32 s44, s69, 11\n\t"
    "buffer_load_dwordx4 v126, %15, s44 offen lds\n\t"
    "s_add_u32 s69, s69, 2\n\t"
    "s_sub_u32 s44, s69, 125\n\t"
    "s_cmp_ge_u32 s69, 125\n\t"
    "s_cselect_b32 s69, s44, s69\n\t"
    "s_mul_i32 m0, s69, 1040\n\t"
    "s_lshl_b32 s44, s69, 11\n\t"
    "buffer_load_dwordx4 v126, %15, s44 offen lds\n\t"
    "s_add_u32 s69, s69, 2\n\t"
    "s_sub_u32 s44, s69, 125\n\t"
    "s_cmp_ge_u32 s69, 125\n\t"
    "s_cselect_b32 s69, s44, s69\n\t"
    "s_mul_i32 m0, s69, 1040\n\t"
    "s_lshl_b32 s44, s69, 11\n\t"
    "buffer_load_dwordx4 v126, %15, s44 offen lds\n\t"
    "s_add_u32 s69, s69, 2\n\t"
    "s_sub_u32 s44, s69, 125\n\t"
    "s_cmp_ge_u32 s69, 125\n\t"
    "s_cselect_b32 s69, s44, s69\n\t"
    "s_mul_i32 m0, s69, 1040\n\t"
    "s_lshl_b32 s44, s69, 11\n\t"
    "buffer_load_dwordx4 v126, %15, s44 offen lds\n\t"
    "s_add_u32 s69, s69, 2\n\t"
    "s_sub_u32 s44, s69, 125\n\t"
    "s_cmp_ge_u32 s69, 125\n\t"
    "s_cselect_b32 s69, s44, s69\n\t"
    "s_mul_i32 m0, s69, 1040\n\t"
    "s_lshl_b32 s44, s69, 11\n\t"
    "buffer_load_dwordx4 v126, %15, s44 offen lds\n\t"
    "s_add_u32 s69, s69, 2\n\t"
    "s_sub_u32 s44, s69, 125\n\t"
    "s_cmp_ge_u32 s69, 125\n\t"
    "s_cselect_b32 s69, s44, s69\n\t"
    "s_mul_i32 m0, s69, 1040\n\t"
    "s_lshl_b32 s44, s69, 11\n\t"
    "buffer_load_dwordx4 v126, %15, s44 offen lds\n\t"
    "s_add_u32 s69, s69, 2\n\t"
    "s_sub_u32 s44, s69, 125\n\t"
    "s_cmp_ge_u32 s69, 125\n\t"
    "s_cselect_b32 s69, s44, s69\n\t"
    "s_mul_i32 m0, s69, 1040\n\t"
    "s_lshl_b32 s44, s69, 11\n\t"
    "buffer_load_dwordx4 v126, %15, s44 offen lds\n\t"
    "s_add_u32 s69, s69, 2\n\t"
    "s_sub_u32 s44, s69, 125\n\t"
    "s_cmp_ge_u32 s69, 125\n\t"
    "s_cselect_b32 s69, s44, s69\n\t"
    "s_mul_i32 m0, s69, 1040\n\t"
    "s_lshl_b32 s44, s69, 11\n\t"
    "buffer_load_dwordx4 v126, %15, s44 offen lds\n\t"
    "s_add_u32 s69, s69, 2\n\t"
    "s_sub_u32 s44, s69, 125\n\t"
    "s_cmp_ge_u32 s69, 125\n\t"
    "s_cselect_b32 s69, s44, s69\n\t"
    "s_mul_i32 m0, s69, 1040\n\t"
    "s_lshl_b32 s44, s69, 11\n\t"
    "buffer_load_dwordx4 v126, %15, s44 offen lds\n\t"
    "s_add_u32 s69, s69, 2\n\t"
    "s_sub_u32 s44, s69, 125\n\t"
    "s_cmp_ge_u32 s69, 125\n\t"
    "s_cselect_b32 s69, s44, s69\n\t"
    "s_mul_i32 m0, s69, 1040\n\t"
    "s_lshl_b32 s44, s69, 11\n\t"
    "buffer_load_dwordx4 v126, %15, s44 offen lds\n\t"
    "s_add_u32 s69, s69, 2\n\t"
    "s_sub_u32 s44, s69, 125\n\t"
    "s_cmp_ge_u32 s69, 125\n\t"
    "s_cselect_b32 s69, s44, s69\n\t"
    "s_mul_i32 m0, s69, 1040\n\t"
    "s_lshl_b32 s44, s69, 11\n\t"
    "buffer_load_dwordx4 v126, %15, s44 offen lds\n\t"
    "s_add_u32 s69, s69, 2\n\t"
    "s_sub_u32 s44, s69, 125\n\t"
    "s_cmp_ge_u32 s69, 125\n\t"
    "s_cselect_b32 s69, s44, s69\n\t"
    "s_mul_i32 m0, s69, 1040\n\t"
    "s_lshl_b32 s44, s69, 11\n\t"
    "buffer_load_dwordx4 v126, %15, s44 offen lds\n\t"
    "s_add_u32 s69, s69, 2\n\t"
    "s_sub_u32 s44, s69, 125\n\t"
    "s_cmp_ge_u32 s69, 125\n\t"
    "s_cselect_b32 s69, s44, s69\n\t"
    "s_mul_i32 m0, s69, 1040\n\t"
    "s_lshl_b32 s44, s69, 11\n\t"
    "buffer_load_dwordx4 v126, %15, s44 offen lds\n\t"
    "s_add_u32 s69, s69, 2\n\t"
    "s_sub_u32 s44, s69, 125\n\t"
    "s_cmp_ge_u32 s69, 125\n\t"
    "s_cselect_b32 s69, s44, s69\n\t"
    "s_mul_i32 m0, s69, 1040\n\t"
    "s_lshl_b32 s44, s69, 11\n\t"
    "buffer_load_dwordx4 v126, %15, s44 offen lds\n\t"
    "s_add_u32 s69, s69, 2\n\t"
    "s_sub_u32 s44, s69, 125\n\t"
    "s_cmp_ge_u32 s69, 125\n\t"
    "s_cselect_b32 s69, s44, s69\n\t"
    "s_mul_i32 m0, s69, 1040\n\t"
    "s_lshl_b32 s44, s69, 11\n\t"
    "buffer_load_dwordx4 v126, %15, s44 offen lds\n\t"
    "s_add_u32 s69, s69, 2\n\t"
    "s_sub_u32 s44, s69, 125\n\t"
    "s_cmp_ge_u32 s69, 125\n\t"
    "s_cselect_b32 s69, s44, s69\n\t"
    "s_mul_i32 m0, s69, 1040\n\t"
    "s_lshl_b32 s44, s69, 11\n\t"
    "buffer_load_dwordx4 v126, %15, s44 offen lds\n\t"
    "s_add_u32 s69, s69, 2\n\t"
    "s_sub_u32 s44, s69, 125\n\t"
    "s_cmp_ge_u32 s69, 125\n\t"
    "s_cselect_b32 s69, s44, s69\n\t"
    "s_mul_i32 m0, s69, 1040\n\t"
    "s_lshl_b32 s44, s69, 11\n\t"
    "buffer_load_dwordx4 v126, %15, s44 offen lds\n\t"
    "s_add_u32 s69, s69, 2\n\t"
    "s_sub_u32 s44, s69, 125\n\t"
    "s_cmp_ge_u32 s69, 125\n\t"
    "s_cselect_b32 s69, s44, s69\n\t"
    "s_mul_i32 m0, s69, 1040\n\t"
    "s_lshl_b32 s44, s69, 11\n\t"
    "buffer_load_dwordx4 v126, %15, s44 offen lds\n\t"
    "s_add_u32 s69, s69, 2\n\t"
    "s_sub_u32 s44, s69, 125\n\t"
    "s_cmp_ge_u32 s69, 125\n\t"
    "s_cselect_b32 s69, s44, s69\n\t"
    "s_mul_i32 m0, s69, 1040\n\t"
    "s_lshl_b32 s44, s69, 11\n\t"
    "buffer_load_dwordx4 v126, %15, s44 offen lds\n\t"
    "s_add_u32 s69, s69, 2\n\t"
    "s_sub_u32 s44, s69, 125\n\t"
    "s_cmp_ge_u32 s69, 125\n\t"
    "s_cselect_b32 s69, s44, s69\n\t"
    "s_mul_i32 m0, s69, 1040\n\t"
    "s_lshl_b32 s44, s69, 11\n\t"
    "buffer_load_dwordx4 v126, %15, s44 offen lds\n\t"
    "s_add_u32 s69, s69, 2\n\t"
    "s_sub_u32 s44, s69, 125\n\t"
    "s_cmp_ge_u32 s69, 125\n\t"
    "s_cselect_b32 s69, s44, s69\n\t"
    "s_mul_i32 m0, s69, 1040\n\t"
    "s_lshl_b32 s44, s69, 11\n\t"
    "buffer_load_dwordx4 v126, %15, s44 offen lds\n\t"
    "s_add_u32 s69, s69, 2\n\t"
    "s_sub_u32 s44, s69, 125\n\t"
    "s_cmp_ge_u32 s69, 125\n\t"
    "s_cselect_b32 s69, s44, s69\n\t"
    "s_mul_i32 m0, s69, 1040\n\t"
    "s_lshl_b32 s44, s69, 11\n\t"
    "buffer_load_dwordx4 v126, %15, s44 offen lds\n\t"
    "s_add_u32 s69, s69, 2\n\t"
    "s_sub_u32 s44, s69, 125\n\t"
    "s_cmp_ge_u32 s69, 125\n\t"
    "s_cselect_b32 s69, s44, s69\n\t"
    "s_mul_i32 m0, s69, 1040\n\t"
    "s_lshl_b32 s44, s69, 11\n\t"
    "buffer_load_dwordx4 v126, %15, s44 offen lds\n\t"
    "s_add_u32 s69, s69, 2\n\t"
    "s_sub_u32 s44, s69, 125\n\t"
    "s_cmp_ge_u32 s69, 125\n\t"
    "s_cselect_b32 s69, s44, s69\n\t"
    "s_mul_i32 m0, s69, 1040\n\t"
    "s_lshl_b32 s44, s69, 11\n\t"
    "buffer_load_dwordx4 v126, %15, s44 offen lds\n\t"
    "s_add_u32 s69, s69, 2\n\t"
    "s_sub_u32 s44, s69, 125\n\t"
    "s_cmp_ge_u32 s69, 125\n\t"
    "s_cselect_b32 s69, s44, s69\n\t"
    "s_mul_i32 m0, s69, 1040\n\t"
    "s_lshl_b32 s44, s69, 11\n\t"
    "buffer_load_dwordx4 v126, %15, s44 offen lds\n\t"
    "s_add_u32 s69, s69, 2\n\t"
    "s_sub_u32 s44, s69, 125\n\t"
    "s_cmp_ge_u32 s69, 125\n\t"
    "s_cselect_b32 s69, s44, s69\n\t"
    "s_mul_i32 m0, s69, 1040\n\t"
    "s_lshl_b32 s44, s69, 11\n\t"
    "buffer_load_dwordx4 v126, %15, s44 offen lds\n\t"
    "s_add_u32 s69, s69, 2\n\t"
    "s_sub_u32 s44, s69, 125\n\t"
    "s_cmp_ge_u32 s69, 125\n\t"
    "s_cselect_b32 s69, s44, s69\n\t"
    "s_mul_i32 m0, s69, 1040\n\t"
    "s_lshl_b32 s44, s69, 11\n\t"
    "buffer_load_dwordx4 v126, %15, s44 offen lds\n\t"
    "s_add_u32 s69, s69, 2\n\t"
    "s_sub_u32 s44, s69, 125\n\t"
    "s_cmp_ge_u32 s69, 125\n\t"
    "s_cselect_b32 s69, s44, s69\n\t"
    "s_mul_i32 m0, s69, 1040\n\t"
    "s_lshl_b32 s44, s69, 11\n\t"
    "buffer_load_dwordx4 v126, %15, s44 offen lds\n\t"
    "s_add_u32 s69, s69, 2\n\t"
    "s_sub_u32 s44, s69, 125\n\t"
    "s_cmp_ge_u32 s69, 125\n\t"
    "s_cselect_b32 s69, s44, s69\n\t"
    "s_mul_i32 m0, s69, 1040\n\t"
    "s_lshl_b32 s44, s69, 11\n\t"
    "buffer_load_dwordx4 v126, %15, s44 offen lds\n\t"
    "s_add_u32 s69, s69, 2\n\t"
    "s_sub_u32 s44, s69, 125\n\t"
    "s_cmp_ge_u32 s69, 125\n\t"
    "s_cselect_b32 s69, s44, s69\n\t"
    "s_mul_i32 m0, s69, 1040\n\t"
    "s_lshl_b32 s44, s69, 11\n\t"
    "buffer_load_dwordx4 v126, %15, s44 offen lds\n\t"
    "s_add_u32 s69, s69, 2\n\t"
    "s_sub_u32 s44, s69, 125\n\t"
    "s_cmp_ge_u32 s69, 125\n\t"
    "s_cselect_b32 s69, s44, s69\n\t"
    "s_mul_i32 m0, s69, 1040\n\t"
    "s_lshl_b32 s44, s69, 11\n\t"
    "buffer_load_dwordx4 v126, %15, s44 offen lds\n\t"
    "s_add_u32 s69, s69, 2\n\t"
    "s_sub_u32 s44, s69, 125\n\t"
    "s_cmp_ge_u32 s69, 125\n\t"
    "s_cselect_b32 s69, s44, s69\n\t"
    "s_mul_i32 m0, s69, 1040\n\t"
    "s_lshl_b32 s44, s69, 11\n\t"
    "buffer_load_dwordx4 v126, %15, s44 offen lds\n\t"
    "s_add_u32 s69, s69, 2\n\t"
    "s_sub_u32 s44, s69, 125\n\t"
    "s_cmp_ge_u32 s69, 125\n\t"
    "s_cselect_b32 s69, s44, s69\n\t"
    "s_mul_i32 m0, s69, 1040\n\t"
    "s_lshl_b32 s44, s69, 11\n\t"
    "buffer_load_dwordx4 v126, %15, s44 offen lds\n\t"
    "s_add_u32 s69, s69, 2\n\t"
    "s_sub_u32 s44, s69, 125\n\t"
    "s_cmp_ge_u32 s69, 125\n\t"
    "s_cselect_b32 s69, s44, s69\n\t"
    "L_ab_%=:\n\t"
    "s_mul_i32 s66, s53, 400\n\t"
    "buffer_load_dwordx3 v[128:130], v153, %0, s66 offen\n\t"
    "buffer_load_dwordx4 v[132:135], v153, %0, s66 offen offset:12\n\t"
    "s_mul_i32 s66, s54, 400\n\t"
    "buffer_load_dwordx3 v[136:138], v153, %0, s66 offen\n\t"
    "buffer_load_dwordx4 v[140:143], v153, %0, s66 offen offset:12\n\t"
    "s_call_b64 s[98:99], L_setup_%=\n\t"
    "L_asp8_%=:\n\t"
    "ds_read_b32 v119, v117 offset:4\n\t"
    "s_waitcnt lgkmcnt(0)\n\t"
    "v_readfirstlane_b32 s46, v119\n\t"
    "s_nop 0\n\t"
    "s_cmp_ge_u32 s46, 4\n\t"
    "s_cbranch_scc1 L_ago8_%=\n\t"
    "s_sleep 1\n\t"
    "s_branch L_asp8_%=\n\t"
    "L_ago8_%=:\n\t"
    "ds_read_b128 v[0:3], v95 offset:0\n\t"
    "ds_read_b128 v[4:7], v95 offset:1024\n\t"
    "ds_read_b128 v[8:11], v95 offset:2048\n\t"
    "ds_read_b128 v[12:15], v95 offset:3072\n\t"
    "ds_read_b128 v[16:19], v95 offset:4096\n\t"
    "ds_read_b128 v[20:23], v95 offset:5120\n\t"
    "ds_read_b128 v[24:27], v95 offset:6144\n\t"
    "ds_read_b128 v[28:31], v95 offset:7168\n\t"
    "ds_read_b128 v[32:35], v95 offset:8192\n\t"
    "ds_read_b128 v[36:39], v95 offset:9216\n\t"
    "ds_read_b128 v[40:43], v95 offset:10240\n\t"
    "ds_read_b128 v[44:47], v95 offset:11264\n\t"
    "ds_read_b128 v[48:51], v95 offset:12288\n\t"
    "ds_read_b128 v[52:55], v95 offset:13312\n\t"
    "ds_read_b128 v[56:59], v95 offset:14336\n\t"
    "ds_read_b128 v[60:63], v95 offset:15360\n\t"
    "ds_read_b128 v[64:67], v95 offset:16384\n\t"
    "ds_read_b128 v[68:71], v95 offset:17408\n\t"
    "ds_read_b128 v[72:75], v95 offset:18432\n\t"
    "ds_read_b128 v[76:79], v95 offset:19456\n\t"
    "ds_read_b128 v[80:83], v95 offset:20480\n\t"
    "ds_read_b128 v[84:87], v95 offset:21504\n\t"
    "ds_read_b128 v[88:91], v95 offset:22528\n\t"
    "s_waitcnt vmcnt(4)\n\t"
    "ds_add_u32 v117, v118 offset:8\n\t"
    "s_waitcnt vmcnt(0)\n\t"
    "s_branch L_awd_%=\n\t"
    "s_waitcnt vmcnt(4)\n\t"
    "ds_add_u32 v117, v118 offset:8\n\t"
    "s_branch L_await_%=\n\t"
    "L_setup_%=:\n\t"
    "v_mbcnt_lo_u32_b32 v100, -1, 0\n\t"
    "v_mbcnt_hi_u32_b32 v100, -1, v100\n\t"
    "s_lshl_b32 s61, %16, 15\n\t"
    "s_add_u32 s61, s61, 16515072\n\t"
    "v_lshlrev_b32 v127, 4, v100\n\t"
    "s_lshr_b32 s37, %14, 4\n\t"
    "s_and_b32 s39, %14, 7\n\t"
    "v_and_b32 v101, 15, v100\n\t"
    "s_lshl_b32 s39, s39, 4\n\t"
    "s_add_u32 s39, s39, s37\n\t"
    "s_mul_i32 s65, %16, 125\n\t"
    "v_add_u32 v155, s65, v101\n\t"
    "s_mul_i32 s63, s39, 102400\n\t"
    "s_add_u32 s63, s63, 3407872\n\t"
    "v_lshrrev_b32 v102, 2, v101\n\t"
    "s_and_b32 s37, s37, 7\n\t"
    "s_lshl_b32 s37, s37, 11\n\t"
    "v_lshrrev_b32 v103, 3, v100\n\t"
    "s_mul_i32 s36, s39, 65536\n\t"
    "s_bfe_u32 s65, %14, 0x10003\n\t"
    "v_and_b32 v103, 2, v103\n\t"
    "s_lshl_b32 s38, s65, 15\n\t"
    "s_add_u32 s36, s36, s38\n\t"
    "v_and_b32 v104, 1, v100\n\t"
    "s_lshl_b32 s38, %16, 12\n\t"
    "s_add_u32 s36, s36, s38\n\t"
    "v_lshlrev_b32 v104, 4, v104\n\t"
    "s_add_u32 s36, s36, 17825792\n\t"
    "v_mov_b32 v101, 0x3f80\n\t"
    "v_lshlrev_b32 v104, v104, v101\n\t"
    "v_cmp_eq_u32 vcc, v102, v103\n\t"
    "s_waitcnt lgkmcnt(0)\n\t"
    "s_lshr_b32 s39, s91, %9\n\t"
    "s_lshl_b32 s39, s39, 16\n\t"
    "v_cndmask_b32 v120, 0, v104, vcc\n\t"
    "v_cndmask_b32 v121, 0, v104, vcc\n\t"
    "v_add_u32 v103, 1, v103\n\t"
    "v_cmp_eq_u32 vcc, v102, v103\n\t"
    "s_lshl_b32 s40, s88, 16\n\t"
    "s_and_b32 s41, s88, 0xffff0000\n\t"
    "v_cndmask_b32 v122, 0, v104, vcc\n\t"
    "v_cndmask_b32 v123, 0, v104, vcc\n\t"
    "v_and_b32 v101, 2, v100\n\t"
    "v_mov_b32 v102, 0x44444444\n\t"
    "v_mov_b32 v103, 0xeeeeeeee\n\t"
    "v_cmp_eq_u32 vcc, 0, v101\n\t"
    "s_lshl_b32 s42, s89, 16\n\t"
    "s_and_b32 s43, s89, 0xffff0000\n\t"
    "v_cndmask_b32 v124, v103, v102, vcc\n\t"
    "v_lshrrev_b32 v101, 4, v100\n\t"
    "v_mul_u32_u24 v101, 8000, v101\n\t"
    "v_lshl_add_u32 v125, v155, 3, v101\n\t"
    "v_add_u32 v125, 64000, v125\n\t"
    "s_lshl_b32 s70, s72, 16\n\t"
    "s_and_b32 s71, s72, 0xffff0000\n\t"
    "s_lshl_b32 s72, s73, 16\n\t"
    "s_and_b32 s73, s73, 0xffff0000\n\t"
    "s_mov_b32 s67, 0xffff0000\n\t"
    "s_and_b32 s65, %14, 8\n\t"
    "s_add_u32 s38, s65, %16\n\t"
    "s_mul_i32 s38, s38, 212992\n\t"
    "s_add_u32 s38, s38, 196608\n\t"
    "s_add_u32 s84, s37, s38\n\t"
    "s_add_u32 s37, s37, 0x800\n\t"
    "s_and_b32 s37, s37, 0x3800\n\t"
    "s_add_u32 s85, s37, s38\n\t"
    "s_add_u32 s37, s37, 0x800\n\t"
    "s_and_b32 s37, s37, 0x3800\n\t"
    "s_add_u32 s86, s37, s38\n\t"
    "s_add_u32 s37, s37, 0x800\n\t"
    "s_and_b32 s37, s37, 0x3800\n\t"
    "s_add_u32 s87, s37, s38\n\t"
    "s_add_u32 s37, s37, 0x800\n\t"
    "s_and_b32 s37, s37, 0x3800\n\t"
    "s_add_u32 s88, s37, s38\n\t"
    "s_add_u32 s37, s37, 0x800\n\t"
    "s_and_b32 s37, s37, 0x3800\n\t"
    "s_add_u32 s89, s37, s38\n\t"
    "s_add_u32 s37, s37, 0x800\n\t"
    "s_and_b32 s37, s37, 0x3800\n\t"
    "s_add_u32 s90, s37, s38\n\t"
    "s_add_u32 s37, s37, 0x800\n\t"
    "s_and_b32 s37, s37, 0x3800\n\t"
    "s_add_u32 s91, s37, s38\n\t"
    "s_mul_i32 s94, s65, 425984\n\t"
    "s_sub_u32 s94, s94, 1703936\n\t"
    "s_sub_u32 s95, 0x8000, s94\n\t"
    "s_lshl_b32 s97, s65, 11\n\t"
    "s_sub_u32 s97, s97, 229376\n\t"
    "s_mov_b32 s92, 64000\n\t"
    "s_mov_b32 s93, -64000\n\t"
    "s_mov_b32 s96, 0x2000\n\t"
    "s_mov_b32 s62, 2\n\t"
    "v_rcp_f32 v101, s40\n\t"
    "v_rcp_f32 v102, s41\n\t"
    "v_rcp_f32 v103, s42\n\t"
    "v_rcp_f32 v104, s43\n\t"
    "v_mov_b32 v100, 0x7f7fffff\n\t"
    "v_med3_f32 v101, v101, -v100, v100\n\t"
    "v_med3_f32 v102, v102, -v100, v100\n\t"
    "v_med3_f32 v103, v103, -v100, v100\n\t"
    "v_med3_f32 v104, v104, -v100, v100\n\t"
    "v_readfirstlane_b32 s40, v101\n\t"
    "v_readfirstlane_b32 s41, v102\n\t"
    "v_readfirstlane_b32 s42, v103\n\t"
    "v_readfirstlane_b32 s43, v104\n\t"
    "s_setpc_b64 s[98:99]\n\t"
    "L_aside_%=:\n\t"
    "s_setprio 3\n\t"
    "v_cmp_gt_u32 vcc, 48, v147\n\t"
    "v_mov_b32 v133, 0x40000000\n\t"
    "v_lshrrev_b32 v134, 3, v147\n\t"
    "v_bfe_u32 v135, v147, 2, 1\n\t"
    "v_and_b32 v130, 3, v147\n\t"
    "v_lshl_add_u32 v132, v135, 5, v130\n\t"
    "v_mul_u32_u24 v64, 400, v132\n\t"
    "v_lshl_add_u32 v64, v134, 5, v64\n\t"
    "s_movk_i32 s50, 0\n\t"
    "buffer_load_dwordx4 v[0:3], v64, %0, s50 offen offset:16\n\t"
    "buffer_load_dwordx4 v[4:7], v64, %0, s50 offen offset:32\n\t"
    "v_lshlrev_b32 v66, 14, v134\n\t"
    "v_lshl_add_u32 v66, v132, 4, v66\n\t"
    "v_lshlrev_b32 v68, 8, v134\n\t"
    "v_lshl_add_u32 v68, v135, 2, v68\n\t"
    "v_add_u32 v68, 130048, v68\n\t"
    "v_cndmask_b32 v68, v133, v68, vcc\n\t"
    "v_cndmask_b32 v66, v133, v66, vcc\n\t"
    "v_lshrrev_b32 v134, 2, v147\n\t"
    "v_bfe_u32 v135, v147, 1, 1\n\t"
    "v_and_b32 v130, 1, v147\n\t"
    "v_lshl_add_u32 v132, v135, 5, v130\n\t"
    "v_mul_u32_u24 v65, 400, v132\n\t"
    "v_lshl_add_u32 v65, v134, 5, v65\n\t"
    "v_lshlrev_b32 v67, 14, v134\n\t"
    "v_lshl_add_u32 v67, v132, 4, v67\n\t"
    "v_lshlrev_b32 v69, 8, v134\n\t"
    "v_lshl_add_u32 v69, v135, 2, v69\n\t"
    "v_add_u32 v69, 130048, v69\n\t"
    "v_cndmask_b32 v69, v133, v69, vcc\n\t"
    "v_cndmask_b32 v67, v133, v67, vcc\n\t"
    "s_barrier\n\t"
    "s_load_dwordx2 s[72:73], %6, 0\n\t"
    "s_load_dwordx2 s[88:89], %7, 0\n\t"
    "s_load_dword s91, %8, 0\n\t"
    "v_lshlrev_b32 v128, 4, v147\n\t"
    "v_add_u32 v128, 133120, v128\n\t"
    "v_and_b32 v130, 1, v147\n\t"
    "v_cndmask_b32 v128, v133, v128, vcc\n\t"
    "v_mul_u32_u24 v127, 7, v130\n\t"
    "v_cmp_eq_u32 vcc, 1, v130\n\t"
    "v_mov_b32 v126, 0x07060100\n\t"
    "v_mov_b32 v133, 0x05040100\n\t"
    "v_mov_b32 v136, 1\n\t"
    "v_cndmask_b32 v126, v126, v133, vcc\n\t"
    "s_mov_b32 s36, 0\n\t"
    "s_mov_b32 s37, 8000\n\t"
    "s_mov_b32 s38, 16000\n\t"
    "s_mov_b32 s39, 24000\n\t"
    "s_mov_b32 s40, 32000\n\t"
    "s_mov_b32 s41, 40000\n\t"
    "s_mov_b32 s42, 48000\n\t"
    "s_mov_b32 s43, 56000\n\t"
    "s_movk_i32 s44, 0xf8\n\t"
    "s_mov_b32 s45, 0x01010000\n\t"
    "s_mov_b32 s46, 0x020c000c\n\t"
    "s_lshr_b32 s49, %14, 4\n\t"
    "s_mul_i32 s49, s49, 212992\n\t"
    "s_and_b32 s50, %14, 15\n\t"
    "s_lshl_b32 s50, s50, 10\n\t"
    "s_add_u32 s49, s49, s50\n\t"
    "v_lshrrev_b32 v134, 4, v147\n\t"
    "v_bfe_u32 v135, v147, 2, 2\n\t"
    "v_and_b32 v130, 3, v147\n\t"
    "v_lshlrev_b32 v137, 17, v134\n\t"
    "v_lshl_add_u32 v137, v135, 10, v137\n\t"
    "v_lshl_add_u32 v137, v130, 4, v137\n\t"
    "v_add_u32 v138, 0x1000, v137\n\t"
    "v_mul_u32_u24 v141, 0x210, v134\n\t"
    "v_lshl_add_u32 v141, v135, 6, v141\n\t"
    "v_lshl_add_u32 v141, v130, 4, v141\n\t"
    "v_add_u32 v141, 134656, v141\n\t"
    "v_mul_u32_u24 v142, 0x210, v130\n\t"
    "v_lshl_add_u32 v142, v135, 6, v142\n\t"
    "v_lshl_add_u32 v142, v134, 3, v142\n\t"
    "v_add_u32 v142, 134656, v142\n\t"
    "s_mov_b32 s50, 102400\n\t"
    "v_mul_u32_u24 v139, s50, v134\n\t"
    "v_and_b32 v132, 15, v147\n\t"
    "v_lshl_add_u32 v139, v132, 4, v139\n\t"
    "v_add_u32 v140, 409600, v139\n\t"
    "s_lshr_b32 s50, %14, 4\n\t"
    "s_lshl_b32 s47, s50, 13\n\t"
    "s_lshl_b32 s48, s50, 8\n\t"
    "s_and_b32 s50, %14, 3\n\t"
    "s_mul_i32 s51, s50, 256\n\t"
    "s_add_u32 s47, s47, s51\n\t"
    "s_mul_i32 s51, s50, 3276800\n\t"
    "s_add_u32 s48, s48, s51\n\t"
    "s_bfe_u32 s50, %14, 131074\n\t"
    "s_lshl_b32 s51, s50, 19\n\t"
    "s_add_u32 s47, s47, s51\n\t"
    "s_lshl_b32 s51, s50, 12\n\t"
    "s_add_u32 s48, s48, s51\n\t"
    "s_add_u32 s48, s48, 3407872\n\t"
    "s_waitcnt vmcnt(2)\n\t"
    "v_lshlrev_b32 v134, 20, v147\n\t"
    "v_add_u32 v134, 2068, v134\n\t"
    "ds_add_u32 v134, v136\n\t"
    "v_mov_b32 v110, 65664\n\t"
    "v_mov_b32 v111, 131136\n\t"
    "v_mov_b32 v112, 262176\n\t"
    "v_mov_b32 v113, 524304\n\t"
    "v_mov_b32 v114, 1048584\n\t"
    "v_mov_b32 v115, 2097156\n\t"
    "v_mov_b32 v116, 4194306\n\t"
    "v_mov_b32 v117, 8388609\n\t"
    "s_waitcnt vmcnt(0)\n\t"
    "v_lshl_add_u32 v76, v0, 3, s36\n\t"
    "v_lshl_add_u32 v77, v1, 3, s37\n\t"
    "v_lshl_add_u32 v78, v2, 3, s38\n\t"
    "v_lshl_add_u32 v79, v3, 3, s39\n\t"
    "v_lshl_add_u32 v80, v4, 3, s40\n\t"
    "v_lshl_add_u32 v81, v5, 3, s41\n\t"
    "v_lshl_add_u32 v82, v6, 3, s42\n\t"
    "v_lshl_add_u32 v83, v7, 3, s43\n\t"
    "v_and_or_b32 v92, v76, s44, v68\n\t"
    "ds_or_rtn_b32 v118, v92, v110\n\t"
    "v_and_or_b32 v93, v77, s44, v68\n\t"
    "ds_or_rtn_b32 v119, v93, v111\n\t"
    "v_and_or_b32 v94, v78, s44, v68\n\t"
    "ds_or_rtn_b32 v120, v94, v112\n\t"
    "v_and_or_b32 v95, v79, s44, v68\n\t"
    "ds_or_rtn_b32 v121, v95, v113\n\t"
    "v_and_or_b32 v96, v80, s44, v68\n\t"
    "ds_or_rtn_b32 v122, v96, v114\n\t"
    "v_and_or_b32 v97, v81, s44, v68\n\t"
    "ds_or_rtn_b32 v123, v97, v115\n\t"
    "v_and_or_b32 v98, v82, s44, v68\n\t"
    "ds_or_rtn_b32 v124, v98, v116\n\t"
    "v_and_or_b32 v99, v83, s44, v68\n\t"
    "ds_or_rtn_b32 v125, v99, v117\n\t"
    "v_lshl_or_b32 v164, v77, 16, v76\n\t"
    "v_lshl_or_b32 v165, v79, 16, v78\n\t"
    "v_lshl_or_b32 v166, v81, 16, v80\n\t"
    "v_lshl_or_b32 v167, v83, 16, v82\n\t"
    "s_movk_i32 s50, 0\n\t"
    "buffer_load_dwordx4 v[8:11], v64, %0, s50 offen offset:1616\n\t"
    "buffer_load_dwordx4 v[12:15], v64, %0, s50 offen offset:1632\n\t"
    "s_movk_i32 s50, 3200\n\t"
    "buffer_load_dwordx4 v[16:19], v64, %0, s50 offen offset:16\n\t"
    "buffer_load_dwordx4 v[20:23], v64, %0, s50 offen offset:32\n\t"
    "s_waitcnt vmcnt(2)\n\t"
    "v_lshl_add_u32 v84, v8, 3, s36\n\t"
    "v_lshl_add_u32 v85, v9, 3, s37\n\t"
    "v_lshl_add_u32 v86, v10, 3, s38\n\t"
    "v_lshl_add_u32 v87, v11, 3, s39\n\t"
    "v_lshl_add_u32 v88, v12, 3, s40\n\t"
    "v_lshl_add_u32 v89, v13, 3, s41\n\t"
    "v_lshl_add_u32 v90, v14, 3, s42\n\t"
    "v_lshl_add_u32 v91, v15, 3, s43\n\t"
    "s_waitcnt lgkmcnt(4)\n\t"
    "v_and_b32 v118, v118, v110\n\t"
    "v_perm_b32 v118, v118, v118, s46\n\t"
    "ds_or_b32 v92, v118\n\t"
    "v_and_b32 v119, v119, v111\n\t"
    "v_perm_b32 v119, v119, v119, s46\n\t"
    "ds_or_b32 v93, v119\n\t"
    "v_and_b32 v120, v120, v112\n\t"
    "v_perm_b32 v120, v120, v120, s46\n\t"
    "ds_or_b32 v94, v120\n\t"
    "v_and_b32 v121, v121, v113\n\t"
    "v_perm_b32 v121, v121, v121, s46\n\t"
    "ds_or_b32 v95, v121\n\t"
    "s_waitcnt lgkmcnt(4)\n\t"
    "v_and_b32 v122, v122, v114\n\t"
    "v_perm_b32 v122, v122, v122, s46\n\t"
    "ds_or_b32 v96, v122\n\t"
    "v_and_b32 v123, v123, v115\n\t"
    "v_perm_b32 v123, v123, v123, s46\n\t"
    "ds_or_b32 v97, v123\n\t"
    "v_and_b32 v124, v124, v116\n\t"
    "v_perm_b32 v124, v124, v124, s46\n\t"
    "ds_or_b32 v98, v124\n\t"
    "v_and_b32 v125, v125, v117\n\t"
    "v_perm_b32 v125, v125, v125, s46\n\t"
    "ds_or_b32 v99, v125\n\t"
    "buffer_store_dwordx4 v[164:167], v66, %12, s49 offen offset:0 sc1\n\t"
    "v_and_or_b32 v100, v84, s44, v68\n\t"
    "ds_read_b32 v100, v100\n\t"
    "v_and_or_b32 v101, v85, s44, v68\n\t"
    "ds_read_b32 v101, v101\n\t"
    "v_and_or_b32 v102, v86, s44, v68\n\t"
    "ds_read_b32 v102, v102\n\t"
    "v_and_or_b32 v103, v87, s44, v68\n\t"
    "ds_read_b32 v103, v103\n\t"
    "v_and_or_b32 v104, v88, s44, v68\n\t"
    "ds_read_b32 v104, v104\n\t"
    "v_and_or_b32 v105, v89, s44, v68\n\t"
    "ds_read_b32 v105, v105\n\t"
    "v_and_or_b32 v106, v90, s44, v68\n\t"
    "ds_read_b32 v106, v106\n\t"
    "v_and_or_b32 v107, v91, s44, v68\n\t"
    "ds_read_b32 v107, v107\n\t"
    "s_waitcnt vmcnt(1)\n\t"
    "v_lshl_add_u32 v76, v16, 3, s36\n\t"
    "v_lshl_add_u32 v77, v17, 3, s37\n\t"
    "v_lshl_add_u32 v78, v18, 3, s38\n\t"
    "v_lshl_add_u32 v79, v19, 3, s39\n\t"
    "v_lshl_add_u32 v80, v20, 3, s40\n\t"
    "v_lshl_add_u32 v81, v21, 3, s41\n\t"
    "v_lshl_add_u32 v82, v22, 3, s42\n\t"
    "v_lshl_add_u32 v83, v23, 3, s43\n\t"
    "s_movk_i32 s50, 3200\n\t"
    "buffer_load_dwordx4 v[24:27], v64, %0, s50 offen offset:1616\n\t"
    "buffer_load_dwordx4 v[28:31], v64, %0, s50 offen offset:1632\n\t"
    "s_movk_i32 s50, 6400\n\t"
    "buffer_load_dwordx4 v[32:35], v64, %0, s50 offen offset:16\n\t"
    "buffer_load_dwordx4 v[36:39], v64, %0, s50 offen offset:32\n\t"
    "v_mov_b32 v129, 0xffff0000\n\t"
    "s_waitcnt lgkmcnt(4)\n\t"
    "v_perm_b32 v100, v100, v84, v126\n\t"
    "v_perm_b32 v101, v101, v85, v126\n\t"
    "v_perm_b32 v102, v102, v86, v126\n\t"
    "v_perm_b32 v103, v103, v87, v126\n\t"
    "s_waitcnt lgkmcnt(0)\n\t"
    "v_perm_b32 v104, v104, v88, v126\n\t"
    "v_perm_b32 v105, v105, v89, v126\n\t"
    "v_perm_b32 v106, v106, v90, v126\n\t"
    "v_perm_b32 v107, v107, v91, v126\n\t"
    "v_max_u32 v108, v100, v101\n\t"
    "v_min_u32 v101, v100, v101\n\t"
    "v_max_u32 v100, v102, v103\n\t"
    "v_min_u32 v103, v102, v103\n\t"
    "v_max_u32 v102, v104, v105\n\t"
    "v_min_u32 v105, v104, v105\n\t"
    "v_max_u32 v104, v106, v107\n\t"
    "v_min_u32 v107, v106, v107\n\t"
    "v_max_u32 v106, v108, v100\n\t"
    "v_min_u32 v100, v108, v100\n\t"
    "v_max_u32 v108, v101, v103\n\t"
    "v_min_u32 v103, v101, v103\n\t"
    "v_max_u32 v101, v102, v104\n\t"
    "v_min_u32 v104, v102, v104\n\t"
    "v_max_u32 v102, v105, v107\n\t"
    "v_min_u32 v107, v105, v107\n\t"
    "v_bfi_b32 v130, v106, 0, v129\n\t"
    "v_cmp_eq_u32 vcc, 0, v130\n\t"
    "v_and_or_b32 v92, v106, s44, v68\n\t"
    "s_nop 0\n\t"
    "v_cndmask_b32 v130, v130, v129, vcc\n\t"
    "v_ffbl_b32 v131, v130\n\t"
    "v_and_b32 v134, 7, v131\n\t"
    "v_lshlrev_b32 v134, v134, s45\n\t"
    "v_xor_b32 v129, v129, v134\n\t"
    "v_xor_b32 v132, v131, v127\n\t"
    "v_bfi_b32 v130, v101, 0, v129\n\t"
    "v_cmp_eq_u32 vcc, 0, v130\n\t"
    "v_and_or_b32 v93, v101, s44, v68\n\t"
    "v_xor_b32 v133, 23, v132\n\t"
    "v_lshlrev_b32 v110, v132, v136\n\t"
    "v_and_b32 v132, 7, v132\n\t"
    "v_lshl_or_b32 v110, v136, v133, v110\n\t"
    "v_lshl_add_u32 v132, v132, 1, v128\n\t"
    "ds_or_rtn_b32 v118, v92, v110\n\t"
    "ds_write_b16 v132, v106\n\t"
    "v_cndmask_b32 v130, v130, v129, vcc\n\t"
    "v_ffbl_b32 v131, v130\n\t"
    "v_and_b32 v134, 7, v131\n\t"
    "v_lshlrev_b32 v134, v134, s45\n\t"
    "v_xor_b32 v129, v129, v134\n\t"
    "v_xor_b32 v132, v131, v127\n\t"
    "v_bfi_b32 v130, v108, 0, v129\n\t"
    "v_cmp_eq_u32 vcc, 0, v130\n\t"
    "v_and_or_b32 v94, v108, s44, v68\n\t"
    "v_xor_b32 v133, 23, v132\n\t"
    "v_lshlrev_b32 v111, v132, v136\n\t"
    "v_and_b32 v132, 7, v132\n\t"
    "v_lshl_or_b32 v111, v136, v133, v111\n\t"
    "v_lshl_add_u32 v132, v132, 1, v128\n\t"
    "ds_or_rtn_b32 v119, v93, v111\n\t"
    "ds_write_b16 v132, v101\n\t"
    "v_cndmask_b32 v130, v130, v129, vcc\n\t"
    "v_ffbl_b32 v131, v130\n\t"
    "v_and_b32 v134, 7, v131\n\t"
    "v_lshlrev_b32 v134, v134, s45\n\t"
    "v_xor_b32 v129, v129, v134\n\t"
    "v_xor_b32 v132, v131, v127\n\t"
    "v_bfi_b32 v130, v100, 0, v129\n\t"
    "v_cmp_eq_u32 vcc, 0, v130\n\t"
    "v_and_or_b32 v95, v100, s44, v68\n\t"
    "v_xor_b32 v133, 23, v132\n\t"
    "v_lshlrev_b32 v112, v132, v136\n\t"
    "v_and_b32 v132, 7, v132\n\t"
    "v_lshl_or_b32 v112, v136, v133, v112\n\t"
    "v_lshl_add_u32 v132, v132, 1, v128\n\t"
    "ds_or_rtn_b32 v120, v94, v112\n\t"
    "ds_write_b16 v132, v108\n\t"
    "v_cndmask_b32 v130, v130, v129, vcc\n\t"
    "v_ffbl_b32 v131, v130\n\t"
    "v_and_b32 v134, 7, v131\n\t"
    "v_lshlrev_b32 v134, v134, s45\n\t"
    "v_xor_b32 v129, v129, v134\n\t"
    "v_xor_b32 v132, v131, v127\n\t"
    "v_bfi_b32 v130, v102, 0, v129\n\t"
    "v_cmp_eq_u32 vcc, 0, v130\n\t"
    "v_and_or_b32 v96, v102, s44, v68\n\t"
    "v_xor_b32 v133, 23, v132\n\t"
    "v_lshlrev_b32 v113, v132, v136\n\t"
    "v_and_b32 v132, 7, v132\n\t"
    "v_lshl_or_b32 v113, v136, v133, v113\n\t"
    "v_lshl_add_u32 v132, v132, 1, v128\n\t"
    "ds_or_rtn_b32 v121, v95, v113\n\t"
    "ds_write_b16 v132, v100\n\t"
    "v_cndmask_b32 v130, v130, v129, vcc\n\t"
    "v_ffbl_b32 v131, v130\n\t"
    "v_and_b32 v134, 7, v131\n\t"
    "v_lshlrev_b32 v134, v134, s45\n\t"
    "v_xor_b32 v129, v129, v134\n\t"
    "v_xor_b32 v132, v131, v127\n\t"
    "s_waitcnt lgkmcnt(7)\n\t"
    "v_and_b32 v118, v118, v110\n\t"
    "v_perm_b32 v118, v118, v118, s46\n\t"
    "ds_or_b32 v92, v118\n\t"
    "v_bfi_b32 v130, v104, 0, v129\n\t"
    "v_cmp_eq_u32 vcc, 0, v130\n\t"
    "v_and_or_b32 v97, v104, s44, v68\n\t"
    "v_xor_b32 v133, 23, v132\n\t"
    "v_lshlrev_b32 v114, v132, v136\n\t"
    "v_and_b32 v132, 7, v132\n\t"
    "v_lshl_or_b32 v114, v136, v133, v114\n\t"
    "v_lshl_add_u32 v132, v132, 1, v128\n\t"
    "ds_or_rtn_b32 v122, v96, v114\n\t"
    "ds_write_b16 v132, v102\n\t"
    "v_cndmask_b32 v130, v130, v129, vcc\n\t"
    "v_ffbl_b32 v131, v130\n\t"
    "v_and_b32 v134, 7, v131\n\t"
    "v_lshlrev_b32 v134, v134, s45\n\t"
    "v_xor_b32 v129, v129, v134\n\t"
    "v_xor_b32 v132, v131, v127\n\t"
    "s_waitcnt lgkmcnt(8)\n\t"
    "v_and_b32 v119, v119, v111\n\t"
    "v_perm_b32 v119, v119, v119, s46\n\t"
    "ds_or_b32 v93, v119\n\t"
    "v_bfi_b32 v130, v103, 0, v129\n\t"
    "v_cmp_eq_u32 vcc, 0, v130\n\t"
    "v_and_or_b32 v98, v103, s44, v68\n\t"
    "v_xor_b32 v133, 23, v132\n\t"
    "v_lshlrev_b32 v115, v132, v136\n\t"
    "v_and_b32 v132, 7, v132\n\t"
    "v_lshl_or_b32 v115, v136, v133, v115\n\t"
    "v_lshl_add_u32 v132, v132, 1, v128\n\t"
    "ds_or_rtn_b32 v123, v97, v115\n\t"
    "ds_write_b16 v132, v104\n\t"
    "v_cndmask_b32 v130, v130, v129, vcc\n\t"
    "v_ffbl_b32 v131, v130\n\t"
    "v_and_b32 v134, 7, v131\n\t"
    "v_lshlrev_b32 v134, v134, s45\n\t"
    "v_xor_b32 v129, v129, v134\n\t"
    "v_xor_b32 v132, v131, v127\n\t"
    "s_waitcnt lgkmcnt(9)\n\t"
    "v_and_b32 v120, v120, v112\n\t"
    "v_perm_b32 v120, v120, v120, s46\n\t"
    "ds_or_b32 v94, v120\n\t"
    "v_bfi_b32 v130, v107, 0, v129\n\t"
    "v_cmp_eq_u32 vcc, 0, v130\n\t"
    "v_and_or_b32 v99, v107, s44, v68\n\t"
    "v_xor_b32 v133, 23, v132\n\t"
    "v_lshlrev_b32 v116, v132, v136\n\t"
    "v_and_b32 v132, 7, v132\n\t"
    "v_lshl_or_b32 v116, v136, v133, v116\n\t"
    "v_lshl_add_u32 v132, v132, 1, v128\n\t"
    "ds_or_rtn_b32 v124, v98, v116\n\t"
    "ds_write_b16 v132, v103\n\t"
    "v_cndmask_b32 v130, v130, v129, vcc\n\t"
    "v_ffbl_b32 v131, v130\n\t"
    "v_xor_b32 v132, v131, v127\n\t"
    "s_waitcnt lgkmcnt(10)\n\t"
    "v_and_b32 v121, v121, v113\n\t"
    "v_perm_b32 v121, v121, v121, s46\n\t"
    "ds_or_b32 v95, v121\n\t"
    "v_xor_b32 v133, 23, v132\n\t"
    "v_lshlrev_b32 v117, v132, v136\n\t"
    "v_and_b32 v132, 7, v132\n\t"
    "v_lshl_or_b32 v117, v136, v133, v117\n\t"
    "v_lshl_add_u32 v132, v132, 1, v128\n\t"
    "ds_or_rtn_b32 v125, v99, v117\n\t"
    "ds_write_b16 v132, v107\n\t"
    "s_waitcnt lgkmcnt(10)\n\t"
    "v_and_b32 v122, v122, v114\n\t"
    "v_perm_b32 v122, v122, v122, s46\n\t"
    "ds_or_b32 v96, v122\n\t"
    "ds_read_b128 v[72:75], v128\n\t"
    "v_and_or_b32 v100, v76, s44, v68\n\t"
    "ds_read_b32 v100, v100\n\t"
    "v_and_or_b32 v101, v77, s44, v68\n\t"
    "ds_read_b32 v101, v101\n\t"
    "v_and_or_b32 v102, v78, s44, v68\n\t"
    "ds_read_b32 v102, v102\n\t"
    "v_and_or_b32 v103, v79, s44, v68\n\t"
    "ds_read_b32 v103, v103\n\t"
    "v_and_or_b32 v104, v80, s44, v68\n\t"
    "ds_read_b32 v104, v104\n\t"
    "v_and_or_b32 v105, v81, s44, v68\n\t"
    "ds_read_b32 v105, v105\n\t"
    "v_and_or_b32 v106, v82, s44, v68\n\t"
    "ds_read_b32 v106, v106\n\t"
    "v_and_or_b32 v107, v83, s44, v68\n\t"
    "ds_read_b32 v107, v107\n\t"
    "s_waitcnt vmcnt(2)\n\t"
    "v_lshl_add_u32 v84, v24, 3, s36\n\t"
    "v_lshl_add_u32 v85, v25, 3, s37\n\t"
    "v_lshl_add_u32 v86, v26, 3, s38\n\t"
    "v_lshl_add_u32 v87, v27, 3, s39\n\t"
    "v_lshl_add_u32 v88, v28, 3, s40\n\t"
    "v_lshl_add_u32 v89, v29, 3, s41\n\t"
    "v_lshl_add_u32 v90, v30, 3, s42\n\t"
    "v_lshl_add_u32 v91, v31, 3, s43\n\t"
    "s_movk_i32 s50, 6400\n\t"
    "buffer_load_dwordx4 v[40:43], v65, %0, s50 offen offset:1616\n\t"
    "buffer_load_dwordx4 v[44:47], v65, %0, s50 offen offset:1632\n\t"
    "s_movk_i32 s50, 6400\n\t"
    "buffer_load_dwordx4 v[48:51], v65, %0, s50 offen offset:2416\n\t"
    "buffer_load_dwordx4 v[52:55], v65, %0, s50 offen offset:2432\n\t"
    "s_movk_i32 s50, 9600\n\t"
    "buffer_load_dwordx4 v[56:59], v65, %0, s50 offen offset:16\n\t"
    "buffer_load_dwordx4 v[60:63], v65, %0, s50 offen offset:32\n\t"
    "s_movk_i32 s50, 9600\n\t"
    "buffer_load_dwordx4 v[0:3], v65, %0, s50 offen offset:816\n\t"
    "buffer_load_dwordx4 v[4:7], v65, %0, s50 offen offset:832\n\t"
    "s_movk_i32 s50, 9600\n\t"
    "buffer_load_dwordx4 v[8:11], v65, %0, s50 offen offset:1616\n\t"
    "buffer_load_dwordx4 v[12:15], v65, %0, s50 offen offset:1632\n\t"
    "v_mov_b32 v134, 1032\n\t"
    "L_asvsp8_%=:\n\t"
    "ds_read_b32 v130, v134\n\t"
    "s_waitcnt lgkmcnt(0)\n\t"
    "v_readfirstlane_b32 s50, v130\n\t"
    "s_nop 0\n\t"
    "s_cmp_lt_u32 s50, 6\n\t"
    "s_cbranch_scc1 L_asvsp8_%=\n\t"
    "s_mov_b64 s[96:97], %2\n\t"
    "s_and_b32 s97, s97, 0xffff\n\t"
    "buffer_load_dwordx4 v[156:159], v137, s[96:99], s47 offen offset:0\n\t"
    "buffer_load_dwordx4 v[160:163], v138, s[96:99], s47 offen offset:0\n\t"
    "buffer_load_dwordx4 v[164:167], v137, s[96:99], s47 offen offset:64\n\t"
    "buffer_load_dwordx4 v[168:171], v138, s[96:99], s47 offen offset:64\n\t"
    "buffer_load_dwordx4 v[172:175], v137, s[96:99], s47 offen offset:128\n\t"
    "buffer_load_dwordx4 v[176:179], v138, s[96:99], s47 offen offset:128\n\t"
    "buffer_load_dwordx4 v[180:183], v137, s[96:99], s47 offen offset:192\n\t"
    "buffer_load_dwordx4 v[184:187], v138, s[96:99], s47 offen offset:192\n\t"
    "s_add_u32 s47, s47, 2097152\n\t"
    "buffer_load_dwordx4 v[188:191], v137, s[96:99], s47 offen offset:0\n\t"
    "buffer_load_dwordx4 v[192:195], v138, s[96:99], s47 offen offset:0\n\t"
    "buffer_load_dwordx4 v[196:199], v137, s[96:99], s47 offen offset:64\n\t"
    "buffer_load_dwordx4 v[200:203], v138, s[96:99], s47 offen offset:64\n\t"
    "buffer_load_dwordx4 v[204:207], v137, s[96:99], s47 offen offset:128\n\t"
    "buffer_load_dwordx4 v[208:211], v138, s[96:99], s47 offen offset:128\n\t"
    "buffer_load_dwordx4 v[212:215], v137, s[96:99], s47 offen offset:192\n\t"
    "buffer_load_dwordx4 v[216:219], v138, s[96:99], s47 offen offset:192\n\t"
    "s_add_u32 s47, s47, 2097152\n\t"
    "v_and_b32 v123, v123, v115\n\t"
    "v_perm_b32 v123, v123, v123, s46\n\t"
    "ds_or_b32 v97, v123\n\t"
    "v_and_b32 v124, v124, v116\n\t"
    "v_perm_b32 v124, v124, v124, s46\n\t"
    "ds_or_b32 v98, v124\n\t"
    "v_and_b32 v125, v125, v117\n\t"
    "v_perm_b32 v125, v125, v125, s46\n\t"
    "ds_or_b32 v99, v125\n\t"
    "buffer_store_dwordx4 v[72:75], v66, %12, s49 offen offset:64 sc1\n\t"
    "v_mov_b32 v129, 0xffff0000\n\t"
    "v_perm_b32 v100, v100, v76, v126\n\t"
    "v_perm_b32 v101, v101, v77, v126\n\t"
    "v_perm_b32 v102, v102, v78, v126\n\t"
    "v_perm_b32 v103, v103, v79, v126\n\t"
    "v_perm_b32 v104, v104, v80, v126\n\t"
    "v_perm_b32 v105, v105, v81, v126\n\t"
    "v_perm_b32 v106, v106, v82, v126\n\t"
    "v_perm_b32 v107, v107, v83, v126\n\t"
    "v_max_u32 v108, v100, v101\n\t"
    "v_min_u32 v101, v100, v101\n\t"
    "v_max_u32 v100, v102, v103\n\t"
    "v_min_u32 v103, v102, v103\n\t"
    "v_max_u32 v102, v104, v105\n\t"
    "v_min_u32 v105, v104, v105\n\t"
    "v_max_u32 v104, v106, v107\n\t"
    "v_min_u32 v107, v106, v107\n\t"
    "v_max_u32 v106, v108, v100\n\t"
    "v_min_u32 v100, v108, v100\n\t"
    "v_max_u32 v108, v101, v103\n\t"
    "v_min_u32 v103, v101, v103\n\t"
    "v_max_u32 v101, v102, v104\n\t"
    "v_min_u32 v104, v102, v104\n\t"
    "v_max_u32 v102, v105, v107\n\t"
    "v_min_u32 v107, v105, v107\n\t"
    "v_bfi_b32 v130, v106, 0, v129\n\t"
    "v_cmp_eq_u32 vcc, 0, v130\n\t"
    "v_and_or_b32 v92, v106, s44, v68\n\t"
    "s_nop 0\n\t"
    "v_cndmask_b32 v130, v130, v129, vcc\n\t"
    "v_ffbl_b32 v131, v130\n\t"
    "v_and_b32 v134, 7, v131\n\t"
    "v_lshlrev_b32 v134, v134, s45\n\t"
    "v_xor_b32 v129, v129, v134\n\t"
    "v_xor_b32 v132, v131, v127\n\t"
    "v_bfi_b32 v130, v101, 0, v129\n\t"
    "v_cmp_eq_u32 vcc, 0, v130\n\t"
    "v_and_or_b32 v93, v101, s44, v68\n\t"
    "v_xor_b32 v133, 23, v132\n\t"
    "v_lshlrev_b32 v110, v132, v136\n\t"
    "v_and_b32 v132, 7, v132\n\t"
    "v_lshl_or_b32 v110, v136, v133, v110\n\t"
    "v_lshl_add_u32 v132, v132, 1, v128\n\t"
    "ds_or_rtn_b32 v118, v92, v110\n\t"
    "ds_write_b16 v132, v106\n\t"
    "v_cndmask_b32 v130, v130, v129, vcc\n\t"
    "v_ffbl_b32 v131, v130\n\t"
    "v_and_b32 v134, 7, v131\n\t"
    "v_lshlrev_b32 v134, v134, s45\n\t"
    "v_xor_b32 v129, v129, v134\n\t"
    "v_xor_b32 v132, v131, v127\n\t"
    "v_bfi_b32 v130, v108, 0, v129\n\t"
    "v_cmp_eq_u32 vcc, 0, v130\n\t"
    "v_and_or_b32 v94, v108, s44, v68\n\t"
    "v_xor_b32 v133, 23, v132\n\t"
    "v_lshlrev_b32 v111, v132, v136\n\t"
    "v_and_b32 v132, 7, v132\n\t"
    "v_lshl_or_b32 v111, v136, v133, v111\n\t"
    "v_lshl_add_u32 v132, v132, 1, v128\n\t"
    "ds_or_rtn_b32 v119, v93, v111\n\t"
    "ds_write_b16 v132, v101\n\t"
    "v_cndmask_b32 v130, v130, v129, vcc\n\t"
    "v_ffbl_b32 v131, v130\n\t"
    "v_and_b32 v134, 7, v131\n\t"
    "v_lshlrev_b32 v134, v134, s45\n\t"
    "v_xor_b32 v129, v129, v134\n\t"
    "v_xor_b32 v132, v131, v127\n\t"
    "v_bfi_b32 v130, v100, 0, v129\n\t"
    "v_cmp_eq_u32 vcc, 0, v130\n\t"
    "v_and_or_b32 v95, v100, s44, v68\n\t"
    "v_xor_b32 v133, 23, v132\n\t"
    "v_lshlrev_b32 v112, v132, v136\n\t"
    "v_and_b32 v132, 7, v132\n\t"
    "v_lshl_or_b32 v112, v136, v133, v112\n\t"
    "v_lshl_add_u32 v132, v132, 1, v128\n\t"
    "ds_or_rtn_b32 v120, v94, v112\n\t"
    "ds_write_b16 v132, v108\n\t"
    "v_cndmask_b32 v130, v130, v129, vcc\n\t"
    "v_ffbl_b32 v131, v130\n\t"
    "v_and_b32 v134, 7, v131\n\t"
    "v_lshlrev_b32 v134, v134, s45\n\t"
    "v_xor_b32 v129, v129, v134\n\t"
    "v_xor_b32 v132, v131, v127\n\t"
    "v_bfi_b32 v130, v102, 0, v129\n\t"
    "v_cmp_eq_u32 vcc, 0, v130\n\t"
    "v_and_or_b32 v96, v102, s44, v68\n\t"
    "v_xor_b32 v133, 23, v132\n\t"
    "v_lshlrev_b32 v113, v132, v136\n\t"
    "v_and_b32 v132, 7, v132\n\t"
    "v_lshl_or_b32 v113, v136, v133, v113\n\t"
    "v_lshl_add_u32 v132, v132, 1, v128\n\t"
    "ds_or_rtn_b32 v121, v95, v113\n\t"
    "ds_write_b16 v132, v100\n\t"
    "v_cndmask_b32 v130, v130, v129, vcc\n\t"
    "v_ffbl_b32 v131, v130\n\t"
    "v_and_b32 v134, 7, v131\n\t"
    "v_lshlrev_b32 v134, v134, s45\n\t"
    "v_xor_b32 v129, v129, v134\n\t"
    "v_xor_b32 v132, v131, v127\n\t"
    "s_waitcnt lgkmcnt(7)\n\t"
    "v_and_b32 v118, v118, v110\n\t"
    "v_perm_b32 v118, v118, v118, s46\n\t"
    "ds_or_b32 v92, v118\n\t"
    "v_bfi_b32 v130, v104, 0, v129\n\t"
    "v_cmp_eq_u32 vcc, 0, v130\n\t"
    "v_and_or_b32 v97, v104, s44, v68\n\t"
    "v_xor_b32 v133, 23, v132\n\t"
    "v_lshlrev_b32 v114, v132, v136\n\t"
    "v_and_b32 v132, 7, v132\n\t"
    "v_lshl_or_b32 v114, v136, v133, v114\n\t"
    "v_lshl_add_u32 v132, v132, 1, v128\n\t"
    "ds_or_rtn_b32 v122, v96, v114\n\t"
    "ds_write_b16 v132, v102\n\t"
    "v_cndmask_b32 v130, v130, v129, vcc\n\t"
    "v_ffbl_b32 v131, v130\n\t"
    "v_and_b32 v134, 7, v131\n\t"
    "v_lshlrev_b32 v134, v134, s45\n\t"
    "v_xor_b32 v129, v129, v134\n\t"
    "v_xor_b32 v132, v131, v127\n\t"
    "s_waitcnt lgkmcnt(8)\n\t"
    "v_and_b32 v119, v119, v111\n\t"
    "v_perm_b32 v119, v119, v119, s46\n\t"
    "ds_or_b32 v93, v119\n\t"
    "v_bfi_b32 v130, v103, 0, v129\n\t"
    "v_cmp_eq_u32 vcc, 0, v130\n\t"
    "v_and_or_b32 v98, v103, s44, v68\n\t"
    "v_xor_b32 v133, 23, v132\n\t"
    "v_lshlrev_b32 v115, v132, v136\n\t"
    "v_and_b32 v132, 7, v132\n\t"
    "v_lshl_or_b32 v115, v136, v133, v115\n\t"
    "v_lshl_add_u32 v132, v132, 1, v128\n\t"
    "ds_or_rtn_b32 v123, v97, v115\n\t"
    "ds_write_b16 v132, v104\n\t"
    "v_cndmask_b32 v130, v130, v129, vcc\n\t"
    "v_ffbl_b32 v131, v130\n\t"
    "v_and_b32 v134, 7, v131\n\t"
    "v_lshlrev_b32 v134, v134, s45\n\t"
    "v_xor_b32 v129, v129, v134\n\t"
    "v_xor_b32 v132, v131, v127\n\t"
    "s_waitcnt lgkmcnt(9)\n\t"
    "v_and_b32 v120, v120, v112\n\t"
    "v_perm_b32 v120, v120, v120, s46\n\t"
    "ds_or_b32 v94, v120\n\t"
    "v_bfi_b32 v130, v107, 0, v129\n\t"
    "v_cmp_eq_u32 vcc, 0, v130\n\t"
    "v_and_or_b32 v99, v107, s44, v68\n\t"
    "v_xor_b32 v133, 23, v132\n\t"
    "v_lshlrev_b32 v116, v132, v136\n\t"
    "v_and_b32 v132, 7, v132\n\t"
    "v_lshl_or_b32 v116, v136, v133, v116\n\t"
    "v_lshl_add_u32 v132, v132, 1, v128\n\t"
    "ds_or_rtn_b32 v124, v98, v116\n\t"
    "ds_write_b16 v132, v103\n\t"
    "v_cndmask_b32 v130, v130, v129, vcc\n\t"
    "v_ffbl_b32 v131, v130\n\t"
    "v_xor_b32 v132, v131, v127\n\t"
    "s_waitcnt lgkmcnt(10)\n\t"
    "v_and_b32 v121, v121, v113\n\t"
    "v_perm_b32 v121, v121, v121, s46\n\t"
    "ds_or_b32 v95, v121\n\t"
    "v_xor_b32 v133, 23, v132\n\t"
    "v_lshlrev_b32 v117, v132, v136\n\t"
    "v_and_b32 v132, 7, v132\n\t"
    "v_lshl_or_b32 v117, v136, v133, v117\n\t"
    "v_lshl_add_u32 v132, v132, 1, v128\n\t"
    "ds_or_rtn_b32 v125, v99, v117\n\t"
    "ds_write_b16 v132, v107\n\t"
    "s_waitcnt lgkmcnt(10)\n\t"
    "v_and_b32 v122, v122, v114\n\t"
    "v_perm_b32 v122, v122, v122, s46\n\t"
    "ds_or_b32 v96, v122\n\t"
    "ds_read_b128 v[72:75], v128\n\t"
    "v_and_or_b32 v100, v84, s44, v68\n\t"
    "ds_read_b32 v100, v100\n\t"
    "v_and_or_b32 v101, v85, s44, v68\n\t"
    "ds_read_b32 v101, v101\n\t"
    "v_and_or_b32 v102, v86, s44, v68\n\t"
    "ds_read_b32 v102, v102\n\t"
    "v_and_or_b32 v103, v87, s44, v68\n\t"
    "ds_read_b32 v103, v103\n\t"
    "v_and_or_b32 v104, v88, s44, v68\n\t"
    "ds_read_b32 v104, v104\n\t"
    "v_and_or_b32 v105, v89, s44, v68\n\t"
    "ds_read_b32 v105, v105\n\t"
    "v_and_or_b32 v106, v90, s44, v68\n\t"
    "ds_read_b32 v106, v106\n\t"
    "v_and_or_b32 v107, v91, s44, v68\n\t"
    "ds_read_b32 v107, v107\n\t"
    "s_waitcnt vmcnt(27)\n\t"
    "v_lshl_add_u32 v76, v32, 3, s36\n\t"
    "v_lshl_add_u32 v77, v33, 3, s37\n\t"
    "v_lshl_add_u32 v78, v34, 3, s38\n\t"
    "v_lshl_add_u32 v79, v35, 3, s39\n\t"
    "v_lshl_add_u32 v80, v36, 3, s40\n\t"
    "v_lshl_add_u32 v81, v37, 3, s41\n\t"
    "v_lshl_add_u32 v82, v38, 3, s42\n\t"
    "v_lshl_add_u32 v83, v39, 3, s43\n\t"
    "s_movk_i32 s50, 9600\n\t"
    "buffer_load_dwordx4 v[16:19], v65, %0, s50 offen offset:2416\n\t"
    "buffer_load_dwordx4 v[20:23], v65, %0, s50 offen offset:2432\n\t"
    "buffer_load_dwordx4 v[220:223], v137, s[96:99], s47 offen offset:0\n\t"
    "buffer_load_dwordx4 v[224:227], v138, s[96:99], s47 offen offset:0\n\t"
    "buffer_load_dwordx4 v[228:231], v137, s[96:99], s47 offen offset:64\n\t"
    "buffer_load_dwordx4 v[232:235], v138, s[96:99], s47 offen offset:64\n\t"
    "buffer_load_dwordx4 v[236:239], v137, s[96:99], s47 offen offset:128\n\t"
    "buffer_load_dwordx4 v[240:243], v138, s[96:99], s47 offen offset:128\n\t"
    "buffer_load_dwordx4 v[244:247], v137, s[96:99], s47 offen offset:192\n\t"
    "buffer_load_dwordx4 v[248:251], v138, s[96:99], s47 offen offset:192\n\t"
    "s_add_u32 s47, s47, 2097152\n\t"
    "buffer_load_dwordx4 v[24:27], v137, s[96:99], s47 offen offset:0\n\t"
    "buffer_load_dwordx4 v[28:31], v138, s[96:99], s47 offen offset:0\n\t"
    "buffer_load_dwordx4 v[32:35], v137, s[96:99], s47 offen offset:64\n\t"
    "buffer_load_dwordx4 v[36:39], v138, s[96:99], s47 offen offset:64\n\t"
    "s_waitcnt lgkmcnt(11)\n\t"
    "v_and_b32 v123, v123, v115\n\t"
    "v_perm_b32 v123, v123, v123, s46\n\t"
    "ds_or_b32 v97, v123\n\t"
    "v_and_b32 v124, v124, v116\n\t"
    "v_perm_b32 v124, v124, v124, s46\n\t"
    "ds_or_b32 v98, v124\n\t"
    "v_and_b32 v125, v125, v117\n\t"
    "v_perm_b32 v125, v125, v125, s46\n\t"
    "ds_or_b32 v99, v125\n\t"
    "s_waitcnt lgkmcnt(11)\n\t"
    "buffer_store_dwordx4 v[72:75], v66, %12, s49 offen offset:128 sc1\n\t"
    "v_mov_b32 v129, 0xffff0000\n\t"
    "s_waitcnt lgkmcnt(7)\n\t"
    "v_perm_b32 v100, v100, v84, v126\n\t"
    "v_perm_b32 v101, v101, v85, v126\n\t"
    "v_perm_b32 v102, v102, v86, v126\n\t"
    "v_perm_b32 v103, v103, v87, v126\n\t"
    "s_waitcnt lgkmcnt(3)\n\t"
    "v_perm_b32 v104, v104, v88, v126\n\t"
    "v_perm_b32 v105, v105, v89, v126\n\t"
    "v_perm_b32 v106, v106, v90, v126\n\t"
    "v_perm_b32 v107, v107, v91, v126\n\t"
    "v_max_u32 v108, v100, v101\n\t"
    "v_min_u32 v101, v100, v101\n\t"
    "v_max_u32 v100, v102, v103\n\t"
    "v_min_u32 v103, v102, v103\n\t"
    "v_max_u32 v102, v104, v105\n\t"
    "v_min_u32 v105, v104, v105\n\t"
    "v_max_u32 v104, v106, v107\n\t"
    "v_min_u32 v107, v106, v107\n\t"
    "v_max_u32 v106, v108, v100\n\t"
    "v_min_u32 v100, v108, v100\n\t"
    "v_max_u32 v108, v101, v103\n\t"
    "v_min_u32 v103, v101, v103\n\t"
    "v_max_u32 v101, v102, v104\n\t"
    "v_min_u32 v104, v102, v104\n\t"
    "v_max_u32 v102, v105, v107\n\t"
    "v_min_u32 v107, v105, v107\n\t"
    "v_bfi_b32 v130, v106, 0, v129\n\t"
    "v_cmp_eq_u32 vcc, 0, v130\n\t"
    "v_and_or_b32 v92, v106, s44, v68\n\t"
    "s_nop 0\n\t"
    "v_cndmask_b32 v130, v130, v129, vcc\n\t"
    "v_ffbl_b32 v131, v130\n\t"
    "v_and_b32 v134, 7, v131\n\t"
    "v_lshlrev_b32 v134, v134, s45\n\t"
    "v_xor_b32 v129, v129, v134\n\t"
    "v_xor_b32 v132, v131, v127\n\t"
    "v_bfi_b32 v130, v101, 0, v129\n\t"
    "v_cmp_eq_u32 vcc, 0, v130\n\t"
    "v_and_or_b32 v93, v101, s44, v68\n\t"
    "v_xor_b32 v133, 23, v132\n\t"
    "v_lshlrev_b32 v110, v132, v136\n\t"
    "v_and_b32 v132, 7, v132\n\t"
    "v_lshl_or_b32 v110, v136, v133, v110\n\t"
    "v_lshl_add_u32 v132, v132, 1, v128\n\t"
    "ds_or_rtn_b32 v118, v92, v110\n\t"
    "ds_write_b16 v132, v106\n\t"
    "v_cndmask_b32 v130, v130, v129, vcc\n\t"
    "v_ffbl_b32 v131, v130\n\t"
    "v_and_b32 v134, 7, v131\n\t"
    "v_lshlrev_b32 v134, v134, s45\n\t"
    "v_xor_b32 v129, v129, v134\n\t"
    "v_xor_b32 v132, v131, v127\n\t"
    "v_bfi_b32 v130, v108, 0, v129\n\t"
    "v_cmp_eq_u32 vcc, 0, v130\n\t"
    "v_and_or_b32 v94, v108, s44, v68\n\t"
    "v_xor_b32 v133, 23, v132\n\t"
    "v_lshlrev_b32 v111, v132, v136\n\t"
    "v_and_b32 v132, 7, v132\n\t"
    "v_lshl_or_b32 v111, v136, v133, v111\n\t"
    "v_lshl_add_u32 v132, v132, 1, v128\n\t"
    "ds_or_rtn_b32 v119, v93, v111\n\t"
    "ds_write_b16 v132, v101\n\t"
    "v_cndmask_b32 v130, v130, v129, vcc\n\t"
    "v_ffbl_b32 v131, v130\n\t"
    "v_and_b32 v134, 7, v131\n\t"
    "v_lshlrev_b32 v134, v134, s45\n\t"
    "v_xor_b32 v129, v129, v134\n\t"
    "v_xor_b32 v132, v131, v127\n\t"
    "v_bfi_b32 v130, v100, 0, v129\n\t"
    "v_cmp_eq_u32 vcc, 0, v130\n\t"
    "v_and_or_b32 v95, v100, s44, v68\n\t"
    "v_xor_b32 v133, 23, v132\n\t"
    "v_lshlrev_b32 v112, v132, v136\n\t"
    "v_and_b32 v132, 7, v132\n\t"
    "v_lshl_or_b32 v112, v136, v133, v112\n\t"
    "v_lshl_add_u32 v132, v132, 1, v128\n\t"
    "ds_or_rtn_b32 v120, v94, v112\n\t"
    "ds_write_b16 v132, v108\n\t"
    "v_cndmask_b32 v130, v130, v129, vcc\n\t"
    "v_ffbl_b32 v131, v130\n\t"
    "v_and_b32 v134, 7, v131\n\t"
    "v_lshlrev_b32 v134, v134, s45\n\t"
    "v_xor_b32 v129, v129, v134\n\t"
    "v_xor_b32 v132, v131, v127\n\t"
    "v_bfi_b32 v130, v102, 0, v129\n\t"
    "v_cmp_eq_u32 vcc, 0, v130\n\t"
    "v_and_or_b32 v96, v102, s44, v68\n\t"
    "v_xor_b32 v133, 23, v132\n\t"
    "v_lshlrev_b32 v113, v132, v136\n\t"
    "v_and_b32 v132, 7, v132\n\t"
    "v_lshl_or_b32 v113, v136, v133, v113\n\t"
    "v_lshl_add_u32 v132, v132, 1, v128\n\t"
    "ds_or_rtn_b32 v121, v95, v113\n\t"
    "ds_write_b16 v132, v100\n\t"
    "v_cndmask_b32 v130, v130, v129, vcc\n\t"
    "v_ffbl_b32 v131, v130\n\t"
    "v_and_b32 v134, 7, v131\n\t"
    "v_lshlrev_b32 v134, v134, s45\n\t"
    "v_xor_b32 v129, v129, v134\n\t"
    "v_xor_b32 v132, v131, v127\n\t"
    "s_waitcnt lgkmcnt(7)\n\t"
    "v_and_b32 v118, v118, v110\n\t"
    "v_perm_b32 v118, v118, v118, s46\n\t"
    "ds_or_b32 v92, v118\n\t"
    "v_bfi_b32 v130, v104, 0, v129\n\t"
    "v_cmp_eq_u32 vcc, 0, v130\n\t"
    "v_and_or_b32 v97, v104, s44, v68\n\t"
    "v_xor_b32 v133, 23, v132\n\t"
    "v_lshlrev_b32 v114, v132, v136\n\t"
    "v_and_b32 v132, 7, v132\n\t"
    "v_lshl_or_b32 v114, v136, v133, v114\n\t"
    "v_lshl_add_u32 v132, v132, 1, v128\n\t"
    "ds_or_rtn_b32 v122, v96, v114\n\t"
    "ds_write_b16 v132, v102\n\t"
    "v_cndmask_b32 v130, v130, v129, vcc\n\t"
    "v_ffbl_b32 v131, v130\n\t"
    "v_and_b32 v134, 7, v131\n\t"
    "v_lshlrev_b32 v134, v134, s45\n\t"
    "v_xor_b32 v129, v129, v134\n\t"
    "v_xor_b32 v132, v131, v127\n\t"
    "s_waitcnt lgkmcnt(8)\n\t"
    "v_and_b32 v119, v119, v111\n\t"
    "v_perm_b32 v119, v119, v119, s46\n\t"
    "ds_or_b32 v93, v119\n\t"
    "v_bfi_b32 v130, v103, 0, v129\n\t"
    "v_cmp_eq_u32 vcc, 0, v130\n\t"
    "v_and_or_b32 v98, v103, s44, v68\n\t"
    "v_xor_b32 v133, 23, v132\n\t"
    "v_lshlrev_b32 v115, v132, v136\n\t"
    "v_and_b32 v132, 7, v132\n\t"
    "v_lshl_or_b32 v115, v136, v133, v115\n\t"
    "v_lshl_add_u32 v132, v132, 1, v128\n\t"
    "ds_or_rtn_b32 v123, v97, v115\n\t"
    "ds_write_b16 v132, v104\n\t"
    "v_cndmask_b32 v130, v130, v129, vcc\n\t"
    "v_ffbl_b32 v131, v130\n\t"
    "v_and_b32 v134, 7, v131\n\t"
    "v_lshlrev_b32 v134, v134, s45\n\t"
    "v_xor_b32 v129, v129, v134\n\t"
    "v_xor_b32 v132, v131, v127\n\t"
    "s_waitcnt lgkmcnt(9)\n\t"
    "v_and_b32 v120, v120, v112\n\t"
    "v_perm_b32 v120, v120, v120, s46\n\t"
    "ds_or_b32 v94, v120\n\t"
    "v_bfi_b32 v130, v107, 0, v129\n\t"
    "v_cmp_eq_u32 vcc, 0, v130\n\t"
    "v_and_or_b32 v99, v107, s44, v68\n\t"
    "v_xor_b32 v133, 23, v132\n\t"
    "v_lshlrev_b32 v116, v132, v136\n\t"
    "v_and_b32 v132, 7, v132\n\t"
    "v_lshl_or_b32 v116, v136, v133, v116\n\t"
    "v_lshl_add_u32 v132, v132, 1, v128\n\t"
    "ds_or_rtn_b32 v124, v98, v116\n\t"
    "ds_write_b16 v132, v103\n\t"
    "v_cndmask_b32 v130, v130, v129, vcc\n\t"
    "v_ffbl_b32 v131, v130\n\t"
    "v_xor_b32 v132, v131, v127\n\t"
    "s_waitcnt lgkmcnt(10)\n\t"
    "v_and_b32 v121, v121, v113\n\t"
    "v_perm_b32 v121, v121, v121, s46\n\t"
    "ds_or_b32 v95, v121\n\t"
    "v_xor_b32 v133, 23, v132\n\t"
    "v_lshlrev_b32 v117, v132, v136\n\t"
    "v_and_b32 v132, 7, v132\n\t"
    "v_lshl_or_b32 v117, v136, v133, v117\n\t"
    "v_lshl_add_u32 v132, v132, 1, v128\n\t"
    "ds_or_rtn_b32 v125, v99, v117\n\t"
    "ds_write_b16 v132, v107\n\t"
    "s_waitcnt lgkmcnt(10)\n\t"
    "v_and_b32 v122, v122, v114\n\t"
    "v_perm_b32 v122, v122, v122, s46\n\t"
    "ds_or_b32 v96, v122\n\t"
    "ds_read_b128 v[72:75], v128\n\t"
    "v_and_or_b32 v100, v76, s44, v68\n\t"
    "ds_read_b32 v100, v100\n\t"
    "v_and_or_b32 v101, v77, s44, v68\n\t"
    "ds_read_b32 v101, v101\n\t"
    "v_and_or_b32 v102, v78, s44, v68\n\t"
    "ds_read_b32 v102, v102\n\t"
    "v_and_or_b32 v103, v79, s44, v68\n\t"
    "ds_read_b32 v103, v103\n\t"
    "v_and_or_b32 v104, v80, s44, v68\n\t"
    "ds_read_b32 v104, v104\n\t"
    "v_and_or_b32 v105, v81, s44, v68\n\t"
    "ds_read_b32 v105, v105\n\t"
    "v_and_or_b32 v106, v82, s44, v68\n\t"
    "ds_read_b32 v106, v106\n\t"
    "v_and_or_b32 v107, v83, s44, v68\n\t"
    "ds_read_b32 v107, v107\n\t"
    "s_waitcnt vmcnt(40)\n\t"
    "v_lshl_add_u32 v84, v40, 3, s36\n\t"
    "v_lshl_add_u32 v85, v41, 3, s37\n\t"
    "v_lshl_add_u32 v86, v42, 3, s38\n\t"
    "v_lshl_add_u32 v87, v43, 3, s39\n\t"
    "v_lshl_add_u32 v88, v44, 3, s40\n\t"
    "v_lshl_add_u32 v89, v45, 3, s41\n\t"
    "v_lshl_add_u32 v90, v46, 3, s42\n\t"
    "v_lshl_add_u32 v91, v47, 3, s43\n\t"
    "buffer_load_dwordx4 v[40:43], v137, s[96:99], s47 offen offset:128\n\t"
    "buffer_load_dwordx4 v[44:47], v138, s[96:99], s47 offen offset:128\n\t"
    "s_waitcnt lgkmcnt(11)\n\t"
    "v_and_b32 v123, v123, v115\n\t"
    "v_perm_b32 v123, v123, v123, s46\n\t"
    "ds_or_b32 v97, v123\n\t"
    "v_and_b32 v124, v124, v116\n\t"
    "v_perm_b32 v124, v124, v124, s46\n\t"
    "ds_or_b32 v98, v124\n\t"
    "v_and_b32 v125, v125, v117\n\t"
    "v_perm_b32 v125, v125, v125, s46\n\t"
    "ds_or_b32 v99, v125\n\t"
    "s_waitcnt lgkmcnt(11)\n\t"
    "buffer_store_dwordx4 v[72:75], v66, %12, s49 offen offset:192 sc1\n\t"
    "v_mov_b32 v129, 0xffff0000\n\t"
    "s_waitcnt lgkmcnt(7)\n\t"
    "v_perm_b32 v100, v100, v76, v126\n\t"
    "v_perm_b32 v101, v101, v77, v126\n\t"
    "v_perm_b32 v102, v102, v78, v126\n\t"
    "v_perm_b32 v103, v103, v79, v126\n\t"
    "s_waitcnt lgkmcnt(3)\n\t"
    "v_perm_b32 v104, v104, v80, v126\n\t"
    "v_perm_b32 v105, v105, v81, v126\n\t"
    "v_perm_b32 v106, v106, v82, v126\n\t"
    "v_perm_b32 v107, v107, v83, v126\n\t"
    "s_waitcnt vmcnt(33)\n\t"
    "ds_write_b128 v141, v[156:159] offset:0\n\t"
    "ds_write_b128 v141, v[160:163] offset:256\n\t"
    "ds_read_b64_tr_b16 v[156:157], v142 offset:0\n\t"
    "ds_read_b64_tr_b16 v[158:159], v142 offset:256\n\t"
    "ds_read_b64_tr_b16 v[160:161], v142 offset:32\n\t"
    "ds_read_b64_tr_b16 v[162:163], v142 offset:288\n\t"
    "s_waitcnt vmcnt(31)\n\t"
    "ds_write_b128 v141, v[164:167] offset:2112\n\t"
    "ds_write_b128 v141, v[168:171] offset:2368\n\t"
    "ds_read_b64_tr_b16 v[164:165], v142 offset:2112\n\t"
    "ds_read_b64_tr_b16 v[166:167], v142 offset:2368\n\t"
    "ds_read_b64_tr_b16 v[168:169], v142 offset:2144\n\t"
    "ds_read_b64_tr_b16 v[170:171], v142 offset:2400\n\t"
    "v_max_u32 v108, v100, v101\n\t"
    "v_min_u32 v101, v100, v101\n\t"
    "v_max_u32 v100, v102, v103\n\t"
    "v_min_u32 v103, v102, v103\n\t"
    "v_max_u32 v102, v104, v105\n\t"
    "v_min_u32 v105, v104, v105\n\t"
    "v_max_u32 v104, v106, v107\n\t"
    "v_min_u32 v107, v106, v107\n\t"
    "v_max_u32 v106, v108, v100\n\t"
    "v_min_u32 v100, v108, v100\n\t"
    "v_max_u32 v108, v101, v103\n\t"
    "v_min_u32 v103, v101, v103\n\t"
    "v_max_u32 v101, v102, v104\n\t"
    "v_min_u32 v104, v102, v104\n\t"
    "v_max_u32 v102, v105, v107\n\t"
    "v_min_u32 v107, v105, v107\n\t"
    "v_bfi_b32 v130, v106, 0, v129\n\t"
    "v_cmp_eq_u32 vcc, 0, v130\n\t"
    "v_and_or_b32 v92, v106, s44, v68\n\t"
    "s_nop 0\n\t"
    "v_cndmask_b32 v130, v130, v129, vcc\n\t"
    "v_ffbl_b32 v131, v130\n\t"
    "v_and_b32 v134, 7, v131\n\t"
    "v_lshlrev_b32 v134, v134, s45\n\t"
    "v_xor_b32 v129, v129, v134\n\t"
    "v_xor_b32 v132, v131, v127\n\t"
    "v_bfi_b32 v130, v101, 0, v129\n\t"
    "v_cmp_eq_u32 vcc, 0, v130\n\t"
    "v_and_or_b32 v93, v101, s44, v68\n\t"
    "v_xor_b32 v133, 23, v132\n\t"
    "v_lshlrev_b32 v110, v132, v136\n\t"
    "v_and_b32 v132, 7, v132\n\t"
    "v_lshl_or_b32 v110, v136, v133, v110\n\t"
    "v_lshl_add_u32 v132, v132, 1, v128\n\t"
    "ds_or_rtn_b32 v118, v92, v110\n\t"
    "ds_write_b16 v132, v106\n\t"
    "v_cndmask_b32 v130, v130, v129, vcc\n\t"
    "v_ffbl_b32 v131, v130\n\t"
    "v_and_b32 v134, 7, v131\n\t"
    "v_lshlrev_b32 v134, v134, s45\n\t"
    "v_xor_b32 v129, v129, v134\n\t"
    "v_xor_b32 v132, v131, v127\n\t"
    "s_waitcnt lgkmcnt(8)\n\t"
    "buffer_store_dwordx4 v[156:159], v139, %12, s48 offen sc1\n\t"
    "buffer_store_dwordx4 v[160:163], v140, %12, s48 offen sc1\n\t"
    "s_add_u32 s48, s48, 819200\n\t"
    "s_waitcnt lgkmcnt(2)\n\t"
    "buffer_store_dwordx4 v[164:167], v139, %12, s48 offen sc1\n\t"
    "buffer_store_dwordx4 v[168:171], v140, %12, s48 offen sc1\n\t"
    "s_add_u32 s48, s48, 819200\n\t"
    "s_waitcnt vmcnt(33)\n\t"
    "ds_write_b128 v141, v[172:175] offset:0\n\t"
    "ds_write_b128 v141, v[176:179] offset:256\n\t"
    "ds_read_b64_tr_b16 v[172:173], v142 offset:0\n\t"
    "ds_read_b64_tr_b16 v[174:175], v142 offset:256\n\t"
    "ds_read_b64_tr_b16 v[176:177], v142 offset:32\n\t"
    "ds_read_b64_tr_b16 v[178:179], v142 offset:288\n\t"
    "s_waitcnt vmcnt(31)\n\t"
    "ds_write_b128 v141, v[180:183] offset:2112\n\t"
    "ds_write_b128 v141, v[184:187] offset:2368\n\t"
    "ds_read_b64_tr_b16 v[180:181], v142 offset:2112\n\t"
    "ds_read_b64_tr_b16 v[182:183], v142 offset:2368\n\t"
    "ds_read_b64_tr_b16 v[184:185], v142 offset:2144\n\t"
    "ds_read_b64_tr_b16 v[186:187], v142 offset:2400\n\t"
    "v_bfi_b32 v130, v108, 0, v129\n\t"
    "v_cmp_eq_u32 vcc, 0, v130\n\t"
    "v_and_or_b32 v94, v108, s44, v68\n\t"
    "v_xor_b32 v133, 23, v132\n\t"
    "v_lshlrev_b32 v111, v132, v136\n\t"
    "v_and_b32 v132, 7, v132\n\t"
    "v_lshl_or_b32 v111, v136, v133, v111\n\t"
    "v_lshl_add_u32 v132, v132, 1, v128\n\t"
    "ds_or_rtn_b32 v119, v93, v111\n\t"
    "ds_write_b16 v132, v101\n\t"
    "v_cndmask_b32 v130, v130, v129, vcc\n\t"
    "v_ffbl_b32 v131, v130\n\t"
    "v_and_b32 v134, 7, v131\n\t"
    "v_lshlrev_b32 v134, v134, s45\n\t"
    "v_xor_b32 v129, v129, v134\n\t"
    "v_xor_b32 v132, v131, v127\n\t"
    "v_bfi_b32 v130, v100, 0, v129\n\t"
    "v_cmp_eq_u32 vcc, 0, v130\n\t"
    "v_and_or_b32 v95, v100, s44, v68\n\t"
    "v_xor_b32 v133, 23, v132\n\t"
    "v_lshlrev_b32 v112, v132, v136\n\t"
    "v_and_b32 v132, 7, v132\n\t"
    "v_lshl_or_b32 v112, v136, v133, v112\n\t"
    "v_lshl_add_u32 v132, v132, 1, v128\n\t"
    "ds_or_rtn_b32 v120, v94, v112\n\t"
    "ds_write_b16 v132, v108\n\t"
    "v_cndmask_b32 v130, v130, v129, vcc\n\t"
    "v_ffbl_b32 v131, v130\n\t"
    "v_and_b32 v134, 7, v131\n\t"
    "v_lshlrev_b32 v134, v134, s45\n\t"
    "v_xor_b32 v129, v129, v134\n\t"
    "v_xor_b32 v132, v131, v127\n\t"
    "s_waitcnt lgkmcnt(10)\n\t"
    "buffer_store_dwordx4 v[172:175], v139, %12, s48 offen sc1\n\t"
    "buffer_store_dwordx4 v[176:179], v140, %12, s48 offen sc1\n\t"
    "s_add_u32 s48, s48, 819200\n\t"
    "s_waitcnt lgkmcnt(4)\n\t"
    "buffer_store_dwordx4 v[180:183], v139, %12, s48 offen sc1\n\t"
    "buffer_store_dwordx4 v[184:187], v140, %12, s48 offen sc1\n\t"
    "s_add_u32 s48, s48, -2441216\n\t"
    "s_waitcnt vmcnt(33)\n\t"
    "ds_write_b128 v141, v[188:191] offset:0\n\t"
    "ds_write_b128 v141, v[192:195] offset:256\n\t"
    "ds_read_b64_tr_b16 v[188:189], v142 offset:0\n\t"
    "ds_read_b64_tr_b16 v[190:191], v142 offset:256\n\t"
    "ds_read_b64_tr_b16 v[192:193], v142 offset:32\n\t"
    "ds_read_b64_tr_b16 v[194:195], v142 offset:288\n\t"
    "s_waitcnt vmcnt(31)\n\t"
    "ds_write_b128 v141, v[196:199] offset:2112\n\t"
    "ds_write_b128 v141, v[200:203] offset:2368\n\t"
    "ds_read_b64_tr_b16 v[196:197], v142 offset:2112\n\t"
    "ds_read_b64_tr_b16 v[198:199], v142 offset:2368\n\t"
    "ds_read_b64_tr_b16 v[200:201], v142 offset:2144\n\t"
    "ds_read_b64_tr_b16 v[202:203], v142 offset:2400\n\t"
    "v_bfi_b32 v130, v102, 0, v129\n\t"
    "v_cmp_eq_u32 vcc, 0, v130\n\t"
    "v_and_or_b32 v96, v102, s44, v68\n\t"
    "v_xor_b32 v133, 23, v132\n\t"
    "v_lshlrev_b32 v113, v132, v136\n\t"
    "v_and_b32 v132, 7, v132\n\t"
    "v_lshl_or_b32 v113, v136, v133, v113\n\t"
    "v_lshl_add_u32 v132, v132, 1, v128\n\t"
    "ds_or_rtn_b32 v121, v95, v113\n\t"
    "ds_write_b16 v132, v100\n\t"
    "v_cndmask_b32 v130, v130, v129, vcc\n\t"
    "v_ffbl_b32 v131, v130\n\t"
    "v_and_b32 v134, 7, v131\n\t"
    "v_lshlrev_b32 v134, v134, s45\n\t"
    "v_xor_b32 v129, v129, v134\n\t"
    "v_xor_b32 v132, v131, v127\n\t"
    "v_and_b32 v118, v118, v110\n\t"
    "v_perm_b32 v118, v118, v118, s46\n\t"
    "ds_or_b32 v92, v118\n\t"
    "s_lshl_b32 s52, %14, 2\n\t"
    "s_add_u32 s52, s52, 49291264\n\t"
    "buffer_load_dword v71, off, %12, s52 offset:1024 sc1\n\t"
    "v_bfi_b32 v130, v104, 0, v129\n\t"
    "v_cmp_eq_u32 vcc, 0, v130\n\t"
    "v_and_or_b32 v97, v104, s44, v68\n\t"
    "v_xor_b32 v133, 23, v132\n\t"
    "v_lshlrev_b32 v114, v132, v136\n\t"
    "v_and_b32 v132, 7, v132\n\t"
    "v_lshl_or_b32 v114, v136, v133, v114\n\t"
    "v_lshl_add_u32 v132, v132, 1, v128\n\t"
    "ds_or_rtn_b32 v122, v96, v114\n\t"
    "ds_write_b16 v132, v102\n\t"
    "v_cndmask_b32 v130, v130, v129, vcc\n\t"
    "v_ffbl_b32 v131, v130\n\t"
    "v_and_b32 v134, 7, v131\n\t"
    "v_lshlrev_b32 v134, v134, s45\n\t"
    "v_xor_b32 v129, v129, v134\n\t"
    "v_xor_b32 v132, v131, v127\n\t"
    "s_waitcnt lgkmcnt(15)\n\t"
    "v_and_b32 v119, v119, v111\n\t"
    "v_perm_b32 v119, v119, v119, s46\n\t"
    "ds_or_b32 v93, v119\n\t"
    "s_waitcnt lgkmcnt(12)\n\t"
    "buffer_store_dwordx4 v[188:191], v139, %12, s48 offen sc1\n\t"
    "buffer_store_dwordx4 v[192:195], v140, %12, s48 offen sc1\n\t"
    "s_add_u32 s48, s48, 819200\n\t"
    "s_waitcnt lgkmcnt(6)\n\t"
    "buffer_store_dwordx4 v[196:199], v139, %12, s48 offen sc1\n\t"
    "buffer_store_dwordx4 v[200:203], v140, %12, s48 offen sc1\n\t"
    "s_add_u32 s48, s48, 819200\n\t"
    "s_waitcnt vmcnt(34)\n\t"
    "ds_write_b128 v141, v[204:207] offset:0\n\t"
    "ds_write_b128 v141, v[208:211] offset:256\n\t"
    "ds_read_b64_tr_b16 v[204:205], v142 offset:0\n\t"
    "ds_read_b64_tr_b16 v[206:207], v142 offset:256\n\t"
    "ds_read_b64_tr_b16 v[208:209], v142 offset:32\n\t"
    "ds_read_b64_tr_b16 v[210:211], v142 offset:288\n\t"
    "s_waitcnt vmcnt(32)\n\t"
    "ds_write_b128 v141, v[212:215] offset:2112\n\t"
    "ds_write_b128 v141, v[216:219] offset:2368\n\t"
    "ds_read_b64_tr_b16 v[212:213], v142 offset:2112\n\t"
    "ds_read_b64_tr_b16 v[214:215], v142 offset:2368\n\t"
    "ds_read_b64_tr_b16 v[216:217], v142 offset:2144\n\t"
    "ds_read_b64_tr_b16 v[218:219], v142 offset:2400\n\t"
    "v_bfi_b32 v130, v103, 0, v129\n\t"
    "v_cmp_eq_u32 vcc, 0, v130\n\t"
    "v_and_or_b32 v98, v103, s44, v68\n\t"
    "v_xor_b32 v133, 23, v132\n\t"
    "v_lshlrev_b32 v115, v132, v136\n\t"
    "v_and_b32 v132, 7, v132\n\t"
    "v_lshl_or_b32 v115, v136, v133, v115\n\t"
    "v_lshl_add_u32 v132, v132, 1, v128\n\t"
    "ds_or_rtn_b32 v123, v97, v115\n\t"
    "ds_write_b16 v132, v104\n\t"
    "v_cndmask_b32 v130, v130, v129, vcc\n\t"
    "v_ffbl_b32 v131, v130\n\t"
    "v_and_b32 v134, 7, v131\n\t"
    "v_lshlrev_b32 v134, v134, s45\n\t"
    "v_xor_b32 v129, v129, v134\n\t"
    "v_xor_b32 v132, v131, v127\n\t"
    "v_and_b32 v120, v120, v112\n\t"
    "v_perm_b32 v120, v120, v120, s46\n\t"
    "ds_or_b32 v94, v120\n\t"
    "v_bfi_b32 v130, v107, 0, v129\n\t"
    "v_cmp_eq_u32 vcc, 0, v130\n\t"
    "v_and_or_b32 v99, v107, s44, v68\n\t"
    "v_xor_b32 v133, 23, v132\n\t"
    "v_lshlrev_b32 v116, v132, v136\n\t"
    "v_and_b32 v132, 7, v132\n\t"
    "v_lshl_or_b32 v116, v136, v133, v116\n\t"
    "v_lshl_add_u32 v132, v132, 1, v128\n\t"
    "ds_or_rtn_b32 v124, v98, v116\n\t"
    "ds_write_b16 v132, v103\n\t"
    "v_cndmask_b32 v130, v130, v129, vcc\n\t"
    "v_ffbl_b32 v131, v130\n\t"
    "v_xor_b32 v132, v131, v127\n\t"
    "s_waitcnt lgkmcnt(15)\n\t"
    "v_and_b32 v121, v121, v113\n\t"
    "v_perm_b32 v121, v121, v121, s46\n\t"
    "ds_or_b32 v95, v121\n\t"
    "v_xor_b32 v133, 23, v132\n\t"
    "v_lshlrev_b32 v117, v132, v136\n\t"
    "v_and_b32 v132, 7, v132\n\t"
    "v_lshl_or_b32 v117, v136, v133, v117\n\t"
    "v_lshl_add_u32 v132, v132, 1, v128\n\t"
    "ds_or_rtn_b32 v125, v99, v117\n\t"
    "ds_write_b16 v132, v107\n\t"
    "v_and_b32 v122, v122, v114\n\t"
    "v_perm_b32 v122, v122, v122, s46\n\t"
    "ds_or_b32 v96, v122\n\t"
    "ds_read_b128 v[72:75], v128\n\t"
    "s_waitcnt lgkmcnt(15)\n\t"
    "buffer_store_dwordx4 v[204:207], v139, %12, s48 offen sc1\n\t"
    "buffer_store_dwordx4 v[208:211], v140, %12, s48 offen sc1\n\t"
    "s_add_u32 s48, s48, 819200\n\t"
    "s_waitcnt lgkmcnt(10)\n\t"
    "buffer_store_dwordx4 v[212:215], v139, %12, s48 offen sc1\n\t"
    "buffer_store_dwordx4 v[216:219], v140, %12, s48 offen sc1\n\t"
    "s_add_u32 s48, s48, -2441216\n\t"
    "v_mov_b32 v134, 1036\n\t"
    "L_asvsp12_%=:\n\t"
    "ds_read_b32 v130, v134\n\t"
    "s_waitcnt lgkmcnt(0)\n\t"
    "v_readfirstlane_b32 s50, v130\n\t"
    "s_nop 0\n\t"
    "s_cmp_lt_u32 s50, 1\n\t"
    "s_cbranch_scc1 L_asvsp12_%=\n\t"
    "v_and_or_b32 v100, v84, s44, v69\n\t"
    "ds_read_b32 v100, v100\n\t"
    "v_and_or_b32 v101, v85, s44, v69\n\t"
    "ds_read_b32 v101, v101\n\t"
    "v_and_or_b32 v102, v86, s44, v69\n\t"
    "ds_read_b32 v102, v102\n\t"
    "v_and_or_b32 v103, v87, s44, v69\n\t"
    "ds_read_b32 v103, v103\n\t"
    "v_and_or_b32 v104, v88, s44, v69\n\t"
    "ds_read_b32 v104, v104\n\t"
    "v_and_or_b32 v105, v89, s44, v69\n\t"
    "ds_read_b32 v105, v105\n\t"
    "v_and_or_b32 v106, v90, s44, v69\n\t"
    "ds_read_b32 v106, v106\n\t"
    "v_and_or_b32 v107, v91, s44, v69\n\t"
    "ds_read_b32 v107, v107\n\t"
    "v_lshl_add_u32 v76, v48, 3, s36\n\t"
    "v_lshl_add_u32 v77, v49, 3, s37\n\t"
    "v_lshl_add_u32 v78, v50, 3, s38\n\t"
    "v_lshl_add_u32 v79, v51, 3, s39\n\t"
    "v_lshl_add_u32 v80, v52, 3, s40\n\t"
    "v_lshl_add_u32 v81, v53, 3, s41\n\t"
    "v_lshl_add_u32 v82, v54, 3, s42\n\t"
    "v_lshl_add_u32 v83, v55, 3, s43\n\t"
    "v_mov_b32 v134, 2076\n\t"
    "L_asvsp1052_%=:\n\t"
    "ds_read_b32 v130, v134\n\t"
    "s_waitcnt lgkmcnt(0)\n\t"
    "v_readfirstlane_b32 s50, v130\n\t"
    "s_nop 0\n\t"
    "s_cmp_lt_u32 s50, 1\n\t"
    "s_cbranch_scc1 L_asvsp1052_%=\n\t"
    "v_mov_b32 v70, 2072\n\t"
    "ds_read_b32 v70, v70\n\t"
    "s_waitcnt vmcnt(8)\n\t"
    "s_waitcnt vmcnt(0)\n\t"
    "s_waitcnt lgkmcnt(0)\n\t"
    "v_cmp_ne_u32 vcc, v71, v70\n\t"
    "s_cbranch_vccz L_ahsok_%=\n\t"
    "s_mov_b32 s51, 1048576\n\t"
    "L_ahs_%=:\n\t"
    "buffer_load_dword v71, off, %12, s52 offset:1024 sc1\n\t"
    "s_sub_u32 s51, s51, 1\n\t"
    "s_waitcnt vmcnt(0)\n\t"
    "s_cmp_eq_u32 s51, 0\n\t"
    "s_cbranch_scc1 L_ahsok_%=\n\t"
    "v_cmp_ne_u32 vcc, v71, v70\n\t"
    "s_cbranch_vccnz L_ahs_%=\n\t"
    "L_ahsok_%=:\n\t"
    "s_lshr_b32 s50, %14, 5\n\t"
    "s_lshl_b32 s50, s50, 7\n\t"
    "s_add_u32 s50, s50, 49284096\n\t"
    "s_mov_b64 exec, 1\n\t"
    "buffer_atomic_add v136, off, %12, s50 sc1\n\t"
    "s_mov_b64 exec, -1\n\t"
    "buffer_load_dwordx4 v[156:159], v137, s[96:99], s47 offen offset:192\n\t"
    "buffer_load_dwordx4 v[160:163], v138, s[96:99], s47 offen offset:192\n\t"
    "s_add_u32 s47, s47, 2097152\n\t"
    "buffer_load_dwordx4 v[164:167], v137, s[96:99], s47 offen offset:0\n\t"
    "buffer_load_dwordx4 v[168:171], v138, s[96:99], s47 offen offset:0\n\t"
    "buffer_load_dwordx4 v[172:175], v137, s[96:99], s47 offen offset:64\n\t"
    "buffer_load_dwordx4 v[176:179], v138, s[96:99], s47 offen offset:64\n\t"
    "buffer_load_dwordx4 v[180:183], v137, s[96:99], s47 offen offset:128\n\t"
    "buffer_load_dwordx4 v[184:187], v138, s[96:99], s47 offen offset:128\n\t"
    "buffer_load_dwordx4 v[188:191], v137, s[96:99], s47 offen offset:192\n\t"
    "buffer_load_dwordx4 v[192:195], v138, s[96:99], s47 offen offset:192\n\t"
    "s_add_u32 s47, s47, 2097152\n\t"
    "buffer_load_dwordx4 v[196:199], v137, s[96:99], s47 offen offset:0\n\t"
    "buffer_load_dwordx4 v[200:203], v138, s[96:99], s47 offen offset:0\n\t"
    "v_and_b32 v123, v123, v115\n\t"
    "v_perm_b32 v123, v123, v123, s46\n\t"
    "ds_or_b32 v97, v123\n\t"
    "v_and_b32 v124, v124, v116\n\t"
    "v_perm_b32 v124, v124, v124, s46\n\t"
    "ds_or_b32 v98, v124\n\t"
    "v_and_b32 v125, v125, v117\n\t"
    "v_perm_b32 v125, v125, v125, s46\n\t"
    "ds_or_b32 v99, v125\n\t"
    "buffer_store_dwordx4 v[72:75], v66, %12, s49 offen offset:256 sc1\n\t"
    "v_mov_b32 v129, 0xffff0000\n\t"
    "v_perm_b32 v100, v100, v84, v126\n\t"
    "v_perm_b32 v101, v101, v85, v126\n\t"
    "v_perm_b32 v102, v102, v86, v126\n\t"
    "v_perm_b32 v103, v103, v87, v126\n\t"
    "v_perm_b32 v104, v104, v88, v126\n\t"
    "v_perm_b32 v105, v105, v89, v126\n\t"
    "v_perm_b32 v106, v106, v90, v126\n\t"
    "v_perm_b32 v107, v107, v91, v126\n\t"
    "ds_write_b128 v141, v[220:223] offset:0\n\t"
    "ds_write_b128 v141, v[224:227] offset:256\n\t"
    "ds_read_b64_tr_b16 v[220:221], v142 offset:0\n\t"
    "ds_read_b64_tr_b16 v[222:223], v142 offset:256\n\t"
    "ds_read_b64_tr_b16 v[224:225], v142 offset:32\n\t"
    "ds_read_b64_tr_b16 v[226:227], v142 offset:288\n\t"
    "ds_write_b128 v141, v[228:231] offset:2112\n\t"
    "ds_write_b128 v141, v[232:235] offset:2368\n\t"
    "ds_read_b64_tr_b16 v[228:229], v142 offset:2112\n\t"
    "ds_read_b64_tr_b16 v[230:231], v142 offset:2368\n\t"
    "ds_read_b64_tr_b16 v[232:233], v142 offset:2144\n\t"
    "ds_read_b64_tr_b16 v[234:235], v142 offset:2400\n\t"
    "v_max_u32 v108, v100, v101\n\t"
    "v_min_u32 v101, v100, v101\n\t"
    "v_max_u32 v100, v102, v103\n\t"
    "v_min_u32 v103, v102, v103\n\t"
    "v_max_u32 v102, v104, v105\n\t"
    "v_min_u32 v105, v104, v105\n\t"
    "v_max_u32 v104, v106, v107\n\t"
    "v_min_u32 v107, v106, v107\n\t"
    "v_max_u32 v106, v108, v100\n\t"
    "v_min_u32 v100, v108, v100\n\t"
    "v_max_u32 v108, v101, v103\n\t"
    "v_min_u32 v103, v101, v103\n\t"
    "v_max_u32 v101, v102, v104\n\t"
    "v_min_u32 v104, v102, v104\n\t"
    "v_max_u32 v102, v105, v107\n\t"
    "v_min_u32 v107, v105, v107\n\t"
    "v_bfi_b32 v130, v106, 0, v129\n\t"
    "v_cmp_eq_u32 vcc, 0, v130\n\t"
    "v_and_or_b32 v92, v106, s44, v69\n\t"
    "s_nop 0\n\t"
    "v_cndmask_b32 v130, v130, v129, vcc\n\t"
    "v_ffbl_b32 v131, v130\n\t"
    "v_and_b32 v134, 7, v131\n\t"
    "v_lshlrev_b32 v134, v134, s45\n\t"
    "v_xor_b32 v129, v129, v134\n\t"
    "v_xor_b32 v132, v131, v127\n\t"
    "v_bfi_b32 v130, v101, 0, v129\n\t"
    "v_cmp_eq_u32 vcc, 0, v130\n\t"
    "v_and_or_b32 v93, v101, s44, v69\n\t"
    "v_xor_b32 v133, 23, v132\n\t"
    "v_lshlrev_b32 v110, v132, v136\n\t"
    "v_and_b32 v132, 7, v132\n\t"
    "v_lshl_or_b32 v110, v136, v133, v110\n\t"
    "v_lshl_add_u32 v132, v132, 1, v128\n\t"
    "ds_or_rtn_b32 v118, v92, v110\n\t"
    "ds_write_b16 v132, v106\n\t"
    "v_cndmask_b32 v130, v130, v129, vcc\n\t"
    "v_ffbl_b32 v131, v130\n\t"
    "v_and_b32 v134, 7, v131\n\t"
    "v_lshlrev_b32 v134, v134, s45\n\t"
    "v_xor_b32 v129, v129, v134\n\t"
    "v_xor_b32 v132, v131, v127\n\t"
    "v_bfi_b32 v130, v108, 0, v129\n\t"
    "v_cmp_eq_u32 vcc, 0, v130\n\t"
    "v_and_or_b32 v94, v108, s44, v69\n\t"
    "v_xor_b32 v133, 23, v132\n\t"
    "v_lshlrev_b32 v111, v132, v136\n\t"
    "v_and_b32 v132, 7, v132\n\t"
    "v_lshl_or_b32 v111, v136, v133, v111\n\t"
    "v_lshl_add_u32 v132, v132, 1, v128\n\t"
    "ds_or_rtn_b32 v119, v93, v111\n\t"
    "ds_write_b16 v132, v101\n\t"
    "v_cndmask_b32 v130, v130, v129, vcc\n\t"
    "v_ffbl_b32 v131, v130\n\t"
    "v_and_b32 v134, 7, v131\n\t"
    "v_lshlrev_b32 v134, v134, s45\n\t"
    "v_xor_b32 v129, v129, v134\n\t"
    "v_xor_b32 v132, v131, v127\n\t"
    "s_waitcnt lgkmcnt(10)\n\t"
    "buffer_store_dwordx4 v[220:223], v139, %12, s48 offen sc1\n\t"
    "buffer_store_dwordx4 v[224:227], v140, %12, s48 offen sc1\n\t"
    "s_add_u32 s48, s48, 819200\n\t"
    "s_waitcnt lgkmcnt(4)\n\t"
    "buffer_store_dwordx4 v[228:231], v139, %12, s48 offen sc1\n\t"
    "buffer_store_dwordx4 v[232:235], v140, %12, s48 offen sc1\n\t"
    "s_add_u32 s48, s48, 819200\n\t"
    "ds_write_b128 v141, v[236:239] offset:0\n\t"
    "ds_write_b128 v141, v[240:243] offset:256\n\t"
    "ds_read_b64_tr_b16 v[236:237], v142 offset:0\n\t"
    "ds_read_b64_tr_b16 v[238:239], v142 offset:256\n\t"
    "ds_read_b64_tr_b16 v[240:241], v142 offset:32\n\t"
    "ds_read_b64_tr_b16 v[242:243], v142 offset:288\n\t"
    "ds_write_b128 v141, v[244:247] offset:2112\n\t"
    "ds_write_b128 v141, v[248:251] offset:2368\n\t"
    "ds_read_b64_tr_b16 v[244:245], v142 offset:2112\n\t"
    "ds_read_b64_tr_b16 v[246:247], v142 offset:2368\n\t"
    "ds_read_b64_tr_b16 v[248:249], v142 offset:2144\n\t"
    "ds_read_b64_tr_b16 v[250:251], v142 offset:2400\n\t"
    "v_bfi_b32 v130, v100, 0, v129\n\t"
    "v_cmp_eq_u32 vcc, 0, v130\n\t"
    "v_and_or_b32 v95, v100, s44, v69\n\t"
    "v_xor_b32 v133, 23, v132\n\t"
    "v_lshlrev_b32 v112, v132, v136\n\t"
    "v_and_b32 v132, 7, v132\n\t"
    "v_lshl_or_b32 v112, v136, v133, v112\n\t"
    "v_lshl_add_u32 v132, v132, 1, v128\n\t"
    "ds_or_rtn_b32 v120, v94, v112\n\t"
    "ds_write_b16 v132, v108\n\t"
    "v_cndmask_b32 v130, v130, v129, vcc\n\t"
    "v_ffbl_b32 v131, v130\n\t"
    "v_and_b32 v134, 7, v131\n\t"
    "v_lshlrev_b32 v134, v134, s45\n\t"
    "v_xor_b32 v129, v129, v134\n\t"
    "v_xor_b32 v132, v131, v127\n\t"
    "v_bfi_b32 v130, v102, 0, v129\n\t"
    "v_cmp_eq_u32 vcc, 0, v130\n\t"
    "v_and_or_b32 v96, v102, s44, v69\n\t"
    "v_xor_b32 v133, 23, v132\n\t"
    "v_lshlrev_b32 v113, v132, v136\n\t"
    "v_and_b32 v132, 7, v132\n\t"
    "v_lshl_or_b32 v113, v136, v133, v113\n\t"
    "v_lshl_add_u32 v132, v132, 1, v128\n\t"
    "ds_or_rtn_b32 v121, v95, v113\n\t"
    "ds_write_b16 v132, v100\n\t"
    "v_cndmask_b32 v130, v130, v129, vcc\n\t"
    "v_ffbl_b32 v131, v130\n\t"
    "v_and_b32 v134, 7, v131\n\t"
    "v_lshlrev_b32 v134, v134, s45\n\t"
    "v_xor_b32 v129, v129, v134\n\t"
    "v_xor_b32 v132, v131, v127\n\t"
    "s_waitcnt lgkmcnt(15)\n\t"
    "v_and_b32 v118, v118, v110\n\t"
    "v_perm_b32 v118, v118, v118, s46\n\t"
    "ds_or_b32 v92, v118\n\t"
    "v_bfi_b32 v130, v104, 0, v129\n\t"
    "v_cmp_eq_u32 vcc, 0, v130\n\t"
    "v_and_or_b32 v97, v104, s44, v69\n\t"
    "v_xor_b32 v133, 23, v132\n\t"
    "v_lshlrev_b32 v114, v132, v136\n\t"
    "v_and_b32 v132, 7, v132\n\t"
    "v_lshl_or_b32 v114, v136, v133, v114\n\t"
    "v_lshl_add_u32 v132, v132, 1, v128\n\t"
    "ds_or_rtn_b32 v122, v96, v114\n\t"
    "ds_write_b16 v132, v102\n\t"
    "v_cndmask_b32 v130, v130, v129, vcc\n\t"
    "v_ffbl_b32 v131, v130\n\t"
    "v_and_b32 v134, 7, v131\n\t"
    "v_lshlrev_b32 v134, v134, s45\n\t"
    "v_xor_b32 v129, v129, v134\n\t"
    "v_xor_b32 v132, v131, v127\n\t"
    "v_and_b32 v119, v119, v111\n\t"
    "v_perm_b32 v119, v119, v119, s46\n\t"
    "ds_or_b32 v93, v119\n\t"
    "s_waitcnt lgkmcnt(14)\n\t"
    "buffer_store_dwordx4 v[236:239], v139, %12, s48 offen sc1\n\t"
    "buffer_store_dwordx4 v[240:243], v140, %12, s48 offen sc1\n\t"
    "s_add_u32 s48, s48, 819200\n\t"
    "s_waitcnt lgkmcnt(8)\n\t"
    "buffer_store_dwordx4 v[244:247], v139, %12, s48 offen sc1\n\t"
    "buffer_store_dwordx4 v[248:251], v140, %12, s48 offen sc1\n\t"
    "s_add_u32 s48, s48, -2441216\n\t"
    "ds_write_b128 v141, v[24:27] offset:0\n\t"
    "ds_write_b128 v141, v[28:31] offset:256\n\t"
    "ds_read_b64_tr_b16 v[24:25], v142 offset:0\n\t"
    "ds_read_b64_tr_b16 v[26:27], v142 offset:256\n\t"
    "ds_read_b64_tr_b16 v[28:29], v142 offset:32\n\t"
    "ds_read_b64_tr_b16 v[30:31], v142 offset:288\n\t"
    "ds_write_b128 v141, v[32:35] offset:2112\n\t"
    "ds_write_b128 v141, v[36:39] offset:2368\n\t"
    "ds_read_b64_tr_b16 v[32:33], v142 offset:2112\n\t"
    "ds_read_b64_tr_b16 v[34:35], v142 offset:2368\n\t"
    "ds_read_b64_tr_b16 v[36:37], v142 offset:2144\n\t"
    "ds_read_b64_tr_b16 v[38:39], v142 offset:2400\n\t"
    "v_bfi_b32 v130, v103, 0, v129\n\t"
    "v_cmp_eq_u32 vcc, 0, v130\n\t"
    "v_and_or_b32 v98, v103, s44, v69\n\t"
    "v_xor_b32 v133, 23, v132\n\t"
    "v_lshlrev_b32 v115, v132, v136\n\t"
    "v_and_b32 v132, 7, v132\n\t"
    "v_lshl_or_b32 v115, v136, v133, v115\n\t"
    "v_lshl_add_u32 v132, v132, 1, v128\n\t"
    "ds_or_rtn_b32 v123, v97, v115\n\t"
    "ds_write_b16 v132, v104\n\t"
    "v_cndmask_b32 v130, v130, v129, vcc\n\t"
    "v_ffbl_b32 v131, v130\n\t"
    "v_and_b32 v134, 7, v131\n\t"
    "v_lshlrev_b32 v134, v134, s45\n\t"
    "v_xor_b32 v129, v129, v134\n\t"
    "v_xor_b32 v132, v131, v127\n\t"
    "s_waitcnt lgkmcnt(15)\n\t"
    "v_and_b32 v120, v120, v112\n\t"
    "v_perm_b32 v120, v120, v120, s46\n\t"
    "ds_or_b32 v94, v120\n\t"
    "v_bfi_b32 v130, v107, 0, v129\n\t"
    "v_cmp_eq_u32 vcc, 0, v130\n\t"
    "v_and_or_b32 v99, v107, s44, v69\n\t"
    "v_xor_b32 v133, 23, v132\n\t"
    "v_lshlrev_b32 v116, v132, v136\n\t"
    "v_and_b32 v132, 7, v132\n\t"
    "v_lshl_or_b32 v116, v136, v133, v116\n\t"
    "v_lshl_add_u32 v132, v132, 1, v128\n\t"
    "ds_or_rtn_b32 v124, v98, v116\n\t"
    "ds_write_b16 v132, v103\n\t"
    "v_cndmask_b32 v130, v130, v129, vcc\n\t"
    "v_ffbl_b32 v131, v130\n\t"
    "v_xor_b32 v132, v131, v127\n\t"
    "v_and_b32 v121, v121, v113\n\t"
    "v_perm_b32 v121, v121, v121, s46\n\t"
    "ds_or_b32 v95, v121\n\t"
    "v_xor_b32 v133, 23, v132\n\t"
    "v_lshlrev_b32 v117, v132, v136\n\t"
    "v_and_b32 v132, 7, v132\n\t"
    "v_lshl_or_b32 v117, v136, v133, v117\n\t"
    "v_lshl_add_u32 v132, v132, 1, v128\n\t"
    "ds_or_rtn_b32 v125, v99, v117\n\t"
    "ds_write_b16 v132, v107\n\t"
    "v_and_b32 v122, v122, v114\n\t"
    "v_perm_b32 v122, v122, v122, s46\n\t"
    "ds_or_b32 v96, v122\n\t"
    "ds_read_b128 v[72:75], v128\n\t"
    "v_and_or_b32 v100, v76, s44, v69\n\t"
    "ds_read_b32 v100, v100\n\t"
    "v_and_or_b32 v101, v77, s44, v69\n\t"
    "ds_read_b32 v101, v101\n\t"
    "v_and_or_b32 v102, v78, s44, v69\n\t"
    "ds_read_b32 v102, v102\n\t"
    "v_and_or_b32 v103, v79, s44, v69\n\t"
    "ds_read_b32 v103, v103\n\t"
    "v_and_or_b32 v104, v80, s44, v69\n\t"
    "ds_read_b32 v104, v104\n\t"
    "v_and_or_b32 v105, v81, s44, v69\n\t"
    "ds_read_b32 v105, v105\n\t"
    "v_and_or_b32 v106, v82, s44, v69\n\t"
    "ds_read_b32 v106, v106\n\t"
    "v_and_or_b32 v107, v83, s44, v69\n\t"
    "ds_read_b32 v107, v107\n\t"
    "v_lshl_add_u32 v84, v56, 3, s36\n\t"
    "v_lshl_add_u32 v85, v57, 3, s37\n\t"
    "v_lshl_add_u32 v86, v58, 3, s38\n\t"
    "v_lshl_add_u32 v87, v59, 3, s39\n\t"
    "v_lshl_add_u32 v88, v60, 3, s40\n\t"
    "v_lshl_add_u32 v89, v61, 3, s41\n\t"
    "v_lshl_add_u32 v90, v62, 3, s42\n\t"
    "v_lshl_add_u32 v91, v63, 3, s43\n\t"
    "s_waitcnt lgkmcnt(15)\n\t"
    "buffer_store_dwordx4 v[24:27], v139, %12, s48 offen sc1\n\t"
    "buffer_store_dwordx4 v[28:31], v140, %12, s48 offen sc1\n\t"
    "s_add_u32 s48, s48, 819200\n\t"
    "buffer_store_dwordx4 v[32:35], v139, %12, s48 offen sc1\n\t"
    "buffer_store_dwordx4 v[36:39], v140, %12, s48 offen sc1\n\t"
    "s_add_u32 s48, s48, 819200\n\t"
    "buffer_load_dwordx4 v[204:207], v137, s[96:99], s47 offen offset:64\n\t"
    "buffer_load_dwordx4 v[208:211], v138, s[96:99], s47 offen offset:64\n\t"
    "buffer_load_dwordx4 v[212:215], v137, s[96:99], s47 offen offset:128\n\t"
    "buffer_load_dwordx4 v[216:219], v138, s[96:99], s47 offen offset:128\n\t"
    "buffer_load_dwordx4 v[48:51], v137, s[96:99], s47 offen offset:192\n\t"
    "buffer_load_dwordx4 v[52:55], v138, s[96:99], s47 offen offset:192\n\t"
    "s_add_u32 s47, s47, 2097152\n\t"
    "buffer_load_dwordx4 v[220:223], v137, s[96:99], s47 offen offset:0\n\t"
    "buffer_load_dwordx4 v[224:227], v138, s[96:99], s47 offen offset:0\n\t"
    "buffer_load_dwordx4 v[228:231], v137, s[96:99], s47 offen offset:64\n\t"
    "buffer_load_dwordx4 v[232:235], v138, s[96:99], s47 offen offset:64\n\t"
    "buffer_load_dwordx4 v[236:239], v137, s[96:99], s47 offen offset:128\n\t"
    "buffer_load_dwordx4 v[240:243], v138, s[96:99], s47 offen offset:128\n\t"
    "s_waitcnt lgkmcnt(11)\n\t"
    "v_and_b32 v123, v123, v115\n\t"
    "v_perm_b32 v123, v123, v123, s46\n\t"
    "ds_or_b32 v97, v123\n\t"
    "v_and_b32 v124, v124, v116\n\t"
    "v_perm_b32 v124, v124, v124, s46\n\t"
    "ds_or_b32 v98, v124\n\t"
    "v_and_b32 v125, v125, v117\n\t"
    "v_perm_b32 v125, v125, v125, s46\n\t"
    "ds_or_b32 v99, v125\n\t"
    "s_waitcnt lgkmcnt(11)\n\t"
    "buffer_store_dwordx4 v[72:75], v67, %12, s49 offen offset:320 sc1\n\t"
    "v_mov_b32 v129, 0xffff0000\n\t"
    "s_waitcnt lgkmcnt(7)\n\t"
    "v_perm_b32 v100, v100, v76, v126\n\t"
    "v_perm_b32 v101, v101, v77, v126\n\t"
    "v_perm_b32 v102, v102, v78, v126\n\t"
    "v_perm_b32 v103, v103, v79, v126\n\t"
    "s_waitcnt lgkmcnt(3)\n\t"
    "v_perm_b32 v104, v104, v80, v126\n\t"
    "v_perm_b32 v105, v105, v81, v126\n\t"
    "v_perm_b32 v106, v106, v82, v126\n\t"
    "v_perm_b32 v107, v107, v83, v126\n\t"
    "ds_write_b128 v141, v[40:43] offset:0\n\t"
    "ds_write_b128 v141, v[44:47] offset:256\n\t"
    "ds_read_b64_tr_b16 v[40:41], v142 offset:0\n\t"
    "ds_read_b64_tr_b16 v[42:43], v142 offset:256\n\t"
    "ds_read_b64_tr_b16 v[44:45], v142 offset:32\n\t"
    "ds_read_b64_tr_b16 v[46:47], v142 offset:288\n\t"
    "v_max_u32 v108, v100, v101\n\t"
    "v_min_u32 v101, v100, v101\n\t"
    "v_max_u32 v100, v102, v103\n\t"
    "v_min_u32 v103, v102, v103\n\t"
    "v_max_u32 v102, v104, v105\n\t"
    "v_min_u32 v105, v104, v105\n\t"
    "v_max_u32 v104, v106, v107\n\t"
    "v_min_u32 v107, v106, v107\n\t"
    "v_max_u32 v106, v108, v100\n\t"
    "v_min_u32 v100, v108, v100\n\t"
    "v_max_u32 v108, v101, v103\n\t"
    "v_min_u32 v103, v101, v103\n\t"
    "v_max_u32 v101, v102, v104\n\t"
    "v_min_u32 v104, v102, v104\n\t"
    "v_max_u32 v102, v105, v107\n\t"
    "v_min_u32 v107, v105, v107\n\t"
    "v_bfi_b32 v130, v106, 0, v129\n\t"
    "v_cmp_eq_u32 vcc, 0, v130\n\t"
    "v_and_or_b32 v92, v106, s44, v69\n\t"
    "s_nop 0\n\t"
    "v_cndmask_b32 v130, v130, v129, vcc\n\t"
    "v_ffbl_b32 v131, v130\n\t"
    "v_and_b32 v134, 7, v131\n\t"
    "v_lshlrev_b32 v134, v134, s45\n\t"
    "v_xor_b32 v129, v129, v134\n\t"
    "v_xor_b32 v132, v131, v127\n\t"
    "v_bfi_b32 v130, v101, 0, v129\n\t"
    "v_cmp_eq_u32 vcc, 0, v130\n\t"
    "v_and_or_b32 v93, v101, s44, v69\n\t"
    "v_xor_b32 v133, 23, v132\n\t"
    "v_lshlrev_b32 v110, v132, v136\n\t"
    "v_and_b32 v132, 7, v132\n\t"
    "v_lshl_or_b32 v110, v136, v133, v110\n\t"
    "v_lshl_add_u32 v132, v132, 1, v128\n\t"
    "ds_or_rtn_b32 v118, v92, v110\n\t"
    "ds_write_b16 v132, v106\n\t"
    "v_cndmask_b32 v130, v130, v129, vcc\n\t"
    "v_ffbl_b32 v131, v130\n\t"
    "v_and_b32 v134, 7, v131\n\t"
    "v_lshlrev_b32 v134, v134, s45\n\t"
    "v_xor_b32 v129, v129, v134\n\t"
    "v_xor_b32 v132, v131, v127\n\t"
    "v_bfi_b32 v130, v108, 0, v129\n\t"
    "v_cmp_eq_u32 vcc, 0, v130\n\t"
    "v_and_or_b32 v94, v108, s44, v69\n\t"
    "v_xor_b32 v133, 23, v132\n\t"
    "v_lshlrev_b32 v111, v132, v136\n\t"
    "v_and_b32 v132, 7, v132\n\t"
    "v_lshl_or_b32 v111, v136, v133, v111\n\t"
    "v_lshl_add_u32 v132, v132, 1, v128\n\t"
    "ds_or_rtn_b32 v119, v93, v111\n\t"
    "ds_write_b16 v132, v101\n\t"
    "v_cndmask_b32 v130, v130, v129, vcc\n\t"
    "v_ffbl_b32 v131, v130\n\t"
    "v_and_b32 v134, 7, v131\n\t"
    "v_lshlrev_b32 v134, v134, s45\n\t"
    "v_xor_b32 v129, v129, v134\n\t"
    "v_xor_b32 v132, v131, v127\n\t"
    "s_waitcnt lgkmcnt(4)\n\t"
    "buffer_store_dwordx4 v[40:43], v139, %12, s48 offen sc1\n\t"
    "buffer_store_dwordx4 v[44:47], v140, %12, s48 offen sc1\n\t"
    "s_add_u32 s48, s48, 819200\n\t"
    "v_bfi_b32 v130, v100, 0, v129\n\t"
    "v_cmp_eq_u32 vcc, 0, v130\n\t"
    "v_and_or_b32 v95, v100, s44, v69\n\t"
    "v_xor_b32 v133, 23, v132\n\t"
    "v_lshlrev_b32 v112, v132, v136\n\t"
    "v_and_b32 v132, 7, v132\n\t"
    "v_lshl_or_b32 v112, v136, v133, v112\n\t"
    "v_lshl_add_u32 v132, v132, 1, v128\n\t"
    "ds_or_rtn_b32 v120, v94, v112\n\t"
    "ds_write_b16 v132, v108\n\t"
    "v_cndmask_b32 v130, v130, v129, vcc\n\t"
    "v_ffbl_b32 v131, v130\n\t"
    "v_and_b32 v134, 7, v131\n\t"
    "v_lshlrev_b32 v134, v134, s45\n\t"
    "v_xor_b32 v129, v129, v134\n\t"
    "v_xor_b32 v132, v131, v127\n\t"
    "v_bfi_b32 v130, v102, 0, v129\n\t"
    "v_cmp_eq_u32 vcc, 0, v130\n\t"
    "v_and_or_b32 v96, v102, s44, v69\n\t"
    "v_xor_b32 v133, 23, v132\n\t"
    "v_lshlrev_b32 v113, v132, v136\n\t"
    "v_and_b32 v132, 7, v132\n\t"
    "v_lshl_or_b32 v113, v136, v133, v113\n\t"
    "v_lshl_add_u32 v132, v132, 1, v128\n\t"
    "ds_or_rtn_b32 v121, v95, v113\n\t"
    "ds_write_b16 v132, v100\n\t"
    "v_cndmask_b32 v130, v130, v129, vcc\n\t"
    "v_ffbl_b32 v131, v130\n\t"
    "v_and_b32 v134, 7, v131\n\t"
    "v_lshlrev_b32 v134, v134, s45\n\t"
    "v_xor_b32 v129, v129, v134\n\t"
    "v_xor_b32 v132, v131, v127\n\t"
    "s_waitcnt lgkmcnt(7)\n\t"
    "v_and_b32 v118, v118, v110\n\t"
    "v_perm_b32 v118, v118, v118, s46\n\t"
    "ds_or_b32 v92, v118\n\t"
    "v_bfi_b32 v130, v104, 0, v129\n\t"
    "v_cmp_eq_u32 vcc, 0, v130\n\t"
    "v_and_or_b32 v97, v104, s44, v69\n\t"
    "v_xor_b32 v133, 23, v132\n\t"
    "v_lshlrev_b32 v114, v132, v136\n\t"
    "v_and_b32 v132, 7, v132\n\t"
    "v_lshl_or_b32 v114, v136, v133, v114\n\t"
    "v_lshl_add_u32 v132, v132, 1, v128\n\t"
    "ds_or_rtn_b32 v122, v96, v114\n\t"
    "ds_write_b16 v132, v102\n\t"
    "v_cndmask_b32 v130, v130, v129, vcc\n\t"
    "v_ffbl_b32 v131, v130\n\t"
    "v_and_b32 v134, 7, v131\n\t"
    "v_lshlrev_b32 v134, v134, s45\n\t"
    "v_xor_b32 v129, v129, v134\n\t"
    "v_xor_b32 v132, v131, v127\n\t"
    "s_waitcnt lgkmcnt(8)\n\t"
    "v_and_b32 v119, v119, v111\n\t"
    "v_perm_b32 v119, v119, v119, s46\n\t"
    "ds_or_b32 v93, v119\n\t"
    "v_bfi_b32 v130, v103, 0, v129\n\t"
    "v_cmp_eq_u32 vcc, 0, v130\n\t"
    "v_and_or_b32 v98, v103, s44, v69\n\t"
    "v_xor_b32 v133, 23, v132\n\t"
    "v_lshlrev_b32 v115, v132, v136\n\t"
    "v_and_b32 v132, 7, v132\n\t"
    "v_lshl_or_b32 v115, v136, v133, v115\n\t"
    "v_lshl_add_u32 v132, v132, 1, v128\n\t"
    "ds_or_rtn_b32 v123, v97, v115\n\t"
    "ds_write_b16 v132, v104\n\t"
    "v_cndmask_b32 v130, v130, v129, vcc\n\t"
    "v_ffbl_b32 v131, v130\n\t"
    "v_and_b32 v134, 7, v131\n\t"
    "v_lshlrev_b32 v134, v134, s45\n\t"
    "v_xor_b32 v129, v129, v134\n\t"
    "v_xor_b32 v132, v131, v127\n\t"
    "s_waitcnt lgkmcnt(9)\n\t"
    "v_and_b32 v120, v120, v112\n\t"
    "v_perm_b32 v120, v120, v120, s46\n\t"
    "ds_or_b32 v94, v120\n\t"
    "v_bfi_b32 v130, v107, 0, v129\n\t"
    "v_cmp_eq_u32 vcc, 0, v130\n\t"
    "v_and_or_b32 v99, v107, s44, v69\n\t"
    "v_xor_b32 v133, 23, v132\n\t"
    "v_lshlrev_b32 v116, v132, v136\n\t"
    "v_and_b32 v132, 7, v132\n\t"
    "v_lshl_or_b32 v116, v136, v133, v116\n\t"
    "v_lshl_add_u32 v132, v132, 1, v128\n\t"
    "ds_or_rtn_b32 v124, v98, v116\n\t"
    "ds_write_b16 v132, v103\n\t"
    "v_cndmask_b32 v130, v130, v129, vcc\n\t"
    "v_ffbl_b32 v131, v130\n\t"
    "v_xor_b32 v132, v131, v127\n\t"
    "s_waitcnt lgkmcnt(10)\n\t"
    "v_and_b32 v121, v121, v113\n\t"
    "v_perm_b32 v121, v121, v121, s46\n\t"
    "ds_or_b32 v95, v121\n\t"
    "v_xor_b32 v133, 23, v132\n\t"
    "v_lshlrev_b32 v117, v132, v136\n\t"
    "v_and_b32 v132, 7, v132\n\t"
    "v_lshl_or_b32 v117, v136, v133, v117\n\t"
    "v_lshl_add_u32 v132, v132, 1, v128\n\t"
    "ds_or_rtn_b32 v125, v99, v117\n\t"
    "ds_write_b16 v132, v107\n\t"
    "s_waitcnt lgkmcnt(10)\n\t"
    "v_and_b32 v122, v122, v114\n\t"
    "v_perm_b32 v122, v122, v122, s46\n\t"
    "ds_or_b32 v96, v122\n\t"
    "ds_read_b128 v[72:75], v128\n\t"
    "v_and_or_b32 v100, v84, s44, v69\n\t"
    "ds_read_b32 v100, v100\n\t"
    "v_and_or_b32 v101, v85, s44, v69\n\t"
    "ds_read_b32 v101, v101\n\t"
    "v_and_or_b32 v102, v86, s44, v69\n\t"
    "ds_read_b32 v102, v102\n\t"
    "v_and_or_b32 v103, v87, s44, v69\n\t"
    "ds_read_b32 v103, v103\n\t"
    "v_and_or_b32 v104, v88, s44, v69\n\t"
    "ds_read_b32 v104, v104\n\t"
    "v_and_or_b32 v105, v89, s44, v69\n\t"
    "ds_read_b32 v105, v105\n\t"
    "v_and_or_b32 v106, v90, s44, v69\n\t"
    "ds_read_b32 v106, v106\n\t"
    "v_and_or_b32 v107, v91, s44, v69\n\t"
    "ds_read_b32 v107, v107\n\t"
    "v_lshl_add_u32 v76, v0, 3, s36\n\t"
    "v_lshl_add_u32 v77, v1, 3, s37\n\t"
    "v_lshl_add_u32 v78, v2, 3, s38\n\t"
    "v_lshl_add_u32 v79, v3, 3, s39\n\t"
    "v_lshl_add_u32 v80, v4, 3, s40\n\t"
    "v_lshl_add_u32 v81, v5, 3, s41\n\t"
    "v_lshl_add_u32 v82, v6, 3, s42\n\t"
    "v_lshl_add_u32 v83, v7, 3, s43\n\t"
    "v_mbcnt_lo_u32_b32 v70, -1, 0\n\t"
    "v_mbcnt_hi_u32_b32 v70, -1, v70\n\t"
    "v_add_u32 v70, 8, v70\n\t"
    "v_mov_b32 v148, 32\n\t"
    "v_lshlrev_b32 v70, 7, v70\n\t"
    "s_mov_b32 s52, 49283072\n\t"
    "s_mov_b64 exec, 0xff\n\t"
    "buffer_load_dword v71, v70, %12, s52 offen sc1\n\t"
    "s_mov_b64 exec, -1\n\t"
    "s_mov_b32 s54, 0x40000000\n\t"
    "s_mov_b32 s55, 0\n\t"
    "buffer_load_dwordx4 v[244:247], v137, s[96:99], s47 offen offset:192\n\t"
    "buffer_load_dwordx4 v[248:251], v138, s[96:99], s47 offen offset:192\n\t"
    "s_add_u32 s47, s47, 2097152\n\t"
    "s_waitcnt lgkmcnt(11)\n\t"
    "v_and_b32 v123, v123, v115\n\t"
    "v_perm_b32 v123, v123, v123, s46\n\t"
    "ds_or_b32 v97, v123\n\t"
    "v_and_b32 v124, v124, v116\n\t"
    "v_perm_b32 v124, v124, v124, s46\n\t"
    "ds_or_b32 v98, v124\n\t"
    "v_and_b32 v125, v125, v117\n\t"
    "v_perm_b32 v125, v125, v125, s46\n\t"
    "ds_or_b32 v99, v125\n\t"
    "s_waitcnt lgkmcnt(11)\n\t"
    "buffer_store_dwordx4 v[72:75], v67, %12, s49 offen offset:352 sc1\n\t"
    "v_mov_b32 v129, 0xffff0000\n\t"
    "s_waitcnt lgkmcnt(7)\n\t"
    "v_perm_b32 v100, v100, v84, v126\n\t"
    "v_perm_b32 v101, v101, v85, v126\n\t"
    "v_perm_b32 v102, v102, v86, v126\n\t"
    "v_perm_b32 v103, v103, v87, v126\n\t"
    "s_waitcnt lgkmcnt(3)\n\t"
    "v_perm_b32 v104, v104, v88, v126\n\t"
    "v_perm_b32 v105, v105, v89, v126\n\t"
    "v_perm_b32 v106, v106, v90, v126\n\t"
    "v_perm_b32 v107, v107, v91, v126\n\t"
    "s_waitcnt vmcnt(42)\n\t"
    "ds_write_b128 v141, v[156:159] offset:2112\n\t"
    "ds_write_b128 v141, v[160:163] offset:2368\n\t"
    "ds_read_b64_tr_b16 v[156:157], v142 offset:2112\n\t"
    "ds_read_b64_tr_b16 v[158:159], v142 offset:2368\n\t"
    "ds_read_b64_tr_b16 v[160:161], v142 offset:2144\n\t"
    "ds_read_b64_tr_b16 v[162:163], v142 offset:2400\n\t"
    "s_waitcnt vmcnt(40)\n\t"
    "ds_write_b128 v141, v[164:167] offset:0\n\t"
    "ds_write_b128 v141, v[168:171] offset:256\n\t"
    "ds_read_b64_tr_b16 v[164:165], v142 offset:0\n\t"
    "ds_read_b64_tr_b16 v[166:167], v142 offset:256\n\t"
    "ds_read_b64_tr_b16 v[168:169], v142 offset:32\n\t"
    "ds_read_b64_tr_b16 v[170:171], v142 offset:288\n\t"
    "v_max_u32 v108, v100, v101\n\t"
    "v_min_u32 v101, v100, v101\n\t"
    "v_max_u32 v100, v102, v103\n\t"
    "v_min_u32 v103, v102, v103\n\t"
    "v_max_u32 v102, v104, v105\n\t"
    "v_min_u32 v105, v104, v105\n\t"
    "v_max_u32 v104, v106, v107\n\t"
    "v_min_u32 v107, v106, v107\n\t"
    "v_max_u32 v106, v108, v100\n\t"
    "v_min_u32 v100, v108, v100\n\t"
    "v_max_u32 v108, v101, v103\n\t"
    "v_min_u32 v103, v101, v103\n\t"
    "v_max_u32 v101, v102, v104\n\t"
    "v_min_u32 v104, v102, v104\n\t"
    "v_max_u32 v102, v105, v107\n\t"
    "v_min_u32 v107, v105, v107\n\t"
    "v_bfi_b32 v130, v106, 0, v129\n\t"
    "v_cmp_eq_u32 vcc, 0, v130\n\t"
    "v_and_or_b32 v92, v106, s44, v69\n\t"
    "s_nop 0\n\t"
    "v_cndmask_b32 v130, v130, v129, vcc\n\t"
    "v_ffbl_b32 v131, v130\n\t"
    "v_and_b32 v134, 7, v131\n\t"
    "v_lshlrev_b32 v134, v134, s45\n\t"
    "v_xor_b32 v129, v129, v134\n\t"
    "v_xor_b32 v132, v131, v127\n\t"
    "v_bfi_b32 v130, v101, 0, v129\n\t"
    "v_cmp_eq_u32 vcc, 0, v130\n\t"
    "v_and_or_b32 v93, v101, s44, v69\n\t"
    "v_xor_b32 v133, 23, v132\n\t"
    "v_lshlrev_b32 v110, v132, v136\n\t"
    "v_and_b32 v132, 7, v132\n\t"
    "v_lshl_or_b32 v110, v136, v133, v110\n\t"
    "v_lshl_add_u32 v132, v132, 1, v128\n\t"
    "ds_or_b32 v92, v110\n\t"
    "ds_write_b16 v132, v106\n\t"
    "v_cndmask_b32 v130, v130, v129, vcc\n\t"
    "v_ffbl_b32 v131, v130\n\t"
    "v_and_b32 v134, 7, v131\n\t"
    "v_lshlrev_b32 v134, v134, s45\n\t"
    "v_xor_b32 v129, v129, v134\n\t"
    "v_xor_b32 v132, v131, v127\n\t"
    "v_bfi_b32 v130, v108, 0, v129\n\t"
    "v_cmp_eq_u32 vcc, 0, v130\n\t"
    "v_and_or_b32 v94, v108, s44, v69\n\t"
    "v_xor_b32 v133, 23, v132\n\t"
    "v_lshlrev_b32 v111, v132, v136\n\t"
    "v_and_b32 v132, 7, v132\n\t"
    "v_lshl_or_b32 v111, v136, v133, v111\n\t"
    "v_lshl_add_u32 v132, v132, 1, v128\n\t"
    "ds_or_b32 v93, v111\n\t"
    "ds_write_b16 v132, v101\n\t"
    "v_cndmask_b32 v130, v130, v129, vcc\n\t"
    "v_ffbl_b32 v131, v130\n\t"
    "v_and_b32 v134, 7, v131\n\t"
    "v_lshlrev_b32 v134, v134, s45\n\t"
    "v_xor_b32 v129, v129, v134\n\t"
    "v_xor_b32 v132, v131, v127\n\t"
    "s_waitcnt lgkmcnt(10)\n\t"
    "buffer_store_dwordx4 v[156:159], v139, %12, s48 offen sc1\n\t"
    "buffer_store_dwordx4 v[160:163], v140, %12, s48 offen sc1\n\t"
    "s_add_u32 s48, s48, -2441216\n\t"
    "s_waitcnt lgkmcnt(4)\n\t"
    "buffer_store_dwordx4 v[164:167], v139, %12, s48 offen sc1\n\t"
    "buffer_store_dwordx4 v[168:171], v140, %12, s48 offen sc1\n\t"
    "s_add_u32 s48, s48, 819200\n\t"
    "s_waitcnt vmcnt(42)\n\t"
    "ds_write_b128 v141, v[172:175] offset:2112\n\t"
    "ds_write_b128 v141, v[176:179] offset:2368\n\t"
    "ds_read_b64_tr_b16 v[172:173], v142 offset:2112\n\t"
    "ds_read_b64_tr_b16 v[174:175], v142 offset:2368\n\t"
    "ds_read_b64_tr_b16 v[176:177], v142 offset:2144\n\t"
    "ds_read_b64_tr_b16 v[178:179], v142 offset:2400\n\t"
    "s_waitcnt vmcnt(40)\n\t"
    "ds_write_b128 v141, v[180:183] offset:0\n\t"
    "ds_write_b128 v141, v[184:187] offset:256\n\t"
    "ds_read_b64_tr_b16 v[180:181], v142 offset:0\n\t"
    "ds_read_b64_tr_b16 v[182:183], v142 offset:256\n\t"
    "ds_read_b64_tr_b16 v[184:185], v142 offset:32\n\t"
    "ds_read_b64_tr_b16 v[186:187], v142 offset:288\n\t"
    "v_bfi_b32 v130, v100, 0, v129\n\t"
    "v_cmp_eq_u32 vcc, 0, v130\n\t"
    "v_and_or_b32 v95, v100, s44, v69\n\t"
    "v_xor_b32 v133, 23, v132\n\t"
    "v_lshlrev_b32 v112, v132, v136\n\t"
    "v_and_b32 v132, 7, v132\n\t"
    "v_lshl_or_b32 v112, v136, v133, v112\n\t"
    "v_lshl_add_u32 v132, v132, 1, v128\n\t"
    "ds_or_b32 v94, v112\n\t"
    "ds_write_b16 v132, v108\n\t"
    "v_cndmask_b32 v130, v130, v129, vcc\n\t"
    "v_ffbl_b32 v131, v130\n\t"
    "v_and_b32 v134, 7, v131\n\t"
    "v_lshlrev_b32 v134, v134, s45\n\t"
    "v_xor_b32 v129, v129, v134\n\t"
    "v_xor_b32 v132, v131, v127\n\t"
    "v_bfi_b32 v130, v102, 0, v129\n\t"
    "v_cmp_eq_u32 vcc, 0, v130\n\t"
    "v_and_or_b32 v96, v102, s44, v69\n\t"
    "v_xor_b32 v133, 23, v132\n\t"
    "v_lshlrev_b32 v113, v132, v136\n\t"
    "v_and_b32 v132, 7, v132\n\t"
    "v_lshl_or_b32 v113, v136, v133, v113\n\t"
    "v_lshl_add_u32 v132, v132, 1, v128\n\t"
    "ds_or_b32 v95, v113\n\t"
    "ds_write_b16 v132, v100\n\t"
    "v_cndmask_b32 v130, v130, v129, vcc\n\t"
    "v_ffbl_b32 v131, v130\n\t"
    "v_and_b32 v134, 7, v131\n\t"
    "v_lshlrev_b32 v134, v134, s45\n\t"
    "v_xor_b32 v129, v129, v134\n\t"
    "v_xor_b32 v132, v131, v127\n\t"
    "s_waitcnt vmcnt(7)\n\t"
    "s_call_b64 s[54:55], L_af1w_%=\n\t"
    "s_mov_b64 s[54:55], 0\n\t"
    "L_af1n15_%=:\n\t"
    "v_bfi_b32 v130, v104, 0, v129\n\t"
    "v_cmp_eq_u32 vcc, 0, v130\n\t"
    "v_and_or_b32 v97, v104, s44, v69\n\t"
    "v_xor_b32 v133, 23, v132\n\t"
    "v_lshlrev_b32 v114, v132, v136\n\t"
    "v_and_b32 v132, 7, v132\n\t"
    "v_lshl_or_b32 v114, v136, v133, v114\n\t"
    "v_lshl_add_u32 v132, v132, 1, v128\n\t"
    "ds_or_b32 v96, v114\n\t"
    "ds_write_b16 v132, v102\n\t"
    "v_cndmask_b32 v130, v130, v129, vcc\n\t"
    "v_ffbl_b32 v131, v130\n\t"
    "v_and_b32 v134, 7, v131\n\t"
    "v_lshlrev_b32 v134, v134, s45\n\t"
    "v_xor_b32 v129, v129, v134\n\t"
    "v_xor_b32 v132, v131, v127\n\t"
    "s_waitcnt lgkmcnt(12)\n\t"
    "buffer_store_dwordx4 v[172:175], v139, %12, s48 offen sc1\n\t"
    "buffer_store_dwordx4 v[176:179], v140, %12, s48 offen sc1\n\t"
    "s_add_u32 s48, s48, 819200\n\t"
    "s_waitcnt lgkmcnt(6)\n\t"
    "buffer_store_dwordx4 v[180:183], v139, %12, s48 offen sc1\n\t"
    "buffer_store_dwordx4 v[184:187], v140, %12, s48 offen sc1\n\t"
    "s_add_u32 s48, s48, 819200\n\t"
    "ds_write_b128 v141, v[188:191] offset:2112\n\t"
    "ds_write_b128 v141, v[192:195] offset:2368\n\t"
    "ds_read_b64_tr_b16 v[188:189], v142 offset:2112\n\t"
    "ds_read_b64_tr_b16 v[190:191], v142 offset:2368\n\t"
    "ds_read_b64_tr_b16 v[192:193], v142 offset:2144\n\t"
    "ds_read_b64_tr_b16 v[194:195], v142 offset:2400\n\t"
    "ds_write_b128 v141, v[196:199] offset:0\n\t"
    "ds_write_b128 v141, v[200:203] offset:256\n\t"
    "ds_read_b64_tr_b16 v[196:197], v142 offset:0\n\t"
    "ds_read_b64_tr_b16 v[198:199], v142 offset:256\n\t"
    "ds_read_b64_tr_b16 v[200:201], v142 offset:32\n\t"
    "ds_read_b64_tr_b16 v[202:203], v142 offset:288\n\t"
    "v_bfi_b32 v130, v103, 0, v129\n\t"
    "v_cmp_eq_u32 vcc, 0, v130\n\t"
    "v_and_or_b32 v98, v103, s44, v69\n\t"
    "v_xor_b32 v133, 23, v132\n\t"
    "v_lshlrev_b32 v115, v132, v136\n\t"
    "v_and_b32 v132, 7, v132\n\t"
    "v_lshl_or_b32 v115, v136, v133, v115\n\t"
    "v_lshl_add_u32 v132, v132, 1, v128\n\t"
    "ds_or_b32 v97, v115\n\t"
    "ds_write_b16 v132, v104\n\t"
    "v_cndmask_b32 v130, v130, v129, vcc\n\t"
    "v_ffbl_b32 v131, v130\n\t"
    "v_and_b32 v134, 7, v131\n\t"
    "v_lshlrev_b32 v134, v134, s45\n\t"
    "v_xor_b32 v129, v129, v134\n\t"
    "v_xor_b32 v132, v131, v127\n\t"
    "v_bfi_b32 v130, v107, 0, v129\n\t"
    "v_cmp_eq_u32 vcc, 0, v130\n\t"
    "v_and_or_b32 v99, v107, s44, v69\n\t"
    "v_xor_b32 v133, 23, v132\n\t"
    "v_lshlrev_b32 v116, v132, v136\n\t"
    "v_and_b32 v132, 7, v132\n\t"
    "v_lshl_or_b32 v116, v136, v133, v116\n\t"
    "v_lshl_add_u32 v132, v132, 1, v128\n\t"
    "ds_or_b32 v98, v116\n\t"
    "ds_write_b16 v132, v103\n\t"
    "v_cndmask_b32 v130, v130, v129, vcc\n\t"
    "v_ffbl_b32 v131, v130\n\t"
    "v_xor_b32 v132, v131, v127\n\t"
    "v_xor_b32 v133, 23, v132\n\t"
    "v_lshlrev_b32 v117, v132, v136\n\t"
    "v_and_b32 v132, 7, v132\n\t"
    "v_lshl_or_b32 v117, v136, v133, v117\n\t"
    "v_lshl_add_u32 v132, v132, 1, v128\n\t"
    "ds_or_b32 v99, v117\n\t"
    "ds_write_b16 v132, v107\n\t"
    "ds_read_b128 v[72:75], v128\n\t"
    "v_and_or_b32 v100, v76, s44, v69\n\t"
    "ds_read_b32 v100, v100\n\t"
    "v_and_or_b32 v101, v77, s44, v69\n\t"
    "ds_read_b32 v101, v101\n\t"
    "v_and_or_b32 v102, v78, s44, v69\n\t"
    "ds_read_b32 v102, v102\n\t"
    "v_and_or_b32 v103, v79, s44, v69\n\t"
    "ds_read_b32 v103, v103\n\t"
    "v_and_or_b32 v104, v80, s44, v69\n\t"
    "ds_read_b32 v104, v104\n\t"
    "v_and_or_b32 v105, v81, s44, v69\n\t"
    "ds_read_b32 v105, v105\n\t"
    "v_and_or_b32 v106, v82, s44, v69\n\t"
    "ds_read_b32 v106, v106\n\t"
    "v_and_or_b32 v107, v83, s44, v69\n\t"
    "ds_read_b32 v107, v107\n\t"
    "v_lshl_add_u32 v84, v8, 3, s36\n\t"
    "v_lshl_add_u32 v85, v9, 3, s37\n\t"
    "v_lshl_add_u32 v86, v10, 3, s38\n\t"
    "v_lshl_add_u32 v87, v11, 3, s39\n\t"
    "v_lshl_add_u32 v88, v12, 3, s40\n\t"
    "v_lshl_add_u32 v89, v13, 3, s41\n\t"
    "v_lshl_add_u32 v90, v14, 3, s42\n\t"
    "v_lshl_add_u32 v91, v15, 3, s43\n\t"
    "s_waitcnt lgkmcnt(15)\n\t"
    "buffer_store_dwordx4 v[188:191], v139, %12, s48 offen sc1\n\t"
    "buffer_store_dwordx4 v[192:195], v140, %12, s48 offen sc1\n\t"
    "s_add_u32 s48, s48, -2441216\n\t"
    "buffer_store_dwordx4 v[196:199], v139, %12, s48 offen sc1\n\t"
    "buffer_store_dwordx4 v[200:203], v140, %12, s48 offen sc1\n\t"
    "s_add_u32 s48, s48, 819200\n\t"
    "s_waitcnt lgkmcnt(8)\n\t"
    "buffer_store_dwordx4 v[72:75], v67, %12, s49 offen offset:384 sc1\n\t"
    "v_mov_b32 v129, 0xffff0000\n\t"
    "s_waitcnt lgkmcnt(4)\n\t"
    "v_perm_b32 v100, v100, v76, v126\n\t"
    "v_perm_b32 v101, v101, v77, v126\n\t"
    "v_perm_b32 v102, v102, v78, v126\n\t"
    "v_perm_b32 v103, v103, v79, v126\n\t"
    "s_waitcnt lgkmcnt(0)\n\t"
    "v_perm_b32 v104, v104, v80, v126\n\t"
    "v_perm_b32 v105, v105, v81, v126\n\t"
    "v_perm_b32 v106, v106, v82, v126\n\t"
    "v_perm_b32 v107, v107, v83, v126\n\t"
    "ds_write_b128 v141, v[204:207] offset:2112\n\t"
    "ds_write_b128 v141, v[208:211] offset:2368\n\t"
    "ds_read_b64_tr_b16 v[204:205], v142 offset:2112\n\t"
    "ds_read_b64_tr_b16 v[206:207], v142 offset:2368\n\t"
    "ds_read_b64_tr_b16 v[208:209], v142 offset:2144\n\t"
    "ds_read_b64_tr_b16 v[210:211], v142 offset:2400\n\t"
    "ds_write_b128 v141, v[212:215] offset:0\n\t"
    "ds_write_b128 v141, v[216:219] offset:256\n\t"
    "ds_read_b64_tr_b16 v[212:213], v142 offset:0\n\t"
    "ds_read_b64_tr_b16 v[214:215], v142 offset:256\n\t"
    "ds_read_b64_tr_b16 v[216:217], v142 offset:32\n\t"
    "ds_read_b64_tr_b16 v[218:219], v142 offset:288\n\t"
    "v_max_u32 v108, v100, v101\n\t"
    "v_min_u32 v101, v100, v101\n\t"
    "v_max_u32 v100, v102, v103\n\t"
    "v_min_u32 v103, v102, v103\n\t"
    "v_max_u32 v102, v104, v105\n\t"
    "v_min_u32 v105, v104, v105\n\t"
    "v_max_u32 v104, v106, v107\n\t"
    "v_min_u32 v107, v106, v107\n\t"
    "v_max_u32 v106, v108, v100\n\t"
    "v_min_u32 v100, v108, v100\n\t"
    "v_max_u32 v108, v101, v103\n\t"
    "v_min_u32 v103, v101, v103\n\t"
    "v_max_u32 v101, v102, v104\n\t"
    "v_min_u32 v104, v102, v104\n\t"
    "v_max_u32 v102, v105, v107\n\t"
    "v_min_u32 v107, v105, v107\n\t"
    "v_bfi_b32 v130, v106, 0, v129\n\t"
    "v_cmp_eq_u32 vcc, 0, v130\n\t"
    "v_and_or_b32 v92, v106, s44, v69\n\t"
    "s_nop 0\n\t"
    "v_cndmask_b32 v130, v130, v129, vcc\n\t"
    "v_ffbl_b32 v131, v130\n\t"
    "v_and_b32 v134, 7, v131\n\t"
    "v_lshlrev_b32 v134, v134, s45\n\t"
    "v_xor_b32 v129, v129, v134\n\t"
    "v_xor_b32 v132, v131, v127\n\t"
    "v_bfi_b32 v130, v101, 0, v129\n\t"
    "v_cmp_eq_u32 vcc, 0, v130\n\t"
    "v_and_or_b32 v93, v101, s44, v69\n\t"
    "v_xor_b32 v133, 23, v132\n\t"
    "v_lshlrev_b32 v110, v132, v136\n\t"
    "v_and_b32 v132, 7, v132\n\t"
    "v_lshl_or_b32 v110, v136, v133, v110\n\t"
    "v_lshl_add_u32 v132, v132, 1, v128\n\t"
    "ds_or_b32 v92, v110\n\t"
    "ds_write_b16 v132, v106\n\t"
    "v_cndmask_b32 v130, v130, v129, vcc\n\t"
    "v_ffbl_b32 v131, v130\n\t"
    "v_and_b32 v134, 7, v131\n\t"
    "v_lshlrev_b32 v134, v134, s45\n\t"
    "v_xor_b32 v129, v129, v134\n\t"
    "v_xor_b32 v132, v131, v127\n\t"
    "v_bfi_b32 v130, v108, 0, v129\n\t"
    "v_cmp_eq_u32 vcc, 0, v130\n\t"
    "v_and_or_b32 v94, v108, s44, v69\n\t"
    "v_xor_b32 v133, 23, v132\n\t"
    "v_lshlrev_b32 v111, v132, v136\n\t"
    "v_and_b32 v132, 7, v132\n\t"
    "v_lshl_or_b32 v111, v136, v133, v111\n\t"
    "v_lshl_add_u32 v132, v132, 1, v128\n\t"
    "ds_or_b32 v93, v111\n\t"
    "ds_write_b16 v132, v101\n\t"
    "v_cndmask_b32 v130, v130, v129, vcc\n\t"
    "v_ffbl_b32 v131, v130\n\t"
    "v_and_b32 v134, 7, v131\n\t"
    "v_lshlrev_b32 v134, v134, s45\n\t"
    "v_xor_b32 v129, v129, v134\n\t"
    "v_xor_b32 v132, v131, v127\n\t"
    "s_waitcnt lgkmcnt(10)\n\t"
    "buffer_store_dwordx4 v[204:207], v139, %12, s48 offen sc1\n\t"
    "buffer_store_dwordx4 v[208:211], v140, %12, s48 offen sc1\n\t"
    "s_add_u32 s48, s48, 819200\n\t"
    "s_waitcnt lgkmcnt(4)\n\t"
    "buffer_store_dwordx4 v[212:215], v139, %12, s48 offen sc1\n\t"
    "buffer_store_dwordx4 v[216:219], v140, %12, s48 offen sc1\n\t"
    "s_add_u32 s48, s48, 819200\n\t"
    "ds_write_b128 v141, v[48:51] offset:2112\n\t"
    "ds_write_b128 v141, v[52:55] offset:2368\n\t"
    "ds_read_b64_tr_b16 v[48:49], v142 offset:2112\n\t"
    "ds_read_b64_tr_b16 v[50:51], v142 offset:2368\n\t"
    "ds_read_b64_tr_b16 v[52:53], v142 offset:2144\n\t"
    "ds_read_b64_tr_b16 v[54:55], v142 offset:2400\n\t"
    "ds_write_b128 v141, v[220:223] offset:0\n\t"
    "ds_write_b128 v141, v[224:227] offset:256\n\t"
    "ds_read_b64_tr_b16 v[220:221], v142 offset:0\n\t"
    "ds_read_b64_tr_b16 v[222:223], v142 offset:256\n\t"
    "ds_read_b64_tr_b16 v[224:225], v142 offset:32\n\t"
    "ds_read_b64_tr_b16 v[226:227], v142 offset:288\n\t"
    "v_bfi_b32 v130, v100, 0, v129\n\t"
    "v_cmp_eq_u32 vcc, 0, v130\n\t"
    "v_and_or_b32 v95, v100, s44, v69\n\t"
    "v_xor_b32 v133, 23, v132\n\t"
    "v_lshlrev_b32 v112, v132, v136\n\t"
    "v_and_b32 v132, 7, v132\n\t"
    "v_lshl_or_b32 v112, v136, v133, v112\n\t"
    "v_lshl_add_u32 v132, v132, 1, v128\n\t"
    "ds_or_b32 v94, v112\n\t"
    "ds_write_b16 v132, v108\n\t"
    "v_cndmask_b32 v130, v130, v129, vcc\n\t"
    "v_ffbl_b32 v131, v130\n\t"
    "v_and_b32 v134, 7, v131\n\t"
    "v_lshlrev_b32 v134, v134, s45\n\t"
    "v_xor_b32 v129, v129, v134\n\t"
    "v_xor_b32 v132, v131, v127\n\t"
    "v_bfi_b32 v130, v102, 0, v129\n\t"
    "v_cmp_eq_u32 vcc, 0, v130\n\t"
    "v_and_or_b32 v96, v102, s44, v69\n\t"
    "v_xor_b32 v133, 23, v132\n\t"
    "v_lshlrev_b32 v113, v132, v136\n\t"
    "v_and_b32 v132, 7, v132\n\t"
    "v_lshl_or_b32 v113, v136, v133, v113\n\t"
    "v_lshl_add_u32 v132, v132, 1, v128\n\t"
    "ds_or_b32 v95, v113\n\t"
    "ds_write_b16 v132, v100\n\t"
    "v_cndmask_b32 v130, v130, v129, vcc\n\t"
    "v_ffbl_b32 v131, v130\n\t"
    "v_and_b32 v134, 7, v131\n\t"
    "v_lshlrev_b32 v134, v134, s45\n\t"
    "v_xor_b32 v129, v129, v134\n\t"
    "v_xor_b32 v132, v131, v127\n\t"
    "v_bfi_b32 v130, v104, 0, v129\n\t"
    "v_cmp_eq_u32 vcc, 0, v130\n\t"
    "v_and_or_b32 v97, v104, s44, v69\n\t"
    "v_xor_b32 v133, 23, v132\n\t"
    "v_lshlrev_b32 v114, v132, v136\n\t"
    "v_and_b32 v132, 7, v132\n\t"
    "v_lshl_or_b32 v114, v136, v133, v114\n\t"
    "v_lshl_add_u32 v132, v132, 1, v128\n\t"
    "ds_or_b32 v96, v114\n\t"
    "ds_write_b16 v132, v102\n\t"
    "v_cndmask_b32 v130, v130, v129, vcc\n\t"
    "v_ffbl_b32 v131, v130\n\t"
    "v_and_b32 v134, 7, v131\n\t"
    "v_lshlrev_b32 v134, v134, s45\n\t"
    "v_xor_b32 v129, v129, v134\n\t"
    "v_xor_b32 v132, v131, v127\n\t"
    "s_waitcnt lgkmcnt(12)\n\t"
    "buffer_store_dwordx4 v[48:51], v139, %12, s48 offen sc1\n\t"
    "buffer_store_dwordx4 v[52:55], v140, %12, s48 offen sc1\n\t"
    "s_add_u32 s48, s48, -2441216\n\t"
    "s_waitcnt lgkmcnt(6)\n\t"
    "s_and_b32 s53, %14, 12\n\t"
    "s_cselect_b32 s48, 0x40000000, s48\n\t"
    "buffer_store_dwordx4 v[220:223], v139, %12, s48 offen sc1\n\t"
    "buffer_store_dwordx4 v[224:227], v140, %12, s48 offen sc1\n\t"
    "s_add_u32 s48, s48, 819200\n\t"
    "ds_write_b128 v141, v[228:231] offset:2112\n\t"
    "ds_write_b128 v141, v[232:235] offset:2368\n\t"
    "ds_read_b64_tr_b16 v[228:229], v142 offset:2112\n\t"
    "ds_read_b64_tr_b16 v[230:231], v142 offset:2368\n\t"
    "ds_read_b64_tr_b16 v[232:233], v142 offset:2144\n\t"
    "ds_read_b64_tr_b16 v[234:235], v142 offset:2400\n\t"
    "ds_write_b128 v141, v[236:239] offset:0\n\t"
    "ds_write_b128 v141, v[240:243] offset:256\n\t"
    "ds_read_b64_tr_b16 v[236:237], v142 offset:0\n\t"
    "ds_read_b64_tr_b16 v[238:239], v142 offset:256\n\t"
    "ds_read_b64_tr_b16 v[240:241], v142 offset:32\n\t"
    "ds_read_b64_tr_b16 v[242:243], v142 offset:288\n\t"
    "v_bfi_b32 v130, v103, 0, v129\n\t"
    "v_cmp_eq_u32 vcc, 0, v130\n\t"
    "v_and_or_b32 v98, v103, s44, v69\n\t"
    "v_xor_b32 v133, 23, v132\n\t"
    "v_lshlrev_b32 v115, v132, v136\n\t"
    "v_and_b32 v132, 7, v132\n\t"
    "v_lshl_or_b32 v115, v136, v133, v115\n\t"
    "v_lshl_add_u32 v132, v132, 1, v128\n\t"
    "ds_or_b32 v97, v115\n\t"
    "ds_write_b16 v132, v104\n\t"
    "v_cndmask_b32 v130, v130, v129, vcc\n\t"
    "v_ffbl_b32 v131, v130\n\t"
    "v_and_b32 v134, 7, v131\n\t"
    "v_lshlrev_b32 v134, v134, s45\n\t"
    "v_xor_b32 v129, v129, v134\n\t"
    "v_xor_b32 v132, v131, v127\n\t"
    "v_bfi_b32 v130, v107, 0, v129\n\t"
    "v_cmp_eq_u32 vcc, 0, v130\n\t"
    "v_and_or_b32 v99, v107, s44, v69\n\t"
    "v_xor_b32 v133, 23, v132\n\t"
    "v_lshlrev_b32 v116, v132, v136\n\t"
    "v_and_b32 v132, 7, v132\n\t"
    "v_lshl_or_b32 v116, v136, v133, v116\n\t"
    "v_lshl_add_u32 v132, v132, 1, v128\n\t"
    "ds_or_b32 v98, v116\n\t"
    "ds_write_b16 v132, v103\n\t"
    "v_cndmask_b32 v130, v130, v129, vcc\n\t"
    "v_ffbl_b32 v131, v130\n\t"
    "v_xor_b32 v132, v131, v127\n\t"
    "v_xor_b32 v133, 23, v132\n\t"
    "v_lshlrev_b32 v117, v132, v136\n\t"
    "v_and_b32 v132, 7, v132\n\t"
    "v_lshl_or_b32 v117, v136, v133, v117\n\t"
    "v_lshl_add_u32 v132, v132, 1, v128\n\t"
    "ds_or_b32 v99, v117\n\t"
    "ds_write_b16 v132, v107\n\t"
    "ds_read_b128 v[72:75], v128\n\t"
    "v_and_or_b32 v100, v84, s44, v69\n\t"
    "ds_read_b32 v100, v100\n\t"
    "v_and_or_b32 v101, v85, s44, v69\n\t"
    "ds_read_b32 v101, v101\n\t"
    "v_and_or_b32 v102, v86, s44, v69\n\t"
    "ds_read_b32 v102, v102\n\t"
    "v_and_or_b32 v103, v87, s44, v69\n\t"
    "ds_read_b32 v103, v103\n\t"
    "v_and_or_b32 v104, v88, s44, v69\n\t"
    "ds_read_b32 v104, v104\n\t"
    "v_and_or_b32 v105, v89, s44, v69\n\t"
    "ds_read_b32 v105, v105\n\t"
    "v_and_or_b32 v106, v90, s44, v69\n\t"
    "ds_read_b32 v106, v106\n\t"
    "v_and_or_b32 v107, v91, s44, v69\n\t"
    "ds_read_b32 v107, v107\n\t"
    "v_lshl_add_u32 v76, v16, 3, s36\n\t"
    "v_lshl_add_u32 v77, v17, 3, s37\n\t"
    "v_lshl_add_u32 v78, v18, 3, s38\n\t"
    "v_lshl_add_u32 v79, v19, 3, s39\n\t"
    "v_lshl_add_u32 v80, v20, 3, s40\n\t"
    "v_lshl_add_u32 v81, v21, 3, s41\n\t"
    "v_lshl_add_u32 v82, v22, 3, s42\n\t"
    "v_lshl_add_u32 v83, v23, 3, s43\n\t"
    "s_waitcnt lgkmcnt(15)\n\t"
    "buffer_store_dwordx4 v[228:231], v139, %12, s48 offen sc1\n\t"
    "buffer_store_dwordx4 v[232:235], v140, %12, s48 offen sc1\n\t"
    "s_add_u32 s48, s48, 819200\n\t"
    "buffer_store_dwordx4 v[236:239], v139, %12, s48 offen sc1\n\t"
    "buffer_store_dwordx4 v[240:243], v140, %12, s48 offen sc1\n\t"
    "s_add_u32 s48, s48, 819200\n\t"
    "s_waitcnt lgkmcnt(8)\n\t"
    "buffer_store_dwordx4 v[72:75], v67, %12, s49 offen offset:416 sc1\n\t"
    "v_mov_b32 v129, 0xffff0000\n\t"
    "s_waitcnt lgkmcnt(4)\n\t"
    "v_perm_b32 v100, v100, v84, v126\n\t"
    "v_perm_b32 v101, v101, v85, v126\n\t"
    "v_perm_b32 v102, v102, v86, v126\n\t"
    "v_perm_b32 v103, v103, v87, v126\n\t"
    "s_waitcnt lgkmcnt(0)\n\t"
    "v_perm_b32 v104, v104, v88, v126\n\t"
    "v_perm_b32 v105, v105, v89, v126\n\t"
    "v_perm_b32 v106, v106, v90, v126\n\t"
    "v_perm_b32 v107, v107, v91, v126\n\t"
    "s_waitcnt vmcnt(31)\n\t"
    "ds_write_b128 v141, v[244:247] offset:2112\n\t"
    "ds_write_b128 v141, v[248:251] offset:2368\n\t"
    "ds_read_b64_tr_b16 v[244:245], v142 offset:2112\n\t"
    "ds_read_b64_tr_b16 v[246:247], v142 offset:2368\n\t"
    "ds_read_b64_tr_b16 v[248:249], v142 offset:2144\n\t"
    "ds_read_b64_tr_b16 v[250:251], v142 offset:2400\n\t"
    "v_max_u32 v108, v100, v101\n\t"
    "v_min_u32 v101, v100, v101\n\t"
    "v_max_u32 v100, v102, v103\n\t"
    "v_min_u32 v103, v102, v103\n\t"
    "v_max_u32 v102, v104, v105\n\t"
    "v_min_u32 v105, v104, v105\n\t"
    "v_max_u32 v104, v106, v107\n\t"
    "v_min_u32 v107, v106, v107\n\t"
    "v_max_u32 v106, v108, v100\n\t"
    "v_min_u32 v100, v108, v100\n\t"
    "v_max_u32 v108, v101, v103\n\t"
    "v_min_u32 v103, v101, v103\n\t"
    "v_max_u32 v101, v102, v104\n\t"
    "v_min_u32 v104, v102, v104\n\t"
    "v_max_u32 v102, v105, v107\n\t"
    "v_min_u32 v107, v105, v107\n\t"
    "v_bfi_b32 v130, v106, 0, v129\n\t"
    "v_cmp_eq_u32 vcc, 0, v130\n\t"
    "v_and_or_b32 v92, v106, s44, v69\n\t"
    "s_nop 0\n\t"
    "v_cndmask_b32 v130, v130, v129, vcc\n\t"
    "v_ffbl_b32 v131, v130\n\t"
    "v_and_b32 v134, 7, v131\n\t"
    "v_lshlrev_b32 v134, v134, s45\n\t"
    "v_xor_b32 v129, v129, v134\n\t"
    "v_xor_b32 v132, v131, v127\n\t"
    "v_bfi_b32 v130, v101, 0, v129\n\t"
    "v_cmp_eq_u32 vcc, 0, v130\n\t"
    "v_and_or_b32 v93, v101, s44, v69\n\t"
    "v_xor_b32 v133, 23, v132\n\t"
    "v_lshlrev_b32 v110, v132, v136\n\t"
    "v_and_b32 v132, 7, v132\n\t"
    "v_lshl_or_b32 v110, v136, v133, v110\n\t"
    "v_lshl_add_u32 v132, v132, 1, v128\n\t"
    "ds_or_b32 v92, v110\n\t"
    "ds_write_b16 v132, v106\n\t"
    "v_cndmask_b32 v130, v130, v129, vcc\n\t"
    "v_ffbl_b32 v131, v130\n\t"
    "v_and_b32 v134, 7, v131\n\t"
    "v_lshlrev_b32 v134, v134, s45\n\t"
    "v_xor_b32 v129, v129, v134\n\t"
    "v_xor_b32 v132, v131, v127\n\t"
    "v_bfi_b32 v130, v108, 0, v129\n\t"
    "v_cmp_eq_u32 vcc, 0, v130\n\t"
    "v_and_or_b32 v94, v108, s44, v69\n\t"
    "v_xor_b32 v133, 23, v132\n\t"
    "v_lshlrev_b32 v111, v132, v136\n\t"
    "v_and_b32 v132, 7, v132\n\t"
    "v_lshl_or_b32 v111, v136, v133, v111\n\t"
    "v_lshl_add_u32 v132, v132, 1, v128\n\t"
    "ds_or_b32 v93, v111\n\t"
    "ds_write_b16 v132, v101\n\t"
    "v_cndmask_b32 v130, v130, v129, vcc\n\t"
    "v_ffbl_b32 v131, v130\n\t"
    "v_and_b32 v134, 7, v131\n\t"
    "v_lshlrev_b32 v134, v134, s45\n\t"
    "v_xor_b32 v129, v129, v134\n\t"
    "v_xor_b32 v132, v131, v127\n\t"
    "s_waitcnt lgkmcnt(4)\n\t"
    "buffer_store_dwordx4 v[244:247], v139, %12, s48 offen sc1\n\t"
    "buffer_store_dwordx4 v[248:251], v140, %12, s48 offen sc1\n\t"
    "s_add_u32 s48, s48, -2441216\n\t"
    "v_bfi_b32 v130, v100, 0, v129\n\t"
    "v_cmp_eq_u32 vcc, 0, v130\n\t"
    "v_and_or_b32 v95, v100, s44, v69\n\t"
    "v_xor_b32 v133, 23, v132\n\t"
    "v_lshlrev_b32 v112, v132, v136\n\t"
    "v_and_b32 v132, 7, v132\n\t"
    "v_lshl_or_b32 v112, v136, v133, v112\n\t"
    "v_lshl_add_u32 v132, v132, 1, v128\n\t"
    "ds_or_b32 v94, v112\n\t"
    "ds_write_b16 v132, v108\n\t"
    "v_cndmask_b32 v130, v130, v129, vcc\n\t"
    "v_ffbl_b32 v131, v130\n\t"
    "v_and_b32 v134, 7, v131\n\t"
    "v_lshlrev_b32 v134, v134, s45\n\t"
    "v_xor_b32 v129, v129, v134\n\t"
    "v_xor_b32 v132, v131, v127\n\t"
    "v_bfi_b32 v130, v102, 0, v129\n\t"
    "v_cmp_eq_u32 vcc, 0, v130\n\t"
    "v_and_or_b32 v96, v102, s44, v69\n\t"
    "v_xor_b32 v133, 23, v132\n\t"
    "v_lshlrev_b32 v113, v132, v136\n\t"
    "v_and_b32 v132, 7, v132\n\t"
    "v_lshl_or_b32 v113, v136, v133, v113\n\t"
    "v_lshl_add_u32 v132, v132, 1, v128\n\t"
    "ds_or_b32 v95, v113\n\t"
    "ds_write_b16 v132, v100\n\t"
    "v_cndmask_b32 v130, v130, v129, vcc\n\t"
    "v_ffbl_b32 v131, v130\n\t"
    "v_and_b32 v134, 7, v131\n\t"
    "v_lshlrev_b32 v134, v134, s45\n\t"
    "v_xor_b32 v129, v129, v134\n\t"
    "v_xor_b32 v132, v131, v127\n\t"
    "v_bfi_b32 v130, v104, 0, v129\n\t"
    "v_cmp_eq_u32 vcc, 0, v130\n\t"
    "v_and_or_b32 v97, v104, s44, v69\n\t"
    "v_xor_b32 v133, 23, v132\n\t"
    "v_lshlrev_b32 v114, v132, v136\n\t"
    "v_and_b32 v132, 7, v132\n\t"
    "v_lshl_or_b32 v114, v136, v133, v114\n\t"
    "v_lshl_add_u32 v132, v132, 1, v128\n\t"
    "ds_or_b32 v96, v114\n\t"
    "ds_write_b16 v132, v102\n\t"
    "v_cndmask_b32 v130, v130, v129, vcc\n\t"
    "v_ffbl_b32 v131, v130\n\t"
    "v_and_b32 v134, 7, v131\n\t"
    "v_lshlrev_b32 v134, v134, s45\n\t"
    "v_xor_b32 v129, v129, v134\n\t"
    "v_xor_b32 v132, v131, v127\n\t"
    "v_bfi_b32 v130, v103, 0, v129\n\t"
    "v_cmp_eq_u32 vcc, 0, v130\n\t"
    "v_and_or_b32 v98, v103, s44, v69\n\t"
    "v_xor_b32 v133, 23, v132\n\t"
    "v_lshlrev_b32 v115, v132, v136\n\t"
    "v_and_b32 v132, 7, v132\n\t"
    "v_lshl_or_b32 v115, v136, v133, v115\n\t"
    "v_lshl_add_u32 v132, v132, 1, v128\n\t"
    "ds_or_b32 v97, v115\n\t"
    "ds_write_b16 v132, v104\n\t"
    "v_cndmask_b32 v130, v130, v129, vcc\n\t"
    "v_ffbl_b32 v131, v130\n\t"
    "v_and_b32 v134, 7, v131\n\t"
    "v_lshlrev_b32 v134, v134, s45\n\t"
    "v_xor_b32 v129, v129, v134\n\t"
    "v_xor_b32 v132, v131, v127\n\t"
    "v_bfi_b32 v130, v107, 0, v129\n\t"
    "v_cmp_eq_u32 vcc, 0, v130\n\t"
    "v_and_or_b32 v99, v107, s44, v69\n\t"
    "v_xor_b32 v133, 23, v132\n\t"
    "v_lshlrev_b32 v116, v132, v136\n\t"
    "v_and_b32 v132, 7, v132\n\t"
    "v_lshl_or_b32 v116, v136, v133, v116\n\t"
    "v_lshl_add_u32 v132, v132, 1, v128\n\t"
    "ds_or_b32 v98, v116\n\t"
    "ds_write_b16 v132, v103\n\t"
    "v_cndmask_b32 v130, v130, v129, vcc\n\t"
    "v_ffbl_b32 v131, v130\n\t"
    "v_xor_b32 v132, v131, v127\n\t"
    "v_xor_b32 v133, 23, v132\n\t"
    "v_lshlrev_b32 v117, v132, v136\n\t"
    "v_and_b32 v132, 7, v132\n\t"
    "v_lshl_or_b32 v117, v136, v133, v117\n\t"
    "v_lshl_add_u32 v132, v132, 1, v128\n\t"
    "ds_or_b32 v99, v117\n\t"
    "ds_write_b16 v132, v107\n\t"
    "ds_read_b128 v[72:75], v128\n\t"
    "v_and_or_b32 v100, v76, s44, v69\n\t"
    "ds_read_b32 v100, v100\n\t"
    "v_and_or_b32 v101, v77, s44, v69\n\t"
    "ds_read_b32 v101, v101\n\t"
    "v_and_or_b32 v102, v78, s44, v69\n\t"
    "ds_read_b32 v102, v102\n\t"
    "v_and_or_b32 v103, v79, s44, v69\n\t"
    "ds_read_b32 v103, v103\n\t"
    "v_and_or_b32 v104, v80, s44, v69\n\t"
    "ds_read_b32 v104, v104\n\t"
    "v_and_or_b32 v105, v81, s44, v69\n\t"
    "ds_read_b32 v105, v105\n\t"
    "v_and_or_b32 v106, v82, s44, v69\n\t"
    "ds_read_b32 v106, v106\n\t"
    "v_and_or_b32 v107, v83, s44, v69\n\t"
    "ds_read_b32 v107, v107\n\t"
    "s_waitcnt lgkmcnt(8)\n\t"
    "buffer_store_dwordx4 v[72:75], v67, %12, s49 offen offset:448 sc1\n\t"
    "v_mov_b32 v129, 0xffff0000\n\t"
    "s_waitcnt lgkmcnt(4)\n\t"
    "v_perm_b32 v100, v100, v76, v126\n\t"
    "v_perm_b32 v101, v101, v77, v126\n\t"
    "v_perm_b32 v102, v102, v78, v126\n\t"
    "v_perm_b32 v103, v103, v79, v126\n\t"
    "s_waitcnt lgkmcnt(0)\n\t"
    "v_perm_b32 v104, v104, v80, v126\n\t"
    "v_perm_b32 v105, v105, v81, v126\n\t"
    "v_perm_b32 v106, v106, v82, v126\n\t"
    "v_perm_b32 v107, v107, v83, v126\n\t"
    "v_max_u32 v108, v100, v101\n\t"
    "v_min_u32 v101, v100, v101\n\t"
    "v_max_u32 v100, v102, v103\n\t"
    "v_min_u32 v103, v102, v103\n\t"
    "v_max_u32 v102, v104, v105\n\t"
    "v_min_u32 v105, v104, v105\n\t"
    "v_max_u32 v104, v106, v107\n\t"
    "v_min_u32 v107, v106, v107\n\t"
    "v_max_u32 v106, v108, v100\n\t"
    "v_min_u32 v100, v108, v100\n\t"
    "v_max_u32 v108, v101, v103\n\t"
    "v_min_u32 v103, v101, v103\n\t"
    "v_max_u32 v101, v102, v104\n\t"
    "v_min_u32 v104, v102, v104\n\t"
    "v_max_u32 v102, v105, v107\n\t"
    "v_min_u32 v107, v105, v107\n\t"
    "v_bfi_b32 v130, v106, 0, v129\n\t"
    "v_cmp_eq_u32 vcc, 0, v130\n\t"
    "v_and_or_b32 v92, v106, s44, v69\n\t"
    "s_nop 0\n\t"
    "v_cndmask_b32 v130, v130, v129, vcc\n\t"
    "v_ffbl_b32 v131, v130\n\t"
    "v_and_b32 v134, 7, v131\n\t"
    "v_lshlrev_b32 v134, v134, s45\n\t"
    "v_xor_b32 v129, v129, v134\n\t"
    "v_xor_b32 v132, v131, v127\n\t"
    "v_bfi_b32 v130, v101, 0, v129\n\t"
    "v_cmp_eq_u32 vcc, 0, v130\n\t"
    "v_and_b32 v132, 7, v132\n\t"
    "v_lshl_add_u32 v132, v132, 1, v128\n\t"
    "ds_write_b16 v132, v106\n\t"
    "v_cndmask_b32 v130, v130, v129, vcc\n\t"
    "v_ffbl_b32 v131, v130\n\t"
    "v_and_b32 v134, 7, v131\n\t"
    "v_lshlrev_b32 v134, v134, s45\n\t"
    "v_xor_b32 v129, v129, v134\n\t"
    "v_xor_b32 v132, v131, v127\n\t"
    "v_bfi_b32 v130, v108, 0, v129\n\t"
    "v_cmp_eq_u32 vcc, 0, v130\n\t"
    "v_and_b32 v132, 7, v132\n\t"
    "v_lshl_add_u32 v132, v132, 1, v128\n\t"
    "ds_write_b16 v132, v101\n\t"
    "v_cndmask_b32 v130, v130, v129, vcc\n\t"
    "v_ffbl_b32 v131, v130\n\t"
    "v_and_b32 v134, 7, v131\n\t"
    "v_lshlrev_b32 v134, v134, s45\n\t"
    "v_xor_b32 v129, v129, v134\n\t"
    "v_xor_b32 v132, v131, v127\n\t"
    "v_bfi_b32 v130, v100, 0, v129\n\t"
    "v_cmp_eq_u32 vcc, 0, v130\n\t"
    "v_and_b32 v132, 7, v132\n\t"
    "v_lshl_add_u32 v132, v132, 1, v128\n\t"
    "ds_write_b16 v132, v108\n\t"
    "v_cndmask_b32 v130, v130, v129, vcc\n\t"
    "v_ffbl_b32 v131, v130\n\t"
    "v_and_b32 v134, 7, v131\n\t"
    "v_lshlrev_b32 v134, v134, s45\n\t"
    "v_xor_b32 v129, v129, v134\n\t"
    "v_xor_b32 v132, v131, v127\n\t"
    "v_bfi_b32 v130, v102, 0, v129\n\t"
    "v_cmp_eq_u32 vcc, 0, v130\n\t"
    "v_and_b32 v132, 7, v132\n\t"
    "v_lshl_add_u32 v132, v132, 1, v128\n\t"
    "ds_write_b16 v132, v100\n\t"
    "v_cndmask_b32 v130, v130, v129, vcc\n\t"
    "v_ffbl_b32 v131, v130\n\t"
    "v_and_b32 v134, 7, v131\n\t"
    "v_lshlrev_b32 v134, v134, s45\n\t"
    "v_xor_b32 v129, v129, v134\n\t"
    "v_xor_b32 v132, v131, v127\n\t"
    "v_bfi_b32 v130, v104, 0, v129\n\t"
    "v_cmp_eq_u32 vcc, 0, v130\n\t"
    "v_and_b32 v132, 7, v132\n\t"
    "v_lshl_add_u32 v132, v132, 1, v128\n\t"
    "ds_write_b16 v132, v102\n\t"
    "v_cndmask_b32 v130, v130, v129, vcc\n\t"
    "v_ffbl_b32 v131, v130\n\t"
    "v_and_b32 v134, 7, v131\n\t"
    "v_lshlrev_b32 v134, v134, s45\n\t"
    "v_xor_b32 v129, v129, v134\n\t"
    "v_xor_b32 v132, v131, v127\n\t"
    "v_bfi_b32 v130, v103, 0, v129\n\t"
    "v_cmp_eq_u32 vcc, 0, v130\n\t"
    "v_and_b32 v132, 7, v132\n\t"
    "v_lshl_add_u32 v132, v132, 1, v128\n\t"
    "ds_write_b16 v132, v104\n\t"
    "v_cndmask_b32 v130, v130, v129, vcc\n\t"
    "v_ffbl_b32 v131, v130\n\t"
    "v_and_b32 v134, 7, v131\n\t"
    "v_lshlrev_b32 v134, v134, s45\n\t"
    "v_xor_b32 v129, v129, v134\n\t"
    "v_xor_b32 v132, v131, v127\n\t"
    "v_bfi_b32 v130, v107, 0, v129\n\t"
    "v_cmp_eq_u32 vcc, 0, v130\n\t"
    "v_and_b32 v132, 7, v132\n\t"
    "v_lshl_add_u32 v132, v132, 1, v128\n\t"
    "ds_write_b16 v132, v103\n\t"
    "v_cndmask_b32 v130, v130, v129, vcc\n\t"
    "v_ffbl_b32 v131, v130\n\t"
    "v_xor_b32 v132, v131, v127\n\t"
    "v_and_b32 v132, 7, v132\n\t"
    "v_lshl_add_u32 v132, v132, 1, v128\n\t"
    "ds_write_b16 v132, v107\n\t"
    "ds_read_b128 v[72:75], v128\n\t"
    "s_waitcnt lgkmcnt(0)\n\t"
    "buffer_store_dwordx4 v[72:75], v67, %12, s49 offen offset:480 sc1\n\t"
    "s_setprio 0\n\t"
    "v_mov_b32 v134, 1036\n\t"
    "L_asvsp12d_%=:\n\t"
    "ds_read_b32 v130, v134\n\t"
    "s_waitcnt lgkmcnt(0)\n\t"
    "v_readfirstlane_b32 s50, v130\n\t"
    "s_nop 0\n\t"
    "s_cmp_lt_u32 s50, 4\n\t"
    "s_cbranch_scc1 L_asvsp12d_%=\n\t"
    "s_call_b64 s[98:99], L_setup_%=\n\t"
    "v_lshrrev_b32 v105, 4, v127\n\t"
    "v_and_b32 v105, 48, v105\n\t"
    "s_movk_i32 s65, 130\n\t"
    "v_mad_u32_u24 v104, v155, s65, v105\n\t"
    "v_add_u32 v105, 14170, v104\n\t"
    "v_and_b32 v104, -16, v104\n\t"
    "v_and_b32 v105, -16, v105\n\t"
    "ds_read_b128 v[164:167], v104 offset:0\n\t"
    "ds_read_b128 v[168:171], v104 offset:64\n\t"
    "ds_read_b128 v[172:175], v104 offset:2080\n\t"
    "ds_read_b128 v[176:179], v104 offset:2144\n\t"
    "ds_read_b128 v[180:183], v104 offset:4160\n\t"
    "ds_read_b128 v[184:187], v104 offset:4224\n\t"
    "ds_read_b128 v[188:191], v104 offset:6240\n\t"
    "ds_read_b128 v[192:195], v104 offset:6304\n\t"
    "ds_read_b128 v[196:199], v104 offset:8320\n\t"
    "ds_read_b128 v[200:203], v104 offset:8384\n\t"
    "ds_read_b128 v[204:207], v104 offset:10400\n\t"
    "ds_read_b128 v[208:211], v104 offset:10464\n\t"
    "s_cmp_lt_u32 s62, 2\n\t"
    "s_cbranch_scc1 L_mel0_%=\n\t"
    "buffer_load_dwordx4 v[220:223], v127, %12, s61 offen offset:2048 sc1\n\t"
    "buffer_load_dwordx4 v[224:227], v127, %12, s61 offen offset:3072 sc1\n\t"
    "s_add_u32 s61, s61, 0x1000\n\t"
    "buffer_load_dwordx4 v[228:231], v127, %12, s61 offen offset:2048 sc1\n\t"
    "buffer_load_dwordx4 v[232:235], v127, %12, s61 offen offset:3072 sc1\n\t"
    "s_add_u32 s61, s61, 0x1000\n\t"
    "buffer_load_dwordx4 v[236:239], v127, %12, s61 offen offset:2048 sc1\n\t"
    "buffer_load_dwordx4 v[240:243], v127, %12, s61 offen offset:3072 sc1\n\t"
    "s_add_u32 s61, s61, 0x1000\n\t"
    "buffer_load_dwordx4 v[244:247], v127, %12, s61 offen offset:2048 sc1\n\t"
    "buffer_load_dwordx4 v[248:251], v127, %12, s61 offen offset:3072 sc1\n\t"
    "s_add_u32 s61, s61, 0x1000\n\t"
    "buffer_load_dwordx4 v[252:255], v127, %12, s61 offen offset:2048 sc1\n\t"
    "s_mov_b32 s62, 1\n\t"
    "L_mel0_%=:\n\t"
    "buffer_load_dwordx4 v[0:3], v127, %12, s63 offen offset:0 sc1\n\t"
    "buffer_load_dwordx4 v[4:7], v127, %12, s63 offen offset:1024 sc1\n\t"
    "buffer_load_dwordx4 v[8:11], v127, %12, s63 offen offset:2048 sc1\n\t"
    "buffer_load_dwordx4 v[12:15], v127, %12, s63 offen offset:3072 sc1\n\t"
    "s_add_u32 s63, s63, 0x1000\n\t"
    "s_mov_b32 s62, 0\n\t"
    "s_and_b32 s65, %14, 8\n\t"
    "s_lshl_b32 s65, s65, 10\n\t"
    "s_add_u32 s65, s65, s63\n\t"
    "s_mov_b32 m0, 136192\n\t"
    "s_add_u32 s65, s65, 0\n\t"
    "buffer_load_dwordx4 v127, %12, s65 offen offset:0 sc1 lds\n\t"
    "buffer_load_dwordx4 v127, %12, s65 offen offset:1024 sc1 lds\n\t"
    "buffer_load_dwordx4 v127, %12, s65 offen offset:2048 sc1 lds\n\t"
    "buffer_load_dwordx4 v127, %12, s65 offen offset:3072 sc1 lds\n\t"
    "s_mov_b32 m0, 140288\n\t"
    "s_add_u32 s65, s65, 4096\n\t"
    "buffer_load_dwordx4 v127, %12, s65 offen offset:0 sc1 lds\n\t"
    "buffer_load_dwordx4 v127, %12, s65 offen offset:1024 sc1 lds\n\t"
    "buffer_load_dwordx4 v127, %12, s65 offen offset:2048 sc1 lds\n\t"
    "buffer_load_dwordx4 v127, %12, s65 offen offset:3072 sc1 lds\n\t"
    "s_waitcnt vmcnt(21)\n\t"
    "s_lshr_b32 s50, %14, 6\n\t"
    "s_lshl_b32 s50, s50, 7\n\t"
    "s_add_u32 s50, s50, 49287168\n\t"
    "s_mov_b64 exec, 1\n\t"
    "buffer_atomic_add v136, off, %12, s50 sc1\n\t"
    "s_mov_b64 exec, -1\n\t"
    "s_branch L_af1x_%=\n\t"
    "L_af1w_%=:\n\t"
    "s_mov_b32 s51, 1048576\n\t"
    "s_mov_b64 exec, 0xff\n\t"
    "L_af1_%=:\n\t"
    "v_cmp_gt_u32 vcc, v148, v71\n\t"
    "s_cbranch_vccz L_af1ok_%=\n\t"
    "s_sleep 8\n\t"
    "buffer_load_dword v71, v70, %12, s52 offen sc1\n\t"
    "s_sub_u32 s51, s51, 1\n\t"
    "s_waitcnt vmcnt(0)\n\t"
    "s_cmp_lg_u32 s51, 0\n\t"
    "s_cbranch_scc1 L_af1_%=\n\t"
    "L_af1ok_%=:\n\t"
    "s_mov_b64 exec, -1\n\t"
    "v_mov_b32 v134, 3112\n\t"
    "ds_write_b32 v134, v136\n\t"
    "v_mbcnt_lo_u32_b32 v70, -1, 0\n\t"
    "v_mbcnt_hi_u32_b32 v70, -1, v70\n\t"
    "s_and_b32 s50, %14, 7\n\t"
    "s_lshl_b32 s50, s50, 4\n\t"
    "s_lshr_b32 s51, %14, 4\n\t"
    "s_add_u32 s50, s50, s51\n\t"
    "s_mul_i32 s50, s50, 102400\n\t"
    "s_add_u32 s50, s50, 3407872\n\t"
    "v_lshlrev_b32 v148, 7, v70\n\t"
    "buffer_load_dword v71, v148, %12, s50 offen sc1\n\t"
    "s_and_b32 s51, %14, 8\n\t"
    "s_lshl_b32 s51, s51, 10\n\t"
    "s_add_u32 s50, s50, s51\n\t"
    "s_add_u32 s50, s50, 0x1000\n\t"
    "v_lshrrev_b32 v149, 4, v70\n\t"
    "v_lshl_add_u32 v149, v149, 11, v148\n\t"
    "buffer_load_dword v71, v148, %12, s50 offen sc1\n\t"
    "s_bfe_u32 s50, %14, 0x40003\n\t"
    "s_lshl_b32 s50, s50, 14\n\t"
    "s_add_u32 s50, s50, 16517120\n\t"
    "v_lshrrev_b32 v150, 3, v70\n\t"
    "v_lshl_add_u32 v150, v150, 10, v148\n\t"
    "buffer_load_dword v71, v149, %12, s50 offen sc1\n\t"
    "s_and_b32 s50, %14, 8\n\t"
    "s_bfe_u32 s51, %14, 0x30004\n\t"
    "s_add_u32 s50, s50, s51\n\t"
    "s_mul_i32 s50, s50, 212992\n\t"
    "s_add_u32 s50, s50, 196608\n\t"
    "buffer_load_dword v71, v150, %12, s50 offen sc1\n\t"
    "s_setpc_b64 s[54:55]\n\t"
    "L_af1x_%=:\n\t"
    "s_mov_b32 s69, 0x40000000\n\t"
    "s_branch L_aend2_%=\n\t"
    "L_ahelp_%=:\n\t"
    "s_setprio 3\n\t"
    "v_cmp_gt_u32 vcc, 48, v147\n\t"
    "v_mov_b32 v133, 0x40000000\n\t"
    "v_lshrrev_b32 v134, 3, v147\n\t"
    "v_add_u32 v134, 6, v134\n\t"
    "v_bfe_u32 v135, v147, 2, 1\n\t"
    "v_and_b32 v130, 3, v147\n\t"
    "v_lshl_add_u32 v132, v135, 5, v130\n\t"
    "v_mul_u32_u24 v64, 400, v132\n\t"
    "v_lshl_add_u32 v64, v134, 5, v64\n\t"
    "s_movk_i32 s50, 0\n\t"
    "buffer_load_dwordx4 v[0:3], v64, %0, s50 offen offset:16\n\t"
    "buffer_load_dwordx4 v[4:7], v64, %0, s50 offen offset:32\n\t"
    "v_lshlrev_b32 v66, 14, v134\n\t"
    "v_lshl_add_u32 v66, v132, 4, v66\n\t"
    "v_lshlrev_b32 v68, 8, v134\n\t"
    "v_lshl_add_u32 v68, v135, 2, v68\n\t"
    "v_add_u32 v68, 130048, v68\n\t"
    "v_cndmask_b32 v68, v133, v68, vcc\n\t"
    "v_cndmask_b32 v66, v133, v66, vcc\n\t"
    "s_barrier\n\t"
    "s_load_dwordx2 s[72:73], %6, 0\n\t"
    "s_load_dwordx2 s[88:89], %7, 0\n\t"
    "s_load_dword s91, %8, 0\n\t"
    "v_lshlrev_b32 v128, 4, v147\n\t"
    "v_add_u32 v128, 133888, v128\n\t"
    "v_and_b32 v130, 1, v147\n\t"
    "v_cndmask_b32 v128, v133, v128, vcc\n\t"
    "v_mul_u32_u24 v127, 7, v130\n\t"
    "v_cmp_eq_u32 vcc, 1, v130\n\t"
    "v_mov_b32 v126, 0x07060100\n\t"
    "v_mov_b32 v133, 0x05040100\n\t"
    "v_mov_b32 v136, 1\n\t"
    "v_cndmask_b32 v126, v126, v133, vcc\n\t"
    "s_mov_b32 s36, 0\n\t"
    "s_mov_b32 s37, 8000\n\t"
    "s_mov_b32 s38, 16000\n\t"
    "s_mov_b32 s39, 24000\n\t"
    "s_mov_b32 s40, 32000\n\t"
    "s_mov_b32 s41, 40000\n\t"
    "s_mov_b32 s42, 48000\n\t"
    "s_mov_b32 s43, 56000\n\t"
    "s_movk_i32 s44, 0xf8\n\t"
    "s_mov_b32 s45, 0x01010000\n\t"
    "s_mov_b32 s46, 0x020c000c\n\t"
    "s_lshr_b32 s49, %14, 4\n\t"
    "s_mul_i32 s49, s49, 212992\n\t"
    "s_and_b32 s50, %14, 15\n\t"
    "s_lshl_b32 s50, s50, 10\n\t"
    "s_add_u32 s49, s49, s50\n\t"
    "s_lshl_b32 s52, %14, 2\n\t"
    "s_add_u32 s52, s52, 49291264\n\t"
    "buffer_load_dword v70, off, %12, s52 offset:1024 sc1\n\t"
    "s_cmp_eq_u32 %14, 0\n\t"
    "s_cselect_b32 s47, 49291264, 0x40000000\n\t"
    "s_cselect_b32 s48, 49283072, 0x40000000\n\t"
    "v_lshlrev_b32 v152, 4, v147\n\t"
    "v_lshlrev_b32 v132, 7, v147\n\t"
    "v_mov_b32 v71, 0\n\t"
    "buffer_store_dword v71, v132, %12, s48 offen sc1\n\t"
    "v_mov_b32 v148, 0\n\t"
    "v_mov_b32 v149, 0\n\t"
    "v_mov_b32 v150, 0\n\t"
    "v_mov_b32 v151, 0\n\t"
    "buffer_store_dwordx4 v[148:151], v152, %12, s47 offen offset:2048 sc1\n\t"
    "buffer_store_dwordx4 v[148:151], v152, %12, s47 offen offset:3072 sc1\n\t"
    "s_add_u32 s50, s48, 0x1000\n\t"
    "s_waitcnt vmcnt(6)\n\t"
    "v_lshlrev_b32 v134, 20, v147\n\t"
    "v_add_u32 v134, 2068, v134\n\t"
    "ds_add_u32 v134, v136\n\t"
    "v_mov_b32 v110, 65664\n\t"
    "v_mov_b32 v111, 131136\n\t"
    "v_mov_b32 v112, 262176\n\t"
    "v_mov_b32 v113, 524304\n\t"
    "v_mov_b32 v114, 1048584\n\t"
    "v_mov_b32 v115, 2097156\n\t"
    "v_mov_b32 v116, 4194306\n\t"
    "v_mov_b32 v117, 8388609\n\t"
    "s_waitcnt vmcnt(4)\n\t"
    "v_lshl_add_u32 v76, v0, 3, s36\n\t"
    "v_lshl_add_u32 v77, v1, 3, s37\n\t"
    "v_lshl_add_u32 v78, v2, 3, s38\n\t"
    "v_lshl_add_u32 v79, v3, 3, s39\n\t"
    "v_lshl_add_u32 v80, v4, 3, s40\n\t"
    "v_lshl_add_u32 v81, v5, 3, s41\n\t"
    "v_lshl_add_u32 v82, v6, 3, s42\n\t"
    "v_lshl_add_u32 v83, v7, 3, s43\n\t"
    "v_and_or_b32 v92, v76, s44, v68\n\t"
    "ds_or_rtn_b32 v118, v92, v110\n\t"
    "v_and_or_b32 v93, v77, s44, v68\n\t"
    "ds_or_rtn_b32 v119, v93, v111\n\t"
    "v_and_or_b32 v94, v78, s44, v68\n\t"
    "ds_or_rtn_b32 v120, v94, v112\n\t"
    "v_and_or_b32 v95, v79, s44, v68\n\t"
    "ds_or_rtn_b32 v121, v95, v113\n\t"
    "v_and_or_b32 v96, v80, s44, v68\n\t"
    "ds_or_rtn_b32 v122, v96, v114\n\t"
    "v_and_or_b32 v97, v81, s44, v68\n\t"
    "ds_or_rtn_b32 v123, v97, v115\n\t"
    "v_and_or_b32 v98, v82, s44, v68\n\t"
    "ds_or_rtn_b32 v124, v98, v116\n\t"
    "v_and_or_b32 v99, v83, s44, v68\n\t"
    "ds_or_rtn_b32 v125, v99, v117\n\t"
    "v_lshl_or_b32 v164, v77, 16, v76\n\t"
    "v_lshl_or_b32 v165, v79, 16, v78\n\t"
    "v_lshl_or_b32 v166, v81, 16, v80\n\t"
    "v_lshl_or_b32 v167, v83, 16, v82\n\t"
    "s_movk_i32 s50, 0\n\t"
    "buffer_load_dwordx4 v[8:11], v64, %0, s50 offen offset:1616\n\t"
    "buffer_load_dwordx4 v[12:15], v64, %0, s50 offen offset:1632\n\t"
    "s_movk_i32 s50, 3200\n\t"
    "buffer_load_dwordx4 v[16:19], v64, %0, s50 offen offset:16\n\t"
    "buffer_load_dwordx4 v[20:23], v64, %0, s50 offen offset:32\n\t"
    "s_waitcnt vmcnt(7)\n\t"
    "v_add_u32 v70, 1, v70\n\t"
    "buffer_store_dword v70, off, %12, s52 sc1\n\t"
    "v_mov_b32 v71, 2072\n\t"
    "ds_write_b32 v71, v70\n\t"
    "s_waitcnt vmcnt(3)\n\t"
    "v_lshl_add_u32 v84, v8, 3, s36\n\t"
    "v_lshl_add_u32 v85, v9, 3, s37\n\t"
    "v_lshl_add_u32 v86, v10, 3, s38\n\t"
    "v_lshl_add_u32 v87, v11, 3, s39\n\t"
    "v_lshl_add_u32 v88, v12, 3, s40\n\t"
    "v_lshl_add_u32 v89, v13, 3, s41\n\t"
    "v_lshl_add_u32 v90, v14, 3, s42\n\t"
    "v_lshl_add_u32 v91, v15, 3, s43\n\t"
    "s_waitcnt lgkmcnt(5)\n\t"
    "v_and_b32 v118, v118, v110\n\t"
    "v_perm_b32 v118, v118, v118, s46\n\t"
    "ds_or_b32 v92, v118\n\t"
    "v_and_b32 v119, v119, v111\n\t"
    "v_perm_b32 v119, v119, v119, s46\n\t"
    "ds_or_b32 v93, v119\n\t"
    "v_and_b32 v120, v120, v112\n\t"
    "v_perm_b32 v120, v120, v120, s46\n\t"
    "ds_or_b32 v94, v120\n\t"
    "v_and_b32 v121, v121, v113\n\t"
    "v_perm_b32 v121, v121, v121, s46\n\t"
    "ds_or_b32 v95, v121\n\t"
    "s_waitcnt lgkmcnt(5)\n\t"
    "v_and_b32 v122, v122, v114\n\t"
    "v_perm_b32 v122, v122, v122, s46\n\t"
    "ds_or_b32 v96, v122\n\t"
    "v_and_b32 v123, v123, v115\n\t"
    "v_perm_b32 v123, v123, v123, s46\n\t"
    "ds_or_b32 v97, v123\n\t"
    "v_and_b32 v124, v124, v116\n\t"
    "v_perm_b32 v124, v124, v124, s46\n\t"
    "ds_or_b32 v98, v124\n\t"
    "v_and_b32 v125, v125, v117\n\t"
    "v_perm_b32 v125, v125, v125, s46\n\t"
    "ds_or_b32 v99, v125\n\t"
    "buffer_store_dwordx4 v[164:167], v66, %12, s49 offen offset:0 sc1\n\t"
    "v_and_or_b32 v100, v84, s44, v68\n\t"
    "ds_read_b32 v100, v100\n\t"
    "v_and_or_b32 v101, v85, s44, v68\n\t"
    "ds_read_b32 v101, v101\n\t"
    "v_and_or_b32 v102, v86, s44, v68\n\t"
    "ds_read_b32 v102, v102\n\t"
    "v_and_or_b32 v103, v87, s44, v68\n\t"
    "ds_read_b32 v103, v103\n\t"
    "v_and_or_b32 v104, v88, s44, v68\n\t"
    "ds_read_b32 v104, v104\n\t"
    "v_and_or_b32 v105, v89, s44, v68\n\t"
    "ds_read_b32 v105, v105\n\t"
    "v_and_or_b32 v106, v90, s44, v68\n\t"
    "ds_read_b32 v106, v106\n\t"
    "v_and_or_b32 v107, v91, s44, v68\n\t"
    "ds_read_b32 v107, v107\n\t"
    "s_waitcnt vmcnt(2)\n\t"
    "v_lshl_add_u32 v76, v16, 3, s36\n\t"
    "v_lshl_add_u32 v77, v17, 3, s37\n\t"
    "v_lshl_add_u32 v78, v18, 3, s38\n\t"
    "v_lshl_add_u32 v79, v19, 3, s39\n\t"
    "v_lshl_add_u32 v80, v20, 3, s40\n\t"
    "v_lshl_add_u32 v81, v21, 3, s41\n\t"
    "v_lshl_add_u32 v82, v22, 3, s42\n\t"
    "v_lshl_add_u32 v83, v23, 3, s43\n\t"
    "s_movk_i32 s50, 3200\n\t"
    "buffer_load_dwordx4 v[24:27], v64, %0, s50 offen offset:1616\n\t"
    "buffer_load_dwordx4 v[28:31], v64, %0, s50 offen offset:1632\n\t"
    "s_movk_i32 s50, 6400\n\t"
    "buffer_load_dwordx4 v[32:35], v64, %0, s50 offen offset:16\n\t"
    "buffer_load_dwordx4 v[36:39], v64, %0, s50 offen offset:32\n\t"
    "s_cmp_lg_u32 %14, 0\n\t"
    "s_cbranch_scc1 L_ahe184_%=\n\t"
    "buffer_load_dwordx4 v[148:151], v152, %12, s47 offen sc1\n\t"
    "L_ahe184_%=:\n\t"
    "v_mov_b32 v129, 0xffff0000\n\t"
    "s_waitcnt lgkmcnt(4)\n\t"
    "v_perm_b32 v100, v100, v84, v126\n\t"
    "v_perm_b32 v101, v101, v85, v126\n\t"
    "v_perm_b32 v102, v102, v86, v126\n\t"
    "v_perm_b32 v103, v103, v87, v126\n\t"
    "s_waitcnt lgkmcnt(0)\n\t"
    "v_perm_b32 v104, v104, v88, v126\n\t"
    "v_perm_b32 v105, v105, v89, v126\n\t"
    "v_perm_b32 v106, v106, v90, v126\n\t"
    "v_perm_b32 v107, v107, v91, v126\n\t"
    "v_max_u32 v108, v100, v101\n\t"
    "v_min_u32 v101, v100, v101\n\t"
    "v_max_u32 v100, v102, v103\n\t"
    "v_min_u32 v103, v102, v103\n\t"
    "v_max_u32 v102, v104, v105\n\t"
    "v_min_u32 v105, v104, v105\n\t"
    "v_max_u32 v104, v106, v107\n\t"
    "v_min_u32 v107, v106, v107\n\t"
    "v_max_u32 v106, v108, v100\n\t"
    "v_min_u32 v100, v108, v100\n\t"
    "v_max_u32 v108, v101, v103\n\t"
    "v_min_u32 v103, v101, v103\n\t"
    "v_max_u32 v101, v102, v104\n\t"
    "v_min_u32 v104, v102, v104\n\t"
    "v_max_u32 v102, v105, v107\n\t"
    "v_min_u32 v107, v105, v107\n\t"
    "v_bfi_b32 v130, v106, 0, v129\n\t"
    "v_cmp_eq_u32 vcc, 0, v130\n\t"
    "v_and_or_b32 v92, v106, s44, v68\n\t"
    "s_nop 0\n\t"
    "v_cndmask_b32 v130, v130, v129, vcc\n\t"
    "v_ffbl_b32 v131, v130\n\t"
    "v_and_b32 v134, 7, v131\n\t"
    "v_lshlrev_b32 v134, v134, s45\n\t"
    "v_xor_b32 v129, v129, v134\n\t"
    "v_xor_b32 v132, v131, v127\n\t"
    "v_bfi_b32 v130, v101, 0, v129\n\t"
    "v_cmp_eq_u32 vcc, 0, v130\n\t"
    "v_and_or_b32 v93, v101, s44, v68\n\t"
    "v_xor_b32 v133, 23, v132\n\t"
    "v_lshlrev_b32 v110, v132, v136\n\t"
    "v_and_b32 v132, 7, v132\n\t"
    "v_lshl_or_b32 v110, v136, v133, v110\n\t"
    "v_lshl_add_u32 v132, v132, 1, v128\n\t"
    "ds_or_rtn_b32 v118, v92, v110\n\t"
    "ds_write_b16 v132, v106\n\t"
    "v_cndmask_b32 v130, v130, v129, vcc\n\t"
    "v_ffbl_b32 v131, v130\n\t"
    "v_and_b32 v134, 7, v131\n\t"
    "v_lshlrev_b32 v134, v134, s45\n\t"
    "v_xor_b32 v129, v129, v134\n\t"
    "v_xor_b32 v132, v131, v127\n\t"
    "v_bfi_b32 v130, v108, 0, v129\n\t"
    "v_cmp_eq_u32 vcc, 0, v130\n\t"
    "v_and_or_b32 v94, v108, s44, v68\n\t"
    "v_xor_b32 v133, 23, v132\n\t"
    "v_lshlrev_b32 v111, v132, v136\n\t"
    "v_and_b32 v132, 7, v132\n\t"
    "v_lshl_or_b32 v111, v136, v133, v111\n\t"
    "v_lshl_add_u32 v132, v132, 1, v128\n\t"
    "ds_or_rtn_b32 v119, v93, v111\n\t"
    "ds_write_b16 v132, v101\n\t"
    "v_cndmask_b32 v130, v130, v129, vcc\n\t"
    "v_ffbl_b32 v131, v130\n\t"
    "v_and_b32 v134, 7, v131\n\t"
    "v_lshlrev_b32 v134, v134, s45\n\t"
    "v_xor_b32 v129, v129, v134\n\t"
    "v_xor_b32 v132, v131, v127\n\t"
    "v_bfi_b32 v130, v100, 0, v129\n\t"
    "v_cmp_eq_u32 vcc, 0, v130\n\t"
    "v_and_or_b32 v95, v100, s44, v68\n\t"
    "v_xor_b32 v133, 23, v132\n\t"
    "v_lshlrev_b32 v112, v132, v136\n\t"
    "v_and_b32 v132, 7, v132\n\t"
    "v_lshl_or_b32 v112, v136, v133, v112\n\t"
    "v_lshl_add_u32 v132, v132, 1, v128\n\t"
    "ds_or_rtn_b32 v120, v94, v112\n\t"
    "ds_write_b16 v132, v108\n\t"
    "v_cndmask_b32 v130, v130, v129, vcc\n\t"
    "v_ffbl_b32 v131, v130\n\t"
    "v_and_b32 v134, 7, v131\n\t"
    "v_lshlrev_b32 v134, v134, s45\n\t"
    "v_xor_b32 v129, v129, v134\n\t"
    "v_xor_b32 v132, v131, v127\n\t"
    "v_bfi_b32 v130, v102, 0, v129\n\t"
    "v_cmp_eq_u32 vcc, 0, v130\n\t"
    "v_and_or_b32 v96, v102, s44, v68\n\t"
    "v_xor_b32 v133, 23, v132\n\t"
    "v_lshlrev_b32 v113, v132, v136\n\t"
    "v_and_b32 v132, 7, v132\n\t"
    "v_lshl_or_b32 v113, v136, v133, v113\n\t"
    "v_lshl_add_u32 v132, v132, 1, v128\n\t"
    "ds_or_rtn_b32 v121, v95, v113\n\t"
    "ds_write_b16 v132, v100\n\t"
    "v_cndmask_b32 v130, v130, v129, vcc\n\t"
    "v_ffbl_b32 v131, v130\n\t"
    "v_and_b32 v134, 7, v131\n\t"
    "v_lshlrev_b32 v134, v134, s45\n\t"
    "v_xor_b32 v129, v129, v134\n\t"
    "v_xor_b32 v132, v131, v127\n\t"
    "s_waitcnt lgkmcnt(7)\n\t"
    "v_and_b32 v118, v118, v110\n\t"
    "v_perm_b32 v118, v118, v118, s46\n\t"
    "ds_or_b32 v92, v118\n\t"
    "v_bfi_b32 v130, v104, 0, v129\n\t"
    "v_cmp_eq_u32 vcc, 0, v130\n\t"
    "v_and_or_b32 v97, v104, s44, v68\n\t"
    "v_xor_b32 v133, 23, v132\n\t"
    "v_lshlrev_b32 v114, v132, v136\n\t"
    "v_and_b32 v132, 7, v132\n\t"
    "v_lshl_or_b32 v114, v136, v133, v114\n\t"
    "v_lshl_add_u32 v132, v132, 1, v128\n\t"
    "ds_or_rtn_b32 v122, v96, v114\n\t"
    "ds_write_b16 v132, v102\n\t"
    "v_cndmask_b32 v130, v130, v129, vcc\n\t"
    "v_ffbl_b32 v131, v130\n\t"
    "v_and_b32 v134, 7, v131\n\t"
    "v_lshlrev_b32 v134, v134, s45\n\t"
    "v_xor_b32 v129, v129, v134\n\t"
    "v_xor_b32 v132, v131, v127\n\t"
    "s_waitcnt lgkmcnt(8)\n\t"
    "v_and_b32 v119, v119, v111\n\t"
    "v_perm_b32 v119, v119, v119, s46\n\t"
    "ds_or_b32 v93, v119\n\t"
    "v_bfi_b32 v130, v103, 0, v129\n\t"
    "v_cmp_eq_u32 vcc, 0, v130\n\t"
    "v_and_or_b32 v98, v103, s44, v68\n\t"
    "v_xor_b32 v133, 23, v132\n\t"
    "v_lshlrev_b32 v115, v132, v136\n\t"
    "v_and_b32 v132, 7, v132\n\t"
    "v_lshl_or_b32 v115, v136, v133, v115\n\t"
    "v_lshl_add_u32 v132, v132, 1, v128\n\t"
    "ds_or_rtn_b32 v123, v97, v115\n\t"
    "ds_write_b16 v132, v104\n\t"
    "v_cndmask_b32 v130, v130, v129, vcc\n\t"
    "v_ffbl_b32 v131, v130\n\t"
    "v_and_b32 v134, 7, v131\n\t"
    "v_lshlrev_b32 v134, v134, s45\n\t"
    "v_xor_b32 v129, v129, v134\n\t"
    "v_xor_b32 v132, v131, v127\n\t"
    "s_waitcnt lgkmcnt(9)\n\t"
    "v_and_b32 v120, v120, v112\n\t"
    "v_perm_b32 v120, v120, v120, s46\n\t"
    "ds_or_b32 v94, v120\n\t"
    "v_bfi_b32 v130, v107, 0, v129\n\t"
    "v_cmp_eq_u32 vcc, 0, v130\n\t"
    "v_and_or_b32 v99, v107, s44, v68\n\t"
    "v_xor_b32 v133, 23, v132\n\t"
    "v_lshlrev_b32 v116, v132, v136\n\t"
    "v_and_b32 v132, 7, v132\n\t"
    "v_lshl_or_b32 v116, v136, v133, v116\n\t"
    "v_lshl_add_u32 v132, v132, 1, v128\n\t"
    "ds_or_rtn_b32 v124, v98, v116\n\t"
    "ds_write_b16 v132, v103\n\t"
    "v_cndmask_b32 v130, v130, v129, vcc\n\t"
    "v_ffbl_b32 v131, v130\n\t"
    "v_xor_b32 v132, v131, v127\n\t"
    "s_waitcnt lgkmcnt(10)\n\t"
    "v_and_b32 v121, v121, v113\n\t"
    "v_perm_b32 v121, v121, v121, s46\n\t"
    "ds_or_b32 v95, v121\n\t"
    "v_xor_b32 v133, 23, v132\n\t"
    "v_lshlrev_b32 v117, v132, v136\n\t"
    "v_and_b32 v132, 7, v132\n\t"
    "v_lshl_or_b32 v117, v136, v133, v117\n\t"
    "v_lshl_add_u32 v132, v132, 1, v128\n\t"
    "ds_or_rtn_b32 v125, v99, v117\n\t"
    "ds_write_b16 v132, v107\n\t"
    "s_waitcnt lgkmcnt(10)\n\t"
    "v_and_b32 v122, v122, v114\n\t"
    "v_perm_b32 v122, v122, v122, s46\n\t"
    "ds_or_b32 v96, v122\n\t"
    "ds_read_b128 v[72:75], v128\n\t"
    "v_and_or_b32 v100, v76, s44, v68\n\t"
    "ds_read_b32 v100, v100\n\t"
    "v_and_or_b32 v101, v77, s44, v68\n\t"
    "ds_read_b32 v101, v101\n\t"
    "v_and_or_b32 v102, v78, s44, v68\n\t"
    "ds_read_b32 v102, v102\n\t"
    "v_and_or_b32 v103, v79, s44, v68\n\t"
    "ds_read_b32 v103, v103\n\t"
    "v_and_or_b32 v104, v80, s44, v68\n\t"
    "ds_read_b32 v104, v104\n\t"
    "v_and_or_b32 v105, v81, s44, v68\n\t"
    "ds_read_b32 v105, v105\n\t"
    "v_and_or_b32 v106, v82, s44, v68\n\t"
    "ds_read_b32 v106, v106\n\t"
    "v_and_or_b32 v107, v83, s44, v68\n\t"
    "ds_read_b32 v107, v107\n\t"
    "s_cmp_lg_u32 %14, 0\n\t"
    "s_cbranch_scc1 L_ahe378_%=\n\t"
    "s_waitcnt vmcnt(0)\n\t"
    "buffer_store_dwordx4 v[148:151], v152, %12, s47 offen offset:1024 sc1\n\t"
    "L_ahe378_%=:\n\t"
    "s_waitcnt vmcnt(2)\n\t"
    "v_lshl_add_u32 v84, v24, 3, s36\n\t"
    "v_lshl_add_u32 v85, v25, 3, s37\n\t"
    "v_lshl_add_u32 v86, v26, 3, s38\n\t"
    "v_lshl_add_u32 v87, v27, 3, s39\n\t"
    "v_lshl_add_u32 v88, v28, 3, s40\n\t"
    "v_lshl_add_u32 v89, v29, 3, s41\n\t"
    "v_lshl_add_u32 v90, v30, 3, s42\n\t"
    "v_lshl_add_u32 v91, v31, 3, s43\n\t"
    "v_mul_u32_u24 v58, 400, v147\n\t"
    "buffer_load_dwordx4 v[48:51], v58, %0, 0 offen\n\t"
    "s_lshr_b32 s50, %14, 5\n\t"
    "s_mul_i32 s50, s50, 125\n\t"
    "s_bfe_u32 s51, %14, 0x30002\n\t"
    "s_lshl_b32 s51, s51, 4\n\t"
    "s_min_u32 s51, s51, 109\n\t"
    "s_add_u32 s50, s50, s51\n\t"
    "s_lshl_b32 s50, s50, 8\n\t"
    "s_and_b32 s51, %14, 3\n\t"
    "s_lshl_b32 s51, s51, 6\n\t"
    "s_add_u32 s50, s50, s51\n\t"
    "s_bitcmp1_b32 %14, 1\n\t"
    "s_cselect_b32 s50, s50, 0x40000000\n\t"
    "v_and_b32 v59, 48, v147\n\t"
    "v_and_b32 v60, 15, v147\n\t"
    "v_lshl_add_u32 v59, v60, 8, v59\n\t"
    "buffer_load_dwordx4 v[52:55], v59, %15, s50 offen\n\t"
    "s_cmp_lg_u32 %14, 0\n\t"
    "s_cbranch_scc1 L_ahe410_%=\n\t"
    "buffer_load_dwordx4 v[148:151], v152, %12, s47 offen sc1\n\t"
    "L_ahe410_%=:\n\t"
    "s_waitcnt lgkmcnt(11)\n\t"
    "v_and_b32 v123, v123, v115\n\t"
    "v_perm_b32 v123, v123, v123, s46\n\t"
    "ds_or_b32 v97, v123\n\t"
    "v_and_b32 v124, v124, v116\n\t"
    "v_perm_b32 v124, v124, v124, s46\n\t"
    "ds_or_b32 v98, v124\n\t"
    "v_and_b32 v125, v125, v117\n\t"
    "v_perm_b32 v125, v125, v125, s46\n\t"
    "ds_or_b32 v99, v125\n\t"
    "s_waitcnt lgkmcnt(11)\n\t"
    "buffer_store_dwordx4 v[72:75], v66, %12, s49 offen offset:64 sc1\n\t"
    "v_mov_b32 v129, 0xffff0000\n\t"
    "s_waitcnt lgkmcnt(7)\n\t"
    "v_perm_b32 v100, v100, v76, v126\n\t"
    "v_perm_b32 v101, v101, v77, v126\n\t"
    "v_perm_b32 v102, v102, v78, v126\n\t"
    "v_perm_b32 v103, v103, v79, v126\n\t"
    "s_waitcnt lgkmcnt(3)\n\t"
    "v_perm_b32 v104, v104, v80, v126\n\t"
    "v_perm_b32 v105, v105, v81, v126\n\t"
    "v_perm_b32 v106, v106, v82, v126\n\t"
    "v_perm_b32 v107, v107, v83, v126\n\t"
    "v_max_u32 v108, v100, v101\n\t"
    "v_min_u32 v101, v100, v101\n\t"
    "v_max_u32 v100, v102, v103\n\t"
    "v_min_u32 v103, v102, v103\n\t"
    "v_max_u32 v102, v104, v105\n\t"
    "v_min_u32 v105, v104, v105\n\t"
    "v_max_u32 v104, v106, v107\n\t"
    "v_min_u32 v107, v106, v107\n\t"
    "v_max_u32 v106, v108, v100\n\t"
    "v_min_u32 v100, v108, v100\n\t"
    "v_max_u32 v108, v101, v103\n\t"
    "v_min_u32 v103, v101, v103\n\t"
    "v_max_u32 v101, v102, v104\n\t"
    "v_min_u32 v104, v102, v104\n\t"
    "v_max_u32 v102, v105, v107\n\t"
    "v_min_u32 v107, v105, v107\n\t"
    "v_bfi_b32 v130, v106, 0, v129\n\t"
    "v_cmp_eq_u32 vcc, 0, v130\n\t"
    "v_and_or_b32 v92, v106, s44, v68\n\t"
    "s_nop 0\n\t"
    "v_cndmask_b32 v130, v130, v129, vcc\n\t"
    "v_ffbl_b32 v131, v130\n\t"
    "v_and_b32 v134, 7, v131\n\t"
    "v_lshlrev_b32 v134, v134, s45\n\t"
    "v_xor_b32 v129, v129, v134\n\t"
    "v_xor_b32 v132, v131, v127\n\t"
    "v_bfi_b32 v130, v101, 0, v129\n\t"
    "v_cmp_eq_u32 vcc, 0, v130\n\t"
    "v_and_or_b32 v93, v101, s44, v68\n\t"
    "v_xor_b32 v133, 23, v132\n\t"
    "v_lshlrev_b32 v110, v132, v136\n\t"
    "v_and_b32 v132, 7, v132\n\t"
    "v_lshl_or_b32 v110, v136, v133, v110\n\t"
    "v_lshl_add_u32 v132, v132, 1, v128\n\t"
    "ds_or_rtn_b32 v118, v92, v110\n\t"
    "ds_write_b16 v132, v106\n\t"
    "v_cndmask_b32 v130, v130, v129, vcc\n\t"
    "v_ffbl_b32 v131, v130\n\t"
    "v_and_b32 v134, 7, v131\n\t"
    "v_lshlrev_b32 v134, v134, s45\n\t"
    "v_xor_b32 v129, v129, v134\n\t"
    "v_xor_b32 v132, v131, v127\n\t"
    "v_bfi_b32 v130, v108, 0, v129\n\t"
    "v_cmp_eq_u32 vcc, 0, v130\n\t"
    "v_and_or_b32 v94, v108, s44, v68\n\t"
    "v_xor_b32 v133, 23, v132\n\t"
    "v_lshlrev_b32 v111, v132, v136\n\t"
    "v_and_b32 v132, 7, v132\n\t"
    "v_lshl_or_b32 v111, v136, v133, v111\n\t"
    "v_lshl_add_u32 v132, v132, 1, v128\n\t"
    "ds_or_rtn_b32 v119, v93, v111\n\t"
    "ds_write_b16 v132, v101\n\t"
    "v_cndmask_b32 v130, v130, v129, vcc\n\t"
    "v_ffbl_b32 v131, v130\n\t"
    "v_and_b32 v134, 7, v131\n\t"
    "v_lshlrev_b32 v134, v134, s45\n\t"
    "v_xor_b32 v129, v129, v134\n\t"
    "v_xor_b32 v132, v131, v127\n\t"
    "v_bfi_b32 v130, v100, 0, v129\n\t"
    "v_cmp_eq_u32 vcc, 0, v130\n\t"
    "v_and_or_b32 v95, v100, s44, v68\n\t"
    "v_xor_b32 v133, 23, v132\n\t"
    "v_lshlrev_b32 v112, v132, v136\n\t"
    "v_and_b32 v132, 7, v132\n\t"
    "v_lshl_or_b32 v112, v136, v133, v112\n\t"
    "v_lshl_add_u32 v132, v132, 1, v128\n\t"
    "ds_or_rtn_b32 v120, v94, v112\n\t"
    "ds_write_b16 v132, v108\n\t"
    "v_cndmask_b32 v130, v130, v129, vcc\n\t"
    "v_ffbl_b32 v131, v130\n\t"
    "v_and_b32 v134, 7, v131\n\t"
    "v_lshlrev_b32 v134, v134, s45\n\t"
    "v_xor_b32 v129, v129, v134\n\t"
    "v_xor_b32 v132, v131, v127\n\t"
    "v_bfi_b32 v130, v102, 0, v129\n\t"
    "v_cmp_eq_u32 vcc, 0, v130\n\t"
    "v_and_or_b32 v96, v102, s44, v68\n\t"
    "v_xor_b32 v133, 23, v132\n\t"
    "v_lshlrev_b32 v113, v132, v136\n\t"
    "v_and_b32 v132, 7, v132\n\t"
    "v_lshl_or_b32 v113, v136, v133, v113\n\t"
    "v_lshl_add_u32 v132, v132, 1, v128\n\t"
    "ds_or_rtn_b32 v121, v95, v113\n\t"
    "ds_write_b16 v132, v100\n\t"
    "v_cndmask_b32 v130, v130, v129, vcc\n\t"
    "v_ffbl_b32 v131, v130\n\t"
    "v_and_b32 v134, 7, v131\n\t"
    "v_lshlrev_b32 v134, v134, s45\n\t"
    "v_xor_b32 v129, v129, v134\n\t"
    "v_xor_b32 v132, v131, v127\n\t"
    "s_waitcnt lgkmcnt(7)\n\t"
    "v_and_b32 v118, v118, v110\n\t"
    "v_perm_b32 v118, v118, v118, s46\n\t"
    "ds_or_b32 v92, v118\n\t"
    "v_bfi_b32 v130, v104, 0, v129\n\t"
    "v_cmp_eq_u32 vcc, 0, v130\n\t"
    "v_and_or_b32 v97, v104, s44, v68\n\t"
    "v_xor_b32 v133, 23, v132\n\t"
    "v_lshlrev_b32 v114, v132, v136\n\t"
    "v_and_b32 v132, 7, v132\n\t"
    "v_lshl_or_b32 v114, v136, v133, v114\n\t"
    "v_lshl_add_u32 v132, v132, 1, v128\n\t"
    "ds_or_rtn_b32 v122, v96, v114\n\t"
    "ds_write_b16 v132, v102\n\t"
    "v_cndmask_b32 v130, v130, v129, vcc\n\t"
    "v_ffbl_b32 v131, v130\n\t"
    "v_and_b32 v134, 7, v131\n\t"
    "v_lshlrev_b32 v134, v134, s45\n\t"
    "v_xor_b32 v129, v129, v134\n\t"
    "v_xor_b32 v132, v131, v127\n\t"
    "s_waitcnt lgkmcnt(8)\n\t"
    "v_and_b32 v119, v119, v111\n\t"
    "v_perm_b32 v119, v119, v119, s46\n\t"
    "ds_or_b32 v93, v119\n\t"
    "v_bfi_b32 v130, v103, 0, v129\n\t"
    "v_cmp_eq_u32 vcc, 0, v130\n\t"
    "v_and_or_b32 v98, v103, s44, v68\n\t"
    "v_xor_b32 v133, 23, v132\n\t"
    "v_lshlrev_b32 v115, v132, v136\n\t"
    "v_and_b32 v132, 7, v132\n\t"
    "v_lshl_or_b32 v115, v136, v133, v115\n\t"
    "v_lshl_add_u32 v132, v132, 1, v128\n\t"
    "ds_or_rtn_b32 v123, v97, v115\n\t"
    "ds_write_b16 v132, v104\n\t"
    "v_cndmask_b32 v130, v130, v129, vcc\n\t"
    "v_ffbl_b32 v131, v130\n\t"
    "v_and_b32 v134, 7, v131\n\t"
    "v_lshlrev_b32 v134, v134, s45\n\t"
    "v_xor_b32 v129, v129, v134\n\t"
    "v_xor_b32 v132, v131, v127\n\t"
    "s_waitcnt lgkmcnt(9)\n\t"
    "v_and_b32 v120, v120, v112\n\t"
    "v_perm_b32 v120, v120, v120, s46\n\t"
    "ds_or_b32 v94, v120\n\t"
    "v_bfi_b32 v130, v107, 0, v129\n\t"
    "v_cmp_eq_u32 vcc, 0, v130\n\t"
    "v_and_or_b32 v99, v107, s44, v68\n\t"
    "v_xor_b32 v133, 23, v132\n\t"
    "v_lshlrev_b32 v116, v132, v136\n\t"
    "v_and_b32 v132, 7, v132\n\t"
    "v_lshl_or_b32 v116, v136, v133, v116\n\t"
    "v_lshl_add_u32 v132, v132, 1, v128\n\t"
    "ds_or_rtn_b32 v124, v98, v116\n\t"
    "ds_write_b16 v132, v103\n\t"
    "v_cndmask_b32 v130, v130, v129, vcc\n\t"
    "v_ffbl_b32 v131, v130\n\t"
    "v_xor_b32 v132, v131, v127\n\t"
    "s_waitcnt lgkmcnt(10)\n\t"
    "v_and_b32 v121, v121, v113\n\t"
    "v_perm_b32 v121, v121, v121, s46\n\t"
    "ds_or_b32 v95, v121\n\t"
    "v_xor_b32 v133, 23, v132\n\t"
    "v_lshlrev_b32 v117, v132, v136\n\t"
    "v_and_b32 v132, 7, v132\n\t"
    "v_lshl_or_b32 v117, v136, v133, v117\n\t"
    "v_lshl_add_u32 v132, v132, 1, v128\n\t"
    "ds_or_rtn_b32 v125, v99, v117\n\t"
    "ds_write_b16 v132, v107\n\t"
    "s_waitcnt lgkmcnt(10)\n\t"
    "v_and_b32 v122, v122, v114\n\t"
    "v_perm_b32 v122, v122, v122, s46\n\t"
    "ds_or_b32 v96, v122\n\t"
    "ds_read_b128 v[72:75], v128\n\t"
    "v_and_or_b32 v100, v84, s44, v68\n\t"
    "ds_read_b32 v100, v100\n\t"
    "v_and_or_b32 v101, v85, s44, v68\n\t"
    "ds_read_b32 v101, v101\n\t"
    "v_and_or_b32 v102, v86, s44, v68\n\t"
    "ds_read_b32 v102, v102\n\t"
    "v_and_or_b32 v103, v87, s44, v68\n\t"
    "ds_read_b32 v103, v103\n\t"
    "v_and_or_b32 v104, v88, s44, v68\n\t"
    "ds_read_b32 v104, v104\n\t"
    "v_and_or_b32 v105, v89, s44, v68\n\t"
    "ds_read_b32 v105, v105\n\t"
    "v_and_or_b32 v106, v90, s44, v68\n\t"
    "ds_read_b32 v106, v106\n\t"
    "v_and_or_b32 v107, v91, s44, v68\n\t"
    "ds_read_b32 v107, v107\n\t"
    "s_cmp_lg_u32 %14, 0\n\t"
    "s_cbranch_scc1 L_ahe616_%=\n\t"
    "s_waitcnt vmcnt(0)\n\t"
    "buffer_store_dwordx4 v[148:151], v152, %12, s47 offen offset:1024 sc1\n\t"
    "L_ahe616_%=:\n\t"
    "s_waitcnt vmcnt(3)\n\t"
    "v_lshl_add_u32 v76, v32, 3, s36\n\t"
    "v_lshl_add_u32 v77, v33, 3, s37\n\t"
    "v_lshl_add_u32 v78, v34, 3, s38\n\t"
    "v_lshl_add_u32 v79, v35, 3, s39\n\t"
    "v_lshl_add_u32 v80, v36, 3, s40\n\t"
    "v_lshl_add_u32 v81, v37, 3, s41\n\t"
    "v_lshl_add_u32 v82, v38, 3, s42\n\t"
    "v_lshl_add_u32 v83, v39, 3, s43\n\t"
    "s_waitcnt vmcnt(1)\n\t"
    "v_lshl_add_u32 v48, v48, 3, s36\n\t"
    "v_lshl_add_u32 v49, v49, 3, s37\n\t"
    "v_lshl_add_u32 v50, v50, 3, s38\n\t"
    "v_lshl_add_u32 v51, v51, 3, s39\n\t"
    "v_lshl_or_b32 v56, v49, 16, v48\n\t"
    "v_lshl_or_b32 v57, v51, 16, v50\n\t"
    "s_and_b32 s51, %14, 1\n\t"
    "s_mul_i32 s51, s51, 1016\n\t"
    "s_sub_u32 s50, s49, s51\n\t"
    "s_add_u32 s50, s50, 196608\n\t"
    "buffer_store_dwordx2 v[56:57], v152, %12, s50 offen sc1\n\t"
    "s_lshl_b32 s50, %14, 10\n\t"
    "s_add_u32 s50, s50, 16515072\n\t"
    "s_bitcmp1_b32 %14, 1\n\t"
    "s_cselect_b32 s50, s50, 0x40000000\n\t"
    "buffer_store_dwordx4 v[52:55], v152, %12, s50 offen sc1\n\t"
    "s_mul_i32 s50, %14, 69632\n\t"
    "s_add_u32 s50, s50, 31522816\n\t"
    "v_mov_b32 v60, 0\n\t"
    "v_lshlrev_b32 v61, 3, v152\n\t"
    "s_mov_b64 exec, 0xffffffff\n\t"
    "buffer_store_dword v60, v61, %12, s50 offen sc1\n\t"
    "s_lshl_b32 s50, %14, 7\n\t"
    "s_add_u32 s50, s50, 49295360\n\t"
    "v_lshrrev_b32 v62, 2, v152\n\t"
    "buffer_store_dword v60, v62, %12, s50 offen sc1\n\t"
    "s_mov_b64 exec, -1\n\t"
    "s_cmp_lg_u32 %14, 0\n\t"
    "s_cbranch_scc1 L_ahe658_%=\n\t"
    "buffer_load_dwordx4 v[148:151], v152, %12, s47 offen sc1\n\t"
    "L_ahe658_%=:\n\t"
    "s_waitcnt lgkmcnt(11)\n\t"
    "v_and_b32 v123, v123, v115\n\t"
    "v_perm_b32 v123, v123, v123, s46\n\t"
    "ds_or_b32 v97, v123\n\t"
    "v_and_b32 v124, v124, v116\n\t"
    "v_perm_b32 v124, v124, v124, s46\n\t"
    "ds_or_b32 v98, v124\n\t"
    "v_and_b32 v125, v125, v117\n\t"
    "v_perm_b32 v125, v125, v125, s46\n\t"
    "ds_or_b32 v99, v125\n\t"
    "s_waitcnt lgkmcnt(11)\n\t"
    "buffer_store_dwordx4 v[72:75], v66, %12, s49 offen offset:128 sc1\n\t"
    "v_mov_b32 v129, 0xffff0000\n\t"
    "s_waitcnt lgkmcnt(7)\n\t"
    "v_perm_b32 v100, v100, v84, v126\n\t"
    "v_perm_b32 v101, v101, v85, v126\n\t"
    "v_perm_b32 v102, v102, v86, v126\n\t"
    "v_perm_b32 v103, v103, v87, v126\n\t"
    "s_waitcnt lgkmcnt(3)\n\t"
    "v_perm_b32 v104, v104, v88, v126\n\t"
    "v_perm_b32 v105, v105, v89, v126\n\t"
    "v_perm_b32 v106, v106, v90, v126\n\t"
    "v_perm_b32 v107, v107, v91, v126\n\t"
    "v_max_u32 v108, v100, v101\n\t"
    "v_min_u32 v101, v100, v101\n\t"
    "v_max_u32 v100, v102, v103\n\t"
    "v_min_u32 v103, v102, v103\n\t"
    "v_max_u32 v102, v104, v105\n\t"
    "v_min_u32 v105, v104, v105\n\t"
    "v_max_u32 v104, v106, v107\n\t"
    "v_min_u32 v107, v106, v107\n\t"
    "v_max_u32 v106, v108, v100\n\t"
    "v_min_u32 v100, v108, v100\n\t"
    "v_max_u32 v108, v101, v103\n\t"
    "v_min_u32 v103, v101, v103\n\t"
    "v_max_u32 v101, v102, v104\n\t"
    "v_min_u32 v104, v102, v104\n\t"
    "v_max_u32 v102, v105, v107\n\t"
    "v_min_u32 v107, v105, v107\n\t"
    "v_bfi_b32 v130, v106, 0, v129\n\t"
    "v_cmp_eq_u32 vcc, 0, v130\n\t"
    "v_and_or_b32 v92, v106, s44, v68\n\t"
    "s_nop 0\n\t"
    "v_cndmask_b32 v130, v130, v129, vcc\n\t"
    "v_ffbl_b32 v131, v130\n\t"
    "v_and_b32 v134, 7, v131\n\t"
    "v_lshlrev_b32 v134, v134, s45\n\t"
    "v_xor_b32 v129, v129, v134\n\t"
    "v_xor_b32 v132, v131, v127\n\t"
    "v_bfi_b32 v130, v101, 0, v129\n\t"
    "v_cmp_eq_u32 vcc, 0, v130\n\t"
    "v_and_or_b32 v93, v101, s44, v68\n\t"
    "v_xor_b32 v133, 23, v132\n\t"
    "v_lshlrev_b32 v110, v132, v136\n\t"
    "v_and_b32 v132, 7, v132\n\t"
    "v_lshl_or_b32 v110, v136, v133, v110\n\t"
    "v_lshl_add_u32 v132, v132, 1, v128\n\t"
    "ds_or_rtn_b32 v118, v92, v110\n\t"
    "ds_write_b16 v132, v106\n\t"
    "v_cndmask_b32 v130, v130, v129, vcc\n\t"
    "v_ffbl_b32 v131, v130\n\t"
    "v_and_b32 v134, 7, v131\n\t"
    "v_lshlrev_b32 v134, v134, s45\n\t"
    "v_xor_b32 v129, v129, v134\n\t"
    "v_xor_b32 v132, v131, v127\n\t"
    "v_bfi_b32 v130, v108, 0, v129\n\t"
    "v_cmp_eq_u32 vcc, 0, v130\n\t"
    "v_and_or_b32 v94, v108, s44, v68\n\t"
    "v_xor_b32 v133, 23, v132\n\t"
    "v_lshlrev_b32 v111, v132, v136\n\t"
    "v_and_b32 v132, 7, v132\n\t"
    "v_lshl_or_b32 v111, v136, v133, v111\n\t"
    "v_lshl_add_u32 v132, v132, 1, v128\n\t"
    "ds_or_rtn_b32 v119, v93, v111\n\t"
    "ds_write_b16 v132, v101\n\t"
    "v_cndmask_b32 v130, v130, v129, vcc\n\t"
    "v_ffbl_b32 v131, v130\n\t"
    "v_and_b32 v134, 7, v131\n\t"
    "v_lshlrev_b32 v134, v134, s45\n\t"
    "v_xor_b32 v129, v129, v134\n\t"
    "v_xor_b32 v132, v131, v127\n\t"
    "v_bfi_b32 v130, v100, 0, v129\n\t"
    "v_cmp_eq_u32 vcc, 0, v130\n\t"
    "v_and_or_b32 v95, v100, s44, v68\n\t"
    "v_xor_b32 v133, 23, v132\n\t"
    "v_lshlrev_b32 v112, v132, v136\n\t"
    "v_and_b32 v132, 7, v132\n\t"
    "v_lshl_or_b32 v112, v136, v133, v112\n\t"
    "v_lshl_add_u32 v132, v132, 1, v128\n\t"
    "ds_or_rtn_b32 v120, v94, v112\n\t"
    "ds_write_b16 v132, v108\n\t"
    "v_cndmask_b32 v130, v130, v129, vcc\n\t"
    "v_ffbl_b32 v131, v130\n\t"
    "v_and_b32 v134, 7, v131\n\t"
    "v_lshlrev_b32 v134, v134, s45\n\t"
    "v_xor_b32 v129, v129, v134\n\t"
    "v_xor_b32 v132, v131, v127\n\t"
    "v_bfi_b32 v130, v102, 0, v129\n\t"
    "v_cmp_eq_u32 vcc, 0, v130\n\t"
    "v_and_or_b32 v96, v102, s44, v68\n\t"
    "v_xor_b32 v133, 23, v132\n\t"
    "v_lshlrev_b32 v113, v132, v136\n\t"
    "v_and_b32 v132, 7, v132\n\t"
    "v_lshl_or_b32 v113, v136, v133, v113\n\t"
    "v_lshl_add_u32 v132, v132, 1, v128\n\t"
    "ds_or_rtn_b32 v121, v95, v113\n\t"
    "ds_write_b16 v132, v100\n\t"
    "v_cndmask_b32 v130, v130, v129, vcc\n\t"
    "v_ffbl_b32 v131, v130\n\t"
    "v_and_b32 v134, 7, v131\n\t"
    "v_lshlrev_b32 v134, v134, s45\n\t"
    "v_xor_b32 v129, v129, v134\n\t"
    "v_xor_b32 v132, v131, v127\n\t"
    "s_waitcnt lgkmcnt(7)\n\t"
    "v_and_b32 v118, v118, v110\n\t"
    "v_perm_b32 v118, v118, v118, s46\n\t"
    "ds_or_b32 v92, v118\n\t"
    "v_bfi_b32 v130, v104, 0, v129\n\t"
    "v_cmp_eq_u32 vcc, 0, v130\n\t"
    "v_and_or_b32 v97, v104, s44, v68\n\t"
    "v_xor_b32 v133, 23, v132\n\t"
    "v_lshlrev_b32 v114, v132, v136\n\t"
    "v_and_b32 v132, 7, v132\n\t"
    "v_lshl_or_b32 v114, v136, v133, v114\n\t"
    "v_lshl_add_u32 v132, v132, 1, v128\n\t"
    "ds_or_rtn_b32 v122, v96, v114\n\t"
    "ds_write_b16 v132, v102\n\t"
    "v_cndmask_b32 v130, v130, v129, vcc\n\t"
    "v_ffbl_b32 v131, v130\n\t"
    "v_and_b32 v134, 7, v131\n\t"
    "v_lshlrev_b32 v134, v134, s45\n\t"
    "v_xor_b32 v129, v129, v134\n\t"
    "v_xor_b32 v132, v131, v127\n\t"
    "s_waitcnt lgkmcnt(8)\n\t"
    "v_and_b32 v119, v119, v111\n\t"
    "v_perm_b32 v119, v119, v119, s46\n\t"
    "ds_or_b32 v93, v119\n\t"
    "v_bfi_b32 v130, v103, 0, v129\n\t"
    "v_cmp_eq_u32 vcc, 0, v130\n\t"
    "v_and_or_b32 v98, v103, s44, v68\n\t"
    "v_xor_b32 v133, 23, v132\n\t"
    "v_lshlrev_b32 v115, v132, v136\n\t"
    "v_and_b32 v132, 7, v132\n\t"
    "v_lshl_or_b32 v115, v136, v133, v115\n\t"
    "v_lshl_add_u32 v132, v132, 1, v128\n\t"
    "ds_or_rtn_b32 v123, v97, v115\n\t"
    "ds_write_b16 v132, v104\n\t"
    "v_cndmask_b32 v130, v130, v129, vcc\n\t"
    "v_ffbl_b32 v131, v130\n\t"
    "v_and_b32 v134, 7, v131\n\t"
    "v_lshlrev_b32 v134, v134, s45\n\t"
    "v_xor_b32 v129, v129, v134\n\t"
    "v_xor_b32 v132, v131, v127\n\t"
    "s_waitcnt lgkmcnt(9)\n\t"
    "v_and_b32 v120, v120, v112\n\t"
    "v_perm_b32 v120, v120, v120, s46\n\t"
    "ds_or_b32 v94, v120\n\t"
    "v_bfi_b32 v130, v107, 0, v129\n\t"
    "v_cmp_eq_u32 vcc, 0, v130\n\t"
    "v_and_or_b32 v99, v107, s44, v68\n\t"
    "v_xor_b32 v133, 23, v132\n\t"
    "v_lshlrev_b32 v116, v132, v136\n\t"
    "v_and_b32 v132, 7, v132\n\t"
    "v_lshl_or_b32 v116, v136, v133, v116\n\t"
    "v_lshl_add_u32 v132, v132, 1, v128\n\t"
    "ds_or_rtn_b32 v124, v98, v116\n\t"
    "ds_write_b16 v132, v103\n\t"
    "v_cndmask_b32 v130, v130, v129, vcc\n\t"
    "v_ffbl_b32 v131, v130\n\t"
    "v_xor_b32 v132, v131, v127\n\t"
    "s_waitcnt lgkmcnt(10)\n\t"
    "v_and_b32 v121, v121, v113\n\t"
    "v_perm_b32 v121, v121, v121, s46\n\t"
    "ds_or_b32 v95, v121\n\t"
    "v_xor_b32 v133, 23, v132\n\t"
    "v_lshlrev_b32 v117, v132, v136\n\t"
    "v_and_b32 v132, 7, v132\n\t"
    "v_lshl_or_b32 v117, v136, v133, v117\n\t"
    "v_lshl_add_u32 v132, v132, 1, v128\n\t"
    "ds_or_rtn_b32 v125, v99, v117\n\t"
    "ds_write_b16 v132, v107\n\t"
    "s_waitcnt lgkmcnt(10)\n\t"
    "v_and_b32 v122, v122, v114\n\t"
    "v_perm_b32 v122, v122, v122, s46\n\t"
    "ds_or_b32 v96, v122\n\t"
    "ds_read_b128 v[72:75], v128\n\t"
    "v_and_or_b32 v100, v76, s44, v68\n\t"
    "ds_read_b32 v100, v100\n\t"
    "v_and_or_b32 v101, v77, s44, v68\n\t"
    "ds_read_b32 v101, v101\n\t"
    "v_and_or_b32 v102, v78, s44, v68\n\t"
    "ds_read_b32 v102, v102\n\t"
    "v_and_or_b32 v103, v79, s44, v68\n\t"
    "ds_read_b32 v103, v103\n\t"
    "v_and_or_b32 v104, v80, s44, v68\n\t"
    "ds_read_b32 v104, v104\n\t"
    "v_and_or_b32 v105, v81, s44, v68\n\t"
    "ds_read_b32 v105, v105\n\t"
    "v_and_or_b32 v106, v82, s44, v68\n\t"
    "ds_read_b32 v106, v106\n\t"
    "v_and_or_b32 v107, v83, s44, v68\n\t"
    "ds_read_b32 v107, v107\n\t"
    "s_cmp_lg_u32 %14, 0\n\t"
    "s_cbranch_scc1 L_ahe864_%=\n\t"
    "s_waitcnt vmcnt(0)\n\t"
    "buffer_store_dwordx4 v[148:151], v152, %12, s47 offen offset:1024 sc1\n\t"
    "L_ahe864_%=:\n\t"
    "s_cmp_lg_u32 %14, 0\n\t"
    "s_cbranch_scc1 L_ahe869_%=\n\t"
    "buffer_load_dwordx4 v[148:151], v152, %12, s47 offen sc1\n\t"
    "L_ahe869_%=:\n\t"
    "s_waitcnt lgkmcnt(11)\n\t"
    "v_and_b32 v123, v123, v115\n\t"
    "v_perm_b32 v123, v123, v123, s46\n\t"
    "ds_or_b32 v97, v123\n\t"
    "v_and_b32 v124, v124, v116\n\t"
    "v_perm_b32 v124, v124, v124, s46\n\t"
    "ds_or_b32 v98, v124\n\t"
    "v_and_b32 v125, v125, v117\n\t"
    "v_perm_b32 v125, v125, v125, s46\n\t"
    "ds_or_b32 v99, v125\n\t"
    "s_waitcnt lgkmcnt(11)\n\t"
    "buffer_store_dwordx4 v[72:75], v66, %12, s49 offen offset:192 sc1\n\t"
    "v_mov_b32 v129, 0xffff0000\n\t"
    "s_waitcnt lgkmcnt(7)\n\t"
    "v_perm_b32 v100, v100, v76, v126\n\t"
    "v_perm_b32 v101, v101, v77, v126\n\t"
    "v_perm_b32 v102, v102, v78, v126\n\t"
    "v_perm_b32 v103, v103, v79, v126\n\t"
    "s_waitcnt lgkmcnt(3)\n\t"
    "v_perm_b32 v104, v104, v80, v126\n\t"
    "v_perm_b32 v105, v105, v81, v126\n\t"
    "v_perm_b32 v106, v106, v82, v126\n\t"
    "v_perm_b32 v107, v107, v83, v126\n\t"
    "v_max_u32 v108, v100, v101\n\t"
    "v_min_u32 v101, v100, v101\n\t"
    "v_max_u32 v100, v102, v103\n\t"
    "v_min_u32 v103, v102, v103\n\t"
    "v_max_u32 v102, v104, v105\n\t"
    "v_min_u32 v105, v104, v105\n\t"
    "v_max_u32 v104, v106, v107\n\t"
    "v_min_u32 v107, v106, v107\n\t"
    "v_max_u32 v106, v108, v100\n\t"
    "v_min_u32 v100, v108, v100\n\t"
    "v_max_u32 v108, v101, v103\n\t"
    "v_min_u32 v103, v101, v103\n\t"
    "v_max_u32 v101, v102, v104\n\t"
    "v_min_u32 v104, v102, v104\n\t"
    "v_max_u32 v102, v105, v107\n\t"
    "v_min_u32 v107, v105, v107\n\t"
    "v_bfi_b32 v130, v106, 0, v129\n\t"
    "v_cmp_eq_u32 vcc, 0, v130\n\t"
    "v_and_or_b32 v92, v106, s44, v68\n\t"
    "s_nop 0\n\t"
    "v_cndmask_b32 v130, v130, v129, vcc\n\t"
    "v_ffbl_b32 v131, v130\n\t"
    "v_and_b32 v134, 7, v131\n\t"
    "v_lshlrev_b32 v134, v134, s45\n\t"
    "v_xor_b32 v129, v129, v134\n\t"
    "v_xor_b32 v132, v131, v127\n\t"
    "v_bfi_b32 v130, v101, 0, v129\n\t"
    "v_cmp_eq_u32 vcc, 0, v130\n\t"
    "v_and_or_b32 v93, v101, s44, v68\n\t"
    "v_xor_b32 v133, 23, v132\n\t"
    "v_lshlrev_b32 v110, v132, v136\n\t"
    "v_and_b32 v132, 7, v132\n\t"
    "v_lshl_or_b32 v110, v136, v133, v110\n\t"
    "v_lshl_add_u32 v132, v132, 1, v128\n\t"
    "ds_or_rtn_b32 v118, v92, v110\n\t"
    "ds_write_b16 v132, v106\n\t"
    "v_cndmask_b32 v130, v130, v129, vcc\n\t"
    "v_ffbl_b32 v131, v130\n\t"
    "v_and_b32 v134, 7, v131\n\t"
    "v_lshlrev_b32 v134, v134, s45\n\t"
    "v_xor_b32 v129, v129, v134\n\t"
    "v_xor_b32 v132, v131, v127\n\t"
    "v_bfi_b32 v130, v108, 0, v129\n\t"
    "v_cmp_eq_u32 vcc, 0, v130\n\t"
    "v_and_or_b32 v94, v108, s44, v68\n\t"
    "v_xor_b32 v133, 23, v132\n\t"
    "v_lshlrev_b32 v111, v132, v136\n\t"
    "v_and_b32 v132, 7, v132\n\t"
    "v_lshl_or_b32 v111, v136, v133, v111\n\t"
    "v_lshl_add_u32 v132, v132, 1, v128\n\t"
    "ds_or_rtn_b32 v119, v93, v111\n\t"
    "ds_write_b16 v132, v101\n\t"
    "v_cndmask_b32 v130, v130, v129, vcc\n\t"
    "v_ffbl_b32 v131, v130\n\t"
    "v_and_b32 v134, 7, v131\n\t"
    "v_lshlrev_b32 v134, v134, s45\n\t"
    "v_xor_b32 v129, v129, v134\n\t"
    "v_xor_b32 v132, v131, v127\n\t"
    "v_bfi_b32 v130, v100, 0, v129\n\t"
    "v_cmp_eq_u32 vcc, 0, v130\n\t"
    "v_and_or_b32 v95, v100, s44, v68\n\t"
    "v_xor_b32 v133, 23, v132\n\t"
    "v_lshlrev_b32 v112, v132, v136\n\t"
    "v_and_b32 v132, 7, v132\n\t"
    "v_lshl_or_b32 v112, v136, v133, v112\n\t"
    "v_lshl_add_u32 v132, v132, 1, v128\n\t"
    "ds_or_rtn_b32 v120, v94, v112\n\t"
    "ds_write_b16 v132, v108\n\t"
    "v_cndmask_b32 v130, v130, v129, vcc\n\t"
    "v_ffbl_b32 v131, v130\n\t"
    "v_and_b32 v134, 7, v131\n\t"
    "v_lshlrev_b32 v134, v134, s45\n\t"
    "v_xor_b32 v129, v129, v134\n\t"
    "v_xor_b32 v132, v131, v127\n\t"
    "v_bfi_b32 v130, v102, 0, v129\n\t"
    "v_cmp_eq_u32 vcc, 0, v130\n\t"
    "v_and_or_b32 v96, v102, s44, v68\n\t"
    "v_xor_b32 v133, 23, v132\n\t"
    "v_lshlrev_b32 v113, v132, v136\n\t"
    "v_and_b32 v132, 7, v132\n\t"
    "v_lshl_or_b32 v113, v136, v133, v113\n\t"
    "v_lshl_add_u32 v132, v132, 1, v128\n\t"
    "ds_or_rtn_b32 v121, v95, v113\n\t"
    "ds_write_b16 v132, v100\n\t"
    "v_cndmask_b32 v130, v130, v129, vcc\n\t"
    "v_ffbl_b32 v131, v130\n\t"
    "v_and_b32 v134, 7, v131\n\t"
    "v_lshlrev_b32 v134, v134, s45\n\t"
    "v_xor_b32 v129, v129, v134\n\t"
    "v_xor_b32 v132, v131, v127\n\t"
    "s_waitcnt lgkmcnt(7)\n\t"
    "v_and_b32 v118, v118, v110\n\t"
    "v_perm_b32 v118, v118, v118, s46\n\t"
    "ds_or_b32 v92, v118\n\t"
    "s_waitcnt vmcnt(2)\n\t"
    "ds_write_b32 v71, v136 offset:4\n\t"
    "v_bfi_b32 v130, v104, 0, v129\n\t"
    "v_cmp_eq_u32 vcc, 0, v130\n\t"
    "v_and_or_b32 v97, v104, s44, v68\n\t"
    "v_xor_b32 v133, 23, v132\n\t"
    "v_lshlrev_b32 v114, v132, v136\n\t"
    "v_and_b32 v132, 7, v132\n\t"
    "v_lshl_or_b32 v114, v136, v133, v114\n\t"
    "v_lshl_add_u32 v132, v132, 1, v128\n\t"
    "ds_or_rtn_b32 v122, v96, v114\n\t"
    "ds_write_b16 v132, v102\n\t"
    "v_cndmask_b32 v130, v130, v129, vcc\n\t"
    "v_ffbl_b32 v131, v130\n\t"
    "v_and_b32 v134, 7, v131\n\t"
    "v_lshlrev_b32 v134, v134, s45\n\t"
    "v_xor_b32 v129, v129, v134\n\t"
    "v_xor_b32 v132, v131, v127\n\t"
    "s_waitcnt lgkmcnt(9)\n\t"
    "v_and_b32 v119, v119, v111\n\t"
    "v_perm_b32 v119, v119, v119, s46\n\t"
    "ds_or_b32 v93, v119\n\t"
    "v_bfi_b32 v130, v103, 0, v129\n\t"
    "v_cmp_eq_u32 vcc, 0, v130\n\t"
    "v_and_or_b32 v98, v103, s44, v68\n\t"
    "v_xor_b32 v133, 23, v132\n\t"
    "v_lshlrev_b32 v115, v132, v136\n\t"
    "v_and_b32 v132, 7, v132\n\t"
    "v_lshl_or_b32 v115, v136, v133, v115\n\t"
    "v_lshl_add_u32 v132, v132, 1, v128\n\t"
    "ds_or_rtn_b32 v123, v97, v115\n\t"
    "ds_write_b16 v132, v104\n\t"
    "v_cndmask_b32 v130, v130, v129, vcc\n\t"
    "v_ffbl_b32 v131, v130\n\t"
    "v_and_b32 v134, 7, v131\n\t"
    "v_lshlrev_b32 v134, v134, s45\n\t"
    "v_xor_b32 v129, v129, v134\n\t"
    "v_xor_b32 v132, v131, v127\n\t"
    "s_waitcnt lgkmcnt(10)\n\t"
    "v_and_b32 v120, v120, v112\n\t"
    "v_perm_b32 v120, v120, v120, s46\n\t"
    "ds_or_b32 v94, v120\n\t"
    "v_bfi_b32 v130, v107, 0, v129\n\t"
    "v_cmp_eq_u32 vcc, 0, v130\n\t"
    "v_and_or_b32 v99, v107, s44, v68\n\t"
    "v_xor_b32 v133, 23, v132\n\t"
    "v_lshlrev_b32 v116, v132, v136\n\t"
    "v_and_b32 v132, 7, v132\n\t"
    "v_lshl_or_b32 v116, v136, v133, v116\n\t"
    "v_lshl_add_u32 v132, v132, 1, v128\n\t"
    "ds_or_rtn_b32 v124, v98, v116\n\t"
    "ds_write_b16 v132, v103\n\t"
    "v_cndmask_b32 v130, v130, v129, vcc\n\t"
    "v_ffbl_b32 v131, v130\n\t"
    "v_xor_b32 v132, v131, v127\n\t"
    "s_waitcnt lgkmcnt(11)\n\t"
    "v_and_b32 v121, v121, v113\n\t"
    "v_perm_b32 v121, v121, v121, s46\n\t"
    "ds_or_b32 v95, v121\n\t"
    "v_xor_b32 v133, 23, v132\n\t"
    "v_lshlrev_b32 v117, v132, v136\n\t"
    "v_and_b32 v132, 7, v132\n\t"
    "v_lshl_or_b32 v117, v136, v133, v117\n\t"
    "v_lshl_add_u32 v132, v132, 1, v128\n\t"
    "ds_or_rtn_b32 v125, v99, v117\n\t"
    "ds_write_b16 v132, v107\n\t"
    "s_waitcnt lgkmcnt(10)\n\t"
    "v_and_b32 v122, v122, v114\n\t"
    "v_perm_b32 v122, v122, v122, s46\n\t"
    "ds_or_b32 v96, v122\n\t"
    "ds_read_b128 v[72:75], v128\n\t"
    "s_waitcnt lgkmcnt(3)\n\t"
    "v_and_b32 v123, v123, v115\n\t"
    "v_perm_b32 v123, v123, v123, s46\n\t"
    "ds_or_b32 v97, v123\n\t"
    "v_and_b32 v124, v124, v116\n\t"
    "v_perm_b32 v124, v124, v124, s46\n\t"
    "ds_or_b32 v98, v124\n\t"
    "v_and_b32 v125, v125, v117\n\t"
    "v_perm_b32 v125, v125, v125, s46\n\t"
    "ds_or_b32 v99, v125\n\t"
    "s_waitcnt lgkmcnt(3)\n\t"
    "buffer_store_dwordx4 v[72:75], v66, %12, s49 offen offset:256 sc1\n\t"
    "v_mov_b32 v134, 1036\n\t"
    "ds_write_b32 v134, v136\n\t"
    "s_cmp_lg_u32 %14, 0\n\t"
    "s_cbranch_scc1 L_ahsend_%=\n\t"
    "s_waitcnt vmcnt(0)\n\t"
    "buffer_store_dwordx4 v[148:151], v152, %12, s47 offen offset:1024 sc1\n\t"
    "s_setprio 0\n\t"
    "v_and_b32 v132, 0x70, v152\n\t"
    "v_lshlrev_b32 v132, 3, v132\n\t"
    "s_mov_b32 s51, 1048576\n\t"
    "L_ahs2_%=:\n\t"
    "s_sleep 8\n\t"
    "buffer_load_dword v71, v132, %12, s48 offen offset:1024 sc1\n\t"
    "buffer_load_dwordx4 v[148:151], v152, %12, s47 offen sc1\n\t"
    "s_sub_u32 s51, s51, 1\n\t"
    "s_cmp_eq_u32 s51, 0\n\t"
    "s_cbranch_scc1 L_ahsend_%=\n\t"
    "s_waitcnt vmcnt(0)\n\t"
    "buffer_store_dwordx4 v[148:151], v152, %12, s47 offen offset:1024 sc1\n\t"
    "v_cmp_gt_u32 vcc, 32, v71\n\t"
    "s_cbranch_vccnz L_ahs2_%=\n\t"
    "L_ahsend_%=:\n\t"
    "s_setprio 0\n\t"
    "s_and_b32 s46, %16, 3\n\t"
    "s_mul_i32 s49, s46, 1040\n\t"
    "s_add_u32 s49, s49, 2064\n\t"
    "s_lshl_b32 s46, s46, 3\n\t"
    "s_lshr_b32 s47, 1076697610, s46\n\t"
    "s_and_b32 s47, s47, 0xff\n\t"
    "s_lshl_b32 s48, s47, 16\n\t"
    "s_lshr_b32 s47, 756681216, s46\n\t"
    "s_and_b32 s47, s47, 0xff\n\t"
    "s_add_u32 s48, s48, s47\n\t"
    "s_lshr_b32 s47, %16, 2\n\t"
    "s_add_u32 s47, s47, 0x200\n\t"
    "s_cmp_eq_u32 s46, 0\n\t"
    "s_cselect_b32 s47, 0x100, s47\n\t"
    "s_add_u32 s48, s48, s47\n\t"
    "v_bfe_u32 v154, v147, 2, 2\n\t"
    "v_and_b32 v155, 3, v147\n\t"
    "v_mul_u32_u24 v154, 25, v154\n\t"
    "v_mad_u32_u24 v154, v155, 6, v154\n\t"
    "v_lshlrev_b32 v153, 2, v154\n\t"
    "s_and_b32 s53, s48, 0xff\n\t"
    "s_bfe_u32 s55, s48, 0x80008\n\t"
    "s_add_u32 s54, s53, s55\n\t"
    "s_add_u32 s55, s54, s55\n\t"
    "s_lshr_b32 s64, s48, 16\n\t"
    "s_mul_i32 s66, s53, 400\n\t"
    "buffer_load_dwordx3 v[128:130], v153, %0, s66 offen\n\t"
    "buffer_load_dwordx4 v[132:135], v153, %0, s66 offen offset:12\n\t"
    "s_mul_i32 s66, s54, 400\n\t"
    "buffer_load_dwordx3 v[136:138], v153, %0, s66 offen\n\t"
    "buffer_load_dwordx4 v[140:143], v153, %0, s66 offen offset:12\n\t"
    "v_cmp_eq_u32 s[62:63], 0, v155\n\t"
    "v_and_b32 v151, 48, v147\n\t"
    "v_or_b32 v155, 0x100000, v151\n\t"
    "v_cndmask_b32 v152, v155, v151, s[62:63]\n\t"
    "v_lshlrev_b32 v117, 20, v147\n\t"
    "v_add_u32 v117, 1024, v117\n\t"
    "v_mov_b32 v118, 1\n\t"
    "v_lshlrev_b32 v95, 4, v147\n\t"
    "v_add_u32 v95, 140288, v95\n\t"
    "s_call_b64 s[98:99], L_setup_%=\n\t"
    "L_await_%=:\n\t"
    "L_asp9_%=:\n\t"
    "ds_read_b32 v118, v117 offset:8\n\t"
    "ds_read_b32 v119, v117 offset:4\n\t"
    "s_waitcnt lgkmcnt(0)\n\t"
    "v_readfirstlane_b32 s46, v119\n\t"
    "s_nop 0\n\t"
    "s_cmp_ge_u32 s46, 4\n\t"
    "s_cbranch_scc1 L_ago9_%=\n\t"
    "s_sleep 1\n\t"
    "s_branch L_asp9_%=\n\t"
    "L_ago9_%=:\n\t"
    "v_readfirstlane_b32 s47, v118\n\t"
    "ds_read_b128 v[0:3], v95 offset:0\n\t"
    "ds_read_b128 v[4:7], v95 offset:1024\n\t"
    "ds_read_b128 v[8:11], v95 offset:2048\n\t"
    "ds_read_b128 v[12:15], v95 offset:3072\n\t"
    "ds_read_b128 v[16:19], v95 offset:4096\n\t"
    "ds_read_b128 v[20:23], v95 offset:5120\n\t"
    "ds_read_b128 v[24:27], v95 offset:6144\n\t"
    "ds_read_b128 v[28:31], v95 offset:7168\n\t"
    "ds_read_b128 v[32:35], v95 offset:8192\n\t"
    "ds_read_b128 v[36:39], v95 offset:9216\n\t"
    "ds_read_b128 v[40:43], v95 offset:10240\n\t"
    "ds_read_b128 v[44:47], v95 offset:11264\n\t"
    "ds_read_b128 v[48:51], v95 offset:12288\n\t"
    "ds_read_b128 v[52:55], v95 offset:13312\n\t"
    "ds_read_b128 v[56:59], v95 offset:14336\n\t"
    "ds_read_b128 v[60:63], v95 offset:15360\n\t"
    "ds_read_b128 v[64:67], v95 offset:16384\n\t"
    "ds_read_b128 v[68:71], v95 offset:17408\n\t"
    "ds_read_b128 v[72:75], v95 offset:18432\n\t"
    "ds_read_b128 v[76:79], v95 offset:19456\n\t"
    "ds_read_b128 v[80:83], v95 offset:20480\n\t"
    "ds_read_b128 v[84:87], v95 offset:21504\n\t"
    "ds_read_b128 v[88:91], v95 offset:22528\n\t"
    "s_waitcnt vmcnt(0)\n\t"
    "s_cmp_lg_u32 %16, 1\n\t"
    "s_cbranch_scc1 L_anh3_%=\n\t"
    "v_mov_b32 v119, 4\n\t"
    "ds_write_b32 v117, v119 offset:12\n\t"
    "s_lshl_b32 s65, %14, 12\n\t"
    "s_add_u32 s65, s65, 16777216\n\t"
    "s_waitcnt lgkmcnt(15)\n\t"
    "buffer_store_dwordx4 v[0:3], v127, %12, s65 offen offset:0 sc1\n\t"
    "buffer_store_dwordx4 v[4:7], v127, %12, s65 offen offset:1024 sc1\n\t"
    "buffer_store_dwordx4 v[8:11], v127, %12, s65 offen offset:2048 sc1\n\t"
    "buffer_store_dwordx4 v[12:15], v127, %12, s65 offen offset:3072 sc1\n\t"
    "L_anh3_%=:\n\t"
    "s_cmp_ge_u32 s47, 6\n\t"
    "s_cbranch_scc1 L_adok_%=\n\t"
    "L_awd_%=:\n\t"
    "L_asp10_%=:\n\t"
    "ds_read_b32 v119, v117 offset:8\n\t"
    "s_waitcnt lgkmcnt(0)\n\t"
    "v_readfirstlane_b32 s46, v119\n\t"
    "s_nop 0\n\t"
    "s_cmp_ge_u32 s46, 6\n\t"
    "s_cbranch_scc1 L_ago10_%=\n\t"
    "s_sleep 1\n\t"
    "s_branch L_asp10_%=\n\t"
    "L_ago10_%=:\n\t"
    "L_adok_%=:\n\t"
    "L_amain_%=:\n\t"
    "s_mov_b32 s68, 130\n\t"
    "v_mad_u32_u24 v128, v128, s68, v152\n\t"
    "v_and_b32 v128, -16, v128\n\t"
    "ds_read_b128 v[156:159], v128 offset:0\n\t"
    "ds_read_b128 v[160:163], v128 offset:64\n\t"
    "v_mad_u32_u24 v129, v129, s68, v151\n\t"
    "v_and_b32 v129, -16, v129\n\t"
    "ds_read_b128 v[164:167], v129 offset:0\n\t"
    "ds_read_b128 v[168:171], v129 offset:64\n\t"
    "v_mad_u32_u24 v130, v130, s68, v151\n\t"
    "v_and_b32 v130, -16, v130\n\t"
    "ds_read_b128 v[172:175], v130 offset:0\n\t"
    "ds_read_b128 v[176:179], v130 offset:64\n\t"
    "s_mul_i32 s66, s55, 400\n\t"
    "buffer_load_dwordx3 v[128:130], v153, %0, s66 offen\n\t"
    "v_mad_u32_u24 v132, v132, s68, v151\n\t"
    "v_and_b32 v132, -16, v132\n\t"
    "ds_read_b128 v[180:183], v132 offset:0\n\t"
    "ds_read_b128 v[184:187], v132 offset:64\n\t"
    "v_mad_u32_u24 v133, v133, s68, v151\n\t"
    "v_and_b32 v133, -16, v133\n\t"
    "ds_read_b128 v[188:191], v133 offset:0\n\t"
    "ds_read_b128 v[192:195], v133 offset:64\n\t"
    "v_mad_u32_u24 v134, v134, s68, v151\n\t"
    "v_and_b32 v134, -16, v134\n\t"
    "ds_read_b128 v[196:199], v134 offset:0\n\t"
    "ds_read_b128 v[200:203], v134 offset:64\n\t"
    "v_mad_u32_u24 v135, v135, s68, v151\n\t"
    "v_and_b32 v135, -16, v135\n\t"
    "ds_read_b128 v[204:207], v135 offset:0\n\t"
    "ds_read_b128 v[208:211], v135 offset:64\n\t"
    "v_mov_b32 v150, 1\n\t"
    "v_lshlrev_b32 v149, 20, v147\n\t"
    "v_add_u32 v149, s49, v149\n\t"
    "v_lshlrev_b32 v146, 2, v147\n\t"
    "s_mov_b32 s52, s53\n\t"
    "s_lshl_b32 s50, %14, 14\n\t"
    "s_add_u32 s50, s50, 27262976\n\t"
    "L_apnn_%=:\n\t"
    "s_mul_i32 s69, s52, 256\n\t"
    "s_add_u32 s69, s69, s50\n\t"
    "s_waitcnt lgkmcnt(8)\n\t"
    "v_mfma_f32_16x16x32_bf16 v[92:95], v[156:159], v[156:159], 0\n\t"
    "v_fmac_f32 v145, v108, v72\n\t"
    "v_fmac_f32 v144, v109, v73\n\t"
    "ds_add_rtn_u32 v148, v149, v150\n\t"
    "v_mfma_f32_16x16x32_bf16 v[92:95], v[160:163], v[160:163], v[92:95]\n\t"
    "v_fmac_f32 v145, v110, v74\n\t"
    "v_fmac_f32 v144, v111, v75\n\t"
    "buffer_load_dwordx4 v[132:135], v153, %0, s66 offen offset:12\n\t"
    "v_mfma_f32_16x16x32_bf16 v[96:99], v[164:167], v[164:167], 0\n\t"
    "v_fmac_f32 v145, v112, v76\n\t"
    "v_fmac_f32 v144, v113, v77\n\t"
    "v_mfma_f32_16x16x32_bf16 v[96:99], v[168:171], v[168:171], v[96:99]\n\t"
    "v_fmac_f32 v145, v114, v78\n\t"
    "v_fmac_f32 v144, v115, v79\n\t"
    "v_mfma_f32_16x16x32_bf16 v[100:103], v[164:167], v[172:175], 0\n\t"
    "v_fmac_f32 v145, v116, v80\n\t"
    "v_fmac_f32 v144, v117, v81\n\t"
    "v_mfma_f32_16x16x32_bf16 v[100:103], v[168:171], v[176:179], v[100:103]\n\t"
    "v_fmac_f32 v145, v118, v82\n\t"
    "v_fmac_f32 v144, v119, v83\n\t"
    "s_waitcnt vmcnt(4)\n\t"
    "s_cmp_ge_u32 s55, s64\n\t"
    "s_cbranch_scc1 L_ahk00_%=\n\t"
    "L_ahb00_%=:\n\t"
    "v_mfma_f32_16x16x32_bf16 v[104:107], v[172:175], v[172:175], 0\n\t"
    "v_mad_u32_u24 v136, v136, s68, v152\n\t"
    "v_and_b32 v136, -16, v136\n\t"
    "v_mfma_f32_16x16x32_bf16 v[104:107], v[176:179], v[176:179], v[104:107]\n\t"
    "v_add_f32 v154, v144, v145\n\t"
    "buffer_store_dword v154, v146, %12, s69 offen sc1\n\t"
    "v_mul_f32 v144, v92, v84\n\t"
    "v_mfma_f32_16x16x32_bf16 v[108:111], v[156:159], v[164:167], 0\n\t"
    "v_mul_f32 v145, v96, v0\n\t"
    "v_fmac_f32 v144, v97, v1\n\t"
    "v_mfma_f32_16x16x32_bf16 v[108:111], v[160:163], v[168:171], v[108:111]\n\t"
    "v_fmac_f32 v145, v98, v2\n\t"
    "v_fmac_f32 v144, v99, v3\n\t"
    "s_waitcnt lgkmcnt(7)\n\t"
    "v_mfma_f32_16x16x32_bf16 v[112:115], v[164:167], v[180:183], 0\n\t"
    "v_fmac_f32 v145, v100, v4\n\t"
    "v_fmac_f32 v144, v101, v5\n\t"
    "v_mfma_f32_16x16x32_bf16 v[112:115], v[168:171], v[184:187], v[112:115]\n\t"
    "v_fmac_f32 v145, v102, v6\n\t"
    "v_fmac_f32 v144, v103, v7\n\t"
    "v_mfma_f32_16x16x32_bf16 v[116:119], v[172:175], v[180:183], 0\n\t"
    "v_fmac_f32 v145, v104, v8\n\t"
    "v_fmac_f32 v144, v105, v9\n\t"
    "v_mfma_f32_16x16x32_bf16 v[116:119], v[176:179], v[184:187], v[116:119]\n\t"
    "v_fmac_f32 v145, v106, v10\n\t"
    "v_fmac_f32 v144, v107, v11\n\t"
    "ds_read_b128 v[212:215], v136 offset:0\n\t"
    "ds_read_b128 v[216:219], v136 offset:64\n\t"
    "v_mfma_f32_16x16x32_bf16 v[92:95], v[180:183], v[180:183], 0\n\t"
    "v_mad_u32_u24 v137, v137, s68, v151\n\t"
    "v_and_b32 v137, -16, v137\n\t"
    "v_mfma_f32_16x16x32_bf16 v[92:95], v[184:187], v[184:187], v[92:95]\n\t"
    "v_fmac_f32 v145, v108, v85\n\t"
    "v_mad_u32_u24 v138, v138, s68, v151\n\t"
    "v_mfma_f32_16x16x32_bf16 v[96:99], v[156:159], v[172:175], 0\n\t"
    "v_fmac_f32 v144, v112, v12\n\t"
    "v_fmac_f32 v145, v113, v13\n\t"
    "v_mfma_f32_16x16x32_bf16 v[96:99], v[160:163], v[176:179], v[96:99]\n\t"
    "v_fmac_f32 v144, v114, v14\n\t"
    "v_fmac_f32 v145, v115, v15\n\t"
    "s_waitcnt lgkmcnt(7)\n\t"
    "v_mfma_f32_16x16x32_bf16 v[100:103], v[164:167], v[188:191], 0\n\t"
    "v_and_b32 v138, -16, v138\n\t"
    "v_fmac_f32 v144, v116, v16\n\t"
    "v_mfma_f32_16x16x32_bf16 v[100:103], v[168:171], v[192:195], v[100:103]\n\t"
    "v_fmac_f32 v145, v117, v17\n\t"
    "v_fmac_f32 v144, v118, v18\n\t"
    "v_mfma_f32_16x16x32_bf16 v[104:107], v[172:175], v[188:191], 0\n\t"
    "v_fmac_f32 v145, v119, v19\n\t"
    "v_fmac_f32 v144, v92, v20\n\t"
    "v_mfma_f32_16x16x32_bf16 v[104:107], v[176:179], v[192:195], v[104:107]\n\t"
    "v_fmac_f32 v145, v93, v21\n\t"
    "v_fmac_f32 v144, v94, v22\n\t"
    "s_waitcnt lgkmcnt(5)\n\t"
    "v_mfma_f32_16x16x32_bf16 v[108:111], v[164:167], v[196:199], 0\n\t"
    "v_fmac_f32 v145, v95, v23\n\t"
    "v_fmac_f32 v144, v96, v86\n\t"
    "v_mfma_f32_16x16x32_bf16 v[108:111], v[168:171], v[200:203], v[108:111]\n\t"
    "v_mfma_f32_16x16x32_bf16 v[112:115], v[156:159], v[180:183], 0\n\t"
    "v_fmac_f32 v145, v100, v24\n\t"
    "v_fmac_f32 v144, v101, v25\n\t"
    "v_mfma_f32_16x16x32_bf16 v[112:115], v[160:163], v[184:187], v[112:115]\n\t"
    "v_fmac_f32 v145, v102, v26\n\t"
    "v_fmac_f32 v144, v103, v27\n\t"
    "s_waitcnt lgkmcnt(2)\n\t"
    "v_mfma_f32_16x16x32_bf16 v[116:119], v[164:167], v[204:207], 0\n\t"
    "v_readfirstlane_b32 s60, v148\n\t"
    "v_fmac_f32 v145, v104, v28\n\t"
    "v_mfma_f32_16x16x32_bf16 v[116:119], v[168:171], v[208:211], v[116:119]\n\t"
    "v_fmac_f32 v144, v105, v29\n\t"
    "v_fmac_f32 v145, v106, v30\n\t"
    "ds_read_b128 v[164:167], v137 offset:0\n\t"
    "ds_read_b128 v[168:171], v137 offset:64\n\t"
    "v_mfma_f32_16x16x32_bf16 v[92:95], v[172:175], v[196:199], 0\n\t"
    "v_fmac_f32 v144, v107, v31\n\t"
    "v_fmac_f32 v145, v108, v32\n\t"
    "v_mfma_f32_16x16x32_bf16 v[92:95], v[176:179], v[200:203], v[92:95]\n\t"
    "v_fmac_f32 v144, v109, v33\n\t"
    "v_fmac_f32 v145, v110, v34\n\t"
    "s_waitcnt vmcnt(4)\n\t"
    "s_cmp_ge_u32 s55, s64\n\t"
    "s_cbranch_scc1 L_ahk10_%=\n\t"
    "L_ahb10_%=:\n\t"
    "v_mfma_f32_16x16x32_bf16 v[96:99], v[172:175], v[204:207], 0\n\t"
    "v_mad_u32_u24 v140, v140, s68, v151\n\t"
    "v_and_b32 v140, -16, v140\n\t"
    "v_mfma_f32_16x16x32_bf16 v[96:99], v[176:179], v[208:211], v[96:99]\n\t"
    "v_fmac_f32 v144, v111, v35\n\t"
    "v_fmac_f32 v145, v112, v87\n\t"
    "ds_read_b128 v[172:175], v138 offset:0\n\t"
    "ds_read_b128 v[176:179], v138 offset:64\n\t"
    "s_mul_i32 s66, s60, 400\n\t"
    "v_mfma_f32_16x16x32_bf16 v[100:103], v[156:159], v[188:191], 0\n\t"
    "v_fmac_f32 v144, v116, v36\n\t"
    "v_fmac_f32 v145, v117, v37\n\t"
    "v_mfma_f32_16x16x32_bf16 v[100:103], v[160:163], v[192:195], v[100:103]\n\t"
    "v_fmac_f32 v144, v118, v38\n\t"
    "v_fmac_f32 v145, v119, v39\n\t"
    "buffer_load_dwordx3 v[136:138], v153, %0, s66 offen\n\t"
    "v_mfma_f32_16x16x32_bf16 v[104:107], v[180:183], v[188:191], 0\n\t"
    "v_mad_u32_u24 v141, v141, s68, v151\n\t"
    "v_and_b32 v141, -16, v141\n\t"
    "v_mfma_f32_16x16x32_bf16 v[104:107], v[184:187], v[192:195], v[104:107]\n\t"
    "v_fmac_f32 v144, v92, v40\n\t"
    "v_fmac_f32 v145, v93, v41\n\t"
    "v_mfma_f32_16x16x32_bf16 v[108:111], v[180:183], v[196:199], 0\n\t"
    "v_fmac_f32 v144, v94, v42\n\t"
    "v_fmac_f32 v145, v95, v43\n\t"
    "v_mfma_f32_16x16x32_bf16 v[108:111], v[184:187], v[200:203], v[108:111]\n\t"
    "v_fmac_f32 v144, v96, v44\n\t"
    "v_fmac_f32 v145, v97, v45\n\t"
    "v_mfma_f32_16x16x32_bf16 v[112:115], v[180:183], v[204:207], 0\n\t"
    "v_fmac_f32 v144, v98, v46\n\t"
    "v_fmac_f32 v145, v99, v47\n\t"
    "v_mfma_f32_16x16x32_bf16 v[112:115], v[184:187], v[208:211], v[112:115]\n\t"
    "v_fmac_f32 v144, v100, v88\n\t"
    "v_mad_u32_u24 v142, v142, s68, v151\n\t"
    "ds_read_b128 v[180:183], v140 offset:0\n\t"
    "ds_read_b128 v[184:187], v140 offset:64\n\t"
    "v_mfma_f32_16x16x32_bf16 v[116:119], v[156:159], v[196:199], 0\n\t"
    "v_and_b32 v142, -16, v142\n\t"
    "v_fmac_f32 v145, v104, v48\n\t"
    "v_mfma_f32_16x16x32_bf16 v[116:119], v[160:163], v[200:203], v[116:119]\n\t"
    "v_fmac_f32 v144, v105, v49\n\t"
    "v_fmac_f32 v145, v106, v50\n\t"
    "v_mfma_f32_16x16x32_bf16 v[92:95], v[188:191], v[188:191], 0\n\t"
    "v_fmac_f32 v144, v107, v51\n\t"
    "v_mad_u32_u24 v143, v143, s68, v151\n\t"
    "v_mfma_f32_16x16x32_bf16 v[92:95], v[192:195], v[192:195], v[92:95]\n\t"
    "v_and_b32 v143, -16, v143\n\t"
    "v_fmac_f32 v145, v108, v52\n\t"
    "v_mfma_f32_16x16x32_bf16 v[96:99], v[188:191], v[196:199], 0\n\t"
    "v_fmac_f32 v144, v109, v53\n\t"
    "v_fmac_f32 v145, v110, v54\n\t"
    "v_mfma_f32_16x16x32_bf16 v[96:99], v[192:195], v[200:203], v[96:99]\n\t"
    "v_fmac_f32 v144, v111, v55\n\t"
    "v_fmac_f32 v145, v112, v56\n\t"
    "v_mfma_f32_16x16x32_bf16 v[100:103], v[188:191], v[204:207], 0\n\t"
    "v_fmac_f32 v144, v113, v57\n\t"
    "v_fmac_f32 v145, v114, v58\n\t"
    "v_mfma_f32_16x16x32_bf16 v[100:103], v[192:195], v[208:211], v[100:103]\n\t"
    "v_fmac_f32 v144, v115, v59\n\t"
    "v_fmac_f32 v145, v116, v89\n\t"
    "ds_read_b128 v[188:191], v141 offset:0\n\t"
    "ds_read_b128 v[192:195], v141 offset:64\n\t"
    "v_mfma_f32_16x16x32_bf16 v[104:107], v[156:159], v[204:207], 0\n\t"
    "v_fmac_f32 v144, v92, v60\n\t"
    "v_fmac_f32 v145, v93, v61\n\t"
    "v_mfma_f32_16x16x32_bf16 v[104:107], v[160:163], v[208:211], v[104:107]\n\t"
    "v_fmac_f32 v144, v94, v62\n\t"
    "v_fmac_f32 v145, v95, v63\n\t"
    "v_mfma_f32_16x16x32_bf16 v[108:111], v[196:199], v[196:199], 0\n\t"
    "v_fmac_f32 v144, v96, v64\n\t"
    "v_fmac_f32 v145, v97, v65\n\t"
    "v_mfma_f32_16x16x32_bf16 v[108:111], v[200:203], v[200:203], v[108:111]\n\t"
    "v_fmac_f32 v144, v98, v66\n\t"
    "v_fmac_f32 v145, v99, v67\n\t"
    "v_mfma_f32_16x16x32_bf16 v[112:115], v[196:199], v[204:207], 0\n\t"
    "v_fmac_f32 v144, v100, v68\n\t"
    "v_fmac_f32 v145, v101, v69\n\t"
    "v_mfma_f32_16x16x32_bf16 v[112:115], v[200:203], v[208:211], v[112:115]\n\t"
    "v_fmac_f32 v144, v102, v70\n\t"
    "v_fmac_f32 v145, v103, v71\n\t"
    "ds_read_b128 v[196:199], v142 offset:0\n\t"
    "ds_read_b128 v[200:203], v142 offset:64\n\t"
    "v_mfma_f32_16x16x32_bf16 v[116:119], v[204:207], v[204:207], 0\n\t"
    "v_fmac_f32 v144, v104, v90\n\t"
    "v_mfma_f32_16x16x32_bf16 v[116:119], v[208:211], v[208:211], v[116:119]\n\t"
    "ds_read_b128 v[204:207], v143 offset:0\n\t"
    "ds_read_b128 v[208:211], v143 offset:64\n\t"
    "s_mov_b32 s52, s53\n\t"
    "s_mov_b32 s53, s54\n\t"
    "s_mov_b32 s54, s55\n\t"
    "s_mov_b32 s55, s60\n\t"
    "s_cmp_lt_u32 s53, s64\n\t"
    "s_cbranch_scc0 L_adone_%=\n\t"
    "s_mul_i32 s69, s52, 256\n\t"
    "s_add_u32 s69, s69, s50\n\t"
    "s_waitcnt lgkmcnt(8)\n\t"
    "v_mfma_f32_16x16x32_bf16 v[92:95], v[212:215], v[212:215], 0\n\t"
    "v_fmac_f32 v145, v108, v72\n\t"
    "v_fmac_f32 v144, v109, v73\n\t"
    "ds_add_rtn_u32 v148, v149, v150\n\t"
    "v_mfma_f32_16x16x32_bf16 v[92:95], v[216:219], v[216:219], v[92:95]\n\t"
    "v_fmac_f32 v145, v110, v74\n\t"
    "v_fmac_f32 v144, v111, v75\n\t"
    "buffer_load_dwordx4 v[140:143], v153, %0, s66 offen offset:12\n\t"
    "v_mfma_f32_16x16x32_bf16 v[96:99], v[164:167], v[164:167], 0\n\t"
    "v_fmac_f32 v145, v112, v76\n\t"
    "v_fmac_f32 v144, v113, v77\n\t"
    "v_mfma_f32_16x16x32_bf16 v[96:99], v[168:171], v[168:171], v[96:99]\n\t"
    "v_fmac_f32 v145, v114, v78\n\t"
    "v_fmac_f32 v144, v115, v79\n\t"
    "v_mfma_f32_16x16x32_bf16 v[100:103], v[164:167], v[172:175], 0\n\t"
    "v_fmac_f32 v145, v116, v80\n\t"
    "v_fmac_f32 v144, v117, v81\n\t"
    "v_mfma_f32_16x16x32_bf16 v[100:103], v[168:171], v[176:179], v[100:103]\n\t"
    "v_fmac_f32 v145, v118, v82\n\t"
    "v_fmac_f32 v144, v119, v83\n\t"
    "s_waitcnt vmcnt(4)\n\t"
    "s_cmp_ge_u32 s55, s64\n\t"
    "s_cbranch_scc1 L_ahk01_%=\n\t"
    "L_ahb01_%=:\n\t"
    "v_mfma_f32_16x16x32_bf16 v[104:107], v[172:175], v[172:175], 0\n\t"
    "v_mad_u32_u24 v128, v128, s68, v152\n\t"
    "v_and_b32 v128, -16, v128\n\t"
    "v_mfma_f32_16x16x32_bf16 v[104:107], v[176:179], v[176:179], v[104:107]\n\t"
    "v_add_f32 v154, v144, v145\n\t"
    "buffer_store_dword v154, v146, %12, s69 offen sc1\n\t"
    "v_mul_f32 v144, v92, v84\n\t"
    "v_mfma_f32_16x16x32_bf16 v[108:111], v[212:215], v[164:167], 0\n\t"
    "v_mul_f32 v145, v96, v0\n\t"
    "v_fmac_f32 v144, v97, v1\n\t"
    "v_mfma_f32_16x16x32_bf16 v[108:111], v[216:219], v[168:171], v[108:111]\n\t"
    "v_fmac_f32 v145, v98, v2\n\t"
    "v_fmac_f32 v144, v99, v3\n\t"
    "s_waitcnt lgkmcnt(7)\n\t"
    "v_mfma_f32_16x16x32_bf16 v[112:115], v[164:167], v[180:183], 0\n\t"
    "v_fmac_f32 v145, v100, v4\n\t"
    "v_fmac_f32 v144, v101, v5\n\t"
    "v_mfma_f32_16x16x32_bf16 v[112:115], v[168:171], v[184:187], v[112:115]\n\t"
    "v_fmac_f32 v145, v102, v6\n\t"
    "v_fmac_f32 v144, v103, v7\n\t"
    "v_mfma_f32_16x16x32_bf16 v[116:119], v[172:175], v[180:183], 0\n\t"
    "v_fmac_f32 v145, v104, v8\n\t"
    "v_fmac_f32 v144, v105, v9\n\t"
    "v_mfma_f32_16x16x32_bf16 v[116:119], v[176:179], v[184:187], v[116:119]\n\t"
    "v_fmac_f32 v145, v106, v10\n\t"
    "v_fmac_f32 v144, v107, v11\n\t"
    "ds_read_b128 v[156:159], v128 offset:0\n\t"
    "ds_read_b128 v[160:163], v128 offset:64\n\t"
    "v_mfma_f32_16x16x32_bf16 v[92:95], v[180:183], v[180:183], 0\n\t"
    "v_mad_u32_u24 v129, v129, s68, v151\n\t"
    "v_and_b32 v129, -16, v129\n\t"
    "v_mfma_f32_16x16x32_bf16 v[92:95], v[184:187], v[184:187], v[92:95]\n\t"
    "v_fmac_f32 v145, v108, v85\n\t"
    "v_mad_u32_u24 v130, v130, s68, v151\n\t"
    "v_mfma_f32_16x16x32_bf16 v[96:99], v[212:215], v[172:175], 0\n\t"
    "v_fmac_f32 v144, v112, v12\n\t"
    "v_fmac_f32 v145, v113, v13\n\t"
    "v_mfma_f32_16x16x32_bf16 v[96:99], v[216:219], v[176:179], v[96:99]\n\t"
    "v_fmac_f32 v144, v114, v14\n\t"
    "v_fmac_f32 v145, v115, v15\n\t"
    "s_waitcnt lgkmcnt(7)\n\t"
    "v_mfma_f32_16x16x32_bf16 v[100:103], v[164:167], v[188:191], 0\n\t"
    "v_and_b32 v130, -16, v130\n\t"
    "v_fmac_f32 v144, v116, v16\n\t"
    "v_mfma_f32_16x16x32_bf16 v[100:103], v[168:171], v[192:195], v[100:103]\n\t"
    "v_fmac_f32 v145, v117, v17\n\t"
    "v_fmac_f32 v144, v118, v18\n\t"
    "v_mfma_f32_16x16x32_bf16 v[104:107], v[172:175], v[188:191], 0\n\t"
    "v_fmac_f32 v145, v119, v19\n\t"
    "v_fmac_f32 v144, v92, v20\n\t"
    "v_mfma_f32_16x16x32_bf16 v[104:107], v[176:179], v[192:195], v[104:107]\n\t"
    "v_fmac_f32 v145, v93, v21\n\t"
    "v_fmac_f32 v144, v94, v22\n\t"
    "s_waitcnt lgkmcnt(5)\n\t"
    "v_mfma_f32_16x16x32_bf16 v[108:111], v[164:167], v[196:199], 0\n\t"
    "v_fmac_f32 v145, v95, v23\n\t"
    "v_fmac_f32 v144, v96, v86\n\t"
    "v_mfma_f32_16x16x32_bf16 v[108:111], v[168:171], v[200:203], v[108:111]\n\t"
    "v_mfma_f32_16x16x32_bf16 v[112:115], v[212:215], v[180:183], 0\n\t"
    "v_fmac_f32 v145, v100, v24\n\t"
    "v_fmac_f32 v144, v101, v25\n\t"
    "v_mfma_f32_16x16x32_bf16 v[112:115], v[216:219], v[184:187], v[112:115]\n\t"
    "v_fmac_f32 v145, v102, v26\n\t"
    "v_fmac_f32 v144, v103, v27\n\t"
    "s_waitcnt lgkmcnt(2)\n\t"
    "v_mfma_f32_16x16x32_bf16 v[116:119], v[164:167], v[204:207], 0\n\t"
    "v_readfirstlane_b32 s60, v148\n\t"
    "v_fmac_f32 v145, v104, v28\n\t"
    "v_mfma_f32_16x16x32_bf16 v[116:119], v[168:171], v[208:211], v[116:119]\n\t"
    "v_fmac_f32 v144, v105, v29\n\t"
    "v_fmac_f32 v145, v106, v30\n\t"
    "ds_read_b128 v[164:167], v129 offset:0\n\t"
    "ds_read_b128 v[168:171], v129 offset:64\n\t"
    "v_mfma_f32_16x16x32_bf16 v[92:95], v[172:175], v[196:199], 0\n\t"
    "v_fmac_f32 v144, v107, v31\n\t"
    "v_fmac_f32 v145, v108, v32\n\t"
    "v_mfma_f32_16x16x32_bf16 v[92:95], v[176:179], v[200:203], v[92:95]\n\t"
    "v_fmac_f32 v144, v109, v33\n\t"
    "v_fmac_f32 v145, v110, v34\n\t"
    "s_waitcnt vmcnt(4)\n\t"
    "s_cmp_ge_u32 s55, s64\n\t"
    "s_cbranch_scc1 L_ahk11_%=\n\t"
    "L_ahb11_%=:\n\t"
    "v_mfma_f32_16x16x32_bf16 v[96:99], v[172:175], v[204:207], 0\n\t"
    "v_mad_u32_u24 v132, v132, s68, v151\n\t"
    "v_and_b32 v132, -16, v132\n\t"
    "v_mfma_f32_16x16x32_bf16 v[96:99], v[176:179], v[208:211], v[96:99]\n\t"
    "v_fmac_f32 v144, v111, v35\n\t"
    "v_fmac_f32 v145, v112, v87\n\t"
    "ds_read_b128 v[172:175], v130 offset:0\n\t"
    "ds_read_b128 v[176:179], v130 offset:64\n\t"
    "s_mul_i32 s66, s60, 400\n\t"
    "v_mfma_f32_16x16x32_bf16 v[100:103], v[212:215], v[188:191], 0\n\t"
    "v_fmac_f32 v144, v116, v36\n\t"
    "v_fmac_f32 v145, v117, v37\n\t"
    "v_mfma_f32_16x16x32_bf16 v[100:103], v[216:219], v[192:195], v[100:103]\n\t"
    "v_fmac_f32 v144, v118, v38\n\t"
    "v_fmac_f32 v145, v119, v39\n\t"
    "buffer_load_dwordx3 v[128:130], v153, %0, s66 offen\n\t"
    "v_mfma_f32_16x16x32_bf16 v[104:107], v[180:183], v[188:191], 0\n\t"
    "v_mad_u32_u24 v133, v133, s68, v151\n\t"
    "v_and_b32 v133, -16, v133\n\t"
    "v_mfma_f32_16x16x32_bf16 v[104:107], v[184:187], v[192:195], v[104:107]\n\t"
    "v_fmac_f32 v144, v92, v40\n\t"
    "v_fmac_f32 v145, v93, v41\n\t"
    "v_mfma_f32_16x16x32_bf16 v[108:111], v[180:183], v[196:199], 0\n\t"
    "v_fmac_f32 v144, v94, v42\n\t"
    "v_fmac_f32 v145, v95, v43\n\t"
    "v_mfma_f32_16x16x32_bf16 v[108:111], v[184:187], v[200:203], v[108:111]\n\t"
    "v_fmac_f32 v144, v96, v44\n\t"
    "v_fmac_f32 v145, v97, v45\n\t"
    "v_mfma_f32_16x16x32_bf16 v[112:115], v[180:183], v[204:207], 0\n\t"
    "v_fmac_f32 v144, v98, v46\n\t"
    "v_fmac_f32 v145, v99, v47\n\t"
    "v_mfma_f32_16x16x32_bf16 v[112:115], v[184:187], v[208:211], v[112:115]\n\t"
    "v_fmac_f32 v144, v100, v88\n\t"
    "v_mad_u32_u24 v134, v134, s68, v151\n\t"
    "ds_read_b128 v[180:183], v132 offset:0\n\t"
    "ds_read_b128 v[184:187], v132 offset:64\n\t"
    "v_mfma_f32_16x16x32_bf16 v[116:119], v[212:215], v[196:199], 0\n\t"
    "v_and_b32 v134, -16, v134\n\t"
    "v_fmac_f32 v145, v104, v48\n\t"
    "v_mfma_f32_16x16x32_bf16 v[116:119], v[216:219], v[200:203], v[116:119]\n\t"
    "v_fmac_f32 v144, v105, v49\n\t"
    "v_fmac_f32 v145, v106, v50\n\t"
    "v_mfma_f32_16x16x32_bf16 v[92:95], v[188:191], v[188:191], 0\n\t"
    "v_fmac_f32 v144, v107, v51\n\t"
    "v_mad_u32_u24 v135, v135, s68, v151\n\t"
    "v_mfma_f32_16x16x32_bf16 v[92:95], v[192:195], v[192:195], v[92:95]\n\t"
    "v_and_b32 v135, -16, v135\n\t"
    "v_fmac_f32 v145, v108, v52\n\t"
    "v_mfma_f32_16x16x32_bf16 v[96:99], v[188:191], v[196:199], 0\n\t"
    "v_fmac_f32 v144, v109, v53\n\t"
    "v_fmac_f32 v145, v110, v54\n\t"
    "v_mfma_f32_16x16x32_bf16 v[96:99], v[192:195], v[200:203], v[96:99]\n\t"
    "v_fmac_f32 v144, v111, v55\n\t"
    "v_fmac_f32 v145, v112, v56\n\t"
    "v_mfma_f32_16x16x32_bf16 v[100:103], v[188:191], v[204:207], 0\n\t"
    "v_fmac_f32 v144, v113, v57\n\t"
    "v_fmac_f32 v145, v114, v58\n\t"
    "v_mfma_f32_16x16x32_bf16 v[100:103], v[192:195], v[208:211], v[100:103]\n\t"
    "v_fmac_f32 v144, v115, v59\n\t"
    "v_fmac_f32 v145, v116, v89\n\t"
    "ds_read_b128 v[188:191], v133 offset:0\n\t"
    "ds_read_b128 v[192:195], v133 offset:64\n\t"
    "v_mfma_f32_16x16x32_bf16 v[104:107], v[212:215], v[204:207], 0\n\t"
    "v_fmac_f32 v144, v92, v60\n\t"
    "v_fmac_f32 v145, v93, v61\n\t"
    "v_mfma_f32_16x16x32_bf16 v[104:107], v[216:219], v[208:211], v[104:107]\n\t"
    "v_fmac_f32 v144, v94, v62\n\t"
    "v_fmac_f32 v145, v95, v63\n\t"
    "v_mfma_f32_16x16x32_bf16 v[108:111], v[196:199], v[196:199], 0\n\t"
    "v_fmac_f32 v144, v96, v64\n\t"
    "v_fmac_f32 v145, v97, v65\n\t"
    "v_mfma_f32_16x16x32_bf16 v[108:111], v[200:203], v[200:203], v[108:111]\n\t"
    "v_fmac_f32 v144, v98, v66\n\t"
    "v_fmac_f32 v145, v99, v67\n\t"
    "v_mfma_f32_16x16x32_bf16 v[112:115], v[196:199], v[204:207], 0\n\t"
    "v_fmac_f32 v144, v100, v68\n\t"
    "v_fmac_f32 v145, v101, v69\n\t"
    "v_mfma_f32_16x16x32_bf16 v[112:115], v[200:203], v[208:211], v[112:115]\n\t"
    "v_fmac_f32 v144, v102, v70\n\t"
    "v_fmac_f32 v145, v103, v71\n\t"
    "ds_read_b128 v[196:199], v134 offset:0\n\t"
    "ds_read_b128 v[200:203], v134 offset:64\n\t"
    "v_mfma_f32_16x16x32_bf16 v[116:119], v[204:207], v[204:207], 0\n\t"
    "v_fmac_f32 v144, v104, v90\n\t"
    "v_mfma_f32_16x16x32_bf16 v[116:119], v[208:211], v[208:211], v[116:119]\n\t"
    "ds_read_b128 v[204:207], v135 offset:0\n\t"
    "ds_read_b128 v[208:211], v135 offset:64\n\t"
    "s_mov_b32 s52, s53\n\t"
    "s_mov_b32 s53, s54\n\t"
    "s_mov_b32 s54, s55\n\t"
    "s_mov_b32 s55, s60\n\t"
    "s_cmp_lt_u32 s53, s64\n\t"
    "s_cbranch_scc1 L_apnn_%=\n\t"
    "L_adone_%=:\n\t"
    "s_mul_i32 s69, s52, 256\n\t"
    "s_add_u32 s69, s69, s50\n\t"
    "v_fmac_f32 v145, v108, v72\n\t"
    "v_fmac_f32 v144, v109, v73\n\t"
    "v_fmac_f32 v145, v110, v74\n\t"
    "v_fmac_f32 v144, v111, v75\n\t"
    "v_fmac_f32 v145, v112, v76\n\t"
    "v_fmac_f32 v144, v113, v77\n\t"
    "v_fmac_f32 v145, v114, v78\n\t"
    "v_fmac_f32 v144, v115, v79\n\t"
    "v_fmac_f32 v145, v116, v80\n\t"
    "v_fmac_f32 v144, v117, v81\n\t"
    "v_fmac_f32 v145, v118, v82\n\t"
    "v_fmac_f32 v144, v119, v83\n\t"
    "v_add_f32 v40, v144, v145\n\t"
    "L_aend_%=:\n\t"
    "s_branch L_aend2_%=\n\t"
    "L_ahk00_%=:\n\t"
    "v_mov_b32 v147, 3112\n\t"
    "s_cmp_lt_u32 s54, s64\n\t"
    "ds_read_b32 v147, v147\n\t"
    "s_cbranch_scc1 L_ahb00_%=\n\t"
    "s_waitcnt vmcnt(3)\n\t"
    "v_add_u32 v137, 0, v155\n\t"
    "v_add_u32 v138, 16, v155\n\t"
    "v_add_u32 v140, 32, v155\n\t"
    "v_add_u32 v141, 48, v155\n\t"
    "v_add_u32 v142, 64, v155\n\t"
    "v_add_u32 v143, 80, v155\n\t"
    "s_branch L_ahb00_%=\n\t"
    "L_ahk10_%=:\n\t"
    "s_waitcnt lgkmcnt(4)\n\t"
    "v_readfirstlane_b32 s65, v147\n\t"
    "s_nop 0\n\t"
    "s_cmp_eq_u32 s65, 0\n\t"
    "s_cbranch_scc1 L_ahb10_%=\n\t"
    "s_cmp_lt_u32 s62, 2\n\t"
    "s_cbranch_scc1 L_mel1_%=\n\t"
    "buffer_load_dwordx4 v[220:223], v127, %12, s61 offen offset:2048 sc1\n\t"
    "buffer_load_dwordx4 v[224:227], v127, %12, s61 offen offset:3072 sc1\n\t"
    "s_add_u32 s61, s61, 0x1000\n\t"
    "buffer_load_dwordx4 v[228:231], v127, %12, s61 offen offset:2048 sc1\n\t"
    "buffer_load_dwordx4 v[232:235], v127, %12, s61 offen offset:3072 sc1\n\t"
    "s_add_u32 s61, s61, 0x1000\n\t"
    "buffer_load_dwordx4 v[236:239], v127, %12, s61 offen offset:2048 sc1\n\t"
    "buffer_load_dwordx4 v[240:243], v127, %12, s61 offen offset:3072 sc1\n\t"
    "s_add_u32 s61, s61, 0x1000\n\t"
    "buffer_load_dwordx4 v[244:247], v127, %12, s61 offen offset:2048 sc1\n\t"
    "buffer_load_dwordx4 v[248:251], v127, %12, s61 offen offset:3072 sc1\n\t"
    "s_add_u32 s61, s61, 0x1000\n\t"
    "buffer_load_dwordx4 v[252:255], v127, %12, s61 offen offset:2048 sc1\n\t"
    "s_mov_b32 s62, 1\n\t"
    "L_mel1_%=:\n\t"
    "s_cmp_lt_u32 s54, s64\n\t"
    "s_cbranch_scc1 L_ahb10_%=\n\t"
    "buffer_load_dwordx4 v[0:3], v127, %12, s63 offen offset:0 sc1\n\t"
    "buffer_load_dwordx4 v[4:7], v127, %12, s63 offen offset:1024 sc1\n\t"
    "buffer_load_dwordx4 v[8:11], v127, %12, s63 offen offset:2048 sc1\n\t"
    "buffer_load_dwordx4 v[12:15], v127, %12, s63 offen offset:3072 sc1\n\t"
    "s_add_u32 s63, s63, 0x1000\n\t"
    "s_mov_b32 s62, 0\n\t"
    "s_branch L_ahb10_%=\n\t"
    "L_ahk01_%=:\n\t"
    "v_mov_b32 v147, 3112\n\t"
    "s_cmp_lt_u32 s54, s64\n\t"
    "ds_read_b32 v147, v147\n\t"
    "s_cbranch_scc1 L_ahb01_%=\n\t"
    "s_waitcnt vmcnt(3)\n\t"
    "v_add_u32 v129, 0, v155\n\t"
    "v_add_u32 v130, 16, v155\n\t"
    "v_add_u32 v132, 32, v155\n\t"
    "v_add_u32 v133, 48, v155\n\t"
    "v_add_u32 v134, 64, v155\n\t"
    "v_add_u32 v135, 80, v155\n\t"
    "s_branch L_ahb01_%=\n\t"
    "L_ahk11_%=:\n\t"
    "s_waitcnt lgkmcnt(4)\n\t"
    "v_readfirstlane_b32 s65, v147\n\t"
    "s_nop 0\n\t"
    "s_cmp_eq_u32 s65, 0\n\t"
    "s_cbranch_scc1 L_ahb11_%=\n\t"
    "s_cmp_lt_u32 s62, 2\n\t"
    "s_cbranch_scc1 L_mel3_%=\n\t"
    "buffer_load_dwordx4 v[220:223], v127, %12, s61 offen offset:2048 sc1\n\t"
    "buffer_load_dwordx4 v[224:227], v127, %12, s61 offen offset:3072 sc1\n\t"
    "s_add_u32 s61, s61, 0x1000\n\t"
    "buffer_load_dwordx4 v[228:231], v127, %12, s61 offen offset:2048 sc1\n\t"
    "buffer_load_dwordx4 v[232:235], v127, %12, s61 offen offset:3072 sc1\n\t"
    "s_add_u32 s61, s61, 0x1000\n\t"
    "buffer_load_dwordx4 v[236:239], v127, %12, s61 offen offset:2048 sc1\n\t"
    "buffer_load_dwordx4 v[240:243], v127, %12, s61 offen offset:3072 sc1\n\t"
    "s_add_u32 s61, s61, 0x1000\n\t"
    "buffer_load_dwordx4 v[244:247], v127, %12, s61 offen offset:2048 sc1\n\t"
    "buffer_load_dwordx4 v[248:251], v127, %12, s61 offen offset:3072 sc1\n\t"
    "s_add_u32 s61, s61, 0x1000\n\t"
    "buffer_load_dwordx4 v[252:255], v127, %12, s61 offen offset:2048 sc1\n\t"
    "s_mov_b32 s62, 1\n\t"
    "L_mel3_%=:\n\t"
    "s_cmp_lt_u32 s54, s64\n\t"
    "s_cbranch_scc1 L_ahb11_%=\n\t"
    "buffer_load_dwordx4 v[0:3], v127, %12, s63 offen offset:0 sc1\n\t"
    "buffer_load_dwordx4 v[4:7], v127, %12, s63 offen offset:1024 sc1\n\t"
    "buffer_load_dwordx4 v[8:11], v127, %12, s63 offen offset:2048 sc1\n\t"
    "buffer_load_dwordx4 v[12:15], v127, %12, s63 offen offset:3072 sc1\n\t"
    "s_add_u32 s63, s63, 0x1000\n\t"
    "s_mov_b32 s62, 0\n\t"
    "s_branch L_ahb11_%=\n\t"
    "L_aend2_%=:\n\t"
    "s_setprio 0\n\t"
    "v_lshlrev_b32 v113, 16, v127\n\t"
    "v_add_u32 v113, 3108, v113\n\t"
    "s_cmp_eq_u32 s62, 0\n\t"
    "s_cbranch_scc1 L_mpf_%=\n\t"
    "L_ml0_%=:\n\t"
    "ds_read_b32 v114, v113 offset:4\n\t"
    "s_waitcnt lgkmcnt(0)\n\t"
    "v_readfirstlane_b32 s65, v114\n\t"
    "s_nop 0\n\t"
    "s_cmp_lg_u32 s65, 0\n\t"
    "s_cbranch_scc1 L_mm0_%=\n\t"
    "s_sleep 2\n\t"
    "s_branch L_ml0_%=\n\t"
    "L_mm0_%=:\n\t"
    "s_cmp_lt_u32 s62, 2\n\t"
    "s_cbranch_scc1 L_mel5_%=\n\t"
    "buffer_load_dwordx4 v[220:223], v127, %12, s61 offen offset:2048 sc1\n\t"
    "buffer_load_dwordx4 v[224:227], v127, %12, s61 offen offset:3072 sc1\n\t"
    "s_add_u32 s61, s61, 0x1000\n\t"
    "buffer_load_dwordx4 v[228:231], v127, %12, s61 offen offset:2048 sc1\n\t"
    "buffer_load_dwordx4 v[232:235], v127, %12, s61 offen offset:3072 sc1\n\t"
    "s_add_u32 s61, s61, 0x1000\n\t"
    "buffer_load_dwordx4 v[236:239], v127, %12, s61 offen offset:2048 sc1\n\t"
    "buffer_load_dwordx4 v[240:243], v127, %12, s61 offen offset:3072 sc1\n\t"
    "s_add_u32 s61, s61, 0x1000\n\t"
    "buffer_load_dwordx4 v[244:247], v127, %12, s61 offen offset:2048 sc1\n\t"
    "buffer_load_dwordx4 v[248:251], v127, %12, s61 offen offset:3072 sc1\n\t"
    "s_add_u32 s61, s61, 0x1000\n\t"
    "buffer_load_dwordx4 v[252:255], v127, %12, s61 offen offset:2048 sc1\n\t"
    "s_mov_b32 s62, 1\n\t"
    "L_mel5_%=:\n\t"
    "buffer_load_dwordx4 v[0:3], v127, %12, s63 offen offset:0 sc1\n\t"
    "buffer_load_dwordx4 v[4:7], v127, %12, s63 offen offset:1024 sc1\n\t"
    "buffer_load_dwordx4 v[8:11], v127, %12, s63 offen offset:2048 sc1\n\t"
    "buffer_load_dwordx4 v[12:15], v127, %12, s63 offen offset:3072 sc1\n\t"
    "s_add_u32 s63, s63, 0x1000\n\t"
    "s_mov_b32 s62, 0\n\t"
    "L_mpf_%=:\n\t"
    "v_lshrrev_b32 v105, 4, v127\n\t"
    "v_and_b32 v105, 48, v105\n\t"
    "s_movk_i32 s65, 130\n\t"
    "v_mad_u32_u24 v104, v155, s65, v105\n\t"
    "v_add_u32 v105, 14170, v104\n\t"
    "v_and_b32 v104, -16, v104\n\t"
    "v_and_b32 v105, -16, v105\n\t"
    "buffer_load_dwordx4 v[128:131], v127, %12, s61 offen offset:3072 sc1\n\t"
    "s_add_u32 s61, s61, 0x1000\n\t"
    "buffer_load_dwordx4 v[132:135], v127, %12, s61 offen offset:2048 sc1\n\t"
    "ds_read_b128 v[156:159], v104 offset:12480\n\t"
    "ds_read_b128 v[160:163], v104 offset:12544\n\t"
    "buffer_load_dwordx4 v[136:139], v127, %12, s61 offen offset:3072 sc1\n\t"
    "s_add_u32 s61, s61, 0x1000\n\t"
    "buffer_load_dwordx4 v[140:143], v127, %12, s61 offen offset:2048 sc1\n\t"
    "ds_read_b128 v[212:215], v105 offset:0\n\t"
    "ds_read_b128 v[216:219], v105 offset:64\n\t"
    "buffer_load_dwordx4 v[144:147], v127, %12, s61 offen offset:3072 sc1\n\t"
    "s_add_u32 s61, s61, 0x1000\n\t"
    "buffer_load_dwordx4 v[148:151], v127, %12, s61 offen offset:2048 sc1\n\t"
    "buffer_load_dwordx4 v[152:155], v127, %12, s61 offen offset:3072 sc1\n\t"
    "s_add_u32 s61, s61, 0x1000\n\t"
    "v_mov_b32 v114, 1\n\t"
    "ds_add_u32 v113, v114\n\t"
    "s_waitcnt lgkmcnt(5)\n\t"
    "s_waitcnt vmcnt(7)\n\t"
    "v_mfma_f32_16x16x32_bf16 v[16:19], v[0:3], v[164:167], 0\n\t"
    "v_mfma_f32_16x16x32_bf16 v[16:19], v[4:7], v[168:171], v[16:19]\n\t"
    "v_mfma_f32_16x16x32_bf16 v[16:19], v[8:11], v[220:223], v[16:19]\n\t"
    "v_mfma_f32_16x16x32_bf16 v[16:19], v[12:15], v[224:227], v[16:19]\n\t"
    "v_mfma_f32_16x16x32_bf16 v[20:23], v[0:3], v[172:175], 0\n\t"
    "v_mfma_f32_16x16x32_bf16 v[20:23], v[4:7], v[176:179], v[20:23]\n\t"
    "v_mfma_f32_16x16x32_bf16 v[20:23], v[8:11], v[228:231], v[20:23]\n\t"
    "v_mfma_f32_16x16x32_bf16 v[20:23], v[12:15], v[232:235], v[20:23]\n\t"
    "v_mfma_f32_16x16x32_bf16 v[80:83], v[0:3], v[180:183], 0\n\t"
    "v_mfma_f32_16x16x32_bf16 v[80:83], v[4:7], v[184:187], v[80:83]\n\t"
    "v_mfma_f32_16x16x32_bf16 v[80:83], v[8:11], v[236:239], v[80:83]\n\t"
    "v_mfma_f32_16x16x32_bf16 v[80:83], v[12:15], v[240:243], v[80:83]\n\t"
    "v_cvt_pk_bf16_f32 v24, v16, v17\n\t"
    "v_cvt_pk_bf16_f32 v25, v18, v19\n\t"
    "v_mfma_f32_16x16x32_bf16 v[16:19], v[0:3], v[188:191], 0\n\t"
    "v_mfma_f32_16x16x32_bf16 v[16:19], v[4:7], v[192:195], v[16:19]\n\t"
    "v_mfma_f32_16x16x32_bf16 v[16:19], v[8:11], v[244:247], v[16:19]\n\t"
    "v_mfma_f32_16x16x32_bf16 v[16:19], v[12:15], v[248:251], v[16:19]\n\t"
    "v_cvt_pk_bf16_f32 v26, v20, v21\n\t"
    "v_cvt_pk_bf16_f32 v27, v22, v23\n\t"
    "s_waitcnt vmcnt(6)\n\t"
    "v_mfma_f32_16x16x32_bf16 v[20:23], v[0:3], v[196:199], 0\n\t"
    "v_mfma_f32_16x16x32_bf16 v[20:23], v[4:7], v[200:203], v[20:23]\n\t"
    "v_mfma_f32_16x16x32_bf16 v[20:23], v[8:11], v[252:255], v[20:23]\n\t"
    "v_mfma_f32_16x16x32_bf16 v[20:23], v[12:15], v[128:131], v[20:23]\n\t"
    "v_cvt_pk_bf16_f32 v28, v80, v81\n\t"
    "v_cvt_pk_bf16_f32 v29, v82, v83\n\t"
    "s_waitcnt lgkmcnt(0)\n\t"
    "s_waitcnt vmcnt(4)\n\t"
    "v_mfma_f32_16x16x32_bf16 v[80:83], v[0:3], v[204:207], 0\n\t"
    "v_mfma_f32_16x16x32_bf16 v[80:83], v[4:7], v[208:211], v[80:83]\n\t"
    "v_mfma_f32_16x16x32_bf16 v[80:83], v[8:11], v[132:135], v[80:83]\n\t"
    "v_mfma_f32_16x16x32_bf16 v[80:83], v[12:15], v[136:139], v[80:83]\n\t"
    "v_cvt_pk_bf16_f32 v30, v16, v17\n\t"
    "v_cvt_pk_bf16_f32 v31, v18, v19\n\t"
    "s_waitcnt vmcnt(2)\n\t"
    "v_mfma_f32_16x16x32_bf16 v[16:19], v[0:3], v[156:159], 0\n\t"
    "buffer_load_dwordx4 v[100:103], v127, %12, s84 offen offset:0 sc1\n\t"
    "s_add_u32 s84, s84, s97\n\t"
    "v_mfma_f32_16x16x32_bf16 v[16:19], v[4:7], v[160:163], v[16:19]\n\t"
    "buffer_load_dwordx4 v[104:107], v127, %12, s85 offen offset:0 sc1\n\t"
    "s_add_u32 s85, s85, s97\n\t"
    "v_mfma_f32_16x16x32_bf16 v[16:19], v[8:11], v[140:143], v[16:19]\n\t"
    "v_mfma_f32_16x16x32_bf16 v[16:19], v[12:15], v[144:147], v[16:19]\n\t"
    "v_cvt_pk_bf16_f32 v32, v20, v21\n\t"
    "v_cvt_pk_bf16_f32 v33, v22, v23\n\t"
    "s_waitcnt vmcnt(2)\n\t"
    "v_mfma_f32_16x16x32_bf16 v[20:23], v[0:3], v[212:215], 0\n\t"
    "buffer_load_dwordx4 v[108:111], v127, %12, s86 offen offset:0 sc1\n\t"
    "s_add_u32 s86, s86, s97\n\t"
    "v_mfma_f32_16x16x32_bf16 v[20:23], v[4:7], v[216:219], v[20:23]\n\t"
    "buffer_load_dwordx4 v[112:115], v127, %12, s87 offen offset:0 sc1\n\t"
    "s_add_u32 s87, s87, s97\n\t"
    "v_mfma_f32_16x16x32_bf16 v[20:23], v[8:11], v[148:151], v[20:23]\n\t"
    "v_mfma_f32_16x16x32_bf16 v[20:23], v[12:15], v[152:155], v[20:23]\n\t"
    "v_cvt_pk_bf16_f32 v34, v80, v81\n\t"
    "v_cvt_pk_bf16_f32 v35, v82, v83\n\t"
    "v_cvt_pk_bf16_f32 v36, v16, v17\n\t"
    "v_cvt_pk_bf16_f32 v37, v18, v19\n\t"
    "s_nop 3\n\t"
    "v_cvt_pk_bf16_f32 v38, v20, v21\n\t"
    "v_cvt_pk_bf16_f32 v39, v22, v23\n\t"
    "v_lshrrev_b32 v41, 2, v127\n\t"
    "s_and_b32 s65, %14, 8\n\t"
    "s_lshl_b32 s65, s65, 10\n\t"
    "s_add_u32 s63, s63, s65\n\t"
    "buffer_store_dword v40, v41, %12, s69 offen sc1\n\t"
    "v_mov_b32 v82, 3108\n\t"
    "s_cmp_eq_u32 %16, 0\n\t"
    "s_cselect_b32 s47, 0x1ff, 0\n\t"
    "L_my_%=:\n\t"
    "ds_read_b32 v81, v82\n\t"
    "s_waitcnt lgkmcnt(0)\n\t"
    "v_cmp_gt_u32 vcc, 8, v81\n\t"
    "s_cbranch_vccz L_mz_%=\n\t"
    "s_sleep 2\n\t"
    "s_branch L_my_%=\n\t"
    "L_mz_%=:\n\t"
    "ds_write_b64 v125, v[24:25] offset:0\n\t"
    "v_mov_b64 v[0:1], 0\n\t"
    "ds_write_b64 v125, v[26:27] offset:128\n\t"
    "v_mov_b64 v[2:3], 0\n\t"
    "ds_write_b64 v125, v[28:29] offset:256\n\t"
    "v_mov_b64 v[4:5], 0\n\t"
    "ds_write_b64 v125, v[30:31] offset:384\n\t"
    "v_mov_b64 v[6:7], 0\n\t"
    "ds_write_b64 v125, v[32:33] offset:512\n\t"
    "ds_write_b64 v125, v[34:35] offset:640\n\t"
    "ds_write_b64 v125, v[36:37] offset:768\n\t"
    "ds_write_b64 v125, v[38:39] offset:872\n\t"
    "v_add_u32 v125, s93, v125\n\t"
    "s_sub_u32 s93, 0, s93\n\t"
    "s_waitcnt lgkmcnt(0)\n\t"
    "s_barrier\n\t"
    "s_memrealtime s[44:45]\n\t"
    "s_waitcnt lgkmcnt(0)\n\t"
    "s_waitcnt vmcnt(4)\n\t"
    "v_add_u32_sdwa v126, s92, v100 dst_sel:DWORD dst_unused:UNUSED_PAD src0_sel:DWORD src1_sel:WORD_1\n\t"
    "v_add_u32_sdwa v100, s92, v100 dst_sel:DWORD dst_unused:UNUSED_PAD src0_sel:DWORD src1_sel:WORD_0\n\t"
    "ds_read_b64 v[84:85], v100 offset:0\n\t"
    "ds_read_b64 v[86:87], v126 offset:0\n\t"
    "v_add_u32_sdwa v90, s92, v101 dst_sel:DWORD dst_unused:UNUSED_PAD src0_sel:DWORD src1_sel:WORD_1\n\t"
    "v_add_u32_sdwa v101, s92, v101 dst_sel:DWORD dst_unused:UNUSED_PAD src0_sel:DWORD src1_sel:WORD_0\n\t"
    "ds_read_b64 v[88:89], v101 offset:0\n\t"
    "ds_read_b64 v[90:91], v90 offset:0\n\t"
    "v_add_u32 v63, s96, v127\n\t"
    "s_add_u32 s96, s96, 0x1000\n\t"
    "s_and_b32 s96, s96, 0x3000\n\t"
    "v_add_u32 v63, 128000, v63\n\t"
    "ds_read_b128 v[48:51], v63 offset:0\n\t"
    "ds_read_b128 v[52:55], v63 offset:1024\n\t"
    "ds_read_b128 v[56:59], v63 offset:2048\n\t"
    "ds_read_b128 v[60:63], v63 offset:3072\n\t"
    "v_add_u32_sdwa v126, s92, v102 dst_sel:DWORD dst_unused:UNUSED_PAD src0_sel:DWORD src1_sel:WORD_1\n\t"
    "v_add_u32_sdwa v102, s92, v102 dst_sel:DWORD dst_unused:UNUSED_PAD src0_sel:DWORD src1_sel:WORD_0\n\t"
    "ds_read_b64 v[92:93], v102 offset:0\n\t"
    "ds_read_b64 v[94:95], v126 offset:0\n\t"
    "v_add_u32_sdwa v98, s92, v103 dst_sel:DWORD dst_unused:UNUSED_PAD src0_sel:DWORD src1_sel:WORD_1\n\t"
    "v_add_u32_sdwa v103, s92, v103 dst_sel:DWORD dst_unused:UNUSED_PAD src0_sel:DWORD src1_sel:WORD_0\n\t"
    "ds_read_b64 v[96:97], v103 offset:0\n\t"
    "ds_read_b64 v[98:99], v98 offset:0\n\t"
    "buffer_load_dwordx4 v[100:103], v127, %12, s88 offen offset:0 sc1\n\t"
    "s_add_u32 s88, s88, s97\n\t"
    "s_waitcnt vmcnt(4)\n\t"
    "v_add_u32_sdwa v126, s92, v104 dst_sel:DWORD dst_unused:UNUSED_PAD src0_sel:DWORD src1_sel:WORD_1\n\t"
    "v_add_u32_sdwa v104, s92, v104 dst_sel:DWORD dst_unused:UNUSED_PAD src0_sel:DWORD src1_sel:WORD_0\n\t"
    "v_mov_b64 v[8:9], 0\n\t"
    "v_mov_b64 v[10:11], 0\n\t"
    "v_mov_b64 v[12:13], 0\n\t"
    "v_mov_b64 v[14:15], 0\n\t"
    "v_mov_b64 v[16:17], 0\n\t"
    "v_mov_b64 v[18:19], 0\n\t"
    "s_cmp_lt_u32 %16, 4\n\t"
    "s_cbranch_scc1 L_spp0_%=\n\t"
    "s_setprio 1\n\t"
    "L_spp0_%=:\n\t"
    "s_waitcnt lgkmcnt(8)\n\t"
    "v_smfmac_f32_16x16x64_bf16 v[0:3], v[120:123], v[84:91], v124\n\t"
    "ds_read_b64 v[84:85], v104 offset:0\n\t"
    "s_waitcnt lgkmcnt(5)\n\t"
    "v_mfma_f32_16x16x32_bf16 v[116:119], v[48:51], v[164:167], 0\n\t"
    "ds_read_b64 v[86:87], v126 offset:0\n\t"
    "v_mfma_f32_16x16x32_bf16 v[116:119], v[52:55], v[168:171], v[116:119]\n\t"
    "v_add_u32_sdwa v90, s92, v105 dst_sel:DWORD dst_unused:UNUSED_PAD src0_sel:DWORD src1_sel:WORD_1\n\t"
    "v_add_u32_sdwa v105, s92, v105 dst_sel:DWORD dst_unused:UNUSED_PAD src0_sel:DWORD src1_sel:WORD_0\n\t"
    "ds_read_b64 v[88:89], v105 offset:0\n\t"
    "v_mfma_f32_16x16x32_bf16 v[116:119], v[56:59], v[220:223], v[116:119]\n\t"
    "ds_read_b64 v[90:91], v90 offset:0\n\t"
    "v_mfma_f32_16x16x32_bf16 v[116:119], v[60:63], v[224:227], v[116:119]\n\t"
    "v_add_u32_sdwa v126, s92, v106 dst_sel:DWORD dst_unused:UNUSED_PAD src0_sel:DWORD src1_sel:WORD_1\n\t"
    "v_add_u32_sdwa v106, s92, v106 dst_sel:DWORD dst_unused:UNUSED_PAD src0_sel:DWORD src1_sel:WORD_0\n\t"
    "v_mfma_f32_16x16x32_bf16 v[80:83], v[48:51], v[172:175], 0\n\t"
    "v_mov_b64 v[20:21], 0\n\t"
    "v_mov_b64 v[22:23], 0\n\t"
    "v_mov_b64 v[24:25], 0\n\t"
    "v_mov_b64 v[26:27], 0\n\t"
    "v_mov_b64 v[28:29], 0\n\t"
    "v_mov_b64 v[30:31], 0\n\t"
    "s_waitcnt vmcnt(3) lgkmcnt(4)\n\t"
    "v_smfmac_f32_16x16x64_bf16 v[4:7], v[120:123], v[92:99], v124\n\t"
    "ds_read_b64 v[92:93], v106 offset:0\n\t"
    "v_mfma_f32_16x16x32_bf16 v[80:83], v[52:55], v[176:179], v[80:83]\n\t"
    "ds_read_b64 v[94:95], v126 offset:0\n\t"
    "v_mfma_f32_16x16x32_bf16 v[80:83], v[56:59], v[228:231], v[80:83]\n\t"
    "v_add_u32_sdwa v98, s92, v107 dst_sel:DWORD dst_unused:UNUSED_PAD src0_sel:DWORD src1_sel:WORD_1\n\t"
    "v_add_u32_sdwa v107, s92, v107 dst_sel:DWORD dst_unused:UNUSED_PAD src0_sel:DWORD src1_sel:WORD_0\n\t"
    "ds_read_b64 v[96:97], v107 offset:0\n\t"
    "v_mfma_f32_16x16x32_bf16 v[80:83], v[60:63], v[232:235], v[80:83]\n\t"
    "ds_read_b64 v[98:99], v98 offset:0\n\t"
    "v_cvt_pk_bf16_f32 v116, v116, v117\n\t"
    "v_cvt_pk_bf16_f32 v117, v118, v119\n\t"
    "ds_write_b64 v125, v[116:117] offset:0\n\t"
    "v_mfma_f32_16x16x32_bf16 v[116:119], v[48:51], v[180:183], 0\n\t"
    "buffer_load_dwordx4 v[104:107], v127, %12, s89 offen offset:0 sc1\n\t"
    "s_add_u32 s89, s89, s97\n\t"
    "v_add_u32_sdwa v126, s92, v108 dst_sel:DWORD dst_unused:UNUSED_PAD src0_sel:DWORD src1_sel:WORD_1\n\t"
    "v_add_u32_sdwa v108, s92, v108 dst_sel:DWORD dst_unused:UNUSED_PAD src0_sel:DWORD src1_sel:WORD_0\n\t"
    "v_mfma_f32_16x16x32_bf16 v[116:119], v[52:55], v[184:187], v[116:119]\n\t"
    "v_mov_b64 v[32:33], 0\n\t"
    "v_mov_b64 v[34:35], 0\n\t"
    "v_mov_b64 v[36:37], 0\n\t"
    "v_mov_b64 v[38:39], 0\n\t"
    "v_mov_b64 v[40:41], 0\n\t"
    "v_mov_b64 v[42:43], 0\n\t"
    "v_mov_b64 v[44:45], 0\n\t"
    "v_mov_b64 v[46:47], 0\n\t"
    "s_waitcnt lgkmcnt(5)\n\t"
    "v_smfmac_f32_16x16x64_bf16 v[8:11], v[120:123], v[84:91], v124\n\t"
    "ds_read_b64 v[84:85], v108 offset:0\n\t"
    "v_mfma_f32_16x16x32_bf16 v[116:119], v[56:59], v[236:239], v[116:119]\n\t"
    "ds_read_b64 v[86:87], v126 offset:0\n\t"
    "v_mfma_f32_16x16x32_bf16 v[116:119], v[60:63], v[240:243], v[116:119]\n\t"
    "v_add_u32_sdwa v90, s92, v109 dst_sel:DWORD dst_unused:UNUSED_PAD src0_sel:DWORD src1_sel:WORD_1\n\t"
    "v_add_u32_sdwa v109, s92, v109 dst_sel:DWORD dst_unused:UNUSED_PAD src0_sel:DWORD src1_sel:WORD_0\n\t"
    "ds_read_b64 v[88:89], v109 offset:0\n\t"
    "v_cvt_pk_bf16_f32 v80, v80, v81\n\t"
    "v_cvt_pk_bf16_f32 v81, v82, v83\n\t"
    "ds_write_b64 v125, v[80:81] offset:128\n\t"
    "v_mfma_f32_16x16x32_bf16 v[80:83], v[48:51], v[188:191], 0\n\t"
    "ds_read_b64 v[90:91], v90 offset:0\n\t"
    "v_mfma_f32_16x16x32_bf16 v[80:83], v[52:55], v[192:195], v[80:83]\n\t"
    "v_add_u32_sdwa v126, s92, v110 dst_sel:DWORD dst_unused:UNUSED_PAD src0_sel:DWORD src1_sel:WORD_1\n\t"
    "v_add_u32_sdwa v110, s92, v110 dst_sel:DWORD dst_unused:UNUSED_PAD src0_sel:DWORD src1_sel:WORD_0\n\t"
    "v_mfma_f32_16x16x32_bf16 v[80:83], v[56:59], v[244:247], v[80:83]\n\t"
    "s_waitcnt vmcnt(3) lgkmcnt(6)\n\t"
    "v_smfmac_f32_16x16x64_bf16 v[12:15], v[120:123], v[92:99], v124\n\t"
    "ds_read_b64 v[92:93], v110 offset:0\n\t"
    "v_mfma_f32_16x16x32_bf16 v[80:83], v[60:63], v[248:251], v[80:83]\n\t"
    "ds_read_b64 v[94:95], v126 offset:0\n\t"
    "v_cvt_pk_bf16_f32 v116, v116, v117\n\t"
    "v_cvt_pk_bf16_f32 v117, v118, v119\n\t"
    "ds_write_b64 v125, v[116:117] offset:256\n\t"
    "v_mfma_f32_16x16x32_bf16 v[116:119], v[48:51], v[196:199], 0\n\t"
    "v_add_u32_sdwa v98, s92, v111 dst_sel:DWORD dst_unused:UNUSED_PAD src0_sel:DWORD src1_sel:WORD_1\n\t"
    "v_add_u32_sdwa v111, s92, v111 dst_sel:DWORD dst_unused:UNUSED_PAD src0_sel:DWORD src1_sel:WORD_0\n\t"
    "ds_read_b64 v[96:97], v111 offset:0\n\t"
    "v_mfma_f32_16x16x32_bf16 v[116:119], v[52:55], v[200:203], v[116:119]\n\t"
    "ds_read_b64 v[98:99], v98 offset:0\n\t"
    "v_mfma_f32_16x16x32_bf16 v[116:119], v[56:59], v[252:255], v[116:119]\n\t"
    "buffer_load_dwordx4 v[108:111], v127, %12, s90 offen offset:0 sc1\n\t"
    "s_add_u32 s90, s90, s97\n\t"
    "v_add_u32_sdwa v126, s92, v112 dst_sel:DWORD dst_unused:UNUSED_PAD src0_sel:DWORD src1_sel:WORD_1\n\t"
    "v_add_u32_sdwa v112, s92, v112 dst_sel:DWORD dst_unused:UNUSED_PAD src0_sel:DWORD src1_sel:WORD_0\n\t"
    "v_mfma_f32_16x16x32_bf16 v[116:119], v[60:63], v[128:131], v[116:119]\n\t"
    "v_cvt_pk_bf16_f32 v0, v0, v1\n\t"
    "v_cvt_pk_bf16_f32 v1, v2, v3\n\t"
    "v_cvt_pk_bf16_f32 v80, v80, v81\n\t"
    "v_cvt_pk_bf16_f32 v81, v82, v83\n\t"
    "ds_write_b64 v125, v[80:81] offset:384\n\t"
    "v_add_u32 v79, s96, v127\n\t"
    "s_add_u32 s96, s96, 0x1000\n\t"
    "s_and_b32 s96, s96, 0x3000\n\t"
    "v_add_u32 v79, 128000, v79\n\t"
    "ds_read_b128 v[64:67], v79 offset:0\n\t"
    "ds_read_b128 v[68:71], v79 offset:1024\n\t"
    "ds_read_b128 v[72:75], v79 offset:2048\n\t"
    "ds_read_b128 v[76:79], v79 offset:3072\n\t"
    "s_waitcnt lgkmcnt(10)\n\t"
    "v_smfmac_f32_16x16x64_bf16 v[16:19], v[120:123], v[84:91], v124\n\t"
    "ds_read_b64 v[84:85], v112 offset:0\n\t"
    "v_mfma_f32_16x16x32_bf16 v[80:83], v[48:51], v[204:207], 0\n\t"
    "ds_read_b64 v[86:87], v126 offset:0\n\t"
    "v_mfma_f32_16x16x32_bf16 v[80:83], v[52:55], v[208:211], v[80:83]\n\t"
    "v_add_u32_sdwa v90, s92, v113 dst_sel:DWORD dst_unused:UNUSED_PAD src0_sel:DWORD src1_sel:WORD_1\n\t"
    "v_add_u32_sdwa v113, s92, v113 dst_sel:DWORD dst_unused:UNUSED_PAD src0_sel:DWORD src1_sel:WORD_0\n\t"
    "ds_read_b64 v[88:89], v113 offset:0\n\t"
    "v_mfma_f32_16x16x32_bf16 v[80:83], v[56:59], v[132:135], v[80:83]\n\t"
    "ds_read_b64 v[90:91], v90 offset:0\n\t"
    "v_mfma_f32_16x16x32_bf16 v[80:83], v[60:63], v[136:139], v[80:83]\n\t"
    "v_add_u32_sdwa v126, s92, v114 dst_sel:DWORD dst_unused:UNUSED_PAD src0_sel:DWORD src1_sel:WORD_1\n\t"
    "v_add_u32_sdwa v114, s92, v114 dst_sel:DWORD dst_unused:UNUSED_PAD src0_sel:DWORD src1_sel:WORD_0\n\t"
    "v_cvt_pk_bf16_f32 v116, v116, v117\n\t"
    "v_cvt_pk_bf16_f32 v117, v118, v119\n\t"
    "ds_write_b64 v125, v[116:117] offset:512\n\t"
    "v_mfma_f32_16x16x32_bf16 v[116:119], v[48:51], v[156:159], 0\n\t"
    "v_cvt_pk_bf16_f32 v2, v4, v5\n\t"
    "v_cvt_pk_bf16_f32 v3, v6, v7\n\t"
    "v_mov_b64 v[4:5], 0\n\t"
    "v_mov_b64 v[6:7], 0\n\t"
    "s_cmp_lt_u32 %16, 4\n\t"
    "s_cbranch_scc1 L_spp5_%=\n\t"
    "s_setprio 0\n\t"
    "L_spp5_%=:\n\t"
    "s_waitcnt vmcnt(2) lgkmcnt(10)\n\t"
    "v_smfmac_f32_16x16x64_bf16 v[20:23], v[120:123], v[92:99], v124\n\t"
    "ds_read_b64 v[92:93], v114 offset:0\n\t"
    "v_mfma_f32_16x16x32_bf16 v[116:119], v[52:55], v[160:163], v[116:119]\n\t"
    "ds_read_b64 v[94:95], v126 offset:0\n\t"
    "v_mfma_f32_16x16x32_bf16 v[116:119], v[56:59], v[140:143], v[116:119]\n\t"
    "v_add_u32_sdwa v98, s92, v115 dst_sel:DWORD dst_unused:UNUSED_PAD src0_sel:DWORD src1_sel:WORD_1\n\t"
    "v_add_u32_sdwa v115, s92, v115 dst_sel:DWORD dst_unused:UNUSED_PAD src0_sel:DWORD src1_sel:WORD_0\n\t"
    "ds_read_b64 v[96:97], v115 offset:0\n\t"
    "v_mfma_f32_16x16x32_bf16 v[116:119], v[60:63], v[144:147], v[116:119]\n\t"
    "ds_read_b64 v[98:99], v98 offset:0\n\t"
    "v_cvt_pk_bf16_f32 v80, v80, v81\n\t"
    "v_cvt_pk_bf16_f32 v81, v82, v83\n\t"
    "ds_write_b64 v125, v[80:81] offset:640\n\t"
    "v_mfma_f32_16x16x32_bf16 v[80:83], v[48:51], v[212:215], 0\n\t"
    "buffer_load_dwordx4 v[112:115], v127, %12, s91 offen offset:0 sc1\n\t"
    "s_add_u32 s91, s91, s97\n\t"
    "v_add_u32_sdwa v126, s92, v100 dst_sel:DWORD dst_unused:UNUSED_PAD src0_sel:DWORD src1_sel:WORD_1\n\t"
    "v_add_u32_sdwa v100, s92, v100 dst_sel:DWORD dst_unused:UNUSED_PAD src0_sel:DWORD src1_sel:WORD_0\n\t"
    "v_mfma_f32_16x16x32_bf16 v[80:83], v[52:55], v[216:219], v[80:83]\n\t"
    "v_cvt_pk_bf16_f32 v8, v8, v9\n\t"
    "v_cvt_pk_bf16_f32 v9, v10, v11\n\t"
    "s_waitcnt lgkmcnt(6)\n\t"
    "v_smfmac_f32_16x16x64_bf16 v[24:27], v[120:123], v[84:91], v124\n\t"
    "ds_read_b64 v[84:85], v100 offset:0\n\t"
    "v_mfma_f32_16x16x32_bf16 v[80:83], v[56:59], v[148:151], v[80:83]\n\t"
    "ds_read_b64 v[86:87], v126 offset:0\n\t"
    "v_mfma_f32_16x16x32_bf16 v[80:83], v[60:63], v[152:155], v[80:83]\n\t"
    "v_add_u32_sdwa v90, s92, v101 dst_sel:DWORD dst_unused:UNUSED_PAD src0_sel:DWORD src1_sel:WORD_1\n\t"
    "v_add_u32_sdwa v101, s92, v101 dst_sel:DWORD dst_unused:UNUSED_PAD src0_sel:DWORD src1_sel:WORD_0\n\t"
    "ds_read_b64 v[88:89], v101 offset:0\n\t"
    "v_cvt_pk_bf16_f32 v116, v116, v117\n\t"
    "v_cvt_pk_bf16_f32 v117, v118, v119\n\t"
    "ds_write_b64 v125, v[116:117] offset:768\n\t"
    "v_mfma_f32_16x16x32_bf16 v[116:119], v[64:67], v[164:167], 0\n\t"
    "ds_read_b64 v[90:91], v90 offset:0\n\t"
    "v_mfma_f32_16x16x32_bf16 v[116:119], v[68:71], v[168:171], v[116:119]\n\t"
    "v_add_u32_sdwa v126, s92, v102 dst_sel:DWORD dst_unused:UNUSED_PAD src0_sel:DWORD src1_sel:WORD_1\n\t"
    "v_add_u32_sdwa v102, s92, v102 dst_sel:DWORD dst_unused:UNUSED_PAD src0_sel:DWORD src1_sel:WORD_0\n\t"
    "v_mfma_f32_16x16x32_bf16 v[116:119], v[72:75], v[220:223], v[116:119]\n\t"
    "v_cvt_pk_bf16_f32 v10, v12, v13\n\t"
    "v_cvt_pk_bf16_f32 v11, v14, v15\n\t"
    "v_mov_b64 v[12:13], 0\n\t"
    "v_mov_b64 v[14:15], 0\n\t"
    "s_cmp_lg_u32 s47, 0\n\t"
    "s_cbranch_scc1 L_mp2_%=\n\t"
    "L_mp2b_%=:\n\t"
    "s_waitcnt vmcnt(2) lgkmcnt(6)\n\t"
    "v_smfmac_f32_16x16x64_bf16 v[28:31], v[120:123], v[92:99], v124\n\t"
    "ds_read_b64 v[92:93], v102 offset:0\n\t"
    "v_mfma_f32_16x16x32_bf16 v[116:119], v[76:79], v[224:227], v[116:119]\n\t"
    "ds_read_b64 v[94:95], v126 offset:0\n\t"
    "v_cvt_pk_bf16_f32 v80, v80, v81\n\t"
    "v_cvt_pk_bf16_f32 v81, v82, v83\n\t"
    "ds_write_b64 v125, v[80:81] offset:872\n\t"
    "v_mfma_f32_16x16x32_bf16 v[80:83], v[64:67], v[172:175], 0\n\t"
    "v_add_u32_sdwa v98, s92, v103 dst_sel:DWORD dst_unused:UNUSED_PAD src0_sel:DWORD src1_sel:WORD_1\n\t"
    "v_add_u32_sdwa v103, s92, v103 dst_sel:DWORD dst_unused:UNUSED_PAD src0_sel:DWORD src1_sel:WORD_0\n\t"
    "ds_read_b64 v[96:97], v103 offset:0\n\t"
    "v_mfma_f32_16x16x32_bf16 v[80:83], v[68:71], v[176:179], v[80:83]\n\t"
    "ds_read_b64 v[98:99], v98 offset:0\n\t"
    "v_mfma_f32_16x16x32_bf16 v[80:83], v[72:75], v[228:231], v[80:83]\n\t"
    "v_add_u32_sdwa v126, s92, v104 dst_sel:DWORD dst_unused:UNUSED_PAD src0_sel:DWORD src1_sel:WORD_1\n\t"
    "v_add_u32_sdwa v104, s92, v104 dst_sel:DWORD dst_unused:UNUSED_PAD src0_sel:DWORD src1_sel:WORD_0\n\t"
    "v_mfma_f32_16x16x32_bf16 v[80:83], v[76:79], v[232:235], v[80:83]\n\t"
    "v_cvt_pk_bf16_f32 v16, v16, v17\n\t"
    "v_cvt_pk_bf16_f32 v17, v18, v19\n\t"
    "v_cvt_pk_bf16_f32 v116, v116, v117\n\t"
    "v_cvt_pk_bf16_f32 v117, v118, v119\n\t"
    "ds_write_b64 v125, v[116:117] offset:32000\n\t"
    "s_waitcnt lgkmcnt(6)\n\t"
    "v_smfmac_f32_16x16x64_bf16 v[32:35], v[120:123], v[84:91], v124\n\t"
    "ds_read_b64 v[84:85], v104 offset:0\n\t"
    "v_mfma_f32_16x16x32_bf16 v[116:119], v[64:67], v[180:183], 0\n\t"
    "ds_read_b64 v[86:87], v126 offset:0\n\t"
    "v_mfma_f32_16x16x32_bf16 v[116:119], v[68:71], v[184:187], v[116:119]\n\t"
    "v_add_u32_sdwa v90, s92, v105 dst_sel:DWORD dst_unused:UNUSED_PAD src0_sel:DWORD src1_sel:WORD_1\n\t"
    "v_add_u32_sdwa v105, s92, v105 dst_sel:DWORD dst_unused:UNUSED_PAD src0_sel:DWORD src1_sel:WORD_0\n\t"
    "ds_read_b64 v[88:89], v105 offset:0\n\t"
    "v_mfma_f32_16x16x32_bf16 v[116:119], v[72:75], v[236:239], v[116:119]\n\t"
    "ds_read_b64 v[90:91], v90 offset:0\n\t"
    "v_mfma_f32_16x16x32_bf16 v[116:119], v[76:79], v[240:243], v[116:119]\n\t"
    "v_add_u32_sdwa v126, s92, v106 dst_sel:DWORD dst_unused:UNUSED_PAD src0_sel:DWORD src1_sel:WORD_1\n\t"
    "v_add_u32_sdwa v106, s92, v106 dst_sel:DWORD dst_unused:UNUSED_PAD src0_sel:DWORD src1_sel:WORD_0\n\t"
    "v_cvt_pk_bf16_f32 v80, v80, v81\n\t"
    "v_cvt_pk_bf16_f32 v81, v82, v83\n\t"
    "ds_write_b64 v125, v[80:81] offset:32128\n\t"
    "v_mfma_f32_16x16x32_bf16 v[80:83], v[64:67], v[188:191], 0\n\t"
    "v_cvt_pk_bf16_f32 v18, v20, v21\n\t"
    "v_cvt_pk_bf16_f32 v19, v22, v23\n\t"
    "v_mov_b64 v[20:21], 0\n\t"
    "v_mov_b64 v[22:23], 0\n\t"
    "s_waitcnt vmcnt(0)\n\t"
    "s_cmp_lg_u32 s47, 0\n\t"
    "s_cbranch_scc1 L_mp3_%=\n\t"
    "L_mp3b_%=:\n\t"
    "s_barrier\n\t"
    "s_add_u32 s84, s84, s95\n\t"
    "buffer_load_dwordx4 v[100:103], v127, %12, s84 offen offset:0 sc1\n\t"
    "s_lshl_b32 s50, %16, 10\n\t"
    "s_add_u32 s63, s63, s50\n\t"
    "s_add_u32 s50, s50, 0x2000\n\t"
    "s_xor_b32 s50, s50, 0x2000\n\t"
    "s_add_u32 m0, s50, 128000\n\t"
    "s_add_u32 s63, s63, 0x4000\n\t"
    "buffer_load_dwordx4 v127, %12, s63 offen sc1 lds\n\t"
    "s_cmp_lt_u32 %16, 4\n\t"
    "s_cbranch_scc1 L_spp9_%=\n\t"
    "s_setprio 1\n\t"
    "L_spp9_%=:\n\t"
    "s_waitcnt lgkmcnt(6)\n\t"
    "v_smfmac_f32_16x16x64_bf16 v[36:39], v[120:123], v[92:99], v124\n\t"
    "ds_read_b64 v[92:93], v106 offset:0\n\t"
    "v_mfma_f32_16x16x32_bf16 v[80:83], v[68:71], v[192:195], v[80:83]\n\t"
    "ds_read_b64 v[94:95], v126 offset:0\n\t"
    "v_mfma_f32_16x16x32_bf16 v[80:83], v[72:75], v[244:247], v[80:83]\n\t"
    "v_add_u32_sdwa v98, s92, v107 dst_sel:DWORD dst_unused:UNUSED_PAD src0_sel:DWORD src1_sel:WORD_1\n\t"
    "v_add_u32_sdwa v107, s92, v107 dst_sel:DWORD dst_unused:UNUSED_PAD src0_sel:DWORD src1_sel:WORD_0\n\t"
    "ds_read_b64 v[96:97], v107 offset:0\n\t"
    "v_mfma_f32_16x16x32_bf16 v[80:83], v[76:79], v[248:251], v[80:83]\n\t"
    "ds_read_b64 v[98:99], v98 offset:0\n\t"
    "v_cvt_pk_bf16_f32 v116, v116, v117\n\t"
    "v_cvt_pk_bf16_f32 v117, v118, v119\n\t"
    "ds_write_b64 v125, v[116:117] offset:32256\n\t"
    "v_mfma_f32_16x16x32_bf16 v[116:119], v[64:67], v[196:199], 0\n\t"
    "buffer_load_dwordx4 v[104:107], v127, %12, s84 offen offset:1024 sc1\n\t"
    "v_add_u32_sdwa v126, s92, v108 dst_sel:DWORD dst_unused:UNUSED_PAD src0_sel:DWORD src1_sel:WORD_1\n\t"
    "v_add_u32_sdwa v108, s92, v108 dst_sel:DWORD dst_unused:UNUSED_PAD src0_sel:DWORD src1_sel:WORD_0\n\t"
    "v_mfma_f32_16x16x32_bf16 v[116:119], v[68:71], v[200:203], v[116:119]\n\t"
    "v_cvt_pk_bf16_f32 v24, v24, v25\n\t"
    "v_cvt_pk_bf16_f32 v25, v26, v27\n\t"
    "v_mov_b64 v[48:49], 0\n\t"
    "v_mov_b64 v[50:51], 0\n\t"
    "v_mov_b64 v[52:53], 0\n\t"
    "v_mov_b64 v[54:55], 0\n\t"
    "s_waitcnt lgkmcnt(6)\n\t"
    "v_smfmac_f32_16x16x64_bf16 v[40:43], v[120:123], v[84:91], v124\n\t"
    "ds_read_b64 v[84:85], v108 offset:0\n\t"
    "v_mfma_f32_16x16x32_bf16 v[116:119], v[72:75], v[252:255], v[116:119]\n\t"
    "ds_read_b64 v[86:87], v126 offset:0\n\t"
    "v_mfma_f32_16x16x32_bf16 v[116:119], v[76:79], v[128:131], v[116:119]\n\t"
    "v_add_u32_sdwa v90, s92, v109 dst_sel:DWORD dst_unused:UNUSED_PAD src0_sel:DWORD src1_sel:WORD_1\n\t"
    "v_add_u32_sdwa v109, s92, v109 dst_sel:DWORD dst_unused:UNUSED_PAD src0_sel:DWORD src1_sel:WORD_0\n\t"
    "ds_read_b64 v[88:89], v109 offset:0\n\t"
    "v_cvt_pk_bf16_f32 v80, v80, v81\n\t"
    "v_cvt_pk_bf16_f32 v81, v82, v83\n\t"
    "ds_write_b64 v125, v[80:81] offset:32384\n\t"
    "v_mfma_f32_16x16x32_bf16 v[80:83], v[64:67], v[204:207], 0\n\t"
    "ds_read_b64 v[90:91], v90 offset:0\n\t"
    "v_mfma_f32_16x16x32_bf16 v[80:83], v[68:71], v[208:211], v[80:83]\n\t"
    "v_add_u32_sdwa v126, s92, v110 dst_sel:DWORD dst_unused:UNUSED_PAD src0_sel:DWORD src1_sel:WORD_1\n\t"
    "v_add_u32_sdwa v110, s92, v110 dst_sel:DWORD dst_unused:UNUSED_PAD src0_sel:DWORD src1_sel:WORD_0\n\t"
    "v_mfma_f32_16x16x32_bf16 v[80:83], v[72:75], v[132:135], v[80:83]\n\t"
    "v_cvt_pk_bf16_f32 v26, v28, v29\n\t"
    "v_cvt_pk_bf16_f32 v27, v30, v31\n\t"
    "v_mov_b64 v[28:29], 0\n\t"
    "v_mov_b64 v[30:31], 0\n\t"
    "s_waitcnt lgkmcnt(6)\n\t"
    "v_smfmac_f32_16x16x64_bf16 v[44:47], v[120:123], v[92:99], v124\n\t"
    "ds_read_b64 v[92:93], v110 offset:0\n\t"
    "v_mfma_f32_16x16x32_bf16 v[80:83], v[76:79], v[136:139], v[80:83]\n\t"
    "ds_read_b64 v[94:95], v126 offset:0\n\t"
    "v_cvt_pk_bf16_f32 v116, v116, v117\n\t"
    "v_cvt_pk_bf16_f32 v117, v118, v119\n\t"
    "ds_write_b64 v125, v[116:117] offset:32512\n\t"
    "v_mfma_f32_16x16x32_bf16 v[116:119], v[64:67], v[156:159], 0\n\t"
    "v_add_u32_sdwa v98, s92, v111 dst_sel:DWORD dst_unused:UNUSED_PAD src0_sel:DWORD src1_sel:WORD_1\n\t"
    "v_add_u32_sdwa v111, s92, v111 dst_sel:DWORD dst_unused:UNUSED_PAD src0_sel:DWORD src1_sel:WORD_0\n\t"
    "ds_read_b64 v[96:97], v111 offset:0\n\t"
    "v_mfma_f32_16x16x32_bf16 v[116:119], v[68:71], v[160:163], v[116:119]\n\t"
    "ds_read_b64 v[98:99], v98 offset:0\n\t"
    "v_mfma_f32_16x16x32_bf16 v[116:119], v[72:75], v[140:143], v[116:119]\n\t"
    "s_add_u32 s85, s85, s95\n\t"
    "buffer_load_dwordx4 v[108:111], v127, %12, s85 offen offset:0 sc1\n\t"
    "v_add_u32_sdwa v126, s92, v112 dst_sel:DWORD dst_unused:UNUSED_PAD src0_sel:DWORD src1_sel:WORD_1\n\t"
    "v_add_u32_sdwa v112, s92, v112 dst_sel:DWORD dst_unused:UNUSED_PAD src0_sel:DWORD src1_sel:WORD_0\n\t"
    "v_mfma_f32_16x16x32_bf16 v[116:119], v[76:79], v[144:147], v[116:119]\n\t"
    "v_cvt_pk_bf16_f32 v32, v32, v33\n\t"
    "v_cvt_pk_bf16_f32 v33, v34, v35\n\t"
    "v_cvt_pk_bf16_f32 v80, v80, v81\n\t"
    "v_cvt_pk_bf16_f32 v81, v82, v83\n\t"
    "ds_write_b64 v125, v[80:81] offset:32640\n\t"
    "v_mov_b64 v[56:57], 0\n\t"
    "v_mov_b64 v[58:59], 0\n\t"
    "v_mov_b64 v[60:61], 0\n\t"
    "v_mov_b64 v[62:63], 0\n\t"
    "s_cmp_lt_u32 %16, 4\n\t"
    "s_cbranch_scc1 L_spp12_%=\n\t"
    "s_setprio 0\n\t"
    "L_spp12_%=:\n\t"
    "s_waitcnt lgkmcnt(6)\n\t"
    "v_smfmac_f32_16x16x64_bf16 v[48:51], v[120:123], v[84:91], v124\n\t"
    "ds_read_b64 v[84:85], v112 offset:0\n\t"
    "v_mfma_f32_16x16x32_bf16 v[80:83], v[64:67], v[212:215], 0\n\t"
    "ds_read_b64 v[86:87], v126 offset:0\n\t"
    "v_mfma_f32_16x16x32_bf16 v[80:83], v[68:71], v[216:219], v[80:83]\n\t"
    "v_add_u32_sdwa v90, s92, v113 dst_sel:DWORD dst_unused:UNUSED_PAD src0_sel:DWORD src1_sel:WORD_1\n\t"
    "v_add_u32_sdwa v113, s92, v113 dst_sel:DWORD dst_unused:UNUSED_PAD src0_sel:DWORD src1_sel:WORD_0\n\t"
    "ds_read_b64 v[88:89], v113 offset:0\n\t"
    "v_mfma_f32_16x16x32_bf16 v[80:83], v[72:75], v[148:151], v[80:83]\n\t"
    "ds_read_b64 v[90:91], v90 offset:0\n\t"
    "v_mfma_f32_16x16x32_bf16 v[80:83], v[76:79], v[152:155], v[80:83]\n\t"
    "v_add_u32_sdwa v126, s92, v114 dst_sel:DWORD dst_unused:UNUSED_PAD src0_sel:DWORD src1_sel:WORD_1\n\t"
    "v_add_u32_sdwa v114, s92, v114 dst_sel:DWORD dst_unused:UNUSED_PAD src0_sel:DWORD src1_sel:WORD_0\n\t"
    "v_cvt_pk_bf16_f32 v116, v116, v117\n\t"
    "v_cvt_pk_bf16_f32 v117, v118, v119\n\t"
    "ds_write_b64 v125, v[116:117] offset:32768\n\t"
    "s_nop 2\n\t"
    "v_cvt_pk_bf16_f32 v80, v80, v81\n\t"
    "v_cvt_pk_bf16_f32 v81, v82, v83\n\t"
    "ds_write_b64 v125, v[80:81] offset:32872\n\t"
    "v_cvt_pk_bf16_f32 v34, v36, v37\n\t"
    "v_cvt_pk_bf16_f32 v35, v38, v39\n\t"
    "v_mov_b64 v[36:37], 0\n\t"
    "v_mov_b64 v[38:39], 0\n\t"
    "v_cvt_pk_bf16_f32 v40, v40, v41\n\t"
    "v_cvt_pk_bf16_f32 v41, v42, v43\n\t"
    "v_cvt_pk_bf16_f32 v42, v44, v45\n\t"
    "v_cvt_pk_bf16_f32 v43, v46, v47\n\t"
    "v_mov_b64 v[44:45], 0\n\t"
    "v_mov_b64 v[46:47], 0\n\t"
    "s_waitcnt vmcnt(3) lgkmcnt(7)\n\t"
    "v_smfmac_f32_16x16x64_bf16 v[52:55], v[120:123], v[92:99], v124\n\t"
    "ds_read_b64 v[92:93], v114 offset:0\n\t"
    "ds_read_b64 v[94:95], v126 offset:0\n\t"
    "v_add_u32_sdwa v98, s92, v115 dst_sel:DWORD dst_unused:UNUSED_PAD src0_sel:DWORD src1_sel:WORD_1\n\t"
    "v_add_u32_sdwa v115, s92, v115 dst_sel:DWORD dst_unused:UNUSED_PAD src0_sel:DWORD src1_sel:WORD_0\n\t"
    "ds_read_b64 v[96:97], v115 offset:0\n\t"
    "ds_read_b64 v[98:99], v98 offset:0\n\t"
    "buffer_load_dwordx4 v[112:115], v127, %12, s85 offen offset:1024 sc1\n\t"
    "s_sub_u32 s92, 64000, s92\n\t"
    "v_add_u32_sdwa v126, s92, v100 dst_sel:DWORD dst_unused:UNUSED_PAD src0_sel:DWORD src1_sel:WORD_1\n\t"
    "v_add_u32_sdwa v100, s92, v100 dst_sel:DWORD dst_unused:UNUSED_PAD src0_sel:DWORD src1_sel:WORD_0\n\t"
    "v_cvt_pk_bf16_f32 v48, v48, v49\n\t"
    "v_cvt_pk_bf16_f32 v49, v50, v51\n\t"
    "s_waitcnt lgkmcnt(4)\n\t"
    "s_waitcnt lgkmcnt(0)\n\t"
    "s_waitcnt vmcnt(3)\n\t"
    "s_barrier\n\t"
    "v_add_u32 v125, s93, v125\n\t"
    "s_sub_u32 s93, 0, s93\n\t"
    "s_cmp_lt_u32 %16, 4\n\t"
    "s_cbranch_scc1 L_spp14_%=\n\t"
    "s_setprio 1\n\t"
    "L_spp14_%=:\n\t"
    "v_smfmac_f32_16x16x64_bf16 v[56:59], v[120:123], v[84:91], v124\n\t"
    "ds_read_b64 v[84:85], v100 offset:0\n\t"
    "ds_read_b64 v[86:87], v126 offset:0\n\t"
    "v_add_u32_sdwa v90, s92, v101 dst_sel:DWORD dst_unused:UNUSED_PAD src0_sel:DWORD src1_sel:WORD_1\n\t"
    "v_add_u32_sdwa v101, s92, v101 dst_sel:DWORD dst_unused:UNUSED_PAD src0_sel:DWORD src1_sel:WORD_0\n\t"
    "ds_read_b64 v[88:89], v101 offset:0\n\t"
    "ds_read_b64 v[90:91], v90 offset:0\n\t"
    "v_add_u32_sdwa v126, s92, v102 dst_sel:DWORD dst_unused:UNUSED_PAD src0_sel:DWORD src1_sel:WORD_1\n\t"
    "v_add_u32_sdwa v102, s92, v102 dst_sel:DWORD dst_unused:UNUSED_PAD src0_sel:DWORD src1_sel:WORD_0\n\t"
    "v_cvt_pk_bf16_f32 v50, v52, v53\n\t"
    "v_cvt_pk_bf16_f32 v51, v54, v55\n\t"
    "v_mov_b64 v[52:53], 0\n\t"
    "v_mov_b64 v[54:55], 0\n\t"
    "s_waitcnt vmcnt(2)\n\t"
    "v_smfmac_f32_16x16x64_bf16 v[60:63], v[120:123], v[92:99], v124\n\t"
    "ds_read_b64 v[92:93], v102 offset:0\n\t"
    "ds_read_b64 v[94:95], v126 offset:0\n\t"
    "v_add_u32_sdwa v98, s92, v103 dst_sel:DWORD dst_unused:UNUSED_PAD src0_sel:DWORD src1_sel:WORD_1\n\t"
    "v_add_u32_sdwa v103, s92, v103 dst_sel:DWORD dst_unused:UNUSED_PAD src0_sel:DWORD src1_sel:WORD_0\n\t"
    "ds_read_b64 v[96:97], v103 offset:0\n\t"
    "ds_read_b64 v[98:99], v98 offset:0\n\t"
    "s_add_u32 s86, s86, s95\n\t"
    "buffer_load_dwordx4 v[100:103], v127, %12, s86 offen offset:0 sc1\n\t"
    "v_add_u32_sdwa v126, s92, v104 dst_sel:DWORD dst_unused:UNUSED_PAD src0_sel:DWORD src1_sel:WORD_1\n\t"
    "v_add_u32_sdwa v104, s92, v104 dst_sel:DWORD dst_unused:UNUSED_PAD src0_sel:DWORD src1_sel:WORD_0\n\t"
    "v_cvt_pk_bf16_f32 v56, v56, v57\n\t"
    "v_cvt_pk_bf16_f32 v57, v58, v59\n\t"
    "v_add_u32 v79, s96, v127\n\t"
    "s_add_u32 s96, s96, 0x1000\n\t"
    "s_and_b32 s96, s96, 0x3000\n\t"
    "v_add_u32 v79, 128000, v79\n\t"
    "ds_read_b128 v[64:67], v79 offset:0\n\t"
    "ds_read_b128 v[68:71], v79 offset:1024\n\t"
    "ds_read_b128 v[72:75], v79 offset:2048\n\t"
    "ds_read_b128 v[76:79], v79 offset:3072\n\t"
    "v_mov_b64 v[116:117], 0\n\t"
    "v_mov_b64 v[118:119], 0\n\t"
    "v_cvt_pk_bf16_f32 v58, v60, v61\n\t"
    "v_cvt_pk_bf16_f32 v59, v62, v63\n\t"
    "v_mov_b64 v[60:61], 0\n\t"
    "v_mov_b64 v[62:63], 0\n\t"
    "s_cmp_eq_u32 %16, 0\n\t"
    "s_cselect_b32 s48, 2, -1\n\t"
    "s_mov_b32 s66, 4\n\t"
    "s_branch L_loop_%=\n\t"
    ".p2align 9\n\t"
    "s_nop 0\n\t"
    "s_nop 0\n\t"
    "s_nop 0\n\t"
    "s_nop 0\n\t"
    "s_nop 0\n\t"
    "s_nop 0\n\t"
    "s_nop 0\n\t"
    "s_nop 0\n\t"
    "s_nop 0\n\t"
    "s_nop 0\n\t"
    "s_nop 0\n\t"
    "s_nop 0\n\t"
    "s_nop 0\n\t"
    "s_nop 0\n\t"
    "s_nop 0\n\t"
    "s_nop 0\n\t"
    "s_nop 0\n\t"
    "s_nop 0\n\t"
    "s_nop 0\n\t"
    "s_nop 0\n\t"
    "s_nop 0\n\t"
    "s_nop 0\n\t"
    "s_nop 0\n\t"
    "s_nop 0\n\t"
    "s_nop 0\n\t"
    "s_nop 0\n\t"
    "s_nop 0\n\t"
    "s_nop 0\n\t"
    "s_nop 0\n\t"
    "s_nop 0\n\t"
    "s_nop 0\n\t"
    "s_nop 0\n\t"
    "s_nop 0\n\t"
    "L_loop_%=:\n\t"
    "s_waitcnt lgkmcnt(8)\n\t"
    "v_smfmac_f32_16x16x64_bf16 v[116:119], v[120:123], v[84:91], v124\n\t"
    "ds_read_b64 v[84:85], v104 offset:0\n\t"
    "ds_read_b64 v[86:87], v126 offset:0\n\t"
    "v_add_u32_sdwa v90, s92, v105 dst_sel:DWORD dst_unused:UNUSED_PAD src0_sel:DWORD src1_sel:WORD_1\n\t"
    "v_add_u32_sdwa v105, s92, v105 dst_sel:DWORD dst_unused:UNUSED_PAD src0_sel:DWORD src1_sel:WORD_0\n\t"
    "ds_read_b64 v[88:89], v105 offset:0\n\t"
    "ds_read_b64 v[90:91], v90 offset:0\n\t"
    "v_add_u32_sdwa v126, s92, v106 dst_sel:DWORD dst_unused:UNUSED_PAD src0_sel:DWORD src1_sel:WORD_1\n\t"
    "v_add_u32_sdwa v106, s92, v106 dst_sel:DWORD dst_unused:UNUSED_PAD src0_sel:DWORD src1_sel:WORD_0\n\t"
    "s_waitcnt vmcnt(2) lgkmcnt(8)\n\t"
    "v_smfmac_f32_16x16x64_bf16 v[116:119], v[120:123], v[92:99], v124\n\t"
    "ds_read_b64 v[92:93], v106 offset:0\n\t"
    "ds_read_b64 v[94:95], v126 offset:0\n\t"
    "v_add_u32_sdwa v98, s92, v107 dst_sel:DWORD dst_unused:UNUSED_PAD src0_sel:DWORD src1_sel:WORD_1\n\t"
    "v_add_u32_sdwa v107, s92, v107 dst_sel:DWORD dst_unused:UNUSED_PAD src0_sel:DWORD src1_sel:WORD_0\n\t"
    "ds_read_b64 v[96:97], v107 offset:0\n\t"
    "ds_read_b64 v[98:99], v98 offset:0\n\t"
    "buffer_load_dwordx4 v[104:107], v127, %12, s86 offen offset:1024 sc1\n\t"
    "v_add_u32_sdwa v126, s92, v108 dst_sel:DWORD dst_unused:UNUSED_PAD src0_sel:DWORD src1_sel:WORD_1\n\t"
    "v_cvt_pk_bf16_f32 v4, v116, v117\n\t"
    "v_cvt_pk_bf16_f32 v5, v118, v119\n\t"
    "v_and_b32 v119, s67, v7\n\t"
    "v_lshlrev_b32 v118, 16, v7\n\t"
    "v_and_b32 v117, s67, v6\n\t"
    "v_lshlrev_b32 v116, 16, v6\n\t"
    "v_add_u32_sdwa v108, s92, v108 dst_sel:DWORD dst_unused:UNUSED_PAD src0_sel:DWORD src1_sel:WORD_0\n\t"
    "s_waitcnt lgkmcnt(4)\n\t"
    "v_smfmac_f32_16x16x64_bf16 v[116:119], v[120:123], v[84:91], v124\n\t"
    "ds_read_b64 v[84:85], v108 offset:0\n\t"
    "v_mfma_f32_16x16x32_bf16 v[80:83], v[64:67], v[164:167], 0\n\t"
    "ds_read_b64 v[86:87], v126 offset:0\n\t"
    "v_mfma_f32_16x16x32_bf16 v[80:83], v[68:71], v[168:171], v[80:83]\n\t"
    "v_add_u32_sdwa v90, s92, v109 dst_sel:DWORD dst_unused:UNUSED_PAD src0_sel:DWORD src1_sel:WORD_1\n\t"
    "v_add_u32_sdwa v109, s92, v109 dst_sel:DWORD dst_unused:UNUSED_PAD src0_sel:DWORD src1_sel:WORD_0\n\t"
    "ds_read_b64 v[88:89], v109 offset:0\n\t"
    "v_mfma_f32_16x16x32_bf16 v[80:83], v[72:75], v[220:223], v[80:83]\n\t"
    "ds_read_b64 v[90:91], v90 offset:0\n\t"
    "v_mfma_f32_16x16x32_bf16 v[80:83], v[76:79], v[224:227], v[80:83]\n\t"
    "v_add_u32_sdwa v126, s92, v110 dst_sel:DWORD dst_unused:UNUSED_PAD src0_sel:DWORD src1_sel:WORD_1\n\t"
    "v_add_u32_sdwa v110, s92, v110 dst_sel:DWORD dst_unused:UNUSED_PAD src0_sel:DWORD src1_sel:WORD_0\n\t"
    "s_waitcnt vmcnt(2) lgkmcnt(4)\n\t"
    "v_smfmac_f32_16x16x64_bf16 v[116:119], v[120:123], v[92:99], v124\n\t"
    "ds_read_b64 v[92:93], v110 offset:0\n\t"
    "ds_read_b64 v[94:95], v126 offset:0\n\t"
    "v_add_u32_sdwa v98, s92, v111 dst_sel:DWORD dst_unused:UNUSED_PAD src0_sel:DWORD src1_sel:WORD_1\n\t"
    "v_add_u32_sdwa v111, s92, v111 dst_sel:DWORD dst_unused:UNUSED_PAD src0_sel:DWORD src1_sel:WORD_0\n\t"
    "ds_read_b64 v[96:97], v111 offset:0\n\t"
    "ds_read_b64 v[98:99], v98 offset:0\n\t"
    "s_add_u32 s87, s87, s95\n\t"
    "buffer_load_dwordx4 v[108:111], v127, %12, s87 offen offset:0 sc1\n\t"
    "v_add_u32_sdwa v126, s92, v112 dst_sel:DWORD dst_unused:UNUSED_PAD src0_sel:DWORD src1_sel:WORD_1\n\t"
    "v_cvt_pk_bf16_f32 v6, v116, v117\n\t"
    "v_cvt_pk_bf16_f32 v7, v118, v119\n\t"
    "v_and_b32 v119, s67, v13\n\t"
    "v_lshlrev_b32 v118, 16, v13\n\t"
    "v_and_b32 v117, s67, v12\n\t"
    "v_lshlrev_b32 v116, 16, v12\n\t"
    "v_add_u32_sdwa v112, s92, v112 dst_sel:DWORD dst_unused:UNUSED_PAD src0_sel:DWORD src1_sel:WORD_0\n\t"
    "s_waitcnt lgkmcnt(4)\n\t"
    "v_smfmac_f32_16x16x64_bf16 v[116:119], v[120:123], v[84:91], v124\n\t"
    "ds_read_b64 v[84:85], v112 offset:0\n\t"
    "ds_read_b64 v[86:87], v126 offset:0\n\t"
    "v_cvt_pk_bf16_f32 v80, v80, v81\n\t"
    "v_cvt_pk_bf16_f32 v81, v82, v83\n\t"
    "ds_write_b64 v125, v[80:81] offset:0\n\t"
    "v_mfma_f32_16x16x32_bf16 v[80:83], v[64:67], v[172:175], 0\n\t"
    "v_add_u32_sdwa v90, s92, v113 dst_sel:DWORD dst_unused:UNUSED_PAD src0_sel:DWORD src1_sel:WORD_1\n\t"
    "v_add_u32_sdwa v113, s92, v113 dst_sel:DWORD dst_unused:UNUSED_PAD src0_sel:DWORD src1_sel:WORD_0\n\t"
    "ds_read_b64 v[88:89], v113 offset:0\n\t"
    "v_mfma_f32_16x16x32_bf16 v[80:83], v[68:71], v[176:179], v[80:83]\n\t"
    "ds_read_b64 v[90:91], v90 offset:0\n\t"
    "v_mfma_f32_16x16x32_bf16 v[80:83], v[72:75], v[228:231], v[80:83]\n\t"
    "v_mfma_f32_16x16x32_bf16 v[80:83], v[76:79], v[232:235], v[80:83]\n\t"
    "v_add_u32_sdwa v126, s92, v114 dst_sel:DWORD dst_unused:UNUSED_PAD src0_sel:DWORD src1_sel:WORD_1\n\t"
    "v_add_u32_sdwa v114, s92, v114 dst_sel:DWORD dst_unused:UNUSED_PAD src0_sel:DWORD src1_sel:WORD_0\n\t"
    "s_waitcnt vmcnt(2) lgkmcnt(5)\n\t"
    "v_smfmac_f32_16x16x64_bf16 v[116:119], v[120:123], v[92:99], v124\n\t"
    "ds_read_b64 v[92:93], v114 offset:0\n\t"
    "ds_read_b64 v[94:95], v126 offset:0\n\t"
    "v_add_u32_sdwa v98, s92, v115 dst_sel:DWORD dst_unused:UNUSED_PAD src0_sel:DWORD src1_sel:WORD_1\n\t"
    "v_add_u32_sdwa v115, s92, v115 dst_sel:DWORD dst_unused:UNUSED_PAD src0_sel:DWORD src1_sel:WORD_0\n\t"
    "ds_read_b64 v[96:97], v115 offset:0\n\t"
    "ds_read_b64 v[98:99], v98 offset:0\n\t"
    "buffer_load_dwordx4 v[112:115], v127, %12, s87 offen offset:1024 sc1\n\t"
    "v_add_u32_sdwa v126, s92, v100 dst_sel:DWORD dst_unused:UNUSED_PAD src0_sel:DWORD src1_sel:WORD_1\n\t"
    "v_cvt_pk_bf16_f32 v12, v116, v117\n\t"
    "v_cvt_pk_bf16_f32 v13, v118, v119\n\t"
    "v_and_b32 v119, s67, v15\n\t"
    "v_lshlrev_b32 v118, 16, v15\n\t"
    "v_and_b32 v117, s67, v14\n\t"
    "v_lshlrev_b32 v116, 16, v14\n\t"
    "v_add_u32_sdwa v100, s92, v100 dst_sel:DWORD dst_unused:UNUSED_PAD src0_sel:DWORD src1_sel:WORD_0\n\t"
    "s_waitcnt lgkmcnt(4)\n\t"
    "v_smfmac_f32_16x16x64_bf16 v[116:119], v[120:123], v[84:91], v124\n\t"
    "ds_read_b64 v[84:85], v100 offset:0\n\t"
    "ds_read_b64 v[86:87], v126 offset:0\n\t"
    "v_cvt_pk_bf16_f32 v80, v80, v81\n\t"
    "v_cvt_pk_bf16_f32 v81, v82, v83\n\t"
    "ds_write_b64 v125, v[80:81] offset:128\n\t"
    "v_mfma_f32_16x16x32_bf16 v[80:83], v[64:67], v[180:183], 0\n\t"
    "v_add_u32_sdwa v90, s92, v101 dst_sel:DWORD dst_unused:UNUSED_PAD src0_sel:DWORD src1_sel:WORD_1\n\t"
    "v_add_u32_sdwa v101, s92, v101 dst_sel:DWORD dst_unused:UNUSED_PAD src0_sel:DWORD src1_sel:WORD_0\n\t"
    "ds_read_b64 v[88:89], v101 offset:0\n\t"
    "v_mfma_f32_16x16x32_bf16 v[80:83], v[68:71], v[184:187], v[80:83]\n\t"
    "ds_read_b64 v[90:91], v90 offset:0\n\t"
    "v_mfma_f32_16x16x32_bf16 v[80:83], v[72:75], v[236:239], v[80:83]\n\t"
    "v_mfma_f32_16x16x32_bf16 v[80:83], v[76:79], v[240:243], v[80:83]\n\t"
    "v_add_u32_sdwa v126, s92, v102 dst_sel:DWORD dst_unused:UNUSED_PAD src0_sel:DWORD src1_sel:WORD_1\n\t"
    "v_add_u32_sdwa v102, s92, v102 dst_sel:DWORD dst_unused:UNUSED_PAD src0_sel:DWORD src1_sel:WORD_0\n\t"
    "s_waitcnt vmcnt(2) lgkmcnt(5)\n\t"
    "v_smfmac_f32_16x16x64_bf16 v[116:119], v[120:123], v[92:99], v124\n\t"
    "ds_read_b64 v[92:93], v102 offset:0\n\t"
    "ds_read_b64 v[94:95], v126 offset:0\n\t"
    "v_add_u32_sdwa v98, s92, v103 dst_sel:DWORD dst_unused:UNUSED_PAD src0_sel:DWORD src1_sel:WORD_1\n\t"
    "v_add_u32_sdwa v103, s92, v103 dst_sel:DWORD dst_unused:UNUSED_PAD src0_sel:DWORD src1_sel:WORD_0\n\t"
    "ds_read_b64 v[96:97], v103 offset:0\n\t"
    "ds_read_b64 v[98:99], v98 offset:0\n\t"
    "s_add_u32 s88, s88, s95\n\t"
    "buffer_load_dwordx4 v[100:103], v127, %12, s88 offen offset:0 sc1\n\t"
    "v_add_u32_sdwa v126, s92, v104 dst_sel:DWORD dst_unused:UNUSED_PAD src0_sel:DWORD src1_sel:WORD_1\n\t"
    "v_cvt_pk_bf16_f32 v14, v116, v117\n\t"
    "v_cvt_pk_bf16_f32 v15, v118, v119\n\t"
    "v_and_b32 v119, s67, v21\n\t"
    "v_lshlrev_b32 v118, 16, v21\n\t"
    "v_and_b32 v117, s67, v20\n\t"
    "v_lshlrev_b32 v116, 16, v20\n\t"
    "v_add_u32_sdwa v104, s92, v104 dst_sel:DWORD dst_unused:UNUSED_PAD src0_sel:DWORD src1_sel:WORD_0\n\t"
    "s_waitcnt lgkmcnt(4)\n\t"
    "v_smfmac_f32_16x16x64_bf16 v[116:119], v[120:123], v[84:91], v124\n\t"
    "ds_read_b64 v[84:85], v104 offset:0\n\t"
    "ds_read_b64 v[86:87], v126 offset:0\n\t"
    "v_cvt_pk_bf16_f32 v80, v80, v81\n\t"
    "v_cvt_pk_bf16_f32 v81, v82, v83\n\t"
    "ds_write_b64 v125, v[80:81] offset:256\n\t"
    "v_add_u32_sdwa v90, s92, v105 dst_sel:DWORD dst_unused:UNUSED_PAD src0_sel:DWORD src1_sel:WORD_1\n\t"
    "v_add_u32_sdwa v105, s92, v105 dst_sel:DWORD dst_unused:UNUSED_PAD src0_sel:DWORD src1_sel:WORD_0\n\t"
    "ds_read_b64 v[88:89], v105 offset:0\n\t"
    "ds_read_b64 v[90:91], v90 offset:0\n\t"
    "v_add_u32_sdwa v126, s92, v106 dst_sel:DWORD dst_unused:UNUSED_PAD src0_sel:DWORD src1_sel:WORD_1\n\t"
    "v_add_u32_sdwa v106, s92, v106 dst_sel:DWORD dst_unused:UNUSED_PAD src0_sel:DWORD src1_sel:WORD_0\n\t"
    "s_waitcnt vmcnt(2) lgkmcnt(5)\n\t"
    "v_smfmac_f32_16x16x64_bf16 v[116:119], v[120:123], v[92:99], v124\n\t"
    "ds_read_b64 v[92:93], v106 offset:0\n\t"
    "ds_read_b64 v[94:95], v126 offset:0\n\t"
    "v_add_u32_sdwa v98, s92, v107 dst_sel:DWORD dst_unused:UNUSED_PAD src0_sel:DWORD src1_sel:WORD_1\n\t"
    "v_add_u32_sdwa v107, s92, v107 dst_sel:DWORD dst_unused:UNUSED_PAD src0_sel:DWORD src1_sel:WORD_0\n\t"
    "ds_read_b64 v[96:97], v107 offset:0\n\t"
    "ds_read_b64 v[98:99], v98 offset:0\n\t"
    "buffer_load_dwordx4 v[104:107], v127, %12, s88 offen offset:1024 sc1\n\t"
    "v_add_u32_sdwa v126, s92, v108 dst_sel:DWORD dst_unused:UNUSED_PAD src0_sel:DWORD src1_sel:WORD_1\n\t"
    "v_cvt_pk_bf16_f32 v20, v116, v117\n\t"
    "v_cvt_pk_bf16_f32 v21, v118, v119\n\t"
    "v_and_b32 v119, s67, v23\n\t"
    "v_lshlrev_b32 v118, 16, v23\n\t"
    "v_and_b32 v117, s67, v22\n\t"
    "v_lshlrev_b32 v116, 16, v22\n\t"
    "v_add_u32_sdwa v108, s92, v108 dst_sel:DWORD dst_unused:UNUSED_PAD src0_sel:DWORD src1_sel:WORD_0\n\t"
    "s_waitcnt lgkmcnt(4)\n\t"
    "v_smfmac_f32_16x16x64_bf16 v[116:119], v[120:123], v[84:91], v124\n\t"
    "ds_read_b64 v[84:85], v108 offset:0\n\t"
    "v_mfma_f32_16x16x32_bf16 v[80:83], v[64:67], v[188:191], 0\n\t"
    "ds_read_b64 v[86:87], v126 offset:0\n\t"
    "v_mfma_f32_16x16x32_bf16 v[80:83], v[68:71], v[192:195], v[80:83]\n\t"
    "v_add_u32_sdwa v90, s92, v109 dst_sel:DWORD dst_unused:UNUSED_PAD src0_sel:DWORD src1_sel:WORD_1\n\t"
    "v_add_u32_sdwa v109, s92, v109 dst_sel:DWORD dst_unused:UNUSED_PAD src0_sel:DWORD src1_sel:WORD_0\n\t"
    "ds_read_b64 v[88:89], v109 offset:0\n\t"
    "v_mfma_f32_16x16x32_bf16 v[80:83], v[72:75], v[244:247], v[80:83]\n\t"
    "ds_read_b64 v[90:91], v90 offset:0\n\t"
    "v_mfma_f32_16x16x32_bf16 v[80:83], v[76:79], v[248:251], v[80:83]\n\t"
    "v_add_u32_sdwa v126, s92, v110 dst_sel:DWORD dst_unused:UNUSED_PAD src0_sel:DWORD src1_sel:WORD_1\n\t"
    "v_add_u32_sdwa v110, s92, v110 dst_sel:DWORD dst_unused:UNUSED_PAD src0_sel:DWORD src1_sel:WORD_0\n\t"
    "s_waitcnt vmcnt(2) lgkmcnt(4)\n\t"
    "v_smfmac_f32_16x16x64_bf16 v[116:119], v[120:123], v[92:99], v124\n\t"
    "ds_read_b64 v[92:93], v110 offset:0\n\t"
    "ds_read_b64 v[94:95], v126 offset:0\n\t"
    "v_add_u32_sdwa v98, s92, v111 dst_sel:DWORD dst_unused:UNUSED_PAD src0_sel:DWORD src1_sel:WORD_1\n\t"
    "v_add_u32_sdwa v111, s92, v111 dst_sel:DWORD dst_unused:UNUSED_PAD src0_sel:DWORD src1_sel:WORD_0\n\t"
    "ds_read_b64 v[96:97], v111 offset:0\n\t"
    "ds_read_b64 v[98:99], v98 offset:0\n\t"
    "s_add_u32 s89, s89, s95\n\t"
    "buffer_load_dwordx4 v[108:111], v127, %12, s89 offen offset:0 sc1\n\t"
    "v_add_u32_sdwa v126, s92, v112 dst_sel:DWORD dst_unused:UNUSED_PAD src0_sel:DWORD src1_sel:WORD_1\n\t"
    "v_cvt_pk_bf16_f32 v22, v116, v117\n\t"
    "v_cvt_pk_bf16_f32 v23, v118, v119\n\t"
    "v_and_b32 v119, s67, v29\n\t"
    "v_lshlrev_b32 v118, 16, v29\n\t"
    "v_and_b32 v117, s67, v28\n\t"
    "v_lshlrev_b32 v116, 16, v28\n\t"
    "v_add_u32_sdwa v112, s92, v112 dst_sel:DWORD dst_unused:UNUSED_PAD src0_sel:DWORD src1_sel:WORD_0\n\t"
    "s_waitcnt lgkmcnt(4)\n\t"
    "v_smfmac_f32_16x16x64_bf16 v[116:119], v[120:123], v[84:91], v124\n\t"
    "ds_read_b64 v[84:85], v112 offset:0\n\t"
    "ds_read_b64 v[86:87], v126 offset:0\n\t"
    "v_cvt_pk_bf16_f32 v80, v80, v81\n\t"
    "v_cvt_pk_bf16_f32 v81, v82, v83\n\t"
    "ds_write_b64 v125, v[80:81] offset:384\n\t"
    "v_add_u32_sdwa v90, s92, v113 dst_sel:DWORD dst_unused:UNUSED_PAD src0_sel:DWORD src1_sel:WORD_1\n\t"
    "v_add_u32_sdwa v113, s92, v113 dst_sel:DWORD dst_unused:UNUSED_PAD src0_sel:DWORD src1_sel:WORD_0\n\t"
    "ds_read_b64 v[88:89], v113 offset:0\n\t"
    "ds_read_b64 v[90:91], v90 offset:0\n\t"
    "v_add_u32_sdwa v126, s92, v114 dst_sel:DWORD dst_unused:UNUSED_PAD src0_sel:DWORD src1_sel:WORD_1\n\t"
    "v_add_u32_sdwa v114, s92, v114 dst_sel:DWORD dst_unused:UNUSED_PAD src0_sel:DWORD src1_sel:WORD_0\n\t"
    "s_waitcnt vmcnt(2) lgkmcnt(5)\n\t"
    "v_smfmac_f32_16x16x64_bf16 v[116:119], v[120:123], v[92:99], v124\n\t"
    "ds_read_b64 v[92:93], v114 offset:0\n\t"
    "ds_read_b64 v[94:95], v126 offset:0\n\t"
    "v_add_u32_sdwa v98, s92, v115 dst_sel:DWORD dst_unused:UNUSED_PAD src0_sel:DWORD src1_sel:WORD_1\n\t"
    "v_add_u32_sdwa v115, s92, v115 dst_sel:DWORD dst_unused:UNUSED_PAD src0_sel:DWORD src1_sel:WORD_0\n\t"
    "ds_read_b64 v[96:97], v115 offset:0\n\t"
    "ds_read_b64 v[98:99], v98 offset:0\n\t"
    "buffer_load_dwordx4 v[112:115], v127, %12, s89 offen offset:1024 sc1\n\t"
    "v_add_u32_sdwa v126, s92, v100 dst_sel:DWORD dst_unused:UNUSED_PAD src0_sel:DWORD src1_sel:WORD_1\n\t"
    "v_cvt_pk_bf16_f32 v28, v116, v117\n\t"
    "v_cvt_pk_bf16_f32 v29, v118, v119\n\t"
    "v_and_b32 v119, s67, v31\n\t"
    "v_lshlrev_b32 v118, 16, v31\n\t"
    "v_and_b32 v117, s67, v30\n\t"
    "v_lshlrev_b32 v116, 16, v30\n\t"
    "v_add_u32_sdwa v100, s92, v100 dst_sel:DWORD dst_unused:UNUSED_PAD src0_sel:DWORD src1_sel:WORD_0\n\t"
    "s_waitcnt lgkmcnt(4)\n\t"
    "v_smfmac_f32_16x16x64_bf16 v[116:119], v[120:123], v[84:91], v124\n\t"
    "ds_read_b64 v[84:85], v100 offset:0\n\t"
    "v_mfma_f32_16x16x32_bf16 v[80:83], v[64:67], v[196:199], 0\n\t"
    "ds_read_b64 v[86:87], v126 offset:0\n\t"
    "v_mfma_f32_16x16x32_bf16 v[80:83], v[68:71], v[200:203], v[80:83]\n\t"
    "v_add_u32_sdwa v90, s92, v101 dst_sel:DWORD dst_unused:UNUSED_PAD src0_sel:DWORD src1_sel:WORD_1\n\t"
    "v_add_u32_sdwa v101, s92, v101 dst_sel:DWORD dst_unused:UNUSED_PAD src0_sel:DWORD src1_sel:WORD_0\n\t"
    "ds_read_b64 v[88:89], v101 offset:0\n\t"
    "v_mfma_f32_16x16x32_bf16 v[80:83], v[72:75], v[252:255], v[80:83]\n\t"
    "ds_read_b64 v[90:91], v90 offset:0\n\t"
    "v_mfma_f32_16x16x32_bf16 v[80:83], v[76:79], v[128:131], v[80:83]\n\t"
    "v_add_u32_sdwa v126, s92, v102 dst_sel:DWORD dst_unused:UNUSED_PAD src0_sel:DWORD src1_sel:WORD_1\n\t"
    "v_add_u32_sdwa v102, s92, v102 dst_sel:DWORD dst_unused:UNUSED_PAD src0_sel:DWORD src1_sel:WORD_0\n\t"
    "s_xor_b32 s50, s50, 0x2000\n\t"
    "s_add_u32 m0, s50, 128000\n\t"
    "s_add_u32 s63, s63, 0x4000\n\t"
    "buffer_load_dwordx4 v127, %12, s63 offen sc1 lds\n\t"
    "s_waitcnt vmcnt(3) lgkmcnt(4)\n\t"
    "v_smfmac_f32_16x16x64_bf16 v[116:119], v[120:123], v[92:99], v124\n\t"
    "ds_read_b64 v[92:93], v102 offset:0\n\t"
    "ds_read_b64 v[94:95], v126 offset:0\n\t"
    "v_add_u32_sdwa v98, s92, v103 dst_sel:DWORD dst_unused:UNUSED_PAD src0_sel:DWORD src1_sel:WORD_1\n\t"
    "v_add_u32_sdwa v103, s92, v103 dst_sel:DWORD dst_unused:UNUSED_PAD src0_sel:DWORD src1_sel:WORD_0\n\t"
    "ds_read_b64 v[96:97], v103 offset:0\n\t"
    "ds_read_b64 v[98:99], v98 offset:0\n\t"
    "s_add_u32 s90, s90, s95\n\t"
    "buffer_load_dwordx4 v[100:103], v127, %12, s90 offen offset:0 sc1\n\t"
    "v_add_u32_sdwa v126, s92, v104 dst_sel:DWORD dst_unused:UNUSED_PAD src0_sel:DWORD src1_sel:WORD_1\n\t"
    "v_cvt_pk_bf16_f32 v30, v116, v117\n\t"
    "v_cvt_pk_bf16_f32 v31, v118, v119\n\t"
    "v_and_b32 v119, s67, v37\n\t"
    "v_lshlrev_b32 v118, 16, v37\n\t"
    "v_and_b32 v117, s67, v36\n\t"
    "v_lshlrev_b32 v116, 16, v36\n\t"
    "v_add_u32_sdwa v104, s92, v104 dst_sel:DWORD dst_unused:UNUSED_PAD src0_sel:DWORD src1_sel:WORD_0\n\t"
    "s_waitcnt lgkmcnt(4)\n\t"
    "v_smfmac_f32_16x16x64_bf16 v[116:119], v[120:123], v[84:91], v124\n\t"
    "ds_read_b64 v[84:85], v104 offset:0\n\t"
    "ds_read_b64 v[86:87], v126 offset:0\n\t"
    "v_cvt_pk_bf16_f32 v80, v80, v81\n\t"
    "v_cvt_pk_bf16_f32 v81, v82, v83\n\t"
    "ds_write_b64 v125, v[80:81] offset:512\n\t"
    "v_add_u32_sdwa v90, s92, v105 dst_sel:DWORD dst_unused:UNUSED_PAD src0_sel:DWORD src1_sel:WORD_1\n\t"
    "v_add_u32_sdwa v105, s92, v105 dst_sel:DWORD dst_unused:UNUSED_PAD src0_sel:DWORD src1_sel:WORD_0\n\t"
    "ds_read_b64 v[88:89], v105 offset:0\n\t"
    "ds_read_b64 v[90:91], v90 offset:0\n\t"
    "v_add_u32_sdwa v126, s92, v106 dst_sel:DWORD dst_unused:UNUSED_PAD src0_sel:DWORD src1_sel:WORD_1\n\t"
    "v_add_u32_sdwa v106, s92, v106 dst_sel:DWORD dst_unused:UNUSED_PAD src0_sel:DWORD src1_sel:WORD_0\n\t"
    "s_waitcnt vmcnt(3) lgkmcnt(5)\n\t"
    "v_smfmac_f32_16x16x64_bf16 v[116:119], v[120:123], v[92:99], v124\n\t"
    "ds_read_b64 v[92:93], v106 offset:0\n\t"
    "ds_read_b64 v[94:95], v126 offset:0\n\t"
    "v_add_u32_sdwa v98, s92, v107 dst_sel:DWORD dst_unused:UNUSED_PAD src0_sel:DWORD src1_sel:WORD_1\n\t"
    "v_add_u32_sdwa v107, s92, v107 dst_sel:DWORD dst_unused:UNUSED_PAD src0_sel:DWORD src1_sel:WORD_0\n\t"
    "ds_read_b64 v[96:97], v107 offset:0\n\t"
    "ds_read_b64 v[98:99], v98 offset:0\n\t"
    "buffer_load_dwordx4 v[104:107], v127, %12, s90 offen offset:1024 sc1\n\t"
    "v_add_u32_sdwa v126, s92, v108 dst_sel:DWORD dst_unused:UNUSED_PAD src0_sel:DWORD src1_sel:WORD_1\n\t"
    "v_cvt_pk_bf16_f32 v36, v116, v117\n\t"
    "v_cvt_pk_bf16_f32 v37, v118, v119\n\t"
    "v_and_b32 v119, s67, v39\n\t"
    "v_lshlrev_b32 v118, 16, v39\n\t"
    "v_and_b32 v117, s67, v38\n\t"
    "v_lshlrev_b32 v116, 16, v38\n\t"
    "v_add_u32_sdwa v108, s92, v108 dst_sel:DWORD dst_unused:UNUSED_PAD src0_sel:DWORD src1_sel:WORD_0\n\t"
    "s_waitcnt lgkmcnt(4)\n\t"
    "v_smfmac_f32_16x16x64_bf16 v[116:119], v[120:123], v[84:91], v124\n\t"
    "ds_read_b64 v[84:85], v108 offset:0\n\t"
    "v_mfma_f32_16x16x32_bf16 v[80:83], v[64:67], v[204:207], 0\n\t"
    "ds_read_b64 v[86:87], v126 offset:0\n\t"
    "v_mfma_f32_16x16x32_bf16 v[80:83], v[68:71], v[208:211], v[80:83]\n\t"
    "v_add_u32_sdwa v90, s92, v109 dst_sel:DWORD dst_unused:UNUSED_PAD src0_sel:DWORD src1_sel:WORD_1\n\t"
    "v_add_u32_sdwa v109, s92, v109 dst_sel:DWORD dst_unused:UNUSED_PAD src0_sel:DWORD src1_sel:WORD_0\n\t"
    "ds_read_b64 v[88:89], v109 offset:0\n\t"
    "v_mfma_f32_16x16x32_bf16 v[80:83], v[72:75], v[132:135], v[80:83]\n\t"
    "ds_read_b64 v[90:91], v90 offset:0\n\t"
    "v_mfma_f32_16x16x32_bf16 v[80:83], v[76:79], v[136:139], v[80:83]\n\t"
    "v_add_u32_sdwa v126, s92, v110 dst_sel:DWORD dst_unused:UNUSED_PAD src0_sel:DWORD src1_sel:WORD_1\n\t"
    "v_add_u32_sdwa v110, s92, v110 dst_sel:DWORD dst_unused:UNUSED_PAD src0_sel:DWORD src1_sel:WORD_0\n\t"
    "s_cmp_lt_u32 %16, 4\n\t"
    "s_cbranch_scc1 L_tp19_%=\n\t"
    "s_setprio 0\n\t"
    "L_tp19_%=:\n\t"
    "s_waitcnt vmcnt(3) lgkmcnt(4)\n\t"
    "v_smfmac_f32_16x16x64_bf16 v[116:119], v[120:123], v[92:99], v124\n\t"
    "ds_read_b64 v[92:93], v110 offset:0\n\t"
    "ds_read_b64 v[94:95], v126 offset:0\n\t"
    "v_add_u32_sdwa v98, s92, v111 dst_sel:DWORD dst_unused:UNUSED_PAD src0_sel:DWORD src1_sel:WORD_1\n\t"
    "v_add_u32_sdwa v111, s92, v111 dst_sel:DWORD dst_unused:UNUSED_PAD src0_sel:DWORD src1_sel:WORD_0\n\t"
    "ds_read_b64 v[96:97], v111 offset:0\n\t"
    "ds_read_b64 v[98:99], v98 offset:0\n\t"
    "s_add_u32 s91, s91, s95\n\t"
    "buffer_load_dwordx4 v[108:111], v127, %12, s91 offen offset:0 sc1\n\t"
    "v_add_u32_sdwa v126, s92, v112 dst_sel:DWORD dst_unused:UNUSED_PAD src0_sel:DWORD src1_sel:WORD_1\n\t"
    "v_cvt_pk_bf16_f32 v38, v116, v117\n\t"
    "v_cvt_pk_bf16_f32 v39, v118, v119\n\t"
    "v_and_b32 v119, s67, v45\n\t"
    "v_lshlrev_b32 v118, 16, v45\n\t"
    "v_and_b32 v117, s67, v44\n\t"
    "v_lshlrev_b32 v116, 16, v44\n\t"
    "v_add_u32_sdwa v112, s92, v112 dst_sel:DWORD dst_unused:UNUSED_PAD src0_sel:DWORD src1_sel:WORD_0\n\t"
    "s_waitcnt lgkmcnt(4)\n\t"
    "v_smfmac_f32_16x16x64_bf16 v[116:119], v[120:123], v[84:91], v124\n\t"
    "ds_read_b64 v[84:85], v112 offset:0\n\t"
    "ds_read_b64 v[86:87], v126 offset:0\n\t"
    "v_cvt_pk_bf16_f32 v80, v80, v81\n\t"
    "v_cvt_pk_bf16_f32 v81, v82, v83\n\t"
    "ds_write_b64 v125, v[80:81] offset:640\n\t"
    "v_add_u32_sdwa v90, s92, v113 dst_sel:DWORD dst_unused:UNUSED_PAD src0_sel:DWORD src1_sel:WORD_1\n\t"
    "v_add_u32_sdwa v113, s92, v113 dst_sel:DWORD dst_unused:UNUSED_PAD src0_sel:DWORD src1_sel:WORD_0\n\t"
    "ds_read_b64 v[88:89], v113 offset:0\n\t"
    "ds_read_b64 v[90:91], v90 offset:0\n\t"
    "v_add_u32_sdwa v126, s92, v114 dst_sel:DWORD dst_unused:UNUSED_PAD src0_sel:DWORD src1_sel:WORD_1\n\t"
    "v_add_u32_sdwa v114, s92, v114 dst_sel:DWORD dst_unused:UNUSED_PAD src0_sel:DWORD src1_sel:WORD_0\n\t"
    "s_waitcnt vmcnt(2) lgkmcnt(5)\n\t"
    "v_smfmac_f32_16x16x64_bf16 v[116:119], v[120:123], v[92:99], v124\n\t"
    "ds_read_b64 v[92:93], v114 offset:0\n\t"
    "ds_read_b64 v[94:95], v126 offset:0\n\t"
    "v_add_u32_sdwa v98, s92, v115 dst_sel:DWORD dst_unused:UNUSED_PAD src0_sel:DWORD src1_sel:WORD_1\n\t"
    "v_add_u32_sdwa v115, s92, v115 dst_sel:DWORD dst_unused:UNUSED_PAD src0_sel:DWORD src1_sel:WORD_0\n\t"
    "ds_read_b64 v[96:97], v115 offset:0\n\t"
    "ds_read_b64 v[98:99], v98 offset:0\n\t"
    "buffer_load_dwordx4 v[112:115], v127, %12, s91 offen offset:1024 sc1\n\t"
    "v_add_u32_sdwa v126, s92, v100 dst_sel:DWORD dst_unused:UNUSED_PAD src0_sel:DWORD src1_sel:WORD_1\n\t"
    "v_cvt_pk_bf16_f32 v44, v116, v117\n\t"
    "v_cvt_pk_bf16_f32 v45, v118, v119\n\t"
    "v_and_b32 v119, s67, v47\n\t"
    "v_lshlrev_b32 v118, 16, v47\n\t"
    "v_and_b32 v117, s67, v46\n\t"
    "v_lshlrev_b32 v116, 16, v46\n\t"
    "v_add_u32_sdwa v100, s92, v100 dst_sel:DWORD dst_unused:UNUSED_PAD src0_sel:DWORD src1_sel:WORD_0\n\t"
    "s_waitcnt lgkmcnt(4)\n\t"
    "v_smfmac_f32_16x16x64_bf16 v[116:119], v[120:123], v[84:91], v124\n\t"
    "ds_read_b64 v[84:85], v100 offset:0\n\t"
    "v_mfma_f32_16x16x32_bf16 v[80:83], v[64:67], v[156:159], 0\n\t"
    "ds_read_b64 v[86:87], v126 offset:0\n\t"
    "v_mfma_f32_16x16x32_bf16 v[80:83], v[68:71], v[160:163], v[80:83]\n\t"
    "v_add_u32_sdwa v90, s92, v101 dst_sel:DWORD dst_unused:UNUSED_PAD src0_sel:DWORD src1_sel:WORD_1\n\t"
    "v_add_u32_sdwa v101, s92, v101 dst_sel:DWORD dst_unused:UNUSED_PAD src0_sel:DWORD src1_sel:WORD_0\n\t"
    "ds_read_b64 v[88:89], v101 offset:0\n\t"
    "v_mfma_f32_16x16x32_bf16 v[80:83], v[72:75], v[140:143], v[80:83]\n\t"
    "ds_read_b64 v[90:91], v90 offset:0\n\t"
    "v_mfma_f32_16x16x32_bf16 v[80:83], v[76:79], v[144:147], v[80:83]\n\t"
    "v_add_u32_sdwa v126, s92, v102 dst_sel:DWORD dst_unused:UNUSED_PAD src0_sel:DWORD src1_sel:WORD_1\n\t"
    "v_add_u32_sdwa v102, s92, v102 dst_sel:DWORD dst_unused:UNUSED_PAD src0_sel:DWORD src1_sel:WORD_0\n\t"
    "s_waitcnt vmcnt(2) lgkmcnt(4)\n\t"
    "v_smfmac_f32_16x16x64_bf16 v[116:119], v[120:123], v[92:99], v124\n\t"
    "ds_read_b64 v[92:93], v102 offset:0\n\t"
    "ds_read_b64 v[94:95], v126 offset:0\n\t"
    "v_add_u32_sdwa v98, s92, v103 dst_sel:DWORD dst_unused:UNUSED_PAD src0_sel:DWORD src1_sel:WORD_1\n\t"
    "v_add_u32_sdwa v103, s92, v103 dst_sel:DWORD dst_unused:UNUSED_PAD src0_sel:DWORD src1_sel:WORD_0\n\t"
    "ds_read_b64 v[96:97], v103 offset:0\n\t"
    "ds_read_b64 v[98:99], v98 offset:0\n\t"
    "s_add_u32 s84, s84, s94\n\t"
    "buffer_load_dwordx4 v[100:103], v127, %12, s84 offen offset:0 sc1\n\t"
    "v_add_u32_sdwa v126, s92, v104 dst_sel:DWORD dst_unused:UNUSED_PAD src0_sel:DWORD src1_sel:WORD_1\n\t"
    "v_cvt_pk_bf16_f32 v46, v116, v117\n\t"
    "v_cvt_pk_bf16_f32 v47, v118, v119\n\t"
    "v_and_b32 v119, s67, v53\n\t"
    "v_lshlrev_b32 v118, 16, v53\n\t"
    "v_and_b32 v117, s67, v52\n\t"
    "v_lshlrev_b32 v116, 16, v52\n\t"
    "v_add_u32_sdwa v104, s92, v104 dst_sel:DWORD dst_unused:UNUSED_PAD src0_sel:DWORD src1_sel:WORD_0\n\t"
    "s_waitcnt lgkmcnt(4)\n\t"
    "v_smfmac_f32_16x16x64_bf16 v[116:119], v[120:123], v[84:91], v124\n\t"
    "ds_read_b64 v[84:85], v104 offset:0\n\t"
    "ds_read_b64 v[86:87], v126 offset:0\n\t"
    "v_cvt_pk_bf16_f32 v80, v80, v81\n\t"
    "v_cvt_pk_bf16_f32 v81, v82, v83\n\t"
    "ds_write_b64 v125, v[80:81] offset:768\n\t"
    "v_add_u32_sdwa v90, s92, v105 dst_sel:DWORD dst_unused:UNUSED_PAD src0_sel:DWORD src1_sel:WORD_1\n\t"
    "v_add_u32_sdwa v105, s92, v105 dst_sel:DWORD dst_unused:UNUSED_PAD src0_sel:DWORD src1_sel:WORD_0\n\t"
    "ds_read_b64 v[88:89], v105 offset:0\n\t"
    "ds_read_b64 v[90:91], v90 offset:0\n\t"
    "v_add_u32_sdwa v126, s92, v106 dst_sel:DWORD dst_unused:UNUSED_PAD src0_sel:DWORD src1_sel:WORD_1\n\t"
    "v_add_u32_sdwa v106, s92, v106 dst_sel:DWORD dst_unused:UNUSED_PAD src0_sel:DWORD src1_sel:WORD_0\n\t"
    "s_waitcnt vmcnt(2) lgkmcnt(5)\n\t"
    "v_smfmac_f32_16x16x64_bf16 v[116:119], v[120:123], v[92:99], v124\n\t"
    "ds_read_b64 v[92:93], v106 offset:0\n\t"
    "ds_read_b64 v[94:95], v126 offset:0\n\t"
    "v_add_u32_sdwa v98, s92, v107 dst_sel:DWORD dst_unused:UNUSED_PAD src0_sel:DWORD src1_sel:WORD_1\n\t"
    "v_add_u32_sdwa v107, s92, v107 dst_sel:DWORD dst_unused:UNUSED_PAD src0_sel:DWORD src1_sel:WORD_0\n\t"
    "ds_read_b64 v[96:97], v107 offset:0\n\t"
    "ds_read_b64 v[98:99], v98 offset:0\n\t"
    "buffer_load_dwordx4 v[104:107], v127, %12, s84 offen offset:1024 sc1\n\t"
    "v_add_u32_sdwa v126, s92, v108 dst_sel:DWORD dst_unused:UNUSED_PAD src0_sel:DWORD src1_sel:WORD_1\n\t"
    "v_cvt_pk_bf16_f32 v52, v116, v117\n\t"
    "v_cvt_pk_bf16_f32 v53, v118, v119\n\t"
    "v_and_b32 v119, s67, v55\n\t"
    "v_lshlrev_b32 v118, 16, v55\n\t"
    "v_and_b32 v117, s67, v54\n\t"
    "v_lshlrev_b32 v116, 16, v54\n\t"
    "v_add_u32_sdwa v108, s92, v108 dst_sel:DWORD dst_unused:UNUSED_PAD src0_sel:DWORD src1_sel:WORD_0\n\t"
    "s_waitcnt lgkmcnt(4)\n\t"
    "v_smfmac_f32_16x16x64_bf16 v[116:119], v[120:123], v[84:91], v124\n\t"
    "ds_read_b64 v[84:85], v108 offset:0\n\t"
    "v_mfma_f32_16x16x32_bf16 v[80:83], v[64:67], v[212:215], 0\n\t"
    "ds_read_b64 v[86:87], v126 offset:0\n\t"
    "v_mfma_f32_16x16x32_bf16 v[80:83], v[68:71], v[216:219], v[80:83]\n\t"
    "v_add_u32_sdwa v90, s92, v109 dst_sel:DWORD dst_unused:UNUSED_PAD src0_sel:DWORD src1_sel:WORD_1\n\t"
    "v_add_u32_sdwa v109, s92, v109 dst_sel:DWORD dst_unused:UNUSED_PAD src0_sel:DWORD src1_sel:WORD_0\n\t"
    "ds_read_b64 v[88:89], v109 offset:0\n\t"
    "v_mfma_f32_16x16x32_bf16 v[80:83], v[72:75], v[148:151], v[80:83]\n\t"
    "ds_read_b64 v[90:91], v90 offset:0\n\t"
    "v_mfma_f32_16x16x32_bf16 v[80:83], v[76:79], v[152:155], v[80:83]\n\t"
    "v_add_u32 v79, s96, v127\n\t"
    "s_add_u32 s96, s96, 0x1000\n\t"
    "s_and_b32 s96, s96, 0x3000\n\t"
    "v_add_u32 v79, 128000, v79\n\t"
    "ds_read_b128 v[64:67], v79 offset:0\n\t"
    "ds_read_b128 v[68:71], v79 offset:1024\n\t"
    "ds_read_b128 v[72:75], v79 offset:2048\n\t"
    "ds_read_b128 v[76:79], v79 offset:3072\n\t"
    "v_add_u32_sdwa v126, s92, v110 dst_sel:DWORD dst_unused:UNUSED_PAD src0_sel:DWORD src1_sel:WORD_1\n\t"
    "v_add_u32_sdwa v110, s92, v110 dst_sel:DWORD dst_unused:UNUSED_PAD src0_sel:DWORD src1_sel:WORD_0\n\t"
    "s_waitcnt vmcnt(2) lgkmcnt(8)\n\t"
    "v_smfmac_f32_16x16x64_bf16 v[116:119], v[120:123], v[92:99], v124\n\t"
    "ds_read_b64 v[92:93], v110 offset:0\n\t"
    "ds_read_b64 v[94:95], v126 offset:0\n\t"
    "v_add_u32_sdwa v98, s92, v111 dst_sel:DWORD dst_unused:UNUSED_PAD src0_sel:DWORD src1_sel:WORD_1\n\t"
    "v_add_u32_sdwa v111, s92, v111 dst_sel:DWORD dst_unused:UNUSED_PAD src0_sel:DWORD src1_sel:WORD_0\n\t"
    "ds_read_b64 v[96:97], v111 offset:0\n\t"
    "ds_read_b64 v[98:99], v98 offset:0\n\t"
    "s_add_u32 s85, s85, s94\n\t"
    "buffer_load_dwordx4 v[108:111], v127, %12, s85 offen offset:0 sc1\n\t"
    "v_add_u32_sdwa v126, s92, v112 dst_sel:DWORD dst_unused:UNUSED_PAD src0_sel:DWORD src1_sel:WORD_1\n\t"
    "v_cvt_pk_bf16_f32 v54, v116, v117\n\t"
    "v_cvt_pk_bf16_f32 v55, v118, v119\n\t"
    "v_and_b32 v119, s67, v61\n\t"
    "v_lshlrev_b32 v118, 16, v61\n\t"
    "v_and_b32 v117, s67, v60\n\t"
    "v_lshlrev_b32 v116, 16, v60\n\t"
    "v_add_u32_sdwa v112, s92, v112 dst_sel:DWORD dst_unused:UNUSED_PAD src0_sel:DWORD src1_sel:WORD_0\n\t"
    "s_waitcnt lgkmcnt(8)\n\t"
    "v_smfmac_f32_16x16x64_bf16 v[116:119], v[120:123], v[84:91], v124\n\t"
    "ds_read_b64 v[84:85], v112 offset:0\n\t"
    "ds_read_b64 v[86:87], v126 offset:0\n\t"
    "v_cvt_pk_bf16_f32 v80, v80, v81\n\t"
    "v_cvt_pk_bf16_f32 v81, v82, v83\n\t"
    "ds_write_b64 v125, v[80:81] offset:872\n\t"
    "v_add_u32_sdwa v90, s92, v113 dst_sel:DWORD dst_unused:UNUSED_PAD src0_sel:DWORD src1_sel:WORD_1\n\t"
    "v_add_u32_sdwa v113, s92, v113 dst_sel:DWORD dst_unused:UNUSED_PAD src0_sel:DWORD src1_sel:WORD_0\n\t"
    "ds_read_b64 v[88:89], v113 offset:0\n\t"
    "ds_read_b64 v[90:91], v90 offset:0\n\t"
    "v_add_u32_sdwa v126, s92, v114 dst_sel:DWORD dst_unused:UNUSED_PAD src0_sel:DWORD src1_sel:WORD_1\n\t"
    "v_add_u32_sdwa v114, s92, v114 dst_sel:DWORD dst_unused:UNUSED_PAD src0_sel:DWORD src1_sel:WORD_0\n\t"
    "s_waitcnt vmcnt(2) lgkmcnt(5)\n\t"
    "v_smfmac_f32_16x16x64_bf16 v[116:119], v[120:123], v[92:99], v124\n\t"
    "ds_read_b64 v[92:93], v114 offset:0\n\t"
    "ds_read_b64 v[94:95], v126 offset:0\n\t"
    "v_add_u32_sdwa v98, s92, v115 dst_sel:DWORD dst_unused:UNUSED_PAD src0_sel:DWORD src1_sel:WORD_1\n\t"
    "v_add_u32_sdwa v115, s92, v115 dst_sel:DWORD dst_unused:UNUSED_PAD src0_sel:DWORD src1_sel:WORD_0\n\t"
    "ds_read_b64 v[96:97], v115 offset:0\n\t"
    "ds_read_b64 v[98:99], v98 offset:0\n\t"
    "buffer_load_dwordx4 v[112:115], v127, %12, s85 offen offset:1024 sc1\n\t"
    "v_add_u32_sdwa v126, s92, v100 dst_sel:DWORD dst_unused:UNUSED_PAD src0_sel:DWORD src1_sel:WORD_1\n\t"
    "v_cvt_pk_bf16_f32 v60, v116, v117\n\t"
    "v_cvt_pk_bf16_f32 v61, v118, v119\n\t"
    "v_and_b32 v119, s67, v63\n\t"
    "v_lshlrev_b32 v118, 16, v63\n\t"
    "v_and_b32 v117, s67, v62\n\t"
    "v_lshlrev_b32 v116, 16, v62\n\t"
    "v_add_u32_sdwa v100, s92, v100 dst_sel:DWORD dst_unused:UNUSED_PAD src0_sel:DWORD src1_sel:WORD_0\n\t"
    "s_waitcnt lgkmcnt(4)\n\t"
    "v_smfmac_f32_16x16x64_bf16 v[116:119], v[120:123], v[84:91], v124\n\t"
    "ds_read_b64 v[84:85], v100 offset:0\n\t"
    "ds_read_b64 v[86:87], v126 offset:0\n\t"
    "v_add_u32_sdwa v90, s92, v101 dst_sel:DWORD dst_unused:UNUSED_PAD src0_sel:DWORD src1_sel:WORD_1\n\t"
    "v_add_u32_sdwa v101, s92, v101 dst_sel:DWORD dst_unused:UNUSED_PAD src0_sel:DWORD src1_sel:WORD_0\n\t"
    "ds_read_b64 v[88:89], v101 offset:0\n\t"
    "ds_read_b64 v[90:91], v90 offset:0\n\t"
    "v_add_u32_sdwa v126, s92, v102 dst_sel:DWORD dst_unused:UNUSED_PAD src0_sel:DWORD src1_sel:WORD_1\n\t"
    "v_add_u32_sdwa v102, s92, v102 dst_sel:DWORD dst_unused:UNUSED_PAD src0_sel:DWORD src1_sel:WORD_0\n\t"
    "s_cmp_lt_u32 %16, 4\n\t"
    "s_cbranch_scc1 L_tp31_%=\n\t"
    "s_setprio 1\n\t"
    "L_tp31_%=:\n\t"
    "s_waitcnt vmcnt(2) lgkmcnt(4)\n\t"
    "v_smfmac_f32_16x16x64_bf16 v[116:119], v[120:123], v[92:99], v124\n\t"
    "ds_read_b64 v[92:93], v102 offset:0\n\t"
    "ds_read_b64 v[94:95], v126 offset:0\n\t"
    "v_add_u32_sdwa v98, s92, v103 dst_sel:DWORD dst_unused:UNUSED_PAD src0_sel:DWORD src1_sel:WORD_1\n\t"
    "v_add_u32_sdwa v103, s92, v103 dst_sel:DWORD dst_unused:UNUSED_PAD src0_sel:DWORD src1_sel:WORD_0\n\t"
    "ds_read_b64 v[96:97], v103 offset:0\n\t"
    "ds_read_b64 v[98:99], v98 offset:0\n\t"
    "s_add_u32 s86, s86, s94\n\t"
    "buffer_load_dwordx4 v[100:103], v127, %12, s86 offen offset:0 sc1\n\t"
    "v_add_u32_sdwa v126, s92, v104 dst_sel:DWORD dst_unused:UNUSED_PAD src0_sel:DWORD src1_sel:WORD_1\n\t"
    "v_cvt_pk_bf16_f32 v62, v116, v117\n\t"
    "v_cvt_pk_bf16_f32 v63, v118, v119\n\t"
    "v_and_b32 v119, s67, v1\n\t"
    "v_lshlrev_b32 v118, 16, v1\n\t"
    "v_and_b32 v117, s67, v0\n\t"
    "v_lshlrev_b32 v116, 16, v0\n\t"
    "v_add_u32_sdwa v104, s92, v104 dst_sel:DWORD dst_unused:UNUSED_PAD src0_sel:DWORD src1_sel:WORD_0\n\t"
    "s_waitcnt lgkmcnt(4)\n\t"
    "v_smfmac_f32_16x16x64_bf16 v[116:119], v[120:123], v[84:91], v124\n\t"
    "ds_read_b64 v[84:85], v104 offset:0\n\t"
    "v_mfma_f32_16x16x32_bf16 v[80:83], v[64:67], v[164:167], 0\n\t"
    "ds_read_b64 v[86:87], v126 offset:0\n\t"
    "v_mfma_f32_16x16x32_bf16 v[80:83], v[68:71], v[168:171], v[80:83]\n\t"
    "v_add_u32_sdwa v90, s92, v105 dst_sel:DWORD dst_unused:UNUSED_PAD src0_sel:DWORD src1_sel:WORD_1\n\t"
    "v_add_u32_sdwa v105, s92, v105 dst_sel:DWORD dst_unused:UNUSED_PAD src0_sel:DWORD src1_sel:WORD_0\n\t"
    "ds_read_b64 v[88:89], v105 offset:0\n\t"
    "v_mfma_f32_16x16x32_bf16 v[80:83], v[72:75], v[220:223], v[80:83]\n\t"
    "ds_read_b64 v[90:91], v90 offset:0\n\t"
    "v_mfma_f32_16x16x32_bf16 v[80:83], v[76:79], v[224:227], v[80:83]\n\t"
    "v_add_u32_sdwa v126, s92, v106 dst_sel:DWORD dst_unused:UNUSED_PAD src0_sel:DWORD src1_sel:WORD_1\n\t"
    "v_add_u32_sdwa v106, s92, v106 dst_sel:DWORD dst_unused:UNUSED_PAD src0_sel:DWORD src1_sel:WORD_0\n\t"
    "s_waitcnt vmcnt(2) lgkmcnt(4)\n\t"
    "v_smfmac_f32_16x16x64_bf16 v[116:119], v[120:123], v[92:99], v124\n\t"
    "ds_read_b64 v[92:93], v106 offset:0\n\t"
    "ds_read_b64 v[94:95], v126 offset:0\n\t"
    "v_add_u32_sdwa v98, s92, v107 dst_sel:DWORD dst_unused:UNUSED_PAD src0_sel:DWORD src1_sel:WORD_1\n\t"
    "v_add_u32_sdwa v107, s92, v107 dst_sel:DWORD dst_unused:UNUSED_PAD src0_sel:DWORD src1_sel:WORD_0\n\t"
    "ds_read_b64 v[96:97], v107 offset:0\n\t"
    "ds_read_b64 v[98:99], v98 offset:0\n\t"
    "buffer_load_dwordx4 v[104:107], v127, %12, s86 offen offset:1024 sc1\n\t"
    "v_add_u32_sdwa v126, s92, v108 dst_sel:DWORD dst_unused:UNUSED_PAD src0_sel:DWORD src1_sel:WORD_1\n\t"
    "v_cvt_pk_bf16_f32 v0, v116, v117\n\t"
    "v_cvt_pk_bf16_f32 v1, v118, v119\n\t"
    "v_and_b32 v119, s67, v3\n\t"
    "v_lshlrev_b32 v118, 16, v3\n\t"
    "v_and_b32 v117, s67, v2\n\t"
    "v_lshlrev_b32 v116, 16, v2\n\t"
    "v_add_u32_sdwa v108, s92, v108 dst_sel:DWORD dst_unused:UNUSED_PAD src0_sel:DWORD src1_sel:WORD_0\n\t"
    "s_waitcnt lgkmcnt(4)\n\t"
    "v_smfmac_f32_16x16x64_bf16 v[116:119], v[120:123], v[84:91], v124\n\t"
    "ds_read_b64 v[84:85], v108 offset:0\n\t"
    "ds_read_b64 v[86:87], v126 offset:0\n\t"
    "v_cvt_pk_bf16_f32 v80, v80, v81\n\t"
    "v_cvt_pk_bf16_f32 v81, v82, v83\n\t"
    "ds_write_b64 v125, v[80:81] offset:32000\n\t"
    "v_mfma_f32_16x16x32_bf16 v[80:83], v[64:67], v[172:175], 0\n\t"
    "v_add_u32_sdwa v90, s92, v109 dst_sel:DWORD dst_unused:UNUSED_PAD src0_sel:DWORD src1_sel:WORD_1\n\t"
    "v_add_u32_sdwa v109, s92, v109 dst_sel:DWORD dst_unused:UNUSED_PAD src0_sel:DWORD src1_sel:WORD_0\n\t"
    "ds_read_b64 v[88:89], v109 offset:0\n\t"
    "v_mfma_f32_16x16x32_bf16 v[80:83], v[68:71], v[176:179], v[80:83]\n\t"
    "ds_read_b64 v[90:91], v90 offset:0\n\t"
    "v_mfma_f32_16x16x32_bf16 v[80:83], v[72:75], v[228:231], v[80:83]\n\t"
    "v_mfma_f32_16x16x32_bf16 v[80:83], v[76:79], v[232:235], v[80:83]\n\t"
    "v_add_u32_sdwa v126, s92, v110 dst_sel:DWORD dst_unused:UNUSED_PAD src0_sel:DWORD src1_sel:WORD_1\n\t"
    "v_add_u32_sdwa v110, s92, v110 dst_sel:DWORD dst_unused:UNUSED_PAD src0_sel:DWORD src1_sel:WORD_0\n\t"
    "s_waitcnt vmcnt(2) lgkmcnt(5)\n\t"
    "v_smfmac_f32_16x16x64_bf16 v[116:119], v[120:123], v[92:99], v124\n\t"
    "ds_read_b64 v[92:93], v110 offset:0\n\t"
    "ds_read_b64 v[94:95], v126 offset:0\n\t"
    "v_add_u32_sdwa v98, s92, v111 dst_sel:DWORD dst_unused:UNUSED_PAD src0_sel:DWORD src1_sel:WORD_1\n\t"
    "v_add_u32_sdwa v111, s92, v111 dst_sel:DWORD dst_unused:UNUSED_PAD src0_sel:DWORD src1_sel:WORD_0\n\t"
    "ds_read_b64 v[96:97], v111 offset:0\n\t"
    "ds_read_b64 v[98:99], v98 offset:0\n\t"
    "s_add_u32 s87, s87, s94\n\t"
    "buffer_load_dwordx4 v[108:111], v127, %12, s87 offen offset:0 sc1\n\t"
    "v_add_u32_sdwa v126, s92, v112 dst_sel:DWORD dst_unused:UNUSED_PAD src0_sel:DWORD src1_sel:WORD_1\n\t"
    "v_cvt_pk_bf16_f32 v2, v116, v117\n\t"
    "v_cvt_pk_bf16_f32 v3, v118, v119\n\t"
    "v_and_b32 v119, s67, v9\n\t"
    "v_lshlrev_b32 v118, 16, v9\n\t"
    "v_and_b32 v117, s67, v8\n\t"
    "v_lshlrev_b32 v116, 16, v8\n\t"
    "v_add_u32_sdwa v112, s92, v112 dst_sel:DWORD dst_unused:UNUSED_PAD src0_sel:DWORD src1_sel:WORD_0\n\t"
    "s_waitcnt lgkmcnt(4)\n\t"
    "v_smfmac_f32_16x16x64_bf16 v[116:119], v[120:123], v[84:91], v124\n\t"
    "ds_read_b64 v[84:85], v112 offset:0\n\t"
    "ds_read_b64 v[86:87], v126 offset:0\n\t"
    "v_cvt_pk_bf16_f32 v80, v80, v81\n\t"
    "v_cvt_pk_bf16_f32 v81, v82, v83\n\t"
    "ds_write_b64 v125, v[80:81] offset:32128\n\t"
    "v_add_u32_sdwa v90, s92, v113 dst_sel:DWORD dst_unused:UNUSED_PAD src0_sel:DWORD src1_sel:WORD_1\n\t"
    "v_add_u32_sdwa v113, s92, v113 dst_sel:DWORD dst_unused:UNUSED_PAD src0_sel:DWORD src1_sel:WORD_0\n\t"
    "ds_read_b64 v[88:89], v113 offset:0\n\t"
    "ds_read_b64 v[90:91], v90 offset:0\n\t"
    "v_add_u32_sdwa v126, s92, v114 dst_sel:DWORD dst_unused:UNUSED_PAD src0_sel:DWORD src1_sel:WORD_1\n\t"
    "v_add_u32_sdwa v114, s92, v114 dst_sel:DWORD dst_unused:UNUSED_PAD src0_sel:DWORD src1_sel:WORD_0\n\t"
    "s_waitcnt vmcnt(2) lgkmcnt(5)\n\t"
    "v_smfmac_f32_16x16x64_bf16 v[116:119], v[120:123], v[92:99], v124\n\t"
    "ds_read_b64 v[92:93], v114 offset:0\n\t"
    "ds_read_b64 v[94:95], v126 offset:0\n\t"
    "v_add_u32_sdwa v98, s92, v115 dst_sel:DWORD dst_unused:UNUSED_PAD src0_sel:DWORD src1_sel:WORD_1\n\t"
    "v_add_u32_sdwa v115, s92, v115 dst_sel:DWORD dst_unused:UNUSED_PAD src0_sel:DWORD src1_sel:WORD_0\n\t"
    "ds_read_b64 v[96:97], v115 offset:0\n\t"
    "ds_read_b64 v[98:99], v98 offset:0\n\t"
    "buffer_load_dwordx4 v[112:115], v127, %12, s87 offen offset:1024 sc1\n\t"
    "v_add_u32_sdwa v126, s92, v100 dst_sel:DWORD dst_unused:UNUSED_PAD src0_sel:DWORD src1_sel:WORD_1\n\t"
    "v_cvt_pk_bf16_f32 v8, v116, v117\n\t"
    "v_cvt_pk_bf16_f32 v9, v118, v119\n\t"
    "v_and_b32 v119, s67, v11\n\t"
    "v_lshlrev_b32 v118, 16, v11\n\t"
    "v_and_b32 v117, s67, v10\n\t"
    "v_lshlrev_b32 v116, 16, v10\n\t"
    "v_add_u32_sdwa v100, s92, v100 dst_sel:DWORD dst_unused:UNUSED_PAD src0_sel:DWORD src1_sel:WORD_0\n\t"
    "s_waitcnt lgkmcnt(4)\n\t"
    "v_smfmac_f32_16x16x64_bf16 v[116:119], v[120:123], v[84:91], v124\n\t"
    "ds_read_b64 v[84:85], v100 offset:0\n\t"
    "v_mfma_f32_16x16x32_bf16 v[80:83], v[64:67], v[180:183], 0\n\t"
    "ds_read_b64 v[86:87], v126 offset:0\n\t"
    "v_mfma_f32_16x16x32_bf16 v[80:83], v[68:71], v[184:187], v[80:83]\n\t"
    "v_add_u32_sdwa v90, s92, v101 dst_sel:DWORD dst_unused:UNUSED_PAD src0_sel:DWORD src1_sel:WORD_1\n\t"
    "v_add_u32_sdwa v101, s92, v101 dst_sel:DWORD dst_unused:UNUSED_PAD src0_sel:DWORD src1_sel:WORD_0\n\t"
    "ds_read_b64 v[88:89], v101 offset:0\n\t"
    "v_mfma_f32_16x16x32_bf16 v[80:83], v[72:75], v[236:239], v[80:83]\n\t"
    "ds_read_b64 v[90:91], v90 offset:0\n\t"
    "v_mfma_f32_16x16x32_bf16 v[80:83], v[76:79], v[240:243], v[80:83]\n\t"
    "v_add_u32_sdwa v126, s92, v102 dst_sel:DWORD dst_unused:UNUSED_PAD src0_sel:DWORD src1_sel:WORD_1\n\t"
    "v_add_u32_sdwa v102, s92, v102 dst_sel:DWORD dst_unused:UNUSED_PAD src0_sel:DWORD src1_sel:WORD_0\n\t"
    "s_waitcnt vmcnt(2) lgkmcnt(4)\n\t"
    "v_smfmac_f32_16x16x64_bf16 v[116:119], v[120:123], v[92:99], v124\n\t"
    "ds_read_b64 v[92:93], v102 offset:0\n\t"
    "ds_read_b64 v[94:95], v126 offset:0\n\t"
    "v_add_u32_sdwa v98, s92, v103 dst_sel:DWORD dst_unused:UNUSED_PAD src0_sel:DWORD src1_sel:WORD_1\n\t"
    "v_add_u32_sdwa v103, s92, v103 dst_sel:DWORD dst_unused:UNUSED_PAD src0_sel:DWORD src1_sel:WORD_0\n\t"
    "ds_read_b64 v[96:97], v103 offset:0\n\t"
    "ds_read_b64 v[98:99], v98 offset:0\n\t"
    "s_add_u32 s88, s88, s94\n\t"
    "buffer_load_dwordx4 v[100:103], v127, %12, s88 offen offset:0 sc1\n\t"
    "v_add_u32_sdwa v126, s92, v104 dst_sel:DWORD dst_unused:UNUSED_PAD src0_sel:DWORD src1_sel:WORD_1\n\t"
    "v_cvt_pk_bf16_f32 v10, v116, v117\n\t"
    "v_cvt_pk_bf16_f32 v11, v118, v119\n\t"
    "v_and_b32 v119, s67, v17\n\t"
    "v_lshlrev_b32 v118, 16, v17\n\t"
    "v_and_b32 v117, s67, v16\n\t"
    "v_lshlrev_b32 v116, 16, v16\n\t"
    "v_add_u32_sdwa v104, s92, v104 dst_sel:DWORD dst_unused:UNUSED_PAD src0_sel:DWORD src1_sel:WORD_0\n\t"
    "s_waitcnt lgkmcnt(4)\n\t"
    "v_smfmac_f32_16x16x64_bf16 v[116:119], v[120:123], v[84:91], v124\n\t"
    "ds_read_b64 v[84:85], v104 offset:0\n\t"
    "ds_read_b64 v[86:87], v126 offset:0\n\t"
    "v_cvt_pk_bf16_f32 v80, v80, v81\n\t"
    "v_cvt_pk_bf16_f32 v81, v82, v83\n\t"
    "ds_write_b64 v125, v[80:81] offset:32256\n\t"
    "v_add_u32_sdwa v90, s92, v105 dst_sel:DWORD dst_unused:UNUSED_PAD src0_sel:DWORD src1_sel:WORD_1\n\t"
    "v_add_u32_sdwa v105, s92, v105 dst_sel:DWORD dst_unused:UNUSED_PAD src0_sel:DWORD src1_sel:WORD_0\n\t"
    "ds_read_b64 v[88:89], v105 offset:0\n\t"
    "ds_read_b64 v[90:91], v90 offset:0\n\t"
    "v_add_u32_sdwa v126, s92, v106 dst_sel:DWORD dst_unused:UNUSED_PAD src0_sel:DWORD src1_sel:WORD_1\n\t"
    "v_add_u32_sdwa v106, s92, v106 dst_sel:DWORD dst_unused:UNUSED_PAD src0_sel:DWORD src1_sel:WORD_0\n\t"
    "s_waitcnt vmcnt(2) lgkmcnt(5)\n\t"
    "v_smfmac_f32_16x16x64_bf16 v[116:119], v[120:123], v[92:99], v124\n\t"
    "ds_read_b64 v[92:93], v106 offset:0\n\t"
    "ds_read_b64 v[94:95], v126 offset:0\n\t"
    "v_add_u32_sdwa v98, s92, v107 dst_sel:DWORD dst_unused:UNUSED_PAD src0_sel:DWORD src1_sel:WORD_1\n\t"
    "v_add_u32_sdwa v107, s92, v107 dst_sel:DWORD dst_unused:UNUSED_PAD src0_sel:DWORD src1_sel:WORD_0\n\t"
    "ds_read_b64 v[96:97], v107 offset:0\n\t"
    "ds_read_b64 v[98:99], v98 offset:0\n\t"
    "buffer_load_dwordx4 v[104:107], v127, %12, s88 offen offset:1024 sc1\n\t"
    "v_add_u32_sdwa v126, s92, v108 dst_sel:DWORD dst_unused:UNUSED_PAD src0_sel:DWORD src1_sel:WORD_1\n\t"
    "v_cvt_pk_bf16_f32 v16, v116, v117\n\t"
    "v_cvt_pk_bf16_f32 v17, v118, v119\n\t"
    "v_and_b32 v119, s67, v19\n\t"
    "v_lshlrev_b32 v118, 16, v19\n\t"
    "v_and_b32 v117, s67, v18\n\t"
    "v_lshlrev_b32 v116, 16, v18\n\t"
    "v_add_u32_sdwa v108, s92, v108 dst_sel:DWORD dst_unused:UNUSED_PAD src0_sel:DWORD src1_sel:WORD_0\n\t"
    "s_waitcnt lgkmcnt(4)\n\t"
    "v_smfmac_f32_16x16x64_bf16 v[116:119], v[120:123], v[84:91], v124\n\t"
    "ds_read_b64 v[84:85], v108 offset:0\n\t"
    "v_mfma_f32_16x16x32_bf16 v[80:83], v[64:67], v[188:191], 0\n\t"
    "ds_read_b64 v[86:87], v126 offset:0\n\t"
    "v_mfma_f32_16x16x32_bf16 v[80:83], v[68:71], v[192:195], v[80:83]\n\t"
    "v_add_u32_sdwa v90, s92, v109 dst_sel:DWORD dst_unused:UNUSED_PAD src0_sel:DWORD src1_sel:WORD_1\n\t"
    "v_add_u32_sdwa v109, s92, v109 dst_sel:DWORD dst_unused:UNUSED_PAD src0_sel:DWORD src1_sel:WORD_0\n\t"
    "ds_read_b64 v[88:89], v109 offset:0\n\t"
    "v_mfma_f32_16x16x32_bf16 v[80:83], v[72:75], v[244:247], v[80:83]\n\t"
    "ds_read_b64 v[90:91], v90 offset:0\n\t"
    "v_mfma_f32_16x16x32_bf16 v[80:83], v[76:79], v[248:251], v[80:83]\n\t"
    "v_add_u32_sdwa v126, s92, v110 dst_sel:DWORD dst_unused:UNUSED_PAD src0_sel:DWORD src1_sel:WORD_1\n\t"
    "v_add_u32_sdwa v110, s92, v110 dst_sel:DWORD dst_unused:UNUSED_PAD src0_sel:DWORD src1_sel:WORD_0\n\t"
    "s_waitcnt vmcnt(2) lgkmcnt(4)\n\t"
    "v_smfmac_f32_16x16x64_bf16 v[116:119], v[120:123], v[92:99], v124\n\t"
    "ds_read_b64 v[92:93], v110 offset:0\n\t"
    "ds_read_b64 v[94:95], v126 offset:0\n\t"
    "v_add_u32_sdwa v98, s92, v111 dst_sel:DWORD dst_unused:UNUSED_PAD src0_sel:DWORD src1_sel:WORD_1\n\t"
    "v_add_u32_sdwa v111, s92, v111 dst_sel:DWORD dst_unused:UNUSED_PAD src0_sel:DWORD src1_sel:WORD_0\n\t"
    "ds_read_b64 v[96:97], v111 offset:0\n\t"
    "ds_read_b64 v[98:99], v98 offset:0\n\t"
    "s_add_u32 s89, s89, s94\n\t"
    "buffer_load_dwordx4 v[108:111], v127, %12, s89 offen offset:0 sc1\n\t"
    "v_add_u32_sdwa v126, s92, v112 dst_sel:DWORD dst_unused:UNUSED_PAD src0_sel:DWORD src1_sel:WORD_1\n\t"
    "v_cvt_pk_bf16_f32 v18, v116, v117\n\t"
    "v_cvt_pk_bf16_f32 v19, v118, v119\n\t"
    "v_and_b32 v119, s67, v25\n\t"
    "v_lshlrev_b32 v118, 16, v25\n\t"
    "v_and_b32 v117, s67, v24\n\t"
    "v_lshlrev_b32 v116, 16, v24\n\t"
    "v_add_u32_sdwa v112, s92, v112 dst_sel:DWORD dst_unused:UNUSED_PAD src0_sel:DWORD src1_sel:WORD_0\n\t"
    "s_waitcnt lgkmcnt(4)\n\t"
    "v_smfmac_f32_16x16x64_bf16 v[116:119], v[120:123], v[84:91], v124\n\t"
    "ds_read_b64 v[84:85], v112 offset:0\n\t"
    "ds_read_b64 v[86:87], v126 offset:0\n\t"
    "v_cvt_pk_bf16_f32 v80, v80, v81\n\t"
    "v_cvt_pk_bf16_f32 v81, v82, v83\n\t"
    "ds_write_b64 v125, v[80:81] offset:32384\n\t"
    "v_add_u32_sdwa v90, s92, v113 dst_sel:DWORD dst_unused:UNUSED_PAD src0_sel:DWORD src1_sel:WORD_1\n\t"
    "v_add_u32_sdwa v113, s92, v113 dst_sel:DWORD dst_unused:UNUSED_PAD src0_sel:DWORD src1_sel:WORD_0\n\t"
    "ds_read_b64 v[88:89], v113 offset:0\n\t"
    "ds_read_b64 v[90:91], v90 offset:0\n\t"
    "v_add_u32_sdwa v126, s92, v114 dst_sel:DWORD dst_unused:UNUSED_PAD src0_sel:DWORD src1_sel:WORD_1\n\t"
    "v_add_u32_sdwa v114, s92, v114 dst_sel:DWORD dst_unused:UNUSED_PAD src0_sel:DWORD src1_sel:WORD_0\n\t"
    "s_waitcnt vmcnt(2) lgkmcnt(5)\n\t"
    "v_smfmac_f32_16x16x64_bf16 v[116:119], v[120:123], v[92:99], v124\n\t"
    "ds_read_b64 v[92:93], v114 offset:0\n\t"
    "ds_read_b64 v[94:95], v126 offset:0\n\t"
    "v_add_u32_sdwa v98, s92, v115 dst_sel:DWORD dst_unused:UNUSED_PAD src0_sel:DWORD src1_sel:WORD_1\n\t"
    "v_add_u32_sdwa v115, s92, v115 dst_sel:DWORD dst_unused:UNUSED_PAD src0_sel:DWORD src1_sel:WORD_0\n\t"
    "ds_read_b64 v[96:97], v115 offset:0\n\t"
    "ds_read_b64 v[98:99], v98 offset:0\n\t"
    "buffer_load_dwordx4 v[112:115], v127, %12, s89 offen offset:1024 sc1\n\t"
    "v_add_u32_sdwa v126, s92, v100 dst_sel:DWORD dst_unused:UNUSED_PAD src0_sel:DWORD src1_sel:WORD_1\n\t"
    "v_cvt_pk_bf16_f32 v24, v116, v117\n\t"
    "v_cvt_pk_bf16_f32 v25, v118, v119\n\t"
    "v_and_b32 v119, s67, v27\n\t"
    "v_lshlrev_b32 v118, 16, v27\n\t"
    "v_and_b32 v117, s67, v26\n\t"
    "v_lshlrev_b32 v116, 16, v26\n\t"
    "v_add_u32_sdwa v100, s92, v100 dst_sel:DWORD dst_unused:UNUSED_PAD src0_sel:DWORD src1_sel:WORD_0\n\t"
    "s_waitcnt lgkmcnt(4)\n\t"
    "v_smfmac_f32_16x16x64_bf16 v[116:119], v[120:123], v[84:91], v124\n\t"
    "ds_read_b64 v[84:85], v100 offset:0\n\t"
    "v_mfma_f32_16x16x32_bf16 v[80:83], v[64:67], v[196:199], 0\n\t"
    "ds_read_b64 v[86:87], v126 offset:0\n\t"
    "v_mfma_f32_16x16x32_bf16 v[80:83], v[68:71], v[200:203], v[80:83]\n\t"
    "v_add_u32_sdwa v90, s92, v101 dst_sel:DWORD dst_unused:UNUSED_PAD src0_sel:DWORD src1_sel:WORD_1\n\t"
    "v_add_u32_sdwa v101, s92, v101 dst_sel:DWORD dst_unused:UNUSED_PAD src0_sel:DWORD src1_sel:WORD_0\n\t"
    "ds_read_b64 v[88:89], v101 offset:0\n\t"
    "v_mfma_f32_16x16x32_bf16 v[80:83], v[72:75], v[252:255], v[80:83]\n\t"
    "ds_read_b64 v[90:91], v90 offset:0\n\t"
    "v_mfma_f32_16x16x32_bf16 v[80:83], v[76:79], v[128:131], v[80:83]\n\t"
    "v_add_u32_sdwa v126, s92, v102 dst_sel:DWORD dst_unused:UNUSED_PAD src0_sel:DWORD src1_sel:WORD_1\n\t"
    "v_add_u32_sdwa v102, s92, v102 dst_sel:DWORD dst_unused:UNUSED_PAD src0_sel:DWORD src1_sel:WORD_0\n\t"
    "s_waitcnt vmcnt(2) lgkmcnt(4)\n\t"
    "v_smfmac_f32_16x16x64_bf16 v[116:119], v[120:123], v[92:99], v124\n\t"
    "ds_read_b64 v[92:93], v102 offset:0\n\t"
    "ds_read_b64 v[94:95], v126 offset:0\n\t"
    "v_add_u32_sdwa v98, s92, v103 dst_sel:DWORD dst_unused:UNUSED_PAD src0_sel:DWORD src1_sel:WORD_1\n\t"
    "v_add_u32_sdwa v103, s92, v103 dst_sel:DWORD dst_unused:UNUSED_PAD src0_sel:DWORD src1_sel:WORD_0\n\t"
    "ds_read_b64 v[96:97], v103 offset:0\n\t"
    "ds_read_b64 v[98:99], v98 offset:0\n\t"
    "s_add_u32 s90, s90, s94\n\t"
    "buffer_load_dwordx4 v[100:103], v127, %12, s90 offen offset:0 sc1\n\t"
    "v_add_u32_sdwa v126, s92, v104 dst_sel:DWORD dst_unused:UNUSED_PAD src0_sel:DWORD src1_sel:WORD_1\n\t"
    "v_cvt_pk_bf16_f32 v26, v116, v117\n\t"
    "v_cvt_pk_bf16_f32 v27, v118, v119\n\t"
    "v_and_b32 v119, s67, v33\n\t"
    "v_lshlrev_b32 v118, 16, v33\n\t"
    "v_and_b32 v117, s67, v32\n\t"
    "v_lshlrev_b32 v116, 16, v32\n\t"
    "v_add_u32_sdwa v104, s92, v104 dst_sel:DWORD dst_unused:UNUSED_PAD src0_sel:DWORD src1_sel:WORD_0\n\t"
    "s_waitcnt lgkmcnt(4)\n\t"
    "v_smfmac_f32_16x16x64_bf16 v[116:119], v[120:123], v[84:91], v124\n\t"
    "ds_read_b64 v[84:85], v104 offset:0\n\t"
    "ds_read_b64 v[86:87], v126 offset:0\n\t"
    "v_cvt_pk_bf16_f32 v80, v80, v81\n\t"
    "v_cvt_pk_bf16_f32 v81, v82, v83\n\t"
    "ds_write_b64 v125, v[80:81] offset:32512\n\t"
    "v_add_u32_sdwa v90, s92, v105 dst_sel:DWORD dst_unused:UNUSED_PAD src0_sel:DWORD src1_sel:WORD_1\n\t"
    "v_add_u32_sdwa v105, s92, v105 dst_sel:DWORD dst_unused:UNUSED_PAD src0_sel:DWORD src1_sel:WORD_0\n\t"
    "ds_read_b64 v[88:89], v105 offset:0\n\t"
    "ds_read_b64 v[90:91], v90 offset:0\n\t"
    "v_add_u32_sdwa v126, s92, v106 dst_sel:DWORD dst_unused:UNUSED_PAD src0_sel:DWORD src1_sel:WORD_1\n\t"
    "v_add_u32_sdwa v106, s92, v106 dst_sel:DWORD dst_unused:UNUSED_PAD src0_sel:DWORD src1_sel:WORD_0\n\t"
    "s_waitcnt vmcnt(2) lgkmcnt(5)\n\t"
    "v_smfmac_f32_16x16x64_bf16 v[116:119], v[120:123], v[92:99], v124\n\t"
    "ds_read_b64 v[92:93], v106 offset:0\n\t"
    "ds_read_b64 v[94:95], v126 offset:0\n\t"
    "v_add_u32_sdwa v98, s92, v107 dst_sel:DWORD dst_unused:UNUSED_PAD src0_sel:DWORD src1_sel:WORD_1\n\t"
    "v_add_u32_sdwa v107, s92, v107 dst_sel:DWORD dst_unused:UNUSED_PAD src0_sel:DWORD src1_sel:WORD_0\n\t"
    "ds_read_b64 v[96:97], v107 offset:0\n\t"
    "ds_read_b64 v[98:99], v98 offset:0\n\t"
    "buffer_load_dwordx4 v[104:107], v127, %12, s90 offen offset:1024 sc1\n\t"
    "v_add_u32_sdwa v126, s92, v108 dst_sel:DWORD dst_unused:UNUSED_PAD src0_sel:DWORD src1_sel:WORD_1\n\t"
    "v_cvt_pk_bf16_f32 v32, v116, v117\n\t"
    "v_cvt_pk_bf16_f32 v33, v118, v119\n\t"
    "v_and_b32 v119, s67, v35\n\t"
    "v_lshlrev_b32 v118, 16, v35\n\t"
    "v_and_b32 v117, s67, v34\n\t"
    "v_lshlrev_b32 v116, 16, v34\n\t"
    "v_add_u32_sdwa v108, s92, v108 dst_sel:DWORD dst_unused:UNUSED_PAD src0_sel:DWORD src1_sel:WORD_0\n\t"
    "s_waitcnt lgkmcnt(4)\n\t"
    "v_smfmac_f32_16x16x64_bf16 v[116:119], v[120:123], v[84:91], v124\n\t"
    "ds_read_b64 v[84:85], v108 offset:0\n\t"
    "v_mfma_f32_16x16x32_bf16 v[80:83], v[64:67], v[204:207], 0\n\t"
    "ds_read_b64 v[86:87], v126 offset:0\n\t"
    "v_mfma_f32_16x16x32_bf16 v[80:83], v[68:71], v[208:211], v[80:83]\n\t"
    "v_add_u32_sdwa v90, s92, v109 dst_sel:DWORD dst_unused:UNUSED_PAD src0_sel:DWORD src1_sel:WORD_1\n\t"
    "v_add_u32_sdwa v109, s92, v109 dst_sel:DWORD dst_unused:UNUSED_PAD src0_sel:DWORD src1_sel:WORD_0\n\t"
    "ds_read_b64 v[88:89], v109 offset:0\n\t"
    "v_mfma_f32_16x16x32_bf16 v[80:83], v[72:75], v[132:135], v[80:83]\n\t"
    "ds_read_b64 v[90:91], v90 offset:0\n\t"
    "v_mfma_f32_16x16x32_bf16 v[80:83], v[76:79], v[136:139], v[80:83]\n\t"
    "v_add_u32_sdwa v126, s92, v110 dst_sel:DWORD dst_unused:UNUSED_PAD src0_sel:DWORD src1_sel:WORD_1\n\t"
    "v_add_u32_sdwa v110, s92, v110 dst_sel:DWORD dst_unused:UNUSED_PAD src0_sel:DWORD src1_sel:WORD_0\n\t"
    "s_cmp_lt_u32 %16, 4\n\t"
    "s_cbranch_scc1 L_tp51_%=\n\t"
    "s_setprio 0\n\t"
    "L_tp51_%=:\n\t"
    "s_waitcnt vmcnt(2) lgkmcnt(4)\n\t"
    "v_smfmac_f32_16x16x64_bf16 v[116:119], v[120:123], v[92:99], v124\n\t"
    "ds_read_b64 v[92:93], v110 offset:0\n\t"
    "ds_read_b64 v[94:95], v126 offset:0\n\t"
    "v_add_u32_sdwa v98, s92, v111 dst_sel:DWORD dst_unused:UNUSED_PAD src0_sel:DWORD src1_sel:WORD_1\n\t"
    "v_add_u32_sdwa v111, s92, v111 dst_sel:DWORD dst_unused:UNUSED_PAD src0_sel:DWORD src1_sel:WORD_0\n\t"
    "ds_read_b64 v[96:97], v111 offset:0\n\t"
    "ds_read_b64 v[98:99], v98 offset:0\n\t"
    "s_add_u32 s91, s91, s94\n\t"
    "buffer_load_dwordx4 v[108:111], v127, %12, s91 offen offset:0 sc1\n\t"
    "v_add_u32_sdwa v126, s92, v112 dst_sel:DWORD dst_unused:UNUSED_PAD src0_sel:DWORD src1_sel:WORD_1\n\t"
    "v_cvt_pk_bf16_f32 v34, v116, v117\n\t"
    "v_cvt_pk_bf16_f32 v35, v118, v119\n\t"
    "v_and_b32 v119, s67, v41\n\t"
    "v_lshlrev_b32 v118, 16, v41\n\t"
    "v_and_b32 v117, s67, v40\n\t"
    "v_lshlrev_b32 v116, 16, v40\n\t"
    "v_add_u32_sdwa v112, s92, v112 dst_sel:DWORD dst_unused:UNUSED_PAD src0_sel:DWORD src1_sel:WORD_0\n\t"
    "s_waitcnt lgkmcnt(4)\n\t"
    "v_smfmac_f32_16x16x64_bf16 v[116:119], v[120:123], v[84:91], v124\n\t"
    "ds_read_b64 v[84:85], v112 offset:0\n\t"
    "ds_read_b64 v[86:87], v126 offset:0\n\t"
    "v_cvt_pk_bf16_f32 v80, v80, v81\n\t"
    "v_cvt_pk_bf16_f32 v81, v82, v83\n\t"
    "ds_write_b64 v125, v[80:81] offset:32640\n\t"
    "v_add_u32_sdwa v90, s92, v113 dst_sel:DWORD dst_unused:UNUSED_PAD src0_sel:DWORD src1_sel:WORD_1\n\t"
    "v_add_u32_sdwa v113, s92, v113 dst_sel:DWORD dst_unused:UNUSED_PAD src0_sel:DWORD src1_sel:WORD_0\n\t"
    "ds_read_b64 v[88:89], v113 offset:0\n\t"
    "ds_read_b64 v[90:91], v90 offset:0\n\t"
    "v_add_u32_sdwa v126, s92, v114 dst_sel:DWORD dst_unused:UNUSED_PAD src0_sel:DWORD src1_sel:WORD_1\n\t"
    "v_add_u32_sdwa v114, s92, v114 dst_sel:DWORD dst_unused:UNUSED_PAD src0_sel:DWORD src1_sel:WORD_0\n\t"
    "s_waitcnt vmcnt(2) lgkmcnt(5)\n\t"
    "v_smfmac_f32_16x16x64_bf16 v[116:119], v[120:123], v[92:99], v124\n\t"
    "ds_read_b64 v[92:93], v114 offset:0\n\t"
    "ds_read_b64 v[94:95], v126 offset:0\n\t"
    "v_add_u32_sdwa v98, s92, v115 dst_sel:DWORD dst_unused:UNUSED_PAD src0_sel:DWORD src1_sel:WORD_1\n\t"
    "v_add_u32_sdwa v115, s92, v115 dst_sel:DWORD dst_unused:UNUSED_PAD src0_sel:DWORD src1_sel:WORD_0\n\t"
    "ds_read_b64 v[96:97], v115 offset:0\n\t"
    "ds_read_b64 v[98:99], v98 offset:0\n\t"
    "buffer_load_dwordx4 v[112:115], v127, %12, s91 offen offset:1024 sc1\n\t"
    "v_add_u32_sdwa v126, s92, v100 dst_sel:DWORD dst_unused:UNUSED_PAD src0_sel:DWORD src1_sel:WORD_1\n\t"
    "v_cvt_pk_bf16_f32 v40, v116, v117\n\t"
    "v_cvt_pk_bf16_f32 v41, v118, v119\n\t"
    "v_and_b32 v119, s67, v43\n\t"
    "v_lshlrev_b32 v118, 16, v43\n\t"
    "v_and_b32 v117, s67, v42\n\t"
    "v_lshlrev_b32 v116, 16, v42\n\t"
    "v_add_u32_sdwa v100, s92, v100 dst_sel:DWORD dst_unused:UNUSED_PAD src0_sel:DWORD src1_sel:WORD_0\n\t"
    "s_waitcnt lgkmcnt(4)\n\t"
    "v_smfmac_f32_16x16x64_bf16 v[116:119], v[120:123], v[84:91], v124\n\t"
    "ds_read_b64 v[84:85], v100 offset:0\n\t"
    "v_mfma_f32_16x16x32_bf16 v[80:83], v[64:67], v[156:159], 0\n\t"
    "ds_read_b64 v[86:87], v126 offset:0\n\t"
    "v_mfma_f32_16x16x32_bf16 v[80:83], v[68:71], v[160:163], v[80:83]\n\t"
    "v_add_u32_sdwa v90, s92, v101 dst_sel:DWORD dst_unused:UNUSED_PAD src0_sel:DWORD src1_sel:WORD_1\n\t"
    "v_add_u32_sdwa v101, s92, v101 dst_sel:DWORD dst_unused:UNUSED_PAD src0_sel:DWORD src1_sel:WORD_0\n\t"
    "ds_read_b64 v[88:89], v101 offset:0\n\t"
    "v_mfma_f32_16x16x32_bf16 v[80:83], v[72:75], v[140:143], v[80:83]\n\t"
    "ds_read_b64 v[90:91], v90 offset:0\n\t"
    "v_mfma_f32_16x16x32_bf16 v[80:83], v[76:79], v[144:147], v[80:83]\n\t"
    "v_add_u32_sdwa v126, s92, v102 dst_sel:DWORD dst_unused:UNUSED_PAD src0_sel:DWORD src1_sel:WORD_1\n\t"
    "v_add_u32_sdwa v102, s92, v102 dst_sel:DWORD dst_unused:UNUSED_PAD src0_sel:DWORD src1_sel:WORD_0\n\t"
    "s_waitcnt vmcnt(2) lgkmcnt(4)\n\t"
    "v_smfmac_f32_16x16x64_bf16 v[116:119], v[120:123], v[92:99], v124\n\t"
    "ds_read_b64 v[92:93], v102 offset:0\n\t"
    "ds_read_b64 v[94:95], v126 offset:0\n\t"
    "v_add_u32_sdwa v98, s92, v103 dst_sel:DWORD dst_unused:UNUSED_PAD src0_sel:DWORD src1_sel:WORD_1\n\t"
    "v_add_u32_sdwa v103, s92, v103 dst_sel:DWORD dst_unused:UNUSED_PAD src0_sel:DWORD src1_sel:WORD_0\n\t"
    "ds_read_b64 v[96:97], v103 offset:0\n\t"
    "ds_read_b64 v[98:99], v98 offset:0\n\t"
    "s_add_u32 s84, s84, s95\n\t"
    "buffer_load_dwordx4 v[100:103], v127, %12, s84 offen offset:0 sc1\n\t"
    "v_add_u32_sdwa v126, s92, v104 dst_sel:DWORD dst_unused:UNUSED_PAD src0_sel:DWORD src1_sel:WORD_1\n\t"
    "v_cvt_pk_bf16_f32 v42, v116, v117\n\t"
    "v_cvt_pk_bf16_f32 v43, v118, v119\n\t"
    "v_and_b32 v119, s67, v49\n\t"
    "v_lshlrev_b32 v118, 16, v49\n\t"
    "v_and_b32 v117, s67, v48\n\t"
    "v_lshlrev_b32 v116, 16, v48\n\t"
    "v_add_u32_sdwa v104, s92, v104 dst_sel:DWORD dst_unused:UNUSED_PAD src0_sel:DWORD src1_sel:WORD_0\n\t"
    "s_waitcnt lgkmcnt(4)\n\t"
    "v_smfmac_f32_16x16x64_bf16 v[116:119], v[120:123], v[84:91], v124\n\t"
    "ds_read_b64 v[84:85], v104 offset:0\n\t"
    "ds_read_b64 v[86:87], v126 offset:0\n\t"
    "v_cvt_pk_bf16_f32 v80, v80, v81\n\t"
    "v_cvt_pk_bf16_f32 v81, v82, v83\n\t"
    "ds_write_b64 v125, v[80:81] offset:32768\n\t"
    "v_add_u32_sdwa v90, s92, v105 dst_sel:DWORD dst_unused:UNUSED_PAD src0_sel:DWORD src1_sel:WORD_1\n\t"
    "v_add_u32_sdwa v105, s92, v105 dst_sel:DWORD dst_unused:UNUSED_PAD src0_sel:DWORD src1_sel:WORD_0\n\t"
    "ds_read_b64 v[88:89], v105 offset:0\n\t"
    "ds_read_b64 v[90:91], v90 offset:0\n\t"
    "v_add_u32_sdwa v126, s92, v106 dst_sel:DWORD dst_unused:UNUSED_PAD src0_sel:DWORD src1_sel:WORD_1\n\t"
    "v_add_u32_sdwa v106, s92, v106 dst_sel:DWORD dst_unused:UNUSED_PAD src0_sel:DWORD src1_sel:WORD_0\n\t"
    "s_waitcnt vmcnt(2) lgkmcnt(5)\n\t"
    "v_smfmac_f32_16x16x64_bf16 v[116:119], v[120:123], v[92:99], v124\n\t"
    "ds_read_b64 v[92:93], v106 offset:0\n\t"
    "ds_read_b64 v[94:95], v126 offset:0\n\t"
    "v_add_u32_sdwa v98, s92, v107 dst_sel:DWORD dst_unused:UNUSED_PAD src0_sel:DWORD src1_sel:WORD_1\n\t"
    "v_add_u32_sdwa v107, s92, v107 dst_sel:DWORD dst_unused:UNUSED_PAD src0_sel:DWORD src1_sel:WORD_0\n\t"
    "ds_read_b64 v[96:97], v107 offset:0\n\t"
    "ds_read_b64 v[98:99], v98 offset:0\n\t"
    "buffer_load_dwordx4 v[104:107], v127, %12, s84 offen offset:1024 sc1\n\t"
    "v_add_u32_sdwa v126, s92, v108 dst_sel:DWORD dst_unused:UNUSED_PAD src0_sel:DWORD src1_sel:WORD_1\n\t"
    "v_cvt_pk_bf16_f32 v48, v116, v117\n\t"
    "v_cvt_pk_bf16_f32 v49, v118, v119\n\t"
    "v_and_b32 v119, s67, v51\n\t"
    "v_lshlrev_b32 v118, 16, v51\n\t"
    "v_and_b32 v117, s67, v50\n\t"
    "v_lshlrev_b32 v116, 16, v50\n\t"
    "v_add_u32_sdwa v108, s92, v108 dst_sel:DWORD dst_unused:UNUSED_PAD src0_sel:DWORD src1_sel:WORD_0\n\t"
    "s_waitcnt lgkmcnt(4)\n\t"
    "v_smfmac_f32_16x16x64_bf16 v[116:119], v[120:123], v[84:91], v124\n\t"
    "ds_read_b64 v[84:85], v108 offset:0\n\t"
    "v_mfma_f32_16x16x32_bf16 v[80:83], v[64:67], v[212:215], 0\n\t"
    "ds_read_b64 v[86:87], v126 offset:0\n\t"
    "v_mfma_f32_16x16x32_bf16 v[80:83], v[68:71], v[216:219], v[80:83]\n\t"
    "v_add_u32_sdwa v90, s92, v109 dst_sel:DWORD dst_unused:UNUSED_PAD src0_sel:DWORD src1_sel:WORD_1\n\t"
    "v_add_u32_sdwa v109, s92, v109 dst_sel:DWORD dst_unused:UNUSED_PAD src0_sel:DWORD src1_sel:WORD_0\n\t"
    "ds_read_b64 v[88:89], v109 offset:0\n\t"
    "v_mfma_f32_16x16x32_bf16 v[80:83], v[72:75], v[148:151], v[80:83]\n\t"
    "ds_read_b64 v[90:91], v90 offset:0\n\t"
    "v_mfma_f32_16x16x32_bf16 v[80:83], v[76:79], v[152:155], v[80:83]\n\t"
    "v_add_u32_sdwa v126, s92, v110 dst_sel:DWORD dst_unused:UNUSED_PAD src0_sel:DWORD src1_sel:WORD_1\n\t"
    "v_add_u32_sdwa v110, s92, v110 dst_sel:DWORD dst_unused:UNUSED_PAD src0_sel:DWORD src1_sel:WORD_0\n\t"
    "s_waitcnt vmcnt(2) lgkmcnt(4)\n\t"
    "v_smfmac_f32_16x16x64_bf16 v[116:119], v[120:123], v[92:99], v124\n\t"
    "ds_read_b64 v[92:93], v110 offset:0\n\t"
    "ds_read_b64 v[94:95], v126 offset:0\n\t"
    "v_add_u32_sdwa v98, s92, v111 dst_sel:DWORD dst_unused:UNUSED_PAD src0_sel:DWORD src1_sel:WORD_1\n\t"
    "v_add_u32_sdwa v111, s92, v111 dst_sel:DWORD dst_unused:UNUSED_PAD src0_sel:DWORD src1_sel:WORD_0\n\t"
    "ds_read_b64 v[96:97], v111 offset:0\n\t"
    "ds_read_b64 v[98:99], v98 offset:0\n\t"
    "s_add_u32 s85, s85, s95\n\t"
    "buffer_load_dwordx4 v[108:111], v127, %12, s85 offen offset:0 sc1\n\t"
    "v_add_u32_sdwa v126, s92, v112 dst_sel:DWORD dst_unused:UNUSED_PAD src0_sel:DWORD src1_sel:WORD_1\n\t"
    "v_cvt_pk_bf16_f32 v50, v116, v117\n\t"
    "v_cvt_pk_bf16_f32 v51, v118, v119\n\t"
    "v_and_b32 v119, s67, v57\n\t"
    "v_lshlrev_b32 v118, 16, v57\n\t"
    "v_and_b32 v117, s67, v56\n\t"
    "v_lshlrev_b32 v116, 16, v56\n\t"
    "v_add_u32_sdwa v112, s92, v112 dst_sel:DWORD dst_unused:UNUSED_PAD src0_sel:DWORD src1_sel:WORD_0\n\t"
    "s_waitcnt lgkmcnt(4)\n\t"
    "v_smfmac_f32_16x16x64_bf16 v[116:119], v[120:123], v[84:91], v124\n\t"
    "ds_read_b64 v[84:85], v112 offset:0\n\t"
    "ds_read_b64 v[86:87], v126 offset:0\n\t"
    "v_cvt_pk_bf16_f32 v80, v80, v81\n\t"
    "v_cvt_pk_bf16_f32 v81, v82, v83\n\t"
    "ds_write_b64 v125, v[80:81] offset:32872\n\t"
    "v_add_u32_sdwa v90, s92, v113 dst_sel:DWORD dst_unused:UNUSED_PAD src0_sel:DWORD src1_sel:WORD_1\n\t"
    "v_add_u32_sdwa v113, s92, v113 dst_sel:DWORD dst_unused:UNUSED_PAD src0_sel:DWORD src1_sel:WORD_0\n\t"
    "ds_read_b64 v[88:89], v113 offset:0\n\t"
    "ds_read_b64 v[90:91], v90 offset:0\n\t"
    "v_add_u32_sdwa v126, s92, v114 dst_sel:DWORD dst_unused:UNUSED_PAD src0_sel:DWORD src1_sel:WORD_1\n\t"
    "v_add_u32_sdwa v114, s92, v114 dst_sel:DWORD dst_unused:UNUSED_PAD src0_sel:DWORD src1_sel:WORD_0\n\t"
    "s_waitcnt vmcnt(2) lgkmcnt(5)\n\t"
    "v_smfmac_f32_16x16x64_bf16 v[116:119], v[120:123], v[92:99], v124\n\t"
    "ds_read_b64 v[92:93], v114 offset:0\n\t"
    "ds_read_b64 v[94:95], v126 offset:0\n\t"
    "v_add_u32_sdwa v98, s92, v115 dst_sel:DWORD dst_unused:UNUSED_PAD src0_sel:DWORD src1_sel:WORD_1\n\t"
    "v_add_u32_sdwa v115, s92, v115 dst_sel:DWORD dst_unused:UNUSED_PAD src0_sel:DWORD src1_sel:WORD_0\n\t"
    "ds_read_b64 v[96:97], v115 offset:0\n\t"
    "ds_read_b64 v[98:99], v98 offset:0\n\t"
    "buffer_load_dwordx4 v[112:115], v127, %12, s85 offen offset:1024 sc1\n\t"
    "s_sub_u32 s92, 64000, s92\n\t"
    "v_add_u32_sdwa v126, s92, v100 dst_sel:DWORD dst_unused:UNUSED_PAD src0_sel:DWORD src1_sel:WORD_1\n\t"
    "v_cvt_pk_bf16_f32 v56, v116, v117\n\t"
    "v_cvt_pk_bf16_f32 v57, v118, v119\n\t"
    "v_and_b32 v119, s67, v59\n\t"
    "v_lshlrev_b32 v118, 16, v59\n\t"
    "v_and_b32 v117, s67, v58\n\t"
    "v_lshlrev_b32 v116, 16, v58\n\t"
    "v_add_u32_sdwa v100, s92, v100 dst_sel:DWORD dst_unused:UNUSED_PAD src0_sel:DWORD src1_sel:WORD_0\n\t"
    "s_waitcnt lgkmcnt(0)\n\t"
    "s_barrier\n\t"
    "v_add_u32 v125, s93, v125\n\t"
    "s_sub_u32 s93, 0, s93\n\t"
    "v_smfmac_f32_16x16x64_bf16 v[116:119], v[120:123], v[84:91], v124\n\t"
    "ds_read_b64 v[84:85], v100 offset:0\n\t"
    "ds_read_b64 v[86:87], v126 offset:0\n\t"
    "v_add_u32_sdwa v90, s92, v101 dst_sel:DWORD dst_unused:UNUSED_PAD src0_sel:DWORD src1_sel:WORD_1\n\t"
    "v_add_u32_sdwa v101, s92, v101 dst_sel:DWORD dst_unused:UNUSED_PAD src0_sel:DWORD src1_sel:WORD_0\n\t"
    "ds_read_b64 v[88:89], v101 offset:0\n\t"
    "ds_read_b64 v[90:91], v90 offset:0\n\t"
    "v_add_u32_sdwa v126, s92, v102 dst_sel:DWORD dst_unused:UNUSED_PAD src0_sel:DWORD src1_sel:WORD_1\n\t"
    "v_add_u32_sdwa v102, s92, v102 dst_sel:DWORD dst_unused:UNUSED_PAD src0_sel:DWORD src1_sel:WORD_0\n\t"
    "s_cmp_lt_u32 %16, 4\n\t"
    "s_cbranch_scc1 L_tp63_%=\n\t"
    "s_setprio 1\n\t"
    "L_tp63_%=:\n\t"
    "s_waitcnt vmcnt(2)\n\t"
    "v_smfmac_f32_16x16x64_bf16 v[116:119], v[120:123], v[92:99], v124\n\t"
    "ds_read_b64 v[92:93], v102 offset:0\n\t"
    "ds_read_b64 v[94:95], v126 offset:0\n\t"
    "v_add_u32_sdwa v98, s92, v103 dst_sel:DWORD dst_unused:UNUSED_PAD src0_sel:DWORD src1_sel:WORD_1\n\t"
    "v_add_u32_sdwa v103, s92, v103 dst_sel:DWORD dst_unused:UNUSED_PAD src0_sel:DWORD src1_sel:WORD_0\n\t"
    "ds_read_b64 v[96:97], v103 offset:0\n\t"
    "ds_read_b64 v[98:99], v98 offset:0\n\t"
    "s_add_u32 s86, s86, s95\n\t"
    "buffer_load_dwordx4 v[100:103], v127, %12, s86 offen offset:0 sc1\n\t"
    "v_add_u32_sdwa v126, s92, v104 dst_sel:DWORD dst_unused:UNUSED_PAD src0_sel:DWORD src1_sel:WORD_1\n\t"
    "v_cvt_pk_bf16_f32 v58, v116, v117\n\t"
    "v_cvt_pk_bf16_f32 v59, v118, v119\n\t"
    "v_and_b32 v119, s67, v5\n\t"
    "v_lshlrev_b32 v118, 16, v5\n\t"
    "v_and_b32 v117, s67, v4\n\t"
    "v_lshlrev_b32 v116, 16, v4\n\t"
    "v_add_u32_sdwa v104, s92, v104 dst_sel:DWORD dst_unused:UNUSED_PAD src0_sel:DWORD src1_sel:WORD_0\n\t"
    "v_add_u32 v79, s96, v127\n\t"
    "s_add_u32 s96, s96, 0x1000\n\t"
    "s_and_b32 s96, s96, 0x3000\n\t"
    "v_add_u32 v79, 128000, v79\n\t"
    "ds_read_b128 v[64:67], v79 offset:0\n\t"
    "ds_read_b128 v[68:71], v79 offset:1024\n\t"
    "ds_read_b128 v[72:75], v79 offset:2048\n\t"
    "ds_read_b128 v[76:79], v79 offset:3072\n\t"
    "s_cmp_eq_u32 s66, s48\n\t"
    "s_cbranch_scc1 L_pub_%=\n\t"
    "L_pubb_%=:\n\t"
    "s_sub_u32 s66, s66, 1\n\t"
    "s_cmp_lg_u32 s66, 0\n\t"
    "s_cbranch_scc1 L_loop_%=\n\t"
    "s_mov_b32 s92, 0xffff\n\t"
    "s_waitcnt lgkmcnt(8)\n\t"
    "v_smfmac_f32_16x16x64_bf16 v[116:119], v[120:123], v[84:91], v124\n\t"
    "ds_read_b64 v[84:85], v104 offset:0\n\t"
    "ds_read_b64 v[86:87], v126 offset:0\n\t"
    "v_lshrrev_b32 v90, 16, v105\n\t"
    "v_and_b32 v105, s92, v105\n\t"
    "ds_read_b64 v[88:89], v105 offset:0\n\t"
    "ds_read_b64 v[90:91], v90 offset:0\n\t"
    "v_lshrrev_b32 v126, 16, v106\n\t"
    "v_and_b32 v106, s92, v106\n\t"
    "s_waitcnt vmcnt(2) lgkmcnt(8)\n\t"
    "v_smfmac_f32_16x16x64_bf16 v[116:119], v[120:123], v[92:99], v124\n\t"
    "ds_read_b64 v[92:93], v106 offset:0\n\t"
    "s_waitcnt lgkmcnt(5)\n\t"
    "v_mfma_f32_16x16x32_bf16 v[80:83], v[64:67], v[164:167], 0\n\t"
    "ds_read_b64 v[94:95], v126 offset:0\n\t"
    "v_mfma_f32_16x16x32_bf16 v[80:83], v[68:71], v[168:171], v[80:83]\n\t"
    "v_lshrrev_b32 v98, 16, v107\n\t"
    "v_and_b32 v107, s92, v107\n\t"
    "ds_read_b64 v[96:97], v107 offset:0\n\t"
    "v_mfma_f32_16x16x32_bf16 v[80:83], v[72:75], v[220:223], v[80:83]\n\t"
    "ds_read_b64 v[98:99], v98 offset:0\n\t"
    "v_mfma_f32_16x16x32_bf16 v[80:83], v[76:79], v[224:227], v[80:83]\n\t"
    "v_lshrrev_b32 v126, 16, v108\n\t"
    "v_cvt_pk_bf16_f32 v4, v116, v117\n\t"
    "v_cvt_pk_bf16_f32 v5, v118, v119\n\t"
    "v_and_b32 v119, s67, v7\n\t"
    "v_lshlrev_b32 v118, 16, v7\n\t"
    "v_and_b32 v117, s67, v6\n\t"
    "v_lshlrev_b32 v116, 16, v6\n\t"
    "v_and_b32 v108, s92, v108\n\t"
    "s_waitcnt lgkmcnt(4)\n\t"
    "v_smfmac_f32_16x16x64_bf16 v[116:119], v[120:123], v[84:91], v124\n\t"
    "ds_read_b64 v[84:85], v108 offset:0\n\t"
    "v_mfma_f32_16x16x32_bf16 v[104:107], v[64:67], v[172:175], 0\n\t"
    "ds_read_b64 v[86:87], v126 offset:0\n\t"
    "v_mfma_f32_16x16x32_bf16 v[104:107], v[68:71], v[176:179], v[104:107]\n\t"
    "v_lshrrev_b32 v90, 16, v109\n\t"
    "v_and_b32 v109, s92, v109\n\t"
    "ds_read_b64 v[88:89], v109 offset:0\n\t"
    "v_mfma_f32_16x16x32_bf16 v[104:107], v[72:75], v[228:231], v[104:107]\n\t"
    "ds_read_b64 v[90:91], v90 offset:0\n\t"
    "v_mfma_f32_16x16x32_bf16 v[104:107], v[76:79], v[232:235], v[104:107]\n\t"
    "v_lshrrev_b32 v126, 16, v110\n\t"
    "v_cvt_pk_bf16_f32 v80, v80, v81\n\t"
    "v_cvt_pk_bf16_f32 v81, v82, v83\n\t"
    "ds_write_b64 v125, v[80:81] offset:0\n\t"
    "v_mfma_f32_16x16x32_bf16 v[80:83], v[64:67], v[180:183], 0\n\t"
    "v_and_b32 v110, s92, v110\n\t"
    "v_mfma_f32_16x16x32_bf16 v[80:83], v[68:71], v[184:187], v[80:83]\n\t"
    "v_mfma_f32_16x16x32_bf16 v[80:83], v[72:75], v[236:239], v[80:83]\n\t"
    "s_waitcnt vmcnt(1) lgkmcnt(5)\n\t"
    "v_smfmac_f32_16x16x64_bf16 v[116:119], v[120:123], v[92:99], v124\n\t"
    "ds_read_b64 v[92:93], v110 offset:0\n\t"
    "v_mfma_f32_16x16x32_bf16 v[80:83], v[76:79], v[240:243], v[80:83]\n\t"
    "ds_read_b64 v[94:95], v126 offset:0\n\t"
    "v_cvt_pk_bf16_f32 v104, v104, v105\n\t"
    "v_cvt_pk_bf16_f32 v105, v106, v107\n\t"
    "ds_write_b64 v125, v[104:105] offset:128\n\t"
    "v_mfma_f32_16x16x32_bf16 v[104:107], v[64:67], v[188:191], 0\n\t"
    "v_lshrrev_b32 v98, 16, v111\n\t"
    "v_and_b32 v111, s92, v111\n\t"
    "ds_read_b64 v[96:97], v111 offset:0\n\t"
    "v_mfma_f32_16x16x32_bf16 v[104:107], v[68:71], v[192:195], v[104:107]\n\t"
    "ds_read_b64 v[98:99], v98 offset:0\n\t"
    "v_mfma_f32_16x16x32_bf16 v[104:107], v[72:75], v[244:247], v[104:107]\n\t"
    "buffer_load_dwordx4 v[108:111], v127, %12, s86 offen offset:1024 sc1\n\t"
    "v_lshrrev_b32 v126, 16, v112\n\t"
    "v_cvt_pk_bf16_f32 v6, v116, v117\n\t"
    "v_cvt_pk_bf16_f32 v7, v118, v119\n\t"
    "v_and_b32 v119, s67, v13\n\t"
    "v_lshlrev_b32 v118, 16, v13\n\t"
    "v_and_b32 v117, s67, v12\n\t"
    "v_lshlrev_b32 v116, 16, v12\n\t"
    "v_and_b32 v112, s92, v112\n\t"
    "s_waitcnt lgkmcnt(6)\n\t"
    "v_smfmac_f32_16x16x64_bf16 v[116:119], v[120:123], v[84:91], v124\n\t"
    "ds_read_b64 v[84:85], v112 offset:0\n\t"
    "v_mfma_f32_16x16x32_bf16 v[104:107], v[76:79], v[248:251], v[104:107]\n\t"
    "ds_read_b64 v[86:87], v126 offset:0\n\t"
    "v_cvt_pk_bf16_f32 v80, v80, v81\n\t"
    "v_cvt_pk_bf16_f32 v81, v82, v83\n\t"
    "ds_write_b64 v125, v[80:81] offset:256\n\t"
    "v_mfma_f32_16x16x32_bf16 v[80:83], v[64:67], v[196:199], 0\n\t"
    "v_lshrrev_b32 v90, 16, v113\n\t"
    "v_and_b32 v113, s92, v113\n\t"
    "ds_read_b64 v[88:89], v113 offset:0\n\t"
    "v_mfma_f32_16x16x32_bf16 v[80:83], v[68:71], v[200:203], v[80:83]\n\t"
    "ds_read_b64 v[90:91], v90 offset:0\n\t"
    "v_mfma_f32_16x16x32_bf16 v[80:83], v[72:75], v[252:255], v[80:83]\n\t"
    "v_lshrrev_b32 v126, 16, v114\n\t"
    "v_mfma_f32_16x16x32_bf16 v[80:83], v[76:79], v[128:131], v[80:83]\n\t"
    "v_and_b32 v114, s92, v114\n\t"
    "v_cvt_pk_bf16_f32 v104, v104, v105\n\t"
    "v_cvt_pk_bf16_f32 v105, v106, v107\n\t"
    "ds_write_b64 v125, v[104:105] offset:384\n\t"
    "v_mfma_f32_16x16x32_bf16 v[104:107], v[64:67], v[204:207], 0\n\t"
    "s_waitcnt vmcnt(1) lgkmcnt(6)\n\t"
    "v_smfmac_f32_16x16x64_bf16 v[116:119], v[120:123], v[92:99], v124\n\t"
    "ds_read_b64 v[92:93], v114 offset:0\n\t"
    "v_mfma_f32_16x16x32_bf16 v[104:107], v[68:71], v[208:211], v[104:107]\n\t"
    "ds_read_b64 v[94:95], v126 offset:0\n\t"
    "v_mfma_f32_16x16x32_bf16 v[104:107], v[72:75], v[132:135], v[104:107]\n\t"
    "v_lshrrev_b32 v98, 16, v115\n\t"
    "v_and_b32 v115, s92, v115\n\t"
    "ds_read_b64 v[96:97], v115 offset:0\n\t"
    "v_mfma_f32_16x16x32_bf16 v[104:107], v[76:79], v[136:139], v[104:107]\n\t"
    "ds_read_b64 v[98:99], v98 offset:0\n\t"
    "v_cvt_pk_bf16_f32 v80, v80, v81\n\t"
    "v_cvt_pk_bf16_f32 v81, v82, v83\n\t"
    "ds_write_b64 v125, v[80:81] offset:512\n\t"
    "v_mfma_f32_16x16x32_bf16 v[80:83], v[64:67], v[156:159], 0\n\t"
    "s_add_u32 s87, s87, s95\n\t"
    "buffer_load_dwordx4 v[112:115], v127, %12, s87 offen offset:0 sc1\n\t"
    "v_lshrrev_b32 v126, 16, v100\n\t"
    "v_mfma_f32_16x16x32_bf16 v[80:83], v[68:71], v[160:163], v[80:83]\n\t"
    "v_cvt_pk_bf16_f32 v12, v116, v117\n\t"
    "v_cvt_pk_bf16_f32 v13, v118, v119\n\t"
    "v_and_b32 v119, s67, v15\n\t"
    "v_lshlrev_b32 v118, 16, v15\n\t"
    "v_and_b32 v117, s67, v14\n\t"
    "v_lshlrev_b32 v116, 16, v14\n\t"
    "v_and_b32 v100, s92, v100\n\t"
    "s_waitcnt lgkmcnt(6)\n\t"
    "v_smfmac_f32_16x16x64_bf16 v[116:119], v[120:123], v[84:91], v124\n\t"
    "ds_read_b64 v[84:85], v100 offset:0\n\t"
    "v_mfma_f32_16x16x32_bf16 v[80:83], v[72:75], v[140:143], v[80:83]\n\t"
    "ds_read_b64 v[86:87], v126 offset:0\n\t"
    "v_mfma_f32_16x16x32_bf16 v[80:83], v[76:79], v[144:147], v[80:83]\n\t"
    "v_lshrrev_b32 v90, 16, v101\n\t"
    "v_and_b32 v101, s92, v101\n\t"
    "ds_read_b64 v[88:89], v101 offset:0\n\t"
    "v_cvt_pk_bf16_f32 v104, v104, v105\n\t"
    "v_cvt_pk_bf16_f32 v105, v106, v107\n\t"
    "ds_write_b64 v125, v[104:105] offset:640\n\t"
    "v_mfma_f32_16x16x32_bf16 v[104:107], v[64:67], v[212:215], 0\n\t"
    "ds_read_b64 v[90:91], v90 offset:0\n\t"
    "v_mfma_f32_16x16x32_bf16 v[104:107], v[68:71], v[216:219], v[104:107]\n\t"
    "v_lshrrev_b32 v126, 16, v102\n\t"
    "v_mfma_f32_16x16x32_bf16 v[104:107], v[72:75], v[148:151], v[104:107]\n\t"
    "v_and_b32 v102, s92, v102\n\t"
    "v_mfma_f32_16x16x32_bf16 v[104:107], v[76:79], v[152:155], v[104:107]\n\t"
    "v_add_u32 v79, s96, v127\n\t"
    "s_add_u32 s96, s96, 0x1000\n\t"
    "s_and_b32 s96, s96, 0x3000\n\t"
    "v_add_u32 v79, 128000, v79\n\t"
    "ds_read_b128 v[64:67], v79 offset:0\n\t"
    "ds_read_b128 v[68:71], v79 offset:1024\n\t"
    "ds_read_b128 v[72:75], v79 offset:2048\n\t"
    "ds_read_b128 v[76:79], v79 offset:3072\n\t"
    "v_cvt_pk_bf16_f32 v80, v80, v81\n\t"
    "v_cvt_pk_bf16_f32 v81, v82, v83\n\t"
    "ds_write_b64 v125, v[80:81] offset:768\n\t"
    "s_waitcnt vmcnt(1) lgkmcnt(11)\n\t"
    "v_smfmac_f32_16x16x64_bf16 v[116:119], v[120:123], v[92:99], v124\n\t"
    "ds_read_b64 v[92:93], v102 offset:0\n\t"
    "ds_read_b64 v[94:95], v126 offset:0\n\t"
    "v_lshrrev_b32 v98, 16, v103\n\t"
    "v_and_b32 v103, s92, v103\n\t"
    "ds_read_b64 v[96:97], v103 offset:0\n\t"
    "ds_read_b64 v[98:99], v98 offset:0\n\t"
    "buffer_load_dwordx4 v[100:103], v127, %12, s87 offen offset:1024 sc1\n\t"
    "v_lshrrev_b32 v126, 16, v108\n\t"
    "v_cvt_pk_bf16_f32 v14, v116, v117\n\t"
    "v_cvt_pk_bf16_f32 v15, v118, v119\n\t"
    "v_and_b32 v119, s67, v21\n\t"
    "v_lshlrev_b32 v118, 16, v21\n\t"
    "v_and_b32 v117, s67, v20\n\t"
    "v_lshlrev_b32 v116, 16, v20\n\t"
    "v_and_b32 v108, s92, v108\n\t"
    "s_cmp_lt_u32 %16, 4\n\t"
    "s_cbranch_scc1 L_tp152_%=\n\t"
    "s_setprio 0\n\t"
    "L_tp152_%=:\n\t"
    "s_waitcnt lgkmcnt(9)\n\t"
    "v_smfmac_f32_16x16x64_bf16 v[116:119], v[120:123], v[84:91], v124\n\t"
    "ds_read_b64 v[84:85], v108 offset:0\n\t"
    "s_waitcnt lgkmcnt(6)\n\t"
    "v_mfma_f32_16x16x32_bf16 v[80:83], v[64:67], v[164:167], 0\n\t"
    "ds_read_b64 v[86:87], v126 offset:0\n\t"
    "v_mfma_f32_16x16x32_bf16 v[80:83], v[68:71], v[168:171], v[80:83]\n\t"
    "v_lshrrev_b32 v90, 16, v109\n\t"
    "v_and_b32 v109, s92, v109\n\t"
    "ds_read_b64 v[88:89], v109 offset:0\n\t"
    "v_mfma_f32_16x16x32_bf16 v[80:83], v[72:75], v[220:223], v[80:83]\n\t"
    "ds_read_b64 v[90:91], v90 offset:0\n\t"
    "v_mfma_f32_16x16x32_bf16 v[80:83], v[76:79], v[224:227], v[80:83]\n\t"
    "v_lshrrev_b32 v126, 16, v110\n\t"
    "v_cvt_pk_bf16_f32 v104, v104, v105\n\t"
    "v_cvt_pk_bf16_f32 v105, v106, v107\n\t"
    "ds_write_b64 v125, v[104:105] offset:872\n\t"
    "v_mfma_f32_16x16x32_bf16 v[104:107], v[64:67], v[172:175], 0\n\t"
    "v_and_b32 v110, s92, v110\n\t"
    "v_mfma_f32_16x16x32_bf16 v[104:107], v[68:71], v[176:179], v[104:107]\n\t"
    "s_waitcnt vmcnt(1) lgkmcnt(5)\n\t"
    "v_smfmac_f32_16x16x64_bf16 v[116:119], v[120:123], v[92:99], v124\n\t"
    "ds_read_b64 v[92:93], v110 offset:0\n\t"
    "v_mfma_f32_16x16x32_bf16 v[104:107], v[72:75], v[228:231], v[104:107]\n\t"
    "ds_read_b64 v[94:95], v126 offset:0\n\t"
    "v_mfma_f32_16x16x32_bf16 v[104:107], v[76:79], v[232:235], v[104:107]\n\t"
    "v_lshrrev_b32 v98, 16, v111\n\t"
    "v_and_b32 v111, s92, v111\n\t"
    "ds_read_b64 v[96:97], v111 offset:0\n\t"
    "v_cvt_pk_bf16_f32 v80, v80, v81\n\t"
    "v_cvt_pk_bf16_f32 v81, v82, v83\n\t"
    "ds_write_b64 v125, v[80:81] offset:32000\n\t"
    "v_mfma_f32_16x16x32_bf16 v[80:83], v[64:67], v[180:183], 0\n\t"
    "ds_read_b64 v[98:99], v98 offset:0\n\t"
    "v_mfma_f32_16x16x32_bf16 v[80:83], v[68:71], v[184:187], v[80:83]\n\t"
    "s_add_u32 s84, s84, 0x8000\n\t"
    "buffer_load_dwordx4 v[108:111], v127, %12, s84 offen offset:0 sc1\n\t"
    "v_lshrrev_b32 v126, 16, v112\n\t"
    "v_mfma_f32_16x16x32_bf16 v[80:83], v[72:75], v[236:239], v[80:83]\n\t"
    "v_cvt_pk_bf16_f32 v20, v116, v117\n\t"
    "v_cvt_pk_bf16_f32 v21, v118, v119\n\t"
    "v_and_b32 v119, s67, v23\n\t"
    "v_lshlrev_b32 v118, 16, v23\n\t"
    "v_and_b32 v117, s67, v22\n\t"
    "v_lshlrev_b32 v116, 16, v22\n\t"
    "v_and_b32 v112, s92, v112\n\t"
    "s_waitcnt lgkmcnt(6)\n\t"
    "v_smfmac_f32_16x16x64_bf16 v[116:119], v[120:123], v[84:91], v124\n\t"
    "ds_read_b64 v[84:85], v112 offset:0\n\t"
    "v_mfma_f32_16x16x32_bf16 v[80:83], v[76:79], v[240:243], v[80:83]\n\t"
    "ds_read_b64 v[86:87], v126 offset:0\n\t"
    "v_cvt_pk_bf16_f32 v104, v104, v105\n\t"
    "v_cvt_pk_bf16_f32 v105, v106, v107\n\t"
    "ds_write_b64 v125, v[104:105] offset:32128\n\t"
    "v_mfma_f32_16x16x32_bf16 v[104:107], v[64:67], v[188:191], 0\n\t"
    "v_lshrrev_b32 v90, 16, v113\n\t"
    "v_and_b32 v113, s92, v113\n\t"
    "ds_read_b64 v[88:89], v113 offset:0\n\t"
    "v_mfma_f32_16x16x32_bf16 v[104:107], v[68:71], v[192:195], v[104:107]\n\t"
    "ds_read_b64 v[90:91], v90 offset:0\n\t"
    "v_mfma_f32_16x16x32_bf16 v[104:107], v[72:75], v[244:247], v[104:107]\n\t"
    "v_lshrrev_b32 v126, 16, v114\n\t"
    "v_mfma_f32_16x16x32_bf16 v[104:107], v[76:79], v[248:251], v[104:107]\n\t"
    "v_and_b32 v114, s92, v114\n\t"
    "v_cvt_pk_bf16_f32 v80, v80, v81\n\t"
    "v_cvt_pk_bf16_f32 v81, v82, v83\n\t"
    "ds_write_b64 v125, v[80:81] offset:32256\n\t"
    "v_mfma_f32_16x16x32_bf16 v[80:83], v[64:67], v[196:199], 0\n\t"
    "s_waitcnt vmcnt(1) lgkmcnt(6)\n\t"
    "v_smfmac_f32_16x16x64_bf16 v[116:119], v[120:123], v[92:99], v124\n\t"
    "ds_read_b64 v[92:93], v114 offset:0\n\t"
    "v_mfma_f32_16x16x32_bf16 v[80:83], v[68:71], v[200:203], v[80:83]\n\t"
    "ds_read_b64 v[94:95], v126 offset:0\n\t"
    "v_mfma_f32_16x16x32_bf16 v[80:83], v[72:75], v[252:255], v[80:83]\n\t"
    "v_lshrrev_b32 v98, 16, v115\n\t"
    "v_and_b32 v115, s92, v115\n\t"
    "ds_read_b64 v[96:97], v115 offset:0\n\t"
    "v_mfma_f32_16x16x32_bf16 v[80:83], v[76:79], v[128:131], v[80:83]\n\t"
    "ds_read_b64 v[98:99], v98 offset:0\n\t"
    "v_cvt_pk_bf16_f32 v104, v104, v105\n\t"
    "v_cvt_pk_bf16_f32 v105, v106, v107\n\t"
    "ds_write_b64 v125, v[104:105] offset:32384\n\t"
    "v_mfma_f32_16x16x32_bf16 v[104:107], v[64:67], v[204:207], 0\n\t"
    "buffer_load_dwordx4 v[112:115], v127, %12, s84 offen offset:1024 sc1\n\t"
    "v_lshrrev_b32 v126, 16, v100\n\t"
    "v_cvt_pk_bf16_f32 v22, v116, v117\n\t"
    "v_cvt_pk_bf16_f32 v23, v118, v119\n\t"
    "v_and_b32 v119, s67, v29\n\t"
    "v_lshlrev_b32 v118, 16, v29\n\t"
    "v_and_b32 v117, s67, v28\n\t"
    "v_lshlrev_b32 v116, 16, v28\n\t"
    "v_and_b32 v100, s92, v100\n\t"
    "s_waitcnt lgkmcnt(6)\n\t"
    "v_smfmac_f32_16x16x64_bf16 v[116:119], v[120:123], v[84:91], v124\n\t"
    "ds_read_b64 v[84:85], v100 offset:0\n\t"
    "v_mfma_f32_16x16x32_bf16 v[104:107], v[68:71], v[208:211], v[104:107]\n\t"
    "ds_read_b64 v[86:87], v126 offset:0\n\t"
    "v_mfma_f32_16x16x32_bf16 v[104:107], v[72:75], v[132:135], v[104:107]\n\t"
    "v_lshrrev_b32 v90, 16, v101\n\t"
    "v_and_b32 v101, s92, v101\n\t"
    "ds_read_b64 v[88:89], v101 offset:0\n\t"
    "v_mfma_f32_16x16x32_bf16 v[104:107], v[76:79], v[136:139], v[104:107]\n\t"
    "ds_read_b64 v[90:91], v90 offset:0\n\t"
    "v_cvt_pk_bf16_f32 v80, v80, v81\n\t"
    "v_cvt_pk_bf16_f32 v81, v82, v83\n\t"
    "ds_write_b64 v125, v[80:81] offset:32512\n\t"
    "v_mfma_f32_16x16x32_bf16 v[80:83], v[64:67], v[156:159], 0\n\t"
    "v_lshrrev_b32 v126, 16, v102\n\t"
    "v_mfma_f32_16x16x32_bf16 v[80:83], v[68:71], v[160:163], v[80:83]\n\t"
    "v_and_b32 v102, s92, v102\n\t"
    "v_mfma_f32_16x16x32_bf16 v[80:83], v[72:75], v[140:143], v[80:83]\n\t"
    "v_mfma_f32_16x16x32_bf16 v[80:83], v[76:79], v[144:147], v[80:83]\n\t"
    "v_cvt_pk_bf16_f32 v104, v104, v105\n\t"
    "v_cvt_pk_bf16_f32 v105, v106, v107\n\t"
    "ds_write_b64 v125, v[104:105] offset:32640\n\t"
    "s_waitcnt vmcnt(1) lgkmcnt(7)\n\t"
    "v_smfmac_f32_16x16x64_bf16 v[116:119], v[120:123], v[92:99], v124\n\t"
    "ds_read_b64 v[92:93], v102 offset:0\n\t"
    "v_mfma_f32_16x16x32_bf16 v[104:107], v[64:67], v[212:215], 0\n\t"
    "ds_read_b64 v[94:95], v126 offset:0\n\t"
    "v_mfma_f32_16x16x32_bf16 v[104:107], v[68:71], v[216:219], v[104:107]\n\t"
    "v_lshrrev_b32 v98, 16, v103\n\t"
    "v_and_b32 v103, s92, v103\n\t"
    "ds_read_b64 v[96:97], v103 offset:0\n\t"
    "v_mfma_f32_16x16x32_bf16 v[104:107], v[72:75], v[148:151], v[104:107]\n\t"
    "ds_read_b64 v[98:99], v98 offset:0\n\t"
    "v_mfma_f32_16x16x32_bf16 v[104:107], v[76:79], v[152:155], v[104:107]\n\t"
    "s_add_u32 s85, s85, 0x8000\n\t"
    "buffer_load_dwordx4 v[100:103], v127, %12, s85 offen offset:0 sc1\n\t"
    "v_lshrrev_b32 v126, 16, v108\n\t"
    "v_cvt_pk_bf16_f32 v80, v80, v81\n\t"
    "v_cvt_pk_bf16_f32 v81, v82, v83\n\t"
    "ds_write_b64 v125, v[80:81] offset:32768\n\t"
    "s_nop 1\n\t"
    "v_cvt_pk_bf16_f32 v104, v104, v105\n\t"
    "v_cvt_pk_bf16_f32 v105, v106, v107\n\t"
    "ds_write_b64 v125, v[104:105] offset:32872\n\t"
    "v_cvt_pk_bf16_f32 v28, v116, v117\n\t"
    "v_cvt_pk_bf16_f32 v29, v118, v119\n\t"
    "v_and_b32 v119, s67, v31\n\t"
    "v_lshlrev_b32 v118, 16, v31\n\t"
    "v_and_b32 v117, s67, v30\n\t"
    "v_lshlrev_b32 v116, 16, v30\n\t"
    "v_and_b32 v108, s92, v108\n\t"
    "s_waitcnt lgkmcnt(0)\n\t"
    "s_barrier\n\t"
    "v_smfmac_f32_16x16x64_bf16 v[116:119], v[120:123], v[84:91], v124\n\t"
    "ds_read_b64 v[84:85], v108 offset:64000\n\t"
    "ds_read_b64 v[86:87], v126 offset:64000\n\t"
    "v_lshrrev_b32 v90, 16, v109\n\t"
    "v_and_b32 v109, s92, v109\n\t"
    "ds_read_b64 v[88:89], v109 offset:64000\n\t"
    "ds_read_b64 v[90:91], v90 offset:64000\n\t"
    "buffer_load_dwordx4 v[104:107], v127, %12, s85 offen offset:1024 sc1\n\t"
    "v_lshrrev_b32 v126, 16, v110\n\t"
    "v_and_b32 v110, s92, v110\n\t"
    "s_cmp_lt_u32 %16, 4\n\t"
    "s_cbranch_scc1 L_tp159_%=\n\t"
    "s_setprio 1\n\t"
    "L_tp159_%=:\n\t"
    "s_waitcnt vmcnt(2)\n\t"
    "v_smfmac_f32_16x16x64_bf16 v[116:119], v[120:123], v[92:99], v124\n\t"
    "ds_read_b64 v[92:93], v110 offset:64000\n\t"
    "ds_read_b64 v[94:95], v126 offset:64000\n\t"
    "v_lshrrev_b32 v98, 16, v111\n\t"
    "v_and_b32 v111, s92, v111\n\t"
    "ds_read_b64 v[96:97], v111 offset:64000\n\t"
    "ds_read_b64 v[98:99], v98 offset:64000\n\t"
    "s_add_u32 s86, s86, 0x8000\n\t"
    "buffer_load_dwordx4 v[108:111], v127, %12, s86 offen offset:0 sc1\n\t"
    "v_lshrrev_b32 v252, 16, v112\n\t"
    "v_and_b32 v112, s92, v112\n\t"
    "v_lshrrev_b32 v253, 16, v113\n\t"
    "v_cvt_pk_bf16_f32 v30, v116, v117\n\t"
    "v_cvt_pk_bf16_f32 v31, v118, v119\n\t"
    "v_and_b32 v119, s67, v5\n\t"
    "v_lshlrev_b32 v118, 16, v5\n\t"
    "v_and_b32 v117, s67, v4\n\t"
    "v_lshlrev_b32 v116, 16, v4\n\t"
    "v_and_b32 v113, s92, v113\n\t"
    "v_mov_b32 v82, 0xbdd2d3e8\n\t"
    "v_mov_b32 v83, 0xbdd2d3e8\n\t"
    "v_mov_b32 v81, 0xc0135761\n\t"
    "v_lshrrev_b32 v125, 2, v127\n\t"
    "v_mov_b32 v80, 0xc0135761\n\t"
    "s_waitcnt lgkmcnt(4)\n\t"
    "v_smfmac_f32_16x16x64_bf16 v[116:119], v[120:123], v[84:91], v124\n\t"
    "ds_read_b64 v[84:85], v112 offset:64000\n\t"
    "ds_read_b64 v[86:87], v252 offset:64000\n\t"
    "ds_read_b64 v[88:89], v113 offset:64000\n\t"
    "ds_read_b64 v[90:91], v253 offset:64000\n\t"
    "v_lshrrev_b32 v254, 16, v114\n\t"
    "v_and_b32 v114, s92, v114\n\t"
    "v_lshrrev_b32 v255, 16, v115\n\t"
    "v_and_b32 v115, s92, v115\n\t"
    "s_lshl_b32 s65, %16, 10\n\t"
    "s_add_u32 s65, s65, 31457280\n\t"
    "s_xor_b32 s61, %14, 8\n\t"
    "s_mul_i32 s61, s61, 69632\n\t"
    "s_add_u32 s61, s61, s65\n\t"
    "s_mul_i32 s94, %14, 69632\n\t"
    "s_add_u32 s94, s94, s65\n\t"
    "s_lshl_b32 s65, %16, 9\n\t"
    "s_sub_u32 s65, 0x10000, s65\n\t"
    "s_add_u32 s93, s94, s65\n\t"
    "s_add_u32 s96, s61, s65\n\t"
    "v_mov_b32 v165, 1\n\t"
    "s_or_b32 s97, %14, %16\n\t"
    "s_cbranch_scc0 L_part0_%=\n\t"
    "L_partb0_%=:\n\t"
    "s_waitcnt vmcnt(2) lgkmcnt(4)\n\t"
    "v_smfmac_f32_16x16x64_bf16 v[116:119], v[120:123], v[92:99], v124\n\t"
    "ds_read_b64 v[92:93], v114 offset:64000\n\t"
    "ds_read_b64 v[94:95], v254 offset:64000\n\t"
    "ds_read_b64 v[96:97], v115 offset:64000\n\t"
    "ds_read_b64 v[98:99], v255 offset:64000\n\t"
    "buffer_load_dwordx4 v[172:175], v127, %12, s86 offen offset:1024 sc1\n\t"
    "v_lshrrev_b32 v252, 16, v100\n\t"
    "v_and_b32 v100, s92, v100\n\t"
    "v_lshrrev_b32 v253, 16, v101\n\t"
    "v_cvt_pk_bf16_f32 v4, v116, v117\n\t"
    "v_cvt_pk_bf16_f32 v5, v118, v119\n\t"
    "v_and_b32 v119, s67, v7\n\t"
    "v_lshlrev_b32 v118, 16, v7\n\t"
    "v_and_b32 v117, s67, v6\n\t"
    "v_lshlrev_b32 v116, 16, v6\n\t"
    "v_and_b32 v101, s92, v101\n\t"
    "v_mov_b32 v156, v120\n\t"
    "v_mov_b32 v160, 0\n\t"
    "v_mov_b32 v161, v121\n\t"
    "v_mov_b32 v157, 0\n\t"
    "v_mov_b32 v158, v122\n\t"
    "v_mov_b32 v162, 0\n\t"
    "v_mov_b32 v163, v123\n\t"
    "v_mov_b32 v159, 0\n\t"
    "s_waitcnt lgkmcnt(4)\n\t"
    "v_smfmac_f32_16x16x64_bf16 v[116:119], v[120:123], v[84:91], v124\n\t"
    "ds_read_b64 v[84:85], v100 offset:64000\n\t"
    "ds_read_b64 v[86:87], v252 offset:64000\n\t"
    "ds_read_b64 v[88:89], v101 offset:64000\n\t"
    "ds_read_b64 v[90:91], v253 offset:64000\n\t"
    "s_add_u32 s87, s87, 0x8000\n\t"
    "buffer_load_dwordx4 v[176:179], v127, %12, s87 offen offset:0 sc1\n\t"
    "v_lshrrev_b32 v254, 16, v102\n\t"
    "v_and_b32 v102, s92, v102\n\t"
    "v_lshrrev_b32 v255, 16, v103\n\t"
    "v_and_b32 v103, s92, v103\n\t"
    "s_waitcnt vmcnt(3) lgkmcnt(4)\n\t"
    "v_smfmac_f32_16x16x64_bf16 v[116:119], v[120:123], v[92:99], v124\n\t"
    "ds_read_b64 v[92:93], v102 offset:64000\n\t"
    "ds_read_b64 v[94:95], v254 offset:64000\n\t"
    "ds_read_b64 v[96:97], v103 offset:64000\n\t"
    "ds_read_b64 v[98:99], v255 offset:64000\n\t"
    "buffer_load_dwordx4 v[180:183], v127, %12, s87 offen offset:1024 sc1\n\t"
    "v_lshrrev_b32 v252, 16, v104\n\t"
    "v_and_b32 v104, s92, v104\n\t"
    "v_lshrrev_b32 v253, 16, v105\n\t"
    "v_cvt_pk_bf16_f32 v6, v116, v117\n\t"
    "v_cvt_pk_bf16_f32 v7, v118, v119\n\t"
    "v_and_b32 v119, s67, v13\n\t"
    "v_lshlrev_b32 v118, 16, v13\n\t"
    "v_and_b32 v117, s67, v12\n\t"
    "v_lshlrev_b32 v116, 16, v12\n\t"
    "v_and_b32 v105, s92, v105\n\t"
    "buffer_store_dwordx4 v[4:7], v127, %12, s61 offen sc1\n\t"
    "s_waitcnt lgkmcnt(4)\n\t"
    "v_smfmac_f32_16x16x64_bf16 v[116:119], v[120:123], v[84:91], v124\n\t"
    "ds_read_b64 v[84:85], v104 offset:64000\n\t"
    "ds_read_b64 v[86:87], v252 offset:64000\n\t"
    "ds_read_b64 v[88:89], v105 offset:64000\n\t"
    "ds_read_b64 v[90:91], v253 offset:64000\n\t"
    "s_add_u32 s88, s88, s95\n\t"
    "buffer_load_dwordx4 v[184:187], v127, %12, s88 offen offset:0 sc1\n\t"
    "v_lshrrev_b32 v254, 16, v106\n\t"
    "v_and_b32 v106, s92, v106\n\t"
    "v_lshrrev_b32 v255, 16, v107\n\t"
    "v_and_b32 v107, s92, v107\n\t"
    "s_waitcnt vmcnt(5) lgkmcnt(4)\n\t"
    "v_smfmac_f32_16x16x64_bf16 v[116:119], v[120:123], v[92:99], v124\n\t"
    "ds_read_b64 v[92:93], v106 offset:64000\n\t"
    "ds_read_b64 v[94:95], v254 offset:64000\n\t"
    "ds_read_b64 v[96:97], v107 offset:64000\n\t"
    "ds_read_b64 v[98:99], v255 offset:64000\n\t"
    "s_add_u32 s88, s88, 0x8000\n\t"
    "buffer_load_dwordx4 v[188:191], v127, %12, s88 offen offset:0 sc1\n\t"
    "v_lshrrev_b32 v252, 16, v108\n\t"
    "v_and_b32 v108, s92, v108\n\t"
    "v_lshrrev_b32 v253, 16, v109\n\t"
    "v_cvt_pk_bf16_f32 v12, v116, v117\n\t"
    "v_cvt_pk_bf16_f32 v13, v118, v119\n\t"
    "v_and_b32 v119, s67, v15\n\t"
    "v_lshlrev_b32 v118, 16, v15\n\t"
    "v_and_b32 v117, s67, v14\n\t"
    "v_lshlrev_b32 v116, 16, v14\n\t"
    "v_and_b32 v109, s92, v109\n\t"
    "s_waitcnt lgkmcnt(4)\n\t"
    "v_smfmac_f32_16x16x64_bf16 v[116:119], v[120:123], v[84:91], v124\n\t"
    "ds_read_b64 v[84:85], v108 offset:64000\n\t"
    "ds_read_b64 v[86:87], v252 offset:64000\n\t"
    "ds_read_b64 v[88:89], v109 offset:64000\n\t"
    "ds_read_b64 v[90:91], v253 offset:64000\n\t"
    "s_sub_u32 s88, s88, 0x8000\n\t"
    "buffer_load_dwordx4 v[236:239], v127, %12, s88 offen offset:1024 sc1\n\t"
    "v_lshrrev_b32 v254, 16, v110\n\t"
    "v_and_b32 v110, s92, v110\n\t"
    "v_lshrrev_b32 v255, 16, v111\n\t"
    "v_and_b32 v111, s92, v111\n\t"
    "s_waitcnt vmcnt(6) lgkmcnt(4)\n\t"
    "v_smfmac_f32_16x16x64_bf16 v[116:119], v[120:123], v[92:99], v124\n\t"
    "ds_read_b64 v[92:93], v110 offset:64000\n\t"
    "ds_read_b64 v[94:95], v254 offset:64000\n\t"
    "ds_read_b64 v[96:97], v111 offset:64000\n\t"
    "ds_read_b64 v[98:99], v255 offset:64000\n\t"
    "s_add_u32 s88, s88, 0x8000\n\t"
    "buffer_load_dwordx4 v[192:195], v127, %12, s88 offen offset:1024 sc1\n\t"
    "v_lshrrev_b32 v252, 16, v172\n\t"
    "v_and_b32 v172, s92, v172\n\t"
    "v_lshrrev_b32 v253, 16, v173\n\t"
    "v_cvt_pk_bf16_f32 v14, v116, v117\n\t"
    "v_cvt_pk_bf16_f32 v15, v118, v119\n\t"
    "v_and_b32 v119, s67, v21\n\t"
    "v_lshlrev_b32 v118, 16, v21\n\t"
    "v_and_b32 v117, s67, v20\n\t"
    "v_lshlrev_b32 v116, 16, v20\n\t"
    "v_and_b32 v173, s92, v173\n\t"
    "s_waitcnt lgkmcnt(4)\n\t"
    "v_smfmac_f32_16x16x64_bf16 v[116:119], v[120:123], v[84:91], v124\n\t"
    "ds_read_b64 v[84:85], v172 offset:64000\n\t"
    "ds_read_b64 v[86:87], v252 offset:64000\n\t"
    "ds_read_b64 v[88:89], v173 offset:64000\n\t"
    "ds_read_b64 v[90:91], v253 offset:64000\n\t"
    "v_lshrrev_b32 v254, 16, v174\n\t"
    "v_and_b32 v174, s92, v174\n\t"
    "v_lshrrev_b32 v255, 16, v175\n\t"
    "v_and_b32 v175, s92, v175\n\t"
    "s_cmp_lg_u32 s97, 0\n\t"
    "s_cbranch_scc0 L_part1_%=\n\t"
    "L_partb1_%=:\n\t"
    "s_waitcnt vmcnt(6) lgkmcnt(4)\n\t"
    "v_smfmac_f32_16x16x64_bf16 v[116:119], v[120:123], v[92:99], v124\n\t"
    "ds_read_b64 v[92:93], v174 offset:64000\n\t"
    "ds_read_b64 v[94:95], v254 offset:64000\n\t"
    "ds_read_b64 v[96:97], v175 offset:64000\n\t"
    "ds_read_b64 v[98:99], v255 offset:64000\n\t"
    "s_add_u32 s89, s89, s95\n\t"
    "buffer_load_dwordx4 v[196:199], v127, %12, s89 offen offset:0 sc1\n\t"
    "v_lshrrev_b32 v252, 16, v176\n\t"
    "v_and_b32 v176, s92, v176\n\t"
    "v_lshrrev_b32 v253, 16, v177\n\t"
    "v_cvt_pk_bf16_f32 v20, v116, v117\n\t"
    "v_cvt_pk_bf16_f32 v21, v118, v119\n\t"
    "v_and_b32 v119, s67, v23\n\t"
    "v_lshlrev_b32 v118, 16, v23\n\t"
    "v_and_b32 v117, s67, v22\n\t"
    "v_lshlrev_b32 v116, 16, v22\n\t"
    "v_and_b32 v177, s92, v177\n\t"
    "s_waitcnt lgkmcnt(4)\n\t"
    "v_smfmac_f32_16x16x64_bf16 v[116:119], v[120:123], v[84:91], v124\n\t"
    "ds_read_b64 v[84:85], v176 offset:64000\n\t"
    "ds_read_b64 v[86:87], v252 offset:64000\n\t"
    "ds_read_b64 v[88:89], v177 offset:64000\n\t"
    "ds_read_b64 v[90:91], v253 offset:64000\n\t"
    "v_lshrrev_b32 v254, 16, v178\n\t"
    "v_and_b32 v178, s92, v178\n\t"
    "v_lshrrev_b32 v255, 16, v179\n\t"
    "v_and_b32 v179, s92, v179\n\t"
    "s_waitcnt vmcnt(6) lgkmcnt(4)\n\t"
    "v_smfmac_f32_16x16x64_bf16 v[116:119], v[120:123], v[92:99], v124\n\t"
    "ds_read_b64 v[92:93], v178 offset:64000\n\t"
    "ds_read_b64 v[94:95], v254 offset:64000\n\t"
    "ds_read_b64 v[96:97], v179 offset:64000\n\t"
    "ds_read_b64 v[98:99], v255 offset:64000\n\t"
    "s_add_u32 s89, s89, 0x8000\n\t"
    "buffer_load_dwordx4 v[200:203], v127, %12, s89 offen offset:0 sc1\n\t"
    "v_lshrrev_b32 v252, 16, v180\n\t"
    "v_and_b32 v180, s92, v180\n\t"
    "v_lshrrev_b32 v253, 16, v181\n\t"
    "v_cvt_pk_bf16_f32 v22, v116, v117\n\t"
    "v_cvt_pk_bf16_f32 v23, v118, v119\n\t"
    "v_and_b32 v119, s67, v29\n\t"
    "v_lshlrev_b32 v118, 16, v29\n\t"
    "v_and_b32 v117, s67, v28\n\t"
    "v_lshlrev_b32 v116, 16, v28\n\t"
    "v_and_b32 v181, s92, v181\n\t"
    "s_add_u32 s61, s61, 0x2000\n\t"
    "buffer_store_dwordx4 v[12:15], v127, %12, s61 offen sc1\n\t"
    "s_waitcnt lgkmcnt(4)\n\t"
    "v_smfmac_f32_16x16x64_bf16 v[116:119], v[120:123], v[84:91], v124\n\t"
    "ds_read_b64 v[84:85], v180 offset:64000\n\t"
    "ds_read_b64 v[86:87], v252 offset:64000\n\t"
    "ds_read_b64 v[88:89], v181 offset:64000\n\t"
    "ds_read_b64 v[90:91], v253 offset:64000\n\t"
    "v_lshrrev_b32 v254, 16, v182\n\t"
    "v_and_b32 v182, s92, v182\n\t"
    "v_lshrrev_b32 v255, 16, v183\n\t"
    "v_and_b32 v183, s92, v183\n\t"
    "s_waitcnt vmcnt(6) lgkmcnt(4)\n\t"
    "v_smfmac_f32_16x16x64_bf16 v[116:119], v[120:123], v[92:99], v124\n\t"
    "ds_read_b64 v[92:93], v182 offset:64000\n\t"
    "ds_read_b64 v[94:95], v254 offset:64000\n\t"
    "ds_read_b64 v[96:97], v183 offset:64000\n\t"
    "ds_read_b64 v[98:99], v255 offset:64000\n\t"
    "s_sub_u32 s89, s89, 0x8000\n\t"
    "buffer_load_dwordx4 v[204:207], v127, %12, s89 offen offset:1024 sc1\n\t"
    "v_lshrrev_b32 v252, 16, v184\n\t"
    "v_and_b32 v184, s92, v184\n\t"
    "v_lshrrev_b32 v253, 16, v185\n\t"
    "v_cvt_pk_bf16_f32 v28, v116, v117\n\t"
    "v_cvt_pk_bf16_f32 v29, v118, v119\n\t"
    "v_and_b32 v119, s67, v31\n\t"
    "v_lshlrev_b32 v118, 16, v31\n\t"
    "v_and_b32 v117, s67, v30\n\t"
    "v_lshlrev_b32 v116, 16, v30\n\t"
    "v_and_b32 v185, s92, v185\n\t"
    "s_waitcnt lgkmcnt(4)\n\t"
    "v_smfmac_f32_16x16x64_bf16 v[116:119], v[120:123], v[84:91], v124\n\t"
    "ds_read_b64 v[84:85], v184 offset:0\n\t"
    "ds_read_b64 v[86:87], v252 offset:0\n\t"
    "ds_read_b64 v[88:89], v185 offset:0\n\t"
    "ds_read_b64 v[90:91], v253 offset:0\n\t"
    "v_lshrrev_b32 v254, 16, v186\n\t"
    "v_and_b32 v186, s92, v186\n\t"
    "v_lshrrev_b32 v255, 16, v187\n\t"
    "v_and_b32 v187, s92, v187\n\t"
    "s_waitcnt vmcnt(6) lgkmcnt(4)\n\t"
    "v_smfmac_f32_16x16x64_bf16 v[116:119], v[120:123], v[92:99], v124\n\t"
    "ds_read_b64 v[92:93], v186 offset:0\n\t"
    "ds_read_b64 v[94:95], v254 offset:0\n\t"
    "ds_read_b64 v[96:97], v187 offset:0\n\t"
    "ds_read_b64 v[98:99], v255 offset:0\n\t"
    "s_add_u32 s89, s89, 0x8000\n\t"
    "buffer_load_dwordx4 v[144:147], v127, %12, s89 offen offset:1024 sc1\n\t"
    "v_lshrrev_b32 v252, 16, v188\n\t"
    "v_and_b32 v188, s92, v188\n\t"
    "v_lshrrev_b32 v253, 16, v189\n\t"
    "v_cvt_pk_bf16_f32 v30, v116, v117\n\t"
    "v_cvt_pk_bf16_f32 v31, v118, v119\n\t"
    "v_and_b32 v119, s67, v37\n\t"
    "v_lshlrev_b32 v118, 16, v37\n\t"
    "v_and_b32 v117, s67, v36\n\t"
    "v_lshlrev_b32 v116, 16, v36\n\t"
    "v_and_b32 v189, s92, v189\n\t"
    "s_waitcnt lgkmcnt(4)\n\t"
    "v_smfmac_f32_16x16x64_bf16 v[116:119], v[120:123], v[84:91], v124\n\t"
    "ds_read_b64 v[84:85], v188 offset:64000\n\t"
    "ds_read_b64 v[86:87], v252 offset:64000\n\t"
    "ds_read_b64 v[88:89], v189 offset:64000\n\t"
    "ds_read_b64 v[90:91], v253 offset:64000\n\t"
    "v_lshrrev_b32 v254, 16, v190\n\t"
    "v_and_b32 v190, s92, v190\n\t"
    "v_lshrrev_b32 v255, 16, v191\n\t"
    "v_and_b32 v191, s92, v191\n\t"
    "s_waitcnt vmcnt(6) lgkmcnt(4)\n\t"
    "v_smfmac_f32_16x16x64_bf16 v[116:119], v[120:123], v[92:99], v124\n\t"
    "ds_read_b64 v[92:93], v190 offset:64000\n\t"
    "ds_read_b64 v[94:95], v254 offset:64000\n\t"
    "ds_read_b64 v[96:97], v191 offset:64000\n\t"
    "ds_read_b64 v[98:99], v255 offset:64000\n\t"
    "s_add_u32 s90, s90, s95\n\t"
    "buffer_load_dwordx4 v[148:151], v127, %12, s90 offen offset:0 sc1\n\t"
    "v_lshrrev_b32 v252, 16, v236\n\t"
    "v_and_b32 v236, s92, v236\n\t"
    "v_lshrrev_b32 v253, 16, v237\n\t"
    "v_and_b32 v237, s92, v237\n\t"
    "s_waitcnt lgkmcnt(4)\n\t"
    "v_smfmac_f32_16x16x64_bf16 v[116:119], v[120:123], v[84:91], v124\n\t"
    "ds_read_b64 v[84:85], v236 offset:0\n\t"
    "ds_read_b64 v[86:87], v252 offset:0\n\t"
    "ds_read_b64 v[88:89], v237 offset:0\n\t"
    "ds_read_b64 v[90:91], v253 offset:0\n\t"
    "v_lshrrev_b32 v254, 16, v238\n\t"
    "v_and_b32 v238, s92, v238\n\t"
    "v_lshrrev_b32 v255, 16, v239\n\t"
    "v_and_b32 v239, s92, v239\n\t"
    "s_waitcnt vmcnt(6) lgkmcnt(4)\n\t"
    "v_smfmac_f32_16x16x64_bf16 v[116:119], v[120:123], v[92:99], v124\n\t"
    "ds_read_b64 v[92:93], v238 offset:0\n\t"
    "ds_read_b64 v[94:95], v254 offset:0\n\t"
    "ds_read_b64 v[96:97], v239 offset:0\n\t"
    "ds_read_b64 v[98:99], v255 offset:0\n\t"
    "s_add_u32 s90, s90, 0x8000\n\t"
    "buffer_load_dwordx4 v[152:155], v127, %12, s90 offen offset:0 sc1\n\t"
    "v_lshrrev_b32 v252, 16, v192\n\t"
    "v_and_b32 v192, s92, v192\n\t"
    "v_lshrrev_b32 v253, 16, v193\n\t"
    "v_cvt_pk_bf16_f32 v36, v116, v117\n\t"
    "v_cvt_pk_bf16_f32 v37, v118, v119\n\t"
    "v_and_b32 v119, s67, v39\n\t"
    "v_lshlrev_b32 v118, 16, v39\n\t"
    "v_and_b32 v117, s67, v38\n\t"
    "v_lshlrev_b32 v116, 16, v38\n\t"
    "v_and_b32 v193, s92, v193\n\t"
    "s_waitcnt vmcnt(4)\n\t"
    "s_mov_b64 exec, 0xffffffff\n\t"
    "buffer_store_dword v165, v125, %12, s96 offen offset:0 sc1\n\t"
    "s_mov_b64 exec, -1\n\t"
    "s_add_u32 s61, s61, 0x2000\n\t"
    "buffer_store_dwordx4 v[20:23], v127, %12, s61 offen sc1\n\t"
    "s_cmp_lt_u32 %16, 4\n\t"
    "s_cbranch_scc1 L_tp180_%=\n\t"
    "s_setprio 0\n\t"
    "L_tp180_%=:\n\t"
    "s_waitcnt lgkmcnt(4)\n\t"
    "v_smfmac_f32_16x16x64_bf16 v[116:119], v[120:123], v[84:91], v124\n\t"
    "ds_read_b64 v[84:85], v192 offset:64000\n\t"
    "ds_read_b64 v[86:87], v252 offset:64000\n\t"
    "ds_read_b64 v[88:89], v193 offset:64000\n\t"
    "ds_read_b64 v[90:91], v253 offset:64000\n\t"
    "v_lshrrev_b32 v254, 16, v194\n\t"
    "v_and_b32 v194, s92, v194\n\t"
    "v_lshrrev_b32 v255, 16, v195\n\t"
    "v_and_b32 v195, s92, v195\n\t"
    "s_waitcnt lgkmcnt(4)\n\t"
    "v_smfmac_f32_16x16x64_bf16 v[116:119], v[120:123], v[92:99], v124\n\t"
    "ds_read_b64 v[92:93], v194 offset:64000\n\t"
    "ds_read_b64 v[94:95], v254 offset:64000\n\t"
    "ds_read_b64 v[96:97], v195 offset:64000\n\t"
    "ds_read_b64 v[98:99], v255 offset:64000\n\t"
    "s_sub_u32 s90, s90, 0x8000\n\t"
    "buffer_load_dwordx4 v[240:243], v127, %12, s90 offen offset:1024 sc1\n\t"
    "v_lshrrev_b32 v252, 16, v196\n\t"
    "v_and_b32 v196, s92, v196\n\t"
    "v_lshrrev_b32 v253, 16, v197\n\t"
    "v_and_b32 v197, s92, v197\n\t"
    "s_waitcnt lgkmcnt(4)\n\t"
    "v_smfmac_f32_16x16x64_bf16 v[116:119], v[120:123], v[84:91], v124\n\t"
    "ds_read_b64 v[84:85], v196 offset:0\n\t"
    "ds_read_b64 v[86:87], v252 offset:0\n\t"
    "ds_read_b64 v[88:89], v197 offset:0\n\t"
    "ds_read_b64 v[90:91], v253 offset:0\n\t"
    "v_lshrrev_b32 v254, 16, v198\n\t"
    "v_and_b32 v198, s92, v198\n\t"
    "v_lshrrev_b32 v255, 16, v199\n\t"
    "v_and_b32 v199, s92, v199\n\t"
    "s_waitcnt lgkmcnt(4)\n\t"
    "v_smfmac_f32_16x16x64_bf16 v[116:119], v[120:123], v[92:99], v124\n\t"
    "ds_read_b64 v[92:93], v198 offset:0\n\t"
    "ds_read_b64 v[94:95], v254 offset:0\n\t"
    "ds_read_b64 v[96:97], v199 offset:0\n\t"
    "ds_read_b64 v[98:99], v255 offset:0\n\t"
    "s_add_u32 s90, s90, 0x8000\n\t"
    "buffer_load_dwordx4 v[208:211], v127, %12, s90 offen offset:1024 sc1\n\t"
    "v_lshrrev_b32 v252, 16, v200\n\t"
    "v_and_b32 v200, s92, v200\n\t"
    "v_lshrrev_b32 v253, 16, v201\n\t"
    "v_cvt_pk_bf16_f32 v38, v116, v117\n\t"
    "v_cvt_pk_bf16_f32 v39, v118, v119\n\t"
    "v_and_b32 v119, s67, v45\n\t"
    "v_lshlrev_b32 v118, 16, v45\n\t"
    "v_and_b32 v117, s67, v44\n\t"
    "v_lshlrev_b32 v116, 16, v44\n\t"
    "v_and_b32 v201, s92, v201\n\t"
    "s_waitcnt lgkmcnt(4)\n\t"
    "v_smfmac_f32_16x16x64_bf16 v[116:119], v[120:123], v[84:91], v124\n\t"
    "ds_read_b64 v[84:85], v200 offset:64000\n\t"
    "ds_read_b64 v[86:87], v252 offset:64000\n\t"
    "ds_read_b64 v[88:89], v201 offset:64000\n\t"
    "ds_read_b64 v[90:91], v253 offset:64000\n\t"
    "v_lshrrev_b32 v254, 16, v202\n\t"
    "v_and_b32 v202, s92, v202\n\t"
    "v_lshrrev_b32 v255, 16, v203\n\t"
    "v_and_b32 v203, s92, v203\n\t"
    "s_waitcnt vmcnt(7) lgkmcnt(4)\n\t"
    "v_smfmac_f32_16x16x64_bf16 v[116:119], v[120:123], v[92:99], v124\n\t"
    "ds_read_b64 v[92:93], v202 offset:64000\n\t"
    "ds_read_b64 v[94:95], v254 offset:64000\n\t"
    "ds_read_b64 v[96:97], v203 offset:64000\n\t"
    "ds_read_b64 v[98:99], v255 offset:64000\n\t"
    "s_add_u32 s91, s91, s95\n\t"
    "buffer_load_dwordx4 v[244:247], v127, %12, s91 offen offset:0 sc1\n\t"
    "v_lshrrev_b32 v252, 16, v204\n\t"
    "v_and_b32 v204, s92, v204\n\t"
    "v_lshrrev_b32 v253, 16, v205\n\t"
    "v_and_b32 v205, s92, v205\n\t"
    "s_waitcnt lgkmcnt(4)\n\t"
    "v_smfmac_f32_16x16x64_bf16 v[116:119], v[120:123], v[84:91], v124\n\t"
    "ds_read_b64 v[84:85], v204 offset:0\n\t"
    "ds_read_b64 v[86:87], v252 offset:0\n\t"
    "ds_read_b64 v[88:89], v205 offset:0\n\t"
    "ds_read_b64 v[90:91], v253 offset:0\n\t"
    "v_lshrrev_b32 v254, 16, v206\n\t"
    "v_and_b32 v206, s92, v206\n\t"
    "v_lshrrev_b32 v255, 16, v207\n\t"
    "v_and_b32 v207, s92, v207\n\t"
    "s_waitcnt vmcnt(7) lgkmcnt(4)\n\t"
    "v_smfmac_f32_16x16x64_bf16 v[116:119], v[120:123], v[92:99], v124\n\t"
    "ds_read_b64 v[92:93], v206 offset:0\n\t"
    "ds_read_b64 v[94:95], v254 offset:0\n\t"
    "ds_read_b64 v[96:97], v207 offset:0\n\t"
    "ds_read_b64 v[98:99], v255 offset:0\n\t"
    "s_add_u32 s91, s91, 0x8000\n\t"
    "buffer_load_dwordx4 v[248:251], v127, %12, s91 offen offset:0 sc1\n\t"
    "v_lshrrev_b32 v252, 16, v144\n\t"
    "v_and_b32 v144, s92, v144\n\t"
    "v_lshrrev_b32 v253, 16, v145\n\t"
    "v_cvt_pk_bf16_f32 v44, v116, v117\n\t"
    "v_cvt_pk_bf16_f32 v45, v118, v119\n\t"
    "v_and_b32 v119, s67, v47\n\t"
    "v_lshlrev_b32 v118, 16, v47\n\t"
    "v_and_b32 v117, s67, v46\n\t"
    "v_lshlrev_b32 v116, 16, v46\n\t"
    "v_and_b32 v145, s92, v145\n\t"
    "s_add_u32 s61, s61, 0x2000\n\t"
    "buffer_store_dwordx4 v[28:31], v127, %12, s61 offen sc1\n\t"
    "s_waitcnt lgkmcnt(4)\n\t"
    "v_smfmac_f32_16x16x64_bf16 v[116:119], v[120:123], v[84:91], v124\n\t"
    "ds_read_b64 v[84:85], v144 offset:64000\n\t"
    "ds_read_b64 v[86:87], v252 offset:64000\n\t"
    "ds_read_b64 v[88:89], v145 offset:64000\n\t"
    "ds_read_b64 v[90:91], v253 offset:64000\n\t"
    "v_lshrrev_b32 v254, 16, v146\n\t"
    "v_and_b32 v146, s92, v146\n\t"
    "v_lshrrev_b32 v255, 16, v147\n\t"
    "v_and_b32 v147, s92, v147\n\t"
    "s_waitcnt vmcnt(8) lgkmcnt(4)\n\t"
    "v_smfmac_f32_16x16x64_bf16 v[116:119], v[120:123], v[92:99], v124\n\t"
    "ds_read_b64 v[92:93], v146 offset:64000\n\t"
    "ds_read_b64 v[94:95], v254 offset:64000\n\t"
    "ds_read_b64 v[96:97], v147 offset:64000\n\t"
    "ds_read_b64 v[98:99], v255 offset:64000\n\t"
    "s_sub_u32 s91, s91, 0x8000\n\t"
    "buffer_load_dwordx4 v[212:215], v127, %12, s91 offen offset:1024 sc1\n\t"
    "v_lshrrev_b32 v252, 16, v148\n\t"
    "v_and_b32 v148, s92, v148\n\t"
    "v_lshrrev_b32 v253, 16, v149\n\t"
    "v_and_b32 v149, s92, v149\n\t"
    "s_waitcnt lgkmcnt(4)\n\t"
    "v_smfmac_f32_16x16x64_bf16 v[116:119], v[120:123], v[84:91], v124\n\t"
    "ds_read_b64 v[84:85], v148 offset:0\n\t"
    "ds_read_b64 v[86:87], v252 offset:0\n\t"
    "ds_read_b64 v[88:89], v149 offset:0\n\t"
    "ds_read_b64 v[90:91], v253 offset:0\n\t"
    "v_lshrrev_b32 v254, 16, v150\n\t"
    "v_and_b32 v150, s92, v150\n\t"
    "v_lshrrev_b32 v255, 16, v151\n\t"
    "v_and_b32 v151, s92, v151\n\t"
    "s_waitcnt vmcnt(8) lgkmcnt(4)\n\t"
    "v_smfmac_f32_16x16x64_bf16 v[116:119], v[120:123], v[92:99], v124\n\t"
    "ds_read_b64 v[92:93], v150 offset:0\n\t"
    "ds_read_b64 v[94:95], v254 offset:0\n\t"
    "ds_read_b64 v[96:97], v151 offset:0\n\t"
    "ds_read_b64 v[98:99], v255 offset:0\n\t"
    "s_add_u32 s91, s91, 0x8000\n\t"
    "buffer_load_dwordx4 v[216:219], v127, %12, s91 offen offset:1024 sc1\n\t"
    "v_lshrrev_b32 v252, 16, v152\n\t"
    "v_and_b32 v152, s92, v152\n\t"
    "v_lshrrev_b32 v253, 16, v153\n\t"
    "v_cvt_pk_bf16_f32 v46, v116, v117\n\t"
    "v_cvt_pk_bf16_f32 v47, v118, v119\n\t"
    "v_and_b32 v119, s67, v53\n\t"
    "v_lshlrev_b32 v118, 16, v53\n\t"
    "v_and_b32 v117, s67, v52\n\t"
    "v_lshlrev_b32 v116, 16, v52\n\t"
    "v_and_b32 v153, s92, v153\n\t"
    "s_waitcnt lgkmcnt(4)\n\t"
    "v_smfmac_f32_16x16x64_bf16 v[116:119], v[120:123], v[84:91], v124\n\t"
    "ds_read_b64 v[84:85], v152 offset:64000\n\t"
    "ds_read_b64 v[86:87], v252 offset:64000\n\t"
    "ds_read_b64 v[88:89], v153 offset:64000\n\t"
    "ds_read_b64 v[90:91], v253 offset:64000\n\t"
    "v_lshrrev_b32 v254, 16, v154\n\t"
    "v_and_b32 v154, s92, v154\n\t"
    "v_lshrrev_b32 v255, 16, v155\n\t"
    "v_and_b32 v155, s92, v155\n\t"
    "s_cmp_lt_u32 %16, 4\n\t"
    "s_cbranch_scc1 L_tp193_%=\n\t"
    "s_setprio 1\n\t"
    "L_tp193_%=:\n\t"
    "s_waitcnt vmcnt(6) lgkmcnt(4)\n\t"
    "v_smfmac_f32_16x16x64_bf16 v[116:119], v[120:123], v[92:99], v124\n\t"
    "ds_read_b64 v[92:93], v154 offset:64000\n\t"
    "ds_read_b64 v[94:95], v254 offset:64000\n\t"
    "ds_read_b64 v[96:97], v155 offset:64000\n\t"
    "ds_read_b64 v[98:99], v255 offset:64000\n\t"
    "s_sub_u32 s84, s84, s95\n\t"
    "buffer_load_dwordx4 v[220:223], v127, %12, s84 offen offset:0 sc1\n\t"
    "v_lshrrev_b32 v252, 16, v240\n\t"
    "v_and_b32 v240, s92, v240\n\t"
    "v_lshrrev_b32 v253, 16, v241\n\t"
    "v_and_b32 v241, s92, v241\n\t"
    "s_waitcnt lgkmcnt(4)\n\t"
    "v_smfmac_f32_16x16x64_bf16 v[116:119], v[120:123], v[84:91], v124\n\t"
    "ds_read_b64 v[84:85], v240 offset:0\n\t"
    "ds_read_b64 v[86:87], v252 offset:0\n\t"
    "ds_read_b64 v[88:89], v241 offset:0\n\t"
    "ds_read_b64 v[90:91], v253 offset:0\n\t"
    "v_lshrrev_b32 v254, 16, v242\n\t"
    "v_and_b32 v242, s92, v242\n\t"
    "v_lshrrev_b32 v255, 16, v243\n\t"
    "v_and_b32 v243, s92, v243\n\t"
    "s_waitcnt vmcnt(6) lgkmcnt(4)\n\t"
    "v_smfmac_f32_16x16x64_bf16 v[116:119], v[120:123], v[92:99], v124\n\t"
    "ds_read_b64 v[92:93], v242 offset:0\n\t"
    "ds_read_b64 v[94:95], v254 offset:0\n\t"
    "ds_read_b64 v[96:97], v243 offset:0\n\t"
    "ds_read_b64 v[98:99], v255 offset:0\n\t"
    "buffer_load_dwordx4 v[224:227], v127, %12, s84 offen offset:1024 sc1\n\t"
    "v_lshrrev_b32 v252, 16, v208\n\t"
    "v_and_b32 v208, s92, v208\n\t"
    "v_lshrrev_b32 v253, 16, v209\n\t"
    "v_cvt_pk_bf16_f32 v52, v116, v117\n\t"
    "v_cvt_pk_bf16_f32 v53, v118, v119\n\t"
    "v_and_b32 v119, s67, v55\n\t"
    "v_lshlrev_b32 v118, 16, v55\n\t"
    "v_and_b32 v117, s67, v54\n\t"
    "v_lshlrev_b32 v116, 16, v54\n\t"
    "v_and_b32 v209, s92, v209\n\t"
    "s_waitcnt vmcnt(4)\n\t"
    "s_mov_b64 exec, 0xffffffff\n\t"
    "buffer_store_dword v165, v125, %12, s96 offen offset:128 sc1\n\t"
    "s_mov_b64 exec, -1\n\t"
    "s_add_u32 s61, s61, 0x2000\n\t"
    "buffer_store_dwordx4 v[36:39], v127, %12, s61 offen sc1\n\t"
    "s_waitcnt lgkmcnt(4)\n\t"
    "v_smfmac_f32_16x16x64_bf16 v[116:119], v[120:123], v[84:91], v124\n\t"
    "ds_read_b64 v[84:85], v208 offset:64000\n\t"
    "ds_read_b64 v[86:87], v252 offset:64000\n\t"
    "ds_read_b64 v[88:89], v209 offset:64000\n\t"
    "ds_read_b64 v[90:91], v253 offset:64000\n\t"
    "v_lshrrev_b32 v254, 16, v210\n\t"
    "v_and_b32 v210, s92, v210\n\t"
    "v_lshrrev_b32 v255, 16, v211\n\t"
    "v_and_b32 v211, s92, v211\n\t"
    "s_waitcnt lgkmcnt(4)\n\t"
    "v_smfmac_f32_16x16x64_bf16 v[116:119], v[120:123], v[92:99], v124\n\t"
    "ds_read_b64 v[92:93], v210 offset:64000\n\t"
    "ds_read_b64 v[94:95], v254 offset:64000\n\t"
    "ds_read_b64 v[96:97], v211 offset:64000\n\t"
    "ds_read_b64 v[98:99], v255 offset:64000\n\t"
    "s_sub_u32 s85, s85, s95\n\t"
    "buffer_load_dwordx4 v[228:231], v127, %12, s85 offen offset:0 sc1\n\t"
    "v_lshrrev_b32 v252, 16, v244\n\t"
    "v_and_b32 v244, s92, v244\n\t"
    "v_lshrrev_b32 v253, 16, v245\n\t"
    "v_and_b32 v245, s92, v245\n\t"
    "s_waitcnt lgkmcnt(4)\n\t"
    "v_smfmac_f32_16x16x64_bf16 v[116:119], v[120:123], v[84:91], v124\n\t"
    "ds_read_b64 v[84:85], v244 offset:0\n\t"
    "ds_read_b64 v[86:87], v252 offset:0\n\t"
    "ds_read_b64 v[88:89], v245 offset:0\n\t"
    "ds_read_b64 v[90:91], v253 offset:0\n\t"
    "v_lshrrev_b32 v254, 16, v246\n\t"
    "v_and_b32 v246, s92, v246\n\t"
    "v_lshrrev_b32 v255, 16, v247\n\t"
    "v_and_b32 v247, s92, v247\n\t"
    "s_waitcnt lgkmcnt(4)\n\t"
    "v_smfmac_f32_16x16x64_bf16 v[116:119], v[120:123], v[92:99], v124\n\t"
    "ds_read_b64 v[92:93], v246 offset:0\n\t"
    "ds_read_b64 v[94:95], v254 offset:0\n\t"
    "ds_read_b64 v[96:97], v247 offset:0\n\t"
    "ds_read_b64 v[98:99], v255 offset:0\n\t"
    "buffer_load_dwordx4 v[232:235], v127, %12, s85 offen offset:1024 sc1\n\t"
    "v_lshrrev_b32 v252, 16, v248\n\t"
    "v_and_b32 v248, s92, v248\n\t"
    "v_lshrrev_b32 v253, 16, v249\n\t"
    "v_cvt_pk_bf16_f32 v54, v116, v117\n\t"
    "v_cvt_pk_bf16_f32 v55, v118, v119\n\t"
    "v_and_b32 v119, s67, v61\n\t"
    "v_lshlrev_b32 v118, 16, v61\n\t"
    "v_and_b32 v117, s67, v60\n\t"
    "v_lshlrev_b32 v116, 16, v60\n\t"
    "v_and_b32 v249, s92, v249\n\t"
    "s_waitcnt lgkmcnt(4)\n\t"
    "v_smfmac_f32_16x16x64_bf16 v[116:119], v[120:123], v[84:91], v124\n\t"
    "ds_read_b64 v[84:85], v248 offset:64000\n\t"
    "ds_read_b64 v[86:87], v252 offset:64000\n\t"
    "ds_read_b64 v[88:89], v249 offset:64000\n\t"
    "ds_read_b64 v[90:91], v253 offset:64000\n\t"
    "v_lshrrev_b32 v254, 16, v250\n\t"
    "v_and_b32 v250, s92, v250\n\t"
    "v_lshrrev_b32 v255, 16, v251\n\t"
    "v_and_b32 v251, s92, v251\n\t"
    "s_waitcnt vmcnt(7) lgkmcnt(4)\n\t"
    "v_smfmac_f32_16x16x64_bf16 v[116:119], v[120:123], v[92:99], v124\n\t"
    "ds_read_b64 v[92:93], v250 offset:64000\n\t"
    "ds_read_b64 v[94:95], v254 offset:64000\n\t"
    "ds_read_b64 v[96:97], v251 offset:64000\n\t"
    "ds_read_b64 v[98:99], v255 offset:64000\n\t"
    "s_sub_u32 s86, s86, s95\n\t"
    "buffer_load_dwordx4 v[168:171], v127, %12, s86 offen offset:0 sc1\n\t"
    "v_lshrrev_b32 v252, 16, v212\n\t"
    "v_and_b32 v212, s92, v212\n\t"
    "v_lshrrev_b32 v253, 16, v213\n\t"
    "v_and_b32 v213, s92, v213\n\t"
    "s_waitcnt lgkmcnt(4)\n\t"
    "v_smfmac_f32_16x16x64_bf16 v[116:119], v[120:123], v[84:91], v124\n\t"
    "ds_read_b64 v[84:85], v212 offset:0\n\t"
    "ds_read_b64 v[86:87], v252 offset:0\n\t"
    "ds_read_b64 v[88:89], v213 offset:0\n\t"
    "ds_read_b64 v[90:91], v253 offset:0\n\t"
    "v_lshrrev_b32 v254, 16, v214\n\t"
    "v_and_b32 v214, s92, v214\n\t"
    "v_lshrrev_b32 v255, 16, v215\n\t"
    "v_and_b32 v215, s92, v215\n\t"
    "s_waitcnt vmcnt(7) lgkmcnt(4)\n\t"
    "v_smfmac_f32_16x16x64_bf16 v[116:119], v[120:123], v[92:99], v124\n\t"
    "ds_read_b64 v[92:93], v214 offset:0\n\t"
    "ds_read_b64 v[94:95], v254 offset:0\n\t"
    "ds_read_b64 v[96:97], v215 offset:0\n\t"
    "ds_read_b64 v[98:99], v255 offset:0\n\t"
    "s_add_u32 s84, s84, 0x8000\n\t"
    "buffer_load_dwordx4 v[112:115], v127, %12, s84 offen offset:0 sc1\n\t"
    "v_lshrrev_b32 v252, 16, v216\n\t"
    "v_and_b32 v216, s92, v216\n\t"
    "v_lshrrev_b32 v253, 16, v217\n\t"
    "v_cvt_pk_bf16_f32 v60, v116, v117\n\t"
    "v_cvt_pk_bf16_f32 v61, v118, v119\n\t"
    "v_and_b32 v119, s67, v63\n\t"
    "v_lshlrev_b32 v118, 16, v63\n\t"
    "v_and_b32 v117, s67, v62\n\t"
    "v_lshlrev_b32 v116, 16, v62\n\t"
    "v_and_b32 v217, s92, v217\n\t"
    "s_add_u32 s61, s61, 0x2000\n\t"
    "buffer_store_dwordx4 v[44:47], v127, %12, s61 offen sc1\n\t"
    "s_waitcnt lgkmcnt(4)\n\t"
    "v_smfmac_f32_16x16x64_bf16 v[116:119], v[120:123], v[84:91], v124\n\t"
    "ds_read_b64 v[84:85], v216 offset:64000\n\t"
    "ds_read_b64 v[86:87], v252 offset:64000\n\t"
    "ds_read_b64 v[88:89], v217 offset:64000\n\t"
    "ds_read_b64 v[90:91], v253 offset:64000\n\t"
    "v_lshrrev_b32 v254, 16, v218\n\t"
    "v_and_b32 v218, s92, v218\n\t"
    "v_lshrrev_b32 v255, 16, v219\n\t"
    "v_and_b32 v219, s92, v219\n\t"
    "s_mov_b64 exec, 1\n\t"
    "buffer_load_dword v164, off, %12, s93 offset:0 sc1\n\t"
    "s_mov_b64 exec, -1\n\t"
    "s_waitcnt vmcnt(9) lgkmcnt(4)\n\t"
    "v_smfmac_f32_16x16x64_bf16 v[116:119], v[120:123], v[92:99], v124\n\t"
    "ds_read_b64 v[92:93], v218 offset:64000\n\t"
    "ds_read_b64 v[94:95], v254 offset:64000\n\t"
    "ds_read_b64 v[96:97], v219 offset:64000\n\t"
    "ds_read_b64 v[98:99], v255 offset:64000\n\t"
    "buffer_load_dwordx4 v[100:103], v127, %12, s86 offen offset:1024 sc1\n\t"
    "v_lshrrev_b32 v252, 16, v220\n\t"
    "v_and_b32 v220, s92, v220\n\t"
    "v_lshrrev_b32 v253, 16, v221\n\t"
    "v_and_b32 v221, s92, v221\n\t"
    "s_waitcnt lgkmcnt(4)\n\t"
    "v_smfmac_f32_16x16x64_bf16 v[116:119], v[120:123], v[84:91], v124\n\t"
    "ds_read_b64 v[84:85], v220 offset:0\n\t"
    "ds_read_b64 v[86:87], v252 offset:0\n\t"
    "ds_read_b64 v[88:89], v221 offset:0\n\t"
    "ds_read_b64 v[90:91], v253 offset:0\n\t"
    "v_lshrrev_b32 v254, 16, v222\n\t"
    "v_and_b32 v222, s92, v222\n\t"
    "v_lshrrev_b32 v255, 16, v223\n\t"
    "v_and_b32 v223, s92, v223\n\t"
    "s_waitcnt vmcnt(9) lgkmcnt(4)\n\t"
    "v_smfmac_f32_16x16x64_bf16 v[116:119], v[120:123], v[92:99], v124\n\t"
    "ds_read_b64 v[92:93], v222 offset:0\n\t"
    "ds_read_b64 v[94:95], v254 offset:0\n\t"
    "ds_read_b64 v[96:97], v223 offset:0\n\t"
    "ds_read_b64 v[98:99], v255 offset:0\n\t"
    "buffer_load_dwordx4 v[104:107], v127, %12, s84 offen offset:1024 sc1\n\t"
    "v_lshrrev_b32 v252, 16, v224\n\t"
    "v_and_b32 v224, s92, v224\n\t"
    "v_lshrrev_b32 v253, 16, v225\n\t"
    "v_cvt_pk_bf16_f32 v62, v116, v117\n\t"
    "v_cvt_pk_bf16_f32 v63, v118, v119\n\t"
    "v_mov_b64 v[172:173], s[70:71]\n\t"
    "v_mov_b64 v[174:175], s[72:73]\n\t"
    "v_and_b32 v225, s92, v225\n\t"
    "s_waitcnt lgkmcnt(4)\n\t"
    "v_smfmac_f32_16x16x64_bf16 v[172:175], v[120:123], v[84:91], v124\n\t"
    "ds_read_b64 v[84:85], v224 offset:0\n\t"
    "ds_read_b64 v[86:87], v252 offset:0\n\t"
    "ds_read_b64 v[88:89], v225 offset:0\n\t"
    "ds_read_b64 v[90:91], v253 offset:0\n\t"
    "v_lshrrev_b32 v254, 16, v226\n\t"
    "v_and_b32 v226, s92, v226\n\t"
    "v_lshrrev_b32 v255, 16, v227\n\t"
    "v_and_b32 v227, s92, v227\n\t"
    "s_waitcnt vmcnt(7) lgkmcnt(4)\n\t"
    "v_smfmac_f32_16x16x64_bf16 v[172:175], v[120:123], v[92:99], v124\n\t"
    "ds_read_b64 v[92:93], v226 offset:0\n\t"
    "ds_read_b64 v[94:95], v254 offset:0\n\t"
    "ds_read_b64 v[96:97], v227 offset:0\n\t"
    "ds_read_b64 v[98:99], v255 offset:0\n\t"
    "s_sub_u32 s87, s87, s95\n\t"
    "buffer_load_dwordx4 v[108:111], v127, %12, s87 offen offset:0 sc1\n\t"
    "v_lshrrev_b32 v252, 16, v228\n\t"
    "v_and_b32 v228, s92, v228\n\t"
    "v_lshrrev_b32 v253, 16, v229\n\t"
    "v_mov_b64 v[176:177], s[70:71]\n\t"
    "v_mov_b64 v[178:179], s[72:73]\n\t"
    "v_and_b32 v229, s92, v229\n\t"
    "s_waitcnt lgkmcnt(4)\n\t"
    "v_smfmac_f32_16x16x64_bf16 v[176:179], v[120:123], v[84:91], v124\n\t"
    "ds_read_b64 v[84:85], v228 offset:0\n\t"
    "ds_read_b64 v[86:87], v252 offset:0\n\t"
    "ds_read_b64 v[88:89], v229 offset:0\n\t"
    "ds_read_b64 v[90:91], v253 offset:0\n\t"
    "v_lshrrev_b32 v254, 16, v230\n\t"
    "v_and_b32 v230, s92, v230\n\t"
    "v_lshrrev_b32 v255, 16, v231\n\t"
    "v_and_b32 v231, s92, v231\n\t"
    "s_waitcnt vmcnt(3)\n\t"
    "v_readfirstlane_b32 s65, v164\n\t"
    "s_nop 0\n\t"
    "s_cmp_eq_u32 s65, 0\n\t"
    "s_cbranch_scc1 L_xw0_%=\n\t"
    "L_xwb0_%=:\n\t"
    "buffer_load_dwordx4 v[4:7], v127, %12, s94 offen sc1\n\t"
    "s_waitcnt lgkmcnt(4)\n\t"
    "v_smfmac_f32_16x16x64_bf16 v[176:179], v[120:123], v[92:99], v124\n\t"
    "ds_read_b64 v[92:93], v230 offset:0\n\t"
    "ds_read_b64 v[94:95], v254 offset:0\n\t"
    "ds_read_b64 v[96:97], v231 offset:0\n\t"
    "ds_read_b64 v[98:99], v255 offset:0\n\t"
    "s_add_u32 s85, s85, 0x8000\n\t"
    "buffer_load_dwordx4 v[200:203], v127, %12, s85 offen offset:0 sc1\n\t"
    "v_lshrrev_b32 v252, 16, v232\n\t"
    "v_and_b32 v232, s92, v232\n\t"
    "v_lshrrev_b32 v253, 16, v233\n\t"
    "v_mov_b64 v[180:181], s[70:71]\n\t"
    "v_mov_b64 v[182:183], s[72:73]\n\t"
    "v_and_b32 v233, s92, v233\n\t"
    "s_mov_b64 exec, 0xffffffff\n\t"
    "buffer_store_dword v165, v125, %12, s96 offen offset:256 sc1\n\t"
    "s_mov_b64 exec, -1\n\t"
    "s_add_u32 s61, s61, 0x2000\n\t"
    "buffer_store_dwordx4 v[52:55], v127, %12, s61 offen sc1\n\t"
    "s_waitcnt lgkmcnt(4)\n\t"
    "v_smfmac_f32_16x16x64_bf16 v[180:183], v[120:123], v[84:91], v124\n\t"
    "ds_read_b64 v[84:85], v232 offset:0\n\t"
    "ds_read_b64 v[86:87], v252 offset:0\n\t"
    "ds_read_b64 v[88:89], v233 offset:0\n\t"
    "ds_read_b64 v[90:91], v253 offset:0\n\t"
    "v_lshrrev_b32 v254, 16, v234\n\t"
    "v_and_b32 v234, s92, v234\n\t"
    "v_lshrrev_b32 v255, 16, v235\n\t"
    "v_and_b32 v235, s92, v235\n\t"
    "s_waitcnt lgkmcnt(4)\n\t"
    "v_smfmac_f32_16x16x64_bf16 v[180:183], v[120:123], v[92:99], v124\n\t"
    "ds_read_b64 v[92:93], v234 offset:0\n\t"
    "ds_read_b64 v[94:95], v254 offset:0\n\t"
    "ds_read_b64 v[96:97], v235 offset:0\n\t"
    "ds_read_b64 v[98:99], v255 offset:0\n\t"
    "buffer_load_dwordx4 v[204:207], v127, %12, s87 offen offset:1024 sc1\n\t"
    "v_lshrrev_b32 v252, 16, v168\n\t"
    "v_and_b32 v168, s92, v168\n\t"
    "v_lshrrev_b32 v253, 16, v169\n\t"
    "v_mov_b64 v[184:185], s[70:71]\n\t"
    "v_mov_b64 v[186:187], s[72:73]\n\t"
    "v_and_b32 v169, s92, v169\n\t"
    "s_cmp_lt_u32 %16, 4\n\t"
    "s_cbranch_scc1 L_tp214_%=\n\t"
    "s_setprio 0\n\t"
    "L_tp214_%=:\n\t"
    "s_waitcnt lgkmcnt(4)\n\t"
    "v_smfmac_f32_16x16x64_bf16 v[184:187], v[120:123], v[84:91], v124\n\t"
    "ds_read_b64 v[84:85], v168 offset:0\n\t"
    "ds_read_b64 v[86:87], v252 offset:0\n\t"
    "ds_read_b64 v[88:89], v169 offset:0\n\t"
    "ds_read_b64 v[90:91], v253 offset:0\n\t"
    "v_lshrrev_b32 v254, 16, v170\n\t"
    "v_and_b32 v170, s92, v170\n\t"
    "v_lshrrev_b32 v255, 16, v171\n\t"
    "v_and_b32 v171, s92, v171\n\t"
    "s_add_u32 s94, s94, 0x2000\n\t"
    "buffer_load_dwordx4 v[12:15], v127, %12, s94 offen sc1\n\t"
    "s_waitcnt lgkmcnt(4)\n\t"
    "v_smfmac_f32_16x16x64_bf16 v[184:187], v[120:123], v[92:99], v124\n\t"
    "ds_read_b64 v[92:93], v170 offset:0\n\t"
    "ds_read_b64 v[94:95], v254 offset:0\n\t"
    "ds_read_b64 v[96:97], v171 offset:0\n\t"
    "ds_read_b64 v[98:99], v255 offset:0\n\t"
    "buffer_load_dwordx4 v[144:147], v127, %12, s85 offen offset:1024 sc1\n\t"
    "v_lshrrev_b32 v252, 16, v112\n\t"
    "v_and_b32 v112, s92, v112\n\t"
    "v_lshrrev_b32 v253, 16, v113\n\t"
    "v_mov_b64 v[188:189], s[70:71]\n\t"
    "v_mov_b64 v[190:191], s[72:73]\n\t"
    "v_and_b32 v113, s92, v113\n\t"
    "s_waitcnt lgkmcnt(4)\n\t"
    "v_smfmac_f32_16x16x64_bf16 v[188:191], v[120:123], v[84:91], v124\n\t"
    "ds_read_b64 v[84:85], v112 offset:64000\n\t"
    "ds_read_b64 v[86:87], v252 offset:64000\n\t"
    "ds_read_b64 v[88:89], v113 offset:64000\n\t"
    "ds_read_b64 v[90:91], v253 offset:64000\n\t"
    "v_lshrrev_b32 v254, 16, v114\n\t"
    "v_and_b32 v114, s92, v114\n\t"
    "v_lshrrev_b32 v255, 16, v115\n\t"
    "v_and_b32 v115, s92, v115\n\t"
    "s_waitcnt vmcnt(9) lgkmcnt(4)\n\t"
    "v_smfmac_f32_16x16x64_bf16 v[188:191], v[120:123], v[92:99], v124\n\t"
    "ds_read_b64 v[92:93], v114 offset:64000\n\t"
    "ds_read_b64 v[94:95], v254 offset:64000\n\t"
    "ds_read_b64 v[96:97], v115 offset:64000\n\t"
    "ds_read_b64 v[98:99], v255 offset:64000\n\t"
    "s_sub_u32 s88, s88, s95\n\t"
    "buffer_load_dwordx4 v[148:151], v127, %12, s88 offen offset:0 sc1\n\t"
    "v_lshrrev_b32 v252, 16, v100\n\t"
    "v_and_b32 v100, s92, v100\n\t"
    "v_lshrrev_b32 v253, 16, v101\n\t"
    "v_and_b32 v101, s92, v101\n\t"
    "s_waitcnt lgkmcnt(4)\n\t"
    "v_smfmac_f32_16x16x64_bf16 v[172:175], v[120:123], v[84:91], v124\n\t"
    "ds_read_b64 v[84:85], v100 offset:0\n\t"
    "ds_read_b64 v[86:87], v252 offset:0\n\t"
    "ds_read_b64 v[88:89], v101 offset:0\n\t"
    "ds_read_b64 v[90:91], v253 offset:0\n\t"
    "v_lshrrev_b32 v254, 16, v102\n\t"
    "v_and_b32 v102, s92, v102\n\t"
    "v_lshrrev_b32 v255, 16, v103\n\t"
    "v_and_b32 v103, s92, v103\n\t"
    "s_waitcnt vmcnt(7)\n\t"
    "v_smfmac_f32_16x16x64_bf16 v[172:175], v[156:159], v[0:7], v124\n\t"
    "s_waitcnt lgkmcnt(4)\n\t"
    "v_smfmac_f32_16x16x64_bf16 v[172:175], v[120:123], v[92:99], v124\n\t"
    "ds_read_b64 v[92:93], v102 offset:0\n\t"
    "ds_read_b64 v[94:95], v254 offset:0\n\t"
    "ds_read_b64 v[96:97], v103 offset:0\n\t"
    "ds_read_b64 v[98:99], v255 offset:0\n\t"
    "s_add_u32 s86, s86, 0x8000\n\t"
    "buffer_load_dwordx4 v[152:155], v127, %12, s86 offen offset:0 sc1\n\t"
    "v_lshrrev_b32 v252, 16, v104\n\t"
    "v_and_b32 v104, s92, v104\n\t"
    "v_lshrrev_b32 v253, 16, v105\n\t"
    "v_mov_b64 v[192:193], s[70:71]\n\t"
    "v_mov_b64 v[194:195], s[72:73]\n\t"
    "v_and_b32 v105, s92, v105\n\t"
    "s_add_u32 s61, s61, 0x2000\n\t"
    "buffer_store_dwordx4 v[60:63], v127, %12, s61 offen sc1\n\t"
    "v_smfmac_f32_16x16x64_bf16 v[176:179], v[160:163], v[0:7], v124\n\t"
    "v_pk_mul_f32 v[64:65], v[172:173], v[172:173]\n\t"
    "v_pk_mul_f32 v[66:67], v[174:175], v[174:175]\n\t"
    "v_pk_fma_f32 v[64:65], v[64:65], v[82:83], v[80:81]\n\t"
    "v_pk_fma_f32 v[66:67], v[66:67], v[82:83], v[80:81]\n\t"
    "v_pk_mul_f32 v[64:65], v[64:65], v[172:173]\n\t"
    "s_waitcnt lgkmcnt(4)\n\t"
    "v_smfmac_f32_16x16x64_bf16 v[192:195], v[120:123], v[84:91], v124\n\t"
    "ds_read_b64 v[84:85], v104 offset:64000\n\t"
    "ds_read_b64 v[86:87], v252 offset:64000\n\t"
    "ds_read_b64 v[88:89], v105 offset:64000\n\t"
    "ds_read_b64 v[90:91], v253 offset:64000\n\t"
    "v_lshrrev_b32 v254, 16, v106\n\t"
    "v_and_b32 v106, s92, v106\n\t"
    "v_lshrrev_b32 v255, 16, v107\n\t"
    "v_and_b32 v107, s92, v107\n\t"
    "s_mov_b64 exec, 1\n\t"
    "buffer_load_dword v164, off, %12, s93 offset:128 sc1\n\t"
    "s_mov_b64 exec, -1\n\t"
    "v_pk_mul_f32 v[66:67], v[66:67], v[174:175]\n\t"
    "v_exp_f32 v68, v64\n\t"
    "v_exp_f32 v69, v65\n\t"
    "v_exp_f32 v70, v66\n\t"
    "v_exp_f32 v71, v67\n\t"
    "s_waitcnt lgkmcnt(4)\n\t"
    "v_smfmac_f32_16x16x64_bf16 v[192:195], v[120:123], v[92:99], v124\n\t"
    "ds_read_b64 v[92:93], v106 offset:64000\n\t"
    "ds_read_b64 v[94:95], v254 offset:64000\n\t"
    "ds_read_b64 v[96:97], v107 offset:64000\n\t"
    "ds_read_b64 v[98:99], v255 offset:64000\n\t"
    "buffer_load_dwordx4 v[244:247], v127, %12, s88 offen offset:1024 sc1\n\t"
    "v_lshrrev_b32 v252, 16, v108\n\t"
    "v_and_b32 v108, s92, v108\n\t"
    "v_lshrrev_b32 v253, 16, v109\n\t"
    "v_and_b32 v109, s92, v109\n\t"
    "v_pk_fma_f32 v[68:69], v[68:69], s[40:41], s[40:41]\n\t"
    "v_pk_fma_f32 v[70:71], v[70:71], s[42:43], s[42:43]\n\t"
    "v_rcp_f32 v68, v68\n\t"
    "v_rcp_f32 v69, v69\n\t"
    "v_rcp_f32 v70, v70\n\t"
    "s_waitcnt lgkmcnt(4)\n\t"
    "v_smfmac_f32_16x16x64_bf16 v[176:179], v[120:123], v[84:91], v124\n\t"
    "ds_read_b64 v[84:85], v108 offset:0\n\t"
    "ds_read_b64 v[86:87], v252 offset:0\n\t"
    "ds_read_b64 v[88:89], v109 offset:0\n\t"
    "ds_read_b64 v[90:91], v253 offset:0\n\t"
    "v_lshrrev_b32 v254, 16, v110\n\t"
    "v_and_b32 v110, s92, v110\n\t"
    "v_lshrrev_b32 v255, 16, v111\n\t"
    "v_and_b32 v111, s92, v111\n\t"
    "s_waitcnt vmcnt(6)\n\t"
    "v_smfmac_f32_16x16x64_bf16 v[180:183], v[156:159], v[8:15], v124\n\t"
    "v_rcp_f32 v71, v71\n\t"
    "v_pk_mul_f32 v[68:69], v[68:69], v[172:173]\n\t"
    "v_pk_fma_f32 v[68:69], v[70:71], v[174:175], v[68:69]\n\t"
    "v_add_f32 v236, v68, v69\n\t"
    "s_waitcnt lgkmcnt(4)\n\t"
    "v_smfmac_f32_16x16x64_bf16 v[176:179], v[120:123], v[92:99], v124\n\t"
    "ds_read_b64 v[92:93], v110 offset:0\n\t"
    "ds_read_b64 v[94:95], v254 offset:0\n\t"
    "ds_read_b64 v[96:97], v111 offset:0\n\t"
    "ds_read_b64 v[98:99], v255 offset:0\n\t"
    "buffer_load_dwordx4 v[248:251], v127, %12, s86 offen offset:1024 sc1\n\t"
    "v_lshrrev_b32 v252, 16, v200\n\t"
    "v_and_b32 v200, s92, v200\n\t"
    "v_lshrrev_b32 v253, 16, v201\n\t"
    "v_mov_b64 v[196:197], s[70:71]\n\t"
    "v_mov_b64 v[198:199], s[72:73]\n\t"
    "v_and_b32 v201, s92, v201\n\t"
    "v_smfmac_f32_16x16x64_bf16 v[184:187], v[160:163], v[8:15], v124\n\t"
    "v_pk_mul_f32 v[72:73], v[176:177], v[176:177]\n\t"
    "v_pk_mul_f32 v[74:75], v[178:179], v[178:179]\n\t"
    "v_pk_fma_f32 v[72:73], v[72:73], v[82:83], v[80:81]\n\t"
    "v_pk_fma_f32 v[74:75], v[74:75], v[82:83], v[80:81]\n\t"
    "v_pk_mul_f32 v[72:73], v[72:73], v[176:177]\n\t"
    "s_waitcnt lgkmcnt(4)\n\t"
    "v_smfmac_f32_16x16x64_bf16 v[196:199], v[120:123], v[84:91], v124\n\t"
    "ds_read_b64 v[84:85], v200 offset:64000\n\t"
    "ds_read_b64 v[86:87], v252 offset:64000\n\t"
    "ds_read_b64 v[88:89], v201 offset:64000\n\t"
    "ds_read_b64 v[90:91], v253 offset:64000\n\t"
    "v_lshrrev_b32 v254, 16, v202\n\t"
    "v_and_b32 v202, s92, v202\n\t"
    "v_lshrrev_b32 v255, 16, v203\n\t"
    "v_and_b32 v203, s92, v203\n\t"
    "v_pk_mul_f32 v[74:75], v[74:75], v[178:179]\n\t"
    "v_exp_f32 v76, v72\n\t"
    "v_exp_f32 v77, v73\n\t"
    "v_exp_f32 v78, v74\n\t"
    "v_exp_f32 v79, v75\n\t"
    "s_waitcnt lgkmcnt(4)\n\t"
    "v_smfmac_f32_16x16x64_bf16 v[196:199], v[120:123], v[92:99], v124\n\t"
    "ds_read_b64 v[92:93], v202 offset:64000\n\t"
    "ds_read_b64 v[94:95], v254 offset:64000\n\t"
    "ds_read_b64 v[96:97], v203 offset:64000\n\t"
    "ds_read_b64 v[98:99], v255 offset:64000\n\t"
    "s_sub_u32 s89, s89, s95\n\t"
    "buffer_load_dwordx4 v[212:215], v127, %12, s89 offen offset:0 sc1\n\t"
    "v_lshrrev_b32 v252, 16, v204\n\t"
    "v_and_b32 v204, s92, v204\n\t"
    "v_lshrrev_b32 v253, 16, v205\n\t"
    "v_and_b32 v205, s92, v205\n\t"
    "v_pk_fma_f32 v[76:77], v[76:77], s[40:41], s[40:41]\n\t"
    "v_pk_fma_f32 v[78:79], v[78:79], s[42:43], s[42:43]\n\t"
    "v_rcp_f32 v76, v76\n\t"
    "v_rcp_f32 v77, v77\n\t"
    "v_rcp_f32 v78, v78\n\t"
    "s_waitcnt lgkmcnt(4)\n\t"
    "v_smfmac_f32_16x16x64_bf16 v[180:183], v[120:123], v[84:91], v124\n\t"
    "ds_read_b64 v[84:85], v204 offset:0\n\t"
    "ds_read_b64 v[86:87], v252 offset:0\n\t"
    "ds_read_b64 v[88:89], v205 offset:0\n\t"
    "ds_read_b64 v[90:91], v253 offset:0\n\t"
    "v_lshrrev_b32 v254, 16, v206\n\t"
    "v_and_b32 v206, s92, v206\n\t"
    "v_lshrrev_b32 v255, 16, v207\n\t"
    "v_and_b32 v207, s92, v207\n\t"
    "s_waitcnt vmcnt(3)\n\t"
    "v_readfirstlane_b32 s65, v164\n\t"
    "s_nop 0\n\t"
    "s_cmp_eq_u32 s65, 0\n\t"
    "s_cbranch_scc1 L_xw1_%=\n\t"
    "L_xwb1_%=:\n\t"
    "s_add_u32 s94, s94, 0x2000\n\t"
    "buffer_load_dwordx4 v[20:23], v127, %12, s94 offen sc1\n\t"
    "v_rcp_f32 v79, v79\n\t"
    "v_pk_mul_f32 v[76:77], v[76:77], v[176:177]\n\t"
    "v_pk_fma_f32 v[76:77], v[78:79], v[178:179], v[76:77]\n\t"
    "v_add_f32 v237, v76, v77\n\t"
    "s_cmp_lt_u32 %16, 4\n\t"
    "s_cbranch_scc1 L_tp227_%=\n\t"
    "s_setprio 1\n\t"
    "L_tp227_%=:\n\t"
    "s_waitcnt lgkmcnt(4)\n\t"
    "v_smfmac_f32_16x16x64_bf16 v[180:183], v[120:123], v[92:99], v124\n\t"
    "ds_read_b64 v[92:93], v206 offset:0\n\t"
    "ds_read_b64 v[94:95], v254 offset:0\n\t"
    "ds_read_b64 v[96:97], v207 offset:0\n\t"
    "ds_read_b64 v[98:99], v255 offset:0\n\t"
    "s_add_u32 s87, s87, 0x8000\n\t"
    "buffer_load_dwordx4 v[216:219], v127, %12, s87 offen offset:0 sc1\n\t"
    "v_lshrrev_b32 v252, 16, v144\n\t"
    "v_and_b32 v144, s92, v144\n\t"
    "v_lshrrev_b32 v253, 16, v145\n\t"
    "v_mov_b64 v[200:201], s[70:71]\n\t"
    "v_mov_b64 v[202:203], s[72:73]\n\t"
    "v_and_b32 v145, s92, v145\n\t"
    "s_mov_b64 exec, 0xffffffff\n\t"
    "buffer_store_dword v165, v125, %12, s96 offen offset:384 sc1\n\t"
    "s_mov_b64 exec, -1\n\t"
    "v_pk_mul_f32 v[64:65], v[180:181], v[180:181]\n\t"
    "v_pk_mul_f32 v[66:67], v[182:183], v[182:183]\n\t"
    "v_pk_fma_f32 v[64:65], v[64:65], v[82:83], v[80:81]\n\t"
    "v_pk_fma_f32 v[66:67], v[66:67], v[82:83], v[80:81]\n\t"
    "v_pk_mul_f32 v[64:65], v[64:65], v[180:181]\n\t"
    "s_waitcnt lgkmcnt(4)\n\t"
    "v_smfmac_f32_16x16x64_bf16 v[200:203], v[120:123], v[84:91], v124\n\t"
    "ds_read_b64 v[84:85], v144 offset:64000\n\t"
    "ds_read_b64 v[86:87], v252 offset:64000\n\t"
    "ds_read_b64 v[88:89], v145 offset:64000\n\t"
    "ds_read_b64 v[90:91], v253 offset:64000\n\t"
    "v_lshrrev_b32 v254, 16, v146\n\t"
    "v_and_b32 v146, s92, v146\n\t"
    "v_lshrrev_b32 v255, 16, v147\n\t"
    "v_and_b32 v147, s92, v147\n\t"
    "v_pk_mul_f32 v[66:67], v[66:67], v[182:183]\n\t"
    "v_exp_f32 v68, v64\n\t"
    "v_exp_f32 v69, v65\n\t"
    "v_exp_f32 v70, v66\n\t"
    "v_exp_f32 v71, v67\n\t"
    "s_waitcnt lgkmcnt(4)\n\t"
    "v_smfmac_f32_16x16x64_bf16 v[200:203], v[120:123], v[92:99], v124\n\t"
    "ds_read_b64 v[92:93], v146 offset:64000\n\t"
    "ds_read_b64 v[94:95], v254 offset:64000\n\t"
    "ds_read_b64 v[96:97], v147 offset:64000\n\t"
    "ds_read_b64 v[98:99], v255 offset:64000\n\t"
    "buffer_load_dwordx4 v[220:223], v127, %12, s89 offen offset:1024 sc1\n\t"
    "v_lshrrev_b32 v252, 16, v148\n\t"
    "v_and_b32 v148, s92, v148\n\t"
    "v_lshrrev_b32 v253, 16, v149\n\t"
    "v_and_b32 v149, s92, v149\n\t"
    "v_pk_fma_f32 v[68:69], v[68:69], s[40:41], s[40:41]\n\t"
    "v_pk_fma_f32 v[70:71], v[70:71], s[42:43], s[42:43]\n\t"
    "v_rcp_f32 v68, v68\n\t"
    "v_rcp_f32 v69, v69\n\t"
    "v_rcp_f32 v70, v70\n\t"
    "s_waitcnt lgkmcnt(4)\n\t"
    "v_smfmac_f32_16x16x64_bf16 v[184:187], v[120:123], v[84:91], v124\n\t"
    "ds_read_b64 v[84:85], v148 offset:0\n\t"
    "ds_read_b64 v[86:87], v252 offset:0\n\t"
    "ds_read_b64 v[88:89], v149 offset:0\n\t"
    "ds_read_b64 v[90:91], v253 offset:0\n\t"
    "v_lshrrev_b32 v254, 16, v150\n\t"
    "v_and_b32 v150, s92, v150\n\t"
    "v_lshrrev_b32 v255, 16, v151\n\t"
    "v_and_b32 v151, s92, v151\n\t"
    "s_add_u32 s94, s94, 0x2000\n\t"
    "buffer_load_dwordx4 v[28:31], v127, %12, s94 offen sc1\n\t"
    "v_rcp_f32 v71, v71\n\t"
    "v_pk_mul_f32 v[68:69], v[68:69], v[180:181]\n\t"
    "v_pk_fma_f32 v[68:69], v[70:71], v[182:183], v[68:69]\n\t"
    "v_add_f32 v238, v68, v69\n\t"
    "s_waitcnt lgkmcnt(4)\n\t"
    "v_smfmac_f32_16x16x64_bf16 v[184:187], v[120:123], v[92:99], v124\n\t"
    "ds_read_b64 v[92:93], v150 offset:0\n\t"
    "ds_read_b64 v[94:95], v254 offset:0\n\t"
    "ds_read_b64 v[96:97], v151 offset:0\n\t"
    "ds_read_b64 v[98:99], v255 offset:0\n\t"
    "buffer_load_dwordx4 v[116:119], v127, %12, s87 offen offset:1024 sc1\n\t"
    "v_lshrrev_b32 v252, 16, v152\n\t"
    "v_and_b32 v152, s92, v152\n\t"
    "v_lshrrev_b32 v253, 16, v153\n\t"
    "v_mov_b64 v[204:205], s[70:71]\n\t"
    "v_mov_b64 v[206:207], s[72:73]\n\t"
    "v_and_b32 v153, s92, v153\n\t"
    "v_pk_mul_f32 v[72:73], v[184:185], v[184:185]\n\t"
    "v_pk_mul_f32 v[74:75], v[186:187], v[186:187]\n\t"
    "v_pk_fma_f32 v[72:73], v[72:73], v[82:83], v[80:81]\n\t"
    "v_pk_fma_f32 v[74:75], v[74:75], v[82:83], v[80:81]\n\t"
    "v_pk_mul_f32 v[72:73], v[72:73], v[184:185]\n\t"
    "s_waitcnt lgkmcnt(4)\n\t"
    "v_smfmac_f32_16x16x64_bf16 v[204:207], v[120:123], v[84:91], v124\n\t"
    "ds_read_b64 v[84:85], v152 offset:64000\n\t"
    "ds_read_b64 v[86:87], v252 offset:64000\n\t"
    "ds_read_b64 v[88:89], v153 offset:64000\n\t"
    "ds_read_b64 v[90:91], v253 offset:64000\n\t"
    "v_lshrrev_b32 v254, 16, v154\n\t"
    "v_and_b32 v154, s92, v154\n\t"
    "v_lshrrev_b32 v255, 16, v155\n\t"
    "v_and_b32 v155, s92, v155\n\t"
    "v_pk_mul_f32 v[74:75], v[74:75], v[186:187]\n\t"
    "v_exp_f32 v76, v72\n\t"
    "v_exp_f32 v77, v73\n\t"
    "v_exp_f32 v78, v74\n\t"
    "v_exp_f32 v79, v75\n\t"
    "s_waitcnt vmcnt(8) lgkmcnt(4)\n\t"
    "v_smfmac_f32_16x16x64_bf16 v[204:207], v[120:123], v[92:99], v124\n\t"
    "ds_read_b64 v[92:93], v154 offset:64000\n\t"
    "ds_read_b64 v[94:95], v254 offset:64000\n\t"
    "ds_read_b64 v[96:97], v155 offset:64000\n\t"
    "ds_read_b64 v[98:99], v255 offset:64000\n\t"
    "s_sub_u32 s90, s90, s95\n\t"
    "buffer_load_dwordx4 v[224:227], v127, %12, s90 offen offset:0 sc1\n\t"
    "v_lshrrev_b32 v252, 16, v244\n\t"
    "v_and_b32 v244, s92, v244\n\t"
    "v_lshrrev_b32 v253, 16, v245\n\t"
    "v_and_b32 v245, s92, v245\n\t"
    "v_pk_fma_f32 v[76:77], v[76:77], s[40:41], s[40:41]\n\t"
    "v_pk_fma_f32 v[78:79], v[78:79], s[42:43], s[42:43]\n\t"
    "v_rcp_f32 v76, v76\n\t"
    "v_rcp_f32 v77, v77\n\t"
    "v_rcp_f32 v78, v78\n\t"
    "s_waitcnt lgkmcnt(4)\n\t"
    "v_smfmac_f32_16x16x64_bf16 v[188:191], v[120:123], v[84:91], v124\n\t"
    "ds_read_b64 v[84:85], v244 offset:0\n\t"
    "ds_read_b64 v[86:87], v252 offset:0\n\t"
    "ds_read_b64 v[88:89], v245 offset:0\n\t"
    "ds_read_b64 v[90:91], v253 offset:0\n\t"
    "v_lshrrev_b32 v254, 16, v246\n\t"
    "v_and_b32 v246, s92, v246\n\t"
    "v_lshrrev_b32 v255, 16, v247\n\t"
    "v_and_b32 v247, s92, v247\n\t"
    "s_waitcnt vmcnt(6)\n\t"
    "v_smfmac_f32_16x16x64_bf16 v[188:191], v[156:159], v[16:23], v124\n\t"
    "v_rcp_f32 v79, v79\n\t"
    "v_pk_mul_f32 v[76:77], v[76:77], v[184:185]\n\t"
    "v_pk_fma_f32 v[76:77], v[78:79], v[186:187], v[76:77]\n\t"
    "v_add_f32 v239, v76, v77\n\t"
    "v_lshrrev_b32 v147, 4, v127\n\t"
    "v_add_u32 v148, 140288, v127\n\t"
    "s_lshl_b32 s65, %14, 12\n\t"
    "s_add_u32 s65, s65, 16777216\n\t"
    "buffer_load_dwordx4 v[0:3], v127, %12, s65 offen offset:0 sc1\n\t"
    "s_waitcnt lgkmcnt(4)\n\t"
    "v_smfmac_f32_16x16x64_bf16 v[188:191], v[120:123], v[92:99], v124\n\t"
    "ds_read_b64 v[92:93], v246 offset:0\n\t"
    "ds_read_b64 v[94:95], v254 offset:0\n\t"
    "ds_read_b64 v[96:97], v247 offset:0\n\t"
    "ds_read_b64 v[98:99], v255 offset:0\n\t"
    "s_add_u32 s88, s88, 0x8000\n\t"
    "buffer_load_dwordx4 v[228:231], v127, %12, s88 offen offset:0 sc1\n\t"
    "v_lshrrev_b32 v252, 16, v248\n\t"
    "v_and_b32 v248, s92, v248\n\t"
    "v_lshrrev_b32 v253, 16, v249\n\t"
    "v_mov_b64 v[208:209], s[70:71]\n\t"
    "v_mov_b64 v[210:211], s[72:73]\n\t"
    "v_and_b32 v249, s92, v249\n\t"
    "v_smfmac_f32_16x16x64_bf16 v[192:195], v[160:163], v[16:23], v124\n\t"
    "v_pk_mul_f32 v[64:65], v[188:189], v[188:189]\n\t"
    "v_pk_mul_f32 v[66:67], v[190:191], v[190:191]\n\t"
    "v_pk_fma_f32 v[64:65], v[64:65], v[82:83], v[80:81]\n\t"
    "v_pk_fma_f32 v[66:67], v[66:67], v[82:83], v[80:81]\n\t"
    "v_pk_mul_f32 v[64:65], v[64:65], v[188:189]\n\t"
    "s_lshl_b32 s65, %14, 12\n\t"
    "s_add_u32 s65, s65, 16777216\n\t"
    "buffer_load_dwordx4 v[4:7], v127, %12, s65 offen offset:1024 sc1\n\t"
    "s_waitcnt lgkmcnt(4)\n\t"
    "v_smfmac_f32_16x16x64_bf16 v[208:211], v[120:123], v[84:91], v124\n\t"
    "ds_read_b64 v[84:85], v248 offset:64000\n\t"
    "ds_read_b64 v[86:87], v252 offset:64000\n\t"
    "ds_read_b64 v[88:89], v249 offset:64000\n\t"
    "ds_read_b64 v[90:91], v253 offset:64000\n\t"
    "v_lshrrev_b32 v254, 16, v250\n\t"
    "v_and_b32 v250, s92, v250\n\t"
    "v_lshrrev_b32 v255, 16, v251\n\t"
    "v_and_b32 v251, s92, v251\n\t"
    "s_mov_b64 exec, 1\n\t"
    "buffer_load_dword v164, off, %12, s93 offset:256 sc1\n\t"
    "s_mov_b64 exec, -1\n\t"
    "v_pk_mul_f32 v[66:67], v[66:67], v[190:191]\n\t"
    "v_exp_f32 v68, v64\n\t"
    "v_exp_f32 v69, v65\n\t"
    "v_exp_f32 v70, v66\n\t"
    "v_exp_f32 v71, v67\n\t"
    "s_lshl_b32 s65, %14, 12\n\t"
    "s_add_u32 s65, s65, 16777216\n\t"
    "buffer_load_dwordx4 v[8:11], v127, %12, s65 offen offset:2048 sc1\n\t"
    "s_waitcnt lgkmcnt(4)\n\t"
    "v_smfmac_f32_16x16x64_bf16 v[208:211], v[120:123], v[92:99], v124\n\t"
    "ds_read_b64 v[92:93], v250 offset:64000\n\t"
    "ds_read_b64 v[94:95], v254 offset:64000\n\t"
    "ds_read_b64 v[96:97], v251 offset:64000\n\t"
    "ds_read_b64 v[98:99], v255 offset:64000\n\t"
    "buffer_load_dwordx4 v[232:235], v127, %12, s90 offen offset:1024 sc1\n\t"
    "v_lshrrev_b32 v252, 16, v212\n\t"
    "v_and_b32 v212, s92, v212\n\t"
    "v_lshrrev_b32 v253, 16, v213\n\t"
    "v_and_b32 v213, s92, v213\n\t"
    "v_pk_fma_f32 v[68:69], v[68:69], s[40:41], s[40:41]\n\t"
    "v_pk_fma_f32 v[70:71], v[70:71], s[42:43], s[42:43]\n\t"
    "v_rcp_f32 v68, v68\n\t"
    "v_rcp_f32 v69, v69\n\t"
    "v_rcp_f32 v70, v70\n\t"
    "s_lshl_b32 s65, %14, 12\n\t"
    "s_add_u32 s65, s65, 16777216\n\t"
    "buffer_load_dwordx4 v[12:15], v127, %12, s65 offen offset:3072 sc1\n\t"
    "s_waitcnt lgkmcnt(4)\n\t"
    "v_smfmac_f32_16x16x64_bf16 v[192:195], v[120:123], v[84:91], v124\n\t"
    "ds_read_b64 v[84:85], v212 offset:0\n\t"
    "ds_read_b64 v[86:87], v252 offset:0\n\t"
    "ds_read_b64 v[88:89], v213 offset:0\n\t"
    "ds_read_b64 v[90:91], v253 offset:0\n\t"
    "v_lshrrev_b32 v254, 16, v214\n\t"
    "v_and_b32 v214, s92, v214\n\t"
    "v_lshrrev_b32 v255, 16, v215\n\t"
    "v_and_b32 v215, s92, v215\n\t"
    "s_waitcnt vmcnt(9)\n\t"
    "v_smfmac_f32_16x16x64_bf16 v[196:199], v[156:159], v[24:31], v124\n\t"
    "v_rcp_f32 v71, v71\n\t"
    "v_pk_mul_f32 v[68:69], v[68:69], v[188:189]\n\t"
    "v_pk_fma_f32 v[68:69], v[70:71], v[190:191], v[68:69]\n\t"
    "v_add_f32 v240, v68, v69\n\t"
    "ds_read_b128 v[16:19], v148 offset:4096\n\t"
    "s_waitcnt lgkmcnt(5)\n\t"
    "v_smfmac_f32_16x16x64_bf16 v[192:195], v[120:123], v[92:99], v124\n\t"
    "ds_read_b64 v[92:93], v214 offset:0\n\t"
    "ds_read_b64 v[94:95], v254 offset:0\n\t"
    "ds_read_b64 v[96:97], v215 offset:0\n\t"
    "ds_read_b64 v[98:99], v255 offset:0\n\t"
    "buffer_load_dwordx4 v[168:171], v127, %12, s88 offen offset:1024 sc1\n\t"
    "v_lshrrev_b32 v252, 16, v216\n\t"
    "v_and_b32 v216, s92, v216\n\t"
    "v_lshrrev_b32 v253, 16, v217\n\t"
    "v_mov_b64 v[172:173], s[70:71]\n\t"
    "v_mov_b64 v[174:175], s[72:73]\n\t"
    "v_and_b32 v217, s92, v217\n\t"
    "v_smfmac_f32_16x16x64_bf16 v[200:203], v[160:163], v[24:31], v124\n\t"
    "v_pk_mul_f32 v[72:73], v[192:193], v[192:193]\n\t"
    "v_pk_mul_f32 v[74:75], v[194:195], v[194:195]\n\t"
    "v_pk_fma_f32 v[72:73], v[72:73], v[82:83], v[80:81]\n\t"
    "v_pk_fma_f32 v[74:75], v[74:75], v[82:83], v[80:81]\n\t"
    "v_pk_mul_f32 v[72:73], v[72:73], v[192:193]\n\t"
    "ds_read_b128 v[20:23], v148 offset:5120\n\t"
    "s_waitcnt lgkmcnt(6)\n\t"
    "v_smfmac_f32_16x16x64_bf16 v[172:175], v[120:123], v[84:91], v124\n\t"
    "ds_read_b64 v[84:85], v216 offset:64000\n\t"
    "ds_read_b64 v[86:87], v252 offset:64000\n\t"
    "ds_read_b64 v[88:89], v217 offset:64000\n\t"
    "ds_read_b64 v[90:91], v253 offset:64000\n\t"
    "v_lshrrev_b32 v254, 16, v218\n\t"
    "v_and_b32 v218, s92, v218\n\t"
    "v_lshrrev_b32 v255, 16, v219\n\t"
    "v_and_b32 v219, s92, v219\n\t"
    "v_pk_mul_f32 v[74:75], v[74:75], v[194:195]\n\t"
    "v_exp_f32 v76, v72\n\t"
    "v_exp_f32 v77, v73\n\t"
    "v_exp_f32 v78, v74\n\t"
    "v_exp_f32 v79, v75\n\t"
    "ds_read_b128 v[24:27], v148 offset:6144\n\t"
    "s_waitcnt lgkmcnt(6)\n\t"
    "v_smfmac_f32_16x16x64_bf16 v[172:175], v[120:123], v[92:99], v124\n\t"
    "ds_read_b64 v[92:93], v218 offset:64000\n\t"
    "ds_read_b64 v[94:95], v254 offset:64000\n\t"
    "ds_read_b64 v[96:97], v219 offset:64000\n\t"
    "ds_read_b64 v[98:99], v255 offset:64000\n\t"
    "s_sub_u32 s91, s91, s95\n\t"
    "buffer_load_dwordx4 v[112:115], v127, %12, s91 offen offset:0 sc1\n\t"
    "v_lshrrev_b32 v252, 16, v220\n\t"
    "v_and_b32 v220, s92, v220\n\t"
    "v_lshrrev_b32 v253, 16, v221\n\t"
    "v_and_b32 v221, s92, v221\n\t"
    "v_pk_fma_f32 v[76:77], v[76:77], s[40:41], s[40:41]\n\t"
    "v_pk_fma_f32 v[78:79], v[78:79], s[42:43], s[42:43]\n\t"
    "v_rcp_f32 v76, v76\n\t"
    "v_rcp_f32 v77, v77\n\t"
    "v_rcp_f32 v78, v78\n\t"
    "ds_read_b128 v[28:31], v148 offset:7168\n\t"
    "s_waitcnt lgkmcnt(6)\n\t"
    "v_smfmac_f32_16x16x64_bf16 v[196:199], v[120:123], v[84:91], v124\n\t"
    "ds_read_b64 v[84:85], v220 offset:0\n\t"
    "ds_read_b64 v[86:87], v252 offset:0\n\t"
    "ds_read_b64 v[88:89], v221 offset:0\n\t"
    "ds_read_b64 v[90:91], v253 offset:0\n\t"
    "v_lshrrev_b32 v254, 16, v222\n\t"
    "v_and_b32 v222, s92, v222\n\t"
    "v_lshrrev_b32 v255, 16, v223\n\t"
    "v_and_b32 v223, s92, v223\n\t"
    "s_waitcnt vmcnt(5)\n\t"
    "v_readfirstlane_b32 s65, v164\n\t"
    "s_nop 0\n\t"
    "s_cmp_eq_u32 s65, 0\n\t"
    "s_cbranch_scc1 L_xw2_%=\n\t"
    "L_xwb2_%=:\n\t"
    "s_add_u32 s94, s94, 0x2000\n\t"
    "buffer_load_dwordx4 v[36:39], v127, %12, s94 offen sc1\n\t"
    "v_rcp_f32 v79, v79\n\t"
    "v_pk_mul_f32 v[76:77], v[76:77], v[192:193]\n\t"
    "v_pk_fma_f32 v[76:77], v[78:79], v[194:195], v[76:77]\n\t"
    "v_add_f32 v241, v76, v77\n\t"
    "s_waitcnt lgkmcnt(5)\n\t"
    "v_smfmac_f32_16x16x64_bf16 v[196:199], v[120:123], v[92:99], v124\n\t"
    "ds_read_b64 v[92:93], v222 offset:0\n\t"
    "ds_read_b64 v[94:95], v254 offset:0\n\t"
    "ds_read_b64 v[96:97], v223 offset:0\n\t"
    "ds_read_b64 v[98:99], v255 offset:0\n\t"
    "s_add_u32 s89, s89, 0x8000\n\t"
    "buffer_load_dwordx4 v[100:103], v127, %12, s89 offen offset:0 sc1\n\t"
    "v_lshrrev_b32 v252, 16, v116\n\t"
    "v_and_b32 v116, s92, v116\n\t"
    "v_lshrrev_b32 v253, 16, v117\n\t"
    "v_mov_b64 v[176:177], s[70:71]\n\t"
    "v_mov_b64 v[178:179], s[72:73]\n\t"
    "v_and_b32 v117, s92, v117\n\t"
    "v_pk_mul_f32 v[64:65], v[196:197], v[196:197]\n\t"
    "v_pk_mul_f32 v[66:67], v[198:199], v[198:199]\n\t"
    "v_pk_fma_f32 v[64:65], v[64:65], v[82:83], v[80:81]\n\t"
    "v_pk_fma_f32 v[66:67], v[66:67], v[82:83], v[80:81]\n\t"
    "v_pk_mul_f32 v[64:65], v[64:65], v[196:197]\n\t"
    "s_waitcnt lgkmcnt(4)\n\t"
    "v_smfmac_f32_16x16x64_bf16 v[176:179], v[120:123], v[84:91], v124\n\t"
    "ds_read_b64 v[84:85], v116 offset:64000\n\t"
    "ds_read_b64 v[86:87], v252 offset:64000\n\t"
    "ds_read_b64 v[88:89], v117 offset:64000\n\t"
    "ds_read_b64 v[90:91], v253 offset:64000\n\t"
    "v_lshrrev_b32 v254, 16, v118\n\t"
    "v_and_b32 v118, s92, v118\n\t"
    "v_lshrrev_b32 v255, 16, v119\n\t"
    "v_and_b32 v119, s92, v119\n\t"
    "v_pk_mul_f32 v[66:67], v[66:67], v[198:199]\n\t"
    "v_exp_f32 v68, v64\n\t"
    "v_exp_f32 v69, v65\n\t"
    "v_exp_f32 v70, v66\n\t"
    "v_exp_f32 v71, v67\n\t"
    "s_waitcnt lgkmcnt(4)\n\t"
    "v_smfmac_f32_16x16x64_bf16 v[176:179], v[120:123], v[92:99], v124\n\t"
    "ds_read_b64 v[92:93], v118 offset:64000\n\t"
    "ds_read_b64 v[94:95], v254 offset:64000\n\t"
    "ds_read_b64 v[96:97], v119 offset:64000\n\t"
    "ds_read_b64 v[98:99], v255 offset:64000\n\t"
    "buffer_load_dwordx4 v[104:107], v127, %12, s91 offen offset:1024 sc1\n\t"
    "v_lshrrev_b32 v252, 16, v224\n\t"
    "v_and_b32 v224, s92, v224\n\t"
    "v_lshrrev_b32 v253, 16, v225\n\t"
    "v_and_b32 v225, s92, v225\n\t"
    "v_pk_fma_f32 v[68:69], v[68:69], s[40:41], s[40:41]\n\t"
    "v_pk_fma_f32 v[70:71], v[70:71], s[42:43], s[42:43]\n\t"
    "v_rcp_f32 v68, v68\n\t"
    "v_rcp_f32 v69, v69\n\t"
    "v_rcp_f32 v70, v70\n\t"
    "s_waitcnt lgkmcnt(4)\n\t"
    "v_smfmac_f32_16x16x64_bf16 v[200:203], v[120:123], v[84:91], v124\n\t"
    "ds_read_b64 v[84:85], v224 offset:0\n\t"
    "ds_read_b64 v[86:87], v252 offset:0\n\t"
    "ds_read_b64 v[88:89], v225 offset:0\n\t"
    "ds_read_b64 v[90:91], v253 offset:0\n\t"
    "v_lshrrev_b32 v254, 16, v226\n\t"
    "v_and_b32 v226, s92, v226\n\t"
    "v_lshrrev_b32 v255, 16, v227\n\t"
    "v_and_b32 v227, s92, v227\n\t"
    "s_add_u32 s94, s94, 0x2000\n\t"
    "buffer_load_dwordx4 v[44:47], v127, %12, s94 offen sc1\n\t"
    "v_rcp_f32 v71, v71\n\t"
    "v_pk_mul_f32 v[68:69], v[68:69], v[196:197]\n\t"
    "v_pk_fma_f32 v[68:69], v[70:71], v[198:199], v[68:69]\n\t"
    "v_add_f32 v242, v68, v69\n\t"
    "s_lshl_b32 s46, %14, 2\n\t"
    "s_add_u32 s46, s46, 49294336\n\t"
    "s_mov_b64 exec, 1\n\t"
    "buffer_load_dword v144, off, %12, s46 sc1\n\t"
    "s_mov_b64 exec, -1\n\t"
    "s_waitcnt lgkmcnt(4)\n\t"
    "v_smfmac_f32_16x16x64_bf16 v[200:203], v[120:123], v[92:99], v124\n\t"
    "ds_read_b64 v[92:93], v226 offset:0\n\t"
    "ds_read_b64 v[94:95], v254 offset:0\n\t"
    "ds_read_b64 v[96:97], v227 offset:0\n\t"
    "ds_read_b64 v[98:99], v255 offset:0\n\t"
    "buffer_load_dwordx4 v[108:111], v127, %12, s89 offen offset:1024 sc1\n\t"
    "v_lshrrev_b32 v252, 16, v228\n\t"
    "v_and_b32 v228, s92, v228\n\t"
    "v_lshrrev_b32 v253, 16, v229\n\t"
    "v_mov_b64 v[180:181], s[70:71]\n\t"
    "v_mov_b64 v[182:183], s[72:73]\n\t"
    "v_and_b32 v229, s92, v229\n\t"
    "v_pk_mul_f32 v[72:73], v[200:201], v[200:201]\n\t"
    "v_pk_mul_f32 v[74:75], v[202:203], v[202:203]\n\t"
    "v_pk_fma_f32 v[72:73], v[72:73], v[82:83], v[80:81]\n\t"
    "v_pk_fma_f32 v[74:75], v[74:75], v[82:83], v[80:81]\n\t"
    "v_pk_mul_f32 v[72:73], v[72:73], v[200:201]\n\t"
    "s_cmp_lt_u32 %16, 4\n\t"
    "s_cbranch_scc1 L_tp248_%=\n\t"
    "s_setprio 0\n\t"
    "L_tp248_%=:\n\t"
    "s_waitcnt lgkmcnt(4)\n\t"
    "v_smfmac_f32_16x16x64_bf16 v[180:183], v[120:123], v[84:91], v124\n\t"
    "ds_read_b64 v[84:85], v228 offset:64000\n\t"
    "ds_read_b64 v[86:87], v252 offset:64000\n\t"
    "ds_read_b64 v[88:89], v229 offset:64000\n\t"
    "ds_read_b64 v[90:91], v253 offset:64000\n\t"
    "v_lshrrev_b32 v254, 16, v230\n\t"
    "v_and_b32 v230, s92, v230\n\t"
    "v_lshrrev_b32 v255, 16, v231\n\t"
    "v_and_b32 v231, s92, v231\n\t"
    "v_pk_mul_f32 v[74:75], v[74:75], v[202:203]\n\t"
    "v_exp_f32 v76, v72\n\t"
    "v_exp_f32 v77, v73\n\t"
    "v_exp_f32 v78, v74\n\t"
    "v_exp_f32 v79, v75\n\t"
    "s_waitcnt vmcnt(9) lgkmcnt(4)\n\t"
    "v_smfmac_f32_16x16x64_bf16 v[180:183], v[120:123], v[92:99], v124\n\t"
    "ds_read_b64 v[92:93], v230 offset:64000\n\t"
    "ds_read_b64 v[94:95], v254 offset:64000\n\t"
    "ds_read_b64 v[96:97], v231 offset:64000\n\t"
    "ds_read_b64 v[98:99], v255 offset:64000\n\t"
    "s_add_u32 s90, s90, 0x8000\n\t"
    "buffer_load_dwordx4 v[248:251], v127, %12, s90 offen offset:0 sc1\n\t"
    "v_lshrrev_b32 v252, 16, v232\n\t"
    "v_and_b32 v232, s92, v232\n\t"
    "v_lshrrev_b32 v253, 16, v233\n\t"
    "v_and_b32 v233, s92, v233\n\t"
    "v_pk_fma_f32 v[76:77], v[76:77], s[40:41], s[40:41]\n\t"
    "v_pk_fma_f32 v[78:79], v[78:79], s[42:43], s[42:43]\n\t"
    "v_rcp_f32 v76, v76\n\t"
    "v_rcp_f32 v77, v77\n\t"
    "v_rcp_f32 v78, v78\n\t"
    "s_waitcnt lgkmcnt(4)\n\t"
    "v_smfmac_f32_16x16x64_bf16 v[204:207], v[120:123], v[84:91], v124\n\t"
    "ds_read_b64 v[84:85], v232 offset:0\n\t"
    "ds_read_b64 v[86:87], v252 offset:0\n\t"
    "ds_read_b64 v[88:89], v233 offset:0\n\t"
    "ds_read_b64 v[90:91], v253 offset:0\n\t"
    "v_lshrrev_b32 v254, 16, v234\n\t"
    "v_and_b32 v234, s92, v234\n\t"
    "v_lshrrev_b32 v255, 16, v235\n\t"
    "v_and_b32 v235, s92, v235\n\t"
    "s_mov_b64 exec, 1\n\t"
    "buffer_load_dword v164, off, %12, s93 offset:384 sc1\n\t"
    "s_mov_b64 exec, -1\n\t"
    "s_waitcnt vmcnt(7)\n\t"
    "v_smfmac_f32_16x16x64_bf16 v[204:207], v[156:159], v[32:39], v124\n\t"
    "v_rcp_f32 v79, v79\n\t"
    "v_pk_mul_f32 v[76:77], v[76:77], v[200:201]\n\t"
    "v_pk_fma_f32 v[76:77], v[78:79], v[202:203], v[76:77]\n\t"
    "v_add_f32 v243, v76, v77\n\t"
    "s_waitcnt lgkmcnt(4)\n\t"
    "v_smfmac_f32_16x16x64_bf16 v[204:207], v[120:123], v[92:99], v124\n\t"
    "ds_read_b64 v[92:93], v234 offset:0\n\t"
    "ds_read_b64 v[94:95], v254 offset:0\n\t"
    "ds_read_b64 v[96:97], v235 offset:0\n\t"
    "ds_read_b64 v[98:99], v255 offset:0\n\t"
    "buffer_load_dwordx4 v[212:215], v127, %12, s90 offen offset:1024 sc1\n\t"
    "v_lshrrev_b32 v252, 16, v168\n\t"
    "v_and_b32 v168, s92, v168\n\t"
    "v_lshrrev_b32 v253, 16, v169\n\t"
    "v_mov_b64 v[184:185], s[70:71]\n\t"
    "v_mov_b64 v[186:187], s[72:73]\n\t"
    "v_and_b32 v169, s92, v169\n\t"
    "v_smfmac_f32_16x16x64_bf16 v[208:211], v[160:163], v[32:39], v124\n\t"
    "v_pk_mul_f32 v[64:65], v[204:205], v[204:205]\n\t"
    "v_pk_mul_f32 v[66:67], v[206:207], v[206:207]\n\t"
    "v_pk_fma_f32 v[64:65], v[64:65], v[82:83], v[80:81]\n\t"
    "v_pk_fma_f32 v[66:67], v[66:67], v[82:83], v[80:81]\n\t"
    "v_pk_mul_f32 v[64:65], v[64:65], v[204:205]\n\t"
    "ds_read_b128 v[32:35], v148 offset:8192\n\t"
    "s_waitcnt lgkmcnt(5)\n\t"
    "v_smfmac_f32_16x16x64_bf16 v[184:187], v[120:123], v[84:91], v124\n\t"
    "ds_read_b64 v[84:85], v168 offset:64000\n\t"
    "ds_read_b64 v[86:87], v252 offset:64000\n\t"
    "ds_read_b64 v[88:89], v169 offset:64000\n\t"
    "ds_read_b64 v[90:91], v253 offset:64000\n\t"
    "v_lshrrev_b32 v254, 16, v170\n\t"
    "v_and_b32 v170, s92, v170\n\t"
    "v_lshrrev_b32 v255, 16, v171\n\t"
    "v_and_b32 v171, s92, v171\n\t"
    "v_pk_mul_f32 v[66:67], v[66:67], v[206:207]\n\t"
    "v_exp_f32 v68, v64\n\t"
    "v_exp_f32 v69, v65\n\t"
    "v_exp_f32 v70, v66\n\t"
    "v_exp_f32 v71, v67\n\t"
    "ds_read_b128 v[36:39], v148 offset:9216\n\t"
    "s_waitcnt lgkmcnt(6)\n\t"
    "v_smfmac_f32_16x16x64_bf16 v[184:187], v[120:123], v[92:99], v124\n\t"
    "ds_read_b64 v[92:93], v170 offset:64000\n\t"
    "ds_read_b64 v[94:95], v254 offset:64000\n\t"
    "ds_read_b64 v[96:97], v171 offset:64000\n\t"
    "ds_read_b64 v[98:99], v255 offset:64000\n\t"
    "s_add_u32 s91, s91, 0x8000\n\t"
    "buffer_load_dwordx4 v[216:219], v127, %12, s91 offen offset:0 sc1\n\t"
    "v_lshrrev_b32 v252, 16, v112\n\t"
    "v_and_b32 v112, s92, v112\n\t"
    "v_lshrrev_b32 v253, 16, v113\n\t"
    "v_and_b32 v113, s92, v113\n\t"
    "v_pk_fma_f32 v[68:69], v[68:69], s[40:41], s[40:41]\n\t"
    "v_pk_fma_f32 v[70:71], v[70:71], s[42:43], s[42:43]\n\t"
    "v_rcp_f32 v68, v68\n\t"
    "v_rcp_f32 v69, v69\n\t"
    "v_rcp_f32 v70, v70\n\t"
    "s_waitcnt lgkmcnt(5)\n\t"
    "v_smfmac_f32_16x16x64_bf16 v[208:211], v[120:123], v[84:91], v124\n\t"
    "ds_read_b64 v[84:85], v112 offset:0\n\t"
    "ds_read_b64 v[86:87], v252 offset:0\n\t"
    "ds_read_b64 v[88:89], v113 offset:0\n\t"
    "ds_read_b64 v[90:91], v253 offset:0\n\t"
    "v_lshrrev_b32 v254, 16, v114\n\t"
    "v_and_b32 v114, s92, v114\n\t"
    "v_lshrrev_b32 v255, 16, v115\n\t"
    "v_and_b32 v115, s92, v115\n\t"
    "s_waitcnt vmcnt(6)\n\t"
    "v_smfmac_f32_16x16x64_bf16 v[172:175], v[156:159], v[40:47], v124\n\t"
    "v_rcp_f32 v71, v71\n\t"
    "v_pk_mul_f32 v[68:69], v[68:69], v[204:205]\n\t"
    "v_pk_fma_f32 v[68:69], v[70:71], v[206:207], v[68:69]\n\t"
    "v_add_f32 v244, v68, v69\n\t"
    "s_waitcnt vmcnt(5)\n\t"
    "v_readfirstlane_b32 s38, v144\n\t"
    "s_nop 0\n\t"
    "s_cmp_eq_u32 s38, 0\n\t"
    "s_cbranch_scc1 L_rw_%=\n\t"
    "L_rok_%=:\n\t"
    "s_and_b32 s46, %16, 3\n\t"
    "s_mul_i32 s49, s46, 1040\n\t"
    "s_add_u32 s49, s49, 2064\n\t"
    "s_and_b32 s37, s38, 0xffff\n\t"
    "s_lshr_b32 s44, s38, 16\n\t"
    "s_sub_u32 s44, s44, s37\n\t"
    "s_lshl_b32 s37, s37, 2\n\t"
    "s_lshl_b32 s38, s44, 2\n\t"
    "s_mul_i32 s51, s46, s44\n\t"
    "s_add_u32 s48, s51, s44\n\t"
    "s_lshl_b32 s48, s48, 16\n\t"
    "s_add_u32 s48, s48, s51\n\t"
    "s_lshr_b32 s47, %16, 2\n\t"
    "s_add_u32 s48, s48, s47\n\t"
    "s_add_u32 s48, s48, 0x200\n\t"
    "s_mul_i32 s46, s37, 400\n\t"
    "s_add_u32 s96, %18, s46\n\t"
    "s_addc_u32 s97, %19, 0\n\t"
    "s_sub_u32 s98, 6553600, s46\n\t"
    "s_mov_b32 s99, 0x20000\n\t"
    "v_bfe_u32 v154, v147, 2, 2\n\t"
    "v_and_b32 v155, 3, v147\n\t"
    "v_mul_u32_u24 v154, 25, v154\n\t"
    "v_mad_u32_u24 v154, v155, 6, v154\n\t"
    "v_lshlrev_b32 v153, 2, v154\n\t"
    "s_and_b32 s53, s48, 0xff\n\t"
    "s_bfe_u32 s55, s48, 0x80008\n\t"
    "s_add_u32 s54, s53, s55\n\t"
    "s_add_u32 s55, s54, s55\n\t"
    "s_lshr_b32 s64, s48, 16\n\t"
    "s_mul_i32 s66, s53, 400\n\t"
    "buffer_load_dwordx3 v[128:130], v153, s[96:99], s66 offen\n\t"
    "buffer_load_dwordx4 v[132:135], v153, s[96:99], s66 offen offset:12\n\t"
    "s_mul_i32 s66, s54, 400\n\t"
    "buffer_load_dwordx3 v[136:138], v153, s[96:99], s66 offen\n\t"
    "buffer_load_dwordx4 v[140:143], v153, s[96:99], s66 offen offset:12\n\t"
    "v_cmp_eq_u32 s[62:63], 0, v155\n\t"
    "v_and_b32 v151, 48, v147\n\t"
    "v_or_b32 v155, 0x100000, v151\n\t"
    "v_cndmask_b32 v152, v155, v151, s[62:63]\n\t"
    "s_waitcnt lgkmcnt(4)\n\t"
    "v_smfmac_f32_16x16x64_bf16 v[208:211], v[120:123], v[92:99], v124\n\t"
    "ds_read_b64 v[92:93], v114 offset:0\n\t"
    "ds_read_b64 v[94:95], v254 offset:0\n\t"
    "ds_read_b64 v[96:97], v115 offset:0\n\t"
    "ds_read_b64 v[98:99], v255 offset:0\n\t"
    "buffer_load_dwordx4 v[220:223], v127, %12, s91 offen offset:1024 sc1\n\t"
    "v_lshrrev_b32 v252, 16, v100\n\t"
    "v_and_b32 v100, s92, v100\n\t"
    "v_lshrrev_b32 v253, 16, v101\n\t"
    "v_mov_b64 v[188:189], s[70:71]\n\t"
    "v_mov_b64 v[190:191], s[72:73]\n\t"
    "v_and_b32 v101, s92, v101\n\t"
    "v_smfmac_f32_16x16x64_bf16 v[176:179], v[160:163], v[40:47], v124\n\t"
    "v_pk_mul_f32 v[72:73], v[208:209], v[208:209]\n\t"
    "v_pk_mul_f32 v[74:75], v[210:211], v[210:211]\n\t"
    "v_pk_fma_f32 v[72:73], v[72:73], v[82:83], v[80:81]\n\t"
    "v_pk_fma_f32 v[74:75], v[74:75], v[82:83], v[80:81]\n\t"
    "v_pk_mul_f32 v[72:73], v[72:73], v[208:209]\n\t"
    "ds_read_b128 v[40:43], v148 offset:10240\n\t"
    "s_waitcnt lgkmcnt(5)\n\t"
    "v_smfmac_f32_16x16x64_bf16 v[188:191], v[120:123], v[84:91], v124\n\t"
    "ds_read_b64 v[84:85], v100 offset:64000\n\t"
    "ds_read_b64 v[86:87], v252 offset:64000\n\t"
    "ds_read_b64 v[88:89], v101 offset:64000\n\t"
    "ds_read_b64 v[90:91], v253 offset:64000\n\t"
    "v_lshrrev_b32 v254, 16, v102\n\t"
    "v_and_b32 v102, s92, v102\n\t"
    "v_lshrrev_b32 v255, 16, v103\n\t"
    "v_and_b32 v103, s92, v103\n\t"
    "s_waitcnt vmcnt(7)\n\t"
    "v_readfirstlane_b32 s65, v164\n\t"
    "s_nop 0\n\t"
    "s_cmp_eq_u32 s65, 0\n\t"
    "s_cbranch_scc1 L_xw3_%=\n\t"
    "L_xwb3_%=:\n\t"
    "s_add_u32 s94, s94, 0x2000\n\t"
    "buffer_load_dwordx4 v[52:55], v127, %12, s94 offen sc1\n\t"
    "v_pk_mul_f32 v[74:75], v[74:75], v[210:211]\n\t"
    "v_exp_f32 v76, v72\n\t"
    "v_exp_f32 v77, v73\n\t"
    "v_exp_f32 v78, v74\n\t"
    "v_exp_f32 v79, v75\n\t"
    "ds_read_b128 v[44:47], v148 offset:11264\n\t"
    "s_waitcnt lgkmcnt(6)\n\t"
    "v_smfmac_f32_16x16x64_bf16 v[188:191], v[120:123], v[92:99], v124\n\t"
    "ds_read_b64 v[92:93], v102 offset:64000\n\t"
    "ds_read_b64 v[94:95], v254 offset:64000\n\t"
    "ds_read_b64 v[96:97], v103 offset:64000\n\t"
    "ds_read_b64 v[98:99], v255 offset:64000\n\t"
    "v_lshrrev_b32 v252, 16, v104\n\t"
    "v_and_b32 v104, s92, v104\n\t"
    "v_lshrrev_b32 v253, 16, v105\n\t"
    "v_and_b32 v105, s92, v105\n\t"
    "v_pk_fma_f32 v[76:77], v[76:77], s[40:41], s[40:41]\n\t"
    "v_pk_fma_f32 v[78:79], v[78:79], s[42:43], s[42:43]\n\t"
    "v_rcp_f32 v76, v76\n\t"
    "v_rcp_f32 v77, v77\n\t"
    "v_rcp_f32 v78, v78\n\t"
    "s_waitcnt lgkmcnt(5)\n\t"
    "v_smfmac_f32_16x16x64_bf16 v[172:175], v[120:123], v[84:91], v124\n\t"
    "ds_read_b64 v[84:85], v104 offset:0\n\t"
    "ds_read_b64 v[86:87], v252 offset:0\n\t"
    "ds_read_b64 v[88:89], v105 offset:0\n\t"
    "ds_read_b64 v[90:91], v253 offset:0\n\t"
    "v_lshrrev_b32 v254, 16, v106\n\t"
    "v_and_b32 v106, s92, v106\n\t"
    "v_lshrrev_b32 v255, 16, v107\n\t"
    "v_and_b32 v107, s92, v107\n\t"
    "v_rcp_f32 v79, v79\n\t"
    "v_pk_mul_f32 v[76:77], v[76:77], v[208:209]\n\t"
    "v_pk_fma_f32 v[76:77], v[78:79], v[210:211], v[76:77]\n\t"
    "v_add_f32 v245, v76, v77\n\t"
    "s_waitcnt lgkmcnt(4)\n\t"
    "v_smfmac_f32_16x16x64_bf16 v[172:175], v[120:123], v[92:99], v124\n\t"
    "ds_read_b64 v[92:93], v106 offset:0\n\t"
    "ds_read_b64 v[94:95], v254 offset:0\n\t"
    "ds_read_b64 v[96:97], v107 offset:0\n\t"
    "ds_read_b64 v[98:99], v255 offset:0\n\t"
    "v_lshrrev_b32 v252, 16, v108\n\t"
    "v_and_b32 v108, s92, v108\n\t"
    "v_lshrrev_b32 v253, 16, v109\n\t"
    "v_mov_b64 v[192:193], s[70:71]\n\t"
    "v_mov_b64 v[194:195], s[72:73]\n\t"
    "v_and_b32 v109, s92, v109\n\t"
    "v_pk_mul_f32 v[64:65], v[172:173], v[172:173]\n\t"
    "v_pk_mul_f32 v[66:67], v[174:175], v[174:175]\n\t"
    "v_pk_fma_f32 v[64:65], v[64:65], v[82:83], v[80:81]\n\t"
    "v_pk_fma_f32 v[66:67], v[66:67], v[82:83], v[80:81]\n\t"
    "v_pk_mul_f32 v[64:65], v[64:65], v[172:173]\n\t"
    "s_waitcnt lgkmcnt(4)\n\t"
    "v_smfmac_f32_16x16x64_bf16 v[192:195], v[120:123], v[84:91], v124\n\t"
    "ds_read_b64 v[84:85], v108 offset:64000\n\t"
    "ds_read_b64 v[86:87], v252 offset:64000\n\t"
    "ds_read_b64 v[88:89], v109 offset:64000\n\t"
    "ds_read_b64 v[90:91], v253 offset:64000\n\t"
    "v_lshrrev_b32 v254, 16, v110\n\t"
    "v_and_b32 v110, s92, v110\n\t"
    "v_lshrrev_b32 v255, 16, v111\n\t"
    "v_and_b32 v111, s92, v111\n\t"
    "s_add_u32 s94, s94, 0x2000\n\t"
    "buffer_load_dwordx4 v[60:63], v127, %12, s94 offen sc1\n\t"
    "v_pk_mul_f32 v[66:67], v[66:67], v[174:175]\n\t"
    "v_exp_f32 v68, v64\n\t"
    "v_exp_f32 v69, v65\n\t"
    "v_exp_f32 v70, v66\n\t"
    "v_exp_f32 v71, v67\n\t"
    "v_lshrrev_b32 v166, 7, v127\n\t"
    "v_and_b32 v167, 0x70, v127\n\t"
    "v_lshl_add_u32 v126, v166, 8, v167\n\t"
    "v_add_u32 v126, 128, v126\n\t"
    "s_bfe_u32 s46, %14, 0x30003\n\t"
    "s_min_u32 s46, s46, 6\n\t"
    "s_lshl_b32 s69, s46, 3\n\t"
    "s_add_u32 s69, s69, %16\n\t"
    "s_cmp_lt_u32 %16, 4\n\t"
    "s_cbranch_scc1 L_tp261_%=\n\t"
    "s_setprio 1\n\t"
    "L_tp261_%=:\n\t"
    "s_waitcnt lgkmcnt(4)\n\t"
    "v_smfmac_f32_16x16x64_bf16 v[192:195], v[120:123], v[92:99], v124\n\t"
    "ds_read_b64 v[92:93], v110 offset:64000\n\t"
    "ds_read_b64 v[94:95], v254 offset:64000\n\t"
    "ds_read_b64 v[96:97], v111 offset:64000\n\t"
    "ds_read_b64 v[98:99], v255 offset:64000\n\t"
    "v_lshrrev_b32 v252, 16, v248\n\t"
    "v_and_b32 v248, s92, v248\n\t"
    "v_lshrrev_b32 v253, 16, v249\n\t"
    "v_and_b32 v249, s92, v249\n\t"
    "v_pk_fma_f32 v[68:69], v[68:69], s[40:41], s[40:41]\n\t"
    "v_pk_fma_f32 v[70:71], v[70:71], s[42:43], s[42:43]\n\t"
    "v_rcp_f32 v68, v68\n\t"
    "v_rcp_f32 v69, v69\n\t"
    "v_rcp_f32 v70, v70\n\t"
    "s_barrier\n\t"
    "s_waitcnt lgkmcnt(4)\n\t"
    "v_smfmac_f32_16x16x64_bf16 v[176:179], v[120:123], v[84:91], v124\n\t"
    "ds_read_b64 v[84:85], v248 offset:64000\n\t"
    "ds_read_b64 v[86:87], v252 offset:64000\n\t"
    "ds_read_b64 v[88:89], v249 offset:64000\n\t"
    "ds_read_b64 v[90:91], v253 offset:64000\n\t"
    "v_lshrrev_b32 v254, 16, v250\n\t"
    "v_and_b32 v250, s92, v250\n\t"
    "v_lshrrev_b32 v255, 16, v251\n\t"
    "v_and_b32 v251, s92, v251\n\t"
    "v_rcp_f32 v71, v71\n\t"
    "v_pk_mul_f32 v[68:69], v[68:69], v[172:173]\n\t"
    "v_pk_fma_f32 v[68:69], v[70:71], v[174:175], v[68:69]\n\t"
    "v_add_f32 v246, v68, v69\n\t"
    "s_mul_i32 s44, s69, 1040\n\t"
    "s_add_u32 m0, s44, 0\n\t"
    "s_lshl_b32 s44, s69, 11\n\t"
    "buffer_load_dwordx4 v126, %15, s44 offen lds\n\t"
    "s_add_u32 s69, s69, 8\n\t"
    "s_sub_u32 s44, s69, 61\n\t"
    "s_cmp_ge_u32 s69, 61\n\t"
    "s_cselect_b32 s69, s44, s69\n\t"
    "s_waitcnt vmcnt(9) lgkmcnt(4)\n\t"
    "v_smfmac_f32_16x16x64_bf16 v[176:179], v[120:123], v[92:99], v124\n\t"
    "ds_read_b64 v[92:93], v250 offset:64000\n\t"
    "ds_read_b64 v[94:95], v254 offset:64000\n\t"
    "ds_read_b64 v[96:97], v251 offset:64000\n\t"
    "ds_read_b64 v[98:99], v255 offset:64000\n\t"
    "v_lshrrev_b32 v252, 16, v212\n\t"
    "v_and_b32 v212, s92, v212\n\t"
    "v_lshrrev_b32 v253, 16, v213\n\t"
    "v_and_b32 v213, s92, v213\n\t"
    "s_mul_i32 s44, s69, 1040\n\t"
    "s_add_u32 m0, s44, 0\n\t"
    "s_lshl_b32 s44, s69, 11\n\t"
    "buffer_load_dwordx4 v126, %15, s44 offen lds\n\t"
    "s_add_u32 s69, s69, 8\n\t"
    "s_sub_u32 s44, s69, 61\n\t"
    "s_cmp_ge_u32 s69, 61\n\t"
    "s_cselect_b32 s69, s44, s69\n\t"
    "v_lshlrev_b32 v146, 2, v147\n\t"
    "s_lshl_b32 s50, s37, 8\n\t"
    "s_add_u32 s50, s50, 27262976\n\t"
    "s_lshl_b32 s46, s53, 8\n\t"
    "s_add_u32 s46, s46, s50\n\t"
    "buffer_load_dword v144, v146, %12, s46 offen sc1\n\t"
    "buffer_load_dword v145, v146, %12, s46 offen offset:512 sc1\n\t"
    "v_mov_b32 v149, s49\n\t"
    "s_add_u32 s46, s51, 6\n\t"
    "v_mov_b32 v155, s46\n\t"
    "ds_write_b32 v149, v155\n\t"
    "s_mov_b32 s61, 0\n\t"
    "v_mov_b32 v149, 1024\n\t"
    "v_mov_b32 v150, 0\n\t"
    "ds_write_b32 v149, v150\n\t"
    "s_mov_b32 s45, 1\n\t"
    "s_waitcnt lgkmcnt(6)\n\t"
    "v_smfmac_f32_16x16x64_bf16 v[180:183], v[120:123], v[84:91], v124\n\t"
    "ds_read_b64 v[84:85], v212 offset:64000\n\t"
    "ds_read_b64 v[86:87], v252 offset:64000\n\t"
    "ds_read_b64 v[88:89], v213 offset:64000\n\t"
    "ds_read_b64 v[90:91], v253 offset:64000\n\t"
    "v_lshrrev_b32 v254, 16, v214\n\t"
    "v_and_b32 v214, s92, v214\n\t"
    "v_lshrrev_b32 v255, 16, v215\n\t"
    "v_and_b32 v215, s92, v215\n\t"
    "s_waitcnt vmcnt(5)\n\t"
    "v_smfmac_f32_16x16x64_bf16 v[180:183], v[156:159], v[48:55], v124\n\t"
    "v_pk_mul_f32 v[72:73], v[176:177], v[176:177]\n\t"
    "v_pk_mul_f32 v[74:75], v[178:179], v[178:179]\n\t"
    "v_pk_fma_f32 v[72:73], v[72:73], v[82:83], v[80:81]\n\t"
    "v_pk_fma_f32 v[74:75], v[74:75], v[82:83], v[80:81]\n\t"
    "v_pk_mul_f32 v[72:73], v[72:73], v[176:177]\n\t"
    "v_pk_mul_f32 v[74:75], v[74:75], v[178:179]\n\t"
    "v_exp_f32 v76, v72\n\t"
    "s_mul_i32 s44, s69, 1040\n\t"
    "s_add_u32 m0, s44, 0\n\t"
    "s_lshl_b32 s44, s69, 11\n\t"
    "buffer_load_dwordx4 v126, %15, s44 offen lds\n\t"
    "s_add_u32 s69, s69, 8\n\t"
    "s_sub_u32 s44, s69, 61\n\t"
    "s_cmp_ge_u32 s69, 61\n\t"
    "s_cselect_b32 s69, s44, s69\n\t"
    "s_waitcnt lgkmcnt(6)\n\t"
    "v_smfmac_f32_16x16x64_bf16 v[180:183], v[120:123], v[92:99], v124\n\t"
    "ds_read_b64 v[92:93], v214 offset:64000\n\t"
    "ds_read_b64 v[94:95], v254 offset:64000\n\t"
    "ds_read_b64 v[96:97], v215 offset:64000\n\t"
    "ds_read_b64 v[98:99], v255 offset:64000\n\t"
    "v_lshrrev_b32 v252, 16, v216\n\t"
    "v_and_b32 v216, s92, v216\n\t"
    "v_lshrrev_b32 v253, 16, v217\n\t"
    "v_and_b32 v217, s92, v217\n\t"
    "v_smfmac_f32_16x16x64_bf16 v[184:187], v[160:163], v[48:55], v124\n\t"
    "v_exp_f32 v77, v73\n\t"
    "v_exp_f32 v78, v74\n\t"
    "v_exp_f32 v79, v75\n\t"
    "v_pk_fma_f32 v[76:77], v[76:77], s[40:41], s[40:41]\n\t"
    "v_pk_fma_f32 v[78:79], v[78:79], s[42:43], s[42:43]\n\t"
    "v_rcp_f32 v76, v76\n\t"
    "v_rcp_f32 v77, v77\n\t"
    "s_mul_i32 s44, s69, 1040\n\t"
    "s_add_u32 m0, s44, 0\n\t"
    "s_lshl_b32 s44, s69, 11\n\t"
    "buffer_load_dwordx4 v126, %15, s44 offen lds\n\t"
    "s_add_u32 s69, s69, 8\n\t"
    "s_sub_u32 s44, s69, 61\n\t"
    "s_cmp_ge_u32 s69, 61\n\t"
    "s_cselect_b32 s69, s44, s69\n\t"
    "ds_read_b128 v[48:51], v148 offset:12288\n\t"
    "s_waitcnt lgkmcnt(5)\n\t"
    "v_smfmac_f32_16x16x64_bf16 v[184:187], v[120:123], v[84:91], v124\n\t"
    "ds_read_b64 v[84:85], v216 offset:64000\n\t"
    "ds_read_b64 v[86:87], v252 offset:64000\n\t"
    "ds_read_b64 v[88:89], v217 offset:64000\n\t"
    "ds_read_b64 v[90:91], v253 offset:64000\n\t"
    "v_lshrrev_b32 v254, 16, v218\n\t"
    "v_and_b32 v218, s92, v218\n\t"
    "v_lshrrev_b32 v255, 16, v219\n\t"
    "v_and_b32 v219, s92, v219\n\t"
    "v_rcp_f32 v78, v78\n\t"
    "v_rcp_f32 v79, v79\n\t"
    "v_pk_mul_f32 v[76:77], v[76:77], v[176:177]\n\t"
    "v_pk_fma_f32 v[76:77], v[78:79], v[178:179], v[76:77]\n\t"
    "v_add_f32 v247, v76, v77\n\t"
    "v_pk_mul_f32 v[64:65], v[180:181], v[180:181]\n\t"
    "v_pk_mul_f32 v[66:67], v[182:183], v[182:183]\n\t"
    "s_mul_i32 s44, s69, 1040\n\t"
    "s_add_u32 m0, s44, 0\n\t"
    "s_lshl_b32 s44, s69, 11\n\t"
    "buffer_load_dwordx4 v126, %15, s44 offen lds\n\t"
    "s_add_u32 s69, s69, 8\n\t"
    "s_sub_u32 s44, s69, 61\n\t"
    "s_cmp_ge_u32 s69, 61\n\t"
    "s_cselect_b32 s69, s44, s69\n\t"
    "ds_read_b128 v[52:55], v148 offset:13312\n\t"
    "s_waitcnt lgkmcnt(6)\n\t"
    "v_smfmac_f32_16x16x64_bf16 v[184:187], v[120:123], v[92:99], v124\n\t"
    "ds_read_b64 v[92:93], v218 offset:64000\n\t"
    "ds_read_b64 v[94:95], v254 offset:64000\n\t"
    "ds_read_b64 v[96:97], v219 offset:64000\n\t"
    "ds_read_b64 v[98:99], v255 offset:64000\n\t"
    "v_lshrrev_b32 v252, 16, v220\n\t"
    "v_and_b32 v220, s92, v220\n\t"
    "v_lshrrev_b32 v253, 16, v221\n\t"
    "v_and_b32 v221, s92, v221\n\t"
    "v_pk_fma_f32 v[64:65], v[64:65], v[82:83], v[80:81]\n\t"
    "v_pk_fma_f32 v[66:67], v[66:67], v[82:83], v[80:81]\n\t"
    "v_pk_mul_f32 v[64:65], v[64:65], v[180:181]\n\t"
    "v_pk_mul_f32 v[66:67], v[66:67], v[182:183]\n\t"
    "v_exp_f32 v68, v64\n\t"
    "v_exp_f32 v69, v65\n\t"
    "v_exp_f32 v70, v66\n\t"
    "s_mul_i32 s44, s69, 1040\n\t"
    "s_add_u32 m0, s44, 0\n\t"
    "s_lshl_b32 s44, s69, 11\n\t"
    "buffer_load_dwordx4 v126, %15, s44 offen lds\n\t"
    "s_add_u32 s69, s69, 8\n\t"
    "s_sub_u32 s44, s69, 61\n\t"
    "s_cmp_ge_u32 s69, 61\n\t"
    "s_cselect_b32 s69, s44, s69\n\t"
    "s_setprio 0\n\t"
    "s_waitcnt lgkmcnt(5)\n\t"
    "v_smfmac_f32_16x16x64_bf16 v[188:191], v[120:123], v[84:91], v124\n\t"
    "ds_read_b64 v[84:85], v220 offset:64000\n\t"
    "ds_read_b64 v[86:87], v252 offset:64000\n\t"
    "ds_read_b64 v[88:89], v221 offset:64000\n\t"
    "ds_read_b64 v[90:91], v253 offset:64000\n\t"
    "v_lshrrev_b32 v254, 16, v222\n\t"
    "v_and_b32 v222, s92, v222\n\t"
    "v_lshrrev_b32 v255, 16, v223\n\t"
    "v_and_b32 v223, s92, v223\n\t"
    "s_waitcnt vmcnt(8)\n\t"
    "v_smfmac_f32_16x16x64_bf16 v[188:191], v[156:159], v[56:63], v124\n\t"
    "v_exp_f32 v71, v67\n\t"
    "v_pk_fma_f32 v[68:69], v[68:69], s[40:41], s[40:41]\n\t"
    "v_pk_fma_f32 v[70:71], v[70:71], s[42:43], s[42:43]\n\t"
    "v_rcp_f32 v68, v68\n\t"
    "v_rcp_f32 v69, v69\n\t"
    "v_rcp_f32 v70, v70\n\t"
    "v_rcp_f32 v71, v71\n\t"
    "s_mul_i32 s44, s69, 1040\n\t"
    "s_add_u32 m0, s44, 0\n\t"
    "s_lshl_b32 s44, s69, 11\n\t"
    "buffer_load_dwordx4 v126, %15, s44 offen lds\n\t"
    "s_add_u32 s69, s69, 8\n\t"
    "s_sub_u32 s44, s69, 61\n\t"
    "s_cmp_ge_u32 s69, 61\n\t"
    "s_cselect_b32 s69, s44, s69\n\t"
    "s_waitcnt lgkmcnt(4)\n\t"
    "v_smfmac_f32_16x16x64_bf16 v[188:191], v[120:123], v[92:99], v124\n\t"
    "ds_read_b64 v[92:93], v222 offset:64000\n\t"
    "ds_read_b64 v[94:95], v254 offset:64000\n\t"
    "ds_read_b64 v[96:97], v223 offset:64000\n\t"
    "ds_read_b64 v[98:99], v255 offset:64000\n\t"
    "v_smfmac_f32_16x16x64_bf16 v[192:195], v[160:163], v[56:63], v124\n\t"
    "v_pk_mul_f32 v[68:69], v[68:69], v[180:181]\n\t"
    "v_pk_fma_f32 v[68:69], v[70:71], v[182:183], v[68:69]\n\t"
    "v_add_f32 v248, v68, v69\n\t"
    "v_pk_mul_f32 v[72:73], v[184:185], v[184:185]\n\t"
    "v_pk_mul_f32 v[74:75], v[186:187], v[186:187]\n\t"
    "v_pk_fma_f32 v[72:73], v[72:73], v[82:83], v[80:81]\n\t"
    "v_pk_fma_f32 v[74:75], v[74:75], v[82:83], v[80:81]\n\t"
    "s_mul_i32 s44, s69, 1040\n\t"
    "s_add_u32 m0, s44, 0\n\t"
    "s_lshl_b32 s44, s69, 11\n\t"
    "buffer_load_dwordx4 v126, %15, s44 offen lds\n\t"
    "s_add_u32 s69, s69, 8\n\t"
    "s_sub_u32 s44, s69, 61\n\t"
    "s_cmp_ge_u32 s69, 61\n\t"
    "s_cselect_b32 s69, s44, s69\n\t"
    "ds_read_b128 v[56:59], v148 offset:14336\n\t"
    "s_waitcnt lgkmcnt(5)\n\t"
    "v_smfmac_f32_16x16x64_bf16 v[192:195], v[120:123], v[84:91], v124\n\t"
    "v_pk_mul_f32 v[72:73], v[72:73], v[184:185]\n\t"
    "v_pk_mul_f32 v[74:75], v[74:75], v[186:187]\n\t"
    "v_exp_f32 v76, v72\n\t"
    "v_exp_f32 v77, v73\n\t"
    "v_exp_f32 v78, v74\n\t"
    "v_exp_f32 v79, v75\n\t"
    "v_pk_fma_f32 v[76:77], v[76:77], s[40:41], s[40:41]\n\t"
    "ds_read_b128 v[60:63], v148 offset:15360\n\t"
    "s_waitcnt lgkmcnt(2)\n\t"
    "v_smfmac_f32_16x16x64_bf16 v[192:195], v[120:123], v[92:99], v124\n\t"
    "v_pk_fma_f32 v[78:79], v[78:79], s[42:43], s[42:43]\n\t"
    "v_rcp_f32 v76, v76\n\t"
    "v_rcp_f32 v77, v77\n\t"
    "v_rcp_f32 v78, v78\n\t"
    "v_rcp_f32 v79, v79\n\t"
    "v_pk_mul_f32 v[76:77], v[76:77], v[184:185]\n\t"
    "v_pk_fma_f32 v[76:77], v[78:79], v[186:187], v[76:77]\n\t"
    "s_barrier\n\t"
    "v_add_u32 v126, 124928, v126\n\t"
    "s_bfe_u32 s46, %14, 0x30003\n\t"
    "s_min_u32 s46, s46, 6\n\t"
    "s_lshl_b32 s69, s46, 3\n\t"
    "s_add_u32 s69, s69, %16\n\t"
    "s_mul_i32 s44, s69, 1040\n\t"
    "s_add_u32 m0, s44, 63440\n\t"
    "s_lshl_b32 s44, s69, 11\n\t"
    "buffer_load_dwordx4 v126, %15, s44 offen lds\n\t"
    "s_add_u32 s69, s69, 8\n\t"
    "s_sub_u32 s44, s69, 64\n\t"
    "s_cmp_ge_u32 s69, 64\n\t"
    "s_cselect_b32 s69, s44, s69\n\t"
    "s_mul_i32 s44, s69, 1040\n\t"
    "s_add_u32 m0, s44, 63440\n\t"
    "s_lshl_b32 s44, s69, 11\n\t"
    "buffer_load_dwordx4 v126, %15, s44 offen lds\n\t"
    "s_add_u32 s69, s69, 8\n\t"
    "s_sub_u32 s44, s69, 64\n\t"
    "s_cmp_ge_u32 s69, 64\n\t"
    "s_cselect_b32 s69, s44, s69\n\t"
    "s_mul_i32 s44, s69, 1040\n\t"
    "s_add_u32 m0, s44, 63440\n\t"
    "s_lshl_b32 s44, s69, 11\n\t"
    "buffer_load_dwordx4 v126, %15, s44 offen lds\n\t"
    "s_add_u32 s69, s69, 8\n\t"
    "s_sub_u32 s44, s69, 64\n\t"
    "s_cmp_ge_u32 s69, 64\n\t"
    "s_cselect_b32 s69, s44, s69\n\t"
    "s_mul_i32 s44, s69, 1040\n\t"
    "s_add_u32 m0, s44, 63440\n\t"
    "s_lshl_b32 s44, s69, 11\n\t"
    "buffer_load_dwordx4 v126, %15, s44 offen lds\n\t"
    "s_add_u32 s69, s69, 8\n\t"
    "s_sub_u32 s44, s69, 64\n\t"
    "s_cmp_ge_u32 s69, 64\n\t"
    "s_cselect_b32 s69, s44, s69\n\t"
    "s_mul_i32 s44, s69, 1040\n\t"
    "s_add_u32 m0, s44, 63440\n\t"
    "s_lshl_b32 s44, s69, 11\n\t"
    "buffer_load_dwordx4 v126, %15, s44 offen lds\n\t"
    "s_add_u32 s69, s69, 8\n\t"
    "s_sub_u32 s44, s69, 64\n\t"
    "s_cmp_ge_u32 s69, 64\n\t"
    "s_cselect_b32 s69, s44, s69\n\t"
    "s_mul_i32 s44, s69, 1040\n\t"
    "s_add_u32 m0, s44, 63440\n\t"
    "s_lshl_b32 s44, s69, 11\n\t"
    "buffer_load_dwordx4 v126, %15, s44 offen lds\n\t"
    "s_add_u32 s69, s69, 8\n\t"
    "s_sub_u32 s44, s69, 64\n\t"
    "s_cmp_ge_u32 s69, 64\n\t"
    "s_cselect_b32 s69, s44, s69\n\t"
    "s_mul_i32 s44, s69, 1040\n\t"
    "s_add_u32 m0, s44, 63440\n\t"
    "s_lshl_b32 s44, s69, 11\n\t"
    "buffer_load_dwordx4 v126, %15, s44 offen lds\n\t"
    "s_add_u32 s69, s69, 8\n\t"
    "s_sub_u32 s44, s69, 64\n\t"
    "s_cmp_ge_u32 s69, 64\n\t"
    "s_cselect_b32 s69, s44, s69\n\t"
    "s_mul_i32 s44, s69, 1040\n\t"
    "s_add_u32 m0, s44, 63440\n\t"
    "s_lshl_b32 s44, s69, 11\n\t"
    "buffer_load_dwordx4 v126, %15, s44 offen lds\n\t"
    "s_add_u32 s69, s69, 8\n\t"
    "s_sub_u32 s44, s69, 64\n\t"
    "s_cmp_ge_u32 s69, 64\n\t"
    "s_cselect_b32 s69, s44, s69\n\t"
    "ds_read_b128 v[84:87], v148 offset:21504\n\t"
    "ds_read_b128 v[88:91], v148 offset:22528\n\t"
    "v_add_f32 v249, v76, v77\n\t"
    "v_pk_mul_f32 v[64:65], v[188:189], v[188:189]\n\t"
    "v_pk_mul_f32 v[66:67], v[190:191], v[190:191]\n\t"
    "v_pk_fma_f32 v[64:65], v[64:65], v[82:83], v[80:81]\n\t"
    "v_pk_fma_f32 v[66:67], v[66:67], v[82:83], v[80:81]\n\t"
    "v_pk_mul_f32 v[64:65], v[64:65], v[188:189]\n\t"
    "v_pk_mul_f32 v[66:67], v[66:67], v[190:191]\n\t"
    "v_exp_f32 v68, v64\n\t"
    "v_exp_f32 v69, v65\n\t"
    "v_exp_f32 v70, v66\n\t"
    "v_exp_f32 v71, v67\n\t"
    "v_pk_fma_f32 v[68:69], v[68:69], s[40:41], s[40:41]\n\t"
    "v_pk_fma_f32 v[70:71], v[70:71], s[42:43], s[42:43]\n\t"
    "v_rcp_f32 v68, v68\n\t"
    "v_rcp_f32 v69, v69\n\t"
    "v_rcp_f32 v70, v70\n\t"
    "v_rcp_f32 v71, v71\n\t"
    "v_pk_mul_f32 v[68:69], v[68:69], v[188:189]\n\t"
    "v_pk_fma_f32 v[68:69], v[70:71], v[190:191], v[68:69]\n\t"
    "v_add_f32 v250, v68, v69\n\t"
    "v_pk_mul_f32 v[72:73], v[192:193], v[192:193]\n\t"
    "v_pk_mul_f32 v[74:75], v[194:195], v[194:195]\n\t"
    "v_pk_fma_f32 v[72:73], v[72:73], v[82:83], v[80:81]\n\t"
    "v_pk_fma_f32 v[74:75], v[74:75], v[82:83], v[80:81]\n\t"
    "v_pk_mul_f32 v[72:73], v[72:73], v[192:193]\n\t"
    "v_pk_mul_f32 v[74:75], v[74:75], v[194:195]\n\t"
    "v_exp_f32 v76, v72\n\t"
    "v_exp_f32 v77, v73\n\t"
    "v_exp_f32 v78, v74\n\t"
    "v_exp_f32 v79, v75\n\t"
    "v_pk_fma_f32 v[76:77], v[76:77], s[40:41], s[40:41]\n\t"
    "v_pk_fma_f32 v[78:79], v[78:79], s[42:43], s[42:43]\n\t"
    "v_rcp_f32 v76, v76\n\t"
    "v_rcp_f32 v77, v77\n\t"
    "v_rcp_f32 v78, v78\n\t"
    "v_rcp_f32 v79, v79\n\t"
    "v_pk_mul_f32 v[76:77], v[76:77], v[192:193]\n\t"
    "v_pk_fma_f32 v[76:77], v[78:79], v[194:195], v[76:77]\n\t"
    "v_add_f32 v251, v76, v77\n\t"
    "ds_read_b128 v[64:67], v148 offset:16384\n\t"
    "ds_read_b128 v[68:71], v148 offset:17408\n\t"
    "ds_read_b128 v[72:75], v148 offset:18432\n\t"
    "ds_read_b128 v[76:79], v148 offset:19456\n\t"
    "ds_read_b128 v[80:83], v148 offset:20480\n\t"
    "s_waitcnt vmcnt(0) lgkmcnt(0)\n\t"
    "s_mov_b32 s68, 130\n\t"
    "v_mad_u32_u24 v128, v128, s68, v152\n\t"
    "v_and_b32 v128, -16, v128\n\t"
    "v_mad_u32_u24 v129, v129, s68, v151\n\t"
    "v_and_b32 v129, -16, v129\n\t"
    "v_mad_u32_u24 v130, v130, s68, v151\n\t"
    "v_and_b32 v130, -16, v130\n\t"
    "v_mad_u32_u24 v132, v132, s68, v151\n\t"
    "v_and_b32 v132, -16, v132\n\t"
    "v_mad_u32_u24 v133, v133, s68, v151\n\t"
    "v_and_b32 v133, -16, v133\n\t"
    "v_mad_u32_u24 v134, v134, s68, v151\n\t"
    "v_and_b32 v134, -16, v134\n\t"
    "v_mad_u32_u24 v135, v135, s68, v151\n\t"
    "v_and_b32 v135, -16, v135\n\t"
    "v_lshrrev_b32 v122, 4, v147\n\t"
    "v_and_b32 v155, 15, v147\n\t"
    "v_lshlrev_b32 v155, 4, v155\n\t"
    "v_mul_u32_u24 v122, 65536, v122\n\t"
    "v_add_u32 v122, v122, v155\n\t"
    "v_mov_b32 v150, 1\n\t"
    "v_mov_b32 v123, 0\n\t"
    "v_lshlrev_b32 v149, 20, v147\n\t"
    "v_add_u32 v149, s49, v149\n\t"
    "s_mov_b32 s52, s53\n\t"
    "s_cmp_ge_u32 %16, 4\n\t"
    "s_cbranch_scc1 L_st4_%=\n\t"
    "s_bfe_u32 s65, s84, 0xc0002\n\t"
    "s_add_u32 s65, s65, s36\n\t"
    "buffer_store_dword v236, v125, %12, s65 offen offset:0 sc1\n\t"
    "buffer_store_dword v237, v125, %12, s65 offen offset:256 sc1\n\t"
    "s_bfe_u32 s65, s85, 0xc0002\n\t"
    "s_add_u32 s65, s65, s36\n\t"
    "buffer_store_dword v238, v125, %12, s65 offen offset:0 sc1\n\t"
    "buffer_store_dword v239, v125, %12, s65 offen offset:256 sc1\n\t"
    "s_bfe_u32 s65, s86, 0xc0002\n\t"
    "s_add_u32 s65, s65, s36\n\t"
    "buffer_store_dword v240, v125, %12, s65 offen offset:0 sc1\n\t"
    "buffer_store_dword v241, v125, %12, s65 offen offset:256 sc1\n\t"
    "s_bfe_u32 s65, s87, 0xc0002\n\t"
    "s_add_u32 s65, s65, s36\n\t"
    "buffer_store_dword v242, v125, %12, s65 offen offset:0 sc1\n\t"
    "buffer_store_dword v243, v125, %12, s65 offen offset:256 sc1\n\t"
    "s_bfe_u32 s65, s88, 0xc0002\n\t"
    "s_add_u32 s65, s65, s36\n\t"
    "buffer_store_dword v244, v125, %12, s65 offen offset:0 sc1\n\t"
    "buffer_store_dword v245, v125, %12, s65 offen offset:256 sc1\n\t"
    "s_bfe_u32 s65, s89, 0xc0002\n\t"
    "s_add_u32 s65, s65, s36\n\t"
    "buffer_store_dword v246, v125, %12, s65 offen offset:0 sc1\n\t"
    "buffer_store_dword v247, v125, %12, s65 offen offset:256 sc1\n\t"
    "s_bfe_u32 s65, s90, 0xc0002\n\t"
    "s_add_u32 s65, s65, s36\n\t"
    "buffer_store_dword v248, v125, %12, s65 offen offset:0 sc1\n\t"
    "buffer_store_dword v249, v125, %12, s65 offen offset:256 sc1\n\t"
    "s_bfe_u32 s65, s91, 0xc0002\n\t"
    "s_add_u32 s65, s65, s36\n\t"
    "buffer_store_dword v250, v125, %12, s65 offen offset:0 sc1\n\t"
    "buffer_store_dword v251, v125, %12, s65 offen offset:256 sc1\n\t"
    "s_sub_u32 s61, s64, 3\n\t"
    "L_st4_%=:\n\t"
    "s_barrier\n\t"
    "s_mov_b32 s68, 130\n\t"
    "ds_read_b128 v[156:159], v128 offset:0\n\t"
    "ds_read_b128 v[160:163], v128 offset:64\n\t"
    "ds_read_b128 v[164:167], v129 offset:0\n\t"
    "ds_read_b128 v[168:171], v129 offset:64\n\t"
    "ds_read_b128 v[172:175], v130 offset:0\n\t"
    "ds_read_b128 v[176:179], v130 offset:64\n\t"
    "ds_read_b128 v[180:183], v132 offset:0\n\t"
    "ds_read_b128 v[184:187], v132 offset:64\n\t"
    "ds_read_b128 v[188:191], v133 offset:0\n\t"
    "ds_read_b128 v[192:195], v133 offset:64\n\t"
    "ds_read_b128 v[196:199], v134 offset:0\n\t"
    "ds_read_b128 v[200:203], v134 offset:64\n\t"
    "ds_read_b128 v[204:207], v135 offset:0\n\t"
    "ds_read_b128 v[208:211], v135 offset:64\n\t"
    "s_setprio 1\n\t"
    "L_pnn_%=:\n\t"
    "s_mul_i32 s69, s52, 272\n\t"
    "s_add_u32 s69, s69, 130048\n\t"
    "s_mul_i32 s66, s55, 400\n\t"
    "s_waitcnt lgkmcnt(8)\n\t"
    "v_mfma_f32_16x16x32_bf16 v[92:95], v[156:159], v[156:159], 0\n\t"
    "v_add_u32 v147, s69, v146\n\t"
    "v_fmac_f32 v121, v108, v72\n\t"
    "ds_add_rtn_u32 v148, v149, v150\n\t"
    "v_mfma_f32_16x16x32_bf16 v[92:95], v[160:163], v[160:163], v[92:95]\n\t"
    "v_fmac_f32 v120, v109, v73\n\t"
    "v_fmac_f32 v121, v110, v74\n\t"
    "buffer_load_dwordx3 v[128:130], v153, s[96:99], s66 offen\n\t"
    "v_mfma_f32_16x16x32_bf16 v[96:99], v[164:167], v[164:167], 0\n\t"
    "v_fmac_f32 v120, v111, v75\n\t"
    "v_fmac_f32 v121, v112, v76\n\t"
    "v_mfma_f32_16x16x32_bf16 v[96:99], v[168:171], v[168:171], v[96:99]\n\t"
    "v_fmac_f32 v120, v113, v77\n\t"
    "v_fmac_f32 v121, v114, v78\n\t"
    "buffer_load_dwordx4 v[132:135], v153, s[96:99], s66 offen offset:12\n\t"
    "v_mfma_f32_16x16x32_bf16 v[100:103], v[164:167], v[172:175], 0\n\t"
    "v_fmac_f32 v120, v115, v79\n\t"
    "v_fmac_f32 v121, v116, v80\n\t"
    "v_mfma_f32_16x16x32_bf16 v[100:103], v[168:171], v[176:179], v[100:103]\n\t"
    "v_fmac_f32 v120, v117, v81\n\t"
    "v_fmac_f32 v121, v118, v82\n\t"
    "s_waitcnt vmcnt(3)\n\t"
    "s_cmp_ge_u32 s55, s61\n\t"
    "s_cbranch_scc1 L_req0_%=\n\t"
    "L_back0_%=:\n\t"
    "v_mfma_f32_16x16x32_bf16 v[104:107], v[172:175], v[172:175], 0\n\t"
    "v_mad_u32_u24 v136, v136, s68, v152\n\t"
    "v_and_b32 v136, -16, v136\n\t"
    "v_mfma_f32_16x16x32_bf16 v[104:107], v[176:179], v[176:179], v[104:107]\n\t"
    "v_fmac_f32 v120, v119, v83\n\t"
    "v_add_f32 v154, v120, v121\n\t"
    "ds_write_b32 v147, v154\n\t"
    "v_mfma_f32_16x16x32_bf16 v[108:111], v[156:159], v[164:167], 0\n\t"
    "v_fma_f32 v120, v92, v84, v144\n\t"
    "v_mul_f32 v121, v96, v0\n\t"
    "v_mfma_f32_16x16x32_bf16 v[108:111], v[160:163], v[168:171], v[108:111]\n\t"
    "v_fmac_f32 v120, v97, v1\n\t"
    "v_fmac_f32 v121, v98, v2\n\t"
    "s_waitcnt lgkmcnt(8)\n\t"
    "v_mfma_f32_16x16x32_bf16 v[112:115], v[164:167], v[180:183], 0\n\t"
    "v_fmac_f32 v120, v99, v3\n\t"
    "v_fmac_f32 v121, v100, v4\n\t"
    "v_mfma_f32_16x16x32_bf16 v[112:115], v[168:171], v[184:187], v[112:115]\n\t"
    "v_fmac_f32 v120, v101, v5\n\t"
    "v_fmac_f32 v121, v102, v6\n\t"
    "v_mfma_f32_16x16x32_bf16 v[116:119], v[172:175], v[180:183], 0\n\t"
    "v_fmac_f32 v120, v103, v7\n\t"
    "v_fmac_f32 v121, v104, v8\n\t"
    "v_mfma_f32_16x16x32_bf16 v[116:119], v[176:179], v[184:187], v[116:119]\n\t"
    "v_fmac_f32 v120, v105, v9\n\t"
    "v_fmac_f32 v121, v106, v10\n\t"
    "ds_read_b128 v[212:215], v136 offset:0\n\t"
    "ds_read_b128 v[216:219], v136 offset:64\n\t"
    "v_mfma_f32_16x16x32_bf16 v[92:95], v[180:183], v[180:183], 0\n\t"
    "v_fmac_f32 v120, v107, v11\n\t"
    "v_mad_u32_u24 v137, v137, s68, v151\n\t"
    "v_mfma_f32_16x16x32_bf16 v[92:95], v[184:187], v[184:187], v[92:95]\n\t"
    "v_and_b32 v137, -16, v137\n\t"
    "v_fmac_f32 v121, v108, v85\n\t"
    "v_mfma_f32_16x16x32_bf16 v[96:99], v[156:159], v[172:175], 0\n\t"
    "v_fmac_f32 v120, v112, v12\n\t"
    "v_fmac_f32 v121, v113, v13\n\t"
    "v_mfma_f32_16x16x32_bf16 v[96:99], v[160:163], v[176:179], v[96:99]\n\t"
    "v_fmac_f32 v120, v114, v14\n\t"
    "v_fmac_f32 v121, v115, v15\n\t"
    "s_lshl_b32 s51, s55, 8\n\t"
    "s_add_u32 s51, s51, s50\n\t"
    "s_waitcnt lgkmcnt(8)\n\t"
    "v_mfma_f32_16x16x32_bf16 v[100:103], v[164:167], v[188:191], 0\n\t"
    "v_mad_u32_u24 v138, v138, s68, v151\n\t"
    "v_and_b32 v138, -16, v138\n\t"
    "v_mfma_f32_16x16x32_bf16 v[100:103], v[168:171], v[192:195], v[100:103]\n\t"
    "v_fmac_f32 v120, v116, v16\n\t"
    "v_fmac_f32 v121, v117, v17\n\t"
    "buffer_load_dword v144, v146, %12, s51 offen sc1\n\t"
    "v_mfma_f32_16x16x32_bf16 v[104:107], v[172:175], v[188:191], 0\n\t"
    "v_fmac_f32 v120, v118, v18\n\t"
    "v_fmac_f32 v121, v119, v19\n\t"
    "v_mfma_f32_16x16x32_bf16 v[104:107], v[176:179], v[192:195], v[104:107]\n\t"
    "v_fmac_f32 v120, v92, v20\n\t"
    "v_fmac_f32 v121, v93, v21\n\t"
    "s_waitcnt lgkmcnt(6)\n\t"
    "v_mfma_f32_16x16x32_bf16 v[108:111], v[164:167], v[196:199], 0\n\t"
    "v_fmac_f32 v120, v94, v22\n\t"
    "v_fmac_f32 v121, v95, v23\n\t"
    "v_mfma_f32_16x16x32_bf16 v[108:111], v[168:171], v[200:203], v[108:111]\n\t"
    "v_fmac_f32 v120, v96, v86\n\t"
    "v_mad_u32_u24 v140, v140, s68, v151\n\t"
    "v_mfma_f32_16x16x32_bf16 v[112:115], v[156:159], v[180:183], 0\n\t"
    "v_fmac_f32 v121, v100, v24\n\t"
    "v_fmac_f32 v120, v101, v25\n\t"
    "v_mfma_f32_16x16x32_bf16 v[112:115], v[160:163], v[184:187], v[112:115]\n\t"
    "v_fmac_f32 v121, v102, v26\n\t"
    "v_fmac_f32 v120, v103, v27\n\t"
    "s_waitcnt lgkmcnt(3)\n\t"
    "v_mfma_f32_16x16x32_bf16 v[116:119], v[164:167], v[204:207], 0\n\t"
    "v_readfirstlane_b32 s60, v148\n\t"
    "v_and_b32 v140, -16, v140\n\t"
    "v_mfma_f32_16x16x32_bf16 v[116:119], v[168:171], v[208:211], v[116:119]\n\t"
    "v_fmac_f32 v121, v104, v28\n\t"
    "v_fmac_f32 v120, v105, v29\n\t"
    "ds_read_b128 v[164:167], v137 offset:0\n\t"
    "ds_read_b128 v[168:171], v137 offset:64\n\t"
    "v_mfma_f32_16x16x32_bf16 v[92:95], v[172:175], v[196:199], 0\n\t"
    "v_fmac_f32 v121, v106, v30\n\t"
    "v_fmac_f32 v120, v107, v31\n\t"
    "v_mfma_f32_16x16x32_bf16 v[92:95], v[176:179], v[200:203], v[92:95]\n\t"
    "v_fmac_f32 v121, v108, v32\n\t"
    "v_fmac_f32 v120, v109, v33\n\t"
    "v_mfma_f32_16x16x32_bf16 v[96:99], v[172:175], v[204:207], 0\n\t"
    "v_fmac_f32 v121, v110, v34\n\t"
    "v_fmac_f32 v120, v111, v35\n\t"
    "v_mfma_f32_16x16x32_bf16 v[96:99], v[176:179], v[208:211], v[96:99]\n\t"
    "v_fmac_f32 v121, v112, v87\n\t"
    "v_mad_u32_u24 v141, v141, s68, v151\n\t"
    "ds_read_b128 v[172:175], v138 offset:0\n\t"
    "ds_read_b128 v[176:179], v138 offset:64\n\t"
    "v_mfma_f32_16x16x32_bf16 v[100:103], v[156:159], v[188:191], 0\n\t"
    "v_fmac_f32 v120, v116, v36\n\t"
    "v_fmac_f32 v121, v117, v37\n\t"
    "v_mfma_f32_16x16x32_bf16 v[100:103], v[160:163], v[192:195], v[100:103]\n\t"
    "v_fmac_f32 v120, v118, v38\n\t"
    "v_fmac_f32 v121, v119, v39\n\t"
    "v_mfma_f32_16x16x32_bf16 v[104:107], v[180:183], v[188:191], 0\n\t"
    "v_and_b32 v141, -16, v141\n\t"
    "v_fmac_f32 v120, v92, v40\n\t"
    "v_mfma_f32_16x16x32_bf16 v[104:107], v[184:187], v[192:195], v[104:107]\n\t"
    "v_fmac_f32 v121, v93, v41\n\t"
    "v_fmac_f32 v120, v94, v42\n\t"
    "v_mfma_f32_16x16x32_bf16 v[108:111], v[180:183], v[196:199], 0\n\t"
    "v_fmac_f32 v121, v95, v43\n\t"
    "v_fmac_f32 v120, v96, v44\n\t"
    "v_mfma_f32_16x16x32_bf16 v[108:111], v[184:187], v[200:203], v[108:111]\n\t"
    "v_fmac_f32 v121, v97, v45\n\t"
    "v_fmac_f32 v120, v98, v46\n\t"
    "v_mfma_f32_16x16x32_bf16 v[112:115], v[180:183], v[204:207], 0\n\t"
    "v_fmac_f32 v121, v99, v47\n\t"
    "v_fmac_f32 v120, v100, v88\n\t"
    "v_mfma_f32_16x16x32_bf16 v[112:115], v[184:187], v[208:211], v[112:115]\n\t"
    "v_mad_u32_u24 v142, v142, s68, v151\n\t"
    "v_and_b32 v142, -16, v142\n\t"
    "ds_read_b128 v[180:183], v140 offset:0\n\t"
    "ds_read_b128 v[184:187], v140 offset:64\n\t"
    "s_cmp_eq_u32 s45, 1\n\t"
    "s_cbranch_scc1 L_sig_%=\n\t"
    "L_sigb_%=:\n\t"
    "v_mfma_f32_16x16x32_bf16 v[116:119], v[156:159], v[196:199], 0\n\t"
    "v_fmac_f32 v121, v104, v48\n\t"
    "v_fmac_f32 v120, v105, v49\n\t"
    "v_mfma_f32_16x16x32_bf16 v[116:119], v[160:163], v[200:203], v[116:119]\n\t"
    "v_fmac_f32 v121, v106, v50\n\t"
    "v_fmac_f32 v120, v107, v51\n\t"
    "v_mfma_f32_16x16x32_bf16 v[92:95], v[188:191], v[188:191], 0\n\t"
    "v_mad_u32_u24 v143, v143, s68, v151\n\t"
    "v_and_b32 v143, -16, v143\n\t"
    "v_mfma_f32_16x16x32_bf16 v[92:95], v[192:195], v[192:195], v[92:95]\n\t"
    "v_fmac_f32 v121, v108, v52\n\t"
    "v_fmac_f32 v120, v109, v53\n\t"
    "v_mfma_f32_16x16x32_bf16 v[96:99], v[188:191], v[196:199], 0\n\t"
    "v_fmac_f32 v121, v110, v54\n\t"
    "v_fmac_f32 v120, v111, v55\n\t"
    "v_mfma_f32_16x16x32_bf16 v[96:99], v[192:195], v[200:203], v[96:99]\n\t"
    "v_fmac_f32 v121, v112, v56\n\t"
    "v_fmac_f32 v120, v113, v57\n\t"
    "v_mfma_f32_16x16x32_bf16 v[100:103], v[188:191], v[204:207], 0\n\t"
    "v_fmac_f32 v121, v114, v58\n\t"
    "v_fmac_f32 v120, v115, v59\n\t"
    "v_mfma_f32_16x16x32_bf16 v[100:103], v[192:195], v[208:211], v[100:103]\n\t"
    "v_fmac_f32 v121, v116, v89\n\t"
    "ds_read_b128 v[188:191], v141 offset:0\n\t"
    "ds_read_b128 v[192:195], v141 offset:64\n\t"
    "v_mfma_f32_16x16x32_bf16 v[104:107], v[156:159], v[204:207], 0\n\t"
    "v_fmac_f32 v120, v92, v60\n\t"
    "v_fmac_f32 v121, v93, v61\n\t"
    "v_mfma_f32_16x16x32_bf16 v[104:107], v[160:163], v[208:211], v[104:107]\n\t"
    "v_fmac_f32 v120, v94, v62\n\t"
    "v_fmac_f32 v121, v95, v63\n\t"
    "v_mfma_f32_16x16x32_bf16 v[108:111], v[196:199], v[196:199], 0\n\t"
    "v_fmac_f32 v120, v96, v64\n\t"
    "v_fmac_f32 v121, v97, v65\n\t"
    "v_mfma_f32_16x16x32_bf16 v[108:111], v[200:203], v[200:203], v[108:111]\n\t"
    "v_fmac_f32 v120, v98, v66\n\t"
    "v_fmac_f32 v121, v99, v67\n\t"
    "v_mfma_f32_16x16x32_bf16 v[112:115], v[196:199], v[204:207], 0\n\t"
    "v_fmac_f32 v120, v100, v68\n\t"
    "v_fmac_f32 v121, v101, v69\n\t"
    "v_mfma_f32_16x16x32_bf16 v[112:115], v[200:203], v[208:211], v[112:115]\n\t"
    "v_fmac_f32 v120, v102, v70\n\t"
    "v_fmac_f32 v121, v103, v71\n\t"
    "ds_read_b128 v[196:199], v142 offset:0\n\t"
    "ds_read_b128 v[200:203], v142 offset:64\n\t"
    "v_mfma_f32_16x16x32_bf16 v[116:119], v[204:207], v[204:207], 0\n\t"
    "v_fmac_f32 v120, v104, v90\n\t"
    "v_mfma_f32_16x16x32_bf16 v[116:119], v[208:211], v[208:211], v[116:119]\n\t"
    "ds_read_b128 v[204:207], v143 offset:0\n\t"
    "ds_read_b128 v[208:211], v143 offset:64\n\t"
    "s_mov_b32 s52, s53\n\t"
    "s_mov_b32 s53, s54\n\t"
    "s_mov_b32 s54, s55\n\t"
    "s_mov_b32 s55, s60\n\t"
    "s_cmp_lt_u32 s54, s64\n\t"
    "s_cbranch_scc1 L_pr0_%=\n\t"
    "s_setprio 0\n\t"
    "L_pr0_%=:\n\t"
    "s_cmp_lt_u32 s53, s64\n\t"
    "s_cbranch_scc0 L_done_%=\n\t"
    "s_mul_i32 s69, s52, 272\n\t"
    "s_add_u32 s69, s69, 130048\n\t"
    "s_mul_i32 s66, s55, 400\n\t"
    "s_waitcnt lgkmcnt(8)\n\t"
    "v_mfma_f32_16x16x32_bf16 v[92:95], v[212:215], v[212:215], 0\n\t"
    "v_add_u32 v147, s69, v146\n\t"
    "v_fmac_f32 v121, v108, v72\n\t"
    "ds_add_rtn_u32 v148, v149, v150\n\t"
    "v_mfma_f32_16x16x32_bf16 v[92:95], v[216:219], v[216:219], v[92:95]\n\t"
    "v_fmac_f32 v120, v109, v73\n\t"
    "v_fmac_f32 v121, v110, v74\n\t"
    "buffer_load_dwordx3 v[136:138], v153, s[96:99], s66 offen\n\t"
    "v_mfma_f32_16x16x32_bf16 v[96:99], v[164:167], v[164:167], 0\n\t"
    "v_fmac_f32 v120, v111, v75\n\t"
    "v_fmac_f32 v121, v112, v76\n\t"
    "v_mfma_f32_16x16x32_bf16 v[96:99], v[168:171], v[168:171], v[96:99]\n\t"
    "v_fmac_f32 v120, v113, v77\n\t"
    "v_fmac_f32 v121, v114, v78\n\t"
    "buffer_load_dwordx4 v[140:143], v153, s[96:99], s66 offen offset:12\n\t"
    "v_mfma_f32_16x16x32_bf16 v[100:103], v[164:167], v[172:175], 0\n\t"
    "v_fmac_f32 v120, v115, v79\n\t"
    "v_fmac_f32 v121, v116, v80\n\t"
    "v_mfma_f32_16x16x32_bf16 v[100:103], v[168:171], v[176:179], v[100:103]\n\t"
    "v_fmac_f32 v120, v117, v81\n\t"
    "v_fmac_f32 v121, v118, v82\n\t"
    "s_waitcnt vmcnt(3)\n\t"
    "s_cmp_ge_u32 s55, s61\n\t"
    "s_cbranch_scc1 L_req1_%=\n\t"
    "L_back1_%=:\n\t"
    "v_mfma_f32_16x16x32_bf16 v[104:107], v[172:175], v[172:175], 0\n\t"
    "v_mad_u32_u24 v128, v128, s68, v152\n\t"
    "v_and_b32 v128, -16, v128\n\t"
    "v_mfma_f32_16x16x32_bf16 v[104:107], v[176:179], v[176:179], v[104:107]\n\t"
    "v_fmac_f32 v120, v119, v83\n\t"
    "v_add_f32 v154, v120, v121\n\t"
    "ds_write_b32 v147, v154\n\t"
    "v_mfma_f32_16x16x32_bf16 v[108:111], v[212:215], v[164:167], 0\n\t"
    "v_fma_f32 v120, v92, v84, v145\n\t"
    "v_mul_f32 v121, v96, v0\n\t"
    "v_mfma_f32_16x16x32_bf16 v[108:111], v[216:219], v[168:171], v[108:111]\n\t"
    "v_fmac_f32 v120, v97, v1\n\t"
    "v_fmac_f32 v121, v98, v2\n\t"
    "s_waitcnt lgkmcnt(8)\n\t"
    "v_mfma_f32_16x16x32_bf16 v[112:115], v[164:167], v[180:183], 0\n\t"
    "v_fmac_f32 v120, v99, v3\n\t"
    "v_fmac_f32 v121, v100, v4\n\t"
    "v_mfma_f32_16x16x32_bf16 v[112:115], v[168:171], v[184:187], v[112:115]\n\t"
    "v_fmac_f32 v120, v101, v5\n\t"
    "v_fmac_f32 v121, v102, v6\n\t"
    "v_mfma_f32_16x16x32_bf16 v[116:119], v[172:175], v[180:183], 0\n\t"
    "v_fmac_f32 v120, v103, v7\n\t"
    "v_fmac_f32 v121, v104, v8\n\t"
    "v_mfma_f32_16x16x32_bf16 v[116:119], v[176:179], v[184:187], v[116:119]\n\t"
    "v_fmac_f32 v120, v105, v9\n\t"
    "v_fmac_f32 v121, v106, v10\n\t"
    "ds_read_b128 v[156:159], v128 offset:0\n\t"
    "ds_read_b128 v[160:163], v128 offset:64\n\t"
    "v_mfma_f32_16x16x32_bf16 v[92:95], v[180:183], v[180:183], 0\n\t"
    "v_fmac_f32 v120, v107, v11\n\t"
    "v_mad_u32_u24 v129, v129, s68, v151\n\t"
    "v_mfma_f32_16x16x32_bf16 v[92:95], v[184:187], v[184:187], v[92:95]\n\t"
    "v_and_b32 v129, -16, v129\n\t"
    "v_fmac_f32 v121, v108, v85\n\t"
    "v_mfma_f32_16x16x32_bf16 v[96:99], v[212:215], v[172:175], 0\n\t"
    "v_fmac_f32 v120, v112, v12\n\t"
    "v_fmac_f32 v121, v113, v13\n\t"
    "v_mfma_f32_16x16x32_bf16 v[96:99], v[216:219], v[176:179], v[96:99]\n\t"
    "v_fmac_f32 v120, v114, v14\n\t"
    "v_fmac_f32 v121, v115, v15\n\t"
    "s_lshl_b32 s51, s55, 8\n\t"
    "s_add_u32 s51, s51, s50\n\t"
    "s_waitcnt lgkmcnt(8)\n\t"
    "v_mfma_f32_16x16x32_bf16 v[100:103], v[164:167], v[188:191], 0\n\t"
    "v_mad_u32_u24 v130, v130, s68, v151\n\t"
    "v_and_b32 v130, -16, v130\n\t"
    "v_mfma_f32_16x16x32_bf16 v[100:103], v[168:171], v[192:195], v[100:103]\n\t"
    "v_fmac_f32 v120, v116, v16\n\t"
    "v_fmac_f32 v121, v117, v17\n\t"
    "buffer_load_dword v145, v146, %12, s51 offen sc1\n\t"
    "v_mfma_f32_16x16x32_bf16 v[104:107], v[172:175], v[188:191], 0\n\t"
    "v_fmac_f32 v120, v118, v18\n\t"
    "v_fmac_f32 v121, v119, v19\n\t"
    "v_mfma_f32_16x16x32_bf16 v[104:107], v[176:179], v[192:195], v[104:107]\n\t"
    "v_fmac_f32 v120, v92, v20\n\t"
    "v_fmac_f32 v121, v93, v21\n\t"
    "s_waitcnt lgkmcnt(6)\n\t"
    "v_mfma_f32_16x16x32_bf16 v[108:111], v[164:167], v[196:199], 0\n\t"
    "v_fmac_f32 v120, v94, v22\n\t"
    "v_fmac_f32 v121, v95, v23\n\t"
    "v_mfma_f32_16x16x32_bf16 v[108:111], v[168:171], v[200:203], v[108:111]\n\t"
    "v_fmac_f32 v120, v96, v86\n\t"
    "v_mad_u32_u24 v132, v132, s68, v151\n\t"
    "v_mfma_f32_16x16x32_bf16 v[112:115], v[212:215], v[180:183], 0\n\t"
    "v_fmac_f32 v121, v100, v24\n\t"
    "v_fmac_f32 v120, v101, v25\n\t"
    "v_mfma_f32_16x16x32_bf16 v[112:115], v[216:219], v[184:187], v[112:115]\n\t"
    "v_fmac_f32 v121, v102, v26\n\t"
    "v_fmac_f32 v120, v103, v27\n\t"
    "s_waitcnt lgkmcnt(3)\n\t"
    "v_mfma_f32_16x16x32_bf16 v[116:119], v[164:167], v[204:207], 0\n\t"
    "v_readfirstlane_b32 s60, v148\n\t"
    "v_and_b32 v132, -16, v132\n\t"
    "v_mfma_f32_16x16x32_bf16 v[116:119], v[168:171], v[208:211], v[116:119]\n\t"
    "v_fmac_f32 v121, v104, v28\n\t"
    "v_fmac_f32 v120, v105, v29\n\t"
    "ds_read_b128 v[164:167], v129 offset:0\n\t"
    "ds_read_b128 v[168:171], v129 offset:64\n\t"
    "v_mfma_f32_16x16x32_bf16 v[92:95], v[172:175], v[196:199], 0\n\t"
    "v_fmac_f32 v121, v106, v30\n\t"
    "v_fmac_f32 v120, v107, v31\n\t"
    "v_mfma_f32_16x16x32_bf16 v[92:95], v[176:179], v[200:203], v[92:95]\n\t"
    "v_fmac_f32 v121, v108, v32\n\t"
    "v_fmac_f32 v120, v109, v33\n\t"
    "v_mfma_f32_16x16x32_bf16 v[96:99], v[172:175], v[204:207], 0\n\t"
    "v_fmac_f32 v121, v110, v34\n\t"
    "v_fmac_f32 v120, v111, v35\n\t"
    "v_mfma_f32_16x16x32_bf16 v[96:99], v[176:179], v[208:211], v[96:99]\n\t"
    "v_fmac_f32 v121, v112, v87\n\t"
    "v_mad_u32_u24 v133, v133, s68, v151\n\t"
    "ds_read_b128 v[172:175], v130 offset:0\n\t"
    "ds_read_b128 v[176:179], v130 offset:64\n\t"
    "v_mfma_f32_16x16x32_bf16 v[100:103], v[212:215], v[188:191], 0\n\t"
    "v_fmac_f32 v120, v116, v36\n\t"
    "v_fmac_f32 v121, v117, v37\n\t"
    "v_mfma_f32_16x16x32_bf16 v[100:103], v[216:219], v[192:195], v[100:103]\n\t"
    "v_fmac_f32 v120, v118, v38\n\t"
    "v_fmac_f32 v121, v119, v39\n\t"
    "v_mfma_f32_16x16x32_bf16 v[104:107], v[180:183], v[188:191], 0\n\t"
    "v_and_b32 v133, -16, v133\n\t"
    "v_fmac_f32 v120, v92, v40\n\t"
    "v_mfma_f32_16x16x32_bf16 v[104:107], v[184:187], v[192:195], v[104:107]\n\t"
    "v_fmac_f32 v121, v93, v41\n\t"
    "v_fmac_f32 v120, v94, v42\n\t"
    "v_mfma_f32_16x16x32_bf16 v[108:111], v[180:183], v[196:199], 0\n\t"
    "v_fmac_f32 v121, v95, v43\n\t"
    "v_fmac_f32 v120, v96, v44\n\t"
    "v_mfma_f32_16x16x32_bf16 v[108:111], v[184:187], v[200:203], v[108:111]\n\t"
    "v_fmac_f32 v121, v97, v45\n\t"
    "v_fmac_f32 v120, v98, v46\n\t"
    "v_mfma_f32_16x16x32_bf16 v[112:115], v[180:183], v[204:207], 0\n\t"
    "v_fmac_f32 v121, v99, v47\n\t"
    "v_fmac_f32 v120, v100, v88\n\t"
    "v_mfma_f32_16x16x32_bf16 v[112:115], v[184:187], v[208:211], v[112:115]\n\t"
    "v_mad_u32_u24 v134, v134, s68, v151\n\t"
    "v_and_b32 v134, -16, v134\n\t"
    "ds_read_b128 v[180:183], v132 offset:0\n\t"
    "ds_read_b128 v[184:187], v132 offset:64\n\t"
    "v_mfma_f32_16x16x32_bf16 v[116:119], v[212:215], v[196:199], 0\n\t"
    "v_fmac_f32 v121, v104, v48\n\t"
    "v_fmac_f32 v120, v105, v49\n\t"
    "v_mfma_f32_16x16x32_bf16 v[116:119], v[216:219], v[200:203], v[116:119]\n\t"
    "v_fmac_f32 v121, v106, v50\n\t"
    "v_fmac_f32 v120, v107, v51\n\t"
    "v_mfma_f32_16x16x32_bf16 v[92:95], v[188:191], v[188:191], 0\n\t"
    "v_mad_u32_u24 v135, v135, s68, v151\n\t"
    "v_and_b32 v135, -16, v135\n\t"
    "v_mfma_f32_16x16x32_bf16 v[92:95], v[192:195], v[192:195], v[92:95]\n\t"
    "v_fmac_f32 v121, v108, v52\n\t"
    "v_fmac_f32 v120, v109, v53\n\t"
    "v_mfma_f32_16x16x32_bf16 v[96:99], v[188:191], v[196:199], 0\n\t"
    "v_fmac_f32 v121, v110, v54\n\t"
    "v_fmac_f32 v120, v111, v55\n\t"
    "v_mfma_f32_16x16x32_bf16 v[96:99], v[192:195], v[200:203], v[96:99]\n\t"
    "v_fmac_f32 v121, v112, v56\n\t"
    "v_fmac_f32 v120, v113, v57\n\t"
    "v_mfma_f32_16x16x32_bf16 v[100:103], v[188:191], v[204:207], 0\n\t"
    "v_fmac_f32 v121, v114, v58\n\t"
    "v_fmac_f32 v120, v115, v59\n\t"
    "v_mfma_f32_16x16x32_bf16 v[100:103], v[192:195], v[208:211], v[100:103]\n\t"
    "v_fmac_f32 v121, v116, v89\n\t"
    "ds_read_b128 v[188:191], v133 offset:0\n\t"
    "ds_read_b128 v[192:195], v133 offset:64\n\t"
    "v_mfma_f32_16x16x32_bf16 v[104:107], v[212:215], v[204:207], 0\n\t"
    "v_fmac_f32 v120, v92, v60\n\t"
    "v_fmac_f32 v121, v93, v61\n\t"
    "v_mfma_f32_16x16x32_bf16 v[104:107], v[216:219], v[208:211], v[104:107]\n\t"
    "v_fmac_f32 v120, v94, v62\n\t"
    "v_fmac_f32 v121, v95, v63\n\t"
    "v_mfma_f32_16x16x32_bf16 v[108:111], v[196:199], v[196:199], 0\n\t"
    "v_fmac_f32 v120, v96, v64\n\t"
    "v_fmac_f32 v121, v97, v65\n\t"
    "v_mfma_f32_16x16x32_bf16 v[108:111], v[200:203], v[200:203], v[108:111]\n\t"
    "v_fmac_f32 v120, v98, v66\n\t"
    "v_fmac_f32 v121, v99, v67\n\t"
    "v_mfma_f32_16x16x32_bf16 v[112:115], v[196:199], v[204:207], 0\n\t"
    "v_fmac_f32 v120, v100, v68\n\t"
    "v_fmac_f32 v121, v101, v69\n\t"
    "v_mfma_f32_16x16x32_bf16 v[112:115], v[200:203], v[208:211], v[112:115]\n\t"
    "v_fmac_f32 v120, v102, v70\n\t"
    "v_fmac_f32 v121, v103, v71\n\t"
    "ds_read_b128 v[196:199], v134 offset:0\n\t"
    "ds_read_b128 v[200:203], v134 offset:64\n\t"
    "v_mfma_f32_16x16x32_bf16 v[116:119], v[204:207], v[204:207], 0\n\t"
    "v_fmac_f32 v120, v104, v90\n\t"
    "v_mfma_f32_16x16x32_bf16 v[116:119], v[208:211], v[208:211], v[116:119]\n\t"
    "ds_read_b128 v[204:207], v135 offset:0\n\t"
    "ds_read_b128 v[208:211], v135 offset:64\n\t"
    "s_mov_b32 s52, s53\n\t"
    "s_mov_b32 s53, s54\n\t"
    "s_mov_b32 s54, s55\n\t"
    "s_mov_b32 s55, s60\n\t"
    "s_cmp_lt_u32 s54, s64\n\t"
    "s_cbranch_scc1 L_pr1_%=\n\t"
    "s_setprio 0\n\t"
    "L_pr1_%=:\n\t"
    "s_cmp_lt_u32 s53, s64\n\t"
    "s_cbranch_scc1 L_pnn_%=\n\t"
    "L_done_%=:\n\t"
    "s_mul_i32 s69, s52, 272\n\t"
    "s_add_u32 s69, s69, 130048\n\t"
    "v_add_u32 v147, s69, v146\n\t"
    "v_fmac_f32 v121, v108, v72\n\t"
    "v_fmac_f32 v120, v109, v73\n\t"
    "v_fmac_f32 v121, v110, v74\n\t"
    "v_fmac_f32 v120, v111, v75\n\t"
    "v_fmac_f32 v121, v112, v76\n\t"
    "v_fmac_f32 v120, v113, v77\n\t"
    "v_fmac_f32 v121, v114, v78\n\t"
    "v_fmac_f32 v120, v115, v79\n\t"
    "v_fmac_f32 v121, v116, v80\n\t"
    "v_fmac_f32 v120, v117, v81\n\t"
    "v_fmac_f32 v121, v118, v82\n\t"
    "v_fmac_f32 v120, v119, v83\n\t"
    "v_add_f32 v154, v120, v121\n\t"
    "ds_write_b32 v147, v154\n\t"
    "v_lshrrev_b32 v147, 2, v146\n\t"
    "s_mov_b64 s[92:93], %17\n\t"
    "s_and_b32 s93, s93, 0xffff\n\t"
    "s_mov_b32 s94, 32768\n\t"
    "s_mov_b32 s95, 0x20000\n\t"
    "s_lshl_b32 s44, s37, 1\n\t"
    "v_and_b32 v155, 15, v147\n\t"
    "v_lshl_add_u32 v155, %16, 4, v155\n\t"
    "v_lshrrev_b32 v154, 4, v147\n\t"
    "v_mul_u32_u24 v122, 272, v155\n\t"
    "v_lshl_add_u32 v122, v154, 6, v122\n\t"
    "v_add_u32 v122, 130048, v122\n\t"
    "v_mul_u32_u24 v154, 2688, v154\n\t"
    "v_lshl_add_u32 v154, v155, 2, v154\n\t"
    "v_add_u32 v154, 152896, v154\n\t"
    "v_cmp_le_u32 vcc, 48, v147\n\t"
    "v_cmp_gt_u32 s[46:47], s38, v155\n\t"
    "v_lshlrev_b32 v155, 1, v155\n\t"
    "s_and_b64 vcc, vcc, s[46:47]\n\t"
    "s_waitcnt lgkmcnt(0)\n\t"
    "s_barrier\n\t"
    "s_cbranch_vccz L_fin_%=\n\t"
    "ds_read_b128 v[92:95], v122 offset:0\n\t"
    "ds_read_b128 v[96:99], v122 offset:16\n\t"
    "ds_read_b128 v[100:103], v122 offset:32\n\t"
    "ds_read_b128 v[104:107], v122 offset:48\n\t"
    "ds_read2_b32 v[108:109], v154 offset0:0 offset1:84\n\t"
    "ds_read2_b32 v[110:111], v154 offset0:168 offset1:252\n\t"
    "v_add_u32 v154, 1344, v154\n\t"
    "ds_read2_b32 v[112:113], v154 offset0:0 offset1:84\n\t"
    "ds_read2_b32 v[114:115], v154 offset0:168 offset1:252\n\t"
    "s_waitcnt lgkmcnt(6)\n\t"
    "v_add_f32 v92, v92, v96\n\t"
    "v_add_f32 v93, v93, v97\n\t"
    "v_add_f32 v94, v94, v98\n\t"
    "v_add_f32 v95, v95, v99\n\t"
    "s_waitcnt lgkmcnt(5)\n\t"
    "v_add_f32 v92, v92, v100\n\t"
    "v_add_f32 v93, v93, v101\n\t"
    "v_add_f32 v94, v94, v102\n\t"
    "v_add_f32 v95, v95, v103\n\t"
    "s_waitcnt lgkmcnt(4)\n\t"
    "v_add_f32 v92, v92, v104\n\t"
    "v_add_f32 v93, v93, v105\n\t"
    "v_add_f32 v94, v94, v106\n\t"
    "v_add_f32 v95, v95, v107\n\t"
    "s_waitcnt lgkmcnt(2)\n\t"
    "v_add_f32 v92, v92, v108\n\t"
    "v_add_f32 v93, v93, v109\n\t"
    "v_add_f32 v94, v94, v110\n\t"
    "v_add_f32 v95, v95, v111\n\t"
    "s_waitcnt lgkmcnt(0)\n\t"
    "v_add_f32 v92, v92, v112\n\t"
    "v_add_f32 v93, v93, v113\n\t"
    "v_add_f32 v94, v94, v114\n\t"
    "v_add_f32 v95, v95, v115\n\t"
    "v_add_f32 v92, v92, v93\n\t"
    "v_add_f32 v94, v94, v95\n\t"
    "v_add_f32 v92, v92, v94\n\t"
    "v_mov_b32 v93, v92\n\t"
    "s_nop 1\n\t"
    "v_permlane16_swap_b32 v93, v92\n\t"
    "s_nop 1\n\t"
    "v_add_f32 v92, v92, v93\n\t"
    "v_mov_b32 v93, v92\n\t"
    "s_nop 1\n\t"
    "v_permlane32_swap_b32 v93, v92\n\t"
    "s_nop 1\n\t"
    "v_add_f32 v92, v92, v93\n\t"
    "v_add_f32 v92, s39, v92\n\t"
    "v_cvt_pk_bf16_f32 v92, v92, v92\n\t"
    "s_mov_b64 exec, vcc\n\t"
    "buffer_store_short v92, v155, s[92:95], s44 offen\n\t"
    "s_mov_b64 exec, -1\n\t"
    "s_branch L_fin_%=\n\t"
    "L_req0_%=:\n\t"
    "s_cmp_lg_u32 s61, 0\n\t"
    "s_cbranch_scc1 L_req0b_%=\n\t"
    "s_bfe_u32 s65, s84, 0xc0002\n\t"
    "s_add_u32 s65, s65, s36\n\t"
    "buffer_store_dword v236, v125, %12, s65 offen offset:0 sc1\n\t"
    "buffer_store_dword v237, v125, %12, s65 offen offset:256 sc1\n\t"
    "s_bfe_u32 s65, s85, 0xc0002\n\t"
    "s_add_u32 s65, s65, s36\n\t"
    "buffer_store_dword v238, v125, %12, s65 offen offset:0 sc1\n\t"
    "buffer_store_dword v239, v125, %12, s65 offen offset:256 sc1\n\t"
    "s_bfe_u32 s65, s86, 0xc0002\n\t"
    "s_add_u32 s65, s65, s36\n\t"
    "buffer_store_dword v240, v125, %12, s65 offen offset:0 sc1\n\t"
    "buffer_store_dword v241, v125, %12, s65 offen offset:256 sc1\n\t"
    "s_bfe_u32 s65, s87, 0xc0002\n\t"
    "s_add_u32 s65, s65, s36\n\t"
    "buffer_store_dword v242, v125, %12, s65 offen offset:0 sc1\n\t"
    "buffer_store_dword v243, v125, %12, s65 offen offset:256 sc1\n\t"
    "s_bfe_u32 s65, s88, 0xc0002\n\t"
    "s_add_u32 s65, s65, s36\n\t"
    "buffer_store_dword v244, v125, %12, s65 offen offset:0 sc1\n\t"
    "buffer_store_dword v245, v125, %12, s65 offen offset:256 sc1\n\t"
    "s_bfe_u32 s65, s89, 0xc0002\n\t"
    "s_add_u32 s65, s65, s36\n\t"
    "buffer_store_dword v246, v125, %12, s65 offen offset:0 sc1\n\t"
    "buffer_store_dword v247, v125, %12, s65 offen offset:256 sc1\n\t"
    "s_bfe_u32 s65, s90, 0xc0002\n\t"
    "s_add_u32 s65, s65, s36\n\t"
    "buffer_store_dword v248, v125, %12, s65 offen offset:0 sc1\n\t"
    "buffer_store_dword v249, v125, %12, s65 offen offset:256 sc1\n\t"
    "s_bfe_u32 s65, s91, 0xc0002\n\t"
    "s_add_u32 s65, s65, s36\n\t"
    "buffer_store_dword v250, v125, %12, s65 offen offset:0 sc1\n\t"
    "buffer_store_dword v251, v125, %12, s65 offen offset:256 sc1\n\t"
    "s_sub_u32 s61, s64, 3\n\t"
    "s_branch L_back0_%=\n\t"
    "L_req0b_%=:\n\t"
    "s_cmp_lg_u32 s61, 1\n\t"
    "s_cbranch_scc1 L_req0c_%=\n\t"
    "v_lshrrev_b32 v154, 16, v122\n\t"
    "v_lshl_add_u32 v154, %16, 2, v154\n\t"
    "v_mul_u32_u24 v154, 336, v154\n\t"
    "v_and_b32 v122, 0xf0, v122\n\t"
    "v_cmp_gt_u32 vcc, 80, v122\n\t"
    "v_add3_u32 v122, v122, v154, s45\n\t"
    "s_mov_b32 s61, -1\n\t"
    "v_add_f32 v124, v220, v224\n\t"
    "v_add_f32 v154, v228, v232\n\t"
    "v_add_f32 v124, v124, v154\n\t"
    "v_add_f32 v125, v221, v225\n\t"
    "v_add_f32 v154, v229, v233\n\t"
    "v_add_f32 v125, v125, v154\n\t"
    "v_add_f32 v126, v222, v226\n\t"
    "v_add_f32 v154, v230, v234\n\t"
    "v_add_f32 v126, v126, v154\n\t"
    "v_add_f32 v127, v223, v227\n\t"
    "v_add_f32 v154, v231, v235\n\t"
    "v_add_f32 v127, v127, v154\n\t"
    "ds_write_b128 v122, v[124:127] offset:0\n\t"
    "s_cmp_le_u32 s38, 64\n\t"
    "s_cbranch_scc1 L_back0_%=\n\t"
    "v_add_f32 v124, v236, v240\n\t"
    "v_add_f32 v154, v244, v248\n\t"
    "v_add_f32 v124, v124, v154\n\t"
    "v_add_f32 v125, v237, v241\n\t"
    "v_add_f32 v154, v245, v249\n\t"
    "v_add_f32 v125, v125, v154\n\t"
    "v_add_f32 v126, v238, v242\n\t"
    "v_add_f32 v154, v246, v250\n\t"
    "v_add_f32 v126, v126, v154\n\t"
    "v_add_f32 v127, v239, v243\n\t"
    "v_add_f32 v154, v247, v251\n\t"
    "v_add_f32 v127, v127, v154\n\t"
    "s_and_saveexec_b64 s[46:47], vcc\n\t"
    "ds_write_b128 v122, v[124:127] offset:256\n\t"
    "s_mov_b64 exec, s[46:47]\n\t"
    "s_branch L_back0_%=\n\t"
    "L_req0c_%=:\n\t"
    "s_lshl_b32 s46, %14, 7\n\t"
    "s_add_u32 s46, s46, 49295360\n\t"
    "s_mov_b32 s47, 1048576\n\t"
    "s_mov_b64 exec, 1\n\t"
    "s_cmp_ge_u32 s55, s64\n\t"
    "s_cbranch_scc1 L_pq0_%=\n\t"
    "buffer_load_dword v123, off, %12, s46 sc1\n\t"
    "s_mov_b64 exec, -1\n\t"
    "s_branch L_back0_%=\n\t"
    "L_pq0_%=:\n\t"
    "v_cmp_eq_u32 vcc, 0, v123\n\t"
    "s_cbranch_vccz L_in0_%=\n\t"
    "L_poll0_%=:\n\t"
    "buffer_load_dword v155, off, %12, s46 sc1\n\t"
    "s_sub_u32 s47, s47, 1\n\t"
    "s_cmp_eq_u32 s47, 0\n\t"
    "s_cbranch_scc1 L_in0_%=\n\t"
    "s_waitcnt vmcnt(0)\n\t"
    "v_cmp_eq_u32 vcc, 0, v155\n\t"
    "s_cbranch_vccz L_in0_%=\n\t"
    "s_sleep 8\n\t"
    "s_branch L_poll0_%=\n\t"
    "L_in0_%=:\n\t"
    "s_mov_b64 exec, -1\n\t"
    "s_lshl_b32 s46, s37, 2\n\t"
    "s_mul_i32 s47, %16, 1048576\n\t"
    "s_add_u32 s46, s46, s47\n\t"
    "s_add_u32 s46, s46, 17825792\n\t"
    "v_and_b32 v155, 0x80, v122\n\t"
    "v_lshl_add_u32 v155, v155, 23, v122\n\t"
    "buffer_load_dwordx4 v[220:223], v122, %12, s46 offen sc1\n\t"
    "s_add_u32 s46, s46, 262144\n\t"
    "buffer_load_dwordx4 v[224:227], v122, %12, s46 offen sc1\n\t"
    "s_add_u32 s46, s46, 262144\n\t"
    "buffer_load_dwordx4 v[228:231], v122, %12, s46 offen sc1\n\t"
    "s_add_u32 s46, s46, 262144\n\t"
    "buffer_load_dwordx4 v[232:235], v122, %12, s46 offen sc1\n\t"
    "s_add_u32 s46, s46, 262144\n\t"
    "s_mov_b32 s61, 1\n\t"
    "s_cmp_le_u32 s38, 64\n\t"
    "s_cbranch_scc1 L_back0_%=\n\t"
    "s_sub_u32 s46, s46, 262144\n\t"
    "buffer_load_dwordx4 v[248:251], v155, %12, s46 offen offset:256 sc1\n\t"
    "s_sub_u32 s46, s46, 262144\n\t"
    "buffer_load_dwordx4 v[244:247], v155, %12, s46 offen offset:256 sc1\n\t"
    "s_sub_u32 s46, s46, 262144\n\t"
    "buffer_load_dwordx4 v[240:243], v155, %12, s46 offen offset:256 sc1\n\t"
    "s_sub_u32 s46, s46, 262144\n\t"
    "buffer_load_dwordx4 v[236:239], v155, %12, s46 offen offset:256 sc1\n\t"
    "s_branch L_back0_%=\n\t"
    "L_req1_%=:\n\t"
    "s_cmp_lg_u32 s61, 1\n\t"
    "s_cbranch_scc1 L_req1c_%=\n\t"
    "v_lshrrev_b32 v154, 16, v122\n\t"
    "v_lshl_add_u32 v154, %16, 2, v154\n\t"
    "v_mul_u32_u24 v154, 336, v154\n\t"
    "v_and_b32 v122, 0xf0, v122\n\t"
    "v_cmp_gt_u32 vcc, 80, v122\n\t"
    "v_add3_u32 v122, v122, v154, s45\n\t"
    "s_mov_b32 s61, -1\n\t"
    "v_add_f32 v124, v220, v224\n\t"
    "v_add_f32 v154, v228, v232\n\t"
    "v_add_f32 v124, v124, v154\n\t"
    "v_add_f32 v125, v221, v225\n\t"
    "v_add_f32 v154, v229, v233\n\t"
    "v_add_f32 v125, v125, v154\n\t"
    "v_add_f32 v126, v222, v226\n\t"
    "v_add_f32 v154, v230, v234\n\t"
    "v_add_f32 v126, v126, v154\n\t"
    "v_add_f32 v127, v223, v227\n\t"
    "v_add_f32 v154, v231, v235\n\t"
    "v_add_f32 v127, v127, v154\n\t"
    "ds_write_b128 v122, v[124:127] offset:0\n\t"
    "s_cmp_le_u32 s38, 64\n\t"
    "s_cbranch_scc1 L_back1_%=\n\t"
    "v_add_f32 v124, v236, v240\n\t"
    "v_add_f32 v154, v244, v248\n\t"
    "v_add_f32 v124, v124, v154\n\t"
    "v_add_f32 v125, v237, v241\n\t"
    "v_add_f32 v154, v245, v249\n\t"
    "v_add_f32 v125, v125, v154\n\t"
    "v_add_f32 v126, v238, v242\n\t"
    "v_add_f32 v154, v246, v250\n\t"
    "v_add_f32 v126, v126, v154\n\t"
    "v_add_f32 v127, v239, v243\n\t"
    "v_add_f32 v154, v247, v251\n\t"
    "v_add_f32 v127, v127, v154\n\t"
    "s_and_saveexec_b64 s[46:47], vcc\n\t"
    "ds_write_b128 v122, v[124:127] offset:256\n\t"
    "s_mov_b64 exec, s[46:47]\n\t"
    "s_branch L_back1_%=\n\t"
    "L_req1c_%=:\n\t"
    "s_lshl_b32 s46, %14, 7\n\t"
    "s_add_u32 s46, s46, 49295360\n\t"
    "s_mov_b32 s47, 1048576\n\t"
    "s_mov_b64 exec, 1\n\t"
    "s_cmp_ge_u32 s55, s64\n\t"
    "s_cbranch_scc1 L_pq1_%=\n\t"
    "buffer_load_dword v123, off, %12, s46 sc1\n\t"
    "s_mov_b64 exec, -1\n\t"
    "s_branch L_back1_%=\n\t"
    "L_pq1_%=:\n\t"
    "v_cmp_eq_u32 vcc, 0, v123\n\t"
    "s_cbranch_vccz L_in1_%=\n\t"
    "L_poll1_%=:\n\t"
    "buffer_load_dword v155, off, %12, s46 sc1\n\t"
    "s_sub_u32 s47, s47, 1\n\t"
    "s_cmp_eq_u32 s47, 0\n\t"
    "s_cbranch_scc1 L_in1_%=\n\t"
    "s_waitcnt vmcnt(0)\n\t"
    "v_cmp_eq_u32 vcc, 0, v155\n\t"
    "s_cbranch_vccz L_in1_%=\n\t"
    "s_sleep 8\n\t"
    "s_branch L_poll1_%=\n\t"
    "L_in1_%=:\n\t"
    "s_mov_b64 exec, -1\n\t"
    "s_lshl_b32 s46, s37, 2\n\t"
    "s_mul_i32 s47, %16, 1048576\n\t"
    "s_add_u32 s46, s46, s47\n\t"
    "s_add_u32 s46, s46, 17825792\n\t"
    "v_and_b32 v155, 0x80, v122\n\t"
    "v_lshl_add_u32 v155, v155, 23, v122\n\t"
    "buffer_load_dwordx4 v[220:223], v122, %12, s46 offen sc1\n\t"
    "s_add_u32 s46, s46, 262144\n\t"
    "buffer_load_dwordx4 v[224:227], v122, %12, s46 offen sc1\n\t"
    "s_add_u32 s46, s46, 262144\n\t"
    "buffer_load_dwordx4 v[228:231], v122, %12, s46 offen sc1\n\t"
    "s_add_u32 s46, s46, 262144\n\t"
    "buffer_load_dwordx4 v[232:235], v122, %12, s46 offen sc1\n\t"
    "s_add_u32 s46, s46, 262144\n\t"
    "s_mov_b32 s61, 1\n\t"
    "s_cmp_le_u32 s38, 64\n\t"
    "s_cbranch_scc1 L_back1_%=\n\t"
    "s_sub_u32 s46, s46, 262144\n\t"
    "buffer_load_dwordx4 v[248:251], v155, %12, s46 offen offset:256 sc1\n\t"
    "s_sub_u32 s46, s46, 262144\n\t"
    "buffer_load_dwordx4 v[244:247], v155, %12, s46 offen offset:256 sc1\n\t"
    "s_sub_u32 s46, s46, 262144\n\t"
    "buffer_load_dwordx4 v[240:243], v155, %12, s46 offen offset:256 sc1\n\t"
    "s_sub_u32 s46, s46, 262144\n\t"
    "buffer_load_dwordx4 v[236:239], v155, %12, s46 offen offset:256 sc1\n\t"
    "s_branch L_back1_%=\n\t"
    "L_sig_%=:\n\t"
    "v_subrev_u32 v155, s49, v149\n\t"
    "v_add_u32 v155, 1024, v155\n\t"
    "s_waitcnt vmcnt(0)\n\t"
    "ds_add_rtn_u32 v148, v155, v150\n\t"
    "s_mov_b32 s45, 152896\n\t"
    "s_mov_b32 s46, 49283072\n\t"
    "v_mov_b32 v155, 1\n\t"
    "s_waitcnt lgkmcnt(0)\n\t"
    "v_readfirstlane_b32 s47, v148\n\t"
    "s_nop 0\n\t"
    "s_cmp_lg_u32 s47, 7\n\t"
    "s_cbranch_scc1 L_sigb_%=\n\t"
    "s_mov_b64 exec, 1\n\t"
    "buffer_atomic_add v155, off, %12, s46 sc0 sc1\n\t"
    "s_mov_b64 exec, -1\n\t"
    "s_waitcnt vmcnt(0)\n\t"
    "v_readfirstlane_b32 s47, v155\n\t"
    "v_lshlrev_b32 v155, 5, v146\n\t"
    "s_cmp_lg_u32 s47, 255\n\t"
    "s_cbranch_scc1 L_sigb_%=\n\t"
    "s_mov_b32 s46, 49295360\n\t"
    "buffer_store_dword v150, v155, %12, s46 offen sc1\n\t"
    "s_add_u32 s46, s46, 0x2000\n\t"
    "buffer_store_dword v150, v155, %12, s46 offen sc1\n\t"
    "s_add_u32 s46, s46, 0x2000\n\t"
    "buffer_store_dword v150, v155, %12, s46 offen sc1\n\t"
    "s_add_u32 s46, s46, 0x2000\n\t"
    "buffer_store_dword v150, v155, %12, s46 offen sc1\n\t"
    "s_add_u32 s46, s46, 0x2000\n\t"
    "s_branch L_sigb_%=\n\t"
    "L_fin_%=:\n\t"
    "s_branch L_pend_%=\n\t"
    "L_pub_%=:\n\t"
    "s_memrealtime s[46:47]\n\t"
    "s_lshl_b32 s49, %14, 2\n\t"
    "s_add_u32 s49, s49, 49293312\n\t"
    "s_waitcnt lgkmcnt(0)\n\t"
    "s_sub_u32 s47, s46, s44\n\t"
    "s_mul_i32 s47, s47, 320\n\t"
    "s_lshr_b32 s47, s47, 8\n\t"
    "s_add_u32 s46, s46, s47\n\t"
    "s_or_b32 s46, s46, 1\n\t"
    "v_mov_b32 v83, s46\n\t"
    "s_mov_b64 exec, 1\n\t"
    "buffer_store_dword v83, off, %12, s49 sc1\n\t"
    "s_mov_b64 exec, -1\n\t"
    "s_branch L_pubb_%=\n\t"
    "L_part0_%=:\n\t"
    "s_mov_b32 s47, 0\n\t"
    "s_call_b64 s[98:99], L_bld_%=\n\t"
    "s_branch L_partb0_%=\n\t"
    "L_part1_%=:\n\t"
    "s_mov_b32 s47, 1048576\n\t"
    "s_waitcnt vmcnt(8)\n\t"
    "s_call_b64 s[98:99], L_bchk_%=\n\t"
    "s_branch L_partb1_%=\n\t"
    "L_bchk_%=:\n\t"
    "v_min_u32 v132, v128, v129\n\t"
    "v_min3_u32 v132, v132, v130, v131\n\t"
    "v_cmp_eq_u32 vcc, 0, v132\n\t"
    "s_cbranch_vccz L_bok_%=\n\t"
    "L_bld_%=:\n\t"
    "s_mov_b32 s46, 49293312\n\t"
    "buffer_load_dwordx4 v[128:131], v127, %12, s46 offen sc1\n\t"
    "s_cmp_eq_u32 s47, 0\n\t"
    "s_cbranch_scc1 L_bret_%=\n\t"
    "s_sub_u32 s47, s47, 1\n\t"
    "s_sleep 16\n\t"
    "s_waitcnt vmcnt(0)\n\t"
    "s_branch L_bchk_%=\n\t"
    "L_bok_%=:\n\t"
    "v_readfirstlane_b32 s46, v128\n\t"
    "v_mov_b32 v138, -4096\n\t"
    "v_mov_b32 v139, 4096\n\t"
    "v_subrev_u32 v128, s46, v128\n\t"
    "v_subrev_u32 v129, s46, v129\n\t"
    "v_subrev_u32 v130, s46, v130\n\t"
    "v_subrev_u32 v131, s46, v131\n\t"
    "v_writelane_b32 v128, 50, 0\n\t"
    "v_add3_u32 v132, v128, v129, v130\n\t"
    "v_add_u32 v132, v132, v131\n\t"
    "s_nop 1\n\t"
    "v_add_u32_dpp v132, v132, v132 row_shr:1 row_mask:0xf bank_mask:0xf\n\t"
    "s_nop 1\n\t"
    "v_add_u32_dpp v132, v132, v132 row_shr:2 row_mask:0xf bank_mask:0xf\n\t"
    "s_nop 1\n\t"
    "v_add_u32_dpp v132, v132, v132 row_shr:4 row_mask:0xf bank_mask:0xf\n\t"
    "s_nop 1\n\t"
    "v_add_u32_dpp v132, v132, v132 row_shr:8 row_mask:0xf bank_mask:0xf\n\t"
    "s_nop 1\n\t"
    "v_add_u32_dpp v132, v132, v132 row_bcast:15 row_mask:0xa bank_mask:0xf\n\t"
    "s_nop 1\n\t"
    "v_add_u32_dpp v132, v132, v132 row_bcast:31 row_mask:0xc bank_mask:0xf\n\t"
    "s_nop 0\n\t"
    "v_readlane_b32 s46, v132, 63\n\t"
    "s_movk_i32 s47, 20\n\t"
    "v_mov_b32 v133, -8192\n\t"
    "v_mov_b32 v134, 5120\n\t"
    "s_ashr_i32 s46, s46, 8\n\t"
    "v_sub_u32 v128, s46, v128\n\t"
    "v_med3_i32 v128, v128, v138, v139\n\t"
    "v_mul_i32_i24 v128, s47, v128\n\t"
    "v_med3_i32 v128, v128, v133, v134\n\t"
    "v_and_b32 v140, 0x3ff, v128\n\t"
    "v_ashrrev_i32 v128, 10, v128\n\t"
    "v_sub_u32 v129, s46, v129\n\t"
    "v_med3_i32 v129, v129, v138, v139\n\t"
    "v_mul_i32_i24 v129, s47, v129\n\t"
    "v_med3_i32 v129, v129, v133, v134\n\t"
    "v_and_b32 v141, 0x3ff, v129\n\t"
    "v_ashrrev_i32 v129, 10, v129\n\t"
    "v_sub_u32 v130, s46, v130\n\t"
    "v_med3_i32 v130, v130, v138, v139\n\t"
    "v_mul_i32_i24 v130, s47, v130\n\t"
    "v_med3_i32 v130, v130, v133, v134\n\t"
    "v_and_b32 v142, 0x3ff, v130\n\t"
    "v_ashrrev_i32 v130, 10, v130\n\t"
    "v_sub_u32 v131, s46, v131\n\t"
    "v_med3_i32 v131, v131, v138, v139\n\t"
    "v_mul_i32_i24 v131, s47, v131\n\t"
    "v_med3_i32 v131, v131, v133, v134\n\t"
    "v_and_b32 v143, 0x3ff, v131\n\t"
    "v_ashrrev_i32 v131, 10, v131\n\t"
    "v_add3_u32 v132, v128, v129, v130\n\t"
    "v_add_u32 v132, v132, v131\n\t"
    "s_nop 1\n\t"
    "v_add_u32_dpp v132, v132, v132 row_shr:1 row_mask:0xf bank_mask:0xf\n\t"
    "s_nop 1\n\t"
    "v_add_u32_dpp v132, v132, v132 row_shr:2 row_mask:0xf bank_mask:0xf\n\t"
    "s_nop 1\n\t"
    "v_add_u32_dpp v132, v132, v132 row_shr:4 row_mask:0xf bank_mask:0xf\n\t"
    "s_nop 1\n\t"
    "v_add_u32_dpp v132, v132, v132 row_shr:8 row_mask:0xf bank_mask:0xf\n\t"
    "s_nop 1\n\t"
    "v_add_u32_dpp v132, v132, v132 row_bcast:15 row_mask:0xa bank_mask:0xf\n\t"
    "s_nop 1\n\t"
    "v_add_u32_dpp v132, v132, v132 row_bcast:31 row_mask:0xc bank_mask:0xf\n\t"
    "s_nop 0\n\t"
    "v_readlane_b32 s46, v132, 63\n\t"
    "s_movk_i32 s47, 0x400\n\t"
    "s_sub_u32 s48, s47, 512\n\t"
    "s_mov_b32 s49, s46\n\t"
    "v_cmp_le_u32 vcc, s48, v140\n\t"
    "s_bcnt1_i32_b64 s44, vcc\n\t"
    "s_add_u32 s49, s49, s44\n\t"
    "v_cmp_le_u32 vcc, s48, v141\n\t"
    "s_bcnt1_i32_b64 s44, vcc\n\t"
    "s_add_u32 s49, s49, s44\n\t"
    "v_cmp_le_u32 vcc, s48, v142\n\t"
    "s_bcnt1_i32_b64 s44, vcc\n\t"
    "s_add_u32 s49, s49, s44\n\t"
    "v_cmp_le_u32 vcc, s48, v143\n\t"
    "s_bcnt1_i32_b64 s44, vcc\n\t"
    "s_add_u32 s49, s49, s44\n\t"
    "s_cmp_lt_i32 s49, 0\n\t"
    "s_cselect_b32 s47, s48, s47\n\t"
    "s_sub_u32 s48, s47, 256\n\t"
    "s_mov_b32 s49, s46\n\t"
    "v_cmp_le_u32 vcc, s48, v140\n\t"
    "s_bcnt1_i32_b64 s44, vcc\n\t"
    "s_add_u32 s49, s49, s44\n\t"
    "v_cmp_le_u32 vcc, s48, v141\n\t"
    "s_bcnt1_i32_b64 s44, vcc\n\t"
    "s_add_u32 s49, s49, s44\n\t"
    "v_cmp_le_u32 vcc, s48, v142\n\t"
    "s_bcnt1_i32_b64 s44, vcc\n\t"
    "s_add_u32 s49, s49, s44\n\t"
    "v_cmp_le_u32 vcc, s48, v143\n\t"
    "s_bcnt1_i32_b64 s44, vcc\n\t"
    "s_add_u32 s49, s49, s44\n\t"
    "s_cmp_lt_i32 s49, 0\n\t"
    "s_cselect_b32 s47, s48, s47\n\t"
    "s_sub_u32 s48, s47, 128\n\t"
    "s_mov_b32 s49, s46\n\t"
    "v_cmp_le_u32 vcc, s48, v140\n\t"
    "s_bcnt1_i32_b64 s44, vcc\n\t"
    "s_add_u32 s49, s49, s44\n\t"
    "v_cmp_le_u32 vcc, s48, v141\n\t"
    "s_bcnt1_i32_b64 s44, vcc\n\t"
    "s_add_u32 s49, s49, s44\n\t"
    "v_cmp_le_u32 vcc, s48, v142\n\t"
    "s_bcnt1_i32_b64 s44, vcc\n\t"
    "s_add_u32 s49, s49, s44\n\t"
    "v_cmp_le_u32 vcc, s48, v143\n\t"
    "s_bcnt1_i32_b64 s44, vcc\n\t"
    "s_add_u32 s49, s49, s44\n\t"
    "s_cmp_lt_i32 s49, 0\n\t"
    "s_cselect_b32 s47, s48, s47\n\t"
    "s_sub_u32 s48, s47, 64\n\t"
    "s_mov_b32 s49, s46\n\t"
    "v_cmp_le_u32 vcc, s48, v140\n\t"
    "s_bcnt1_i32_b64 s44, vcc\n\t"
    "s_add_u32 s49, s49, s44\n\t"
    "v_cmp_le_u32 vcc, s48, v141\n\t"
    "s_bcnt1_i32_b64 s44, vcc\n\t"
    "s_add_u32 s49, s49, s44\n\t"
    "v_cmp_le_u32 vcc, s48, v142\n\t"
    "s_bcnt1_i32_b64 s44, vcc\n\t"
    "s_add_u32 s49, s49, s44\n\t"
    "v_cmp_le_u32 vcc, s48, v143\n\t"
    "s_bcnt1_i32_b64 s44, vcc\n\t"
    "s_add_u32 s49, s49, s44\n\t"
    "s_cmp_lt_i32 s49, 0\n\t"
    "s_cselect_b32 s47, s48, s47\n\t"
    "s_sub_u32 s48, s47, 32\n\t"
    "s_mov_b32 s49, s46\n\t"
    "v_cmp_le_u32 vcc, s48, v140\n\t"
    "s_bcnt1_i32_b64 s44, vcc\n\t"
    "s_add_u32 s49, s49, s44\n\t"
    "v_cmp_le_u32 vcc, s48, v141\n\t"
    "s_bcnt1_i32_b64 s44, vcc\n\t"
    "s_add_u32 s49, s49, s44\n\t"
    "v_cmp_le_u32 vcc, s48, v142\n\t"
    "s_bcnt1_i32_b64 s44, vcc\n\t"
    "s_add_u32 s49, s49, s44\n\t"
    "v_cmp_le_u32 vcc, s48, v143\n\t"
    "s_bcnt1_i32_b64 s44, vcc\n\t"
    "s_add_u32 s49, s49, s44\n\t"
    "s_cmp_lt_i32 s49, 0\n\t"
    "s_cselect_b32 s47, s48, s47\n\t"
    "s_sub_u32 s47, s47, 32\n\t"
    "s_max_u32 s47, s47, 1\n\t"
    "v_cmp_le_u32 vcc, s47, v140\n\t"
    "s_nop 1\n\t"
    "v_addc_co_u32 v128, vcc, 0, v128, vcc\n\t"
    "v_cmp_le_u32 vcc, s47, v141\n\t"
    "s_nop 1\n\t"
    "v_addc_co_u32 v129, vcc, 0, v129, vcc\n\t"
    "v_cmp_le_u32 vcc, s47, v142\n\t"
    "s_nop 1\n\t"
    "v_addc_co_u32 v130, vcc, 0, v130, vcc\n\t"
    "v_cmp_le_u32 vcc, s47, v143\n\t"
    "s_nop 1\n\t"
    "v_addc_co_u32 v131, vcc, 0, v131, vcc\n\t"
    "v_add3_u32 v132, v128, v129, v130\n\t"
    "v_add_u32 v132, v132, v131\n\t"
    "s_nop 1\n\t"
    "v_add_u32_dpp v132, v132, v132 row_shr:1 row_mask:0xf bank_mask:0xf\n\t"
    "s_nop 1\n\t"
    "v_add_u32_dpp v132, v132, v132 row_shr:2 row_mask:0xf bank_mask:0xf\n\t"
    "s_nop 1\n\t"
    "v_add_u32_dpp v132, v132, v132 row_shr:4 row_mask:0xf bank_mask:0xf\n\t"
    "s_nop 1\n\t"
    "v_add_u32_dpp v132, v132, v132 row_shr:8 row_mask:0xf bank_mask:0xf\n\t"
    "s_nop 1\n\t"
    "v_add_u32_dpp v132, v132, v132 row_bcast:15 row_mask:0xa bank_mask:0xf\n\t"
    "s_nop 1\n\t"
    "v_add_u32_dpp v132, v132, v132 row_bcast:31 row_mask:0xc bank_mask:0xf\n\t"
    "s_nop 0\n\t"
    "v_readlane_b32 s46, v132, 63\n\t"
    "v_lshrrev_b32 v135, 2, v127\n\t"
    "s_cmp_le_u32 s46, 256\n\t"
    "s_cselect_b32 s47, -1, 0\n\t"
    "v_add_u32 v133, 4, v135\n\t"
    "v_mul_u32_u24 v134, s46, v133\n\t"
    "v_lshrrev_b32 v134, 8, v134\n\t"
    "v_sub_u32 v134, v132, v134\n\t"
    "v_and_b32 v134, s47, v134\n\t"
    "v_lshl_add_u32 v136, v133, 4, v134\n\t"
    "v_sub_u32 v132, v132, v131\n\t"
    "v_add_u32 v133, 3, v135\n\t"
    "v_mul_u32_u24 v134, s46, v133\n\t"
    "v_lshrrev_b32 v134, 8, v134\n\t"
    "v_sub_u32 v134, v132, v134\n\t"
    "v_and_b32 v134, s47, v134\n\t"
    "v_lshl_add_u32 v137, v133, 4, v134\n\t"
    "v_lshl_or_b32 v143, v136, 16, v137\n\t"
    "v_sub_u32 v132, v132, v130\n\t"
    "v_add_u32 v133, 2, v135\n\t"
    "v_mul_u32_u24 v134, s46, v133\n\t"
    "v_lshrrev_b32 v134, 8, v134\n\t"
    "v_sub_u32 v134, v132, v134\n\t"
    "v_and_b32 v134, s47, v134\n\t"
    "v_lshl_add_u32 v136, v133, 4, v134\n\t"
    "v_lshl_or_b32 v142, v137, 16, v136\n\t"
    "v_sub_u32 v132, v132, v129\n\t"
    "v_add_u32 v133, 1, v135\n\t"
    "v_mul_u32_u24 v134, s46, v133\n\t"
    "v_lshrrev_b32 v134, 8, v134\n\t"
    "v_sub_u32 v134, v132, v134\n\t"
    "v_and_b32 v134, s47, v134\n\t"
    "v_lshl_add_u32 v137, v133, 4, v134\n\t"
    "v_lshl_or_b32 v141, v136, 16, v137\n\t"
    "v_sub_u32 v132, v132, v128\n\t"
    "v_add_u32 v133, 0, v135\n\t"
    "v_mul_u32_u24 v134, s46, v133\n\t"
    "v_lshrrev_b32 v134, 8, v134\n\t"
    "v_sub_u32 v134, v132, v134\n\t"
    "v_and_b32 v134, s47, v134\n\t"
    "v_lshl_add_u32 v136, v133, 4, v134\n\t"
    "v_lshl_or_b32 v140, v137, 16, v136\n\t"
    "s_mov_b32 s46, 49294336\n\t"
    "buffer_store_dwordx4 v[140:143], v127, %12, s46 offen sc1\n\t"
    "s_mov_b32 s97, 1\n\t"
    "L_bret_%=:\n\t"
    "s_setpc_b64 s[98:99]\n\t"
    "L_rw_%=:\n\t"
    "s_mov_b32 s47, 1048576\n\t"
    "L_rw2_%=:\n\t"
    "s_sleep 16\n\t"
    "s_lshl_b32 s46, %14, 2\n\t"
    "s_add_u32 s46, s46, 49294336\n\t"
    "s_mov_b64 exec, 1\n\t"
    "buffer_load_dword v144, off, %12, s46 sc1\n\t"
    "s_mov_b64 exec, -1\n\t"
    "s_waitcnt vmcnt(0)\n\t"
    "v_readfirstlane_b32 s38, v144\n\t"
    "s_sub_u32 s47, s47, 1\n\t"
    "s_cmp_eq_u32 s47, 0\n\t"
    "s_cselect_b32 s38, 0x100000, s38\n\t"
    "s_cmp_eq_u32 s38, 0\n\t"
    "s_cbranch_scc1 L_rw2_%=\n\t"
    "s_branch L_rok_%=\n\t"
    "L_xw0_%=:\n\t"
    "s_mov_b32 s68, 1048576\n\t"
    "L_xv0_%=:\n\t"
    "s_sleep 4\n\t"
    "s_mov_b64 exec, 1\n\t"
    "buffer_load_dword v164, off, %12, s93 offset:0 sc1\n\t"
    "s_mov_b64 exec, -1\n\t"
    "s_sub_u32 s68, s68, 1\n\t"
    "s_cmp_eq_u32 s68, 0\n\t"
    "s_cbranch_scc1 L_xwb0_%=\n\t"
    "s_waitcnt vmcnt(0)\n\t"
    "v_readfirstlane_b32 s65, v164\n\t"
    "s_nop 0\n\t"
    "s_cmp_eq_u32 s65, 0\n\t"
    "s_cbranch_scc1 L_xv0_%=\n\t"
    "s_branch L_xwb0_%=\n\t"
    "L_xw1_%=:\n\t"
    "s_mov_b32 s68, 1048576\n\t"
    "L_xv1_%=:\n\t"
    "s_sleep 4\n\t"
    "s_mov_b64 exec, 1\n\t"
    "buffer_load_dword v164, off, %12, s93 offset:128 sc1\n\t"
    "s_mov_b64 exec, -1\n\t"
    "s_sub_u32 s68, s68, 1\n\t"
    "s_cmp_eq_u32 s68, 0\n\t"
    "s_cbranch_scc1 L_xwb1_%=\n\t"
    "s_waitcnt vmcnt(0)\n\t"
    "v_readfirstlane_b32 s65, v164\n\t"
    "s_nop 0\n\t"
    "s_cmp_eq_u32 s65, 0\n\t"
    "s_cbranch_scc1 L_xv1_%=\n\t"
    "s_branch L_xwb1_%=\n\t"
    "L_xw2_%=:\n\t"
    "s_mov_b32 s68, 1048576\n\t"
    "L_xv2_%=:\n\t"
    "s_sleep 4\n\t"
    "s_mov_b64 exec, 1\n\t"
    "buffer_load_dword v164, off, %12, s93 offset:256 sc1\n\t"
    "s_mov_b64 exec, -1\n\t"
    "s_sub_u32 s68, s68, 1\n\t"
    "s_cmp_eq_u32 s68, 0\n\t"
    "s_cbranch_scc1 L_xwb2_%=\n\t"
    "s_waitcnt vmcnt(0)\n\t"
    "v_readfirstlane_b32 s65, v164\n\t"
    "s_nop 0\n\t"
    "s_cmp_eq_u32 s65, 0\n\t"
    "s_cbranch_scc1 L_xv2_%=\n\t"
    "s_branch L_xwb2_%=\n\t"
    "L_xw3_%=:\n\t"
    "s_mov_b32 s68, 1048576\n\t"
    "L_xv3_%=:\n\t"
    "s_sleep 4\n\t"
    "s_mov_b64 exec, 1\n\t"
    "buffer_load_dword v164, off, %12, s93 offset:384 sc1\n\t"
    "s_mov_b64 exec, -1\n\t"
    "s_sub_u32 s68, s68, 1\n\t"
    "s_cmp_eq_u32 s68, 0\n\t"
    "s_cbranch_scc1 L_xwb3_%=\n\t"
    "s_waitcnt vmcnt(0)\n\t"
    "v_readfirstlane_b32 s65, v164\n\t"
    "s_nop 0\n\t"
    "s_cmp_eq_u32 s65, 0\n\t"
    "s_cbranch_scc1 L_xv3_%=\n\t"
    "s_branch L_xwb3_%=\n\t"
    "L_mp2_%=:\n\t"
    "v_lshrrev_b32 v57, 4, v127\n\t"
    "v_add_u32 v57, 32, v57\n\t"
    "v_mov_b32 v58, 64\n\t"
    "v_lshlrev_b32 v57, 7, v57\n\t"
    "s_mov_b32 s65, 49283072\n\t"
    "v_mov_b32 v56, 256\n\t"
    "s_mov_b32 s66, 1048576\n\t"
    "s_mov_b32 s65, 49283072\n\t"
    "s_mov_b64 exec, 15\n\t"
    "buffer_load_dword v56, v57, %12, s65 offen sc1\n\t"
    "s_mov_b64 exec, -1\n\t"
    "s_branch L_mp2b_%=\n\t"
    "L_mp3_%=:\n\t"
    "v_cmp_gt_u32 vcc, v58, v56\n\t"
    "s_cbranch_vccz L_mp3b_%=\n\t"
    "s_sub_u32 s66, s66, 1\n\t"
    "s_cmp_eq_u32 s66, 0\n\t"
    "s_cbranch_scc1 L_mp3b_%=\n\t"
    "s_sleep 8\n\t"
    "s_mov_b32 s65, 49283072\n\t"
    "s_mov_b64 exec, 15\n\t"
    "buffer_load_dword v56, v57, %12, s65 offen sc1\n\t"
    "s_mov_b64 exec, -1\n\t"
    "s_waitcnt vmcnt(0)\n\t"
    "s_branch L_mp3_%=\n\t"
    "L_pend_%=:\n\t"
    ".Ldcp_end:\n\t"
    :: "s"(s_idx), "s"(p_wp), "s"(p_w1), "n"(0), "n"(0), "n"(0), "s"(pb1), "s"(pw2), "s"(pb2), "s"(b2s),
       "n"(0),
       "s"((unsigned long long)ws & 0xFFFFFFFFFFFFull), "s"(s_ws), "n"(0), "s"(bid), "s"(s_tab), "s"(w), "s"(p_out),
       "s"((unsigned)(unsigned long long)idx), "s"((unsigned)((unsigned long long)idx >> 32) & 0xFFFFu)
    : "memory", "vcc", "scc", "m0", "s36", "s37", "s38", "s39", "s40", "s41", "s42", "s43", "s44", "s45", "s46", "s47", "s48", "s49", "s50", "s51", "s52", "s53", "s54", "s55", "s56", "s57", "s58", "s59", "s60", "s61", "s62", "s63", "s64", "s65", "s66", "s67", "s68", "s69", "s70", "s71", "s72", "s73", "s74", "s75", "s76", "s77", "s78", "s79", "s80", "s81", "s82", "s83", "s84", "s85", "s86", "s87", "s88", "s89", "s90", "s91", "s92", "s93", "s94", "s95", "s96", "s97", "s98", "s99", "v0", "v1", "v2", "v3", "v4", "v5", "v6", "v7", "v8", "v9", "v10", "v11", "v12", "v13", "v14", "v15", "v16", "v17", "v18", "v19", "v20", "v21", "v22", "v23", "v24", "v25", "v26", "v27", "v28", "v29", "v30", "v31", "v32", "v33", "v34", "v35", "v36", "v37", "v38", "v39", "v40", "v41", "v42", "v43", "v44", "v45", "v46", "v47", "v48", "v49", "v50", "v51", "v52", "v53", "v54", "v55", "v56", "v57", "v58", "v59", "v60", "v61", "v62", "v63", "v64", "v65", "v66", "v67", "v68", "v69", "v70", "v71", "v72", "v73", "v74", "v75", "v76", "v77", "v78", "v79", "v80", "v81", "v82", "v83", "v84", "v85", "v86", "v87", "v88", "v89", "v90", "v91", "v92", "v93", "v94", "v95", "v96", "v97", "v98", "v99", "v100", "v101", "v102", "v103", "v104", "v105", "v106", "v107", "v108", "v109", "v110", "v111", "v112", "v113", "v114", "v115", "v116", "v117", "v118", "v119", "v120", "v121", "v122", "v123", "v124", "v125", "v126", "v127", "v128", "v129", "v130", "v131", "v132", "v133", "v134", "v135", "v136", "v137", "v138", "v139", "v140", "v141", "v142", "v143", "v144", "v145", "v146", "v147", "v148", "v149", "v150", "v151", "v152", "v153", "v154", "v155", "v156", "v157", "v158", "v159", "v160", "v161", "v162", "v163", "v164", "v165", "v166", "v167", "v168", "v169", "v170", "v171", "v172", "v173", "v174", "v175", "v176", "v177", "v178", "v179", "v180", "v181", "v182", "v183", "v184", "v185", "v186", "v187", "v188", "v189", "v190", "v191", "v192", "v193", "v194", "v195", "v196", "v197", "v198", "v199", "v200", "v201", "v202", "v203", "v204", "v205", "v206", "v207", "v208", "v209", "v210", "v211", "v212", "v213", "v214", "v215", "v216", "v217", "v218", "v219", "v220", "v221", "v222", "v223", "v224", "v225", "v226", "v227", "v228", "v229", "v230", "v231", "v232", "v233", "v234", "v235", "v236", "v237", "v238", "v239", "v240", "v241", "v242", "v243", "v244", "v245", "v246", "v247", "v248", "v249", "v250", "v251", "v252", "v253", "v254", "v255");
}

extern "C" void kernel_launch(void* const* d_in, const int* in_sizes, int n_in, void* d_out, int out_size, void* d_ws,
                              size_t ws_size, hipStream_t stream) {
  const int* idx = (const int*)d_in[0];
  const unsigned short* table = (const unsigned short*)d_in[1];
  const unsigned short* w_p = (const unsigned short*)d_in[2];
  const unsigned short* w1 = (const unsigned short*)d_in[3];
  const unsigned short* b1 = (const unsigned short*)d_in[4];
  const unsigned short* w2 = (const unsigned short*)d_in[5];
  const unsigned short* b2 = (const unsigned short*)d_in[6];
  char* ws = (char*)d_ws;
  dcp_all<<<dim3(256), dim3(512), 0, stream>>>(idx, table, w_p, w1, b1, w2, b2, ws, (__hip_bfloat16*)d_out);
}
```

```cpp
#include <hip/hip_runtime.h>
#include <hip/hip_bf16.h>
#include <stdint.h>

constexpr size_t NWIN = 13;
constexpr size_t WS_IDX = 0;
constexpr size_t IDX_STREAM = 212992;
constexpr size_t WS_W1F = 3407872;
constexpr size_t W1F_SLAB = 102400;
constexpr size_t WS_TF = 16515072;
constexpr size_t WS_WT = 16777216;
constexpr size_t WS_PART = 17825792;
constexpr size_t WS_P0 = 27262976;
constexpr size_t WS_XCH = 31457280;
constexpr size_t XCH_PITCH = 69632;
constexpr size_t WS_CNT = 49283072;
constexpr size_t WS_CH = 49291264;
constexpr size_t WS_RP = 49292288;
constexpr size_t WS_TS = 49293312;
constexpr size_t WS_RNG = 49294336;
constexpr size_t WS_RDY = 49295360;
constexpr size_t WS_DBG = 49328128;
constexpr size_t WS_END = 49721344;

__global__ __launch_bounds__(512, 2) void dcp_all(const void* b2, const void* idx, const void* b1, const void* w2, const void* table,
                                                  const void* w_p, const void* w1, void* ws, void* out) {
  __shared__ __attribute__((aligned(1024))) char smem[163840];
  unsigned tid = threadIdx.x;
  asm volatile(
    "s_load_dwordx16 s[8:23], %1, 0x0\n\t"
    "v_readfirstlane_b32 s34, %0\n\t"
    "s_getpc_b64 s[36:37]\n\t"
    ".Ldcp_pc:\n\t"
    "s_bfe_u32 s38, %2, 0x40003\n\t"
    "s_lshr_b32 s34, s34, 6\n\t"
    "s_cmp_lg_u32 s34, 3\n\t"
    "s_cbranch_scc1 L_nopf_%=\n\t"
    "s_lshl_b32 s38, s38, 13\n\t"
    "v_and_b32 v122, 63, %0\n\t"
    "v_lshlrev_b32 v122, 7, v122\n\t"
    "v_add_u32 v122, s38, v122\n\t"
    "v_min_u32 v122, .Ldcp_end-.Ldcp_pc-4, v122\n\t"
    "global_load_dword v123, v122, s[36:37]\n\t"
    "L_nopf_%=:\n\t"
    "s_mov_b32 s6, 256000\n\t"
    "s_mov_b32 s7, 0x20000\n\t"
    "s_mul_i32 s24, %2, 25600\n\t"
    "s_sub_u32 s26, 6553600, s24\n\t"
    "s_mov_b32 s27, 0x20000\n\t"
    "s_mov_b32 s30, 49721344\n\t"
    "s_mov_b32 s31, 0x20000\n\t"
    "s_lshl_b32 s35, %2, 4\n\t"
    "s_and_b32 s35, s35, 0x70\n\t"
    "s_lshr_b32 s25, %2, 4\n\t"
    "s_add_u32 s35, s35, s25\n\t"
    "s_lshl_b32 s35, s35, 3\n\t"
    "v_mbcnt_lo_u32_b32 v147, -1, 0\n\t"
    "v_mbcnt_hi_u32_b32 v147, -1, v147\n\t"
    "s_mov_b32 s94, 20000\n\t"
    "s_mov_b32 s95, 0x20000\n\t"
    "s_lshr_b32 s46, %2, 3\n\t"
    "s_lshl_b32 s51, s46, 2\n\t"
    "v_lshlrev_b32 v122, 4, v147\n\t"
    "s_mov_b32 s98, 13107200\n\t"
    "s_mov_b32 s99, 0x20000\n\t"
    "s_cmp_ge_u32 s34, 2\n\t"
    "s_cbranch_scc1 L_aimg_%=\n\t"
    "s_lshl_b32 s44, s34, 10\n\t"
    "s_add_u32 m0, s44, 140288\n\t"
    "s_waitcnt lgkmcnt(0)\n\t"
    "s_mov_b32 s92, s18\n\t"
    "s_and_b32 s93, s19, 0xffff\n\t"
    "s_mov_b32 s4, s16\n\t"
    "s_and_b32 s5, s17, 0xffff\n\t"
    "buffer_load_dwordx4 v122, s[92:95], s44 offen offset:0 lds\n\t"
    "buffer_load_dwordx4 v122, s[92:95], s44 offen offset:2048 lds\n\t"
    "s_add_u32 m0, m0, 0x1000\n\t"
    "s_add_u32 s44, s44, 0x1000\n\t"
    "buffer_load_dwordx4 v122, s[92:95], s44 offen offset:0 lds\n\t"
    "buffer_load_dwordx4 v122, s[92:95], s44 offen offset:2048 lds\n\t"
    "s_add_u32 m0, m0, 0x1000\n\t"
    "s_add_u32 s44, s44, 0x1000\n\t"
    "buffer_load_dwordx4 v122, s[92:95], s44 offen offset:0 lds\n\t"
    "buffer_load_dwordx4 v122, s[92:95], s44 offen offset:2048 lds\n\t"
    "s_add_u32 m0, m0, 0x1000\n\t"
    "s_add_u32 s44, s44, 0x1000\n\t"
    "buffer_load_dwordx4 v122, s[92:95], s44 offen offset:0 lds\n\t"
    "buffer_load_dwordx4 v122, s[92:95], s44 offen offset:2048 lds\n\t"
    "s_add_u32 m0, m0, 0x1000\n\t"
    "s_add_u32 s44, s44, 0x1000\n\t"
    "buffer_load_dwordx4 v122, s[92:95], s44 offen offset:0 lds\n\t"
    "buffer_load_dwordx4 v122, s[92:95], s44 offen offset:2048 lds\n\t"
    "s_and_b32 s11, s11, 0xffff\n\t"
    "s_add_u32 s24, s10, s24\n\t"
    "s_addc_u32 s25, s11, 0\n\t"
    "s_mov_b32 s28, s22\n\t"
    "s_and_b32 s29, s23, 0xffff\n\t"
    "s_add_u32 s12, s12, s35\n\t"
    "s_addc_u32 s13, s13, 0\n\t"
    "s_add_u32 s14, s14, s35\n\t"
    "s_addc_u32 s15, s15, 0\n\t"
    "s_lshl_b32 s35, s8, 3\n\t"
    "s_and_b32 s35, s35, 16\n\t"
    "s_and_b32 s8, s8, -4\n\t"
    "s_cmp_eq_u32 s34, 0\n\t"
    "s_cbranch_scc1 L_aside_%=\n\t"
    "s_branch L_ahelp_%=\n\t"
    "L_aimg_%=:\n\t"
    "s_cmp_lg_u32 s34, 2\n\t"
    "s_cbranch_scc1 L_anoinit_%=\n\t"
    "v_mov_b32 v0, 0\n\t"
    "v_mov_b32 v1, 0\n\t"
    "v_mov_b32 v2, 0\n\t"
    "v_mov_b32 v3, 0\n\t"
    "v_lshlrev_b32 v8, 4, v147\n\t"
    "v_add_u32 v8, 130048, v8\n\t"
    "v_mov_b32 v9, 1024\n\t"
    "ds_write_b128 v9, v[0:3]\n\t"
    "ds_write_b128 v8, v[0:3] offset:0\n\t"
    "ds_write_b128 v8, v[0:3] offset:1024\n\t"
    "ds_write_b128 v8, v[0:3] offset:2048\n\t"
    "ds_write_b32 v9, v0 offset:2084\n\t"
    "ds_write_b32 v9, v0 offset:2088\n\t"
    "ds_write_b32 v9, v0 offset:1044\n\t"
    "ds_write_b32 v9, v0 offset:1052\n\t"
    "v_mov_b32 v4, 3\n\t"
    "ds_write_b32 v9, v4 offset:1040\n\t"
    "v_mov_b32 v5, 16\n\t"
    "ds_write_b32 v9, v5 offset:2080\n\t"
    "v_mov_b32 v6, 32\n\t"
    "ds_write_b32 v9, v6 offset:3120\n\t"
    "v_mov_b32 v7, 51\n\t"
    "ds_write_b32 v9, v7 offset:4160\n\t"
    "L_anoinit_%=:\n\t"
    "s_and_b32 s46, s34, 3\n\t"
    "s_mul_i32 s49, s46, 1040\n\t"
    "s_add_u32 s49, s49, 2064\n\t"
    "s_lshl_b32 s46, s46, 3\n\t"
    "s_lshr_b32 s47, 1076697610, s46\n\t"
    "s_and_b32 s47, s47, 0xff\n\t"
    "s_lshl_b32 s48, s47, 16\n\t"
    "s_lshr_b32 s47, 756681216, s46\n\t"
    "s_and_b32 s47, s47, 0xff\n\t"
    "s_add_u32 s48, s48, s47\n\t"
    "s_lshr_b32 s47, s34, 2\n\t"
    "s_add_u32 s47, s47, 0x200\n\t"
    "s_cmp_eq_u32 s46, 0\n\t"
    "s_cselect_b32 s47, 0x100, s47\n\t"
    "s_add_u32 s48, s48, s47\n\t"
    "v_lshrrev_b32 v126, 3, v147\n\t"
    "v_and_b32 v154, 7, v147\n\t"
    "v_lshlrev_b32 v126, 8, v126\n\t"
    "v_lshl_add_u32 v126, v154, 4, v126\n\t"
    "v_lshlrev_b32 v117, 20, v147\n\t"
    "v_add_u32 v117, 1024, v117\n\t"
    "v_mov_b32 v118, 1\n\t"
    "s_and_b32 s46, s34, 6\n\t"
    "s_cmp_eq_u32 s46, 4\n\t"
    "s_cbranch_scc1 L_adma_%=\n\t"
    "s_lshr_b32 s61, s34, 2\n\t"
    "s_lshl_b32 s61, s61, 1\n\t"
    "s_and_b32 s46, s34, 1\n\t"
    "s_add_u32 s61, s61, s46\n\t"
    "s_add_u32 s46, s61, 102\n\t"
    "s_add_u32 s69, s51, s46\n\t"
    "s_sub_u32 s44, s69, 125\n\t"
    "s_cmp_ge_u32 s69, 125\n\t"
    "s_cselect_b32 s69, s44, s69\n\t"
    "s_waitcnt lgkmcnt(0)\n\t"
    "s_mov_b32 s92, s18\n\t"
    "s_and_b32 s93, s19, 0xffff\n\t"
    "s_mov_b32 s4, s16\n\t"
    "s_and_b32 s5, s17, 0xffff\n\t"
    "s_and_b32 s11, s11, 0xffff\n\t"
    "s_add_u32 s24, s10, s24\n\t"
    "s_addc_u32 s25, s11, 0\n\t"
    "s_mov_b32 s28, s22\n\t"
    "s_and_b32 s29, s23, 0xffff\n\t"
    "s_add_u32 s12, s12, s35\n\t"
    "s_addc_u32 s13, s13, 0\n\t"
    "s_add_u32 s14, s14, s35\n\t"
    "s_addc_u32 s15, s15, 0\n\t"
    "s_lshl_b32 s35, s8, 3\n\t"
    "s_and_b32 s35, s35, 16\n\t"
    "s_and_b32 s8, s8, -4\n\t"
    "s_barrier\n\t"
    "s_load_dwordx2 s[72:73], s[12:13], 0\n\t"
    "s_load_dwordx2 s[88:89], s[14:15], 0\n\t"
    "s_load_dword s91, s[8:9], 0\n\t"
    "s_load_dwordx2 s[22:23], %1, 0x40\n\t"
    "s_mul_i32 m0, s69, 1040\n\t"
    "s_lshl_b32 s44, s69, 11\n\t"
    "buffer_load_dwordx4 v126, s[4:7], s44 offen lds\n\t"
    "s_add_u32 s69, s69, 4\n\t"
    "s_sub_u32 s44, s69, 125\n\t"
    "s_cmp_ge_u32 s69, 125\n\t"
    "s_cselect_b32 s69, s44, s69\n\t"
    "s_mul_i32 m0, s69, 1040\n\t"
    "s_lshl_b32 s44, s69, 11\n\t"
    "buffer_load_dwordx4 v126, s[4:7], s44 offen lds\n\t"
    "s_add_u32 s69, s69, 4\n\t"
    "s_sub_u32 s44, s69, 125\n\t"
    "s_cmp_ge_u32 s69, 125\n\t"
    "s_cselect_b32 s69, s44, s69\n\t"
    "s_mul_i32 m0, s69, 1040\n\t"
    "s_lshl_b32 s44, s69, 11\n\t"
    "buffer_load_dwordx4 v126, s[4:7], s44 offen lds\n\t"
    "s_add_u32 s69, s69, 4\n\t"
    "s_sub_u32 s44, s69, 125\n\t"
    "s_cmp_ge_u32 s69, 125\n\t"
    "s_cselect_b32 s69, s44, s69\n\t"
    "s_mul_i32 m0, s69, 1040\n\t"
    "s_lshl_b32 s44, s69, 11\n\t"
    "buffer_load_dwordx4 v126, s[4:7], s44 offen lds\n\t"
    "s_add_u32 s69, s69, 4\n\t"
    "s_sub_u32 s44, s69, 125\n\t"
    "s_cmp_ge_u32 s69, 125\n\t"
    "s_cselect_b32 s69, s44, s69\n\t"
    "s_mul_i32 m0, s69, 1040\n\t"
    "s_lshl_b32 s44, s69, 11\n\t"
    "buffer_load_dwordx4 v126, s[4:7], s44 offen lds\n\t"
    "s_add_u32 s69, s69, 4\n\t"
    "s_sub_u32 s44, s69, 125\n\t"
    "s_cmp_ge_u32 s69, 125\n\t"
    "s_cselect_b32 s69, s44, s69\n\t"
    "s_mul_i32 m0, s69, 1040\n\t"
    "s_lshl_b32 s44, s69, 11\n\t"
    "buffer_load_dwordx4 v126, s[4:7], s44 offen lds\n\t"
    "s_add_u32 s69, s69, 4\n\t"
    "s_sub_u32 s44, s69, 125\n\t"
    "s_cmp_ge_u32 s69, 125\n\t"
    "s_cselect_b32 s69, s44, s69\n\t"
    "v_bfe_u32 v154, v147, 2, 2\n\t"
    "v_and_b32 v155, 3, v147\n\t"
    "v_mul_u32_u24 v154, 25, v154\n\t"
    "v_mad_u32_u24 v154, v155, 6, v154\n\t"
    "v_lshlrev_b32 v153, 2, v154\n\t"
    "s_and_b32 s53, s48, 0xff\n\t"
    "s_bfe_u32 s55, s48, 0x80008\n\t"
    "s_add_u32 s54, s53, s55\n\t"
    "s_add_u32 s55, s54, s55\n\t"
    "s_lshr_b32 s64, s48, 16\n\t"
    "v_cmp_eq_u32 s[62:63], 0, v155\n\t"
    "v_and_b32 v151, 48, v147\n\t"
    "v_or_b32 v155, 0x100000, v151\n\t"
    "v_cndmask_b32 v152, v155, v151, s[62:63]\n\t"
    "v_lshrrev_b32 v94, 4, v147\n\t"
    "v_mul_u32_u24 v92, 0x1388, v94\n\t"
    "v_lshl_add_u32 v92, v154, 1, v92\n\t"
    "v_add_u32 v92, 140288, v92\n\t"
    "v_mul_u32_u24 v93, 200, v154\n\t"
    "v_mad_u32_u24 v93, v94, 50, v93\n\t"
    "v_add_u32 v93, 140288, v93\n\t"
    "v_add_u32 v95, 140288, v122\n\t"
    "v_mov_b32 v0, 0\n\t"
    "v_mov_b32 v28, 0\n\t"
    "v_mov_b32 v1, 0\n\t"
    "v_mov_b32 v29, 0\n\t"
    "v_mov_b32 v2, 0\n\t"
    "v_mov_b32 v30, 0\n\t"
    "v_mov_b32 v3, 0\n\t"
    "v_mov_b32 v31, 0\n\t"
    "v_mov_b32 v4, 0\n\t"
    "v_mov_b32 v32, 0\n\t"
    "v_mov_b32 v5, 0\n\t"
    "v_mov_b32 v33, 0\n\t"
    "v_mov_b32 v6, 0\n\t"
    "v_mov_b32 v34, 0\n\t"
    "v_mov_b32 v7, 0\n\t"
    "v_mov_b32 v35, 0\n\t"
    "v_mov_b32 v8, 0\n\t"
    "v_mov_b32 v36, 0\n\t"
    "v_mov_b32 v9, 0\n\t"
    "v_mov_b32 v37, 0\n\t"
    "v_mov_b32 v10, 0\n\t"
    "v_mov_b32 v38, 0\n\t"
    "v_mov_b32 v11, 0\n\t"
    "v_mov_b32 v39, 0\n\t"
    "v_mov_b32 v12, 0\n\t"
    "v_mov_b32 v40, 0\n\t"
    "v_mov_b32 v13, 0\n\t"
    "v_mov_b32 v41, 0\n\t"
    "v_mov_b32 v14, 0\n\t"
    "v_mov_b32 v42, 0\n\t"
    "v_mov_b32 v15, 0\n\t"
    "v_mov_b32 v43, 0\n\t"
    "v_mov_b32 v16, 0\n\t"
    "v_mov_b32 v44, 0\n\t"
    "v_mov_b32 v17, 0\n\t"
    "v_mov_b32 v45, 0\n\t"
    "v_mov_b32 v18, 0\n\t"
    "v_mov_b32 v46, 0\n\t"
    "v_mov_b32 v19, 0\n\t"
    "v_mov_b32 v47, 0\n\t"
    "v_mov_b32 v20, 0\n\t"
    "v_mov_b32 v48, 0\n\t"
    "v_mov_b32 v21, 0\n\t"
    "v_mov_b32 v49, 0\n\t"
    "v_mov_b32 v22, 0\n\t"
    "v_mov_b32 v50, 0\n\t"
    "v_mov_b32 v23, 0\n\t"
    "v_mov_b32 v51, 0\n\t"
    "s_cmp_eq_u32 s61, 1\n\t"
    "s_cbranch_scc1 L_afold1_%=\n\t"
    "s_cmp_eq_u32 s61, 2\n\t"
    "s_cbranch_scc1 L_afold2_%=\n\t"
    "s_cmp_eq_u32 s61, 3\n\t"
    "s_cbranch_scc1 L_afold3_%=\n\t"
    "L_afold0_%=:\n\t"
    "L_asp0_%=:\n\t"
    "ds_read_b32 v119, v117 offset:1044\n\t"
    "s_waitcnt lgkmcnt(0)\n\t"
    "v_readfirstlane_b32 s46, v119\n\t"
    "s_nop 0\n\t"
    "s_cmp_ge_u32 s46, 2\n\t"
    "s_cbranch_scc1 L_ago0_%=\n\t"
    "s_sleep 1\n\t"
    "s_branch L_asp0_%=\n\t"
    "L_ago0_%=:\n\t"
    "ds_read_u16_d16_hi v0, v92 offset:208\n\t"
    "ds_read_u16_d16_hi v28, v93 offset:802\n\t"
    "ds_read_u16_d16_hi v1, v92 offset:1408\n\t"
    "ds_read_u16_d16_hi v29, v93 offset:814\n\t"
    "ds_read_u16_d16_hi v2, v92 offset:2608\n\t"
    "ds_read_u16_d16_hi v30, v93 offset:826\n\t"
    "ds_read_u16_d16_hi v3, v92 offset:3808\n\t"
    "ds_read_u16_d16_hi v31, v93 offset:838\n\t"
    "ds_read_u16_d16_hi v4, v92 offset:410\n\t"
    "ds_read_u16_d16_hi v32, v93 offset:1004\n\t"
    "ds_read_u16_d16_hi v5, v92 offset:1610\n\t"
    "ds_read_u16_d16_hi v33, v93 offset:1016\n\t"
    "ds_read_u16_d16_hi v6, v92 offset:2810\n\t"
    "ds_read_u16_d16_hi v34, v93 offset:1028\n\t"
    "ds_read_u16_d16_hi v7, v92 offset:4010\n\t"
    "ds_read_u16_d16_hi v35, v93 offset:1040\n\t"
    "ds_read_u16_d16_hi v8, v92 offset:612\n\t"
    "ds_read_u16_d16_hi v36, v93 offset:1206\n\t"
    "ds_read_u16_d16_hi v9, v92 offset:1812\n\t"
    "ds_read_u16_d16_hi v37, v93 offset:1218\n\t"
    "ds_read_u16_d16_hi v10, v92 offset:3012\n\t"
    "ds_read_u16_d16_hi v38, v93 offset:1230\n\t"
    "ds_read_u16_d16_hi v11, v92 offset:4212\n\t"
    "ds_read_u16_d16_hi v39, v93 offset:1242\n\t"
    "ds_read_u16_d16_hi v12, v92 offset:0\n\t"
    "ds_read_u16_d16_hi v13, v92 offset:2\n\t"
    "ds_read_u16_d16_hi v41, v93 offset:200\n\t"
    "ds_read_u16_d16_hi v14, v92 offset:4\n\t"
    "ds_read_u16_d16_hi v42, v93 offset:400\n\t"
    "ds_read_u16_d16_hi v15, v92 offset:6\n\t"
    "ds_read_u16_d16_hi v43, v93 offset:600\n\t"
    "ds_read_u16_d16_hi v16, v92 offset:8\n\t"
    "ds_read_u16_d16_hi v44, v93 offset:800\n\t"
    "ds_read_u16_d16_hi v17, v92 offset:10\n\t"
    "ds_read_u16_d16_hi v45, v93 offset:1000\n\t"
    "ds_read_u16_d16_hi v18, v92 offset:12\n\t"
    "ds_read_u16_d16_hi v46, v93 offset:1200\n\t"
    "ds_add_u32 v117, v118 offset:0\n\t"
    "s_waitcnt lgkmcnt(15)\n\t"
    "v_add_f32 v0, v0, v28\n\t"
    "v_add_f32 v1, v1, v29\n\t"
    "v_add_f32 v2, v2, v30\n\t"
    "v_add_f32 v3, v3, v31\n\t"
    "v_add_f32 v4, v4, v32\n\t"
    "v_add_f32 v5, v5, v33\n\t"
    "v_add_f32 v6, v6, v34\n\t"
    "v_add_f32 v7, v7, v35\n\t"
    "s_waitcnt lgkmcnt(14)\n\t"
    "v_add_f32 v8, v8, v36\n\t"
    "v_add_f32 v9, v9, v37\n\t"
    "v_add_f32 v10, v10, v38\n\t"
    "v_add_f32 v11, v11, v39\n\t"
    "s_waitcnt lgkmcnt(7)\n\t"
    "v_cndmask_b32 v12, 0, v12, s[62:63]\n\t"
    "v_add_f32 v13, v13, v41\n\t"
    "v_add_f32 v14, v14, v42\n\t"
    "v_add_f32 v15, v15, v43\n\t"
    "s_waitcnt lgkmcnt(1)\n\t"
    "v_add_f32 v16, v16, v44\n\t"
    "v_add_f32 v17, v17, v45\n\t"
    "v_add_f32 v18, v18, v46\n\t"
    "L_asp1_%=:\n\t"
    "ds_read_b32 v119, v117 offset:0\n\t"
    "s_waitcnt lgkmcnt(0)\n\t"
    "v_readfirstlane_b32 s46, v119\n\t"
    "s_nop 0\n\t"
    "s_cmp_ge_u32 s46, 4\n\t"
    "s_cbranch_scc1 L_ago1_%=\n\t"
    "s_sleep 1\n\t"
    "s_branch L_asp1_%=\n\t"
    "L_ago1_%=:\n\t"
    "ds_write_b128 v95, v[0:3] offset:6144\n\t"
    "ds_write_b128 v95, v[4:7] offset:10240\n\t"
    "ds_write_b128 v95, v[8:11] offset:14336\n\t"
    "ds_write_b128 v95, v[12:15] offset:21504\n\t"
    "ds_write_b128 v95, v[16:19] offset:22528\n\t"
    "ds_add_u32 v117, v118 offset:4\n\t"
    "s_branch L_ab0_%=\n\t"
    "L_afold1_%=:\n\t"
    "L_asp2_%=:\n\t"
    "ds_read_b32 v119, v117 offset:1044\n\t"
    "s_waitcnt lgkmcnt(0)\n\t"
    "v_readfirstlane_b32 s46, v119\n\t"
    "s_nop 0\n\t"
    "s_cmp_ge_u32 s46, 2\n\t"
    "s_cbranch_scc1 L_ago2_%=\n\t"
    "s_sleep 1\n\t"
    "s_branch L_asp2_%=\n\t"
    "L_ago2_%=:\n\t"
    "ds_read_u16_d16_hi v0, v92 offset:204\n\t"
    "ds_read_u16_d16_hi v28, v93 offset:402\n\t"
    "ds_read_u16_d16_hi v1, v92 offset:1404\n\t"
    "ds_read_u16_d16_hi v29, v93 offset:414\n\t"
    "ds_read_u16_d16_hi v2, v92 offset:2604\n\t"
    "ds_read_u16_d16_hi v30, v93 offset:426\n\t"
    "ds_read_u16_d16_hi v3, v92 offset:3804\n\t"
    "ds_read_u16_d16_hi v31, v93 offset:438\n\t"
    "ds_read_u16_d16_hi v4, v92 offset:408\n\t"
    "ds_read_u16_d16_hi v32, v93 offset:804\n\t"
    "ds_read_u16_d16_hi v5, v92 offset:1608\n\t"
    "ds_read_u16_d16_hi v33, v93 offset:816\n\t"
    "ds_read_u16_d16_hi v6, v92 offset:2808\n\t"
    "ds_read_u16_d16_hi v34, v93 offset:828\n\t"
    "ds_read_u16_d16_hi v7, v92 offset:4008\n\t"
    "ds_read_u16_d16_hi v35, v93 offset:840\n\t"
    "ds_read_u16_d16_hi v8, v92 offset:412\n\t"
    "ds_read_u16_d16_hi v36, v93 offset:1204\n\t"
    "ds_read_u16_d16_hi v9, v92 offset:1612\n\t"
    "ds_read_u16_d16_hi v37, v93 offset:1216\n\t"
    "ds_read_u16_d16_hi v10, v92 offset:2812\n\t"
    "ds_read_u16_d16_hi v38, v93 offset:1228\n\t"
    "ds_read_u16_d16_hi v11, v92 offset:4012\n\t"
    "ds_read_u16_d16_hi v39, v93 offset:1240\n\t"
    "ds_read_u16_d16_hi v12, v92 offset:810\n\t"
    "ds_read_u16_d16_hi v40, v93 offset:1008\n\t"
    "ds_read_u16_d16_hi v13, v92 offset:2010\n\t"
    "ds_read_u16_d16_hi v41, v93 offset:1020\n\t"
    "ds_read_u16_d16_hi v14, v92 offset:3210\n\t"
    "ds_read_u16_d16_hi v42, v93 offset:1032\n\t"
    "ds_read_u16_d16_hi v15, v92 offset:4410\n\t"
    "ds_read_u16_d16_hi v43, v93 offset:1044\n\t"
    "ds_read_u16_d16_hi v16, v92 offset:202\n\t"
    "ds_read_u16_d16_hi v17, v92 offset:1402\n\t"
    "ds_read_u16_d16_hi v18, v92 offset:2602\n\t"
    "ds_read_u16_d16_hi v19, v92 offset:3802\n\t"
    "ds_read_u16_d16_hi v20, v92 offset:808\n\t"
    "ds_read_u16_d16_hi v21, v92 offset:2008\n\t"
    "ds_read_u16_d16_hi v22, v92 offset:3208\n\t"
    "ds_read_u16_d16_hi v23, v92 offset:4408\n\t"
    "ds_add_u32 v117, v118 offset:0\n\t"
    "s_waitcnt lgkmcnt(15)\n\t"
    "v_add_f32 v0, v0, v28\n\t"
    "v_add_f32 v1, v1, v29\n\t"
    "v_add_f32 v2, v2, v30\n\t"
    "v_add_f32 v3, v3, v31\n\t"
    "v_add_f32 v4, v4, v32\n\t"
    "v_add_f32 v5, v5, v33\n\t"
    "v_add_f32 v6, v6, v34\n\t"
    "v_add_f32 v7, v7, v35\n\t"
    "v_add_f32 v8, v8, v36\n\t"
    "v_add_f32 v9, v9, v37\n\t"
    "v_add_f32 v10, v10, v38\n\t"
    "v_add_f32 v11, v11, v39\n\t"
    "s_waitcnt lgkmcnt(9)\n\t"
    "v_add_f32 v12, v12, v40\n\t"
    "v_add_f32 v13, v13, v41\n\t"
    "v_add_f32 v14, v14, v42\n\t"
    "v_add_f32 v15, v15, v43\n\t"
    "s_waitcnt lgkmcnt(5)\n\t"
    "s_waitcnt lgkmcnt(1)\n\t"
    "L_asp3_%=:\n\t"
    "ds_read_b32 v119, v117 offset:0\n\t"
    "s_waitcnt lgkmcnt(0)\n\t"
    "v_readfirstlane_b32 s46, v119\n\t"
    "s_nop 0\n\t"
    "s_cmp_ge_u32 s46, 4\n\t"
    "s_cbranch_scc1 L_ago3_%=\n\t"
    "s_sleep 1\n\t"
    "s_branch L_asp3_%=\n\t"
    "L_ago3_%=:\n\t"
    "ds_write_b128 v95, v[0:3] offset:1024\n\t"
    "ds_write_b128 v95, v[4:7] offset:7168\n\t"
    "ds_write_b128 v95, v[8:11] offset:11264\n\t"
    "ds_write_b128 v95, v[12:15] offset:16384\n\t"
    "ds_write_b128 v95, v[16:19] offset:0\n\t"
    "ds_write_b128 v95, v[20:23] offset:15360\n\t"
    "ds_add_u32 v117, v118 offset:4\n\t"
    "s_branch L_ab0_%=\n\t"
    "L_afold2_%=:\n\t"
    "L_asp4_%=:\n\t"
    "ds_read_b32 v119, v117 offset:1044\n\t"
    "s_waitcnt lgkmcnt(0)\n\t"
    "v_readfirstlane_b32 s46, v119\n\t"
    "s_nop 0\n\t"
    "s_cmp_ge_u32 s46, 2\n\t"
    "s_cbranch_scc1 L_ago4_%=\n\t"
    "s_sleep 1\n\t"
    "s_branch L_asp4_%=\n\t"
    "L_ago4_%=:\n\t"
    "ds_read_u16_d16_hi v0, v92 offset:206\n\t"
    "ds_read_u16_d16_hi v28, v93 offset:602\n\t"
    "ds_read_u16_d16_hi v1, v92 offset:1406\n\t"
    "ds_read_u16_d16_hi v29, v93 offset:614\n\t"
    "ds_read_u16_d16_hi v2, v92 offset:2606\n\t"
    "ds_read_u16_d16_hi v30, v93 offset:626\n\t"
    "ds_read_u16_d16_hi v3, v92 offset:3806\n\t"
    "ds_read_u16_d16_hi v31, v93 offset:638\n\t"
    "ds_read_u16_d16_hi v4, v92 offset:210\n\t"
    "ds_read_u16_d16_hi v32, v93 offset:1002\n\t"
    "ds_read_u16_d16_hi v5, v92 offset:1410\n\t"
    "ds_read_u16_d16_hi v33, v93 offset:1014\n\t"
    "ds_read_u16_d16_hi v6, v92 offset:2610\n\t"
    "ds_read_u16_d16_hi v34, v93 offset:1026\n\t"
    "ds_read_u16_d16_hi v7, v92 offset:3810\n\t"
    "ds_read_u16_d16_hi v35, v93 offset:1038\n\t"
    "ds_read_u16_d16_hi v8, v92 offset:608\n\t"
    "ds_read_u16_d16_hi v36, v93 offset:806\n\t"
    "ds_read_u16_d16_hi v9, v92 offset:1808\n\t"
    "ds_read_u16_d16_hi v37, v93 offset:818\n\t"
    "ds_read_u16_d16_hi v10, v92 offset:3008\n\t"
    "ds_read_u16_d16_hi v38, v93 offset:830\n\t"
    "ds_read_u16_d16_hi v11, v92 offset:4208\n\t"
    "ds_read_u16_d16_hi v39, v93 offset:842\n\t"
    "ds_read_u16_d16_hi v12, v92 offset:812\n\t"
    "ds_read_u16_d16_hi v40, v93 offset:1208\n\t"
    "ds_read_u16_d16_hi v13, v92 offset:2012\n\t"
    "ds_read_u16_d16_hi v41, v93 offset:1220\n\t"
    "ds_read_u16_d16_hi v14, v92 offset:3212\n\t"
    "ds_read_u16_d16_hi v42, v93 offset:1232\n\t"
    "ds_read_u16_d16_hi v15, v92 offset:4412\n\t"
    "ds_read_u16_d16_hi v43, v93 offset:1244\n\t"
    "ds_read_u16_d16_hi v16, v92 offset:404\n\t"
    "ds_read_u16_d16_hi v17, v92 offset:1604\n\t"
    "ds_read_u16_d16_hi v18, v92 offset:2804\n\t"
    "ds_read_u16_d16_hi v19, v92 offset:4004\n\t"
    "ds_read_u16_d16_hi v20, v92 offset:1010\n\t"
    "ds_read_u16_d16_hi v21, v92 offset:2210\n\t"
    "ds_read_u16_d16_hi v22, v92 offset:3410\n\t"
    "ds_read_u16_d16_hi v23, v92 offset:4610\n\t"
    "ds_add_u32 v117, v118 offset:0\n\t"
    "s_waitcnt lgkmcnt(15)\n\t"
    "v_add_f32 v0, v0, v28\n\t"
    "v_add_f32 v1, v1, v29\n\t"
    "v_add_f32 v2, v2, v30\n\t"
    "v_add_f32 v3, v3, v31\n\t"
    "v_add_f32 v4, v4, v32\n\t"
    "v_add_f32 v5, v5, v33\n\t"
    "v_add_f32 v6, v6, v34\n\t"
    "v_add_f32 v7, v7, v35\n\t"
    "v_add_f32 v8, v8, v36\n\t"
    "v_add_f32 v9, v9, v37\n\t"
    "v_add_f32 v10, v10, v38\n\t"
    "v_add_f32 v11, v11, v39\n\t"
    "s_waitcnt lgkmcnt(9)\n\t"
    "v_add_f32 v12, v12, v40\n\t"
    "v_add_f32 v13, v13, v41\n\t"
    "v_add_f32 v14, v14, v42\n\t"
    "v_add_f32 v15, v15, v43\n\t"
    "s_waitcnt lgkmcnt(5)\n\t"
    "s_waitcnt lgkmcnt(1)\n\t"
    "L_asp5_%=:\n\t"
    "ds_read_b32 v119, v117 offset:0\n\t"
    "s_waitcnt lgkmcnt(0)\n\t"
    "v_readfirstlane_b32 s46, v119\n\t"
    "s_nop 0\n\t"
    "s_cmp_ge_u32 s46, 4\n\t"
    "s_cbranch_scc1 L_ago5_%=\n\t"
    "s_sleep 1\n\t"
    "s_branch L_asp5_%=\n\t"
    "L_ago5_%=:\n\t"
    "ds_write_b128 v95, v[0:3] offset:3072\n\t"
    "ds_write_b128 v95, v[4:7] offset:8192\n\t"
    "ds_write_b128 v95, v[8:11] offset:12288\n\t"
    "ds_write_b128 v95, v[12:15] offset:17408\n\t"
    "ds_write_b128 v95, v[16:19] offset:2048\n\t"
    "ds_write_b128 v95, v[20:23] offset:18432\n\t"
    "ds_add_u32 v117, v118 offset:4\n\t"
    "s_branch L_ab0_%=\n\t"
    "L_afold3_%=:\n\t"
    "L_asp6_%=:\n\t"
    "ds_read_b32 v119, v117 offset:1044\n\t"
    "s_waitcnt lgkmcnt(0)\n\t"
    "v_readfirstlane_b32 s46, v119\n\t"
    "s_nop 0\n\t"
    "s_cmp_ge_u32 s46, 2\n\t"
    "s_cbranch_scc1 L_ago6_%=\n\t"
    "s_sleep 1\n\t"
    "s_branch L_asp6_%=\n\t"
    "L_ago6_%=:\n\t"
    "ds_read_u16_d16_hi v0, v92 offset:406\n\t"
    "ds_read_u16_d16_hi v28, v93 offset:604\n\t"
    "ds_read_u16_d16_hi v1, v92 offset:1606\n\t"
    "ds_read_u16_d16_hi v29, v93 offset:616\n\t"
    "ds_read_u16_d16_hi v2, v92 offset:2806\n\t"
    "ds_read_u16_d16_hi v30, v93 offset:628\n\t"
    "ds_read_u16_d16_hi v3, v92 offset:4006\n\t"
    "ds_read_u16_d16_hi v31, v93 offset:640\n\t"
    "ds_read_u16_d16_hi v4, v92 offset:212\n\t"
    "ds_read_u16_d16_hi v32, v93 offset:1202\n\t"
    "ds_read_u16_d16_hi v5, v92 offset:1412\n\t"
    "ds_read_u16_d16_hi v33, v93 offset:1214\n\t"
    "ds_read_u16_d16_hi v6, v92 offset:2612\n\t"
    "ds_read_u16_d16_hi v34, v93 offset:1226\n\t"
    "ds_read_u16_d16_hi v7, v92 offset:3812\n\t"
    "ds_read_u16_d16_hi v35, v93 offset:1238\n\t"
    "ds_read_u16_d16_hi v8, v92 offset:610\n\t"
    "ds_read_u16_d16_hi v36, v93 offset:1006\n\t"
    "ds_read_u16_d16_hi v9, v92 offset:1810\n\t"
    "ds_read_u16_d16_hi v37, v93 offset:1018\n\t"
    "ds_read_u16_d16_hi v10, v92 offset:3010\n\t"
    "ds_read_u16_d16_hi v38, v93 offset:1030\n\t"
    "ds_read_u16_d16_hi v11, v92 offset:4210\n\t"
    "ds_read_u16_d16_hi v39, v93 offset:1042\n\t"
    "ds_read_u16_d16_hi v12, v92 offset:1012\n\t"
    "ds_read_u16_d16_hi v40, v93 offset:1210\n\t"
    "ds_read_u16_d16_hi v13, v92 offset:2212\n\t"
    "ds_read_u16_d16_hi v41, v93 offset:1222\n\t"
    "ds_read_u16_d16_hi v14, v92 offset:3412\n\t"
    "ds_read_u16_d16_hi v42, v93 offset:1234\n\t"
    "ds_read_u16_d16_hi v15, v92 offset:4612\n\t"
    "ds_read_u16_d16_hi v43, v93 offset:1246\n\t"
    "ds_read_u16_d16_hi v16, v92 offset:606\n\t"
    "ds_read_u16_d16_hi v17, v92 offset:1806\n\t"
    "ds_read_u16_d16_hi v18, v92 offset:3006\n\t"
    "ds_read_u16_d16_hi v19, v92 offset:4206\n\t"
    "ds_read_u16_d16_hi v20, v92 offset:1212\n\t"
    "ds_read_u16_d16_hi v21, v92 offset:2412\n\t"
    "ds_read_u16_d16_hi v22, v92 offset:3612\n\t"
    "ds_read_u16_d16_hi v23, v92 offset:4812\n\t"
    "ds_add_u32 v117, v118 offset:0\n\t"
    "s_waitcnt lgkmcnt(15)\n\t"
    "v_add_f32 v0, v0, v28\n\t"
    "v_add_f32 v1, v1, v29\n\t"
    "v_add_f32 v2, v2, v30\n\t"
    "v_add_f32 v3, v3, v31\n\t"
    "v_add_f32 v4, v4, v32\n\t"
    "v_add_f32 v5, v5, v33\n\t"
    "v_add_f32 v6, v6, v34\n\t"
    "v_add_f32 v7, v7, v35\n\t"
    "v_add_f32 v8, v8, v36\n\t"
    "v_add_f32 v9, v9, v37\n\t"
    "v_add_f32 v10, v10, v38\n\t"
    "v_add_f32 v11, v11, v39\n\t"
    "s_waitcnt lgkmcnt(9)\n\t"
    "v_add_f32 v12, v12, v40\n\t"
    "v_add_f32 v13, v13, v41\n\t"
    "v_add_f32 v14, v14, v42\n\t"
    "v_add_f32 v15, v15, v43\n\t"
    "s_waitcnt lgkmcnt(5)\n\t"
    "s_waitcnt lgkmcnt(1)\n\t"
    "L_asp7_%=:\n\t"
    "ds_read_b32 v119, v117 offset:0\n\t"
    "s_waitcnt lgkmcnt(0)\n\t"
    "v_readfirstlane_b32 s46, v119\n\t"
    "s_nop 0\n\t"
    "s_cmp_ge_u32 s46, 4\n\t"
    "s_cbranch_scc1 L_ago7_%=\n\t"
    "s_sleep 1\n\t"
    "s_branch L_asp7_%=\n\t"
    "L_ago7_%=:\n\t"
    "ds_write_b128 v95, v[0:3] offset:4096\n\t"
    "ds_write_b128 v95, v[4:7] offset:9216\n\t"
    "ds_write_b128 v95, v[8:11] offset:13312\n\t"
    "ds_write_b128 v95, v[12:15] offset:19456\n\t"
    "ds_write_b128 v95, v[16:19] offset:5120\n\t"
    "ds_write_b128 v95, v[20:23] offset:20480\n\t"
    "ds_add_u32 v117, v118 offset:4\n\t"
    "L_ab0_%=:\n\t"
    "s_branch L_ab_%=\n\t"
    "L_adma_%=:\n\t"
    "s_sub_u32 s46, s34, 4\n\t"
    "s_add_u32 s69, s51, s46\n\t"
    "s_sub_u32 s44, s69, 125\n\t"
    "s_cmp_ge_u32 s69, 125\n\t"
    "s_cselect_b32 s69, s44, s69\n\t"
    "s_waitcnt lgkmcnt(0)\n\t"
    "s_mov_b32 s92, s18\n\t"
    "s_and_b32 s93, s19, 0xffff\n\t"
    "s_mov_b32 s4, s16\n\t"
    "s_and_b32 s5, s17, 0xffff\n\t"
    "s_and_b32 s11, s11, 0xffff\n\t"
    "s_add_u32 s24, s10, s24\n\t"
    "s_addc_u32 s25, s11, 0\n\t"
    "s_mov_b32 s28, s22\n\t"
    "s_and_b32 s29, s23, 0xffff\n\t"
    "s_add_u32 s12, s12, s35\n\t"
    "s_addc_u32 s13, s13, 0\n\t"
    "s_add_u32 s14, s14, s35\n\t"
    "s_addc_u32 s15, s15, 0\n\t"
    "s_lshl_b32 s35, s8, 3\n\t"
    "s_and_b32 s35, s35, 16\n\t"
    "s_and_b32 s8, s8, -4\n\t"
    "s_barrier\n\t"
    "s_load_dwordx2 s[72:73], s[12:13], 0\n\t"
    "s_load_dwordx2 s[88:89], s[14:15], 0\n\t"
    "s_load_dword s91, s[8:9], 0\n\t"
    "s_load_dwordx2 s[22:23], %1, 0x40\n\t"
    "s_mul_i32 m0, s69, 1040\n\t"
    "s_lshl_b32 s44, s69, 11\n\t"
    "buffer_load_dwordx4 v126, s[4:7], s44 offen lds\n\t"
    "s_add_u32 s69, s69, 2\n\t"
    "s_sub_u32 s44, s69, 125\n\t"
    "s_cmp_ge_u32 s69, 125\n\t"
    "s_cselect_b32 s69, s44, s69\n\t"
    "s_mul_i32 m0, s69, 1040\n\t"
    "s_lshl_b32 s44, s69, 11\n\t"
    "buffer_load_dwordx4 v126, s[4:7], s44 offen lds\n\t"
    "s_add_u32 s69, s69, 2\n\t"
    "s_sub_u32 s44, s69, 125\n\t"
    "s_cmp_ge_u32 s69, 125\n\t"
    "s_cselect_b32 s69, s44, s69\n\t"
    "s_mul_i32 m0, s69, 1040\n\t"
    "s_lshl_b32 s44, s69, 11\n\t"
    "buffer_load_dwordx4 v126, s[4:7], s44 offen lds\n\t"
    "s_add_u32 s69, s69, 2\n\t"
    "s_sub_u32 s44, s69, 125\n\t"
    "s_cmp_ge_u32 s69, 125\n\t"
    "s_cselect_b32 s69, s44, s69\n\t"
    "s_mul_i32 m0, s69, 1040\n\t"
    "s_lshl_b32 s44, s69, 11\n\t"
    "buffer_load_dwordx4 v126, s[4:7], s44 offen lds\n\t"
    "s_add_u32 s69, s69, 2\n\t"
    "s_sub_u32 s44, s69, 125\n\t"
    "s_cmp_ge_u32 s69, 125\n\t"
    "s_cselect_b32 s69, s44, s69\n\t"
    "s_mul_i32 m0, s69, 1040\n\t"
    "s_lshl_b32 s44, s69, 11\n\t"
    "buffer_load_dwordx4 v126, s[4:7], s44 offen lds\n\t"
    "s_add_u32 s69, s69, 2\n\t"
    "s_sub_u32 s44, s69, 125\n\t"
    "s_cmp_ge_u32 s69, 125\n\t"
    "s_cselect_b32 s69, s44, s69\n\t"
    "s_mul_i32 m0, s69, 1040\n\t"
    "s_lshl_b32 s44, s69, 11\n\t"
    "buffer_load_dwordx4 v126, s[4:7], s44 offen lds\n\t"
    "s_add_u32 s69, s69, 2\n\t"
    "s_sub_u32 s44, s69, 125\n\t"
    "s_cmp_ge_u32 s69, 125\n\t"
    "s_cselect_b32 s69, s44, s69\n\t"
    "s_mul_i32 m0, s69, 1040\n\t"
    "s_lshl_b32 s44, s69, 11\n\t"
    "buffer_load_dwordx4 v126, s[4:7], s44 offen lds\n\t"
    "s_add_u32 s69, s69, 2\n\t"
    "s_sub_u32 s44, s69, 125\n\t"
    "s_cmp_ge_u32 s69, 125\n\t"
    "s_cselect_b32 s69, s44, s69\n\t"
    "s_mul_i32 m0, s69, 1040\n\t"
    "s_lshl_b32 s44, s69, 11\n\t"
    "buffer_load_dwordx4 v126, s[4:7], s44 offen lds\n\t"
    "s_add_u32 s69, s69, 2\n\t"
    "s_sub_u32 s44, s69, 125\n\t"
    "s_cmp_ge_u32 s69, 125\n\t"
    "s_cselect_b32 s69, s44, s69\n\t"
    "s_mul_i32 m0, s69, 1040\n\t"
    "s_lshl_b32 s44, s69, 11\n\t"
    "buffer_load_dwordx4 v126, s[4:7], s44 offen lds\n\t"
    "s_add_u32 s69, s69, 2\n\t"
    "s_sub_u32 s44, s69, 125\n\t"
    "s_cmp_ge_u32 s69, 125\n\t"
    "s_cselect_b32 s69, s44, s69\n\t"
    "s_mul_i32 m0, s69, 1040\n\t"
    "s_lshl_b32 s44, s69, 11\n\t"
    "buffer_load_dwordx4 v126, s[4:7], s44 offen lds\n\t"
    "s_add_u32 s69, s69, 2\n\t"
    "s_sub_u32 s44, s69, 125\n\t"
    "s_cmp_ge_u32 s69, 125\n\t"
    "s_cselect_b32 s69, s44, s69\n\t"
    "s_mul_i32 m0, s69, 1040\n\t"
    "s_lshl_b32 s44, s69, 11\n\t"
    "buffer_load_dwordx4 v126, s[4:7], s44 offen lds\n\t"
    "s_add_u32 s69, s69, 2\n\t"
    "s_sub_u32 s44, s69, 125\n\t"
    "s_cmp_ge_u32 s69, 125\n\t"
    "s_cselect_b32 s69, s44, s69\n\t"
    "s_mul_i32 m0, s69, 1040\n\t"
    "s_lshl_b32 s44, s69, 11\n\t"
    "buffer_load_dwordx4 v126, s[4:7], s44 offen lds\n\t"
    "s_add_u32 s69, s69, 2\n\t"
    "s_sub_u32 s44, s69, 125\n\t"
    "s_cmp_ge_u32 s69, 125\n\t"
    "s_cselect_b32 s69, s44, s69\n\t"
    "v_bfe_u32 v154, v147, 2, 2\n\t"
    "v_and_b32 v155, 3, v147\n\t"
    "v_mul_u32_u24 v154, 25, v154\n\t"
    "v_mad_u32_u24 v154, v155, 6, v154\n\t"
    "v_lshlrev_b32 v153, 2, v154\n\t"
    "s_and_b32 s53, s48, 0xff\n\t"
    "s_bfe_u32 s55, s48, 0x80008\n\t"
    "s_add_u32 s54, s53, s55\n\t"
    "s_add_u32 s55, s54, s55\n\t"
    "s_lshr_b32 s64, s48, 16\n\t"
    "v_cmp_eq_u32 s[62:63], 0, v155\n\t"
    "v_and_b32 v151, 48, v147\n\t"
    "v_or_b32 v155, 0x100000, v151\n\t"
    "v_cndmask_b32 v152, v155, v151, s[62:63]\n\t"
    "v_add_u32 v95, 140288, v122\n\t"
    "s_mul_i32 m0, s69, 1040\n\t"
    "s_lshl_b32 s44, s69, 11\n\t"
    "buffer_load_dwordx4 v126, s[4:7], s44 offen lds\n\t"
    "s_add_u32 s69, s69, 2\n\t"
    "s_sub_u32 s44, s69, 125\n\t"
    "s_cmp_ge_u32 s69, 125\n\t"
    "s_cselect_b32 s69, s44, s69\n\t"
    "s_mul_i32 m0, s69, 1040\n\t"
    "s_lshl_b32 s44, s69, 11\n\t"
    "buffer_load_dwordx4 v126, s[4:7], s44 offen lds\n\t"
    "s_add_u32 s69, s69, 2\n\t"
    "s_sub_u32 s44, s69, 125\n\t"
    "s_cmp_ge_u32 s69, 125\n\t"
    "s_cselect_b32 s69, s44, s69\n\t"
    "s_mul_i32 m0, s69, 1040\n\t"
    "s_lshl_b32 s44, s69, 11\n\t"
    "buffer_load_dwordx4 v126, s[4:7], s44 offen lds\n\t"
    "s_add_u32 s69, s69, 2\n\t"
    "s_sub_u32 s44, s69, 125\n\t"
    "s_cmp_ge_u32 s69, 125\n\t"
    "s_cselect_b32 s69, s44, s69\n\t"
    "s_mul_i32 m0, s69, 1040\n\t"
    "s_lshl_b32 s44, s69, 11\n\t"
    "buffer_load_dwordx4 v126, s[4:7], s44 offen lds\n\t"
    "s_add_u32 s69, s69, 2\n\t"
    "s_sub_u32 s44, s69, 125\n\t"
    "s_cmp_ge_u32 s69, 125\n\t"
    "s_cselect_b32 s69, s44, s69\n\t"
    "s_mul_i32 m0, s69, 1040\n\t"
    "s_lshl_b32 s44, s69, 11\n\t"
    "buffer_load_dwordx4 v126, s[4:7], s44 offen lds\n\t"
    "s_add_u32 s69, s69, 2\n\t"
    "s_sub_u32 s44, s69, 125\n\t"
    "s_cmp_ge_u32 s69, 125\n\t"
    "s_cselect_b32 s69, s44, s69\n\t"
    "s_mul_i32 m0, s69, 1040\n\t"
    "s_lshl_b32 s44, s69, 11\n\t"
    "buffer_load_dwordx4 v126, s[4:7], s44 offen lds\n\t"
    "s_add_u32 s69, s69, 2\n\t"
    "s_sub_u32 s44, s69, 125\n\t"
    "s_cmp_ge_u32 s69, 125\n\t"
    "s_cselect_b32 s69, s44, s69\n\t"
    "s_mul_i32 m0, s69, 1040\n\t"
    "s_lshl_b32 s44, s69, 11\n\t"
    "buffer_load_dwordx4 v126, s[4:7], s44 offen lds\n\t"
    "s_add_u32 s69, s69, 2\n\t"
    "s_sub_u32 s44, s69, 125\n\t"
    "s_cmp_ge_u32 s69, 125\n\t"
    "s_cselect_b32 s69, s44, s69\n\t"
    "s_mul_i32 m0, s69, 1040\n\t"
    "s_lshl_b32 s44, s69, 11\n\t"
    "buffer_load_dwordx4 v126, s[4:7], s44 offen lds\n\t"
    "s_add_u32 s69, s69, 2\n\t"
    "s_sub_u32 s44, s69, 125\n\t"
    "s_cmp_ge_u32 s69, 125\n\t"
    "s_cselect_b32 s69, s44, s69\n\t"
    "s_mul_i32 m0, s69, 1040\n\t"
    "s_lshl_b32 s44, s69, 11\n\t"
    "buffer_load_dwordx4 v126, s[4:7], s44 offen lds\n\t"
    "s_add_u32 s69, s69, 2\n\t"
    "s_sub_u32 s44, s69, 125\n\t"
    "s_cmp_ge_u32 s69, 125\n\t"
    "s_cselect_b32 s69, s44, s69\n\t"
    "s_mul_i32 m0, s69, 1040\n\t"
    "s_lshl_b32 s44, s69, 11\n\t"
    "buffer_load_dwordx4 v126, s[4:7], s44 offen lds\n\t"
    "s_add_u32 s69, s69, 2\n\t"
    "s_sub_u32 s44, s69, 125\n\t"
    "s_cmp_ge_u32 s69, 125\n\t"
    "s_cselect_b32 s69, s44, s69\n\t"
    "s_mul_i32 m0, s69, 1040\n\t"
    "s_lshl_b32 s44, s69, 11\n\t"
    "buffer_load_dwordx4 v126, s[4:7], s44 offen lds\n\t"
    "s_add_u32 s69, s69, 2\n\t"
    "s_sub_u32 s44, s69, 125\n\t"
    "s_cmp_ge_u32 s69, 125\n\t"
    "s_cselect_b32 s69, s44, s69\n\t"
    "s_mul_i32 m0, s69, 1040\n\t"
    "s_lshl_b32 s44, s69, 11\n\t"
    "buffer_load_dwordx4 v126, s[4:7], s44 offen lds\n\t"
    "s_add_u32 s69, s69, 2\n\t"
    "s_sub_u32 s44, s69, 125\n\t"
    "s_cmp_ge_u32 s69, 125\n\t"
    "s_cselect_b32 s69, s44, s69\n\t"
    "s_mul_i32 m0, s69, 1040\n\t"
    "s_lshl_b32 s44, s69, 11\n\t"
    "buffer_load_dwordx4 v126, s[4:7], s44 offen lds\n\t"
    "s_add_u32 s69, s69, 2\n\t"
    "s_sub_u32 s44, s69, 125\n\t"
    "s_cmp_ge_u32 s69, 125\n\t"
    "s_cselect_b32 s69, s44, s69\n\t"
    "s_mul_i32 m0, s69, 1040\n\t"
    "s_lshl_b32 s44, s69, 11\n\t"
    "buffer_load_dwordx4 v126, s[4:7], s44 offen lds\n\t"
    "s_add_u32 s69, s69, 2\n\t"
    "s_sub_u32 s44, s69, 125\n\t"
    "s_cmp_ge_u32 s69, 125\n\t"
    "s_cselect_b32 s69, s44, s69\n\t"
    "s_mul_i32 m0, s69, 1040\n\t"
    "s_lshl_b32 s44, s69, 11\n\t"
    "buffer_load_dwordx4 v126, s[4:7], s44 offen lds\n\t"
    "s_add_u32 s69, s69, 2\n\t"
    "s_sub_u32 s44, s69, 125\n\t"
    "s_cmp_ge_u32 s69, 125\n\t"
    "s_cselect_b32 s69, s44, s69\n\t"
    "s_mul_i32 m0, s69, 1040\n\t"
    "s_lshl_b32 s44, s69, 11\n\t"
    "buffer_load_dwordx4 v126, s[4:7], s44 offen lds\n\t"
    "s_add_u32 s69, s69, 2\n\t"
    "s_sub_u32 s44, s69, 125\n\t"
    "s_cmp_ge_u32 s69, 125\n\t"
    "s_cselect_b32 s69, s44, s69\n\t"
    "s_mul_i32 m0, s69, 1040\n\t"
    "s_lshl_b32 s44, s69, 11\n\t"
    "buffer_load_dwordx4 v126, s[4:7], s44 offen lds\n\t"
    "s_add_u32 s69, s69, 2\n\t"
    "s_sub_u32 s44, s69, 125\n\t"
    "s_cmp_ge_u32 s69, 125\n\t"
    "s_cselect_b32 s69, s44, s69\n\t"
    "s_mul_i32 m0, s69, 1040\n\t"
    "s_lshl_b32 s44, s69, 11\n\t"
    "buffer_load_dwordx4 v126, s[4:7], s44 offen lds\n\t"
    "s_add_u32 s69, s69, 2\n\t"
    "s_sub_u32 s44, s69, 125\n\t"
    "s_cmp_ge_u32 s69, 125\n\t"
    "s_cselect_b32 s69, s44, s69\n\t"
    "s_mul_i32 m0, s69, 1040\n\t"
    "s_lshl_b32 s44, s69, 11\n\t"
    "buffer_load_dwordx4 v126, s[4:7], s44 offen lds\n\t"
    "s_add_u32 s69, s69, 2\n\t"
    "s_sub_u32 s44, s69, 125\n\t"
    "s_cmp_ge_u32 s69, 125\n\t"
    "s_cselect_b32 s69, s44, s69\n\t"
    "s_mul_i32 m0, s69, 1040\n\t"
    "s_lshl_b32 s44, s69, 11\n\t"
    "buffer_load_dwordx4 v126, s[4:7], s44 offen lds\n\t"
    "s_add_u32 s69, s69, 2\n\t"
    "s_sub_u32 s44, s69, 125\n\t"
    "s_cmp_ge_u32 s69, 125\n\t"
    "s_cselect_b32 s69, s44, s69\n\t"
    "s_mul_i32 m0, s69, 1040\n\t"
    "s_lshl_b32 s44, s69, 11\n\t"
    "buffer_load_dwordx4 v126, s[4:7], s44 offen lds\n\t"
    "s_add_u32 s69, s69, 2\n\t"
    "s_sub_u32 s44, s69, 125\n\t"
    "s_cmp_ge_u32 s69, 125\n\t"
    "s_cselect_b32 s69, s44, s69\n\t"
    "s_mul_i32 m0, s69, 1040\n\t"
    "s_lshl_b32 s44, s69, 11\n\t"
    "buffer_load_dwordx4 v126, s[4:7], s44 offen lds\n\t"
    "s_add_u32 s69, s69, 2\n\t"
    "s_sub_u32 s44, s69, 125\n\t"
    "s_cmp_ge_u32 s69, 125\n\t"
    "s_cselect_b32 s69, s44, s69\n\t"
    "s_mul_i32 m0, s69, 1040\n\t"
    "s_lshl_b32 s44, s69, 11\n\t"
    "buffer_load_dwordx4 v126, s[4:7], s44 offen lds\n\t"
    "s_add_u32 s69, s69, 2\n\t"
    "s_sub_u32 s44, s69, 125\n\t"
    "s_cmp_ge_u32 s69, 125\n\t"
    "s_cselect_b32 s69, s44, s69\n\t"
    "s_mul_i32 m0, s69, 1040\n\t"
    "s_lshl_b32 s44, s69, 11\n\t"
    "buffer_load_dwordx4 v126, s[4:7], s44 offen lds\n\t"
    "s_add_u32 s69, s69, 2\n\t"
    "s_sub_u32 s44, s69, 125\n\t"
    "s_cmp_ge_u32 s69, 125\n\t"
    "s_cselect_b32 s69, s44, s69\n\t"
    "s_mul_i32 m0, s69, 1040\n\t"
    "s_lshl_b32 s44, s69, 11\n\t"
    "buffer_load_dwordx4 v126, s[4:7], s44 offen lds\n\t"
    "s_add_u32 s69, s69, 2\n\t"
    "s_sub_u32 s44, s69, 125\n\t"
    "s_cmp_ge_u32 s69, 125\n\t"
    "s_cselect_b32 s69, s44, s69\n\t"
    "s_mul_i32 m0, s69, 1040\n\t"
    "s_lshl_b32 s44, s69, 11\n\t"
    "buffer_load_dwordx4 v126, s[4:7], s44 offen lds\n\t"
    "s_add_u32 s69, s69, 2\n\t"
    "s_sub_u32 s44, s69, 125\n\t"
    "s_cmp_ge_u32 s69, 125\n\t"
    "s_cselect_b32 s69, s44, s69\n\t"
    "s_mul_i32 m0, s69, 1040\n\t"
    "s_lshl_b32 s44, s69, 11\n\t"
    "buffer_load_dwordx4 v126, s[4:7], s44 offen lds\n\t"
    "s_add_u32 s69, s69, 2\n\t"
    "s_sub_u32 s44, s69, 125\n\t"
    "s_cmp_ge_u32 s69, 125\n\t"
    "s_cselect_b32 s69, s44, s69\n\t"
    "s_mul_i32 m0, s69, 1040\n\t"
    "s_lshl_b32 s44, s69, 11\n\t"
    "buffer_load_dwordx4 v126, s[4:7], s44 offen lds\n\t"
    "s_add_u32 s69, s69, 2\n\t"
    "s_sub_u32 s44, s69, 125\n\t"
    "s_cmp_ge_u32 s69, 125\n\t"
    "s_cselect_b32 s69, s44, s69\n\t"
    "s_mul_i32 m0, s69, 1040\n\t"
    "s_lshl_b32 s44, s69, 11\n\t"
    "buffer_load_dwordx4 v126, s[4:7], s44 offen lds\n\t"
    "s_add_u32 s69, s69, 2\n\t"
    "s_sub_u32 s44, s69, 125\n\t"
    "s_cmp_ge_u32 s69, 125\n\t"
    "s_cselect_b32 s69, s44, s69\n\t"
    "s_mul_i32 m0, s69, 1040\n\t"
    "s_lshl_b32 s44, s69, 11\n\t"
    "buffer_load_dwordx4 v126, s[4:7], s44 offen lds\n\t"
    "s_add_u32 s69, s69, 2\n\t"
    "s_sub_u32 s44, s69, 125\n\t"
    "s_cmp_ge_u32 s69, 125\n\t"
    "s_cselect_b32 s69, s44, s69\n\t"
    "s_mul_i32 m0, s69, 1040\n\t"
    "s_lshl_b32 s44, s69, 11\n\t"
    "buffer_load_dwordx4 v126, s[4:7], s44 offen lds\n\t"
    "s_add_u32 s69, s69, 2\n\t"
    "s_sub_u32 s44, s69, 125\n\t"
    "s_cmp_ge_u32 s69, 125\n\t"
    "s_cselect_b32 s69, s44, s69\n\t"
    "s_mul_i32 m0, s69, 1040\n\t"
    "s_lshl_b32 s44, s69, 11\n\t"
    "buffer_load_dwordx4 v126, s[4:7], s44 offen lds\n\t"
    "s_add_u32 s69, s69, 2\n\t"
    "s_sub_u32 s44, s69, 125\n\t"
    "s_cmp_ge_u32 s69, 125\n\t"
    "s_cselect_b32 s69, s44, s69\n\t"
    "s_mul_i32 m0, s69, 1040\n\t"
    "s_lshl_b32 s44, s69, 11\n\t"
    "buffer_load_dwordx4 v126, s[4:7], s44 offen lds\n\t"
    "s_add_u32 s69, s69, 2\n\t"
    "s_sub_u32 s44, s69, 125\n\t"
    "s_cmp_ge_u32 s69, 125\n\t"
    "s_cselect_b32 s69, s44, s69\n\t"
    "s_mul_i32 m0, s69, 1040\n\t"
    "s_lshl_b32 s44, s69, 11\n\t"
    "buffer_load_dwordx4 v126, s[4:7], s44 offen lds\n\t"
    "s_add_u32 s69, s69, 2\n\t"
    "s_sub_u32 s44, s69, 125\n\t"
    "s_cmp_ge_u32 s69, 125\n\t"
    "s_cselect_b32 s69, s44, s69\n\t"
    "s_mul_i32 m0, s69, 1040\n\t"
    "s_lshl_b32 s44, s69, 11\n\t"
    "buffer_load_dwordx4 v126, s[4:7], s44 offen lds\n\t"
    "s_add_u32 s69, s69, 2\n\t"
    "s_sub_u32 s44, s69, 125\n\t"
    "s_cmp_ge_u32 s69, 125\n\t"
    "s_cselect_b32 s69, s44, s69\n\t"
    "s_mul_i32 m0, s69, 1040\n\t"
    "s_lshl_b32 s44, s69, 11\n\t"
    "buffer_load_dwordx4 v126, s[4:7], s44 offen lds\n\t"
    "s_add_u32 s69, s69, 2\n\t"
    "s_sub_u32 s44, s69, 125\n\t"
    "s_cmp_ge_u32 s69, 125\n\t"
    "s_cselect_b32 s69, s44, s69\n\t"
    "s_mul_i32 m0, s69, 1040\n\t"
    "s_lshl_b32 s44, s69, 11\n\t"
    "buffer_load_dwordx4 v126, s[4:7], s44 offen lds\n\t"
    "s_add_u32 s69, s69, 2\n\t"
    "s_sub_u32 s44, s69, 125\n\t"
    "s_cmp_ge_u32 s69, 125\n\t"
    "s_cselect_b32 s69, s44, s69\n\t"
    "s_mul_i32 m0, s69, 1040\n\t"
    "s_lshl_b32 s44, s69, 11\n\t"
    "buffer_load_dwordx4 v126, s[4:7], s44 offen lds\n\t"
    "s_add_u32 s69, s69, 2\n\t"
    "s_sub_u32 s44, s69, 125\n\t"
    "s_cmp_ge_u32 s69, 125\n\t"
    "s_cselect_b32 s69, s44, s69\n\t"
    "s_mul_i32 m0, s69, 1040\n\t"
    "s_lshl_b32 s44, s69, 11\n\t"
    "buffer_load_dwordx4 v126, s[4:7], s44 offen lds\n\t"
    "s_add_u32 s69, s69, 2\n\t"
    "s_sub_u32 s44, s69, 125\n\t"
    "s_cmp_ge_u32 s69, 125\n\t"
    "s_cselect_b32 s69, s44, s69\n\t"
    "L_ab_%=:\n\t"
    "s_mul_i32 s66, s53, 400\n\t"
    "buffer_load_dwordx3 v[128:130], v153, s[24:27], s66 offen\n\t"
    "buffer_load_dwordx4 v[132:135], v153, s[24:27], s66 offen offset:12\n\t"
    "s_mul_i32 s66, s54, 400\n\t"
    "buffer_load_dwordx3 v[136:138], v153, s[24:27], s66 offen\n\t"
    "buffer_load_dwordx4 v[140:143], v153, s[24:27], s66 offen offset:12\n\t"
    "s_call_b64 s[98:99], L_setup_%=\n\t"
    "L_asp8_%=:\n\t"
    "ds_read_b32 v119, v117 offset:4\n\t"
    "s_waitcnt lgkmcnt(0)\n\t"
    "v_readfirstlane_b32 s46, v119\n\t"
    "s_nop 0\n\t"
    "s_cmp_ge_u32 s46, 4\n\t"
    "s_cbranch_scc1 L_ago8_%=\n\t"
    "s_sleep 1\n\t"
    "s_branch L_asp8_%=\n\t"
    "L_ago8_%=:\n\t"
    "ds_read_b128 v[0:3], v95 offset:0\n\t"
    "ds_read_b128 v[4:7], v95 offset:1024\n\t"
    "ds_read_b128 v[8:11], v95 offset:2048\n\t"
    "ds_read_b128 v[12:15], v95 offset:3072\n\t"
    "ds_read_b128 v[16:19], v95 offset:4096\n\t"
    "ds_read_b128 v[20:23], v95 offset:5120\n\t"
    "ds_read_b128 v[24:27], v95 offset:6144\n\t"
    "ds_read_b128 v[28:31], v95 offset:7168\n\t"
    "ds_read_b128 v[32:35], v95 offset:8192\n\t"
    "ds_read_b128 v[36:39], v95 offset:9216\n\t"
    "ds_read_b128 v[40:43], v95 offset:10240\n\t"
    "ds_read_b128 v[44:47], v95 offset:11264\n\t"
    "ds_read_b128 v[48:51], v95 offset:12288\n\t"
    "ds_read_b128 v[52:55], v95 offset:13312\n\t"
    "ds_read_b128 v[56:59], v95 offset:14336\n\t"
    "ds_read_b128 v[60:63], v95 offset:15360\n\t"
    "ds_read_b128 v[64:67], v95 offset:16384\n\t"
    "ds_read_b128 v[68:71], v95 offset:17408\n\t"
    "ds_read_b128 v[72:75], v95 offset:18432\n\t"
    "ds_read_b128 v[76:79], v95 offset:19456\n\t"
    "ds_read_b128 v[80:83], v95 offset:20480\n\t"
    "ds_read_b128 v[84:87], v95 offset:21504\n\t"
    "ds_read_b128 v[88:91], v95 offset:22528\n\t"
    "s_waitcnt vmcnt(4)\n\t"
    "ds_add_u32 v117, v118 offset:8\n\t"
    "s_waitcnt vmcnt(0)\n\t"
    "s_branch L_awd_%=\n\t"
    "s_waitcnt vmcnt(4)\n\t"
    "ds_add_u32 v117, v118 offset:8\n\t"
    "s_branch L_await_%=\n\t"
    "L_setup_%=:\n\t"
    "v_mbcnt_lo_u32_b32 v100, -1, 0\n\t"
    "v_mbcnt_hi_u32_b32 v100, -1, v100\n\t"
    "s_lshl_b32 s61, s34, 15\n\t"
    "s_add_u32 s61, s61, 16515072\n\t"
    "v_lshlrev_b32 v127, 4, v100\n\t"
    "s_lshr_b32 s37, %2, 4\n\t"
    "s_and_b32 s39, %2, 7\n\t"
    "v_and_b32 v101, 15, v100\n\t"
    "s_lshl_b32 s39, s39, 4\n\t"
    "s_add_u32 s39, s39, s37\n\t"
    "s_mul_i32 s65, s34, 125\n\t"
    "v_add_u32 v155, s65, v101\n\t"
    "s_mul_i32 s63, s39, 102400\n\t"
    "s_add_u32 s63, s63, 3407872\n\t"
    "v_lshrrev_b32 v102, 2, v101\n\t"
    "s_and_b32 s37, s37, 7\n\t"
    "s_lshl_b32 s37, s37, 11\n\t"
    "v_lshrrev_b32 v103, 3, v100\n\t"
    "s_mul_i32 s36, s39, 65536\n\t"
    "s_bfe_u32 s65, %2, 0x10003\n\t"
    "v_and_b32 v103, 2, v103\n\t"
    "s_lshl_b32 s38, s65, 15\n\t"
    "s_add_u32 s36, s36, s38\n\t"
    "v_and_b32 v104, 1, v100\n\t"
    "s_lshl_b32 s38, s34, 12\n\t"
    "s_add_u32 s36, s36, s38\n\t"
    "v_lshlrev_b32 v104, 4, v104\n\t"
    "s_add_u32 s36, s36, 17825792\n\t"
    "v_mov_b32 v101, 0x3f80\n\t"
    "v_lshlrev_b32 v104, v104, v101\n\t"
    "v_cmp_eq_u32 vcc, v102, v103\n\t"
    "s_waitcnt lgkmcnt(0)\n\t"
    "s_lshr_b32 s39, s91, s35\n\t"
    "s_lshl_b32 s39, s39, 16\n\t"
    "v_cndmask_b32 v120, 0, v104, vcc\n\t"
    "v_cndmask_b32 v121, 0, v104, vcc\n\t"
    "v_add_u32 v103, 1, v103\n\t"
    "v_cmp_eq_u32 vcc, v102, v103\n\t"
    "s_lshl_b32 s40, s88, 16\n\t"
    "s_and_b32 s41, s88, 0xffff0000\n\t"
    "v_cndmask_b32 v122, 0, v104, vcc\n\t"
    "v_cndmask_b32 v123, 0, v104, vcc\n\t"
    "v_and_b32 v101, 2, v100\n\t"
    "v_mov_b32 v102, 0x44444444\n\t"
    "v_mov_b32 v103, 0xeeeeeeee\n\t"
    "v_cmp_eq_u32 vcc, 0, v101\n\t"
    "s_lshl_b32 s42, s89, 16\n\t"
    "s_and_b32 s43, s89, 0xffff0000\n\t"
    "v_cndmask_b32 v124, v103, v102, vcc\n\t"
    "v_lshrrev_b32 v101, 4, v100\n\t"
    "v_mul_u32_u24 v101, 8000, v101\n\t"
    "v_lshl_add_u32 v125, v155, 3, v101\n\t"
    "v_add_u32 v125, 64000, v125\n\t"
    "s_lshl_b32 s70, s72, 16\n\t"
    "s_and_b32 s71, s72, 0xffff0000\n\t"
    "s_lshl_b32 s72, s73, 16\n\t"
    "s_and_b32 s73, s73, 0xffff0000\n\t"
    "s_mov_b32 s67, 0xffff0000\n\t"
    "s_and_b32 s65, %2, 8\n\t"
    "s_add_u32 s38, s65, s34\n\t"
    "s_mul_i32 s38, s38, 212992\n\t"
    "s_add_u32 s38, s38, 196608\n\t"
    "s_add_u32 s84, s37, s38\n\t"
    "s_add_u32 s37, s37, 0x800\n\t"
    "s_and_b32 s37, s37, 0x3800\n\t"
    "s_add_u32 s85, s37, s38\n\t"
    "s_add_u32 s37, s37, 0x800\n\t"
    "s_and_b32 s37, s37, 0x3800\n\t"
    "s_add_u32 s86, s37, s38\n\t"
    "s_add_u32 s37, s37, 0x800\n\t"
    "s_and_b32 s37, s37, 0x3800\n\t"
    "s_add_u32 s87, s37, s38\n\t"
    "s_add_u32 s37, s37, 0x800\n\t"
    "s_and_b32 s37, s37, 0x3800\n\t"
    "s_add_u32 s88, s37, s38\n\t"
    "s_add_u32 s37, s37, 0x800\n\t"
    "s_and_b32 s37, s37, 0x3800\n\t"
    "s_add_u32 s89, s37, s38\n\t"
    "s_add_u32 s37, s37, 0x800\n\t"
    "s_and_b32 s37, s37, 0x3800\n\t"
    "s_add_u32 s90, s37, s38\n\t"
    "s_add_u32 s37, s37, 0x800\n\t"
    "s_and_b32 s37, s37, 0x3800\n\t"
    "s_add_u32 s91, s37, s38\n\t"
    "s_mul_i32 s94, s65, 425984\n\t"
    "s_sub_u32 s94, s94, 1703936\n\t"
    "s_sub_u32 s95, 0x8000, s94\n\t"
    "s_lshl_b32 s97, s65, 11\n\t"
    "s_sub_u32 s97, s97, 229376\n\t"
    "s_mov_b32 s92, 64000\n\t"
    "s_mov_b32 s93, -64000\n\t"
    "s_mov_b32 s96, 0x2000\n\t"
    "s_mov_b32 s62, 2\n\t"
    "v_rcp_f32 v101, s40\n\t"
    "v_rcp_f32 v102, s41\n\t"
    "v_rcp_f32 v103, s42\n\t"
    "v_rcp_f32 v104, s43\n\t"
    "v_mov_b32 v100, 0x7f7fffff\n\t"
    "v_med3_f32 v101, v101, -v100, v100\n\t"
    "v_med3_f32 v102, v102, -v100, v100\n\t"
    "v_med3_f32 v103, v103, -v100, v100\n\t"
    "v_med3_f32 v104, v104, -v100, v100\n\t"
    "v_readfirstlane_b32 s40, v101\n\t"
    "v_readfirstlane_b32 s41, v102\n\t"
    "v_readfirstlane_b32 s42, v103\n\t"
    "v_readfirstlane_b32 s43, v104\n\t"
    "s_setpc_b64 s[98:99]\n\t"
    "L_aside_%=:\n\t"
    "s_setprio 3\n\t"
    "v_cmp_gt_u32 vcc, 48, v147\n\t"
    "v_mov_b32 v133, 0x40000000\n\t"
    "v_lshrrev_b32 v134, 3, v147\n\t"
    "v_bfe_u32 v135, v147, 2, 1\n\t"
    "v_and_b32 v130, 3, v147\n\t"
    "v_lshl_add_u32 v132, v135, 5, v130\n\t"
    "v_mul_u32_u24 v64, 400, v132\n\t"
    "v_lshl_add_u32 v64, v134, 5, v64\n\t"
    "s_movk_i32 s50, 0\n\t"
    "buffer_load_dwordx4 v[0:3], v64, s[24:27], s50 offen offset:16\n\t"
    "buffer_load_dwordx4 v[4:7], v64, s[24:27], s50 offen offset:32\n\t"
    "s_movk_i32 s50, 0\n\t"
    "buffer_load_dwordx4 v[8:11], v64, s[24:27], s50 offen offset:1616\n\t"
    "buffer_load_dwordx4 v[12:15], v64, s[24:27], s50 offen offset:1632\n\t"
    "s_movk_i32 s50, 3200\n\t"
    "buffer_load_dwordx4 v[16:19], v64, s[24:27], s50 offen offset:16\n\t"
    "buffer_load_dwordx4 v[20:23], v64, s[24:27], s50 offen offset:32\n\t"
    "v_lshlrev_b32 v66, 14, v134\n\t"
    "v_lshl_add_u32 v66, v132, 4, v66\n\t"
    "v_lshlrev_b32 v68, 8, v134\n\t"
    "v_lshl_add_u32 v68, v135, 2, v68\n\t"
    "v_add_u32 v68, 130048, v68\n\t"
    "v_cndmask_b32 v68, v133, v68, vcc\n\t"
    "v_cndmask_b32 v66, v133, v66, vcc\n\t"
    "v_lshrrev_b32 v134, 2, v147\n\t"
    "v_bfe_u32 v135, v147, 1, 1\n\t"
    "v_and_b32 v130, 1, v147\n\t"
    "v_lshl_add_u32 v132, v135, 5, v130\n\t"
    "v_mul_u32_u24 v65, 400, v132\n\t"
    "v_lshl_add_u32 v65, v134, 5, v65\n\t"
    "v_lshlrev_b32 v67, 14, v134\n\t"
    "v_lshl_add_u32 v67, v132, 4, v67\n\t"
    "v_lshlrev_b32 v69, 8, v134\n\t"
    "v_lshl_add_u32 v69, v135, 2, v69\n\t"
    "v_add_u32 v69, 130048, v69\n\t"
    "v_cndmask_b32 v69, v133, v69, vcc\n\t"
    "v_cndmask_b32 v67, v133, v67, vcc\n\t"
    "s_barrier\n\t"
    "s_load_dwordx2 s[72:73], s[12:13], 0\n\t"
    "s_load_dwordx2 s[88:89], s[14:15], 0\n\t"
    "s_load_dword s91, s[8:9], 0\n\t"
    "s_load_dwordx2 s[22:23], %1, 0x40\n\t"
    "v_lshlrev_b32 v128, 4, v147\n\t"
    "v_add_u32 v128, 133120, v128\n\t"
    "v_and_b32 v130, 1, v147\n\t"
    "v_cndmask_b32 v128, v133, v128, vcc\n\t"
    "v_mul_u32_u24 v127, 7, v130\n\t"
    "v_cmp_eq_u32 vcc, 1, v130\n\t"
    "v_mov_b32 v126, 0x07060100\n\t"
    "v_mov_b32 v133, 0x05040100\n\t"
    "v_mov_b32 v136, 1\n\t"
    "v_cndmask_b32 v126, v126, v133, vcc\n\t"
    "s_mov_b32 s36, 0\n\t"
    "s_mov_b32 s37, 8000\n\t"
    "s_mov_b32 s38, 16000\n\t"
    "s_mov_b32 s39, 24000\n\t"
    "s_mov_b32 s40, 32000\n\t"
    "s_mov_b32 s41, 40000\n\t"
    "s_mov_b32 s42, 48000\n\t"
    "s_mov_b32 s43, 56000\n\t"
    "s_movk_i32 s44, 0xf8\n\t"
    "s_mov_b32 s45, 0x01010000\n\t"
    "s_mov_b32 s46, 0x020c000c\n\t"
    "s_lshr_b32 s49, %2, 4\n\t"
    "s_mul_i32 s49, s49, 212992\n\t"
    "s_and_b32 s50, %2, 15\n\t"
    "s_lshl_b32 s50, s50, 10\n\t"
    "s_add_u32 s49, s49, s50\n\t"
    "v_lshrrev_b32 v134, 4, v147\n\t"
    "v_bfe_u32 v135, v147, 2, 2\n\t"
    "v_and_b32 v130, 3, v147\n\t"
    "v_lshlrev_b32 v137, 17, v134\n\t"
    "v_lshl_add_u32 v137, v135, 10, v137\n\t"
    "v_lshl_add_u32 v137, v130, 4, v137\n\t"
    "v_add_u32 v138, 0x1000, v137\n\t"
    "v_mul_u32_u24 v141, 0x210, v134\n\t"
    "v_lshl_add_u32 v141, v135, 6, v141\n\t"
    "v_lshl_add_u32 v141, v130, 4, v141\n\t"
    "v_add_u32 v141, 134656, v141\n\t"
    "v_mul_u32_u24 v142, 0x210, v130\n\t"
    "v_lshl_add_u32 v142, v135, 6, v142\n\t"
    "v_lshl_add_u32 v142, v134, 3, v142\n\t"
    "v_add_u32 v142, 134656, v142\n\t"
    "s_mov_b32 s50, 102400\n\t"
    "v_mul_u32_u24 v139, s50, v134\n\t"
    "v_and_b32 v132, 15, v147\n\t"
    "v_lshl_add_u32 v139, v132, 4, v139\n\t"
    "v_add_u32 v140, 409600, v139\n\t"
    "s_lshr_b32 s50, %2, 4\n\t"
    "s_lshl_b32 s47, s50, 13\n\t"
    "s_lshl_b32 s48, s50, 8\n\t"
    "s_and_b32 s50, %2, 3\n\t"
    "s_mul_i32 s51, s50, 256\n\t"
    "s_add_u32 s47, s47, s51\n\t"
    "s_mul_i32 s51, s50, 3276800\n\t"
    "s_add_u32 s48, s48, s51\n\t"
    "s_bfe_u32 s50, %2, 131074\n\t"
    "s_lshl_b32 s51, s50, 19\n\t"
    "s_add_u32 s47, s47, s51\n\t"
    "s_lshl_b32 s51, s50, 12\n\t"
    "s_add_u32 s48, s48, s51\n\t"
    "s_add_u32 s48, s48, 3407872\n\t"
    "s_waitcnt vmcnt(6)\n\t"
    "v_lshlrev_b32 v134, 20, v147\n\t"
    "v_add_u32 v134, 2068, v134\n\t"
    "ds_add_u32 v134, v136\n\t"
    "v_mov_b32 v110, 65664\n\t"
    "v_mov_b32 v111, 131136\n\t"
    "v_mov_b32 v112, 262176\n\t"
    "v_mov_b32 v113, 524304\n\t"
    "v_mov_b32 v114, 1048584\n\t"
    "v_mov_b32 v115, 2097156\n\t"
    "v_mov_b32 v116, 4194306\n\t"
    "v_mov_b32 v117, 8388609\n\t"
    "s_waitcnt vmcnt(4)\n\t"
    "v_lshl_add_u32 v76, v0, 3, s36\n\t"
    "v_lshl_add_u32 v77, v1, 3, s37\n\t"
    "v_lshl_add_u32 v78, v2, 3, s38\n\t"
    "v_lshl_add_u32 v79, v3, 3, s39\n\t"
    "v_lshl_add_u32 v80, v4, 3, s40\n\t"
    "v_lshl_add_u32 v81, v5, 3, s41\n\t"
    "v_lshl_add_u32 v82, v6, 3, s42\n\t"
    "v_lshl_add_u32 v83, v7, 3, s43\n\t"
    "v_and_or_b32 v92, v76, s44, v68\n\t"
    "ds_or_rtn_b32 v118, v92, v110\n\t"
    "v_and_or_b32 v93, v77, s44, v68\n\t"
    "ds_or_rtn_b32 v119, v93, v111\n\t"
    "v_and_or_b32 v94, v78, s44, v68\n\t"
    "ds_or_rtn_b32 v120, v94, v112\n\t"
    "v_and_or_b32 v95, v79, s44, v68\n\t"
    "ds_or_rtn_b32 v121, v95, v113\n\t"
    "v_and_or_b32 v96, v80, s44, v68\n\t"
    "ds_or_rtn_b32 v122, v96, v114\n\t"
    "v_and_or_b32 v97, v81, s44, v68\n\t"
    "ds_or_rtn_b32 v123, v97, v115\n\t"
    "v_and_or_b32 v98, v82, s44, v68\n\t"
    "ds_or_rtn_b32 v124, v98, v116\n\t"
    "v_and_or_b32 v99, v83, s44, v68\n\t"
    "ds_or_rtn_b32 v125, v99, v117\n\t"
    "v_lshl_or_b32 v164, v77, 16, v76\n\t"
    "v_lshl_or_b32 v165, v79, 16, v78\n\t"
    "v_lshl_or_b32 v166, v81, 16, v80\n\t"
    "v_lshl_or_b32 v167, v83, 16, v82\n\t"
    "s_waitcnt vmcnt(2)\n\t"
    "v_lshl_add_u32 v84, v8, 3, s36\n\t"
    "v_lshl_add_u32 v85, v9, 3, s37\n\t"
    "v_lshl_add_u32 v86, v10, 3, s38\n\t"
    "v_lshl_add_u32 v87, v11, 3, s39\n\t"
    "v_lshl_add_u32 v88, v12, 3, s40\n\t"
    "v_lshl_add_u32 v89, v13, 3, s41\n\t"
    "v_lshl_add_u32 v90, v14, 3, s42\n\t"
    "v_lshl_add_u32 v91, v15, 3, s43\n\t"
    "s_waitcnt lgkmcnt(4)\n\t"
    "v_and_b32 v118, v118, v110\n\t"
    "v_perm_b32 v118, v118, v118, s46\n\t"
    "ds_or_b32 v92, v118\n\t"
    "v_and_b32 v119, v119, v111\n\t"
    "v_perm_b32 v119, v119, v119, s46\n\t"
    "ds_or_b32 v93, v119\n\t"
    "v_and_b32 v120, v120, v112\n\t"
    "v_perm_b32 v120, v120, v120, s46\n\t"
    "ds_or_b32 v94, v120\n\t"
    "v_and_b32 v121, v121, v113\n\t"
    "v_perm_b32 v121, v121, v121, s46\n\t"
    "ds_or_b32 v95, v121\n\t"
    "s_waitcnt lgkmcnt(4)\n\t"
    "v_and_b32 v122, v122, v114\n\t"
    "v_perm_b32 v122, v122, v122, s46\n\t"
    "ds_or_b32 v96, v122\n\t"
    "v_and_b32 v123, v123, v115\n\t"
    "v_perm_b32 v123, v123, v123, s46\n\t"
    "ds_or_b32 v97, v123\n\t"
    "v_and_b32 v124, v124, v116\n\t"
    "v_perm_b32 v124, v124, v124, s46\n\t"
    "ds_or_b32 v98, v124\n\t"
    "v_and_b32 v125, v125, v117\n\t"
    "v_perm_b32 v125, v125, v125, s46\n\t"
    "ds_or_b32 v99, v125\n\t"
    "buffer_store_dwordx4 v[164:167], v66, s[28:31], s49 offen offset:0 sc1\n\t"
    "v_and_or_b32 v100, v84, s44, v68\n\t"
    "ds_read_b32 v100, v100\n\t"
    "v_and_or_b32 v101, v85, s44, v68\n\t"
    "ds_read_b32 v101, v101\n\t"
    "v_and_or_b32 v102, v86, s44, v68\n\t"
    "ds_read_b32 v102, v102\n\t"
    "v_and_or_b32 v103, v87, s44, v68\n\t"
    "ds_read_b32 v103, v103\n\t"
    "v_and_or_b32 v104, v88, s44, v68\n\t"
    "ds_read_b32 v104, v104\n\t"
    "v_and_or_b32 v105, v89, s44, v68\n\t"
    "ds_read_b32 v105, v105\n\t"
    "v_and_or_b32 v106, v90, s44, v68\n\t"
    "ds_read_b32 v106, v106\n\t"
    "v_and_or_b32 v107, v91, s44, v68\n\t"
    "ds_read_b32 v107, v107\n\t"
    "s_waitcnt vmcnt(1)\n\t"
    "v_lshl_add_u32 v76, v16, 3, s36\n\t"
    "v_lshl_add_u32 v77, v17, 3, s37\n\t"
    "v_lshl_add_u32 v78, v18, 3, s38\n\t"
    "v_lshl_add_u32 v79, v19, 3, s39\n\t"
    "v_lshl_add_u32 v80, v20, 3, s40\n\t"
    "v_lshl_add_u32 v81, v21, 3, s41\n\t"
    "v_lshl_add_u32 v82, v22, 3, s42\n\t"
    "v_lshl_add_u32 v83, v23, 3, s43\n\t"
    "s_movk_i32 s50, 3200\n\t"
    "buffer_load_dwordx4 v[24:27], v64, s[24:27], s50 offen offset:1616\n\t"
    "buffer_load_dwordx4 v[28:31], v64, s[24:27], s50 offen offset:1632\n\t"
    "s_movk_i32 s50, 6400\n\t"
    "buffer_load_dwordx4 v[32:35], v64, s[24:27], s50 offen offset:16\n\t"
    "buffer_load_dwordx4 v[36:39], v64, s[24:27], s50 offen offset:32\n\t"
    "v_mov_b32 v129, 0xffff0000\n\t"
    "s_waitcnt lgkmcnt(4)\n\t"
    "v_perm_b32 v100, v100, v84, v126\n\t"
    "v_perm_b32 v101, v101, v85, v126\n\t"
    "v_perm_b32 v102, v102, v86, v126\n\t"
    "v_perm_b32 v103, v103, v87, v126\n\t"
    "s_waitcnt lgkmcnt(0)\n\t"
    "v_perm_b32 v104, v104, v88, v126\n\t"
    "v_perm_b32 v105, v105, v89, v126\n\t"
    "v_perm_b32 v106, v106, v90, v126\n\t"
    "v_perm_b32 v107, v107, v91, v126\n\t"
    "v_max_u32 v108, v100, v101\n\t"
    "v_min_u32 v101, v100, v101\n\t"
    "v_max_u32 v100, v102, v103\n\t"
    "v_min_u32 v103, v102, v103\n\t"
    "v_max_u32 v102, v104, v105\n\t"
    "v_min_u32 v105, v104, v105\n\t"
    "v_max_u32 v104, v106, v107\n\t"
    "v_min_u32 v107, v106, v107\n\t"
    "v_max_u32 v106, v108, v100\n\t"
    "v_min_u32 v100, v108, v100\n\t"
    "v_max_u32 v108, v101, v103\n\t"
    "v_min_u32 v103, v101, v103\n\t"
    "v_max_u32 v101, v102, v104\n\t"
    "v_min_u32 v104, v102, v104\n\t"
    "v_max_u32 v102, v105, v107\n\t"
    "v_min_u32 v107, v105, v107\n\t"
    "v_bfi_b32 v130, v106, 0, v129\n\t"
    "v_cmp_eq_u32 vcc, 0, v130\n\t"
    "v_and_or_b32 v92, v106, s44, v68\n\t"
    "s_nop 0\n\t"
    "v_cndmask_b32 v130, v130, v129, vcc\n\t"
    "v_ffbl_b32 v131, v130\n\t"
    "v_and_b32 v134, 7, v131\n\t"
    "v_lshlrev_b32 v134, v134, s45\n\t"
    "v_xor_b32 v129, v129, v134\n\t"
    "v_xor_b32 v132, v131, v127\n\t"
    "v_bfi_b32 v130, v101, 0, v129\n\t"
    "v_cmp_eq_u32 vcc, 0, v130\n\t"
    "v_and_or_b32 v93, v101, s44, v68\n\t"
    "v_xor_b32 v133, 23, v132\n\t"
    "v_lshlrev_b32 v110, v132, v136\n\t"
    "v_and_b32 v132, 7, v132\n\t"
    "v_lshl_or_b32 v110, v136, v133, v110\n\t"
    "v_lshl_add_u32 v132, v132, 1, v128\n\t"
    "ds_or_rtn_b32 v118, v92, v110\n\t"
    "ds_write_b16 v132, v106\n\t"
    "v_cndmask_b32 v130, v130, v129, vcc\n\t"
    "v_ffbl_b32 v131, v130\n\t"
    "v_and_b32 v134, 7, v131\n\t"
    "v_lshlrev_b32 v134, v134, s45\n\t"
    "v_xor_b32 v129, v129, v134\n\t"
    "v_xor_b32 v132, v131, v127\n\t"
    "v_bfi_b32 v130, v108, 0, v129\n\t"
    "v_cmp_eq_u32 vcc, 0, v130\n\t"
    "v_and_or_b32 v94, v108, s44, v68\n\t"
    "v_xor_b32 v133, 23, v132\n\t"
    "v_lshlrev_b32 v111, v132, v136\n\t"
    "v_and_b32 v132, 7, v132\n\t"
    "v_lshl_or_b32 v111, v136, v133, v111\n\t"
    "v_lshl_add_u32 v132, v132, 1, v128\n\t"
    "ds_or_rtn_b32 v119, v93, v111\n\t"
    "ds_write_b16 v132, v101\n\t"
    "v_cndmask_b32 v130, v130, v129, vcc\n\t"
    "v_ffbl_b32 v131, v130\n\t"
    "v_and_b32 v134, 7, v131\n\t"
    "v_lshlrev_b32 v134, v134, s45\n\t"
    "v_xor_b32 v129, v129, v134\n\t"
    "v_xor_b32 v132, v131, v127\n\t"
    "v_bfi_b32 v130, v100, 0, v129\n\t"
    "v_cmp_eq_u32 vcc, 0, v130\n\t"
    "v_and_or_b32 v95, v100, s44, v68\n\t"
    "v_xor_b32 v133, 23, v132\n\t"
    "v_lshlrev_b32 v112, v132, v136\n\t"
    "v_and_b32 v132, 7, v132\n\t"
    "v_lshl_or_b32 v112, v136, v133, v112\n\t"
    "v_lshl_add_u32 v132, v132, 1, v128\n\t"
    "ds_or_rtn_b32 v120, v94, v112\n\t"
    "ds_write_b16 v132, v108\n\t"
    "v_cndmask_b32 v130, v130, v129, vcc\n\t"
    "v_ffbl_b32 v131, v130\n\t"
    "v_and_b32 v134, 7, v131\n\t"
    "v_lshlrev_b32 v134, v134, s45\n\t"
    "v_xor_b32 v129, v129, v134\n\t"
    "v_xor_b32 v132, v131, v127\n\t"
    "v_bfi_b32 v130, v102, 0, v129\n\t"
    "v_cmp_eq_u32 vcc, 0, v130\n\t"
    "v_and_or_b32 v96, v102, s44, v68\n\t"
    "v_xor_b32 v133, 23, v132\n\t"
    "v_lshlrev_b32 v113, v132, v136\n\t"
    "v_and_b32 v132, 7, v132\n\t"
    "v_lshl_or_b32 v113, v136, v133, v113\n\t"
    "v_lshl_add_u32 v132, v132, 1, v128\n\t"
    "ds_or_rtn_b32 v121, v95, v113\n\t"
    "ds_write_b16 v132, v100\n\t"
    "v_cndmask_b32 v130, v130, v129, vcc\n\t"
    "v_ffbl_b32 v131, v130\n\t"
    "v_and_b32 v134, 7, v131\n\t"
    "v_lshlrev_b32 v134, v134, s45\n\t"
    "v_xor_b32 v129, v129, v134\n\t"
    "v_xor_b32 v132, v131, v127\n\t"
    "s_waitcnt lgkmcnt(7)\n\t"
    "v_and_b32 v118, v118, v110\n\t"
    "v_perm_b32 v118, v118, v118, s46\n\t"
    "ds_or_b32 v92, v118\n\t"
    "v_bfi_b32 v130, v104, 0, v129\n\t"
    "v_cmp_eq_u32 vcc, 0, v130\n\t"
    "v_and_or_b32 v97, v104, s44, v68\n\t"
    "v_xor_b32 v133, 23, v132\n\t"
    "v_lshlrev_b32 v114, v132, v136\n\t"
    "v_and_b32 v132, 7, v132\n\t"
    "v_lshl_or_b32 v114, v136, v133, v114\n\t"
    "v_lshl_add_u32 v132, v132, 1, v128\n\t"
    "ds_or_rtn_b32 v122, v96, v114\n\t"
    "ds_write_b16 v132, v102\n\t"
    "v_cndmask_b32 v130, v130, v129, vcc\n\t"
    "v_ffbl_b32 v131, v130\n\t"
    "v_and_b32 v134, 7, v131\n\t"
    "v_lshlrev_b32 v134, v134, s45\n\t"
    "v_xor_b32 v129, v129, v134\n\t"
    "v_xor_b32 v132, v131, v127\n\t"
    "s_waitcnt lgkmcnt(8)\n\t"
    "v_and_b32 v119, v119, v111\n\t"
    "v_perm_b32 v119, v119, v119, s46\n\t"
    "ds_or_b32 v93, v119\n\t"
    "v_bfi_b32 v130, v103, 0, v129\n\t"
    "v_cmp_eq_u32 vcc, 0, v130\n\t"
    "v_and_or_b32 v98, v103, s44, v68\n\t"
    "v_xor_b32 v133, 23, v132\n\t"
    "v_lshlrev_b32 v115, v132, v136\n\t"
    "v_and_b32 v132, 7, v132\n\t"
    "v_lshl_or_b32 v115, v136, v133, v115\n\t"
    "v_lshl_add_u32 v132, v132, 1, v128\n\t"
    "ds_or_rtn_b32 v123, v97, v115\n\t"
    "ds_write_b16 v132, v104\n\t"
    "v_cndmask_b32 v130, v130, v129, vcc\n\t"
    "v_ffbl_b32 v131, v130\n\t"
    "v_and_b32 v134, 7, v131\n\t"
    "v_lshlrev_b32 v134, v134, s45\n\t"
    "v_xor_b32 v129, v129, v134\n\t"
    "v_xor_b32 v132, v131, v127\n\t"
    "s_waitcnt lgkmcnt(9)\n\t"
    "v_and_b32 v120, v120, v112\n\t"
    "v_perm_b32 v120, v120, v120, s46\n\t"
    "ds_or_b32 v94, v120\n\t"
    "v_bfi_b32 v130, v107, 0, v129\n\t"
    "v_cmp_eq_u32 vcc, 0, v130\n\t"
    "v_and_or_b32 v99, v107, s44, v68\n\t"
    "v_xor_b32 v133, 23, v132\n\t"
    "v_lshlrev_b32 v116, v132, v136\n\t"
    "v_and_b32 v132, 7, v132\n\t"
    "v_lshl_or_b32 v116, v136, v133, v116\n\t"
    "v_lshl_add_u32 v132, v132, 1, v128\n\t"
    "ds_or_rtn_b32 v124, v98, v116\n\t"
    "ds_write_b16 v132, v103\n\t"
    "v_cndmask_b32 v130, v130, v129, vcc\n\t"
    "v_ffbl_b32 v131, v130\n\t"
    "v_xor_b32 v132, v131, v127\n\t"
    "s_waitcnt lgkmcnt(10)\n\t"
    "v_and_b32 v121, v121, v113\n\t"
    "v_perm_b32 v121, v121, v121, s46\n\t"
    "ds_or_b32 v95, v121\n\t"
    "v_xor_b32 v133, 23, v132\n\t"
    "v_lshlrev_b32 v117, v132, v136\n\t"
    "v_and_b32 v132, 7, v132\n\t"
    "v_lshl_or_b32 v117, v136, v133, v117\n\t"
    "v_lshl_add_u32 v132, v132, 1, v128\n\t"
    "ds_or_rtn_b32 v125, v99, v117\n\t"
    "ds_write_b16 v132, v107\n\t"
    "s_waitcnt lgkmcnt(10)\n\t"
    "v_and_b32 v122, v122, v114\n\t"
    "v_perm_b32 v122, v122, v122, s46\n\t"
    "ds_or_b32 v96, v122\n\t"
    "ds_read_b128 v[72:75], v128\n\t"
    "v_and_or_b32 v100, v76, s44, v68\n\t"
    "ds_read_b32 v100, v100\n\t"
    "v_and_or_b32 v101, v77, s44, v68\n\t"
    "ds_read_b32 v101, v101\n\t"
    "v_and_or_b32 v102, v78, s44, v68\n\t"
    "ds_read_b32 v102, v102\n\t"
    "v_and_or_b32 v103, v79, s44, v68\n\t"
    "ds_read_b32 v103, v103\n\t"
    "v_and_or_b32 v104, v80, s44, v68\n\t"
    "ds_read_b32 v104, v104\n\t"
    "v_and_or_b32 v105, v81, s44, v68\n\t"
    "ds_read_b32 v105, v105\n\t"
    "v_and_or_b32 v106, v82, s44, v68\n\t"
    "ds_read_b32 v106, v106\n\t"
    "v_and_or_b32 v107, v83, s44, v68\n\t"
    "ds_read_b32 v107, v107\n\t"
    "s_waitcnt vmcnt(2)\n\t"
    "v_lshl_add_u32 v84, v24, 3, s36\n\t"
    "v_lshl_add_u32 v85, v25, 3, s37\n\t"
    "v_lshl_add_u32 v86, v26, 3, s38\n\t"
    "v_lshl_add_u32 v87, v27, 3, s39\n\t"
    "v_lshl_add_u32 v88, v28, 3, s40\n\t"
    "v_lshl_add_u32 v89, v29, 3, s41\n\t"
    "v_lshl_add_u32 v90, v30, 3, s42\n\t"
    "v_lshl_add_u32 v91, v31, 3, s43\n\t"
    "s_movk_i32 s50, 6400\n\t"
    "buffer_load_dwordx4 v[40:43], v65, s[24:27], s50 offen offset:1616\n\t"
    "buffer_load_dwordx4 v[44:47], v65, s[24:27], s50 offen offset:1632\n\t"
    "s_movk_i32 s50, 6400\n\t"
    "buffer_load_dwordx4 v[48:51], v65, s[24:27], s50 offen offset:2416\n\t"
    "buffer_load_dwordx4 v[52:55], v65, s[24:27], s50 offen offset:2432\n\t"
    "s_movk_i32 s50, 9600\n\t"
    "buffer_load_dwordx4 v[56:59], v65, s[24:27], s50 offen offset:16\n\t"
    "buffer_load_dwordx4 v[60:63], v65, s[24:27], s50 offen offset:32\n\t"
    "s_movk_i32 s50, 9600\n\t"
    "buffer_load_dwordx4 v[0:3], v65, s[24:27], s50 offen offset:816\n\t"
    "buffer_load_dwordx4 v[4:7], v65, s[24:27], s50 offen offset:832\n\t"
    "s_movk_i32 s50, 9600\n\t"
    "buffer_load_dwordx4 v[8:11], v65, s[24:27], s50 offen offset:1616\n\t"
    "buffer_load_dwordx4 v[12:15], v65, s[24:27], s50 offen offset:1632\n\t"
    "v_mov_b32 v134, 1032\n\t"
    "L_asvsp8_%=:\n\t"
    "ds_read_b32 v130, v134\n\t"
    "s_waitcnt lgkmcnt(0)\n\t"
    "v_readfirstlane_b32 s50, v130\n\t"
    "s_nop 0\n\t"
    "s_cmp_lt_u32 s50, 6\n\t"
    "s_cbranch_scc1 L_asvsp8_%=\n\t"
    "s_mov_b64 s[96:97], s[20:21]\n\t"
    "s_and_b32 s97, s97, 0xffff\n\t"
    "buffer_load_dwordx4 v[156:159], v137, s[96:99], s47 offen offset:0\n\t"
    "buffer_load_dwordx4 v[160:163], v138, s[96:99], s47 offen offset:0\n\t"
    "buffer_load_dwordx4 v[164:167], v137, s[96:99], s47 offen offset:64\n\t"
    "buffer_load_dwordx4 v[168:171], v138, s[96:99], s47 offen offset:64\n\t"
    "buffer_load_dwordx4 v[172:175], v137, s[96:99], s47 offen offset:128\n\t"
    "buffer_load_dwordx4 v[176:179], v138, s[96:99], s47 offen offset:128\n\t"
    "buffer_load_dwordx4 v[180:183], v137, s[96:99], s47 offen offset:192\n\t"
    "buffer_load_dwordx4 v[184:187], v138, s[96:99], s47 offen offset:192\n\t"
    "s_add_u32 s47, s47, 2097152\n\t"
    "buffer_load_dwordx4 v[188:191], v137, s[96:99], s47 offen offset:0\n\t"
    "buffer_load_dwordx4 v[192:195], v138, s[96:99], s47 offen offset:0\n\t"
    "buffer_load_dwordx4 v[196:199], v137, s[96:99], s47 offen offset:64\n\t"
    "buffer_load_dwordx4 v[200:203], v138, s[96:99], s47 offen offset:64\n\t"
    "buffer_load_dwordx4 v[204:207], v137, s[96:99], s47 offen offset:128\n\t"
    "buffer_load_dwordx4 v[208:211], v138, s[96:99], s47 offen offset:128\n\t"
    "buffer_load_dwordx4 v[212:215], v137, s[96:99], s47 offen offset:192\n\t"
    "buffer_load_dwordx4 v[216:219], v138, s[96:99], s47 offen offset:192\n\t"
    "s_add_u32 s47, s47, 2097152\n\t"
    "v_and_b32 v123, v123, v115\n\t"
    "v_perm_b32 v123, v123, v123, s46\n\t"
    "ds_or_b32 v97, v123\n\t"
    "v_and_b32 v124, v124, v116\n\t"
    "v_perm_b32 v124, v124, v124, s46\n\t"
    "ds_or_b32 v98, v124\n\t"
    "v_and_b32 v125, v125, v117\n\t"
    "v_perm_b32 v125, v125, v125, s46\n\t"
    "ds_or_b32 v99, v125\n\t"
    "buffer_store_dwordx4 v[72:75], v66, s[28:31], s49 offen offset:64 sc1\n\t"
    "v_mov_b32 v129, 0xffff0000\n\t"
    "v_perm_b32 v100, v100, v76, v126\n\t"
    "v_perm_b32 v101, v101, v77, v126\n\t"
    "v_perm_b32 v102, v102, v78, v126\n\t"
    "v_perm_b32 v103, v103, v79, v126\n\t"
    "v_perm_b32 v104, v104, v80, v126\n\t"
    "v_perm_b32 v105, v105, v81, v126\n\t"
    "v_perm_b32 v106, v106, v82, v126\n\t"
    "v_perm_b32 v107, v107, v83, v126\n\t"
    "v_max_u32 v108, v100, v101\n\t"
    "v_min_u32 v101, v100, v101\n\t"
    "v_max_u32 v100, v102, v103\n\t"
    "v_min_u32 v103, v102, v103\n\t"
    "v_max_u32 v102, v104, v105\n\t"
    "v_min_u32 v105, v104, v105\n\t"
    "v_max_u32 v104, v106, v107\n\t"
    "v_min_u32 v107, v106, v107\n\t"
    "v_max_u32 v106, v108, v100\n\t"
    "v_min_u32 v100, v108, v100\n\t"
    "v_max_u32 v108, v101, v103\n\t"
    "v_min_u32 v103, v101, v103\n\t"
    "v_max_u32 v101, v102, v104\n\t"
    "v_min_u32 v104, v102, v104\n\t"
    "v_max_u32 v102, v105, v107\n\t"
    "v_min_u32 v107, v105, v107\n\t"
    "v_bfi_b32 v130, v106, 0, v129\n\t"
    "v_cmp_eq_u32 vcc, 0, v130\n\t"
    "v_and_or_b32 v92, v106, s44, v68\n\t"
    "s_nop 0\n\t"
    "v_cndmask_b32 v130, v130, v129, vcc\n\t"
    "v_ffbl_b32 v131, v130\n\t"
    "v_and_b32 v134, 7, v131\n\t"
    "v_lshlrev_b32 v134, v134, s45\n\t"
    "v_xor_b32 v129, v129, v134\n\t"
    "v_xor_b32 v132, v131, v127\n\t"
    "v_bfi_b32 v130, v101, 0, v129\n\t"
    "v_cmp_eq_u32 vcc, 0, v130\n\t"
    "v_and_or_b32 v93, v101, s44, v68\n\t"
    "v_xor_b32 v133, 23, v132\n\t"
    "v_lshlrev_b32 v110, v132, v136\n\t"
    "v_and_b32 v132, 7, v132\n\t"
    "v_lshl_or_b32 v110, v136, v133, v110\n\t"
    "v_lshl_add_u32 v132, v132, 1, v128\n\t"
    "ds_or_rtn_b32 v118, v92, v110\n\t"
    "ds_write_b16 v132, v106\n\t"
    "v_cndmask_b32 v130, v130, v129, vcc\n\t"
    "v_ffbl_b32 v131, v130\n\t"
    "v_and_b32 v134, 7, v131\n\t"
    "v_lshlrev_b32 v134, v134, s45\n\t"
    "v_xor_b32 v129, v129, v134\n\t"
    "v_xor_b32 v132, v131, v127\n\t"
    "v_bfi_b32 v130, v108, 0, v129\n\t"
    "v_cmp_eq_u32 vcc, 0, v130\n\t"
    "v_and_or_b32 v94, v108, s44, v68\n\t"
    "v_xor_b32 v133, 23, v132\n\t"
    "v_lshlrev_b32 v111, v132, v136\n\t"
    "v_and_b32 v132, 7, v132\n\t"
    "v_lshl_or_b32 v111, v136, v133, v111\n\t"
    "v_lshl_add_u32 v132, v132, 1, v128\n\t"
    "ds_or_rtn_b32 v119, v93, v111\n\t"
    "ds_write_b16 v132, v101\n\t"
    "v_cndmask_b32 v130, v130, v129, vcc\n\t"
    "v_ffbl_b32 v131, v130\n\t"
    "v_and_b32 v134, 7, v131\n\t"
    "v_lshlrev_b32 v134, v134, s45\n\t"
    "v_xor_b32 v129, v129, v134\n\t"
    "v_xor_b32 v132, v131, v127\n\t"
    "v_bfi_b32 v130, v100, 0, v129\n\t"
    "v_cmp_eq_u32 vcc, 0, v130\n\t"
    "v_and_or_b32 v95, v100, s44, v68\n\t"
    "v_xor_b32 v133, 23, v132\n\t"
    "v_lshlrev_b32 v112, v132, v136\n\t"
    "v_and_b32 v132, 7, v132\n\t"
    "v_lshl_or_b32 v112, v136, v133, v112\n\t"
    "v_lshl_add_u32 v132, v132, 1, v128\n\t"
    "ds_or_rtn_b32 v120, v94, v112\n\t"
    "ds_write_b16 v132, v108\n\t"
    "v_cndmask_b32 v130, v130, v129, vcc\n\t"
    "v_ffbl_b32 v131, v130\n\t"
    "v_and_b32 v134, 7, v131\n\t"
    "v_lshlrev_b32 v134, v134, s45\n\t"
    "v_xor_b32 v129, v129, v134\n\t"
    "v_xor_b32 v132, v131, v127\n\t"
    "v_bfi_b32 v130, v102, 0, v129\n\t"
    "v_cmp_eq_u32 vcc, 0, v130\n\t"
    "v_and_or_b32 v96, v102, s44, v68\n\t"
    "v_xor_b32 v133, 23, v132\n\t"
    "v_lshlrev_b32 v113, v132, v136\n\t"
    "v_and_b32 v132, 7, v132\n\t"
    "v_lshl_or_b32 v113, v136, v133, v113\n\t"
    "v_lshl_add_u32 v132, v132, 1, v128\n\t"
    "ds_or_rtn_b32 v121, v95, v113\n\t"
    "ds_write_b16 v132, v100\n\t"
    "v_cndmask_b32 v130, v130, v129, vcc\n\t"
    "v_ffbl_b32 v131, v130\n\t"
    "v_and_b32 v134, 7, v131\n\t"
    "v_lshlrev_b32 v134, v134, s45\n\t"
    "v_xor_b32 v129, v129, v134\n\t"
    "v_xor_b32 v132, v131, v127\n\t"
    "s_waitcnt lgkmcnt(7)\n\t"
    "v_and_b32 v118, v118, v110\n\t"
    "v_perm_b32 v118, v118, v118, s46\n\t"
    "ds_or_b32 v92, v118\n\t"
    "v_bfi_b32 v130, v104, 0, v129\n\t"
    "v_cmp_eq_u32 vcc, 0, v130\n\t"
    "v_and_or_b32 v97, v104, s44, v68\n\t"
    "v_xor_b32 v133, 23, v132\n\t"
    "v_lshlrev_b32 v114, v132, v136\n\t"
    "v_and_b32 v132, 7, v132\n\t"
    "v_lshl_or_b32 v114, v136, v133, v114\n\t"
    "v_lshl_add_u32 v132, v132, 1, v128\n\t"
    "ds_or_rtn_b32 v122, v96, v114\n\t"
    "ds_write_b16 v132, v102\n\t"
    "v_cndmask_b32 v130, v130, v129, vcc\n\t"
    "v_ffbl_b32 v131, v130\n\t"
    "v_and_b32 v134, 7, v131\n\t"
    "v_lshlrev_b32 v134, v134, s45\n\t"
    "v_xor_b32 v129, v129, v134\n\t"
    "v_xor_b32 v132, v131, v127\n\t"
    "s_waitcnt lgkmcnt(8)\n\t"
    "v_and_b32 v119, v119, v111\n\t"
    "v_perm_b32 v119, v119, v119, s46\n\t"
    "ds_or_b32 v93, v119\n\t"
    "v_bfi_b32 v130, v103, 0, v129\n\t"
    "v_cmp_eq_u32 vcc, 0, v130\n\t"
    "v_and_or_b32 v98, v103, s44, v68\n\t"
    "v_xor_b32 v133, 23, v132\n\t"
    "v_lshlrev_b32 v115, v132, v136\n\t"
    "v_and_b32 v132, 7, v132\n\t"
    "v_lshl_or_b32 v115, v136, v133, v115\n\t"
    "v_lshl_add_u32 v132, v132, 1, v128\n\t"
    "ds_or_rtn_b32 v123, v97, v115\n\t"
    "ds_write_b16 v132, v104\n\t"
    "v_cndmask_b32 v130, v130, v129, vcc\n\t"
    "v_ffbl_b32 v131, v130\n\t"
    "v_and_b32 v134, 7, v131\n\t"
    "v_lshlrev_b32 v134, v134, s45\n\t"
    "v_xor_b32 v129, v129, v134\n\t"
    "v_xor_b32 v132, v131, v127\n\t"
    "s_waitcnt lgkmcnt(9)\n\t"
    "v_and_b32 v120, v120, v112\n\t"
    "v_perm_b32 v120, v120, v120, s46\n\t"
    "ds_or_b32 v94, v120\n\t"
    "v_bfi_b32 v130, v107, 0, v129\n\t"
    "v_cmp_eq_u32 vcc, 0, v130\n\t"
    "v_and_or_b32 v99, v107, s44, v68\n\t"
    "v_xor_b32 v133, 23, v132\n\t"
    "v_lshlrev_b32 v116, v132, v136\n\t"
    "v_and_b32 v132, 7, v132\n\t"
    "v_lshl_or_b32 v116, v136, v133, v116\n\t"
    "v_lshl_add_u32 v132, v132, 1, v128\n\t"
    "ds_or_rtn_b32 v124, v98, v116\n\t"
    "ds_write_b16 v132, v103\n\t"
    "v_cndmask_b32 v130, v130, v129, vcc\n\t"
    "v_ffbl_b32 v131, v130\n\t"
    "v_xor_b32 v132, v131, v127\n\t"
    "s_waitcnt lgkmcnt(10)\n\t"
    "v_and_b32 v121, v121, v113\n\t"
    "v_perm_b32 v121, v121, v121, s46\n\t"
    "ds_or_b32 v95, v121\n\t"
    "v_xor_b32 v133, 23, v132\n\t"
    "v_lshlrev_b32 v117, v132, v136\n\t"
    "v_and_b32 v132, 7, v132\n\t"
    "v_lshl_or_b32 v117, v136, v133, v117\n\t"
    "v_lshl_add_u32 v132, v132, 1, v128\n\t"
    "ds_or_rtn_b32 v125, v99, v117\n\t"
    "ds_write_b16 v132, v107\n\t"
    "s_waitcnt lgkmcnt(10)\n\t"
    "v_and_b32 v122, v122, v114\n\t"
    "v_perm_b32 v122, v122, v122, s46\n\t"
    "ds_or_b32 v96, v122\n\t"
    "ds_read_b128 v[72:75], v128\n\t"
    "v_and_or_b32 v100, v84, s44, v68\n\t"
    "ds_read_b32 v100, v100\n\t"
    "v_and_or_b32 v101, v85, s44, v68\n\t"
    "ds_read_b32 v101, v101\n\t"
    "v_and_or_b32 v102, v86, s44, v68\n\t"
    "ds_read_b32 v102, v102\n\t"
    "v_and_or_b32 v103, v87, s44, v68\n\t"
    "ds_read_b32 v103, v103\n\t"
    "v_and_or_b32 v104, v88, s44, v68\n\t"
    "ds_read_b32 v104, v104\n\t"
    "v_and_or_b32 v105, v89, s44, v68\n\t"
    "ds_read_b32 v105, v105\n\t"
    "v_and_or_b32 v106, v90, s44, v68\n\t"
    "ds_read_b32 v106, v106\n\t"
    "v_and_or_b32 v107, v91, s44, v68\n\t"
    "ds_read_b32 v107, v107\n\t"
    "s_waitcnt vmcnt(27)\n\t"
    "v_lshl_add_u32 v76, v32, 3, s36\n\t"
    "v_lshl_add_u32 v77, v33, 3, s37\n\t"
    "v_lshl_add_u32 v78, v34, 3, s38\n\t"
    "v_lshl_add_u32 v79, v35, 3, s39\n\t"
    "v_lshl_add_u32 v80, v36, 3, s40\n\t"
    "v_lshl_add_u32 v81, v37, 3, s41\n\t"
    "v_lshl_add_u32 v82, v38, 3, s42\n\t"
    "v_lshl_add_u32 v83, v39, 3, s43\n\t"
    "s_movk_i32 s50, 9600\n\t"
    "buffer_load_dwordx4 v[16:19], v65, s[24:27], s50 offen offset:2416\n\t"
    "buffer_load_dwordx4 v[20:23], v65, s[24:27], s50 offen offset:2432\n\t"
    "buffer_load_dwordx4 v[220:223], v137, s[96:99], s47 offen offset:0\n\t"
    "buffer_load_dwordx4 v[224:227], v138, s[96:99], s47 offen offset:0\n\t"
    "buffer_load_dwordx4 v[228:231], v137, s[96:99], s47 offen offset:64\n\t"
    "buffer_load_dwordx4 v[232:235], v138, s[96:99], s47 offen offset:64\n\t"
    "buffer_load_dwordx4 v[236:239], v137, s[96:99], s47 offen offset:128\n\t"
    "buffer_load_dwordx4 v[240:243], v138, s[96:99], s47 offen offset:128\n\t"
    "buffer_load_dwordx4 v[244:247], v137, s[96:99], s47 offen offset:192\n\t"
    "buffer_load_dwordx4 v[248:251], v138, s[96:99], s47 offen offset:192\n\t"
    "s_add_u32 s47, s47, 2097152\n\t"
    "buffer_load_dwordx4 v[24:27], v137, s[96:99], s47 offen offset:0\n\t"
    "buffer_load_dwordx4 v[28:31], v138, s[96:99], s47 offen offset:0\n\t"
    "buffer_load_dwordx4 v[32:35], v137, s[96:99], s47 offen offset:64\n\t"
    "buffer_load_dwordx4 v[36:39], v138, s[96:99], s47 offen offset:64\n\t"
    "s_waitcnt lgkmcnt(11)\n\t"
    "v_and_b32 v123, v123, v115\n\t"
    "v_perm_b32 v123, v123, v123, s46\n\t"
    "ds_or_b32 v97, v123\n\t"
    "v_and_b32 v124, v124, v116\n\t"
    "v_perm_b32 v124, v124, v124, s46\n\t"
    "ds_or_b32 v98, v124\n\t"
    "v_and_b32 v125, v125, v117\n\t"
    "v_perm_b32 v125, v125, v125, s46\n\t"
    "ds_or_b32 v99, v125\n\t"
    "s_waitcnt lgkmcnt(11)\n\t"
    "buffer_store_dwordx4 v[72:75], v66, s[28:31], s49 offen offset:128 sc1\n\t"
    "v_mov_b32 v129, 0xffff0000\n\t"
    "s_waitcnt lgkmcnt(7)\n\t"
    "v_perm_b32 v100, v100, v84, v126\n\t"
    "v_perm_b32 v101, v101, v85, v126\n\t"
    "v_perm_b32 v102, v102, v86, v126\n\t"
    "v_perm_b32 v103, v103, v87, v126\n\t"
    "s_waitcnt lgkmcnt(3)\n\t"
    "v_perm_b32 v104, v104, v88, v126\n\t"
    "v_perm_b32 v105, v105, v89, v126\n\t"
    "v_perm_b32 v106, v106, v90, v126\n\t"
    "v_perm_b32 v107, v107, v91, v126\n\t"
    "v_max_u32 v108, v100, v101\n\t"
    "v_min_u32 v101, v100, v101\n\t"
    "v_max_u32 v100, v102, v103\n\t"
    "v_min_u32 v103, v102, v103\n\t"
    "v_max_u32 v102, v104, v105\n\t"
    "v_min_u32 v105, v104, v105\n\t"
    "v_max_u32 v104, v106, v107\n\t"
    "v_min_u32 v107, v106, v107\n\t"
    "v_max_u32 v106, v108, v100\n\t"
    "v_min_u32 v100, v108, v100\n\t"
    "v_max_u32 v108, v101, v103\n\t"
    "v_min_u32 v103, v101, v103\n\t"
    "v_max_u32 v101, v102, v104\n\t"
    "v_min_u32 v104, v102, v104\n\t"
    "v_max_u32 v102, v105, v107\n\t"
    "v_min_u32 v107, v105, v107\n\t"
    "v_bfi_b32 v130, v106, 0, v129\n\t"
    "v_cmp_eq_u32 vcc, 0, v130\n\t"
    "v_and_or_b32 v92, v106, s44, v68\n\t"
    "s_nop 0\n\t"
    "v_cndmask_b32 v130, v130, v129, vcc\n\t"
    "v_ffbl_b32 v131, v130\n\t"
    "v_and_b32 v134, 7, v131\n\t"
    "v_lshlrev_b32 v134, v134, s45\n\t"
    "v_xor_b32 v129, v129, v134\n\t"
    "v_xor_b32 v132, v131, v127\n\t"
    "v_bfi_b32 v130, v101, 0, v129\n\t"
    "v_cmp_eq_u32 vcc, 0, v130\n\t"
    "v_and_or_b32 v93, v101, s44, v68\n\t"
    "v_xor_b32 v133, 23, v132\n\t"
    "v_lshlrev_b32 v110, v132, v136\n\t"
    "v_and_b32 v132, 7, v132\n\t"
    "v_lshl_or_b32 v110, v136, v133, v110\n\t"
    "v_lshl_add_u32 v132, v132, 1, v128\n\t"
    "ds_or_rtn_b32 v118, v92, v110\n\t"
    "ds_write_b16 v132, v106\n\t"
    "v_cndmask_b32 v130, v130, v129, vcc\n\t"
    "v_ffbl_b32 v131, v130\n\t"
    "v_and_b32 v134, 7, v131\n\t"
    "v_lshlrev_b32 v134, v134, s45\n\t"
    "v_xor_b32 v129, v129, v134\n\t"
    "v_xor_b32 v132, v131, v127\n\t"
    "v_bfi_b32 v130, v108, 0, v129\n\t"
    "v_cmp_eq_u32 vcc, 0, v130\n\t"
    "v_and_or_b32 v94, v108, s44, v68\n\t"
    "v_xor_b32 v133, 23, v132\n\t"
    "v_lshlrev_b32 v111, v132, v136\n\t"
    "v_and_b32 v132, 7, v132\n\t"
    "v_lshl_or_b32 v111, v136, v133, v111\n\t"
    "v_lshl_add_u32 v132, v132, 1, v128\n\t"
    "ds_or_rtn_b32 v119, v93, v111\n\t"
    "ds_write_b16 v132, v101\n\t"
    "v_cndmask_b32 v130, v130, v129, vcc\n\t"
    "v_ffbl_b32 v131, v130\n\t"
    "v_and_b32 v134, 7, v131\n\t"
    "v_lshlrev_b32 v134, v134, s45\n\t"
    "v_xor_b32 v129, v129, v134\n\t"
    "v_xor_b32 v132, v131, v127\n\t"
    "v_bfi_b32 v130, v100, 0, v129\n\t"
    "v_cmp_eq_u32 vcc, 0, v130\n\t"
    "v_and_or_b32 v95, v100, s44, v68\n\t"
    "v_xor_b32 v133, 23, v132\n\t"
    "v_lshlrev_b32 v112, v132, v136\n\t"
    "v_and_b32 v132, 7, v132\n\t"
    "v_lshl_or_b32 v112, v136, v133, v112\n\t"
    "v_lshl_add_u32 v132, v132, 1, v128\n\t"
    "ds_or_rtn_b32 v120, v94, v112\n\t"
    "ds_write_b16 v132, v108\n\t"
    "v_cndmask_b32 v130, v130, v129, vcc\n\t"
    "v_ffbl_b32 v131, v130\n\t"
    "v_and_b32 v134, 7, v131\n\t"
    "v_lshlrev_b32 v134, v134, s45\n\t"
    "v_xor_b32 v129, v129, v134\n\t"
    "v_xor_b32 v132, v131, v127\n\t"
    "v_bfi_b32 v130, v102, 0, v129\n\t"
    "v_cmp_eq_u32 vcc, 0, v130\n\t"
    "v_and_or_b32 v96, v102, s44, v68\n\t"
    "v_xor_b32 v133, 23, v132\n\t"
    "v_lshlrev_b32 v113, v132, v136\n\t"
    "v_and_b32 v132, 7, v132\n\t"
    "v_lshl_or_b32 v113, v136, v133, v113\n\t"
    "v_lshl_add_u32 v132, v132, 1, v128\n\t"
    "ds_or_rtn_b32 v121, v95, v113\n\t"
    "ds_write_b16 v132, v100\n\t"
    "v_cndmask_b32 v130, v130, v129, vcc\n\t"
    "v_ffbl_b32 v131, v130\n\t"
    "v_and_b32 v134, 7, v131\n\t"
    "v_lshlrev_b32 v134, v134, s45\n\t"
    "v_xor_b32 v129, v129, v134\n\t"
    "v_xor_b32 v132, v131, v127\n\t"
    "s_waitcnt lgkmcnt(7)\n\t"
    "v_and_b32 v118, v118, v110\n\t"
    "v_perm_b32 v118, v118, v118, s46\n\t"
    "ds_or_b32 v92, v118\n\t"
    "v_bfi_b32 v130, v104, 0, v129\n\t"
    "v_cmp_eq_u32 vcc, 0, v130\n\t"
    "v_and_or_b32 v97, v104, s44, v68\n\t"
    "v_xor_b32 v133, 23, v132\n\t"
    "v_lshlrev_b32 v114, v132, v136\n\t"
    "v_and_b32 v132, 7, v132\n\t"
    "v_lshl_or_b32 v114, v136, v133, v114\n\t"
    "v_lshl_add_u32 v132, v132, 1, v128\n\t"
    "ds_or_rtn_b32 v122, v96, v114\n\t"
    "ds_write_b16 v132, v102\n\t"
    "v_cndmask_b32 v130, v130, v129, vcc\n\t"
    "v_ffbl_b32 v131, v130\n\t"
    "v_and_b32 v134, 7, v131\n\t"
    "v_lshlrev_b32 v134, v134, s45\n\t"
    "v_xor_b32 v129, v129, v134\n\t"
    "v_xor_b32 v132, v131, v127\n\t"
    "s_waitcnt lgkmcnt(8)\n\t"
    "v_and_b32 v119, v119, v111\n\t"
    "v_perm_b32 v119, v119, v119, s46\n\t"
    "ds_or_b32 v93, v119\n\t"
    "v_bfi_b32 v130, v103, 0, v129\n\t"
    "v_cmp_eq_u32 vcc, 0, v130\n\t"
    "v_and_or_b32 v98, v103, s44, v68\n\t"
    "v_xor_b32 v133, 23, v132\n\t"
    "v_lshlrev_b32 v115, v132, v136\n\t"
    "v_and_b32 v132, 7, v132\n\t"
    "v_lshl_or_b32 v115, v136, v133, v115\n\t"
    "v_lshl_add_u32 v132, v132, 1, v128\n\t"
    "ds_or_rtn_b32 v123, v97, v115\n\t"
    "ds_write_b16 v132, v104\n\t"
    "v_cndmask_b32 v130, v130, v129, vcc\n\t"
    "v_ffbl_b32 v131, v130\n\t"
    "v_and_b32 v134, 7, v131\n\t"
    "v_lshlrev_b32 v134, v134, s45\n\t"
    "v_xor_b32 v129, v129, v134\n\t"
    "v_xor_b32 v132, v131, v127\n\t"
    "s_waitcnt lgkmcnt(9)\n\t"
    "v_and_b32 v120, v120, v112\n\t"
    "v_perm_b32 v120, v120, v120, s46\n\t"
    "ds_or_b32 v94, v120\n\t"
    "v_bfi_b32 v130, v107, 0, v129\n\t"
    "v_cmp_eq_u32 vcc, 0, v130\n\t"
    "v_and_or_b32 v99, v107, s44, v68\n\t"
    "v_xor_b32 v133, 23, v132\n\t"
    "v_lshlrev_b32 v116, v132, v136\n\t"
    "v_and_b32 v132, 7, v132\n\t"
    "v_lshl_or_b32 v116, v136, v133, v116\n\t"
    "v_lshl_add_u32 v132, v132, 1, v128\n\t"
    "ds_or_rtn_b32 v124, v98, v116\n\t"
    "ds_write_b16 v132, v103\n\t"
    "v_cndmask_b32 v130, v130, v129, vcc\n\t"
    "v_ffbl_b32 v131, v130\n\t"
    "v_xor_b32 v132, v131, v127\n\t"
    "s_waitcnt lgkmcnt(10)\n\t"
    "v_and_b32 v121, v121, v113\n\t"
    "v_perm_b32 v121, v121, v121, s46\n\t"
    "ds_or_b32 v95, v121\n\t"
    "v_xor_b32 v133, 23, v132\n\t"
    "v_lshlrev_b32 v117, v132, v136\n\t"
    "v_and_b32 v132, 7, v132\n\t"
    "v_lshl_or_b32 v117, v136, v133, v117\n\t"
    "v_lshl_add_u32 v132, v132, 1, v128\n\t"
    "ds_or_rtn_b32 v125, v99, v117\n\t"
    "ds_write_b16 v132, v107\n\t"
    "s_waitcnt lgkmcnt(10)\n\t"
    "v_and_b32 v122, v122, v114\n\t"
    "v_perm_b32 v122, v122, v122, s46\n\t"
    "ds_or_b32 v96, v122\n\t"
    "ds_read_b128 v[72:75], v128\n\t"
    "v_and_or_b32 v100, v76, s44, v68\n\t"
    "ds_read_b32 v100, v100\n\t"
    "v_and_or_b32 v101, v77, s44, v68\n\t"
    "ds_read_b32 v101, v101\n\t"
    "v_and_or_b32 v102, v78, s44, v68\n\t"
    "ds_read_b32 v102, v102\n\t"
    "v_and_or_b32 v103, v79, s44, v68\n\t"
    "ds_read_b32 v103, v103\n\t"
    "v_and_or_b32 v104, v80, s44, v68\n\t"
    "ds_read_b32 v104, v104\n\t"
    "v_and_or_b32 v105, v81, s44, v68\n\t"
    "ds_read_b32 v105, v105\n\t"
    "v_and_or_b32 v106, v82, s44, v68\n\t"
    "ds_read_b32 v106, v106\n\t"
    "v_and_or_b32 v107, v83, s44, v68\n\t"
    "ds_read_b32 v107, v107\n\t"
    "s_waitcnt vmcnt(40)\n\t"
    "v_lshl_add_u32 v84, v40, 3, s36\n\t"
    "v_lshl_add_u32 v85, v41, 3, s37\n\t"
    "v_lshl_add_u32 v86, v42, 3, s38\n\t"
    "v_lshl_add_u32 v87, v43, 3, s39\n\t"
    "v_lshl_add_u32 v88, v44, 3, s40\n\t"
    "v_lshl_add_u32 v89, v45, 3, s41\n\t"
    "v_lshl_add_u32 v90, v46, 3, s42\n\t"
    "v_lshl_add_u32 v91, v47, 3, s43\n\t"
    "buffer_load_dwordx4 v[40:43], v137, s[96:99], s47 offen offset:128\n\t"
    "buffer_load_dwordx4 v[44:47], v138, s[96:99], s47 offen offset:128\n\t"
    "s_waitcnt lgkmcnt(11)\n\t"
    "v_and_b32 v123, v123, v115\n\t"
    "v_perm_b32 v123, v123, v123, s46\n\t"
    "ds_or_b32 v97, v123\n\t"
    "v_and_b32 v124, v124, v116\n\t"
    "v_perm_b32 v124, v124, v124, s46\n\t"
    "ds_or_b32 v98, v124\n\t"
    "v_and_b32 v125, v125, v117\n\t"
    "v_perm_b32 v125, v125, v125, s46\n\t"
    "ds_or_b32 v99, v125\n\t"
    "s_waitcnt lgkmcnt(11)\n\t"
    "buffer_store_dwordx4 v[72:75], v66, s[28:31], s49 offen offset:192 sc1\n\t"
    "v_mov_b32 v129, 0xffff0000\n\t"
    "s_waitcnt lgkmcnt(7)\n\t"
    "v_perm_b32 v100, v100, v76, v126\n\t"
    "v_perm_b32 v101, v101, v77, v126\n\t"
    "v_perm_b32 v102, v102, v78, v126\n\t"
    "v_perm_b32 v103, v103, v79, v126\n\t"
    "s_waitcnt lgkmcnt(3)\n\t"
    "v_perm_b32 v104, v104, v80, v126\n\t"
    "v_perm_b32 v105, v105, v81, v126\n\t"
    "v_perm_b32 v106, v106, v82, v126\n\t"
    "v_perm_b32 v107, v107, v83, v126\n\t"
    "s_waitcnt vmcnt(33)\n\t"
    "ds_write_b128 v141, v[156:159] offset:0\n\t"
    "ds_write_b128 v141, v[160:163] offset:256\n\t"
    "ds_read_b64_tr_b16 v[156:157], v142 offset:0\n\t"
    "ds_read_b64_tr_b16 v[158:159], v142 offset:256\n\t"
    "ds_read_b64_tr_b16 v[160:161], v142 offset:32\n\t"
    "ds_read_b64_tr_b16 v[162:163], v142 offset:288\n\t"
    "s_waitcnt vmcnt(31)\n\t"
    "ds_write_b128 v141, v[164:167] offset:2112\n\t"
    "ds_write_b128 v141, v[168:171] offset:2368\n\t"
    "ds_read_b64_tr_b16 v[164:165], v142 offset:2112\n\t"
    "ds_read_b64_tr_b16 v[166:167], v142 offset:2368\n\t"
    "ds_read_b64_tr_b16 v[168:169], v142 offset:2144\n\t"
    "ds_read_b64_tr_b16 v[170:171], v142 offset:2400\n\t"
    "v_max_u32 v108, v100, v101\n\t"
    "v_min_u32 v101, v100, v101\n\t"
    "v_max_u32 v100, v102, v103\n\t"
    "v_min_u32 v103, v102, v103\n\t"
    "v_max_u32 v102, v104, v105\n\t"
    "v_min_u32 v105, v104, v105\n\t"
    "v_max_u32 v104, v106, v107\n\t"
    "v_min_u32 v107, v106, v107\n\t"
    "v_max_u32 v106, v108, v100\n\t"
    "v_min_u32 v100, v108, v100\n\t"
    "v_max_u32 v108, v101, v103\n\t"
    "v_min_u32 v103, v101, v103\n\t"
    "v_max_u32 v101, v102, v104\n\t"
    "v_min_u32 v104, v102, v104\n\t"
    "v_max_u32 v102, v105, v107\n\t"
    "v_min_u32 v107, v105, v107\n\t"
    "v_bfi_b32 v130, v106, 0, v129\n\t"
    "v_cmp_eq_u32 vcc, 0, v130\n\t"
    "v_and_or_b32 v92, v106, s44, v68\n\t"
    "s_nop 0\n\t"
    "v_cndmask_b32 v130, v130, v129, vcc\n\t"
    "v_ffbl_b32 v131, v130\n\t"
    "v_and_b32 v134, 7, v131\n\t"
    "v_lshlrev_b32 v134, v134, s45\n\t"
    "v_xor_b32 v129, v129, v134\n\t"
    "v_xor_b32 v132, v131, v127\n\t"
    "v_bfi_b32 v130, v101, 0, v129\n\t"
    "v_cmp_eq_u32 vcc, 0, v130\n\t"
    "v_and_or_b32 v93, v101, s44, v68\n\t"
    "v_xor_b32 v133, 23, v132\n\t"
    "v_lshlrev_b32 v110, v132, v136\n\t"
    "v_and_b32 v132, 7, v132\n\t"
    "v_lshl_or_b32 v110, v136, v133, v110\n\t"
    "v_lshl_add_u32 v132, v132, 1, v128\n\t"
    "ds_or_rtn_b32 v118, v92, v110\n\t"
    "ds_write_b16 v132, v106\n\t"
    "v_cndmask_b32 v130, v130, v129, vcc\n\t"
    "v_ffbl_b32 v131, v130\n\t"
    "v_and_b32 v134, 7, v131\n\t"
    "v_lshlrev_b32 v134, v134, s45\n\t"
    "v_xor_b32 v129, v129, v134\n\t"
    "v_xor_b32 v132, v131, v127\n\t"
    "s_waitcnt lgkmcnt(8)\n\t"
    "buffer_store_dwordx4 v[156:159], v139, s[28:31], s48 offen sc1\n\t"
    "buffer_store_dwordx4 v[160:163], v140, s[28:31], s48 offen sc1\n\t"
    "s_add_u32 s48, s48, 819200\n\t"
    "s_waitcnt lgkmcnt(2)\n\t"
    "buffer_store_dwordx4 v[164:167], v139, s[28:31], s48 offen sc1\n\t"
    "buffer_store_dwordx4 v[168:171], v140, s[28:31], s48 offen sc1\n\t"
    "s_add_u32 s48, s48, 819200\n\t"
    "s_waitcnt vmcnt(33)\n\t"
    "ds_write_b128 v141, v[172:175] offset:0\n\t"
    "ds_write_b128 v141, v[176:179] offset:256\n\t"
    "ds_read_b64_tr_b16 v[172:173], v142 offset:0\n\t"
    "ds_read_b64_tr_b16 v[174:175], v142 offset:256\n\t"
    "ds_read_b64_tr_b16 v[176:177], v142 offset:32\n\t"
    "ds_read_b64_tr_b16 v[178:179], v142 offset:288\n\t"
    "s_waitcnt vmcnt(31)\n\t"
    "ds_write_b128 v141, v[180:183] offset:2112\n\t"
    "ds_write_b128 v141, v[184:187] offset:2368\n\t"
    "ds_read_b64_tr_b16 v[180:181], v142 offset:2112\n\t"
    "ds_read_b64_tr_b16 v[182:183], v142 offset:2368\n\t"
    "ds_read_b64_tr_b16 v[184:185], v142 offset:2144\n\t"
    "ds_read_b64_tr_b16 v[186:187], v142 offset:2400\n\t"
    "v_bfi_b32 v130, v108, 0, v129\n\t"
    "v_cmp_eq_u32 vcc, 0, v130\n\t"
    "v_and_or_b32 v94, v108, s44, v68\n\t"
    "v_xor_b32 v133, 23, v132\n\t"
    "v_lshlrev_b32 v111, v132, v136\n\t"
    "v_and_b32 v132, 7, v132\n\t"
    "v_lshl_or_b32 v111, v136, v133, v111\n\t"
    "v_lshl_add_u32 v132, v132, 1, v128\n\t"
    "ds_or_rtn_b32 v119, v93, v111\n\t"
    "ds_write_b16 v132, v101\n\t"
    "v_cndmask_b32 v130, v130, v129, vcc\n\t"
    "v_ffbl_b32 v131, v130\n\t"
    "v_and_b32 v134, 7, v131\n\t"
    "v_lshlrev_b32 v134, v134, s45\n\t"
    "v_xor_b32 v129, v129, v134\n\t"
    "v_xor_b32 v132, v131, v127\n\t"
    "v_bfi_b32 v130, v100, 0, v129\n\t"
    "v_cmp_eq_u32 vcc, 0, v130\n\t"
    "v_and_or_b32 v95, v100, s44, v68\n\t"
    "v_xor_b32 v133, 23, v132\n\t"
    "v_lshlrev_b32 v112, v132, v136\n\t"
    "v_and_b32 v132, 7, v132\n\t"
    "v_lshl_or_b32 v112, v136, v133, v112\n\t"
    "v_lshl_add_u32 v132, v132, 1, v128\n\t"
    "ds_or_rtn_b32 v120, v94, v112\n\t"
    "ds_write_b16 v132, v108\n\t"
    "v_cndmask_b32 v130, v130, v129, vcc\n\t"
    "v_ffbl_b32 v131, v130\n\t"
    "v_and_b32 v134, 7, v131\n\t"
    "v_lshlrev_b32 v134, v134, s45\n\t"
    "v_xor_b32 v129, v129, v134\n\t"
    "v_xor_b32 v132, v131, v127\n\t"
    "s_waitcnt lgkmcnt(10)\n\t"
    "buffer_store_dwordx4 v[172:175], v139, s[28:31], s48 offen sc1\n\t"
    "buffer_store_dwordx4 v[176:179], v140, s[28:31], s48 offen sc1\n\t"
    "s_add_u32 s48, s48, 819200\n\t"
    "s_waitcnt lgkmcnt(4)\n\t"
    "buffer_store_dwordx4 v[180:183], v139, s[28:31], s48 offen sc1\n\t"
    "buffer_store_dwordx4 v[184:187], v140, s[28:31], s48 offen sc1\n\t"
    "s_add_u32 s48, s48, -2441216\n\t"
    "s_waitcnt vmcnt(33)\n\t"
    "ds_write_b128 v141, v[188:191] offset:0\n\t"
    "ds_write_b128 v141, v[192:195] offset:256\n\t"
    "ds_read_b64_tr_b16 v[188:189], v142 offset:0\n\t"
    "ds_read_b64_tr_b16 v[190:191], v142 offset:256\n\t"
    "ds_read_b64_tr_b16 v[192:193], v142 offset:32\n\t"
    "ds_read_b64_tr_b16 v[194:195], v142 offset:288\n\t"
    "s_waitcnt vmcnt(31)\n\t"
    "ds_write_b128 v141, v[196:199] offset:2112\n\t"
    "ds_write_b128 v141, v[200:203] offset:2368\n\t"
    "ds_read_b64_tr_b16 v[196:197], v142 offset:2112\n\t"
    "ds_read_b64_tr_b16 v[198:199], v142 offset:2368\n\t"
    "ds_read_b64_tr_b16 v[200:201], v142 offset:2144\n\t"
    "ds_read_b64_tr_b16 v[202:203], v142 offset:2400\n\t"
    "v_bfi_b32 v130, v102, 0, v129\n\t"
    "v_cmp_eq_u32 vcc, 0, v130\n\t"
    "v_and_or_b32 v96, v102, s44, v68\n\t"
    "v_xor_b32 v133, 23, v132\n\t"
    "v_lshlrev_b32 v113, v132, v136\n\t"
    "v_and_b32 v132, 7, v132\n\t"
    "v_lshl_or_b32 v113, v136, v133, v113\n\t"
    "v_lshl_add_u32 v132, v132, 1, v128\n\t"
    "ds_or_rtn_b32 v121, v95, v113\n\t"
    "ds_write_b16 v132, v100\n\t"
    "v_cndmask_b32 v130, v130, v129, vcc\n\t"
    "v_ffbl_b32 v131, v130\n\t"
    "v_and_b32 v134, 7, v131\n\t"
    "v_lshlrev_b32 v134, v134, s45\n\t"
    "v_xor_b32 v129, v129, v134\n\t"
    "v_xor_b32 v132, v131, v127\n\t"
    "v_and_b32 v118, v118, v110\n\t"
    "v_perm_b32 v118, v118, v118, s46\n\t"
    "ds_or_b32 v92, v118\n\t"
    "s_lshl_b32 s52, %2, 2\n\t"
    "s_add_u32 s52, s52, 49291264\n\t"
    "buffer_load_dword v71, off, s[28:31], s52 offset:1024 sc1\n\t"
    "v_bfi_b32 v130, v104, 0, v129\n\t"
    "v_cmp_eq_u32 vcc, 0, v130\n\t"
    "v_and_or_b32 v97, v104, s44, v68\n\t"
    "v_xor_b32 v133, 23, v132\n\t"
    "v_lshlrev_b32 v114, v132, v136\n\t"
    "v_and_b32 v132, 7, v132\n\t"
    "v_lshl_or_b32 v114, v136, v133, v114\n\t"
    "v_lshl_add_u32 v132, v132, 1, v128\n\t"
    "ds_or_rtn_b32 v122, v96, v114\n\t"
    "ds_write_b16 v132, v102\n\t"
    "v_cndmask_b32 v130, v130, v129, vcc\n\t"
    "v_ffbl_b32 v131, v130\n\t"
    "v_and_b32 v134, 7, v131\n\t"
    "v_lshlrev_b32 v134, v134, s45\n\t"
    "v_xor_b32 v129, v129, v134\n\t"
    "v_xor_b32 v132, v131, v127\n\t"
    "s_waitcnt lgkmcnt(15)\n\t"
    "v_and_b32 v119, v119, v111\n\t"
    "v_perm_b32 v119, v119, v119, s46\n\t"
    "ds_or_b32 v93, v119\n\t"
    "s_waitcnt lgkmcnt(12)\n\t"
    "buffer_store_dwordx4 v[188:191], v139, s[28:31], s48 offen sc1\n\t"
    "buffer_store_dwordx4 v[192:195], v140, s[28:31], s48 offen sc1\n\t"
    "s_add_u32 s48, s48, 819200\n\t"
    "s_waitcnt lgkmcnt(6)\n\t"
    "buffer_store_dwordx4 v[196:199], v139, s[28:31], s48 offen sc1\n\t"
    "buffer_store_dwordx4 v[200:203], v140, s[28:31], s48 offen sc1\n\t"
    "s_add_u32 s48, s48, 819200\n\t"
    "s_waitcnt vmcnt(34)\n\t"
    "ds_write_b128 v141, v[204:207] offset:0\n\t"
    "ds_write_b128 v141, v[208:211] offset:256\n\t"
    "ds_read_b64_tr_b16 v[204:205], v142 offset:0\n\t"
    "ds_read_b64_tr_b16 v[206:207], v142 offset:256\n\t"
    "ds_read_b64_tr_b16 v[208:209], v142 offset:32\n\t"
    "ds_read_b64_tr_b16 v[210:211], v142 offset:288\n\t"
    "s_waitcnt vmcnt(32)\n\t"
    "ds_write_b128 v141, v[212:215] offset:2112\n\t"
    "ds_write_b128 v141, v[216:219] offset:2368\n\t"
    "ds_read_b64_tr_b16 v[212:213], v142 offset:2112\n\t"
    "ds_read_b64_tr_b16 v[214:215], v142 offset:2368\n\t"
    "ds_read_b64_tr_b16 v[216:217], v142 offset:2144\n\t"
    "ds_read_b64_tr_b16 v[218:219], v142 offset:2400\n\t"
    "v_bfi_b32 v130, v103, 0, v129\n\t"
    "v_cmp_eq_u32 vcc, 0, v130\n\t"
    "v_and_or_b32 v98, v103, s44, v68\n\t"
    "v_xor_b32 v133, 23, v132\n\t"
    "v_lshlrev_b32 v115, v132, v136\n\t"
    "v_and_b32 v132, 7, v132\n\t"
    "v_lshl_or_b32 v115, v136, v133, v115\n\t"
    "v_lshl_add_u32 v132, v132, 1, v128\n\t"
    "ds_or_rtn_b32 v123, v97, v115\n\t"
    "ds_write_b16 v132, v104\n\t"
    "v_cndmask_b32 v130, v130, v129, vcc\n\t"
    "v_ffbl_b32 v131, v130\n\t"
    "v_and_b32 v134, 7, v131\n\t"
    "v_lshlrev_b32 v134, v134, s45\n\t"
    "v_xor_b32 v129, v129, v134\n\t"
    "v_xor_b32 v132, v131, v127\n\t"
    "v_and_b32 v120, v120, v112\n\t"
    "v_perm_b32 v120, v120, v120, s46\n\t"
    "ds_or_b32 v94, v120\n\t"
    "v_bfi_b32 v130, v107, 0, v129\n\t"
    "v_cmp_eq_u32 vcc, 0, v130\n\t"
    "v_and_or_b32 v99, v107, s44, v68\n\t"
    "v_xor_b32 v133, 23, v132\n\t"
    "v_lshlrev_b32 v116, v132, v136\n\t"
    "v_and_b32 v132, 7, v132\n\t"
    "v_lshl_or_b32 v116, v136, v133, v116\n\t"
    "v_lshl_add_u32 v132, v132, 1, v128\n\t"
    "ds_or_rtn_b32 v124, v98, v116\n\t"
    "ds_write_b16 v132, v103\n\t"
    "v_cndmask_b32 v130, v130, v129, vcc\n\t"
    "v_ffbl_b32 v131, v130\n\t"
    "v_xor_b32 v132, v131, v127\n\t"
    "s_waitcnt lgkmcnt(15)\n\t"
    "v_and_b32 v121, v121, v113\n\t"
    "v_perm_b32 v121, v121, v121, s46\n\t"
    "ds_or_b32 v95, v121\n\t"
    "v_xor_b32 v133, 23, v132\n\t"
    "v_lshlrev_b32 v117, v132, v136\n\t"
    "v_and_b32 v132, 7, v132\n\t"
    "v_lshl_or_b32 v117, v136, v133, v117\n\t"
    "v_lshl_add_u32 v132, v132, 1, v128\n\t"
    "ds_or_rtn_b32 v125, v99, v117\n\t"
    "ds_write_b16 v132, v107\n\t"
    "v_and_b32 v122, v122, v114\n\t"
    "v_perm_b32 v122, v122, v122, s46\n\t"
    "ds_or_b32 v96, v122\n\t"
    "ds_read_b128 v[72:75], v128\n\t"
    "s_waitcnt lgkmcnt(15)\n\t"
    "buffer_store_dwordx4 v[204:207], v139, s[28:31], s48 offen sc1\n\t"
    "buffer_store_dwordx4 v[208:211], v140, s[28:31], s48 offen sc1\n\t"
    "s_add_u32 s48, s48, 819200\n\t"
    "s_waitcnt lgkmcnt(10)\n\t"
    "buffer_store_dwordx4 v[212:215], v139, s[28:31], s48 offen sc1\n\t"
    "buffer_store_dwordx4 v[216:219], v140, s[28:31], s48 offen sc1\n\t"
    "s_add_u32 s48, s48, -2441216\n\t"
    "v_mov_b32 v134, 1036\n\t"
    "L_asvsp12_%=:\n\t"
    "ds_read_b32 v130, v134\n\t"
    "s_waitcnt lgkmcnt(0)\n\t"
    "v_readfirstlane_b32 s50, v130\n\t"
    "s_nop 0\n\t"
    "s_cmp_lt_u32 s50, 1\n\t"
    "s_cbranch_scc1 L_asvsp12_%=\n\t"
    "v_and_or_b32 v100, v84, s44, v69\n\t"
    "ds_read_b32 v100, v100\n\t"
    "v_and_or_b32 v101, v85, s44, v69\n\t"
    "ds_read_b32 v101, v101\n\t"
    "v_and_or_b32 v102, v86, s44, v69\n\t"
    "ds_read_b32 v102, v102\n\t"
    "v_and_or_b32 v103, v87, s44, v69\n\t"
    "ds_read_b32 v103, v103\n\t"
    "v_and_or_b32 v104, v88, s44, v69\n\t"
    "ds_read_b32 v104, v104\n\t"
    "v_and_or_b32 v105, v89, s44, v69\n\t"
    "ds_read_b32 v105, v105\n\t"
    "v_and_or_b32 v106, v90, s44, v69\n\t"
    "ds_read_b32 v106, v106\n\t"
    "v_and_or_b32 v107, v91, s44, v69\n\t"
    "ds_read_b32 v107, v107\n\t"
    "v_lshl_add_u32 v76, v48, 3, s36\n\t"
    "v_lshl_add_u32 v77, v49, 3, s37\n\t"
    "v_lshl_add_u32 v78, v50, 3, s38\n\t"
    "v_lshl_add_u32 v79, v51, 3, s39\n\t"
    "v_lshl_add_u32 v80, v52, 3, s40\n\t"
    "v_lshl_add_u32 v81, v53, 3, s41\n\t"
    "v_lshl_add_u32 v82, v54, 3, s42\n\t"
    "v_lshl_add_u32 v83, v55, 3, s43\n\t"
    "v_mov_b32 v134, 2076\n\t"
    "L_asvsp1052_%=:\n\t"
    "ds_read_b32 v130, v134\n\t"
    "s_waitcnt lgkmcnt(0)\n\t"
    "v_readfirstlane_b32 s50, v130\n\t"
    "s_nop 0\n\t"
    "s_cmp_lt_u32 s50, 1\n\t"
    "s_cbranch_scc1 L_asvsp1052_%=\n\t"
    "v_mov_b32 v70, 2072\n\t"
    "ds_read_b32 v70, v70\n\t"
    "s_waitcnt vmcnt(8)\n\t"
    "s_waitcnt vmcnt(0)\n\t"
    "s_waitcnt lgkmcnt(0)\n\t"
    "v_cmp_ne_u32 vcc, v71, v70\n\t"
    "s_cbranch_vccz L_ahsok_%=\n\t"
    "s_mov_b32 s51, 1048576\n\t"
    "L_ahs_%=:\n\t"
    "buffer_load_dword v71, off, s[28:31], s52 offset:1024 sc1\n\t"
    "s_sub_u32 s51, s51, 1\n\t"
    "s_waitcnt vmcnt(0)\n\t"
    "s_cmp_eq_u32 s51, 0\n\t"
    "s_cbranch_scc1 L_ahsok_%=\n\t"
    "v_cmp_ne_u32 vcc, v71, v70\n\t"
    "s_cbranch_vccnz L_ahs_%=\n\t"
    "L_ahsok_%=:\n\t"
    "s_lshr_b32 s50, %2, 5\n\t"
    "s_lshl_b32 s50, s50, 7\n\t"
    "s_add_u32 s50, s50, 49284096\n\t"
    "s_mov_b64 exec, 1\n\t"
    "buffer_atomic_add v136, off, s[28:31], s50 sc1\n\t"
    "s_mov_b64 exec, -1\n\t"
    "buffer_load_dwordx4 v[156:159], v137, s[96:99], s47 offen offset:192\n\t"
    "buffer_load_dwordx4 v[160:163], v138, s[96:99], s47 offen offset:192\n\t"
    "s_add_u32 s47, s47, 2097152\n\t"
    "buffer_load_dwordx4 v[164:167], v137, s[96:99], s47 offen offset:0\n\t"
    "buffer_load_dwordx4 v[168:171], v138, s[96:99], s47 offen offset:0\n\t"
    "buffer_load_dwordx4 v[172:175], v137, s[96:99], s47 offen offset:64\n\t"
    "buffer_load_dwordx4 v[176:179], v138, s[96:99], s47 offen offset:64\n\t"
    "buffer_load_dwordx4 v[180:183], v137, s[96:99], s47 offen offset:128\n\t"
    "buffer_load_dwordx4 v[184:187], v138, s[96:99], s47 offen offset:128\n\t"
    "buffer_load_dwordx4 v[188:191], v137, s[96:99], s47 offen offset:192\n\t"
    "buffer_load_dwordx4 v[192:195], v138, s[96:99], s47 offen offset:192\n\t"
    "s_add_u32 s47, s47, 2097152\n\t"
    "buffer_load_dwordx4 v[196:199], v137, s[96:99], s47 offen offset:0\n\t"
    "buffer_load_dwordx4 v[200:203], v138, s[96:99], s47 offen offset:0\n\t"
    "v_and_b32 v123, v123, v115\n\t"
    "v_perm_b32 v123, v123, v123, s46\n\t"
    "ds_or_b32 v97, v123\n\t"
    "v_and_b32 v124, v124, v116\n\t"
    "v_perm_b32 v124, v124, v124, s46\n\t"
    "ds_or_b32 v98, v124\n\t"
    "v_and_b32 v125, v125, v117\n\t"
    "v_perm_b32 v125, v125, v125, s46\n\t"
    "ds_or_b32 v99, v125\n\t"
    "buffer_store_dwordx4 v[72:75], v66, s[28:31], s49 offen offset:256 sc1\n\t"
    "v_mov_b32 v129, 0xffff0000\n\t"
    "v_perm_b32 v100, v100, v84, v126\n\t"
    "v_perm_b32 v101, v101, v85, v126\n\t"
    "v_perm_b32 v102, v102, v86, v126\n\t"
    "v_perm_b32 v103, v103, v87, v126\n\t"
    "v_perm_b32 v104, v104, v88, v126\n\t"
    "v_perm_b32 v105, v105, v89, v126\n\t"
    "v_perm_b32 v106, v106, v90, v126\n\t"
    "v_perm_b32 v107, v107, v91, v126\n\t"
    "ds_write_b128 v141, v[220:223] offset:0\n\t"
    "ds_write_b128 v141, v[224:227] offset:256\n\t"
    "ds_read_b64_tr_b16 v[220:221], v142 offset:0\n\t"
    "ds_read_b64_tr_b16 v[222:223], v142 offset:256\n\t"
    "ds_read_b64_tr_b16 v[224:225], v142 offset:32\n\t"
    "ds_read_b64_tr_b16 v[226:227], v142 offset:288\n\t"
    "ds_write_b128 v141, v[228:231] offset:2112\n\t"
    "ds_write_b128 v141, v[232:235] offset:2368\n\t"
    "ds_read_b64_tr_b16 v[228:229], v142 offset:2112\n\t"
    "ds_read_b64_tr_b16 v[230:231], v142 offset:2368\n\t"
    "ds_read_b64_tr_b16 v[232:233], v142 offset:2144\n\t"
    "ds_read_b64_tr_b16 v[234:235], v142 offset:2400\n\t"
    "v_max_u32 v108, v100, v101\n\t"
    "v_min_u32 v101, v100, v101\n\t"
    "v_max_u32 v100, v102, v103\n\t"
    "v_min_u32 v103, v102, v103\n\t"
    "v_max_u32 v102, v104, v105\n\t"
    "v_min_u32 v105, v104, v105\n\t"
    "v_max_u32 v104, v106, v107\n\t"
    "v_min_u32 v107, v106, v107\n\t"
    "v_max_u32 v106, v108, v100\n\t"
    "v_min_u32 v100, v108, v100\n\t"
    "v_max_u32 v108, v101, v103\n\t"
    "v_min_u32 v103, v101, v103\n\t"
    "v_max_u32 v101, v102, v104\n\t"
    "v_min_u32 v104, v102, v104\n\t"
    "v_max_u32 v102, v105, v107\n\t"
    "v_min_u32 v107, v105, v107\n\t"
    "v_bfi_b32 v130, v106, 0, v129\n\t"
    "v_cmp_eq_u32 vcc, 0, v130\n\t"
    "v_and_or_b32 v92, v106, s44, v69\n\t"
    "s_nop 0\n\t"
    "v_cndmask_b32 v130, v130, v129, vcc\n\t"
    "v_ffbl_b32 v131, v130\n\t"
    "v_and_b32 v134, 7, v131\n\t"
    "v_lshlrev_b32 v134, v134, s45\n\t"
    "v_xor_b32 v129, v129, v134\n\t"
    "v_xor_b32 v132, v131, v127\n\t"
    "v_bfi_b32 v130, v101, 0, v129\n\t"
    "v_cmp_eq_u32 vcc, 0, v130\n\t"
    "v_and_or_b32 v93, v101, s44, v69\n\t"
    "v_xor_b32 v133, 23, v132\n\t"
    "v_lshlrev_b32 v110, v132, v136\n\t"
    "v_and_b32 v132, 7, v132\n\t"
    "v_lshl_or_b32 v110, v136, v133, v110\n\t"
    "v_lshl_add_u32 v132, v132, 1, v128\n\t"
    "ds_or_rtn_b32 v118, v92, v110\n\t"
    "ds_write_b16 v132, v106\n\t"
    "v_cndmask_b32 v130, v130, v129, vcc\n\t"
    "v_ffbl_b32 v131, v130\n\t"
    "v_and_b32 v134, 7, v131\n\t"
    "v_lshlrev_b32 v134, v134, s45\n\t"
    "v_xor_b32 v129, v129, v134\n\t"
    "v_xor_b32 v132, v131, v127\n\t"
    "v_bfi_b32 v130, v108, 0, v129\n\t"
    "v_cmp_eq_u32 vcc, 0, v130\n\t"
    "v_and_or_b32 v94, v108, s44, v69\n\t"
    "v_xor_b32 v133, 23, v132\n\t"
    "v_lshlrev_b32 v111, v132, v136\n\t"
    "v_and_b32 v132, 7, v132\n\t"
    "v_lshl_or_b32 v111, v136, v133, v111\n\t"
    "v_lshl_add_u32 v132, v132, 1, v128\n\t"
    "ds_or_rtn_b32 v119, v93, v111\n\t"
    "ds_write_b16 v132, v101\n\t"
    "v_cndmask_b32 v130, v130, v129, vcc\n\t"
    "v_ffbl_b32 v131, v130\n\t"
    "v_and_b32 v134, 7, v131\n\t"
    "v_lshlrev_b32 v134, v134, s45\n\t"
    "v_xor_b32 v129, v129, v134\n\t"
    "v_xor_b32 v132, v131, v127\n\t"
    "s_waitcnt lgkmcnt(10)\n\t"
    "buffer_store_dwordx4 v[220:223], v139, s[28:31], s48 offen sc1\n\t"
    "buffer_store_dwordx4 v[224:227], v140, s[28:31], s48 offen sc1\n\t"
    "s_add_u32 s48, s48, 819200\n\t"
    "s_waitcnt lgkmcnt(4)\n\t"
    "buffer_store_dwordx4 v[228:231], v139, s[28:31], s48 offen sc1\n\t"
    "buffer_store_dwordx4 v[232:235], v140, s[28:31], s48 offen sc1\n\t"
    "s_add_u32 s48, s48, 819200\n\t"
    "ds_write_b128 v141, v[236:239] offset:0\n\t"
    "ds_write_b128 v141, v[240:243] offset:256\n\t"
    "ds_read_b64_tr_b16 v[236:237], v142 offset:0\n\t"
    "ds_read_b64_tr_b16 v[238:239], v142 offset:256\n\t"
    "ds_read_b64_tr_b16 v[240:241], v142 offset:32\n\t"
    "ds_read_b64_tr_b16 v[242:243], v142 offset:288\n\t"
    "ds_write_b128 v141, v[244:247] offset:2112\n\t"
    "ds_write_b128 v141, v[248:251] offset:2368\n\t"
    "ds_read_b64_tr_b16 v[244:245], v142 offset:2112\n\t"
    "ds_read_b64_tr_b16 v[246:247], v142 offset:2368\n\t"
    "ds_read_b64_tr_b16 v[248:249], v142 offset:2144\n\t"
    "ds_read_b64_tr_b16 v[250:251], v142 offset:2400\n\t"
    "v_bfi_b32 v130, v100, 0, v129\n\t"
    "v_cmp_eq_u32 vcc, 0, v130\n\t"
    "v_and_or_b32 v95, v100, s44, v69\n\t"
    "v_xor_b32 v133, 23, v132\n\t"
    "v_lshlrev_b32 v112, v132, v136\n\t"
    "v_and_b32 v132, 7, v132\n\t"
    "v_lshl_or_b32 v112, v136, v133, v112\n\t"
    "v_lshl_add_u32 v132, v132, 1, v128\n\t"
    "ds_or_rtn_b32 v120, v94, v112\n\t"
    "ds_write_b16 v132, v108\n\t"
    "v_cndmask_b32 v130, v130, v129, vcc\n\t"
    "v_ffbl_b32 v131, v130\n\t"
    "v_and_b32 v134, 7, v131\n\t"
    "v_lshlrev_b32 v134, v134, s45\n\t"
    "v_xor_b32 v129, v129, v134\n\t"
    "v_xor_b32 v132, v131, v127\n\t"
    "v_bfi_b32 v130, v102, 0, v129\n\t"
    "v_cmp_eq_u32 vcc, 0, v130\n\t"
    "v_and_or_b32 v96, v102, s44, v69\n\t"
    "v_xor_b32 v133, 23, v132\n\t"
    "v_lshlrev_b32 v113, v132, v136\n\t"
    "v_and_b32 v132, 7, v132\n\t"
    "v_lshl_or_b32 v113, v136, v133, v113\n\t"
    "v_lshl_add_u32 v132, v132, 1, v128\n\t"
    "ds_or_rtn_b32 v121, v95, v113\n\t"
    "ds_write_b16 v132, v100\n\t"
    "v_cndmask_b32 v130, v130, v129, vcc\n\t"
    "v_ffbl_b32 v131, v130\n\t"
    "v_and_b32 v134, 7, v131\n\t"
    "v_lshlrev_b32 v134, v134, s45\n\t"
    "v_xor_b32 v129, v129, v134\n\t"
    "v_xor_b32 v132, v131, v127\n\t"
    "s_waitcnt lgkmcnt(15)\n\t"
    "v_and_b32 v118, v118, v110\n\t"
    "v_perm_b32 v118, v118, v118, s46\n\t"
    "ds_or_b32 v92, v118\n\t"
    "v_bfi_b32 v130, v104, 0, v129\n\t"
    "v_cmp_eq_u32 vcc, 0, v130\n\t"
    "v_and_or_b32 v97, v104, s44, v69\n\t"
    "v_xor_b32 v133, 23, v132\n\t"
    "v_lshlrev_b32 v114, v132, v136\n\t"
    "v_and_b32 v132, 7, v132\n\t"
    "v_lshl_or_b32 v114, v136, v133, v114\n\t"
    "v_lshl_add_u32 v132, v132, 1, v128\n\t"
    "ds_or_rtn_b32 v122, v96, v114\n\t"
    "ds_write_b16 v132, v102\n\t"
    "v_cndmask_b32 v130, v130, v129, vcc\n\t"
    "v_ffbl_b32 v131, v130\n\t"
    "v_and_b32 v134, 7, v131\n\t"
    "v_lshlrev_b32 v134, v134, s45\n\t"
    "v_xor_b32 v129, v129, v134\n\t"
    "v_xor_b32 v132, v131, v127\n\t"
    "v_and_b32 v119, v119, v111\n\t"
    "v_perm_b32 v119, v119, v119, s46\n\t"
    "ds_or_b32 v93, v119\n\t"
    "s_waitcnt lgkmcnt(14)\n\t"
    "buffer_store_dwordx4 v[236:239], v139, s[28:31], s48 offen sc1\n\t"
    "buffer_store_dwordx4 v[240:243], v140, s[28:31], s48 offen sc1\n\t"
    "s_add_u32 s48, s48, 819200\n\t"
    "s_waitcnt lgkmcnt(8)\n\t"
    "buffer_store_dwordx4 v[244:247], v139, s[28:31], s48 offen sc1\n\t"
    "buffer_store_dwordx4 v[248:251], v140, s[28:31], s48 offen sc1\n\t"
    "s_add_u32 s48, s48, -2441216\n\t"
    "ds_write_b128 v141, v[24:27] offset:0\n\t"
    "ds_write_b128 v141, v[28:31] offset:256\n\t"
    "ds_read_b64_tr_b16 v[24:25], v142 offset:0\n\t"
    "ds_read_b64_tr_b16 v[26:27], v142 offset:256\n\t"
    "ds_read_b64_tr_b16 v[28:29], v142 offset:32\n\t"
    "ds_read_b64_tr_b16 v[30:31], v142 offset:288\n\t"
    "ds_write_b128 v141, v[32:35] offset:2112\n\t"
    "ds_write_b128 v141, v[36:39] offset:2368\n\t"
    "ds_read_b64_tr_b16 v[32:33], v142 offset:2112\n\t"
    "ds_read_b64_tr_b16 v[34:35], v142 offset:2368\n\t"
    "ds_read_b64_tr_b16 v[36:37], v142 offset:2144\n\t"
    "ds_read_b64_tr_b16 v[38:39], v142 offset:2400\n\t"
    "v_bfi_b32 v130, v103, 0, v129\n\t"
    "v_cmp_eq_u32 vcc, 0, v130\n\t"
    "v_and_or_b32 v98, v103, s44, v69\n\t"
    "v_xor_b32 v133, 23, v132\n\t"
    "v_lshlrev_b32 v115, v132, v136\n\t"
    "v_and_b32 v132, 7, v132\n\t"
    "v_lshl_or_b32 v115, v136, v133, v115\n\t"
    "v_lshl_add_u32 v132, v132, 1, v128\n\t"
    "ds_or_rtn_b32 v123, v97, v115\n\t"
    "ds_write_b16 v132, v104\n\t"
    "v_cndmask_b32 v130, v130, v129, vcc\n\t"
    "v_ffbl_b32 v131, v130\n\t"
    "v_and_b32 v134, 7, v131\n\t"
    "v_lshlrev_b32 v134, v134, s45\n\t"
    "v_xor_b32 v129, v129, v134\n\t"
    "v_xor_b32 v132, v131, v127\n\t"
    "s_waitcnt lgkmcnt(15)\n\t"
    "v_and_b32 v120, v120, v112\n\t"
    "v_perm_b32 v120, v120, v120, s46\n\t"
    "ds_or_b32 v94, v120\n\t"
    "v_bfi_b32 v130, v107, 0, v129\n\t"
    "v_cmp_eq_u32 vcc, 0, v130\n\t"
    "v_and_or_b32 v99, v107, s44, v69\n\t"
    "v_xor_b32 v133, 23, v132\n\t"
    "v_lshlrev_b32 v116, v132, v136\n\t"
    "v_and_b32 v132, 7, v132\n\t"
    "v_lshl_or_b32 v116, v136, v133, v116\n\t"
    "v_lshl_add_u32 v132, v132, 1, v128\n\t"
    "ds_or_rtn_b32 v124, v98, v116\n\t"
    "ds_write_b16 v132, v103\n\t"
    "v_cndmask_b32 v130, v130, v129, vcc\n\t"
    "v_ffbl_b32 v131, v130\n\t"
    "v_xor_b32 v132, v131, v127\n\t"
    "v_and_b32 v121, v121, v113\n\t"
    "v_perm_b32 v121, v121, v121, s46\n\t"
    "ds_or_b32 v95, v121\n\t"
    "v_xor_b32 v133, 23, v132\n\t"
    "v_lshlrev_b32 v117, v132, v136\n\t"
    "v_and_b32 v132, 7, v132\n\t"
    "v_lshl_or_b32 v117, v136, v133, v117\n\t"
    "v_lshl_add_u32 v132, v132, 1, v128\n\t"
    "ds_or_rtn_b32 v125, v99, v117\n\t"
    "ds_write_b16 v132, v107\n\t"
    "v_and_b32 v122, v122, v114\n\t"
    "v_perm_b32 v122, v122, v122, s46\n\t"
    "ds_or_b32 v96, v122\n\t"
    "ds_read_b128 v[72:75], v128\n\t"
    "v_and_or_b32 v100, v76, s44, v69\n\t"
    "ds_read_b32 v100, v100\n\t"
    "v_and_or_b32 v101, v77, s44, v69\n\t"
    "ds_read_b32 v101, v101\n\t"
    "v_and_or_b32 v102, v78, s44, v69\n\t"
    "ds_read_b32 v102, v102\n\t"
    "v_and_or_b32 v103, v79, s44, v69\n\t"
    "ds_read_b32 v103, v103\n\t"
    "v_and_or_b32 v104, v80, s44, v69\n\t"
    "ds_read_b32 v104, v104\n\t"
    "v_and_or_b32 v105, v81, s44, v69\n\t"
    "ds_read_b32 v105, v105\n\t"
    "v_and_or_b32 v106, v82, s44, v69\n\t"
    "ds_read_b32 v106, v106\n\t"
    "v_and_or_b32 v107, v83, s44, v69\n\t"
    "ds_read_b32 v107, v107\n\t"
    "v_lshl_add_u32 v84, v56, 3, s36\n\t"
    "v_lshl_add_u32 v85, v57, 3, s37\n\t"
    "v_lshl_add_u32 v86, v58, 3, s38\n\t"
    "v_lshl_add_u32 v87, v59, 3, s39\n\t"
    "v_lshl_add_u32 v88, v60, 3, s40\n\t"
    "v_lshl_add_u32 v89, v61, 3, s41\n\t"
    "v_lshl_add_u32 v90, v62, 3, s42\n\t"
    "v_lshl_add_u32 v91, v63, 3, s43\n\t"
    "s_waitcnt lgkmcnt(15)\n\t"
    "buffer_store_dwordx4 v[24:27], v139, s[28:31], s48 offen sc1\n\t"
    "buffer_store_dwordx4 v[28:31], v140, s[28:31], s48 offen sc1\n\t"
    "s_add_u32 s48, s48, 819200\n\t"
    "buffer_store_dwordx4 v[32:35], v139, s[28:31], s48 offen sc1\n\t"
    "buffer_store_dwordx4 v[36:39], v140, s[28:31], s48 offen sc1\n\t"
    "s_add_u32 s48, s48, 819200\n\t"
    "buffer_load_dwordx4 v[204:207], v137, s[96:99], s47 offen offset:64\n\t"
    "buffer_load_dwordx4 v[208:211], v138, s[96:99], s47 offen offset:64\n\t"
    "buffer_load_dwordx4 v[212:215], v137, s[96:99], s47 offen offset:128\n\t"
    "buffer_load_dwordx4 v[216:219], v138, s[96:99], s47 offen offset:128\n\t"
    "buffer_load_dwordx4 v[48:51], v137, s[96:99], s47 offen offset:192\n\t"
    "buffer_load_dwordx4 v[52:55], v138, s[96:99], s47 offen offset:192\n\t"
    "s_add_u32 s47, s47, 2097152\n\t"
    "buffer_load_dwordx4 v[220:223], v137, s[96:99], s47 offen offset:0\n\t"
    "buffer_load_dwordx4 v[224:227], v138, s[96:99], s47 offen offset:0\n\t"
    "buffer_load_dwordx4 v[228:231], v137, s[96:99], s47 offen offset:64\n\t"
    "buffer_load_dwordx4 v[232:235], v138, s[96:99], s47 offen offset:64\n\t"
    "buffer_load_dwordx4 v[236:239], v137, s[96:99], s47 offen offset:128\n\t"
    "buffer_load_dwordx4 v[240:243], v138, s[96:99], s47 offen offset:128\n\t"
    "s_waitcnt lgkmcnt(11)\n\t"
    "v_and_b32 v123, v123, v115\n\t"
    "v_perm_b32 v123, v123, v123, s46\n\t"
    "ds_or_b32 v97, v123\n\t"
    "v_and_b32 v124, v124, v116\n\t"
    "v_perm_b32 v124, v124, v124, s46\n\t"
    "ds_or_b32 v98, v124\n\t"
    "v_and_b32 v125, v125, v117\n\t"
    "v_perm_b32 v125, v125, v125, s46\n\t"
    "ds_or_b32 v99, v125\n\t"
    "s_waitcnt lgkmcnt(11)\n\t"
    "buffer_store_dwordx4 v[72:75], v67, s[28:31], s49 offen offset:320 sc1\n\t"
    "v_mov_b32 v129, 0xffff0000\n\t"
    "s_waitcnt lgkmcnt(7)\n\t"
    "v_perm_b32 v100, v100, v76, v126\n\t"
    "v_perm_b32 v101, v101, v77, v126\n\t"
    "v_perm_b32 v102, v102, v78, v126\n\t"
    "v_perm_b32 v103, v103, v79, v126\n\t"
    "s_waitcnt lgkmcnt(3)\n\t"
    "v_perm_b32 v104, v104, v80, v126\n\t"
    "v_perm_b32 v105, v105, v81, v126\n\t"
    "v_perm_b32 v106, v106, v82, v126\n\t"
    "v_perm_b32 v107, v107, v83, v126\n\t"
    "ds_write_b128 v141, v[40:43] offset:0\n\t"
    "ds_write_b128 v141, v[44:47] offset:256\n\t"
    "ds_read_b64_tr_b16 v[40:41], v142 offset:0\n\t"
    "ds_read_b64_tr_b16 v[42:43], v142 offset:256\n\t"
    "ds_read_b64_tr_b16 v[44:45], v142 offset:32\n\t"
    "ds_read_b64_tr_b16 v[46:47], v142 offset:288\n\t"
    "v_max_u32 v108, v100, v101\n\t"
    "v_min_u32 v101, v100, v101\n\t"
    "v_max_u32 v100, v102, v103\n\t"
    "v_min_u32 v103, v102, v103\n\t"
    "v_max_u32 v102, v104, v105\n\t"
    "v_min_u32 v105, v104, v105\n\t"
    "v_max_u32 v104, v106, v107\n\t"
    "v_min_u32 v107, v106, v107\n\t"
    "v_max_u32 v106, v108, v100\n\t"
    "v_min_u32 v100, v108, v100\n\t"
    "v_max_u32 v108, v101, v103\n\t"
    "v_min_u32 v103, v101, v103\n\t"
    "v_max_u32 v101, v102, v104\n\t"
    "v_min_u32 v104, v102, v104\n\t"
    "v_max_u32 v102, v105, v107\n\t"
    "v_min_u32 v107, v105, v107\n\t"
    "v_bfi_b32 v130, v106, 0, v129\n\t"
    "v_cmp_eq_u32 vcc, 0, v130\n\t"
    "v_and_or_b32 v92, v106, s44, v69\n\t"
    "s_nop 0\n\t"
    "v_cndmask_b32 v130, v130, v129, vcc\n\t"
    "v_ffbl_b32 v131, v130\n\t"
    "v_and_b32 v134, 7, v131\n\t"
    "v_lshlrev_b32 v134, v134, s45\n\t"
    "v_xor_b32 v129, v129, v134\n\t"
    "v_xor_b32 v132, v131, v127\n\t"
    "v_bfi_b32 v130, v101, 0, v129\n\t"
    "v_cmp_eq_u32 vcc, 0, v130\n\t"
    "v_and_or_b32 v93, v101, s44, v69\n\t"
    "v_xor_b32 v133, 23, v132\n\t"
    "v_lshlrev_b32 v110, v132, v136\n\t"
    "v_and_b32 v132, 7, v132\n\t"
    "v_lshl_or_b32 v110, v136, v133, v110\n\t"
    "v_lshl_add_u32 v132, v132, 1, v128\n\t"
    "ds_or_rtn_b32 v118, v92, v110\n\t"
    "ds_write_b16 v132, v106\n\t"
    "v_cndmask_b32 v130, v130, v129, vcc\n\t"
    "v_ffbl_b32 v131, v130\n\t"
    "v_and_b32 v134, 7, v131\n\t"
    "v_lshlrev_b32 v134, v134, s45\n\t"
    "v_xor_b32 v129, v129, v134\n\t"
    "v_xor_b32 v132, v131, v127\n\t"
    "v_bfi_b32 v130, v108, 0, v129\n\t"
    "v_cmp_eq_u32 vcc, 0, v130\n\t"
    "v_and_or_b32 v94, v108, s44, v69\n\t"
    "v_xor_b32 v133, 23, v132\n\t"
    "v_lshlrev_b32 v111, v132, v136\n\t"
    "v_and_b32 v132, 7, v132\n\t"
    "v_lshl_or_b32 v111, v136, v133, v111\n\t"
    "v_lshl_add_u32 v132, v132, 1, v128\n\t"
    "ds_or_rtn_b32 v119, v93, v111\n\t"
    "ds_write_b16 v132, v101\n\t"
    "v_cndmask_b32 v130, v130, v129, vcc\n\t"
    "v_ffbl_b32 v131, v130\n\t"
    "v_and_b32 v134, 7, v131\n\t"
    "v_lshlrev_b32 v134, v134, s45\n\t"
    "v_xor_b32 v129, v129, v134\n\t"
    "v_xor_b32 v132, v131, v127\n\t"
    "s_waitcnt lgkmcnt(4)\n\t"
    "buffer_store_dwordx4 v[40:43], v139, s[28:31], s48 offen sc1\n\t"
    "buffer_store_dwordx4 v[44:47], v140, s[28:31], s48 offen sc1\n\t"
    "s_add_u32 s48, s48, 819200\n\t"
    "v_bfi_b32 v130, v100, 0, v129\n\t"
    "v_cmp_eq_u32 vcc, 0, v130\n\t"
    "v_and_or_b32 v95, v100, s44, v69\n\t"
    "v_xor_b32 v133, 23, v132\n\t"
    "v_lshlrev_b32 v112, v132, v136\n\t"
    "v_and_b32 v132, 7, v132\n\t"
    "v_lshl_or_b32 v112, v136, v133, v112\n\t"
    "v_lshl_add_u32 v132, v132, 1, v128\n\t"
    "ds_or_rtn_b32 v120, v94, v112\n\t"
    "ds_write_b16 v132, v108\n\t"
    "v_cndmask_b32 v130, v130, v129, vcc\n\t"
    "v_ffbl_b32 v131, v130\n\t"
    "v_and_b32 v134, 7, v131\n\t"
    "v_lshlrev_b32 v134, v134, s45\n\t"
    "v_xor_b32 v129, v129, v134\n\t"
    "v_xor_b32 v132, v131, v127\n\t"
    "v_bfi_b32 v130, v102, 0, v129\n\t"
    "v_cmp_eq_u32 vcc, 0, v130\n\t"
    "v_and_or_b32 v96, v102, s44, v69\n\t"
    "v_xor_b32 v133, 23, v132\n\t"
    "v_lshlrev_b32 v113, v132, v136\n\t"
    "v_and_b32 v132, 7, v132\n\t"
    "v_lshl_or_b32 v113, v136, v133, v113\n\t"
    "v_lshl_add_u32 v132, v132, 1, v128\n\t"
    "ds_or_rtn_b32 v121, v95, v113\n\t"
    "ds_write_b16 v132, v100\n\t"
    "v_cndmask_b32 v130, v130, v129, vcc\n\t"
    "v_ffbl_b32 v131, v130\n\t"
    "v_and_b32 v134, 7, v131\n\t"
    "v_lshlrev_b32 v134, v134, s45\n\t"
    "v_xor_b32 v129, v129, v134\n\t"
    "v_xor_b32 v132, v131, v127\n\t"
    "s_waitcnt lgkmcnt(7)\n\t"
    "v_and_b32 v118, v118, v110\n\t"
    "v_perm_b32 v118, v118, v118, s46\n\t"
    "ds_or_b32 v92, v118\n\t"
    "v_bfi_b32 v130, v104, 0, v129\n\t"
    "v_cmp_eq_u32 vcc, 0, v130\n\t"
    "v_and_or_b32 v97, v104, s44, v69\n\t"
    "v_xor_b32 v133, 23, v132\n\t"
    "v_lshlrev_b32 v114, v132, v136\n\t"
    "v_and_b32 v132, 7, v132\n\t"
    "v_lshl_or_b32 v114, v136, v133, v114\n\t"
    "v_lshl_add_u32 v132, v132, 1, v128\n\t"
    "ds_or_rtn_b32 v122, v96, v114\n\t"
    "ds_write_b16 v132, v102\n\t"
    "v_cndmask_b32 v130, v130, v129, vcc\n\t"
    "v_ffbl_b32 v131, v130\n\t"
    "v_and_b32 v134, 7, v131\n\t"
    "v_lshlrev_b32 v134, v134, s45\n\t"
    "v_xor_b32 v129, v129, v134\n\t"
    "v_xor_b32 v132, v131, v127\n\t"
    "s_waitcnt lgkmcnt(8)\n\t"
    "v_and_b32 v119, v119, v111\n\t"
    "v_perm_b32 v119, v119, v119, s46\n\t"
    "ds_or_b32 v93, v119\n\t"
    "v_bfi_b32 v130, v103, 0, v129\n\t"
    "v_cmp_eq_u32 vcc, 0, v130\n\t"
    "v_and_or_b32 v98, v103, s44, v69\n\t"
    "v_xor_b32 v133, 23, v132\n\t"
    "v_lshlrev_b32 v115, v132, v136\n\t"
    "v_and_b32 v132, 7, v132\n\t"
    "v_lshl_or_b32 v115, v136, v133, v115\n\t"
    "v_lshl_add_u32 v132, v132, 1, v128\n\t"
    "ds_or_rtn_b32 v123, v97, v115\n\t"
    "ds_write_b16 v132, v104\n\t"
    "v_cndmask_b32 v130, v130, v129, vcc\n\t"
    "v_ffbl_b32 v131, v130\n\t"
    "v_and_b32 v134, 7, v131\n\t"
    "v_lshlrev_b32 v134, v134, s45\n\t"
    "v_xor_b32 v129, v129, v134\n\t"
    "v_xor_b32 v132, v131, v127\n\t"
    "s_waitcnt lgkmcnt(9)\n\t"
    "v_and_b32 v120, v120, v112\n\t"
    "v_perm_b32 v120, v120, v120, s46\n\t"
    "ds_or_b32 v94, v120\n\t"
    "v_bfi_b32 v130, v107, 0, v129\n\t"
    "v_cmp_eq_u32 vcc, 0, v130\n\t"
    "v_and_or_b32 v99, v107, s44, v69\n\t"
    "v_xor_b32 v133, 23, v132\n\t"
    "v_lshlrev_b32 v116, v132, v136\n\t"
    "v_and_b32 v132, 7, v132\n\t"
    "v_lshl_or_b32 v116, v136, v133, v116\n\t"
    "v_lshl_add_u32 v132, v132, 1, v128\n\t"
    "ds_or_rtn_b32 v124, v98, v116\n\t"
    "ds_write_b16 v132, v103\n\t"
    "v_cndmask_b32 v130, v130, v129, vcc\n\t"
    "v_ffbl_b32 v131, v130\n\t"
    "v_xor_b32 v132, v131, v127\n\t"
    "s_waitcnt lgkmcnt(10)\n\t"
    "v_and_b32 v121, v121, v113\n\t"
    "v_perm_b32 v121, v121, v121, s46\n\t"
    "ds_or_b32 v95, v121\n\t"
    "v_xor_b32 v133, 23, v132\n\t"
    "v_lshlrev_b32 v117, v132, v136\n\t"
    "v_and_b32 v132, 7, v132\n\t"
    "v_lshl_or_b32 v117, v136, v133, v117\n\t"
    "v_lshl_add_u32 v132, v132, 1, v128\n\t"
    "ds_or_rtn_b32 v125, v99, v117\n\t"
    "ds_write_b16 v132, v107\n\t"
    "s_waitcnt lgkmcnt(10)\n\t"
    "v_and_b32 v122, v122, v114\n\t"
    "v_perm_b32 v122, v122, v122, s46\n\t"
    "ds_or_b32 v96, v122\n\t"
    "ds_read_b128 v[72:75], v128\n\t"
    "v_and_or_b32 v100, v84, s44, v69\n\t"
    "ds_read_b32 v100, v100\n\t"
    "v_and_or_b32 v101, v85, s44, v69\n\t"
    "ds_read_b32 v101, v101\n\t"
    "v_and_or_b32 v102, v86, s44, v69\n\t"
    "ds_read_b32 v102, v102\n\t"
    "v_and_or_b32 v103, v87, s44, v69\n\t"
    "ds_read_b32 v103, v103\n\t"
    "v_and_or_b32 v104, v88, s44, v69\n\t"
    "ds_read_b32 v104, v104\n\t"
    "v_and_or_b32 v105, v89, s44, v69\n\t"
    "ds_read_b32 v105, v105\n\t"
    "v_and_or_b32 v106, v90, s44, v69\n\t"
    "ds_read_b32 v106, v106\n\t"
    "v_and_or_b32 v107, v91, s44, v69\n\t"
    "ds_read_b32 v107, v107\n\t"
    "v_lshl_add_u32 v76, v0, 3, s36\n\t"
    "v_lshl_add_u32 v77, v1, 3, s37\n\t"
    "v_lshl_add_u32 v78, v2, 3, s38\n\t"
    "v_lshl_add_u32 v79, v3, 3, s39\n\t"
    "v_lshl_add_u32 v80, v4, 3, s40\n\t"
    "v_lshl_add_u32 v81, v5, 3, s41\n\t"
    "v_lshl_add_u32 v82, v6, 3, s42\n\t"
    "v_lshl_add_u32 v83, v7, 3, s43\n\t"
    "v_mbcnt_lo_u32_b32 v70, -1, 0\n\t"
    "v_mbcnt_hi_u32_b32 v70, -1, v70\n\t"
    "v_add_u32 v70, 8, v70\n\t"
    "v_mov_b32 v148, 32\n\t"
    "v_lshlrev_b32 v70, 7, v70\n\t"
    "s_mov_b32 s52, 49283072\n\t"
    "s_mov_b64 exec, 0xff\n\t"
    "buffer_load_dword v71, v70, s[28:31], s52 offen sc1\n\t"
    "s_mov_b64 exec, -1\n\t"
    "s_mov_b32 s54, 0x40000000\n\t"
    "s_mov_b32 s55, 0\n\t"
    "buffer_load_dwordx4 v[244:247], v137, s[96:99], s47 offen offset:192\n\t"
    "buffer_load_dwordx4 v[248:251], v138, s[96:99], s47 offen offset:192\n\t"
    "s_add_u32 s47, s47, 2097152\n\t"
    "s_waitcnt lgkmcnt(11)\n\t"
    "v_and_b32 v123, v123, v115\n\t"
    "v_perm_b32 v123, v123, v123, s46\n\t"
    "ds_or_b32 v97, v123\n\t"
    "v_and_b32 v124, v124, v116\n\t"
    "v_perm_b32 v124, v124, v124, s46\n\t"
    "ds_or_b32 v98, v124\n\t"
    "v_and_b32 v125, v125, v117\n\t"
    "v_perm_b32 v125, v125, v125, s46\n\t"
    "ds_or_b32 v99, v125\n\t"
    "s_waitcnt lgkmcnt(11)\n\t"
    "buffer_store_dwordx4 v[72:75], v67, s[28:31], s49 offen offset:352 sc1\n\t"
    "v_mov_b32 v129, 0xffff0000\n\t"
    "s_waitcnt lgkmcnt(7)\n\t"
    "v_perm_b32 v100, v100, v84, v126\n\t"
    "v_perm_b32 v101, v101, v85, v126\n\t"
    "v_perm_b32 v102, v102, v86, v126\n\t"
    "v_perm_b32 v103, v103, v87, v126\n\t"
    "s_waitcnt lgkmcnt(3)\n\t"
    "v_perm_b32 v104, v104, v88, v126\n\t"
    "v_perm_b32 v105, v105, v89, v126\n\t"
    "v_perm_b32 v106, v106, v90, v126\n\t"
    "v_perm_b32 v107, v107, v91, v126\n\t"
    "s_waitcnt vmcnt(42)\n\t"
    "ds_write_b128 v141, v[156:159] offset:2112\n\t"
    "ds_write_b128 v141, v[160:163] offset:2368\n\t"
    "ds_read_b64_tr_b16 v[156:157], v142 offset:2112\n\t"
    "ds_read_b64_tr_b16 v[158:159], v142 offset:2368\n\t"
    "ds_read_b64_tr_b16 v[160:161], v142 offset:2144\n\t"
    "ds_read_b64_tr_b16 v[162:163], v142 offset:2400\n\t"
    "s_waitcnt vmcnt(40)\n\t"
    "ds_write_b128 v141, v[164:167] offset:0\n\t"
    "ds_write_b128 v141, v[168:171] offset:256\n\t"
    "ds_read_b64_tr_b16 v[164:165], v142 offset:0\n\t"
    "ds_read_b64_tr_b16 v[166:167], v142 offset:256\n\t"
    "ds_read_b64_tr_b16 v[168:169], v142 offset:32\n\t"
    "ds_read_b64_tr_b16 v[170:171], v142 offset:288\n\t"
    "v_max_u32 v108, v100, v101\n\t"
    "v_min_u32 v101, v100, v101\n\t"
    "v_max_u32 v100, v102, v103\n\t"
    "v_min_u32 v103, v102, v103\n\t"
    "v_max_u32 v102, v104, v105\n\t"
    "v_min_u32 v105, v104, v105\n\t"
    "v_max_u32 v104, v106, v107\n\t"
    "v_min_u32 v107, v106, v107\n\t"
    "v_max_u32 v106, v108, v100\n\t"
    "v_min_u32 v100, v108, v100\n\t"
    "v_max_u32 v108, v101, v103\n\t"
    "v_min_u32 v103, v101, v103\n\t"
    "v_max_u32 v101, v102, v104\n\t"
    "v_min_u32 v104, v102, v104\n\t"
    "v_max_u32 v102, v105, v107\n\t"
    "v_min_u32 v107, v105, v107\n\t"
    "v_bfi_b32 v130, v106, 0, v129\n\t"
    "v_cmp_eq_u32 vcc, 0, v130\n\t"
    "v_and_or_b32 v92, v106, s44, v69\n\t"
    "s_nop 0\n\t"
    "v_cndmask_b32 v130, v130, v129, vcc\n\t"
    "v_ffbl_b32 v131, v130\n\t"
    "v_and_b32 v134, 7, v131\n\t"
    "v_lshlrev_b32 v134, v134, s45\n\t"
    "v_xor_b32 v129, v129, v134\n\t"
    "v_xor_b32 v132, v131, v127\n\t"
    "v_bfi_b32 v130, v101, 0, v129\n\t"
    "v_cmp_eq_u32 vcc, 0, v130\n\t"
    "v_and_or_b32 v93, v101, s44, v69\n\t"
    "v_xor_b32 v133, 23, v132\n\t"
    "v_lshlrev_b32 v110, v132, v136\n\t"
    "v_and_b32 v132, 7, v132\n\t"
    "v_lshl_or_b32 v110, v136, v133, v110\n\t"
    "v_lshl_add_u32 v132, v132, 1, v128\n\t"
    "ds_or_b32 v92, v110\n\t"
    "ds_write_b16 v132, v106\n\t"
    "v_cndmask_b32 v130, v130, v129, vcc\n\t"
    "v_ffbl_b32 v131, v130\n\t"
    "v_and_b32 v134, 7, v131\n\t"
    "v_lshlrev_b32 v134, v134, s45\n\t"
    "v_xor_b32 v129, v129, v134\n\t"
    "v_xor_b32 v132, v131, v127\n\t"
    "v_bfi_b32 v130, v108, 0, v129\n\t"
    "v_cmp_eq_u32 vcc, 0, v130\n\t"
    "v_and_or_b32 v94, v108, s44, v69\n\t"
    "v_xor_b32 v133, 23, v132\n\t"
    "v_lshlrev_b32 v111, v132, v136\n\t"
    "v_and_b32 v132, 7, v132\n\t"
    "v_lshl_or_b32 v111, v136, v133, v111\n\t"
    "v_lshl_add_u32 v132, v132, 1, v128\n\t"
    "ds_or_b32 v93, v111\n\t"
    "ds_write_b16 v132, v101\n\t"
    "v_cndmask_b32 v130, v130, v129, vcc\n\t"
    "v_ffbl_b32 v131, v130\n\t"
    "v_and_b32 v134, 7, v131\n\t"
    "v_lshlrev_b32 v134, v134, s45\n\t"
    "v_xor_b32 v129, v129, v134\n\t"
    "v_xor_b32 v132, v131, v127\n\t"
    "s_waitcnt lgkmcnt(10)\n\t"
    "buffer_store_dwordx4 v[156:159], v139, s[28:31], s48 offen sc1\n\t"
    "buffer_store_dwordx4 v[160:163], v140, s[28:31], s48 offen sc1\n\t"
    "s_add_u32 s48, s48, -2441216\n\t"
    "s_waitcnt lgkmcnt(4)\n\t"
    "buffer_store_dwordx4 v[164:167], v139, s[28:31], s48 offen sc1\n\t"
    "buffer_store_dwordx4 v[168:171], v140, s[28:31], s48 offen sc1\n\t"
    "s_add_u32 s48, s48, 819200\n\t"
    "s_waitcnt vmcnt(42)\n\t"
    "ds_write_b128 v141, v[172:175] offset:2112\n\t"
    "ds_write_b128 v141, v[176:179] offset:2368\n\t"
    "ds_read_b64_tr_b16 v[172:173], v142 offset:2112\n\t"
    "ds_read_b64_tr_b16 v[174:175], v142 offset:2368\n\t"
    "ds_read_b64_tr_b16 v[176:177], v142 offset:2144\n\t"
    "ds_read_b64_tr_b16 v[178:179], v142 offset:2400\n\t"
    "s_waitcnt vmcnt(40)\n\t"
    "ds_write_b128 v141, v[180:183] offset:0\n\t"
    "ds_write_b128 v141, v[184:187] offset:256\n\t"
    "ds_read_b64_tr_b16 v[180:181], v142 offset:0\n\t"
    "ds_read_b64_tr_b16 v[182:183], v142 offset:256\n\t"
    "ds_read_b64_tr_b16 v[184:185], v142 offset:32\n\t"
    "ds_read_b64_tr_b16 v[186:187], v142 offset:288\n\t"
    "v_bfi_b32 v130, v100, 0, v129\n\t"
    "v_cmp_eq_u32 vcc, 0, v130\n\t"
    "v_and_or_b32 v95, v100, s44, v69\n\t"
    "v_xor_b32 v133, 23, v132\n\t"
    "v_lshlrev_b32 v112, v132, v136\n\t"
    "v_and_b32 v132, 7, v132\n\t"
    "v_lshl_or_b32 v112, v136, v133, v112\n\t"
    "v_lshl_add_u32 v132, v132, 1, v128\n\t"
    "ds_or_b32 v94, v112\n\t"
    "ds_write_b16 v132, v108\n\t"
    "v_cndmask_b32 v130, v130, v129, vcc\n\t"
    "v_ffbl_b32 v131, v130\n\t"
    "v_and_b32 v134, 7, v131\n\t"
    "v_lshlrev_b32 v134, v134, s45\n\t"
    "v_xor_b32 v129, v129, v134\n\t"
    "v_xor_b32 v132, v131, v127\n\t"
    "v_bfi_b32 v130, v102, 0, v129\n\t"
    "v_cmp_eq_u32 vcc, 0, v130\n\t"
    "v_and_or_b32 v96, v102, s44, v69\n\t"
    "v_xor_b32 v133, 23, v132\n\t"
    "v_lshlrev_b32 v113, v132, v136\n\t"
    "v_and_b32 v132, 7, v132\n\t"
    "v_lshl_or_b32 v113, v136, v133, v113\n\t"
    "v_lshl_add_u32 v132, v132, 1, v128\n\t"
    "ds_or_b32 v95, v113\n\t"
    "ds_write_b16 v132, v100\n\t"
    "v_cndmask_b32 v130, v130, v129, vcc\n\t"
    "v_ffbl_b32 v131, v130\n\t"
    "v_and_b32 v134, 7, v131\n\t"
    "v_lshlrev_b32 v134, v134, s45\n\t"
    "v_xor_b32 v129, v129, v134\n\t"
    "v_xor_b32 v132, v131, v127\n\t"
    "s_waitcnt vmcnt(7)\n\t"
    "s_call_b64 s[54:55], L_af1w_%=\n\t"
    "s_mov_b64 s[54:55], 0\n\t"
    "L_af1n15_%=:\n\t"
    "v_bfi_b32 v130, v104, 0, v129\n\t"
    "v_cmp_eq_u32 vcc, 0, v130\n\t"
    "v_and_or_b32 v97, v104, s44, v69\n\t"
    "v_xor_b32 v133, 23, v132\n\t"
    "v_lshlrev_b32 v114, v132, v136\n\t"
    "v_and_b32 v132, 7, v132\n\t"
    "v_lshl_or_b32 v114, v136, v133, v114\n\t"
    "v_lshl_add_u32 v132, v132, 1, v128\n\t"
    "ds_or_b32 v96, v114\n\t"
    "ds_write_b16 v132, v102\n\t"
    "v_cndmask_b32 v130, v130, v129, vcc\n\t"
    "v_ffbl_b32 v131, v130\n\t"
    "v_and_b32 v134, 7, v131\n\t"
    "v_lshlrev_b32 v134, v134, s45\n\t"
    "v_xor_b32 v129, v129, v134\n\t"
    "v_xor_b32 v132, v131, v127\n\t"
    "s_waitcnt lgkmcnt(12)\n\t"
    "buffer_store_dwordx4 v[172:175], v139, s[28:31], s48 offen sc1\n\t"
    "buffer_store_dwordx4 v[176:179], v140, s[28:31], s48 offen sc1\n\t"
    "s_add_u32 s48, s48, 819200\n\t"
    "s_waitcnt lgkmcnt(6)\n\t"
    "buffer_store_dwordx4 v[180:183], v139, s[28:31], s48 offen sc1\n\t"
    "buffer_store_dwordx4 v[184:187], v140, s[28:31], s48 offen sc1\n\t"
    "s_add_u32 s48, s48, 819200\n\t"
    "ds_write_b128 v141, v[188:191] offset:2112\n\t"
    "ds_write_b128 v141, v[192:195] offset:2368\n\t"
    "ds_read_b64_tr_b16 v[188:189], v142 offset:2112\n\t"
    "ds_read_b64_tr_b16 v[190:191], v142 offset:2368\n\t"
    "ds_read_b64_tr_b16 v[192:193], v142 offset:2144\n\t"
    "ds_read_b64_tr_b16 v[194:195], v142 offset:2400\n\t"
    "ds_write_b128 v141, v[196:199] offset:0\n\t"
    "ds_write_b128 v141, v[200:203] offset:256\n\t"
    "ds_read_b64_tr_b16 v[196:197], v142 offset:0\n\t"
    "ds_read_b64_tr_b16 v[198:199], v142 offset:256\n\t"
    "ds_read_b64_tr_b16 v[200:201], v142 offset:32\n\t"
    "ds_read_b64_tr_b16 v[202:203], v142 offset:288\n\t"
    "v_bfi_b32 v130, v103, 0, v129\n\t"
    "v_cmp_eq_u32 vcc, 0, v130\n\t"
    "v_and_or_b32 v98, v103, s44, v69\n\t"
    "v_xor_b32 v133, 23, v132\n\t"
    "v_lshlrev_b32 v115, v132, v136\n\t"
    "v_and_b32 v132, 7, v132\n\t"
    "v_lshl_or_b32 v115, v136, v133, v115\n\t"
    "v_lshl_add_u32 v132, v132, 1, v128\n\t"
    "ds_or_b32 v97, v115\n\t"
    "ds_write_b16 v132, v104\n\t"
    "v_cndmask_b32 v130, v130, v129, vcc\n\t"
    "v_ffbl_b32 v131, v130\n\t"
    "v_and_b32 v134, 7, v131\n\t"
    "v_lshlrev_b32 v134, v134, s45\n\t"
    "v_xor_b32 v129, v129, v134\n\t"
    "v_xor_b32 v132, v131, v127\n\t"
    "v_bfi_b32 v130, v107, 0, v129\n\t"
    "v_cmp_eq_u32 vcc, 0, v130\n\t"
    "v_and_or_b32 v99, v107, s44, v69\n\t"
    "v_xor_b32 v133, 23, v132\n\t"
    "v_lshlrev_b32 v116, v132, v136\n\t"
    "v_and_b32 v132, 7, v132\n\t"
    "v_lshl_or_b32 v116, v136, v133, v116\n\t"
    "v_lshl_add_u32 v132, v132, 1, v128\n\t"
    "ds_or_b32 v98, v116\n\t"
    "ds_write_b16 v132, v103\n\t"
    "v_cndmask_b32 v130, v130, v129, vcc\n\t"
    "v_ffbl_b32 v131, v130\n\t"
    "v_xor_b32 v132, v131, v127\n\t"
    "v_xor_b32 v133, 23, v132\n\t"
    "v_lshlrev_b32 v117, v132, v136\n\t"
    "v_and_b32 v132, 7, v132\n\t"
    "v_lshl_or_b32 v117, v136, v133, v117\n\t"
    "v_lshl_add_u32 v132, v132, 1, v128\n\t"
    "ds_or_b32 v99, v117\n\t"
    "ds_write_b16 v132, v107\n\t"
    "ds_read_b128 v[72:75], v128\n\t"
    "v_and_or_b32 v100, v76, s44, v69\n\t"
    "ds_read_b32 v100, v100\n\t"
    "v_and_or_b32 v101, v77, s44, v69\n\t"
    "ds_read_b32 v101, v101\n\t"
    "v_and_or_b32 v102, v78, s44, v69\n\t"
    "ds_read_b32 v102, v102\n\t"
    "v_and_or_b32 v103, v79, s44, v69\n\t"
    "ds_read_b32 v103, v103\n\t"
    "v_and_or_b32 v104, v80, s44, v69\n\t"
    "ds_read_b32 v104, v104\n\t"
    "v_and_or_b32 v105, v81, s44, v69\n\t"
    "ds_read_b32 v105, v105\n\t"
    "v_and_or_b32 v106, v82, s44, v69\n\t"
    "ds_read_b32 v106, v106\n\t"
    "v_and_or_b32 v107, v83, s44, v69\n\t"
    "ds_read_b32 v107, v107\n\t"
    "v_lshl_add_u32 v84, v8, 3, s36\n\t"
    "v_lshl_add_u32 v85, v9, 3, s37\n\t"
    "v_lshl_add_u32 v86, v10, 3, s38\n\t"
    "v_lshl_add_u32 v87, v11, 3, s39\n\t"
    "v_lshl_add_u32 v88, v12, 3, s40\n\t"
    "v_lshl_add_u32 v89, v13, 3, s41\n\t"
    "v_lshl_add_u32 v90, v14, 3, s42\n\t"
    "v_lshl_add_u32 v91, v15, 3, s43\n\t"
    "s_waitcnt lgkmcnt(15)\n\t"
    "buffer_store_dwordx4 v[188:191], v139, s[28:31], s48 offen sc1\n\t"
    "buffer_store_dwordx4 v[192:195], v140, s[28:31], s48 offen sc1\n\t"
    "s_add_u32 s48, s48, -2441216\n\t"
    "buffer_store_dwordx4 v[196:199], v139, s[28:31], s48 offen sc1\n\t"
    "buffer_store_dwordx4 v[200:203], v140, s[28:31], s48 offen sc1\n\t"
    "s_add_u32 s48, s48, 819200\n\t"
    "s_waitcnt lgkmcnt(8)\n\t"
    "buffer_store_dwordx4 v[72:75], v67, s[28:31], s49 offen offset:384 sc1\n\t"
    "v_mov_b32 v129, 0xffff0000\n\t"
    "s_waitcnt lgkmcnt(4)\n\t"
    "v_perm_b32 v100, v100, v76, v126\n\t"
    "v_perm_b32 v101, v101, v77, v126\n\t"
    "v_perm_b32 v102, v102, v78, v126\n\t"
    "v_perm_b32 v103, v103, v79, v126\n\t"
    "s_waitcnt lgkmcnt(0)\n\t"
    "v_perm_b32 v104, v104, v80, v126\n\t"
    "v_perm_b32 v105, v105, v81, v126\n\t"
    "v_perm_b32 v106, v106, v82, v126\n\t"
    "v_perm_b32 v107, v107, v83, v126\n\t"
    "ds_write_b128 v141, v[204:207] offset:2112\n\t"
    "ds_write_b128 v141, v[208:211] offset:2368\n\t"
    "ds_read_b64_tr_b16 v[204:205], v142 offset:2112\n\t"
    "ds_read_b64_tr_b16 v[206:207], v142 offset:2368\n\t"
    "ds_read_b64_tr_b16 v[208:209], v142 offset:2144\n\t"
    "ds_read_b64_tr_b16 v[210:211], v142 offset:2400\n\t"
    "ds_write_b128 v141, v[212:215] offset:0\n\t"
    "ds_write_b128 v141, v[216:219] offset:256\n\t"
    "ds_read_b64_tr_b16 v[212:213], v142 offset:0\n\t"
    "ds_read_b64_tr_b16 v[214:215], v142 offset:256\n\t"
    "ds_read_b64_tr_b16 v[216:217], v142 offset:32\n\t"
    "ds_read_b64_tr_b16 v[218:219], v142 offset:288\n\t"
    "v_max_u32 v108, v100, v101\n\t"
    "v_min_u32 v101, v100, v101\n\t"
    "v_max_u32 v100, v102, v103\n\t"
    "v_min_u32 v103, v102, v103\n\t"
    "v_max_u32 v102, v104, v105\n\t"
    "v_min_u32 v105, v104, v105\n\t"
    "v_max_u32 v104, v106, v107\n\t"
    "v_min_u32 v107, v106, v107\n\t"
    "v_max_u32 v106, v108, v100\n\t"
    "v_min_u32 v100, v108, v100\n\t"
    "v_max_u32 v108, v101, v103\n\t"
    "v_min_u32 v103, v101, v103\n\t"
    "v_max_u32 v101, v102, v104\n\t"
    "v_min_u32 v104, v102, v104\n\t"
    "v_max_u32 v102, v105, v107\n\t"
    "v_min_u32 v107, v105, v107\n\t"
    "v_bfi_b32 v130, v106, 0, v129\n\t"
    "v_cmp_eq_u32 vcc, 0, v130\n\t"
    "v_and_or_b32 v92, v106, s44, v69\n\t"
    "s_nop 0\n\t"
    "v_cndmask_b32 v130, v130, v129, vcc\n\t"
    "v_ffbl_b32 v131, v130\n\t"
    "v_and_b32 v134, 7, v131\n\t"
    "v_lshlrev_b32 v134, v134, s45\n\t"
    "v_xor_b32 v129, v129, v134\n\t"
    "v_xor_b32 v132, v131, v127\n\t"
    "v_bfi_b32 v130, v101, 0, v129\n\t"
    "v_cmp_eq_u32 vcc, 0, v130\n\t"
    "v_and_or_b32 v93, v101, s44, v69\n\t"
    "v_xor_b32 v133, 23, v132\n\t"
    "v_lshlrev_b32 v110, v132, v136\n\t"
    "v_and_b32 v132, 7, v132\n\t"
    "v_lshl_or_b32 v110, v136, v133, v110\n\t"
    "v_lshl_add_u32 v132, v132, 1, v128\n\t"
    "ds_or_b32 v92, v110\n\t"
    "ds_write_b16 v132, v106\n\t"
    "v_cndmask_b32 v130, v130, v129, vcc\n\t"
    "v_ffbl_b32 v131, v130\n\t"
    "v_and_b32 v134, 7, v131\n\t"
    "v_lshlrev_b32 v134, v134, s45\n\t"
    "v_xor_b32 v129, v129, v134\n\t"
    "v_xor_b32 v132, v131, v127\n\t"
    "v_bfi_b32 v130, v108, 0, v129\n\t"
    "v_cmp_eq_u32 vcc, 0, v130\n\t"
    "v_and_or_b32 v94, v108, s44, v69\n\t"
    "v_xor_b32 v133, 23, v132\n\t"
    "v_lshlrev_b32 v111, v132, v136\n\t"
    "v_and_b32 v132, 7, v132\n\t"
    "v_lshl_or_b32 v111, v136, v133, v111\n\t"
    "v_lshl_add_u32 v132, v132, 1, v128\n\t"
    "ds_or_b32 v93, v111\n\t"
    "ds_write_b16 v132, v101\n\t"
    "v_cndmask_b32 v130, v130, v129, vcc\n\t"
    "v_ffbl_b32 v131, v130\n\t"
    "v_and_b32 v134, 7, v131\n\t"
    "v_lshlrev_b32 v134, v134, s45\n\t"
    "v_xor_b32 v129, v129, v134\n\t"
    "v_xor_b32 v132, v131, v127\n\t"
    "s_waitcnt lgkmcnt(10)\n\t"
    "buffer_store_dwordx4 v[204:207], v139, s[28:31], s48 offen sc1\n\t"
    "buffer_store_dwordx4 v[208:211], v140, s[28:31], s48 offen sc1\n\t"
    "s_add_u32 s48, s48, 819200\n\t"
    "s_waitcnt lgkmcnt(4)\n\t"
    "buffer_store_dwordx4 v[212:215], v139, s[28:31], s48 offen sc1\n\t"
    "buffer_store_dwordx4 v[216:219], v140, s[28:31], s48 offen sc1\n\t"
    "s_add_u32 s48, s48, 819200\n\t"
    "ds_write_b128 v141, v[48:51] offset:2112\n\t"
    "ds_write_b128 v141, v[52:55] offset:2368\n\t"
    "ds_read_b64_tr_b16 v[48:49], v142 offset:2112\n\t"
    "ds_read_b64_tr_b16 v[50:51], v142 offset:2368\n\t"
    "ds_read_b64_tr_b16 v[52:53], v142 offset:2144\n\t"
    "ds_read_b64_tr_b16 v[54:55], v142 offset:2400\n\t"
    "ds_write_b128 v141, v[220:223] offset:0\n\t"
    "ds_write_b128 v141, v[224:227] offset:256\n\t"
    "ds_read_b64_tr_b16 v[220:221], v142 offset:0\n\t"
    "ds_read_b64_tr_b16 v[222:223], v142 offset:256\n\t"
    "ds_read_b64_tr_b16 v[224:225], v142 offset:32\n\t"
    "ds_read_b64_tr_b16 v[226:227], v142 offset:288\n\t"
    "v_bfi_b32 v130, v100, 0, v129\n\t"
    "v_cmp_eq_u32 vcc, 0, v130\n\t"
    "v_and_or_b32 v95, v100, s44, v69\n\t"
    "v_xor_b32 v133, 23, v132\n\t"
    "v_lshlrev_b32 v112, v132, v136\n\t"
    "v_and_b32 v132, 7, v132\n\t"
    "v_lshl_or_b32 v112, v136, v133, v112\n\t"
    "v_lshl_add_u32 v132, v132, 1, v128\n\t"
    "ds_or_b32 v94, v112\n\t"
    "ds_write_b16 v132, v108\n\t"
    "v_cndmask_b32 v130, v130, v129, vcc\n\t"
    "v_ffbl_b32 v131, v130\n\t"
    "v_and_b32 v134, 7, v131\n\t"
    "v_lshlrev_b32 v134, v134, s45\n\t"
    "v_xor_b32 v129, v129, v134\n\t"
    "v_xor_b32 v132, v131, v127\n\t"
    "v_bfi_b32 v130, v102, 0, v129\n\t"
    "v_cmp_eq_u32 vcc, 0, v130\n\t"
    "v_and_or_b32 v96, v102, s44, v69\n\t"
    "v_xor_b32 v133, 23, v132\n\t"
    "v_lshlrev_b32 v113, v132, v136\n\t"
    "v_and_b32 v132, 7, v132\n\t"
    "v_lshl_or_b32 v113, v136, v133, v113\n\t"
    "v_lshl_add_u32 v132, v132, 1, v128\n\t"
    "ds_or_b32 v95, v113\n\t"
    "ds_write_b16 v132, v100\n\t"
    "v_cndmask_b32 v130, v130, v129, vcc\n\t"
    "v_ffbl_b32 v131, v130\n\t"
    "v_and_b32 v134, 7, v131\n\t"
    "v_lshlrev_b32 v134, v134, s45\n\t"
    "v_xor_b32 v129, v129, v134\n\t"
    "v_xor_b32 v132, v131, v127\n\t"
    "v_bfi_b32 v130, v104, 0, v129\n\t"
    "v_cmp_eq_u32 vcc, 0, v130\n\t"
    "v_and_or_b32 v97, v104, s44, v69\n\t"
    "v_xor_b32 v133, 23, v132\n\t"
    "v_lshlrev_b32 v114, v132, v136\n\t"
    "v_and_b32 v132, 7, v132\n\t"
    "v_lshl_or_b32 v114, v136, v133, v114\n\t"
    "v_lshl_add_u32 v132, v132, 1, v128\n\t"
    "ds_or_b32 v96, v114\n\t"
    "ds_write_b16 v132, v102\n\t"
    "v_cndmask_b32 v130, v130, v129, vcc\n\t"
    "v_ffbl_b32 v131, v130\n\t"
    "v_and_b32 v134, 7, v131\n\t"
    "v_lshlrev_b32 v134, v134, s45\n\t"
    "v_xor_b32 v129, v129, v134\n\t"
    "v_xor_b32 v132, v131, v127\n\t"
    "s_waitcnt lgkmcnt(12)\n\t"
    "buffer_store_dwordx4 v[48:51], v139, s[28:31], s48 offen sc1\n\t"
    "buffer_store_dwordx4 v[52:55], v140, s[28:31], s48 offen sc1\n\t"
    "s_add_u32 s48, s48, -2441216\n\t"
    "s_waitcnt lgkmcnt(6)\n\t"
    "s_and_b32 s53, %2, 12\n\t"
    "s_cselect_b32 s48, 0x40000000, s48\n\t"
    "buffer_store_dwordx4 v[220:223], v139, s[28:31], s48 offen sc1\n\t"
    "buffer_store_dwordx4 v[224:227], v140, s[28:31], s48 offen sc1\n\t"
    "s_add_u32 s48, s48, 819200\n\t"
    "ds_write_b128 v141, v[228:231] offset:2112\n\t"
    "ds_write_b128 v141, v[232:235] offset:2368\n\t"
    "ds_read_b64_tr_b16 v[228:229], v142 offset:2112\n\t"
    "ds_read_b64_tr_b16 v[230:231], v142 offset:2368\n\t"
    "ds_read_b64_tr_b16 v[232:233], v142 offset:2144\n\t"
    "ds_read_b64_tr_b16 v[234:235], v142 offset:2400\n\t"
    "ds_write_b128 v141, v[236:239] offset:0\n\t"
    "ds_write_b128 v141, v[240:243] offset:256\n\t"
    "ds_read_b64_tr_b16 v[236:237], v142 offset:0\n\t"
    "ds_read_b64_tr_b16 v[238:239], v142 offset:256\n\t"
    "ds_read_b64_tr_b16 v[240:241], v142 offset:32\n\t"
    "ds_read_b64_tr_b16 v[242:243], v142 offset:288\n\t"
    "v_bfi_b32 v130, v103, 0, v129\n\t"
    "v_cmp_eq_u32 vcc, 0, v130\n\t"
    "v_and_or_b32 v98, v103, s44, v69\n\t"
    "v_xor_b32 v133, 23, v132\n\t"
    "v_lshlrev_b32 v115, v132, v136\n\t"
    "v_and_b32 v132, 7, v132\n\t"
    "v_lshl_or_b32 v115, v136, v133, v115\n\t"
    "v_lshl_add_u32 v132, v132, 1, v128\n\t"
    "ds_or_b32 v97, v115\n\t"
    "ds_write_b16 v132, v104\n\t"
    "v_cndmask_b32 v130, v130, v129, vcc\n\t"
    "v_ffbl_b32 v131, v130\n\t"
    "v_and_b32 v134, 7, v131\n\t"
    "v_lshlrev_b32 v134, v134, s45\n\t"
    "v_xor_b32 v129, v129, v134\n\t"
    "v_xor_b32 v132, v131, v127\n\t"
    "v_bfi_b32 v130, v107, 0, v129\n\t"
    "v_cmp_eq_u32 vcc, 0, v130\n\t"
    "v_and_or_b32 v99, v107, s44, v69\n\t"
    "v_xor_b32 v133, 23, v132\n\t"
    "v_lshlrev_b32 v116, v132, v136\n\t"
    "v_and_b32 v132, 7, v132\n\t"
    "v_lshl_or_b32 v116, v136, v133, v116\n\t"
    "v_lshl_add_u32 v132, v132, 1, v128\n\t"
    "ds_or_b32 v98, v116\n\t"
    "ds_write_b16 v132, v103\n\t"
    "v_cndmask_b32 v130, v130, v129, vcc\n\t"
    "v_ffbl_b32 v131, v130\n\t"
    "v_xor_b32 v132, v131, v127\n\t"
    "v_xor_b32 v133, 23, v132\n\t"
    "v_lshlrev_b32 v117, v132, v136\n\t"
    "v_and_b32 v132, 7, v132\n\t"
    "v_lshl_or_b32 v117, v136, v133, v117\n\t"
    "v_lshl_add_u32 v132, v132, 1, v128\n\t"
    "ds_or_b32 v99, v117\n\t"
    "ds_write_b16 v132, v107\n\t"
    "ds_read_b128 v[72:75], v128\n\t"
    "v_and_or_b32 v100, v84, s44, v69\n\t"
    "ds_read_b32 v100, v100\n\t"
    "v_and_or_b32 v101, v85, s44, v69\n\t"
    "ds_read_b32 v101, v101\n\t"
    "v_and_or_b32 v102, v86, s44, v69\n\t"
    "ds_read_b32 v102, v102\n\t"
    "v_and_or_b32 v103, v87, s44, v69\n\t"
    "ds_read_b32 v103, v103\n\t"
    "v_and_or_b32 v104, v88, s44, v69\n\t"
    "ds_read_b32 v104, v104\n\t"
    "v_and_or_b32 v105, v89, s44, v69\n\t"
    "ds_read_b32 v105, v105\n\t"
    "v_and_or_b32 v106, v90, s44, v69\n\t"
    "ds_read_b32 v106, v106\n\t"
    "v_and_or_b32 v107, v91, s44, v69\n\t"
    "ds_read_b32 v107, v107\n\t"
    "v_lshl_add_u32 v76, v16, 3, s36\n\t"
    "v_lshl_add_u32 v77, v17, 3, s37\n\t"
    "v_lshl_add_u32 v78, v18, 3, s38\n\t"
    "v_lshl_add_u32 v79, v19, 3, s39\n\t"
    "v_lshl_add_u32 v80, v20, 3, s40\n\t"
    "v_lshl_add_u32 v81, v21, 3, s41\n\t"
    "v_lshl_add_u32 v82, v22, 3, s42\n\t"
    "v_lshl_add_u32 v83, v23, 3, s43\n\t"
    "s_waitcnt lgkmcnt(15)\n\t"
    "buffer_store_dwordx4 v[228:231], v139, s[28:31], s48 offen sc1\n\t"
    "buffer_store_dwordx4 v[232:235], v140, s[28:31], s48 offen sc1\n\t"
    "s_add_u32 s48, s48, 819200\n\t"
    "buffer_store_dwordx4 v[236:239], v139, s[28:31], s48 offen sc1\n\t"
    "buffer_store_dwordx4 v[240:243], v140, s[28:31], s48 offen sc1\n\t"
    "s_add_u32 s48, s48, 819200\n\t"
    "s_waitcnt lgkmcnt(8)\n\t"
    "buffer_store_dwordx4 v[72:75], v67, s[28:31], s49 offen offset:416 sc1\n\t"
    "v_mov_b32 v129, 0xffff0000\n\t"
    "s_waitcnt lgkmcnt(4)\n\t"
    "v_perm_b32 v100, v100, v84, v126\n\t"
    "v_perm_b32 v101, v101, v85, v126\n\t"
    "v_perm_b32 v102, v102, v86, v126\n\t"
    "v_perm_b32 v103, v103, v87, v126\n\t"
    "s_waitcnt lgkmcnt(0)\n\t"
    "v_perm_b32 v104, v104, v88, v126\n\t"
    "v_perm_b32 v105, v105, v89, v126\n\t"
    "v_perm_b32 v106, v106, v90, v126\n\t"
    "v_perm_b32 v107, v107, v91, v126\n\t"
    "s_waitcnt vmcnt(31)\n\t"
    "ds_write_b128 v141, v[244:247] offset:2112\n\t"
    "ds_write_b128 v141, v[248:251] offset:2368\n\t"
    "ds_read_b64_tr_b16 v[244:245], v142 offset:2112\n\t"
    "ds_read_b64_tr_b16 v[246:247], v142 offset:2368\n\t"
    "ds_read_b64_tr_b16 v[248:249], v142 offset:2144\n\t"
    "ds_read_b64_tr_b16 v[250:251], v142 offset:2400\n\t"
    "v_max_u32 v108, v100, v101\n\t"
    "v_min_u32 v101, v100, v101\n\t"
    "v_max_u32 v100, v102, v103\n\t"
    "v_min_u32 v103, v102, v103\n\t"
    "v_max_u32 v102, v104, v105\n\t"
    "v_min_u32 v105, v104, v105\n\t"
    "v_max_u32 v104, v106, v107\n\t"
    "v_min_u32 v107, v106, v107\n\t"
    "v_max_u32 v106, v108, v100\n\t"
    "v_min_u32 v100, v108, v100\n\t"
    "v_max_u32 v108, v101, v103\n\t"
    "v_min_u32 v103, v101, v103\n\t"
    "v_max_u32 v101, v102, v104\n\t"
    "v_min_u32 v104, v102, v104\n\t"
    "v_max_u32 v102, v105, v107\n\t"
    "v_min_u32 v107, v105, v107\n\t"
    "v_bfi_b32 v130, v106, 0, v129\n\t"
    "v_cmp_eq_u32 vcc, 0, v130\n\t"
    "v_and_or_b32 v92, v106, s44, v69\n\t"
    "s_nop 0\n\t"
    "v_cndmask_b32 v130, v130, v129, vcc\n\t"
    "v_ffbl_b32 v131, v130\n\t"
    "v_and_b32 v134, 7, v131\n\t"
    "v_lshlrev_b32 v134, v134, s45\n\t"
    "v_xor_b32 v129, v129, v134\n\t"
    "v_xor_b32 v132, v131, v127\n\t"
    "v_bfi_b32 v130, v101, 0, v129\n\t"
    "v_cmp_eq_u32 vcc, 0, v130\n\t"
    "v_and_or_b32 v93, v101, s44, v69\n\t"
    "v_xor_b32 v133, 23, v132\n\t"
    "v_lshlrev_b32 v110, v132, v136\n\t"
    "v_and_b32 v132, 7, v132\n\t"
    "v_lshl_or_b32 v110, v136, v133, v110\n\t"
    "v_lshl_add_u32 v132, v132, 1, v128\n\t"
    "ds_or_b32 v92, v110\n\t"
    "ds_write_b16 v132, v106\n\t"
    "v_cndmask_b32 v130, v130, v129, vcc\n\t"
    "v_ffbl_b32 v131, v130\n\t"
    "v_and_b32 v134, 7, v131\n\t"
    "v_lshlrev_b32 v134, v134, s45\n\t"
    "v_xor_b32 v129, v129, v134\n\t"
    "v_xor_b32 v132, v131, v127\n\t"
    "v_bfi_b32 v130, v108, 0, v129\n\t"
    "v_cmp_eq_u32 vcc, 0, v130\n\t"
    "v_and_or_b32 v94, v108, s44, v69\n\t"
    "v_xor_b32 v133, 23, v132\n\t"
    "v_lshlrev_b32 v111, v132, v136\n\t"
    "v_and_b32 v132, 7, v132\n\t"
    "v_lshl_or_b32 v111, v136, v133, v111\n\t"
    "v_lshl_add_u32 v132, v132, 1, v128\n\t"
    "ds_or_b32 v93, v111\n\t"
    "ds_write_b16 v132, v101\n\t"
    "v_cndmask_b32 v130, v130, v129, vcc\n\t"
    "v_ffbl_b32 v131, v130\n\t"
    "v_and_b32 v134, 7, v131\n\t"
    "v_lshlrev_b32 v134, v134, s45\n\t"
    "v_xor_b32 v129, v129, v134\n\t"
    "v_xor_b32 v132, v131, v127\n\t"
    "s_waitcnt lgkmcnt(4)\n\t"
    "buffer_store_dwordx4 v[244:247], v139, s[28:31], s48 offen sc1\n\t"
    "buffer_store_dwordx4 v[248:251], v140, s[28:31], s48 offen sc1\n\t"
    "s_add_u32 s48, s48, -2441216\n\t"
    "v_bfi_b32 v130, v100, 0, v129\n\t"
    "v_cmp_eq_u32 vcc, 0, v130\n\t"
    "v_and_or_b32 v95, v100, s44, v69\n\t"
    "v_xor_b32 v133, 23, v132\n\t"
    "v_lshlrev_b32 v112, v132, v136\n\t"
    "v_and_b32 v132, 7, v132\n\t"
    "v_lshl_or_b32 v112, v136, v133, v112\n\t"
    "v_lshl_add_u32 v132, v132, 1, v128\n\t"
    "ds_or_b32 v94, v112\n\t"
    "ds_write_b16 v132, v108\n\t"
    "v_cndmask_b32 v130, v130, v129, vcc\n\t"
    "v_ffbl_b32 v131, v130\n\t"
    "v_and_b32 v134, 7, v131\n\t"
    "v_lshlrev_b32 v134, v134, s45\n\t"
    "v_xor_b32 v129, v129, v134\n\t"
    "v_xor_b32 v132, v131, v127\n\t"
    "v_bfi_b32 v130, v102, 0, v129\n\t"
    "v_cmp_eq_u32 vcc, 0, v130\n\t"
    "v_and_or_b32 v96, v102, s44, v69\n\t"
    "v_xor_b32 v133, 23, v132\n\t"
    "v_lshlrev_b32 v113, v132, v136\n\t"
    "v_and_b32 v132, 7, v132\n\t"
    "v_lshl_or_b32 v113, v136, v133, v113\n\t"
    "v_lshl_add_u32 v132, v132, 1, v128\n\t"
    "ds_or_b32 v95, v113\n\t"
    "ds_write_b16 v132, v100\n\t"
    "v_cndmask_b32 v130, v130, v129, vcc\n\t"
    "v_ffbl_b32 v131, v130\n\t"
    "v_and_b32 v134, 7, v131\n\t"
    "v_lshlrev_b32 v134, v134, s45\n\t"
    "v_xor_b32 v129, v129, v134\n\t"
    "v_xor_b32 v132, v131, v127\n\t"
    "v_bfi_b32 v130, v104, 0, v129\n\t"
    "v_cmp_eq_u32 vcc, 0, v130\n\t"
    "v_and_or_b32 v97, v104, s44, v69\n\t"
    "v_xor_b32 v133, 23, v132\n\t"
    "v_lshlrev_b32 v114, v132, v136\n\t"
    "v_and_b32 v132, 7, v132\n\t"
    "v_lshl_or_b32 v114, v136, v133, v114\n\t"
    "v_lshl_add_u32 v132, v132, 1, v128\n\t"
    "ds_or_b32 v96, v114\n\t"
    "ds_write_b16 v132, v102\n\t"
    "v_cndmask_b32 v130, v130, v129, vcc\n\t"
    "v_ffbl_b32 v131, v130\n\t"
    "v_and_b32 v134, 7, v131\n\t"
    "v_lshlrev_b32 v134, v134, s45\n\t"
    "v_xor_b32 v129, v129, v134\n\t"
    "v_xor_b32 v132, v131, v127\n\t"
    "v_bfi_b32 v130, v103, 0, v129\n\t"
    "v_cmp_eq_u32 vcc, 0, v130\n\t"
    "v_and_or_b32 v98, v103, s44, v69\n\t"
    "v_xor_b32 v133, 23, v132\n\t"
    "v_lshlrev_b32 v115, v132, v136\n\t"
    "v_and_b32 v132, 7, v132\n\t"
    "v_lshl_or_b32 v115, v136, v133, v115\n\t"
    "v_lshl_add_u32 v132, v132, 1, v128\n\t"
    "ds_or_b32 v97, v115\n\t"
    "ds_write_b16 v132, v104\n\t"
    "v_cndmask_b32 v130, v130, v129, vcc\n\t"
    "v_ffbl_b32 v131, v130\n\t"
    "v_and_b32 v134, 7, v131\n\t"
    "v_lshlrev_b32 v134, v134, s45\n\t"
    "v_xor_b32 v129, v129, v134\n\t"
    "v_xor_b32 v132, v131, v127\n\t"
    "v_bfi_b32 v130, v107, 0, v129\n\t"
    "v_cmp_eq_u32 vcc, 0, v130\n\t"
    "v_and_or_b32 v99, v107, s44, v69\n\t"
    "v_xor_b32 v133, 23, v132\n\t"
    "v_lshlrev_b32 v116, v132, v136\n\t"
    "v_and_b32 v132, 7, v132\n\t"
    "v_lshl_or_b32 v116, v136, v133, v116\n\t"
    "v_lshl_add_u32 v132, v132, 1, v128\n\t"
    "ds_or_b32 v98, v116\n\t"
    "ds_write_b16 v132, v103\n\t"
    "v_cndmask_b32 v130, v130, v129, vcc\n\t"
    "v_ffbl_b32 v131, v130\n\t"
    "v_xor_b32 v132, v131, v127\n\t"
    "v_xor_b32 v133, 23, v132\n\t"
    "v_lshlrev_b32 v117, v132, v136\n\t"
    "v_and_b32 v132, 7, v132\n\t"
    "v_lshl_or_b32 v117, v136, v133, v117\n\t"
    "v_lshl_add_u32 v132, v132, 1, v128\n\t"
    "ds_or_b32 v99, v117\n\t"
    "ds_write_b16 v132, v107\n\t"
    "ds_read_b128 v[72:75], v128\n\t"
    "v_and_or_b32 v100, v76, s44, v69\n\t"
    "ds_read_b32 v100, v100\n\t"
    "v_and_or_b32 v101, v77, s44, v69\n\t"
    "ds_read_b32 v101, v101\n\t"
    "v_and_or_b32 v102, v78, s44, v69\n\t"
    "ds_read_b32 v102, v102\n\t"
    "v_and_or_b32 v103, v79, s44, v69\n\t"
    "ds_read_b32 v103, v103\n\t"
    "v_and_or_b32 v104, v80, s44, v69\n\t"
    "ds_read_b32 v104, v104\n\t"
    "v_and_or_b32 v105, v81, s44, v69\n\t"
    "ds_read_b32 v105, v105\n\t"
    "v_and_or_b32 v106, v82, s44, v69\n\t"
    "ds_read_b32 v106, v106\n\t"
    "v_and_or_b32 v107, v83, s44, v69\n\t"
    "ds_read_b32 v107, v107\n\t"
    "s_waitcnt lgkmcnt(8)\n\t"
    "buffer_store_dwordx4 v[72:75], v67, s[28:31], s49 offen offset:448 sc1\n\t"
    "v_mov_b32 v129, 0xffff0000\n\t"
    "s_waitcnt lgkmcnt(4)\n\t"
    "v_perm_b32 v100, v100, v76, v126\n\t"
    "v_perm_b32 v101, v101, v77, v126\n\t"
    "v_perm_b32 v102, v102, v78, v126\n\t"
    "v_perm_b32 v103, v103, v79, v126\n\t"
    "s_waitcnt lgkmcnt(0)\n\t"
    "v_perm_b32 v104, v104, v80, v126\n\t"
    "v_perm_b32 v105, v105, v81, v126\n\t"
    "v_perm_b32 v106, v106, v82, v126\n\t"
    "v_perm_b32 v107, v107, v83, v126\n\t"
    "v_max_u32 v108, v100, v101\n\t"
    "v_min_u32 v101, v100, v101\n\t"
    "v_max_u32 v100, v102, v103\n\t"
    "v_min_u32 v103, v102, v103\n\t"
    "v_max_u32 v102, v104, v105\n\t"
    "v_min_u32 v105, v104, v105\n\t"
    "v_max_u32 v104, v106, v107\n\t"
    "v_min_u32 v107, v106, v107\n\t"
    "v_max_u32 v106, v108, v100\n\t"
    "v_min_u32 v100, v108, v100\n\t"
    "v_max_u32 v108, v101, v103\n\t"
    "v_min_u32 v103, v101, v103\n\t"
    "v_max_u32 v101, v102, v104\n\t"
    "v_min_u32 v104, v102, v104\n\t"
    "v_max_u32 v102, v105, v107\n\t"
    "v_min_u32 v107, v105, v107\n\t"
    "v_bfi_b32 v130, v106, 0, v129\n\t"
    "v_cmp_eq_u32 vcc, 0, v130\n\t"
    "v_and_or_b32 v92, v106, s44, v69\n\t"
    "s_nop 0\n\t"
    "v_cndmask_b32 v130, v130, v129, vcc\n\t"
    "v_ffbl_b32 v131, v130\n\t"
    "v_and_b32 v134, 7, v131\n\t"
    "v_lshlrev_b32 v134, v134, s45\n\t"
    "v_xor_b32 v129, v129, v134\n\t"
    "v_xor_b32 v132, v131, v127\n\t"
    "v_bfi_b32 v130, v101, 0, v129\n\t"
    "v_cmp_eq_u32 vcc, 0, v130\n\t"
    "v_and_b32 v132, 7, v132\n\t"
    "v_lshl_add_u32 v132, v132, 1, v128\n\t"
    "ds_write_b16 v132, v106\n\t"
    "v_cndmask_b32 v130, v130, v129, vcc\n\t"
    "v_ffbl_b32 v131, v130\n\t"
    "v_and_b32 v134, 7, v131\n\t"
    "v_lshlrev_b32 v134, v134, s45\n\t"
    "v_xor_b32 v129, v129, v134\n\t"
    "v_xor_b32 v132, v131, v127\n\t"
    "v_bfi_b32 v130, v108, 0, v129\n\t"
    "v_cmp_eq_u32 vcc, 0, v130\n\t"
    "v_and_b32 v132, 7, v132\n\t"
    "v_lshl_add_u32 v132, v132, 1, v128\n\t"
    "ds_write_b16 v132, v101\n\t"
    "v_cndmask_b32 v130, v130, v129, vcc\n\t"
    "v_ffbl_b32 v131, v130\n\t"
    "v_and_b32 v134, 7, v131\n\t"
    "v_lshlrev_b32 v134, v134, s45\n\t"
    "v_xor_b32 v129, v129, v134\n\t"
    "v_xor_b32 v132, v131, v127\n\t"
    "v_bfi_b32 v130, v100, 0, v129\n\t"
    "v_cmp_eq_u32 vcc, 0, v130\n\t"
    "v_and_b32 v132, 7, v132\n\t"
    "v_lshl_add_u32 v132, v132, 1, v128\n\t"
    "ds_write_b16 v132, v108\n\t"
    "v_cndmask_b32 v130, v130, v129, vcc\n\t"
    "v_ffbl_b32 v131, v130\n\t"
    "v_and_b32 v134, 7, v131\n\t"
    "v_lshlrev_b32 v134, v134, s45\n\t"
    "v_xor_b32 v129, v129, v134\n\t"
    "v_xor_b32 v132, v131, v127\n\t"
    "v_bfi_b32 v130, v102, 0, v129\n\t"
    "v_cmp_eq_u32 vcc, 0, v130\n\t"
    "v_and_b32 v132, 7, v132\n\t"
    "v_lshl_add_u32 v132, v132, 1, v128\n\t"
    "ds_write_b16 v132, v100\n\t"
    "v_cndmask_b32 v130, v130, v129, vcc\n\t"
    "v_ffbl_b32 v131, v130\n\t"
    "v_and_b32 v134, 7, v131\n\t"
    "v_lshlrev_b32 v134, v134, s45\n\t"
    "v_xor_b32 v129, v129, v134\n\t"
    "v_xor_b32 v132, v131, v127\n\t"
    "v_bfi_b32 v130, v104, 0, v129\n\t"
    "v_cmp_eq_u32 vcc, 0, v130\n\t"
    "v_and_b32 v132, 7, v132\n\t"
    "v_lshl_add_u32 v132, v132, 1, v128\n\t"
    "ds_write_b16 v132, v102\n\t"
    "v_cndmask_b32 v130, v130, v129, vcc\n\t"
    "v_ffbl_b32 v131, v130\n\t"
    "v_and_b32 v134, 7, v131\n\t"
    "v_lshlrev_b32 v134, v134, s45\n\t"
    "v_xor_b32 v129, v129, v134\n\t"
    "v_xor_b32 v132, v131, v127\n\t"
    "v_bfi_b32 v130, v103, 0, v129\n\t"
    "v_cmp_eq_u32 vcc, 0, v130\n\t"
    "v_and_b32 v132, 7, v132\n\t"
    "v_lshl_add_u32 v132, v132, 1, v128\n\t"
    "ds_write_b16 v132, v104\n\t"
    "v_cndmask_b32 v130, v130, v129, vcc\n\t"
    "v_ffbl_b32 v131, v130\n\t"
    "v_and_b32 v134, 7, v131\n\t"
    "v_lshlrev_b32 v134, v134, s45\n\t"
    "v_xor_b32 v129, v129, v134\n\t"
    "v_xor_b32 v132, v131, v127\n\t"
    "v_bfi_b32 v130, v107, 0, v129\n\t"
    "v_cmp_eq_u32 vcc, 0, v130\n\t"
    "v_and_b32 v132, 7, v132\n\t"
    "v_lshl_add_u32 v132, v132, 1, v128\n\t"
    "ds_write_b16 v132, v103\n\t"
    "v_cndmask_b32 v130, v130, v129, vcc\n\t"
    "v_ffbl_b32 v131, v130\n\t"
    "v_xor_b32 v132, v131, v127\n\t"
    "v_and_b32 v132, 7, v132\n\t"
    "v_lshl_add_u32 v132, v132, 1, v128\n\t"
    "ds_write_b16 v132, v107\n\t"
    "ds_read_b128 v[72:75], v128\n\t"
    "s_waitcnt lgkmcnt(0)\n\t"
    "buffer_store_dwordx4 v[72:75], v67, s[28:31], s49 offen offset:480 sc1\n\t"
    "s_setprio 0\n\t"
    "v_mov_b32 v134, 1036\n\t"
    "L_asvsp12d_%=:\n\t"
    "ds_read_b32 v130, v134\n\t"
    "s_waitcnt lgkmcnt(0)\n\t"
    "v_readfirstlane_b32 s50, v130\n\t"
    "s_nop 0\n\t"
    "s_cmp_lt_u32 s50, 4\n\t"
    "s_cbranch_scc1 L_asvsp12d_%=\n\t"
    "s_call_b64 s[98:99], L_setup_%=\n\t"
    "v_lshrrev_b32 v105, 4, v127\n\t"
    "v_and_b32 v105, 48, v105\n\t"
    "s_movk_i32 s65, 130\n\t"
    "v_mad_u32_u24 v104, v155, s65, v105\n\t"
    "v_add_u32 v105, 14170, v104\n\t"
    "v_and_b32 v104, -16, v104\n\t"
    "v_and_b32 v105, -16, v105\n\t"
    "ds_read_b128 v[164:167], v104 offset:0\n\t"
    "ds_read_b128 v[168:171], v104 offset:64\n\t"
    "ds_read_b128 v[172:175], v104 offset:2080\n\t"
    "ds_read_b128 v[176:179], v104 offset:2144\n\t"
    "ds_read_b128 v[180:183], v104 offset:4160\n\t"
    "ds_read_b128 v[184:187], v104 offset:4224\n\t"
    "ds_read_b128 v[188:191], v104 offset:6240\n\t"
    "ds_read_b128 v[192:195], v104 offset:6304\n\t"
    "ds_read_b128 v[196:199], v104 offset:8320\n\t"
    "ds_read_b128 v[200:203], v104 offset:8384\n\t"
    "ds_read_b128 v[204:207], v104 offset:10400\n\t"
    "ds_read_b128 v[208:211], v104 offset:10464\n\t"
    "s_cmp_lt_u32 s62, 2\n\t"
    "s_cbranch_scc1 L_mel0_%=\n\t"
    "buffer_load_dwordx4 v[220:223], v127, s[28:31], s61 offen offset:2048 sc1\n\t"
    "buffer_load_dwordx4 v[224:227], v127, s[28:31], s61 offen offset:3072 sc1\n\t"
    "s_add_u32 s61, s61, 0x1000\n\t"
    "buffer_load_dwordx4 v[228:231], v127, s[28:31], s61 offen offset:2048 sc1\n\t"
    "buffer_load_dwordx4 v[232:235], v127, s[28:31], s61 offen offset:3072 sc1\n\t"
    "s_add_u32 s61, s61, 0x1000\n\t"
    "buffer_load_dwordx4 v[236:239], v127, s[28:31], s61 offen offset:2048 sc1\n\t"
    "buffer_load_dwordx4 v[240:243], v127, s[28:31], s61 offen offset:3072 sc1\n\t"
    "s_add_u32 s61, s61, 0x1000\n\t"
    "buffer_load_dwordx4 v[244:247], v127, s[28:31], s61 offen offset:2048 sc1\n\t"
    "buffer_load_dwordx4 v[248:251], v127, s[28:31], s61 offen offset:3072 sc1\n\t"
    "s_add_u32 s61, s61, 0x1000\n\t"
    "buffer_load_dwordx4 v[252:255], v127, s[28:31], s61 offen offset:2048 sc1\n\t"
    "s_mov_b32 s62, 1\n\t"
    "L_mel0_%=:\n\t"
    "buffer_load_dwordx4 v[0:3], v127, s[28:31], s63 offen offset:0 sc1\n\t"
    "buffer_load_dwordx4 v[4:7], v127, s[28:31], s63 offen offset:1024 sc1\n\t"
    "buffer_load_dwordx4 v[8:11], v127, s[28:31], s63 offen offset:2048 sc1\n\t"
    "buffer_load_dwordx4 v[12:15], v127, s[28:31], s63 offen offset:3072 sc1\n\t"
    "s_add_u32 s63, s63, 0x1000\n\t"
    "s_mov_b32 s62, 0\n\t"
    "s_and_b32 s65, %2, 8\n\t"
    "s_lshl_b32 s65, s65, 10\n\t"
    "s_add_u32 s65, s65, s63\n\t"
    "s_mov_b32 m0, 136192\n\t"
    "s_add_u32 s65, s65, 0\n\t"
    "buffer_load_dwordx4 v127, s[28:31], s65 offen offset:0 sc1 lds\n\t"
    "buffer_load_dwordx4 v127, s[28:31], s65 offen offset:1024 sc1 lds\n\t"
    "buffer_load_dwordx4 v127, s[28:31], s65 offen offset:2048 sc1 lds\n\t"
    "buffer_load_dwordx4 v127, s[28:31], s65 offen offset:3072 sc1 lds\n\t"
    "s_mov_b32 m0, 140288\n\t"
    "s_add_u32 s65, s65, 4096\n\t"
    "buffer_load_dwordx4 v127, s[28:31], s65 offen offset:0 sc1 lds\n\t"
    "buffer_load_dwordx4 v127, s[28:31], s65 offen offset:1024 sc1 lds\n\t"
    "buffer_load_dwordx4 v127, s[28:31], s65 offen offset:2048 sc1 lds\n\t"
    "buffer_load_dwordx4 v127, s[28:31], s65 offen offset:3072 sc1 lds\n\t"
    "s_waitcnt vmcnt(21)\n\t"
    "s_lshr_b32 s50, %2, 6\n\t"
    "s_lshl_b32 s50, s50, 7\n\t"
    "s_add_u32 s50, s50, 49287168\n\t"
    "s_mov_b64 exec, 1\n\t"
    "buffer_atomic_add v136, off, s[28:31], s50 sc1\n\t"
    "s_mov_b64 exec, -1\n\t"
    "s_branch L_af1x_%=\n\t"
    "L_af1w_%=:\n\t"
    "s_mov_b32 s51, 1048576\n\t"
    "s_mov_b64 exec, 0xff\n\t"
    "L_af1_%=:\n\t"
    "v_cmp_gt_u32 vcc, v148, v71\n\t"
    "s_cbranch_vccz L_af1ok_%=\n\t"
    "s_sleep 8\n\t"
    "buffer_load_dword v71, v70, s[28:31], s52 offen sc1\n\t"
    "s_sub_u32 s51, s51, 1\n\t"
    "s_waitcnt vmcnt(0)\n\t"
    "s_cmp_lg_u32 s51, 0\n\t"
    "s_cbranch_scc1 L_af1_%=\n\t"
    "L_af1ok_%=:\n\t"
    "s_mov_b64 exec, -1\n\t"
    "v_mov_b32 v134, 3112\n\t"
    "ds_write_b32 v134, v136\n\t"
    "v_mbcnt_lo_u32_b32 v70, -1, 0\n\t"
    "v_mbcnt_hi_u32_b32 v70, -1, v70\n\t"
    "s_and_b32 s50, %2, 7\n\t"
    "s_lshl_b32 s50, s50, 4\n\t"
    "s_lshr_b32 s51, %2, 4\n\t"
    "s_add_u32 s50, s50, s51\n\t"
    "s_mul_i32 s50, s50, 102400\n\t"
    "s_add_u32 s50, s50, 3407872\n\t"
    "v_lshlrev_b32 v148, 7, v70\n\t"
    "buffer_load_dword v71, v148, s[28:31], s50 offen sc1\n\t"
    "s_and_b32 s51, %2, 8\n\t"
    "s_lshl_b32 s51, s51, 10\n\t"
    "s_add_u32 s50, s50, s51\n\t"
    "s_add_u32 s50, s50, 0x1000\n\t"
    "v_lshrrev_b32 v149, 4, v70\n\t"
    "v_lshl_add_u32 v149, v149, 11, v148\n\t"
    "buffer_load_dword v71, v148, s[28:31], s50 offen sc1\n\t"
    "s_bfe_u32 s50, %2, 0x40003\n\t"
    "s_lshl_b32 s50, s50, 14\n\t"
    "s_add_u32 s50, s50, 16517120\n\t"
    "v_lshrrev_b32 v150, 3, v70\n\t"
    "v_lshl_add_u32 v150, v150, 10, v148\n\t"
    "buffer_load_dword v71, v149, s[28:31], s50 offen sc1\n\t"
    "s_and_b32 s50, %2, 8\n\t"
    "s_bfe_u32 s51, %2, 0x30004\n\t"
    "s_add_u32 s50, s50, s51\n\t"
    "s_mul_i32 s50, s50, 212992\n\t"
    "s_add_u32 s50, s50, 196608\n\t"
    "buffer_load_dword v71, v150, s[28:31], s50 offen sc1\n\t"
    "s_setpc_b64 s[54:55]\n\t"
    "L_af1x_%=:\n\t"
    "s_mov_b32 s69, 0x40000000\n\t"
    "s_branch L_aend2_%=\n\t"
    "L_ahelp_%=:\n\t"
    "s_setprio 3\n\t"
    "v_cmp_gt_u32 vcc, 48, v147\n\t"
    "v_mov_b32 v133, 0x40000000\n\t"
    "v_lshrrev_b32 v134, 3, v147\n\t"
    "v_add_u32 v134, 6, v134\n\t"
    "v_bfe_u32 v135, v147, 2, 1\n\t"
    "v_and_b32 v130, 3, v147\n\t"
    "v_lshl_add_u32 v132, v135, 5, v130\n\t"
    "v_mul_u32_u24 v64, 400, v132\n\t"
    "v_lshl_add_u32 v64, v134, 5, v64\n\t"
    "s_movk_i32 s50, 0\n\t"
    "buffer_load_dwordx4 v[0:3], v64, s[24:27], s50 offen offset:16\n\t"
    "buffer_load_dwordx4 v[4:7], v64, s[24:27], s50 offen offset:32\n\t"
    "s_movk_i32 s50, 0\n\t"
    "buffer_load_dwordx4 v[8:11], v64, s[24:27], s50 offen offset:1616\n\t"
    "buffer_load_dwordx4 v[12:15], v64, s[24:27], s50 offen offset:1632\n\t"
    "s_movk_i32 s50, 3200\n\t"
    "buffer_load_dwordx4 v[16:19], v64, s[24:27], s50 offen offset:16\n\t"
    "buffer_load_dwordx4 v[20:23], v64, s[24:27], s50 offen offset:32\n\t"
    "v_lshlrev_b32 v66, 14, v134\n\t"
    "v_lshl_add_u32 v66, v132, 4, v66\n\t"
    "v_lshlrev_b32 v68, 8, v134\n\t"
    "v_lshl_add_u32 v68, v135, 2, v68\n\t"
    "v_add_u32 v68, 130048, v68\n\t"
    "v_cndmask_b32 v68, v133, v68, vcc\n\t"
    "v_cndmask_b32 v66, v133, v66, vcc\n\t"
    "s_barrier\n\t"
    "s_load_dwordx2 s[72:73], s[12:13], 0\n\t"
    "s_load_dwordx2 s[88:89], s[14:15], 0\n\t"
    "s_load_dword s91, s[8:9], 0\n\t"
    "s_load_dwordx2 s[22:23], %1, 0x40\n\t"
    "v_lshlrev_b32 v128, 4, v147\n\t"
    "v_add_u32 v128, 133888, v128\n\t"
    "v_and_b32 v130, 1, v147\n\t"
    "v_cndmask_b32 v128, v133, v128, vcc\n\t"
    "v_mul_u32_u24 v127, 7, v130\n\t"
    "v_cmp_eq_u32 vcc, 1, v130\n\t"
    "v_mov_b32 v126, 0x07060100\n\t"
    "v_mov_b32 v133, 0x05040100\n\t"
    "v_mov_b32 v136, 1\n\t"
    "v_cndmask_b32 v126, v126, v133, vcc\n\t"
    "s_mov_b32 s36, 0\n\t"
    "s_mov_b32 s37, 8000\n\t"
    "s_mov_b32 s38, 16000\n\t"
    "s_mov_b32 s39, 24000\n\t"
    "s_mov_b32 s40, 32000\n\t"
    "s_mov_b32 s41, 40000\n\t"
    "s_mov_b32 s42, 48000\n\t"
    "s_mov_b32 s43, 56000\n\t"
    "s_movk_i32 s44, 0xf8\n\t"
    "s_mov_b32 s45, 0x01010000\n\t"
    "s_mov_b32 s46, 0x020c000c\n\t"
    "s_lshr_b32 s49, %2, 4\n\t"
    "s_mul_i32 s49, s49, 212992\n\t"
    "s_and_b32 s50, %2, 15\n\t"
    "s_lshl_b32 s50, s50, 10\n\t"
    "s_add_u32 s49, s49, s50\n\t"
    "s_lshl_b32 s52, %2, 2\n\t"
    "s_add_u32 s52, s52, 49291264\n\t"
    "buffer_load_dword v70, off, s[28:31], s52 offset:1024 sc1\n\t"
    "s_cmp_eq_u32 %2, 0\n\t"
    "s_cselect_b32 s47, 49291264, 0x40000000\n\t"
    "s_cselect_b32 s48, 49283072, 0x40000000\n\t"
    "v_lshlrev_b32 v152, 4, v147\n\t"
    "v_lshlrev_b32 v132, 7, v147\n\t"
    "v_mov_b32 v71, 0\n\t"
    "buffer_store_dword v71, v132, s[28:31], s48 offen sc1\n\t"
    "v_mov_b32 v148, 0\n\t"
    "v_mov_b32 v149, 0\n\t"
    "v_mov_b32 v150, 0\n\t"
    "v_mov_b32 v151, 0\n\t"
    "buffer_store_dwordx4 v[148:151], v152, s[28:31], s47 offen offset:2048 sc1\n\t"
    "buffer_store_dwordx4 v[148:151], v152, s[28:31], s47 offen offset:3072 sc1\n\t"
    "s_add_u32 s50, s48, 0x1000\n\t"
    "s_waitcnt vmcnt(10)\n\t"
    "v_lshlrev_b32 v134, 20, v147\n\t"
    "v_add_u32 v134, 2068, v134\n\t"
    "ds_add_u32 v134, v136\n\t"
    "v_mov_b32 v110, 65664\n\t"
    "v_mov_b32 v111, 131136\n\t"
    "v_mov_b32 v112, 262176\n\t"
    "v_mov_b32 v113, 524304\n\t"
    "v_mov_b32 v114, 1048584\n\t"
    "v_mov_b32 v115, 2097156\n\t"
    "v_mov_b32 v116, 4194306\n\t"
    "v_mov_b32 v117, 8388609\n\t"
    "s_waitcnt vmcnt(8)\n\t"
    "v_lshl_add_u32 v76, v0, 3, s36\n\t"
    "v_lshl_add_u32 v77, v1, 3, s37\n\t"
    "v_lshl_add_u32 v78, v2, 3, s38\n\t"
    "v_lshl_add_u32 v79, v3, 3, s39\n\t"
    "v_lshl_add_u32 v80, v4, 3, s40\n\t"
    "v_lshl_add_u32 v81, v5, 3, s41\n\t"
    "v_lshl_add_u32 v82, v6, 3, s42\n\t"
    "v_lshl_add_u32 v83, v7, 3, s43\n\t"
    "v_and_or_b32 v92, v76, s44, v68\n\t"
    "ds_or_rtn_b32 v118, v92, v110\n\t"
    "v_and_or_b32 v93, v77, s44, v68\n\t"
    "ds_or_rtn_b32 v119, v93, v111\n\t"
    "v_and_or_b32 v94, v78, s44, v68\n\t"
    "ds_or_rtn_b32 v120, v94, v112\n\t"
    "v_and_or_b32 v95, v79, s44, v68\n\t"
    "ds_or_rtn_b32 v121, v95, v113\n\t"
    "v_and_or_b32 v96, v80, s44, v68\n\t"
    "ds_or_rtn_b32 v122, v96, v114\n\t"
    "v_and_or_b32 v97, v81, s44, v68\n\t"
    "ds_or_rtn_b32 v123, v97, v115\n\t"
    "v_and_or_b32 v98, v82, s44, v68\n\t"
    "ds_or_rtn_b32 v124, v98, v116\n\t"
    "v_and_or_b32 v99, v83, s44, v68\n\t"
    "ds_or_rtn_b32 v125, v99, v117\n\t"
    "v_lshl_or_b32 v164, v77, 16, v76\n\t"
    "v_lshl_or_b32 v165, v79, 16, v78\n\t"
    "v_lshl_or_b32 v166, v81, 16, v80\n\t"
    "v_lshl_or_b32 v167, v83, 16, v82\n\t"
    "s_waitcnt vmcnt(3)\n\t"
    "v_add_u32 v70, 1, v70\n\t"
    "buffer_store_dword v70, off, s[28:31], s52 sc1\n\t"
    "v_mov_b32 v71, 2072\n\t"
    "ds_write_b32 v71, v70\n\t"
    "v_lshl_add_u32 v84, v8, 3, s36\n\t"
    "v_lshl_add_u32 v85, v9, 3, s37\n\t"
    "v_lshl_add_u32 v86, v10, 3, s38\n\t"
    "v_lshl_add_u32 v87, v11, 3, s39\n\t"
    "v_lshl_add_u32 v88, v12, 3, s40\n\t"
    "v_lshl_add_u32 v89, v13, 3, s41\n\t"
    "v_lshl_add_u32 v90, v14, 3, s42\n\t"
    "v_lshl_add_u32 v91, v15, 3, s43\n\t"
    "s_waitcnt lgkmcnt(5)\n\t"
    "v_and_b32 v118, v118, v110\n\t"
    "v_perm_b32 v118, v118, v118, s46\n\t"
    "ds_or_b32 v92, v118\n\t"
    "v_and_b32 v119, v119, v111\n\t"
    "v_perm_b32 v119, v119, v119, s46\n\t"
    "ds_or_b32 v93, v119\n\t"
    "v_and_b32 v120, v120, v112\n\t"
    "v_perm_b32 v120, v120, v120, s46\n\t"
    "ds_or_b32 v94, v120\n\t"
    "v_and_b32 v121, v121, v113\n\t"
    "v_perm_b32 v121, v121, v121, s46\n\t"
    "ds_or_b32 v95, v121\n\t"
    "s_waitcnt lgkmcnt(5)\n\t"
    "v_and_b32 v122, v122, v114\n\t"
    "v_perm_b32 v122, v122, v122, s46\n\t"
    "ds_or_b32 v96, v122\n\t"
    "v_and_b32 v123, v123, v115\n\t"
    "v_perm_b32 v123, v123, v123, s46\n\t"
    "ds_or_b32 v97, v123\n\t"
    "v_and_b32 v124, v124, v116\n\t"
    "v_perm_b32 v124, v124, v124, s46\n\t"
    "ds_or_b32 v98, v124\n\t"
    "v_and_b32 v125, v125, v117\n\t"
    "v_perm_b32 v125, v125, v125, s46\n\t"
    "ds_or_b32 v99, v125\n\t"
    "buffer_store_dwordx4 v[164:167], v66, s[28:31], s49 offen offset:0 sc1\n\t"
    "v_and_or_b32 v100, v84, s44, v68\n\t"
    "ds_read_b32 v100, v100\n\t"
    "v_and_or_b32 v101, v85, s44, v68\n\t"
    "ds_read_b32 v101, v101\n\t"
    "v_and_or_b32 v102, v86, s44, v68\n\t"
    "ds_read_b32 v102, v102\n\t"
    "v_and_or_b32 v103, v87, s44, v68\n\t"
    "ds_read_b32 v103, v103\n\t"
    "v_and_or_b32 v104, v88, s44, v68\n\t"
    "ds_read_b32 v104, v104\n\t"
    "v_and_or_b32 v105, v89, s44, v68\n\t"
    "ds_read_b32 v105, v105\n\t"
    "v_and_or_b32 v106, v90, s44, v68\n\t"
    "ds_read_b32 v106, v106\n\t"
    "v_and_or_b32 v107, v91, s44, v68\n\t"
    "ds_read_b32 v107, v107\n\t"
    "v_lshl_add_u32 v76, v16, 3, s36\n\t"
    "v_lshl_add_u32 v77, v17, 3, s37\n\t"
    "v_lshl_add_u32 v78, v18, 3, s38\n\t"
    "v_lshl_add_u32 v79, v19, 3, s39\n\t"
    "v_lshl_add_u32 v80, v20, 3, s40\n\t"
    "v_lshl_add_u32 v81, v21, 3, s41\n\t"
    "v_lshl_add_u32 v82, v22, 3, s42\n\t"
    "v_lshl_add_u32 v83, v23, 3, s43\n\t"
    "s_movk_i32 s50, 3200\n\t"
    "buffer_load_dwordx4 v[24:27], v64, s[24:27], s50 offen offset:1616\n\t"
    "buffer_load_dwordx4 v[28:31], v64, s[24:27], s50 offen offset:1632\n\t"
    "s_movk_i32 s50, 6400\n\t"
    "buffer_load_dwordx4 v[32:35], v64, s[24:27], s50 offen offset:16\n\t"
    "buffer_load_dwordx4 v[36:39], v64, s[24:27], s50 offen offset:32\n\t"
    "s_cmp_lg_u32 %2, 0\n\t"
    "s_cbranch_scc1 L_ahe182_%=\n\t"
    "buffer_load_dwordx4 v[148:151], v152, s[28:31], s47 offen sc1\n\t"
    "L_ahe182_%=:\n\t"
    "v_mov_b32 v129, 0xffff0000\n\t"
    "s_waitcnt lgkmcnt(4)\n\t"
    "v_perm_b32 v100, v100, v84, v126\n\t"
    "v_perm_b32 v101, v101, v85, v126\n\t"
    "v_perm_b32 v102, v102, v86, v126\n\t"
    "v_perm_b32 v103, v103, v87, v126\n\t"
    "s_waitcnt lgkmcnt(0)\n\t"
    "v_perm_b32 v104, v104, v88, v126\n\t"
    "v_perm_b32 v105, v105, v89, v126\n\t"
    "v_perm_b32 v106, v106, v90, v126\n\t"
    "v_perm_b32 v107, v107, v91, v126\n\t"
    "v_max_u32 v108, v100, v101\n\t"
    "v_min_u32 v101, v100, v101\n\t"
    "v_max_u32 v100, v102, v103\n\t"
    "v_min_u32 v103, v102, v103\n\t"
    "v_max_u32 v102, v104, v105\n\t"
    "v_min_u32 v105, v104, v105\n\t"
    "v_max_u32 v104, v106, v107\n\t"
    "v_min_u32 v107, v106, v107\n\t"
    "v_max_u32 v106, v108, v100\n\t"
    "v_min_u32 v100, v108, v100\n\t"
    "v_max_u32 v108, v101, v103\n\t"
    "v_min_u32 v103, v101, v103\n\t"
    "v_max_u32 v101, v102, v104\n\t"
    "v_min_u32 v104, v102, v104\n\t"
    "v_max_u32 v102, v105, v107\n\t"
    "v_min_u32 v107, v105, v107\n\t"
    "v_bfi_b32 v130, v106, 0, v129\n\t"
    "v_cmp_eq_u32 vcc, 0, v130\n\t"
    "v_and_or_b32 v92, v106, s44, v68\n\t"
    "s_nop 0\n\t"
    "v_cndmask_b32 v130, v130, v129, vcc\n\t"
    "v_ffbl_b32 v131, v130\n\t"
    "v_and_b32 v134, 7, v131\n\t"
    "v_lshlrev_b32 v134, v134, s45\n\t"
    "v_xor_b32 v129, v129, v134\n\t"
    "v_xor_b32 v132, v131, v127\n\t"
    "v_bfi_b32 v130, v101, 0, v129\n\t"
    "v_cmp_eq_u32 vcc, 0, v130\n\t"
    "v_and_or_b32 v93, v101, s44, v68\n\t"
    "v_xor_b32 v133, 23, v132\n\t"
    "v_lshlrev_b32 v110, v132, v136\n\t"
    "v_and_b32 v132, 7, v132\n\t"
    "v_lshl_or_b32 v110, v136, v133, v110\n\t"
    "v_lshl_add_u32 v132, v132, 1, v128\n\t"
    "ds_or_rtn_b32 v118, v92, v110\n\t"
    "ds_write_b16 v132, v106\n\t"
    "v_cndmask_b32 v130, v130, v129, vcc\n\t"
    "v_ffbl_b32 v131, v130\n\t"
    "v_and_b32 v134, 7, v131\n\t"
    "v_lshlrev_b32 v134, v134, s45\n\t"
    "v_xor_b32 v129, v129, v134\n\t"
    "v_xor_b32 v132, v131, v127\n\t"
    "v_bfi_b32 v130, v108, 0, v129\n\t"
    "v_cmp_eq_u32 vcc, 0, v130\n\t"
    "v_and_or_b32 v94, v108, s44, v68\n\t"
    "v_xor_b32 v133, 23, v132\n\t"
    "v_lshlrev_b32 v111, v132, v136\n\t"
    "v_and_b32 v132, 7, v132\n\t"
    "v_lshl_or_b32 v111, v136, v133, v111\n\t"
    "v_lshl_add_u32 v132, v132, 1, v128\n\t"
    "ds_or_rtn_b32 v119, v93, v111\n\t"
    "ds_write_b16 v132, v101\n\t"
    "v_cndmask_b32 v130, v130, v129, vcc\n\t"
    "v_ffbl_b32 v131, v130\n\t"
    "v_and_b32 v134, 7, v131\n\t"
    "v_lshlrev_b32 v134, v134, s45\n\t"
    "v_xor_b32 v129, v129, v134\n\t"
    "v_xor_b32 v132, v131, v127\n\t"
    "v_bfi_b32 v130, v100, 0, v129\n\t"
    "v_cmp_eq_u32 vcc, 0, v130\n\t"
    "v_and_or_b32 v95, v100, s44, v68\n\t"
    "v_xor_b32 v133, 23, v132\n\t"
    "v_lshlrev_b32 v112, v132, v136\n\t"
    "v_and_b32 v132, 7, v132\n\t"
    "v_lshl_or_b32 v112, v136, v133, v112\n\t"
    "v_lshl_add_u32 v132, v132, 1, v128\n\t"
    "ds_or_rtn_b32 v120, v94, v112\n\t"
    "ds_write_b16 v132, v108\n\t"
    "v_cndmask_b32 v130, v130, v129, vcc\n\t"
    "v_ffbl_b32 v131, v130\n\t"
    "v_and_b32 v134, 7, v131\n\t"
    "v_lshlrev_b32 v134, v134, s45\n\t"
    "v_xor_b32 v129, v129, v134\n\t"
    "v_xor_b32 v132, v131, v127\n\t"
    "v_bfi_b32 v130, v102, 0, v129\n\t"
    "v_cmp_eq_u32 vcc, 0, v130\n\t"
    "v_and_or_b32 v96, v102, s44, v68\n\t"
    "v_xor_b32 v133, 23, v132\n\t"
    "v_lshlrev_b32 v113, v132, v136\n\t"
    "v_and_b32 v132, 7, v132\n\t"
    "v_lshl_or_b32 v113, v136, v133, v113\n\t"
    "v_lshl_add_u32 v132, v132, 1, v128\n\t"
    "ds_or_rtn_b32 v121, v95, v113\n\t"
    "ds_write_b16 v132, v100\n\t"
    "v_cndmask_b32 v130, v130, v129, vcc\n\t"
    "v_ffbl_b32 v131, v130\n\t"
    "v_and_b32 v134, 7, v131\n\t"
    "v_lshlrev_b32 v134, v134, s45\n\t"
    "v_xor_b32 v129, v129, v134\n\t"
    "v_xor_b32 v132, v131, v127\n\t"
    "s_waitcnt lgkmcnt(7)\n\t"
    "v_and_b32 v118, v118, v110\n\t"
    "v_perm_b32 v118, v118, v118, s46\n\t"
    "ds_or_b32 v92, v118\n\t"
    "v_bfi_b32 v130, v104, 0, v129\n\t"
    "v_cmp_eq_u32 vcc, 0, v130\n\t"
    "v_and_or_b32 v97, v104, s44, v68\n\t"
    "v_xor_b32 v133, 23, v132\n\t"
    "v_lshlrev_b32 v114, v132, v136\n\t"
    "v_and_b32 v132, 7, v132\n\t"
    "v_lshl_or_b32 v114, v136, v133, v114\n\t"
    "v_lshl_add_u32 v132, v132, 1, v128\n\t"
    "ds_or_rtn_b32 v122, v96, v114\n\t"
    "ds_write_b16 v132, v102\n\t"
    "v_cndmask_b32 v130, v130, v129, vcc\n\t"
    "v_ffbl_b32 v131, v130\n\t"
    "v_and_b32 v134, 7, v131\n\t"
    "v_lshlrev_b32 v134, v134, s45\n\t"
    "v_xor_b32 v129, v129, v134\n\t"
    "v_xor_b32 v132, v131, v127\n\t"
    "s_waitcnt lgkmcnt(8)\n\t"
    "v_and_b32 v119, v119, v111\n\t"
    "v_perm_b32 v119, v119, v119, s46\n\t"
    "ds_or_b32 v93, v119\n\t"
    "v_bfi_b32 v130, v103, 0, v129\n\t"
    "v_cmp_eq_u32 vcc, 0, v130\n\t"
    "v_and_or_b32 v98, v103, s44, v68\n\t"
    "v_xor_b32 v133, 23, v132\n\t"
    "v_lshlrev_b32 v115, v132, v136\n\t"
    "v_and_b32 v132, 7, v132\n\t"
    "v_lshl_or_b32 v115, v136, v133, v115\n\t"
    "v_lshl_add_u32 v132, v132, 1, v128\n\t"
    "ds_or_rtn_b32 v123, v97, v115\n\t"
    "ds_write_b16 v132, v104\n\t"
    "v_cndmask_b32 v130, v130, v129, vcc\n\t"
    "v_ffbl_b32 v131, v130\n\t"
    "v_and_b32 v134, 7, v131\n\t"
    "v_lshlrev_b32 v134, v134, s45\n\t"
    "v_xor_b32 v129, v129, v134\n\t"
    "v_xor_b32 v132, v131, v127\n\t"
    "s_waitcnt lgkmcnt(9)\n\t"
    "v_and_b32 v120, v120, v112\n\t"
    "v_perm_b32 v120, v120, v120, s46\n\t"
    "ds_or_b32 v94, v120\n\t"
    "v_bfi_b32 v130, v107, 0, v129\n\t"
    "v_cmp_eq_u32 vcc, 0, v130\n\t"
    "v_and_or_b32 v99, v107, s44, v68\n\t"
    "v_xor_b32 v133, 23, v132\n\t"
    "v_lshlrev_b32 v116, v132, v136\n\t"
    "v_and_b32 v132, 7, v132\n\t"
    "v_lshl_or_b32 v116, v136, v133, v116\n\t"
    "v_lshl_add_u32 v132, v132, 1, v128\n\t"
    "ds_or_rtn_b32 v124, v98, v116\n\t"
    "ds_write_b16 v132, v103\n\t"
    "v_cndmask_b32 v130, v130, v129, vcc\n\t"
    "v_ffbl_b32 v131, v130\n\t"
    "v_xor_b32 v132, v131, v127\n\t"
    "s_waitcnt lgkmcnt(10)\n\t"
    "v_and_b32 v121, v121, v113\n\t"
    "v_perm_b32 v121, v121, v121, s46\n\t"
    "ds_or_b32 v95, v121\n\t"
    "v_xor_b32 v133, 23, v132\n\t"
    "v_lshlrev_b32 v117, v132, v136\n\t"
    "v_and_b32 v132, 7, v132\n\t"
    "v_lshl_or_b32 v117, v136, v133, v117\n\t"
    "v_lshl_add_u32 v132, v132, 1, v128\n\t"
    "ds_or_rtn_b32 v125, v99, v117\n\t"
    "ds_write_b16 v132, v107\n\t"
    "s_waitcnt lgkmcnt(10)\n\t"
    "v_and_b32 v122, v122, v114\n\t"
    "v_perm_b32 v122, v122, v122, s46\n\t"
    "ds_or_b32 v96, v122\n\t"
    "ds_read_b128 v[72:75], v128\n\t"
    "v_and_or_b32 v100, v76, s44, v68\n\t"
    "ds_read_b32 v100, v100\n\t"
    "v_and_or_b32 v101, v77, s44, v68\n\t"
    "ds_read_b32 v101, v101\n\t"
    "v_and_or_b32 v102, v78, s44, v68\n\t"
    "ds_read_b32 v102, v102\n\t"
    "v_and_or_b32 v103, v79, s44, v68\n\t"
    "ds_read_b32 v103, v103\n\t"
    "v_and_or_b32 v104, v80, s44, v68\n\t"
    "ds_read_b32 v104, v104\n\t"
    "v_and_or_b32 v105, v81, s44, v68\n\t"
    "ds_read_b32 v105, v105\n\t"
    "v_and_or_b32 v106, v82, s44, v68\n\t"
    "ds_read_b32 v106, v106\n\t"
    "v_and_or_b32 v107, v83, s44, v68\n\t"
    "ds_read_b32 v107, v107\n\t"
    "s_cmp_lg_u32 %2, 0\n\t"
    "s_cbranch_scc1 L_ahe376_%=\n\t"
    "s_waitcnt vmcnt(0)\n\t"
    "buffer_store_dwordx4 v[148:151], v152, s[28:31], s47 offen offset:1024 sc1\n\t"
    "L_ahe376_%=:\n\t"
    "s_waitcnt vmcnt(2)\n\t"
    "v_lshl_add_u32 v84, v24, 3, s36\n\t"
    "v_lshl_add_u32 v85, v25, 3, s37\n\t"
    "v_lshl_add_u32 v86, v26, 3, s38\n\t"
    "v_lshl_add_u32 v87, v27, 3, s39\n\t"
    "v_lshl_add_u32 v88, v28, 3, s40\n\t"
    "v_lshl_add_u32 v89, v29, 3, s41\n\t"
    "v_lshl_add_u32 v90, v30, 3, s42\n\t"
    "v_lshl_add_u32 v91, v31, 3, s43\n\t"
    "v_mul_u32_u24 v58, 400, v147\n\t"
    "buffer_load_dwordx4 v[48:51], v58, s[24:27], 0 offen\n\t"
    "s_lshr_b32 s50, %2, 5\n\t"
    "s_mul_i32 s50, s50, 125\n\t"
    "s_bfe_u32 s51, %2, 0x30002\n\t"
    "s_lshl_b32 s51, s51, 4\n\t"
    "s_min_u32 s51, s51, 109\n\t"
    "s_add_u32 s50, s50, s51\n\t"
    "s_lshl_b32 s50, s50, 8\n\t"
    "s_and_b32 s51, %2, 3\n\t"
    "s_lshl_b32 s51, s51, 6\n\t"
    "s_add_u32 s50, s50, s51\n\t"
    "s_bitcmp1_b32 %2, 1\n\t"
    "s_cselect_b32 s50, s50, 0x40000000\n\t"
    "v_and_b32 v59, 48, v147\n\t"
    "v_and_b32 v60, 15, v147\n\t"
    "v_lshl_add_u32 v59, v60, 8, v59\n\t"
    "buffer_load_dwordx4 v[52:55], v59, s[4:7], s50 offen\n\t"
    "s_cmp_lg_u32 %2, 0\n\t"
    "s_cbranch_scc1 L_ahe408_%=\n\t"
    "buffer_load_dwordx4 v[148:151], v152, s[28:31], s47 offen sc1\n\t"
    "L_ahe408_%=:\n\t"
    "s_waitcnt lgkmcnt(11)\n\t"
    "v_and_b32 v123, v123, v115\n\t"
    "v_perm_b32 v123, v123, v123, s46\n\t"
    "ds_or_b32 v97, v123\n\t"
    "v_and_b32 v124, v124, v116\n\t"
    "v_perm_b32 v124, v124, v124, s46\n\t"
    "ds_or_b32 v98, v124\n\t"
    "v_and_b32 v125, v125, v117\n\t"
    "v_perm_b32 v125, v125, v125, s46\n\t"
    "ds_or_b32 v99, v125\n\t"
    "s_waitcnt lgkmcnt(11)\n\t"
    "buffer_store_dwordx4 v[72:75], v66, s[28:31], s49 offen offset:64 sc1\n\t"
    "v_mov_b32 v129, 0xffff0000\n\t"
    "s_waitcnt lgkmcnt(7)\n\t"
    "v_perm_b32 v100, v100, v76, v126\n\t"
    "v_perm_b32 v101, v101, v77, v126\n\t"
    "v_perm_b32 v102, v102, v78, v126\n\t"
    "v_perm_b32 v103, v103, v79, v126\n\t"
    "s_waitcnt lgkmcnt(3)\n\t"
    "v_perm_b32 v104, v104, v80, v126\n\t"
    "v_perm_b32 v105, v105, v81, v126\n\t"
    "v_perm_b32 v106, v106, v82, v126\n\t"
    "v_perm_b32 v107, v107, v83, v126\n\t"
    "v_max_u32 v108, v100, v101\n\t"
    "v_min_u32 v101, v100, v101\n\t"
    "v_max_u32 v100, v102, v103\n\t"
    "v_min_u32 v103, v102, v103\n\t"
    "v_max_u32 v102, v104, v105\n\t"
    "v_min_u32 v105, v104, v105\n\t"
    "v_max_u32 v104, v106, v107\n\t"
    "v_min_u32 v107, v106, v107\n\t"
    "v_max_u32 v106, v108, v100\n\t"
    "v_min_u32 v100, v108, v100\n\t"
    "v_max_u32 v108, v101, v103\n\t"
    "v_min_u32 v103, v101, v103\n\t"
    "v_max_u32 v101, v102, v104\n\t"
    "v_min_u32 v104, v102, v104\n\t"
    "v_max_u32 v102, v105, v107\n\t"
    "v_min_u32 v107, v105, v107\n\t"
    "v_bfi_b32 v130, v106, 0, v129\n\t"
    "v_cmp_eq_u32 vcc, 0, v130\n\t"
    "v_and_or_b32 v92, v106, s44, v68\n\t"
    "s_nop 0\n\t"
    "v_cndmask_b32 v130, v130, v129, vcc\n\t"
    "v_ffbl_b32 v131, v130\n\t"
    "v_and_b32 v134, 7, v131\n\t"
    "v_lshlrev_b32 v134, v134, s45\n\t"
    "v_xor_b32 v129, v129, v134\n\t"
    "v_xor_b32 v132, v131, v127\n\t"
    "v_bfi_b32 v130, v101, 0, v129\n\t"
    "v_cmp_eq_u32 vcc, 0, v130\n\t"
    "v_and_or_b32 v93, v101, s44, v68\n\t"
    "v_xor_b32 v133, 23, v132\n\t"
    "v_lshlrev_b32 v110, v132, v136\n\t"
    "v_and_b32 v132, 7, v132\n\t"
    "v_lshl_or_b32 v110, v136, v133, v110\n\t"
    "v_lshl_add_u32 v132, v132, 1, v128\n\t"
    "ds_or_rtn_b32 v118, v92, v110\n\t"
    "ds_write_b16 v132, v106\n\t"
    "v_cndmask_b32 v130, v130, v129, vcc\n\t"
    "v_ffbl_b32 v131, v130\n\t"
    "v_and_b32 v134, 7, v131\n\t"
    "v_lshlrev_b32 v134, v134, s45\n\t"
    "v_xor_b32 v129, v129, v134\n\t"
    "v_xor_b32 v132, v131, v127\n\t"
    "v_bfi_b32 v130, v108, 0, v129\n\t"
    "v_cmp_eq_u32 vcc, 0, v130\n\t"
    "v_and_or_b32 v94, v108, s44, v68\n\t"
    "v_xor_b32 v133, 23, v132\n\t"
    "v_lshlrev_b32 v111, v132, v136\n\t"
    "v_and_b32 v132, 7, v132\n\t"
    "v_lshl_or_b32 v111, v136, v133, v111\n\t"
    "v_lshl_add_u32 v132, v132, 1, v128\n\t"
    "ds_or_rtn_b32 v119, v93, v111\n\t"
    "ds_write_b16 v132, v101\n\t"
    "v_cndmask_b32 v130, v130, v129, vcc\n\t"
    "v_ffbl_b32 v131, v130\n\t"
    "v_and_b32 v134, 7, v131\n\t"
    "v_lshlrev_b32 v134, v134, s45\n\t"
    "v_xor_b32 v129, v129, v134\n\t"
    "v_xor_b32 v132, v131, v127\n\t"
    "v_bfi_b32 v130, v100, 0, v129\n\t"
    "v_cmp_eq_u32 vcc, 0, v130\n\t"
    "v_and_or_b32 v95, v100, s44, v68\n\t"
    "v_xor_b32 v133, 23, v132\n\t"
    "v_lshlrev_b32 v112, v132, v136\n\t"
    "v_and_b32 v132, 7, v132\n\t"
    "v_lshl_or_b32 v112, v136, v133, v112\n\t"
    "v_lshl_add_u32 v132, v132, 1, v128\n\t"
    "ds_or_rtn_b32 v120, v94, v112\n\t"
    "ds_write_b16 v132, v108\n\t"
    "v_cndmask_b32 v130, v130, v129, vcc\n\t"
    "v_ffbl_b32 v131, v130\n\t"
    "v_and_b32 v134, 7, v131\n\t"
    "v_lshlrev_b32 v134, v134, s45\n\t"
    "v_xor_b32 v129, v129, v134\n\t"
    "v_xor_b32 v132, v131, v127\n\t"
    "v_bfi_b32 v130, v102, 0, v129\n\t"
    "v_cmp_eq_u32 vcc, 0, v130\n\t"
    "v_and_or_b32 v96, v102, s44, v68\n\t"
    "v_xor_b32 v133, 23, v132\n\t"
    "v_lshlrev_b32 v113, v132, v136\n\t"
    "v_and_b32 v132, 7, v132\n\t"
    "v_lshl_or_b32 v113, v136, v133, v113\n\t"
    "v_lshl_add_u32 v132, v132, 1, v128\n\t"
    "ds_or_rtn_b32 v121, v95, v113\n\t"
    "ds_write_b16 v132, v100\n\t"
    "v_cndmask_b32 v130, v130, v129, vcc\n\t"
    "v_ffbl_b32 v131, v130\n\t"
    "v_and_b32 v134, 7, v131\n\t"
    "v_lshlrev_b32 v134, v134, s45\n\t"
    "v_xor_b32 v129, v129, v134\n\t"
    "v_xor_b32 v132, v131, v127\n\t"
    "s_waitcnt lgkmcnt(7)\n\t"
    "v_and_b32 v118, v118, v110\n\t"
    "v_perm_b32 v118, v118, v118, s46\n\t"
    "ds_or_b32 v92, v118\n\t"
    "v_bfi_b32 v130, v104, 0, v129\n\t"
    "v_cmp_eq_u32 vcc, 0, v130\n\t"
    "v_and_or_b32 v97, v104, s44, v68\n\t"
    "v_xor_b32 v133, 23, v132\n\t"
    "v_lshlrev_b32 v114, v132, v136\n\t"
    "v_and_b32 v132, 7, v132\n\t"
    "v_lshl_or_b32 v114, v136, v133, v114\n\t"
    "v_lshl_add_u32 v132, v132, 1, v128\n\t"
    "ds_or_rtn_b32 v122, v96, v114\n\t"
    "ds_write_b16 v132, v102\n\t"
    "v_cndmask_b32 v130, v130, v129, vcc\n\t"
    "v_ffbl_b32 v131, v130\n\t"
    "v_and_b32 v134, 7, v131\n\t"
    "v_lshlrev_b32 v134, v134, s45\n\t"
    "v_xor_b32 v129, v129, v134\n\t"
    "v_xor_b32 v132, v131, v127\n\t"
    "s_waitcnt lgkmcnt(8)\n\t"
    "v_and_b32 v119, v119, v111\n\t"
    "v_perm_b32 v119, v119, v119, s46\n\t"
    "ds_or_b32 v93, v119\n\t"
    "v_bfi_b32 v130, v103, 0, v129\n\t"
    "v_cmp_eq_u32 vcc, 0, v130\n\t"
    "v_and_or_b32 v98, v103, s44, v68\n\t"
    "v_xor_b32 v133, 23, v132\n\t"
    "v_lshlrev_b32 v115, v132, v136\n\t"
    "v_and_b32 v132, 7, v132\n\t"
    "v_lshl_or_b32 v115, v136, v133, v115\n\t"
    "v_lshl_add_u32 v132, v132, 1, v128\n\t"
    "ds_or_rtn_b32 v123, v97, v115\n\t"
    "ds_write_b16 v132, v104\n\t"
    "v_cndmask_b32 v130, v130, v129, vcc\n\t"
    "v_ffbl_b32 v131, v130\n\t"
    "v_and_b32 v134, 7, v131\n\t"
    "v_lshlrev_b32 v134, v134, s45\n\t"
    "v_xor_b32 v129, v129, v134\n\t"
    "v_xor_b32 v132, v131, v127\n\t"
    "s_waitcnt lgkmcnt(9)\n\t"
    "v_and_b32 v120, v120, v112\n\t"
    "v_perm_b32 v120, v120, v120, s46\n\t"
    "ds_or_b32 v94, v120\n\t"
    "v_bfi_b32 v130, v107, 0, v129\n\t"
    "v_cmp_eq_u32 vcc, 0, v130\n\t"
    "v_and_or_b32 v99, v107, s44, v68\n\t"
    "v_xor_b32 v133, 23, v132\n\t"
    "v_lshlrev_b32 v116, v132, v136\n\t"
    "v_and_b32 v132, 7, v132\n\t"
    "v_lshl_or_b32 v116, v136, v133, v116\n\t"
    "v_lshl_add_u32 v132, v132, 1, v128\n\t"
    "ds_or_rtn_b32 v124, v98, v116\n\t"
    "ds_write_b16 v132, v103\n\t"
    "v_cndmask_b32 v130, v130, v129, vcc\n\t"
    "v_ffbl_b32 v131, v130\n\t"
    "v_xor_b32 v132, v131, v127\n\t"
    "s_waitcnt lgkmcnt(10)\n\t"
    "v_and_b32 v121, v121, v113\n\t"
    "v_perm_b32 v121, v121, v121, s46\n\t"
    "ds_or_b32 v95, v121\n\t"
    "v_xor_b32 v133, 23, v132\n\t"
    "v_lshlrev_b32 v117, v132, v136\n\t"
    "v_and_b32 v132, 7, v132\n\t"
    "v_lshl_or_b32 v117, v136, v133, v117\n\t"
    "v_lshl_add_u32 v132, v132, 1, v128\n\t"
    "ds_or_rtn_b32 v125, v99, v117\n\t"
    "ds_write_b16 v132, v107\n\t"
    "s_waitcnt lgkmcnt(10)\n\t"
    "v_and_b32 v122, v122, v114\n\t"
    "v_perm_b32 v122, v122, v122, s46\n\t"
    "ds_or_b32 v96, v122\n\t"
    "ds_read_b128 v[72:75], v128\n\t"
    "v_and_or_b32 v100, v84, s44, v68\n\t"
    "ds_read_b32 v100, v100\n\t"
    "v_and_or_b32 v101, v85, s44, v68\n\t"
    "ds_read_b32 v101, v101\n\t"
    "v_and_or_b32 v102, v86, s44, v68\n\t"
    "ds_read_b32 v102, v102\n\t"
    "v_and_or_b32 v103, v87, s44, v68\n\t"
    "ds_read_b32 v103, v103\n\t"
    "v_and_or_b32 v104, v88, s44, v68\n\t"
    "ds_read_b32 v104, v104\n\t"
    "v_and_or_b32 v105, v89, s44, v68\n\t"
    "ds_read_b32 v105, v105\n\t"
    "v_and_or_b32 v106, v90, s44, v68\n\t"
    "ds_read_b32 v106, v106\n\t"
    "v_and_or_b32 v107, v91, s44, v68\n\t"
    "ds_read_b32 v107, v107\n\t"
    "s_cmp_lg_u32 %2, 0\n\t"
    "s_cbranch_scc1 L_ahe614_%=\n\t"
    "s_waitcnt vmcnt(0)\n\t"
    "buffer_store_dwordx4 v[148:151], v152, s[28:31], s47 offen offset:1024 sc1\n\t"
    "L_ahe614_%=:\n\t"
    "s_waitcnt vmcnt(3)\n\t"
    "v_lshl_add_u32 v76, v32, 3, s36\n\t"
    "v_lshl_add_u32 v77, v33, 3, s37\n\t"
    "v_lshl_add_u32 v78, v34, 3, s38\n\t"
    "v_lshl_add_u32 v79, v35, 3, s39\n\t"
    "v_lshl_add_u32 v80, v36, 3, s40\n\t"
    "v_lshl_add_u32 v81, v37, 3, s41\n\t"
    "v_lshl_add_u32 v82, v38, 3, s42\n\t"
    "v_lshl_add_u32 v83, v39, 3, s43\n\t"
    "s_waitcnt vmcnt(1)\n\t"
    "v_lshl_add_u32 v48, v48, 3, s36\n\t"
    "v_lshl_add_u32 v49, v49, 3, s37\n\t"
    "v_lshl_add_u32 v50, v50, 3, s38\n\t"
    "v_lshl_add_u32 v51, v51, 3, s39\n\t"
    "v_lshl_or_b32 v56, v49, 16, v48\n\t"
    "v_lshl_or_b32 v57, v51, 16, v50\n\t"
    "s_and_b32 s51, %2, 1\n\t"
    "s_mul_i32 s51, s51, 1016\n\t"
    "s_sub_u32 s50, s49, s51\n\t"
    "s_add_u32 s50, s50, 196608\n\t"
    "buffer_store_dwordx2 v[56:57], v152, s[28:31], s50 offen sc1\n\t"
    "s_lshl_b32 s50, %2, 10\n\t"
    "s_add_u32 s50, s50, 16515072\n\t"
    "s_bitcmp1_b32 %2, 1\n\t"
    "s_cselect_b32 s50, s50, 0x40000000\n\t"
    "buffer_store_dwordx4 v[52:55], v152, s[28:31], s50 offen sc1\n\t"
    "s_mul_i32 s50, %2, 69632\n\t"
    "s_add_u32 s50, s50, 31522816\n\t"
    "v_mov_b32 v60, 0\n\t"
    "v_lshlrev_b32 v61, 3, v152\n\t"
    "s_mov_b64 exec, 0xffffffff\n\t"
    "buffer_store_dword v60, v61, s[28:31], s50 offen sc1\n\t"
    "s_lshl_b32 s50, %2, 7\n\t"
    "s_add_u32 s50, s50, 49295360\n\t"
    "v_lshrrev_b32 v62, 2, v152\n\t"
    "buffer_store_dword v60, v62, s[28:31], s50 offen sc1\n\t"
    "s_mov_b64 exec, -1\n\t"
    "s_cmp_lg_u32 %2, 0\n\t"
    "s_cbranch_scc1 L_ahe656_%=\n\t"
    "buffer_load_dwordx4 v[148:151], v152, s[28:31], s47 offen sc1\n\t"
    "L_ahe656_%=:\n\t"
    "s_waitcnt lgkmcnt(11)\n\t"
    "v_and_b32 v123, v123, v115\n\t"
    "v_perm_b32 v123, v123, v123, s46\n\t"
    "ds_or_b32 v97, v123\n\t"
    "v_and_b32 v124, v124, v116\n\t"
    "v_perm_b32 v124, v124, v124, s46\n\t"
    "ds_or_b32 v98, v124\n\t"
    "v_and_b32 v125, v125, v117\n\t"
    "v_perm_b32 v125, v125, v125, s46\n\t"
    "ds_or_b32 v99, v125\n\t"
    "s_waitcnt lgkmcnt(11)\n\t"
    "buffer_store_dwordx4 v[72:75], v66, s[28:31], s49 offen offset:128 sc1\n\t"
    "v_mov_b32 v129, 0xffff0000\n\t"
    "s_waitcnt lgkmcnt(7)\n\t"
    "v_perm_b32 v100, v100, v84, v126\n\t"
    "v_perm_b32 v101, v101, v85, v126\n\t"
    "v_perm_b32 v102, v102, v86, v126\n\t"
    "v_perm_b32 v103, v103, v87, v126\n\t"
    "s_waitcnt lgkmcnt(3)\n\t"
    "v_perm_b32 v104, v104, v88, v126\n\t"
    "v_perm_b32 v105, v105, v89, v126\n\t"
    "v_perm_b32 v106, v106, v90, v126\n\t"
    "v_perm_b32 v107, v107, v91, v126\n\t"
    "v_max_u32 v108, v100, v101\n\t"
    "v_min_u32 v101, v100, v101\n\t"
    "v_max_u32 v100, v102, v103\n\t"
    "v_min_u32 v103, v102, v103\n\t"
    "v_max_u32 v102, v104, v105\n\t"
    "v_min_u32 v105, v104, v105\n\t"
    "v_max_u32 v104, v106, v107\n\t"
    "v_min_u32 v107, v106, v107\n\t"
    "v_max_u32 v106, v108, v100\n\t"
    "v_min_u32 v100, v108, v100\n\t"
    "v_max_u32 v108, v101, v103\n\t"
    "v_min_u32 v103, v101, v103\n\t"
    "v_max_u32 v101, v102, v104\n\t"
    "v_min_u32 v104, v102, v104\n\t"
    "v_max_u32 v102, v105, v107\n\t"
    "v_min_u32 v107, v105, v107\n\t"
    "v_bfi_b32 v130, v106, 0, v129\n\t"
    "v_cmp_eq_u32 vcc, 0, v130\n\t"
    "v_and_or_b32 v92, v106, s44, v68\n\t"
    "s_nop 0\n\t"
    "v_cndmask_b32 v130, v130, v129, vcc\n\t"
    "v_ffbl_b32 v131, v130\n\t"
    "v_and_b32 v134, 7, v131\n\t"
    "v_lshlrev_b32 v134, v134, s45\n\t"
    "v_xor_b32 v129, v129, v134\n\t"
    "v_xor_b32 v132, v131, v127\n\t"
    "v_bfi_b32 v130, v101, 0, v129\n\t"
    "v_cmp_eq_u32 vcc, 0, v130\n\t"
    "v_and_or_b32 v93, v101, s44, v68\n\t"
    "v_xor_b32 v133, 23, v132\n\t"
    "v_lshlrev_b32 v110, v132, v136\n\t"
    "v_and_b32 v132, 7, v132\n\t"
    "v_lshl_or_b32 v110, v136, v133, v110\n\t"
    "v_lshl_add_u32 v132, v132, 1, v128\n\t"
    "ds_or_rtn_b32 v118, v92, v110\n\t"
    "ds_write_b16 v132, v106\n\t"
    "v_cndmask_b32 v130, v130, v129, vcc\n\t"
    "v_ffbl_b32 v131, v130\n\t"
    "v_and_b32 v134, 7, v131\n\t"
    "v_lshlrev_b32 v134, v134, s45\n\t"
    "v_xor_b32 v129, v129, v134\n\t"
    "v_xor_b32 v132, v131, v127\n\t"
    "v_bfi_b32 v130, v108, 0, v129\n\t"
    "v_cmp_eq_u32 vcc, 0, v130\n\t"
    "v_and_or_b32 v94, v108, s44, v68\n\t"
    "v_xor_b32 v133, 23, v132\n\t"
    "v_lshlrev_b32 v111, v132, v136\n\t"
    "v_and_b32 v132, 7, v132\n\t"
    "v_lshl_or_b32 v111, v136, v133, v111\n\t"
    "v_lshl_add_u32 v132, v132, 1, v128\n\t"
    "ds_or_rtn_b32 v119, v93, v111\n\t"
    "ds_write_b16 v132, v101\n\t"
    "v_cndmask_b32 v130, v130, v129, vcc\n\t"
    "v_ffbl_b32 v131, v130\n\t"
    "v_and_b32 v134, 7, v131\n\t"
    "v_lshlrev_b32 v134, v134, s45\n\t"
    "v_xor_b32 v129, v129, v134\n\t"
    "v_xor_b32 v132, v131, v127\n\t"
    "v_bfi_b32 v130, v100, 0, v129\n\t"
    "v_cmp_eq_u32 vcc, 0, v130\n\t"
    "v_and_or_b32 v95, v100, s44, v68\n\t"
    "v_xor_b32 v133, 23, v132\n\t"
    "v_lshlrev_b32 v112, v132, v136\n\t"
    "v_and_b32 v132, 7, v132\n\t"
    "v_lshl_or_b32 v112, v136, v133, v112\n\t"
    "v_lshl_add_u32 v132, v132, 1, v128\n\t"
    "ds_or_rtn_b32 v120, v94, v112\n\t"
    "ds_write_b16 v132, v108\n\t"
    "v_cndmask_b32 v130, v130, v129, vcc\n\t"
    "v_ffbl_b32 v131, v130\n\t"
    "v_and_b32 v134, 7, v131\n\t"
    "v_lshlrev_b32 v134, v134, s45\n\t"
    "v_xor_b32 v129, v129, v134\n\t"
    "v_xor_b32 v132, v131, v127\n\t"
    "v_bfi_b32 v130, v102, 0, v129\n\t"
    "v_cmp_eq_u32 vcc, 0, v130\n\t"
    "v_and_or_b32 v96, v102, s44, v68\n\t"
    "v_xor_b32 v133, 23, v132\n\t"
    "v_lshlrev_b32 v113, v132, v136\n\t"
    "v_and_b32 v132, 7, v132\n\t"
    "v_lshl_or_b32 v113, v136, v133, v113\n\t"
    "v_lshl_add_u32 v132, v132, 1, v128\n\t"
    "ds_or_rtn_b32 v121, v95, v113\n\t"
    "ds_write_b16 v132, v100\n\t"
    "v_cndmask_b32 v130, v130, v129, vcc\n\t"
    "v_ffbl_b32 v131, v130\n\t"
    "v_and_b32 v134, 7, v131\n\t"
    "v_lshlrev_b32 v134, v134, s45\n\t"
    "v_xor_b32 v129, v129, v134\n\t"
    "v_xor_b32 v132, v131, v127\n\t"
    "s_waitcnt lgkmcnt(7)\n\t"
    "v_and_b32 v118, v118, v110\n\t"
    "v_perm_b32 v118, v118, v118, s46\n\t"
    "ds_or_b32 v92, v118\n\t"
    "v_bfi_b32 v130, v104, 0, v129\n\t"
    "v_cmp_eq_u32 vcc, 0, v130\n\t"
    "v_and_or_b32 v97, v104, s44, v68\n\t"
    "v_xor_b32 v133, 23, v132\n\t"
    "v_lshlrev_b32 v114, v132, v136\n\t"
    "v_and_b32 v132, 7, v132\n\t"
    "v_lshl_or_b32 v114, v136, v133, v114\n\t"
    "v_lshl_add_u32 v132, v132, 1, v128\n\t"
    "ds_or_rtn_b32 v122, v96, v114\n\t"
    "ds_write_b16 v132, v102\n\t"
    "v_cndmask_b32 v130, v130, v129, vcc\n\t"
    "v_ffbl_b32 v131, v130\n\t"
    "v_and_b32 v134, 7, v131\n\t"
    "v_lshlrev_b32 v134, v134, s45\n\t"
    "v_xor_b32 v129, v129, v134\n\t"
    "v_xor_b32 v132, v131, v127\n\t"
    "s_waitcnt lgkmcnt(8)\n\t"
    "v_and_b32 v119, v119, v111\n\t"
    "v_perm_b32 v119, v119, v119, s46\n\t"
    "ds_or_b32 v93, v119\n\t"
    "v_bfi_b32 v130, v103, 0, v129\n\t"
    "v_cmp_eq_u32 vcc, 0, v130\n\t"
    "v_and_or_b32 v98, v103, s44, v68\n\t"
    "v_xor_b32 v133, 23, v132\n\t"
    "v_lshlrev_b32 v115, v132, v136\n\t"
    "v_and_b32 v132, 7, v132\n\t"
    "v_lshl_or_b32 v115, v136, v133, v115\n\t"
    "v_lshl_add_u32 v132, v132, 1, v128\n\t"
    "ds_or_rtn_b32 v123, v97, v115\n\t"
    "ds_write_b16 v132, v104\n\t"
    "v_cndmask_b32 v130, v130, v129, vcc\n\t"
    "v_ffbl_b32 v131, v130\n\t"
    "v_and_b32 v134, 7, v131\n\t"
    "v_lshlrev_b32 v134, v134, s45\n\t"
    "v_xor_b32 v129, v129, v134\n\t"
    "v_xor_b32 v132, v131, v127\n\t"
    "s_waitcnt lgkmcnt(9)\n\t"
    "v_and_b32 v120, v120, v112\n\t"
    "v_perm_b32 v120, v120, v120, s46\n\t"
    "ds_or_b32 v94, v120\n\t"
    "v_bfi_b32 v130, v107, 0, v129\n\t"
    "v_cmp_eq_u32 vcc, 0, v130\n\t"
    "v_and_or_b32 v99, v107, s44, v68\n\t"
    "v_xor_b32 v133, 23, v132\n\t"
    "v_lshlrev_b32 v116, v132, v136\n\t"
    "v_and_b32 v132, 7, v132\n\t"
    "v_lshl_or_b32 v116, v136, v133, v116\n\t"
    "v_lshl_add_u32 v132, v132, 1, v128\n\t"
    "ds_or_rtn_b32 v124, v98, v116\n\t"
    "ds_write_b16 v132, v103\n\t"
    "v_cndmask_b32 v130, v130, v129, vcc\n\t"
    "v_ffbl_b32 v131, v130\n\t"
    "v_xor_b32 v132, v131, v127\n\t"
    "s_waitcnt lgkmcnt(10)\n\t"
    "v_and_b32 v121, v121, v113\n\t"
    "v_perm_b32 v121, v121, v121, s46\n\t"
    "ds_or_b32 v95, v121\n\t"
    "v_xor_b32 v133, 23, v132\n\t"
    "v_lshlrev_b32 v117, v132, v136\n\t"
    "v_and_b32 v132, 7, v132\n\t"
    "v_lshl_or_b32 v117, v136, v133, v117\n\t"
    "v_lshl_add_u32 v132, v132, 1, v128\n\t"
    "ds_or_rtn_b32 v125, v99, v117\n\t"
    "ds_write_b16 v132, v107\n\t"
    "s_waitcnt lgkmcnt(10)\n\t"
    "v_and_b32 v122, v122, v114\n\t"
    "v_perm_b32 v122, v122, v122, s46\n\t"
    "ds_or_b32 v96, v122\n\t"
    "ds_read_b128 v[72:75], v128\n\t"
    "v_and_or_b32 v100, v76, s44, v68\n\t"
    "ds_read_b32 v100, v100\n\t"
    "v_and_or_b32 v101, v77, s44, v68\n\t"
    "ds_read_b32 v101, v101\n\t"
    "v_and_or_b32 v102, v78, s44, v68\n\t"
    "ds_read_b32 v102, v102\n\t"
    "v_and_or_b32 v103, v79, s44, v68\n\t"
    "ds_read_b32 v103, v103\n\t"
    "v_and_or_b32 v104, v80, s44, v68\n\t"
    "ds_read_b32 v104, v104\n\t"
    "v_and_or_b32 v105, v81, s44, v68\n\t"
    "ds_read_b32 v105, v105\n\t"
    "v_and_or_b32 v106, v82, s44, v68\n\t"
    "ds_read_b32 v106, v106\n\t"
    "v_and_or_b32 v107, v83, s44, v68\n\t"
    "ds_read_b32 v107, v107\n\t"
    "s_cmp_lg_u32 %2, 0\n\t"
    "s_cbranch_scc1 L_ahe862_%=\n\t"
    "s_waitcnt vmcnt(0)\n\t"
    "buffer_store_dwordx4 v[148:151], v152, s[28:31], s47 offen offset:1024 sc1\n\t"
    "L_ahe862_%=:\n\t"
    "s_cmp_lg_u32 %2, 0\n\t"
    "s_cbranch_scc1 L_ahe867_%=\n\t"
    "buffer_load_dwordx4 v[148:151], v152, s[28:31], s47 offen sc1\n\t"
    "L_ahe867_%=:\n\t"
    "s_waitcnt lgkmcnt(11)\n\t"
    "v_and_b32 v123, v123, v115\n\t"
    "v_perm_b32 v123, v123, v123, s46\n\t"
    "ds_or_b32 v97, v123\n\t"
    "v_and_b32 v124, v124, v116\n\t"
    "v_perm_b32 v124, v124, v124, s46\n\t"
    "ds_or_b32 v98, v124\n\t"
    "v_and_b32 v125, v125, v117\n\t"
    "v_perm_b32 v125, v125, v125, s46\n\t"
    "ds_or_b32 v99, v125\n\t"
    "s_waitcnt lgkmcnt(11)\n\t"
    "buffer_store_dwordx4 v[72:75], v66, s[28:31], s49 offen offset:192 sc1\n\t"
    "v_mov_b32 v129, 0xffff0000\n\t"
    "s_waitcnt lgkmcnt(7)\n\t"
    "v_perm_b32 v100, v100, v76, v126\n\t"
    "v_perm_b32 v101, v101, v77, v126\n\t"
    "v_perm_b32 v102, v102, v78, v126\n\t"
    "v_perm_b32 v103, v103, v79, v126\n\t"
    "s_waitcnt lgkmcnt(3)\n\t"
    "v_perm_b32 v104, v104, v80, v126\n\t"
    "v_perm_b32 v105, v105, v81, v126\n\t"
    "v_perm_b32 v106, v106, v82, v126\n\t"
    "v_perm_b32 v107, v107, v83, v126\n\t"
    "v_max_u32 v108, v100, v101\n\t"
    "v_min_u32 v101, v100, v101\n\t"
    "v_max_u32 v100, v102, v103\n\t"
    "v_min_u32 v103, v102, v103\n\t"
    "v_max_u32 v102, v104, v105\n\t"
    "v_min_u32 v105, v104, v105\n\t"
    "v_max_u32 v104, v106, v107\n\t"
    "v_min_u32 v107, v106, v107\n\t"
    "v_max_u32 v106, v108, v100\n\t"
    "v_min_u32 v100, v108, v100\n\t"
    "v_max_u32 v108, v101, v103\n\t"
    "v_min_u32 v103, v101, v103\n\t"
    "v_max_u32 v101, v102, v104\n\t"
    "v_min_u32 v104, v102, v104\n\t"
    "v_max_u32 v102, v105, v107\n\t"
    "v_min_u32 v107, v105, v107\n\t"
    "v_bfi_b32 v130, v106, 0, v129\n\t"
    "v_cmp_eq_u32 vcc, 0, v130\n\t"
    "v_and_or_b32 v92, v106, s44, v68\n\t"
    "s_nop 0\n\t"
    "v_cndmask_b32 v130, v130, v129, vcc\n\t"
    "v_ffbl_b32 v131, v130\n\t"
    "v_and_b32 v134, 7, v131\n\t"
    "v_lshlrev_b32 v134, v134, s45\n\t"
    "v_xor_b32 v129, v129, v134\n\t"
    "v_xor_b32 v132, v131, v127\n\t"
    "v_bfi_b32 v130, v101, 0, v129\n\t"
    "v_cmp_eq_u32 vcc, 0, v130\n\t"
    "v_and_or_b32 v93, v101, s44, v68\n\t"
    "v_xor_b32 v133, 23, v132\n\t"
    "v_lshlrev_b32 v110, v132, v136\n\t"
    "v_and_b32 v132, 7, v132\n\t"
    "v_lshl_or_b32 v110, v136, v133, v110\n\t"
    "v_lshl_add_u32 v132, v132, 1, v128\n\t"
    "ds_or_rtn_b32 v118, v92, v110\n\t"
    "ds_write_b16 v132, v106\n\t"
    "v_cndmask_b32 v130, v130, v129, vcc\n\t"
    "v_ffbl_b32 v131, v130\n\t"
    "v_and_b32 v134, 7, v131\n\t"
    "v_lshlrev_b32 v134, v134, s45\n\t"
    "v_xor_b32 v129, v129, v134\n\t"
    "v_xor_b32 v132, v131, v127\n\t"
    "v_bfi_b32 v130, v108, 0, v129\n\t"
    "v_cmp_eq_u32 vcc, 0, v130\n\t"
    "v_and_or_b32 v94, v108, s44, v68\n\t"
    "v_xor_b32 v133, 23, v132\n\t"
    "v_lshlrev_b32 v111, v132, v136\n\t"
    "v_and_b32 v132, 7, v132\n\t"
    "v_lshl_or_b32 v111, v136, v133, v111\n\t"
    "v_lshl_add_u32 v132, v132, 1, v128\n\t"
    "ds_or_rtn_b32 v119, v93, v111\n\t"
    "ds_write_b16 v132, v101\n\t"
    "v_cndmask_b32 v130, v130, v129, vcc\n\t"
    "v_ffbl_b32 v131, v130\n\t"
    "v_and_b32 v134, 7, v131\n\t"
    "v_lshlrev_b32 v134, v134, s45\n\t"
    "v_xor_b32 v129, v129, v134\n\t"
    "v_xor_b32 v132, v131, v127\n\t"
    "v_bfi_b32 v130, v100, 0, v129\n\t"
    "v_cmp_eq_u32 vcc, 0, v130\n\t"
    "v_and_or_b32 v95, v100, s44, v68\n\t"
    "v_xor_b32 v133, 23, v132\n\t"
    "v_lshlrev_b32 v112, v132, v136\n\t"
    "v_and_b32 v132, 7, v132\n\t"
    "v_lshl_or_b32 v112, v136, v133, v112\n\t"
    "v_lshl_add_u32 v132, v132, 1, v128\n\t"
    "ds_or_rtn_b32 v120, v94, v112\n\t"
    "ds_write_b16 v132, v108\n\t"
    "v_cndmask_b32 v130, v130, v129, vcc\n\t"
    "v_ffbl_b32 v131, v130\n\t"
    "v_and_b32 v134, 7, v131\n\t"
    "v_lshlrev_b32 v134, v134, s45\n\t"
    "v_xor_b32 v129, v129, v134\n\t"
    "v_xor_b32 v132, v131, v127\n\t"
    "v_bfi_b32 v130, v102, 0, v129\n\t"
    "v_cmp_eq_u32 vcc, 0, v130\n\t"
    "v_and_or_b32 v96, v102, s44, v68\n\t"
    "v_xor_b32 v133, 23, v132\n\t"
    "v_lshlrev_b32 v113, v132, v136\n\t"
    "v_and_b32 v132, 7, v132\n\t"
    "v_lshl_or_b32 v113, v136, v133, v113\n\t"
    "v_lshl_add_u32 v132, v132, 1, v128\n\t"
    "ds_or_rtn_b32 v121, v95, v113\n\t"
    "ds_write_b16 v132, v100\n\t"
    "v_cndmask_b32 v130, v130, v129, vcc\n\t"
    "v_ffbl_b32 v131, v130\n\t"
    "v_and_b32 v134, 7, v131\n\t"
    "v_lshlrev_b32 v134, v134, s45\n\t"
    "v_xor_b32 v129, v129, v134\n\t"
    "v_xor_b32 v132, v131, v127\n\t"
    "s_waitcnt lgkmcnt(7)\n\t"
    "v_and_b32 v118, v118, v110\n\t"
    "v_perm_b32 v118, v118, v118, s46\n\t"
    "ds_or_b32 v92, v118\n\t"
    "s_waitcnt vmcnt(2)\n\t"
    "ds_write_b32 v71, v136 offset:4\n\t"
    "v_bfi_b32 v130, v104, 0, v129\n\t"
    "v_cmp_eq_u32 vcc, 0, v130\n\t"
    "v_and_or_b32 v97, v104, s44, v68\n\t"
    "v_xor_b32 v133, 23, v132\n\t"
    "v_lshlrev_b32 v114, v132, v136\n\t"
    "v_and_b32 v132, 7, v132\n\t"
    "v_lshl_or_b32 v114, v136, v133, v114\n\t"
    "v_lshl_add_u32 v132, v132, 1, v128\n\t"
    "ds_or_rtn_b32 v122, v96, v114\n\t"
    "ds_write_b16 v132, v102\n\t"
    "v_cndmask_b32 v130, v130, v129, vcc\n\t"
    "v_ffbl_b32 v131, v130\n\t"
    "v_and_b32 v134, 7, v131\n\t"
    "v_lshlrev_b32 v134, v134, s45\n\t"
    "v_xor_b32 v129, v129, v134\n\t"
    "v_xor_b32 v132, v131, v127\n\t"
    "s_waitcnt lgkmcnt(9)\n\t"
    "v_and_b32 v119, v119, v111\n\t"
    "v_perm_b32 v119, v119, v119, s46\n\t"
    "ds_or_b32 v93, v119\n\t"
    "v_bfi_b32 v130, v103, 0, v129\n\t"
    "v_cmp_eq_u32 vcc, 0, v130\n\t"
    "v_and_or_b32 v98, v103, s44, v68\n\t"
    "v_xor_b32 v133, 23, v132\n\t"
    "v_lshlrev_b32 v115, v132, v136\n\t"
    "v_and_b32 v132, 7, v132\n\t"
    "v_lshl_or_b32 v115, v136, v133, v115\n\t"
    "v_lshl_add_u32 v132, v132, 1, v128\n\t"
    "ds_or_rtn_b32 v123, v97, v115\n\t"
    "ds_write_b16 v132, v104\n\t"
    "v_cndmask_b32 v130, v130, v129, vcc\n\t"
    "v_ffbl_b32 v131, v130\n\t"
    "v_and_b32 v134, 7, v131\n\t"
    "v_lshlrev_b32 v134, v134, s45\n\t"
    "v_xor_b32 v129, v129, v134\n\t"
    "v_xor_b32 v132, v131, v127\n\t"
    "s_waitcnt lgkmcnt(10)\n\t"
    "v_and_b32 v120, v120, v112\n\t"
    "v_perm_b32 v120, v120, v120, s46\n\t"
    "ds_or_b32 v94, v120\n\t"
    "v_bfi_b32 v130, v107, 0, v129\n\t"
    "v_cmp_eq_u32 vcc, 0, v130\n\t"
    "v_and_or_b32 v99, v107, s44, v68\n\t"
    "v_xor_b32 v133, 23, v132\n\t"
    "v_lshlrev_b32 v116, v132, v136\n\t"
    "v_and_b32 v132, 7, v132\n\t"
    "v_lshl_or_b32 v116, v136, v133, v116\n\t"
    "v_lshl_add_u32 v132, v132, 1, v128\n\t"
    "ds_or_rtn_b32 v124, v98, v116\n\t"
    "ds_write_b16 v132, v103\n\t"
    "v_cndmask_b32 v130, v130, v129, vcc\n\t"
    "v_ffbl_b32 v131, v130\n\t"
    "v_xor_b32 v132, v131, v127\n\t"
    "s_waitcnt lgkmcnt(11)\n\t"
    "v_and_b32 v121, v121, v113\n\t"
    "v_perm_b32 v121, v121, v121, s46\n\t"
    "ds_or_b32 v95, v121\n\t"
    "v_xor_b32 v133, 23, v132\n\t"
    "v_lshlrev_b32 v117, v132, v136\n\t"
    "v_and_b32 v132, 7, v132\n\t"
    "v_lshl_or_b32 v117, v136, v133, v117\n\t"
    "v_lshl_add_u32 v132, v132, 1, v128\n\t"
    "ds_or_rtn_b32 v125, v99, v117\n\t"
    "ds_write_b16 v132, v107\n\t"
    "s_waitcnt lgkmcnt(10)\n\t"
    "v_and_b32 v122, v122, v114\n\t"
    "v_perm_b32 v122, v122, v122, s46\n\t"
    "ds_or_b32 v96, v122\n\t"
    "ds_read_b128 v[72:75], v128\n\t"
    "s_waitcnt lgkmcnt(3)\n\t"
    "v_and_b32 v123, v123, v115\n\t"
    "v_perm_b32 v123, v123, v123, s46\n\t"
    "ds_or_b32 v97, v123\n\t"
    "v_and_b32 v124, v124, v116\n\t"
    "v_perm_b32 v124, v124, v124, s46\n\t"
    "ds_or_b32 v98, v124\n\t"
    "v_and_b32 v125, v125, v117\n\t"
    "v_perm_b32 v125, v125, v125, s46\n\t"
    "ds_or_b32 v99, v125\n\t"
    "s_waitcnt lgkmcnt(3)\n\t"
    "buffer_store_dwordx4 v[72:75], v66, s[28:31], s49 offen offset:256 sc1\n\t"
    "v_mov_b32 v134, 1036\n\t"
    "ds_write_b32 v134, v136\n\t"
    "s_cmp_lg_u32 %2, 0\n\t"
    "s_cbranch_scc1 L_ahsend_%=\n\t"
    "s_waitcnt vmcnt(0)\n\t"
    "buffer_store_dwordx4 v[148:151], v152, s[28:31], s47 offen offset:1024 sc1\n\t"
    "s_setprio 0\n\t"
    "v_and_b32 v132, 0x70, v152\n\t"
    "v_lshlrev_b32 v132, 3, v132\n\t"
    "s_mov_b32 s51, 1048576\n\t"
    "L_ahs2_%=:\n\t"
    "s_sleep 8\n\t"
    "buffer_load_dword v71, v132, s[28:31], s48 offen offset:1024 sc1\n\t"
    "buffer_load_dwordx4 v[148:151], v152, s[28:31], s47 offen sc1\n\t"
    "s_sub_u32 s51, s51, 1\n\t"
    "s_cmp_eq_u32 s51, 0\n\t"
    "s_cbranch_scc1 L_ahsend_%=\n\t"
    "s_waitcnt vmcnt(0)\n\t"
    "buffer_store_dwordx4 v[148:151], v152, s[28:31], s47 offen offset:1024 sc1\n\t"
    "v_cmp_gt_u32 vcc, 32, v71\n\t"
    "s_cbranch_vccnz L_ahs2_%=\n\t"
    "L_ahsend_%=:\n\t"
    "s_setprio 0\n\t"
    "s_and_b32 s46, s34, 3\n\t"
    "s_mul_i32 s49, s46, 1040\n\t"
    "s_add_u32 s49, s49, 2064\n\t"
    "s_lshl_b32 s46, s46, 3\n\t"
    "s_lshr_b32 s47, 1076697610, s46\n\t"
    "s_and_b32 s47, s47, 0xff\n\t"
    "s_lshl_b32 s48, s47, 16\n\t"
    "s_lshr_b32 s47, 756681216, s46\n\t"
    "s_and_b32 s47, s47, 0xff\n\t"
    "s_add_u32 s48, s48, s47\n\t"
    "s_lshr_b32 s47, s34, 2\n\t"
    "s_add_u32 s47, s47, 0x200\n\t"
    "s_cmp_eq_u32 s46, 0\n\t"
    "s_cselect_b32 s47, 0x100, s47\n\t"
    "s_add_u32 s48, s48, s47\n\t"
    "v_bfe_u32 v154, v147, 2, 2\n\t"
    "v_and_b32 v155, 3, v147\n\t"
    "v_mul_u32_u24 v154, 25, v154\n\t"
    "v_mad_u32_u24 v154, v155, 6, v154\n\t"
    "v_lshlrev_b32 v153, 2, v154\n\t"
    "s_and_b32 s53, s48, 0xff\n\t"
    "s_bfe_u32 s55, s48, 0x80008\n\t"
    "s_add_u32 s54, s53, s55\n\t"
    "s_add_u32 s55, s54, s55\n\t"
    "s_lshr_b32 s64, s48, 16\n\t"
    "s_mul_i32 s66, s53, 400\n\t"
    "buffer_load_dwordx3 v[128:130], v153, s[24:27], s66 offen\n\t"
    "buffer_load_dwordx4 v[132:135], v153, s[24:27], s66 offen offset:12\n\t"
    "s_mul_i32 s66, s54, 400\n\t"
    "buffer_load_dwordx3 v[136:138], v153, s[24:27], s66 offen\n\t"
    "buffer_load_dwordx4 v[140:143], v153, s[24:27], s66 offen offset:12\n\t"
    "v_cmp_eq_u32 s[62:63], 0, v155\n\t"
    "v_and_b32 v151, 48, v147\n\t"
    "v_or_b32 v155, 0x100000, v151\n\t"
    "v_cndmask_b32 v152, v155, v151, s[62:63]\n\t"
    "v_lshlrev_b32 v117, 20, v147\n\t"
    "v_add_u32 v117, 1024, v117\n\t"
    "v_mov_b32 v118, 1\n\t"
    "v_lshlrev_b32 v95, 4, v147\n\t"
    "v_add_u32 v95, 140288, v95\n\t"
    "s_call_b64 s[98:99], L_setup_%=\n\t"
    "L_await_%=:\n\t"
    "L_asp9_%=:\n\t"
    "ds_read_b32 v118, v117 offset:8\n\t"
    "ds_read_b32 v119, v117 offset:4\n\t"
    "s_waitcnt lgkmcnt(0)\n\t"
    "v_readfirstlane_b32 s46, v119\n\t"
    "s_nop 0\n\t"
    "s_cmp_ge_u32 s46, 4\n\t"
    "s_cbranch_scc1 L_ago9_%=\n\t"
    "s_sleep 1\n\t"
    "s_branch L_asp9_%=\n\t"
    "L_ago9_%=:\n\t"
    "v_readfirstlane_b32 s47, v118\n\t"
    "ds_read_b128 v[0:3], v95 offset:0\n\t"
    "ds_read_b128 v[4:7], v95 offset:1024\n\t"
    "ds_read_b128 v[8:11], v95 offset:2048\n\t"
    "ds_read_b128 v[12:15], v95 offset:3072\n\t"
    "ds_read_b128 v[16:19], v95 offset:4096\n\t"
    "ds_read_b128 v[20:23], v95 offset:5120\n\t"
    "ds_read_b128 v[24:27], v95 offset:6144\n\t"
    "ds_read_b128 v[28:31], v95 offset:7168\n\t"
    "ds_read_b128 v[32:35], v95 offset:8192\n\t"
    "ds_read_b128 v[36:39], v95 offset:9216\n\t"
    "ds_read_b128 v[40:43], v95 offset:10240\n\t"
    "ds_read_b128 v[44:47], v95 offset:11264\n\t"
    "ds_read_b128 v[48:51], v95 offset:12288\n\t"
    "ds_read_b128 v[52:55], v95 offset:13312\n\t"
    "ds_read_b128 v[56:59], v95 offset:14336\n\t"
    "ds_read_b128 v[60:63], v95 offset:15360\n\t"
    "ds_read_b128 v[64:67], v95 offset:16384\n\t"
    "ds_read_b128 v[68:71], v95 offset:17408\n\t"
    "ds_read_b128 v[72:75], v95 offset:18432\n\t"
    "ds_read_b128 v[76:79], v95 offset:19456\n\t"
    "ds_read_b128 v[80:83], v95 offset:20480\n\t"
    "ds_read_b128 v[84:87], v95 offset:21504\n\t"
    "ds_read_b128 v[88:91], v95 offset:22528\n\t"
    "s_waitcnt vmcnt(0)\n\t"
    "s_cmp_lg_u32 s34, 1\n\t"
    "s_cbranch_scc1 L_anh3_%=\n\t"
    "v_mov_b32 v119, 4\n\t"
    "ds_write_b32 v117, v119 offset:12\n\t"
    "s_lshl_b32 s65, %2, 12\n\t"
    "s_add_u32 s65, s65, 16777216\n\t"
    "s_waitcnt lgkmcnt(15)\n\t"
    "buffer_store_dwordx4 v[0:3], v127, s[28:31], s65 offen offset:0 sc1\n\t"
    "buffer_store_dwordx4 v[4:7], v127, s[28:31], s65 offen offset:1024 sc1\n\t"
    "buffer_store_dwordx4 v[8:11], v127, s[28:31], s65 offen offset:2048 sc1\n\t"
    "buffer_store_dwordx4 v[12:15], v127, s[28:31], s65 offen offset:3072 sc1\n\t"
    "L_anh3_%=:\n\t"
    "s_cmp_ge_u32 s47, 6\n\t"
    "s_cbranch_scc1 L_adok_%=\n\t"
    "L_awd_%=:\n\t"
    "L_asp10_%=:\n\t"
    "ds_read_b32 v119, v117 offset:8\n\t"
    "s_waitcnt lgkmcnt(0)\n\t"
    "v_readfirstlane_b32 s46, v119\n\t"
    "s_nop 0\n\t"
    "s_cmp_ge_u32 s46, 6\n\t"
    "s_cbranch_scc1 L_ago10_%=\n\t"
    "s_sleep 1\n\t"
    "s_branch L_asp10_%=\n\t"
    "L_ago10_%=:\n\t"
    "L_adok_%=:\n\t"
    "L_amain_%=:\n\t"
    "s_mov_b32 s68, 130\n\t"
    "v_mad_u32_u24 v128, v128, s68, v152\n\t"
    "v_and_b32 v128, -16, v128\n\t"
    "ds_read_b128 v[156:159], v128 offset:0\n\t"
    "ds_read_b128 v[160:163], v128 offset:64\n\t"
    "v_mad_u32_u24 v129, v129, s68, v151\n\t"
    "v_and_b32 v129, -16, v129\n\t"
    "ds_read_b128 v[164:167], v129 offset:0\n\t"
    "ds_read_b128 v[168:171], v129 offset:64\n\t"
    "v_mad_u32_u24 v130, v130, s68, v151\n\t"
    "v_and_b32 v130, -16, v130\n\t"
    "ds_read_b128 v[172:175], v130 offset:0\n\t"
    "ds_read_b128 v[176:179], v130 offset:64\n\t"
    "s_mul_i32 s66, s55, 400\n\t"
    "buffer_load_dwordx3 v[128:130], v153, s[24:27], s66 offen\n\t"
    "v_mad_u32_u24 v132, v132, s68, v151\n\t"
    "v_and_b32 v132, -16, v132\n\t"
    "ds_read_b128 v[180:183], v132 offset:0\n\t"
    "ds_read_b128 v[184:187], v132 offset:64\n\t"
    "v_mad_u32_u24 v133, v133, s68, v151\n\t"
    "v_and_b32 v133, -16, v133\n\t"
    "ds_read_b128 v[188:191], v133 offset:0\n\t"
    "ds_read_b128 v[192:195], v133 offset:64\n\t"
    "v_mad_u32_u24 v134, v134, s68, v151\n\t"
    "v_and_b32 v134, -16, v134\n\t"
    "ds_read_b128 v[196:199], v134 offset:0\n\t"
    "ds_read_b128 v[200:203], v134 offset:64\n\t"
    "v_mad_u32_u24 v135, v135, s68, v151\n\t"
    "v_and_b32 v135, -16, v135\n\t"
    "ds_read_b128 v[204:207], v135 offset:0\n\t"
    "ds_read_b128 v[208:211], v135 offset:64\n\t"
    "v_mov_b32 v150, 1\n\t"
    "v_lshlrev_b32 v149, 20, v147\n\t"
    "v_add_u32 v149, s49, v149\n\t"
    "v_lshlrev_b32 v146, 2, v147\n\t"
    "s_mov_b32 s52, s53\n\t"
    "s_lshl_b32 s50, %2, 14\n\t"
    "s_add_u32 s50, s50, 27262976\n\t"
    "L_apnn_%=:\n\t"
    "s_mul_i32 s69, s52, 256\n\t"
    "s_add_u32 s69, s69, s50\n\t"
    "s_waitcnt lgkmcnt(8)\n\t"
    "v_mfma_f32_16x16x32_bf16 v[92:95], v[156:159], v[156:159], 0\n\t"
    "v_fmac_f32 v145, v108, v72\n\t"
    "v_fmac_f32 v144, v109, v73\n\t"
    "ds_add_rtn_u32 v148, v149, v150\n\t"
    "v_mfma_f32_16x16x32_bf16 v[92:95], v[160:163], v[160:163], v[92:95]\n\t"
    "v_fmac_f32 v145, v110, v74\n\t"
    "v_fmac_f32 v144, v111, v75\n\t"
    "buffer_load_dwordx4 v[132:135], v153, s[24:27], s66 offen offset:12\n\t"
    "v_mfma_f32_16x16x32_bf16 v[96:99], v[164:167], v[164:167], 0\n\t"
    "v_fmac_f32 v145, v112, v76\n\t"
    "v_fmac_f32 v144, v113, v77\n\t"
    "v_mfma_f32_16x16x32_bf16 v[96:99], v[168:171], v[168:171], v[96:99]\n\t"
    "v_fmac_f32 v145, v114, v78\n\t"
    "v_fmac_f32 v144, v115, v79\n\t"
    "v_mfma_f32_16x16x32_bf16 v[100:103], v[164:167], v[172:175], 0\n\t"
    "v_fmac_f32 v145, v116, v80\n\t"
    "v_fmac_f32 v144, v117, v81\n\t"
    "v_mfma_f32_16x16x32_bf16 v[100:103], v[168:171], v[176:179], v[100:103]\n\t"
    "v_fmac_f32 v145, v118, v82\n\t"
    "v_fmac_f32 v144, v119, v83\n\t"
    "s_waitcnt vmcnt(4)\n\t"
    "s_cmp_ge_u32 s55, s64\n\t"
    "s_cbranch_scc1 L_ahk00_%=\n\t"
    "L_ahb00_%=:\n\t"
    "v_mfma_f32_16x16x32_bf16 v[104:107], v[172:175], v[172:175], 0\n\t"
    "v_mad_u32_u24 v136, v136, s68, v152\n\t"
    "v_and_b32 v136, -16, v136\n\t"
    "v_mfma_f32_16x16x32_bf16 v[104:107], v[176:179], v[176:179], v[104:107]\n\t"
    "v_add_f32 v154, v144, v145\n\t"
    "buffer_store_dword v154, v146, s[28:31], s69 offen sc1\n\t"
    "v_mul_f32 v144, v92, v84\n\t"
    "v_mfma_f32_16x16x32_bf16 v[108:111], v[156:159], v[164:167], 0\n\t"
    "v_mul_f32 v145, v96, v0\n\t"
    "v_fmac_f32 v144, v97, v1\n\t"
    "v_mfma_f32_16x16x32_bf16 v[108:111], v[160:163], v[168:171], v[108:111]\n\t"
    "v_fmac_f32 v145, v98, v2\n\t"
    "v_fmac_f32 v144, v99, v3\n\t"
    "s_waitcnt lgkmcnt(7)\n\t"
    "v_mfma_f32_16x16x32_bf16 v[112:115], v[164:167], v[180:183], 0\n\t"
    "v_fmac_f32 v145, v100, v4\n\t"
    "v_fmac_f32 v144, v101, v5\n\t"
    "v_mfma_f32_16x16x32_bf16 v[112:115], v[168:171], v[184:187], v[112:115]\n\t"
    "v_fmac_f32 v145, v102, v6\n\t"
    "v_fmac_f32 v144, v103, v7\n\t"
    "v_mfma_f32_16x16x32_bf16 v[116:119], v[172:175], v[180:183], 0\n\t"
    "v_fmac_f32 v145, v104, v8\n\t"
    "v_fmac_f32 v144, v105, v9\n\t"
    "v_mfma_f32_16x16x32_bf16 v[116:119], v[176:179], v[184:187], v[116:119]\n\t"
    "v_fmac_f32 v145, v106, v10\n\t"
    "v_fmac_f32 v144, v107, v11\n\t"
    "ds_read_b128 v[212:215], v136 offset:0\n\t"
    "ds_read_b128 v[216:219], v136 offset:64\n\t"
    "v_mfma_f32_16x16x32_bf16 v[92:95], v[180:183], v[180:183], 0\n\t"
    "v_mad_u32_u24 v137, v137, s68, v151\n\t"
    "v_and_b32 v137, -16, v137\n\t"
    "v_mfma_f32_16x16x32_bf16 v[92:95], v[184:187], v[184:187], v[92:95]\n\t"
    "v_fmac_f32 v145, v108, v85\n\t"
    "v_mad_u32_u24 v138, v138, s68, v151\n\t"
    "v_mfma_f32_16x16x32_bf16 v[96:99], v[156:159], v[172:175], 0\n\t"
    "v_fmac_f32 v144, v112, v12\n\t"
    "v_fmac_f32 v145, v113, v13\n\t"
    "v_mfma_f32_16x16x32_bf16 v[96:99], v[160:163], v[176:179], v[96:99]\n\t"
    "v_fmac_f32 v144, v114, v14\n\t"
    "v_fmac_f32 v145, v115, v15\n\t"
    "s_waitcnt lgkmcnt(7)\n\t"
    "v_mfma_f32_16x16x32_bf16 v[100:103], v[164:167], v[188:191], 0\n\t"
    "v_and_b32 v138, -16, v138\n\t"
    "v_fmac_f32 v144, v116, v16\n\t"
    "v_mfma_f32_16x16x32_bf16 v[100:103], v[168:171], v[192:195], v[100:103]\n\t"
    "v_fmac_f32 v145, v117, v17\n\t"
    "v_fmac_f32 v144, v118, v18\n\t"
    "v_mfma_f32_16x16x32_bf16 v[104:107], v[172:175], v[188:191], 0\n\t"
    "v_fmac_f32 v145, v119, v19\n\t"
    "v_fmac_f32 v144, v92, v20\n\t"
    "v_mfma_f32_16x16x32_bf16 v[104:107], v[176:179], v[192:195], v[104:107]\n\t"
    "v_fmac_f32 v145, v93, v21\n\t"
    "v_fmac_f32 v144, v94, v22\n\t"
    "s_waitcnt lgkmcnt(5)\n\t"
    "v_mfma_f32_16x16x32_bf16 v[108:111], v[164:167], v[196:199], 0\n\t"
    "v_fmac_f32 v145, v95, v23\n\t"
    "v_fmac_f32 v144, v96, v86\n\t"
    "v_mfma_f32_16x16x32_bf16 v[108:111], v[168:171], v[200:203], v[108:111]\n\t"
    "v_mfma_f32_16x16x32_bf16 v[112:115], v[156:159], v[180:183], 0\n\t"
    "v_fmac_f32 v145, v100, v24\n\t"
    "v_fmac_f32 v144, v101, v25\n\t"
    "v_mfma_f32_16x16x32_bf16 v[112:115], v[160:163], v[184:187], v[112:115]\n\t"
    "v_fmac_f32 v145, v102, v26\n\t"
    "v_fmac_f32 v144, v103, v27\n\t"
    "s_waitcnt lgkmcnt(2)\n\t"
    "v_mfma_f32_16x16x32_bf16 v[116:119], v[164:167], v[204:207], 0\n\t"
    "v_readfirstlane_b32 s60, v148\n\t"
    "v_fmac_f32 v145, v104, v28\n\t"
    "v_mfma_f32_16x16x32_bf16 v[116:119], v[168:171], v[208:211], v[116:119]\n\t"
    "v_fmac_f32 v144, v105, v29\n\t"
    "v_fmac_f32 v145, v106, v30\n\t"
    "ds_read_b128 v[164:167], v137 offset:0\n\t"
    "ds_read_b128 v[168:171], v137 offset:64\n\t"
    "v_mfma_f32_16x16x32_bf16 v[92:95], v[172:175], v[196:199], 0\n\t"
    "v_fmac_f32 v144, v107, v31\n\t"
    "v_fmac_f32 v145, v108, v32\n\t"
    "v_mfma_f32_16x16x32_bf16 v[92:95], v[176:179], v[200:203], v[92:95]\n\t"
    "v_fmac_f32 v144, v109, v33\n\t"
    "v_fmac_f32 v145, v110, v34\n\t"
    "s_waitcnt vmcnt(4)\n\t"
    "s_cmp_ge_u32 s55, s64\n\t"
    "s_cbranch_scc1 L_ahk10_%=\n\t"
    "L_ahb10_%=:\n\t"
    "v_mfma_f32_16x16x32_bf16 v[96:99], v[172:175], v[204:207], 0\n\t"
    "v_mad_u32_u24 v140, v140, s68, v151\n\t"
    "v_and_b32 v140, -16, v140\n\t"
    "v_mfma_f32_16x16x32_bf16 v[96:99], v[176:179], v[208:211], v[96:99]\n\t"
    "v_fmac_f32 v144, v111, v35\n\t"
    "v_fmac_f32 v145, v112, v87\n\t"
    "ds_read_b128 v[172:175], v138 offset:0\n\t"
    "ds_read_b128 v[176:179], v138 offset:64\n\t"
    "s_mul_i32 s66, s60, 400\n\t"
    "v_mfma_f32_16x16x32_bf16 v[100:103], v[156:159], v[188:191], 0\n\t"
    "v_fmac_f32 v144, v116, v36\n\t"
    "v_fmac_f32 v145, v117, v37\n\t"
    "v_mfma_f32_16x16x32_bf16 v[100:103], v[160:163], v[192:195], v[100:103]\n\t"
    "v_fmac_f32 v144, v118, v38\n\t"
    "v_fmac_f32 v145, v119, v39\n\t"
    "buffer_load_dwordx3 v[136:138], v153, s[24:27], s66 offen\n\t"
    "v_mfma_f32_16x16x32_bf16 v[104:107], v[180:183], v[188:191], 0\n\t"
    "v_mad_u32_u24 v141, v141, s68, v151\n\t"
    "v_and_b32 v141, -16, v141\n\t"
    "v_mfma_f32_16x16x32_bf16 v[104:107], v[184:187], v[192:195], v[104:107]\n\t"
    "v_fmac_f32 v144, v92, v40\n\t"
    "v_fmac_f32 v145, v93, v41\n\t"
    "v_mfma_f32_16x16x32_bf16 v[108:111], v[180:183], v[196:199], 0\n\t"
    "v_fmac_f32 v144, v94, v42\n\t"
    "v_fmac_f32 v145, v95, v43\n\t"
    "v_mfma_f32_16x16x32_bf16 v[108:111], v[184:187], v[200:203], v[108:111]\n\t"
    "v_fmac_f32 v144, v96, v44\n\t"
    "v_fmac_f32 v145, v97, v45\n\t"
    "v_mfma_f32_16x16x32_bf16 v[112:115], v[180:183], v[204:207], 0\n\t"
    "v_fmac_f32 v144, v98, v46\n\t"
    "v_fmac_f32 v145, v99, v47\n\t"
    "v_mfma_f32_16x16x32_bf16 v[112:115], v[184:187], v[208:211], v[112:115]\n\t"
    "v_fmac_f32 v144, v100, v88\n\t"
    "v_mad_u32_u24 v142, v142, s68, v151\n\t"
    "ds_read_b128 v[180:183], v140 offset:0\n\t"
    "ds_read_b128 v[184:187], v140 offset:64\n\t"
    "v_mfma_f32_16x16x32_bf16 v[116:119], v[156:159], v[196:199], 0\n\t"
    "v_and_b32 v142, -16, v142\n\t"
    "v_fmac_f32 v145, v104, v48\n\t"
    "v_mfma_f32_16x16x32_bf16 v[116:119], v[160:163], v[200:203], v[116:119]\n\t"
    "v_fmac_f32 v144, v105, v49\n\t"
    "v_fmac_f32 v145, v106, v50\n\t"
    "v_mfma_f32_16x16x32_bf16 v[92:95], v[188:191], v[188:191], 0\n\t"
    "v_fmac_f32 v144, v107, v51\n\t"
    "v_mad_u32_u24 v143, v143, s68, v151\n\t"
    "v_mfma_f32_16x16x32_bf16 v[92:95], v[192:195], v[192:195], v[92:95]\n\t"
    "v_and_b32 v143, -16, v143\n\t"
    "v_fmac_f32 v145, v108, v52\n\t"
    "v_mfma_f32_16x16x32_bf16 v[96:99], v[188:191], v[196:199], 0\n\t"
    "v_fmac_f32 v144, v109, v53\n\t"
    "v_fmac_f32 v145, v110, v54\n\t"
    "v_mfma_f32_16x16x32_bf16 v[96:99], v[192:195], v[200:203], v[96:99]\n\t"
    "v_fmac_f32 v144, v111, v55\n\t"
    "v_fmac_f32 v145, v112, v56\n\t"
    "v_mfma_f32_16x16x32_bf16 v[100:103], v[188:191], v[204:207], 0\n\t"
    "v_fmac_f32 v144, v113, v57\n\t"
    "v_fmac_f32 v145, v114, v58\n\t"
    "v_mfma_f32_16x16x32_bf16 v[100:103], v[192:195], v[208:211], v[100:103]\n\t"
    "v_fmac_f32 v144, v115, v59\n\t"
    "v_fmac_f32 v145, v116, v89\n\t"
    "ds_read_b128 v[188:191], v141 offset:0\n\t"
    "ds_read_b128 v[192:195], v141 offset:64\n\t"
    "v_mfma_f32_16x16x32_bf16 v[104:107], v[156:159], v[204:207], 0\n\t"
    "v_fmac_f32 v144, v92, v60\n\t"
    "v_fmac_f32 v145, v93, v61\n\t"
    "v_mfma_f32_16x16x32_bf16 v[104:107], v[160:163], v[208:211], v[104:107]\n\t"
    "v_fmac_f32 v144, v94, v62\n\t"
    "v_fmac_f32 v145, v95, v63\n\t"
    "v_mfma_f32_16x16x32_bf16 v[108:111], v[196:199], v[196:199], 0\n\t"
    "v_fmac_f32 v144, v96, v64\n\t"
    "v_fmac_f32 v145, v97, v65\n\t"
    "v_mfma_f32_16x16x32_bf16 v[108:111], v[200:203], v[200:203], v[108:111]\n\t"
    "v_fmac_f32 v144, v98, v66\n\t"
    "v_fmac_f32 v145, v99, v67\n\t"
    "v_mfma_f32_16x16x32_bf16 v[112:115], v[196:199], v[204:207], 0\n\t"
    "v_fmac_f32 v144, v100, v68\n\t"
    "v_fmac_f32 v145, v101, v69\n\t"
    "v_mfma_f32_16x16x32_bf16 v[112:115], v[200:203], v[208:211], v[112:115]\n\t"
    "v_fmac_f32 v144, v102, v70\n\t"
    "v_fmac_f32 v145, v103, v71\n\t"
    "ds_read_b128 v[196:199], v142 offset:0\n\t"
    "ds_read_b128 v[200:203], v142 offset:64\n\t"
    "v_mfma_f32_16x16x32_bf16 v[116:119], v[204:207], v[204:207], 0\n\t"
    "v_fmac_f32 v144, v104, v90\n\t"
    "v_mfma_f32_16x16x32_bf16 v[116:119], v[208:211], v[208:211], v[116:119]\n\t"
    "ds_read_b128 v[204:207], v143 offset:0\n\t"
    "ds_read_b128 v[208:211], v143 offset:64\n\t"
    "s_mov_b32 s52, s53\n\t"
    "s_mov_b32 s53, s54\n\t"
    "s_mov_b32 s54, s55\n\t"
    "s_mov_b32 s55, s60\n\t"
    "s_cmp_lt_u32 s53, s64\n\t"
    "s_cbranch_scc0 L_adone_%=\n\t"
    "s_mul_i32 s69, s52, 256\n\t"
    "s_add_u32 s69, s69, s50\n\t"
    "s_waitcnt lgkmcnt(8)\n\t"
    "v_mfma_f32_16x16x32_bf16 v[92:95], v[212:215], v[212:215], 0\n\t"
    "v_fmac_f32 v145, v108, v72\n\t"
    "v_fmac_f32 v144, v109, v73\n\t"
    "ds_add_rtn_u32 v148, v149, v150\n\t"
    "v_mfma_f32_16x16x32_bf16 v[92:95], v[216:219], v[216:219], v[92:95]\n\t"
    "v_fmac_f32 v145, v110, v74\n\t"
    "v_fmac_f32 v144, v111, v75\n\t"
    "buffer_load_dwordx4 v[140:143], v153, s[24:27], s66 offen offset:12\n\t"
    "v_mfma_f32_16x16x32_bf16 v[96:99], v[164:167], v[164:167], 0\n\t"
    "v_fmac_f32 v145, v112, v76\n\t"
    "v_fmac_f32 v144, v113, v77\n\t"
    "v_mfma_f32_16x16x32_bf16 v[96:99], v[168:171], v[168:171], v[96:99]\n\t"
    "v_fmac_f32 v145, v114, v78\n\t"
    "v_fmac_f32 v144, v115, v79\n\t"
    "v_mfma_f32_16x16x32_bf16 v[100:103], v[164:167], v[172:175], 0\n\t"
    "v_fmac_f32 v145, v116, v80\n\t"
    "v_fmac_f32 v144, v117, v81\n\t"
    "v_mfma_f32_16x16x32_bf16 v[100:103], v[168:171], v[176:179], v[100:103]\n\t"
    "v_fmac_f32 v145, v118, v82\n\t"
    "v_fmac_f32 v144, v119, v83\n\t"
    "s_waitcnt vmcnt(4)\n\t"
    "s_cmp_ge_u32 s55, s64\n\t"
    "s_cbranch_scc1 L_ahk01_%=\n\t"
    "L_ahb01_%=:\n\t"
    "v_mfma_f32_16x16x32_bf16 v[104:107], v[172:175], v[172:175], 0\n\t"
    "v_mad_u32_u24 v128, v128, s68, v152\n\t"
    "v_and_b32 v128, -16, v128\n\t"
    "v_mfma_f32_16x16x32_bf16 v[104:107], v[176:179], v[176:179], v[104:107]\n\t"
    "v_add_f32 v154, v144, v145\n\t"
    "buffer_store_dword v154, v146, s[28:31], s69 offen sc1\n\t"
    "v_mul_f32 v144, v92, v84\n\t"
    "v_mfma_f32_16x16x32_bf16 v[108:111], v[212:215], v[164:167], 0\n\t"
    "v_mul_f32 v145, v96, v0\n\t"
    "v_fmac_f32 v144, v97, v1\n\t"
    "v_mfma_f32_16x16x32_bf16 v[108:111], v[216:219], v[168:171], v[108:111]\n\t"
    "v_fmac_f32 v145, v98, v2\n\t"
    "v_fmac_f32 v144, v99, v3\n\t"
    "s_waitcnt lgkmcnt(7)\n\t"
    "v_mfma_f32_16x16x32_bf16 v[112:115], v[164:167], v[180:183], 0\n\t"
    "v_fmac_f32 v145, v100, v4\n\t"
    "v_fmac_f32 v144, v101, v5\n\t"
    "v_mfma_f32_16x16x32_bf16 v[112:115], v[168:171], v[184:187], v[112:115]\n\t"
    "v_fmac_f32 v145, v102, v6\n\t"
    "v_fmac_f32 v144, v103, v7\n\t"
    "v_mfma_f32_16x16x32_bf16 v[116:119], v[172:175], v[180:183], 0\n\t"
    "v_fmac_f32 v145, v104, v8\n\t"
    "v_fmac_f32 v144, v105, v9\n\t"
    "v_mfma_f32_16x16x32_bf16 v[116:119], v[176:179], v[184:187], v[116:119]\n\t"
    "v_fmac_f32 v145, v106, v10\n\t"
    "v_fmac_f32 v144, v107, v11\n\t"
    "ds_read_b128 v[156:159], v128 offset:0\n\t"
    "ds_read_b128 v[160:163], v128 offset:64\n\t"
    "v_mfma_f32_16x16x32_bf16 v[92:95], v[180:183], v[180:183], 0\n\t"
    "v_mad_u32_u24 v129, v129, s68, v151\n\t"
    "v_and_b32 v129, -16, v129\n\t"
    "v_mfma_f32_16x16x32_bf16 v[92:95], v[184:187], v[184:187], v[92:95]\n\t"
    "v_fmac_f32 v145, v108, v85\n\t"
    "v_mad_u32_u24 v130, v130, s68, v151\n\t"
    "v_mfma_f32_16x16x32_bf16 v[96:99], v[212:215], v[172:175], 0\n\t"
    "v_fmac_f32 v144, v112, v12\n\t"
    "v_fmac_f32 v145, v113, v13\n\t"
    "v_mfma_f32_16x16x32_bf16 v[96:99], v[216:219], v[176:179], v[96:99]\n\t"
    "v_fmac_f32 v144, v114, v14\n\t"
    "v_fmac_f32 v145, v115, v15\n\t"
    "s_waitcnt lgkmcnt(7)\n\t"
    "v_mfma_f32_16x16x32_bf16 v[100:103], v[164:167], v[188:191], 0\n\t"
    "v_and_b32 v130, -16, v130\n\t"
    "v_fmac_f32 v144, v116, v16\n\t"
    "v_mfma_f32_16x16x32_bf16 v[100:103], v[168:171], v[192:195], v[100:103]\n\t"
    "v_fmac_f32 v145, v117, v17\n\t"
    "v_fmac_f32 v144, v118, v18\n\t"
    "v_mfma_f32_16x16x32_bf16 v[104:107], v[172:175], v[188:191], 0\n\t"
    "v_fmac_f32 v145, v119, v19\n\t"
    "v_fmac_f32 v144, v92, v20\n\t"
    "v_mfma_f32_16x16x32_bf16 v[104:107], v[176:179], v[192:195], v[104:107]\n\t"
    "v_fmac_f32 v145, v93, v21\n\t"
    "v_fmac_f32 v144, v94, v22\n\t"
    "s_waitcnt lgkmcnt(5)\n\t"
    "v_mfma_f32_16x16x32_bf16 v[108:111], v[164:167], v[196:199], 0\n\t"
    "v_fmac_f32 v145, v95, v23\n\t"
    "v_fmac_f32 v144, v96, v86\n\t"
    "v_mfma_f32_16x16x32_bf16 v[108:111], v[168:171], v[200:203], v[108:111]\n\t"
    "v_mfma_f32_16x16x32_bf16 v[112:115], v[212:215], v[180:183], 0\n\t"
    "v_fmac_f32 v145, v100, v24\n\t"
    "v_fmac_f32 v144, v101, v25\n\t"
    "v_mfma_f32_16x16x32_bf16 v[112:115], v[216:219], v[184:187], v[112:115]\n\t"
    "v_fmac_f32 v145, v102, v26\n\t"
    "v_fmac_f32 v144, v103, v27\n\t"
    "s_waitcnt lgkmcnt(2)\n\t"
    "v_mfma_f32_16x16x32_bf16 v[116:119], v[164:167], v[204:207], 0\n\t"
    "v_readfirstlane_b32 s60, v148\n\t"
    "v_fmac_f32 v145, v104, v28\n\t"
    "v_mfma_f32_16x16x32_bf16 v[116:119], v[168:171], v[208:211], v[116:119]\n\t"
    "v_fmac_f32 v144, v105, v29\n\t"
    "v_fmac_f32 v145, v106, v30\n\t"
    "ds_read_b128 v[164:167], v129 offset:0\n\t"
    "ds_read_b128 v[168:171], v129 offset:64\n\t"
    "v_mfma_f32_16x16x32_bf16 v[92:95], v[172:175], v[196:199], 0\n\t"
    "v_fmac_f32 v144, v107, v31\n\t"
    "v_fmac_f32 v145, v108, v32\n\t"
    "v_mfma_f32_16x16x32_bf16 v[92:95], v[176:179], v[200:203], v[92:95]\n\t"
    "v_fmac_f32 v144, v109, v33\n\t"
    "v_fmac_f32 v145, v110, v34\n\t"
    "s_waitcnt vmcnt(4)\n\t"
    "s_cmp_ge_u32 s55, s64\n\t"
    "s_cbranch_scc1 L_ahk11_%=\n\t"
    "L_ahb11_%=:\n\t"
    "v_mfma_f32_16x16x32_bf16 v[96:99], v[172:175], v[204:207], 0\n\t"
    "v_mad_u32_u24 v132, v132, s68, v151\n\t"
    "v_and_b32 v132, -16, v132\n\t"
    "v_mfma_f32_16x16x32_bf16 v[96:99], v[176:179], v[208:211], v[96:99]\n\t"
    "v_fmac_f32 v144, v111, v35\n\t"
    "v_fmac_f32 v145, v112, v87\n\t"
    "ds_read_b128 v[172:175], v130 offset:0\n\t"
    "ds_read_b128 v[176:179], v130 offset:64\n\t"
    "s_mul_i32 s66, s60, 400\n\t"
    "v_mfma_f32_16x16x32_bf16 v[100:103], v[212:215], v[188:191], 0\n\t"
    "v_fmac_f32 v144, v116, v36\n\t"
    "v_fmac_f32 v145, v117, v37\n\t"
    "v_mfma_f32_16x16x32_bf16 v[100:103], v[216:219], v[192:195], v[100:103]\n\t"
    "v_fmac_f32 v144, v118, v38\n\t"
    "v_fmac_f32 v145, v119, v39\n\t"
    "buffer_load_dwordx3 v[128:130], v153, s[24:27], s66 offen\n\t"
    "v_mfma_f32_16x16x32_bf16 v[104:107], v[180:183], v[188:191], 0\n\t"
    "v_mad_u32_u24 v133, v133, s68, v151\n\t"
    "v_and_b32 v133, -16, v133\n\t"
    "v_mfma_f32_16x16x32_bf16 v[104:107], v[184:187], v[192:195], v[104:107]\n\t"
    "v_fmac_f32 v144, v92, v40\n\t"
    "v_fmac_f32 v145, v93, v41\n\t"
    "v_mfma_f32_16x16x32_bf16 v[108:111], v[180:183], v[196:199], 0\n\t"
    "v_fmac_f32 v144, v94, v42\n\t"
    "v_fmac_f32 v145, v95, v43\n\t"
    "v_mfma_f32_16x16x32_bf16 v[108:111], v[184:187], v[200:203], v[108:111]\n\t"
    "v_fmac_f32 v144, v96, v44\n\t"
    "v_fmac_f32 v145, v97, v45\n\t"
    "v_mfma_f32_16x16x32_bf16 v[112:115], v[180:183], v[204:207], 0\n\t"
    "v_fmac_f32 v144, v98, v46\n\t"
    "v_fmac_f32 v145, v99, v47\n\t"
    "v_mfma_f32_16x16x32_bf16 v[112:115], v[184:187], v[208:211], v[112:115]\n\t"
    "v_fmac_f32 v144, v100, v88\n\t"
    "v_mad_u32_u24 v134, v134, s68, v151\n\t"
    "ds_read_b128 v[180:183], v132 offset:0\n\t"
    "ds_read_b128 v[184:187], v132 offset:64\n\t"
    "v_mfma_f32_16x16x32_bf16 v[116:119], v[212:215], v[196:199], 0\n\t"
    "v_and_b32 v134, -16, v134\n\t"
    "v_fmac_f32 v145, v104, v48\n\t"
    "v_mfma_f32_16x16x32_bf16 v[116:119], v[216:219], v[200:203], v[116:119]\n\t"
    "v_fmac_f32 v144, v105, v49\n\t"
    "v_fmac_f32 v145, v106, v50\n\t"
    "v_mfma_f32_16x16x32_bf16 v[92:95], v[188:191], v[188:191], 0\n\t"
    "v_fmac_f32 v144, v107, v51\n\t"
    "v_mad_u32_u24 v135, v135, s68, v151\n\t"
    "v_mfma_f32_16x16x32_bf16 v[92:95], v[192:195], v[192:195], v[92:95]\n\t"
    "v_and_b32 v135, -16, v135\n\t"
    "v_fmac_f32 v145, v108, v52\n\t"
    "v_mfma_f32_16x16x32_bf16 v[96:99], v[188:191], v[196:199], 0\n\t"
    "v_fmac_f32 v144, v109, v53\n\t"
    "v_fmac_f32 v145, v110, v54\n\t"
    "v_mfma_f32_16x16x32_bf16 v[96:99], v[192:195], v[200:203], v[96:99]\n\t"
    "v_fmac_f32 v144, v111, v55\n\t"
    "v_fmac_f32 v145, v112, v56\n\t"
    "v_mfma_f32_16x16x32_bf16 v[100:103], v[188:191], v[204:207], 0\n\t"
    "v_fmac_f32 v144, v113, v57\n\t"
    "v_fmac_f32 v145, v114, v58\n\t"
    "v_mfma_f32_16x16x32_bf16 v[100:103], v[192:195], v[208:211], v[100:103]\n\t"
    "v_fmac_f32 v144, v115, v59\n\t"
    "v_fmac_f32 v145, v116, v89\n\t"
    "ds_read_b128 v[188:191], v133 offset:0\n\t"
    "ds_read_b128 v[192:195], v133 offset:64\n\t"
    "v_mfma_f32_16x16x32_bf16 v[104:107], v[212:215], v[204:207], 0\n\t"
    "v_fmac_f32 v144, v92, v60\n\t"
    "v_fmac_f32 v145, v93, v61\n\t"
    "v_mfma_f32_16x16x32_bf16 v[104:107], v[216:219], v[208:211], v[104:107]\n\t"
    "v_fmac_f32 v144, v94, v62\n\t"
    "v_fmac_f32 v145, v95, v63\n\t"
    "v_mfma_f32_16x16x32_bf16 v[108:111], v[196:199], v[196:199], 0\n\t"
    "v_fmac_f32 v144, v96, v64\n\t"
    "v_fmac_f32 v145, v97, v65\n\t"
    "v_mfma_f32_16x16x32_bf16 v[108:111], v[200:203], v[200:203], v[108:111]\n\t"
    "v_fmac_f32 v144, v98, v66\n\t"
    "v_fmac_f32 v145, v99, v67\n\t"
    "v_mfma_f32_16x16x32_bf16 v[112:115], v[196:199], v[204:207], 0\n\t"
    "v_fmac_f32 v144, v100, v68\n\t"
    "v_fmac_f32 v145, v101, v69\n\t"
    "v_mfma_f32_16x16x32_bf16 v[112:115], v[200:203], v[208:211], v[112:115]\n\t"
    "v_fmac_f32 v144, v102, v70\n\t"
    "v_fmac_f32 v145, v103, v71\n\t"
    "ds_read_b128 v[196:199], v134 offset:0\n\t"
    "ds_read_b128 v[200:203], v134 offset:64\n\t"
    "v_mfma_f32_16x16x32_bf16 v[116:119], v[204:207], v[204:207], 0\n\t"
    "v_fmac_f32 v144, v104, v90\n\t"
    "v_mfma_f32_16x16x32_bf16 v[116:119], v[208:211], v[208:211], v[116:119]\n\t"
    "ds_read_b128 v[204:207], v135 offset:0\n\t"
    "ds_read_b128 v[208:211], v135 offset:64\n\t"
    "s_mov_b32 s52, s53\n\t"
    "s_mov_b32 s53, s54\n\t"
    "s_mov_b32 s54, s55\n\t"
    "s_mov_b32 s55, s60\n\t"
    "s_cmp_lt_u32 s53, s64\n\t"
    "s_cbranch_scc1 L_apnn_%=\n\t"
    "L_adone_%=:\n\t"
    "s_mul_i32 s69, s52, 256\n\t"
    "s_add_u32 s69, s69, s50\n\t"
    "v_fmac_f32 v145, v108, v72\n\t"
    "v_fmac_f32 v144, v109, v73\n\t"
    "v_fmac_f32 v145, v110, v74\n\t"
    "v_fmac_f32 v144, v111, v75\n\t"
    "v_fmac_f32 v145, v112, v76\n\t"
    "v_fmac_f32 v144, v113, v77\n\t"
    "v_fmac_f32 v145, v114, v78\n\t"
    "v_fmac_f32 v144, v115, v79\n\t"
    "v_fmac_f32 v145, v116, v80\n\t"
    "v_fmac_f32 v144, v117, v81\n\t"
    "v_fmac_f32 v145, v118, v82\n\t"
    "v_fmac_f32 v144, v119, v83\n\t"
    "v_add_f32 v40, v144, v145\n\t"
    "L_aend_%=:\n\t"
    "s_branch L_aend2_%=\n\t"
    "L_ahk00_%=:\n\t"
    "v_mov_b32 v147, 3112\n\t"
    "s_cmp_lt_u32 s54, s64\n\t"
    "ds_read_b32 v147, v147\n\t"
    "s_cbranch_scc1 L_ahb00_%=\n\t"
    "s_waitcnt vmcnt(3)\n\t"
    "v_add_u32 v137, 0, v155\n\t"
    "v_add_u32 v138, 16, v155\n\t"
    "v_add_u32 v140, 32, v155\n\t"
    "v_add_u32 v141, 48, v155\n\t"
    "v_add_u32 v142, 64, v155\n\t"
    "v_add_u32 v143, 80, v155\n\t"
    "s_branch L_ahb00_%=\n\t"
    "L_ahk10_%=:\n\t"
    "s_waitcnt lgkmcnt(4)\n\t"
    "v_readfirstlane_b32 s65, v147\n\t"
    "s_nop 0\n\t"
    "s_cmp_eq_u32 s65, 0\n\t"
    "s_cbranch_scc1 L_ahb10_%=\n\t"
    "s_cmp_lt_u32 s62, 2\n\t"
    "s_cbranch_scc1 L_mel1_%=\n\t"
    "buffer_load_dwordx4 v[220:223], v127, s[28:31], s61 offen offset:2048 sc1\n\t"
    "buffer_load_dwordx4 v[224:227], v127, s[28:31], s61 offen offset:3072 sc1\n\t"
    "s_add_u32 s61, s61, 0x1000\n\t"
    "buffer_load_dwordx4 v[228:231], v127, s[28:31], s61 offen offset:2048 sc1\n\t"
    "buffer_load_dwordx4 v[232:235], v127, s[28:31], s61 offen offset:3072 sc1\n\t"
    "s_add_u32 s61, s61, 0x1000\n\t"
    "buffer_load_dwordx4 v[236:239], v127, s[28:31], s61 offen offset:2048 sc1\n\t"
    "buffer_load_dwordx4 v[240:243], v127, s[28:31], s61 offen offset:3072 sc1\n\t"
    "s_add_u32 s61, s61, 0x1000\n\t"
    "buffer_load_dwordx4 v[244:247], v127, s[28:31], s61 offen offset:2048 sc1\n\t"
    "buffer_load_dwordx4 v[248:251], v127, s[28:31], s61 offen offset:3072 sc1\n\t"
    "s_add_u32 s61, s61, 0x1000\n\t"
    "buffer_load_dwordx4 v[252:255], v127, s[28:31], s61 offen offset:2048 sc1\n\t"
    "s_mov_b32 s62, 1\n\t"
    "L_mel1_%=:\n\t"
    "s_cmp_lt_u32 s54, s64\n\t"
    "s_cbranch_scc1 L_ahb10_%=\n\t"
    "buffer_load_dwordx4 v[0:3], v127, s[28:31], s63 offen offset:0 sc1\n\t"
    "buffer_load_dwordx4 v[4:7], v127, s[28:31], s63 offen offset:1024 sc1\n\t"
    "buffer_load_dwordx4 v[8:11], v127, s[28:31], s63 offen offset:2048 sc1\n\t"
    "buffer_load_dwordx4 v[12:15], v127, s[28:31], s63 offen offset:3072 sc1\n\t"
    "s_add_u32 s63, s63, 0x1000\n\t"
    "s_mov_b32 s62, 0\n\t"
    "s_branch L_ahb10_%=\n\t"
    "L_ahk01_%=:\n\t"
    "v_mov_b32 v147, 3112\n\t"
    "s_cmp_lt_u32 s54, s64\n\t"
    "ds_read_b32 v147, v147\n\t"
    "s_cbranch_scc1 L_ahb01_%=\n\t"
    "s_waitcnt vmcnt(3)\n\t"
    "v_add_u32 v129, 0, v155\n\t"
    "v_add_u32 v130, 16, v155\n\t"
    "v_add_u32 v132, 32, v155\n\t"
    "v_add_u32 v133, 48, v155\n\t"
    "v_add_u32 v134, 64, v155\n\t"
    "v_add_u32 v135, 80, v155\n\t"
    "s_branch L_ahb01_%=\n\t"
    "L_ahk11_%=:\n\t"
    "s_waitcnt lgkmcnt(4)\n\t"
    "v_readfirstlane_b32 s65, v147\n\t"
    "s_nop 0\n\t"
    "s_cmp_eq_u32 s65, 0\n\t"
    "s_cbranch_scc1 L_ahb11_%=\n\t"
    "s_cmp_lt_u32 s62, 2\n\t"
    "s_cbranch_scc1 L_mel3_%=\n\t"
    "buffer_load_dwordx4 v[220:223], v127, s[28:31], s61 offen offset:2048 sc1\n\t"
    "buffer_load_dwordx4 v[224:227], v127, s[28:31], s61 offen offset:3072 sc1\n\t"
    "s_add_u32 s61, s61, 0x1000\n\t"
    "buffer_load_dwordx4 v[228:231], v127, s[28:31], s61 offen offset:2048 sc1\n\t"
    "buffer_load_dwordx4 v[232:235], v127, s[28:31], s61 offen offset:3072 sc1\n\t"
    "s_add_u32 s61, s61, 0x1000\n\t"
    "buffer_load_dwordx4 v[236:239], v127, s[28:31], s61 offen offset:2048 sc1\n\t"
    "buffer_load_dwordx4 v[240:243], v127, s[28:31], s61 offen offset:3072 sc1\n\t"
    "s_add_u32 s61, s61, 0x1000\n\t"
    "buffer_load_dwordx4 v[244:247], v127, s[28:31], s61 offen offset:2048 sc1\n\t"
    "buffer_load_dwordx4 v[248:251], v127, s[28:31], s61 offen offset:3072 sc1\n\t"
    "s_add_u32 s61, s61, 0x1000\n\t"
    "buffer_load_dwordx4 v[252:255], v127, s[28:31], s61 offen offset:2048 sc1\n\t"
    "s_mov_b32 s62, 1\n\t"
    "L_mel3_%=:\n\t"
    "s_cmp_lt_u32 s54, s64\n\t"
    "s_cbranch_scc1 L_ahb11_%=\n\t"
    "buffer_load_dwordx4 v[0:3], v127, s[28:31], s63 offen offset:0 sc1\n\t"
    "buffer_load_dwordx4 v[4:7], v127, s[28:31], s63 offen offset:1024 sc1\n\t"
    "buffer_load_dwordx4 v[8:11], v127, s[28:31], s63 offen offset:2048 sc1\n\t"
    "buffer_load_dwordx4 v[12:15], v127, s[28:31], s63 offen offset:3072 sc1\n\t"
    "s_add_u32 s63, s63, 0x1000\n\t"
    "s_mov_b32 s62, 0\n\t"
    "s_branch L_ahb11_%=\n\t"
    "L_aend2_%=:\n\t"
    "s_setprio 0\n\t"
    "v_lshlrev_b32 v113, 16, v127\n\t"
    "v_add_u32 v113, 3108, v113\n\t"
    "s_cmp_eq_u32 s62, 0\n\t"
    "s_cbranch_scc1 L_mpf_%=\n\t"
    "L_ml0_%=:\n\t"
    "ds_read_b32 v114, v113 offset:4\n\t"
    "s_waitcnt lgkmcnt(0)\n\t"
    "v_readfirstlane_b32 s65, v114\n\t"
    "s_nop 0\n\t"
    "s_cmp_lg_u32 s65, 0\n\t"
    "s_cbranch_scc1 L_mm0_%=\n\t"
    "s_sleep 2\n\t"
    "s_branch L_ml0_%=\n\t"
    "L_mm0_%=:\n\t"
    "s_cmp_lt_u32 s62, 2\n\t"
    "s_cbranch_scc1 L_mel5_%=\n\t"
    "buffer_load_dwordx4 v[220:223], v127, s[28:31], s61 offen offset:2048 sc1\n\t"
    "buffer_load_dwordx4 v[224:227], v127, s[28:31], s61 offen offset:3072 sc1\n\t"
    "s_add_u32 s61, s61, 0x1000\n\t"
    "buffer_load_dwordx4 v[228:231], v127, s[28:31], s61 offen offset:2048 sc1\n\t"
    "buffer_load_dwordx4 v[232:235], v127, s[28:31], s61 offen offset:3072 sc1\n\t"
    "s_add_u32 s61, s61, 0x1000\n\t"
    "buffer_load_dwordx4 v[236:239], v127, s[28:31], s61 offen offset:2048 sc1\n\t"
    "buffer_load_dwordx4 v[240:243], v127, s[28:31], s61 offen offset:3072 sc1\n\t"
    "s_add_u32 s61, s61, 0x1000\n\t"
    "buffer_load_dwordx4 v[244:247], v127, s[28:31], s61 offen offset:2048 sc1\n\t"
    "buffer_load_dwordx4 v[248:251], v127, s[28:31], s61 offen offset:3072 sc1\n\t"
    "s_add_u32 s61, s61, 0x1000\n\t"
    "buffer_load_dwordx4 v[252:255], v127, s[28:31], s61 offen offset:2048 sc1\n\t"
    "s_mov_b32 s62, 1\n\t"
    "L_mel5_%=:\n\t"
    "buffer_load_dwordx4 v[0:3], v127, s[28:31], s63 offen offset:0 sc1\n\t"
    "buffer_load_dwordx4 v[4:7], v127, s[28:31], s63 offen offset:1024 sc1\n\t"
    "buffer_load_dwordx4 v[8:11], v127, s[28:31], s63 offen offset:2048 sc1\n\t"
    "buffer_load_dwordx4 v[12:15], v127, s[28:31], s63 offen offset:3072 sc1\n\t"
    "s_add_u32 s63, s63, 0x1000\n\t"
    "s_mov_b32 s62, 0\n\t"
    "L_mpf_%=:\n\t"
    "v_lshrrev_b32 v105, 4, v127\n\t"
    "v_and_b32 v105, 48, v105\n\t"
    "s_movk_i32 s65, 130\n\t"
    "v_mad_u32_u24 v104, v155, s65, v105\n\t"
    "v_add_u32 v105, 14170, v104\n\t"
    "v_and_b32 v104, -16, v104\n\t"
    "v_and_b32 v105, -16, v105\n\t"
    "buffer_load_dwordx4 v[128:131], v127, s[28:31], s61 offen offset:3072 sc1\n\t"
    "s_add_u32 s61, s61, 0x1000\n\t"
    "buffer_load_dwordx4 v[132:135], v127, s[28:31], s61 offen offset:2048 sc1\n\t"
    "ds_read_b128 v[156:159], v104 offset:12480\n\t"
    "ds_read_b128 v[160:163], v104 offset:12544\n\t"
    "buffer_load_dwordx4 v[136:139], v127, s[28:31], s61 offen offset:3072 sc1\n\t"
    "s_add_u32 s61, s61, 0x1000\n\t"
    "buffer_load_dwordx4 v[140:143], v127, s[28:31], s61 offen offset:2048 sc1\n\t"
    "ds_read_b128 v[212:215], v105 offset:0\n\t"
    "ds_read_b128 v[216:219], v105 offset:64\n\t"
    "buffer_load_dwordx4 v[144:147], v127, s[28:31], s61 offen offset:3072 sc1\n\t"
    "s_add_u32 s61, s61, 0x1000\n\t"
    "buffer_load_dwordx4 v[148:151], v127, s[28:31], s61 offen offset:2048 sc1\n\t"
    "buffer_load_dwordx4 v[152:155], v127, s[28:31], s61 offen offset:3072 sc1\n\t"
    "s_add_u32 s61, s61, 0x1000\n\t"
    "v_mov_b32 v114, 1\n\t"
    "ds_add_u32 v113, v114\n\t"
    "s_waitcnt lgkmcnt(5)\n\t"
    "s_waitcnt vmcnt(7)\n\t"
    "v_mfma_f32_16x16x32_bf16 v[16:19], v[0:3], v[164:167], 0\n\t"
    "v_mfma_f32_16x16x32_bf16 v[16:19], v[4:7], v[168:171], v[16:19]\n\t"
    "v_mfma_f32_16x16x32_bf16 v[16:19], v[8:11], v[220:223], v[16:19]\n\t"
    "v_mfma_f32_16x16x32_bf16 v[16:19], v[12:15], v[224:227], v[16:19]\n\t"
    "v_mfma_f32_16x16x32_bf16 v[20:23], v[0:3], v[172:175], 0\n\t"
    "v_mfma_f32_16x16x32_bf16 v[20:23], v[4:7], v[176:179], v[20:23]\n\t"
    "v_mfma_f32_16x16x32_bf16 v[20:23], v[8:11], v[228:231], v[20:23]\n\t"
    "v_mfma_f32_16x16x32_bf16 v[20:23], v[12:15], v[232:235], v[20:23]\n\t"
    "v_mfma_f32_16x16x32_bf16 v[80:83], v[0:3], v[180:183], 0\n\t"
    "v_mfma_f32_16x16x32_bf16 v[80:83], v[4:7], v[184:187], v[80:83]\n\t"
    "v_mfma_f32_16x16x32_bf16 v[80:83], v[8:11], v[236:239], v[80:83]\n\t"
    "v_mfma_f32_16x16x32_bf16 v[80:83], v[12:15], v[240:243], v[80:83]\n\t"
    "v_cvt_pk_bf16_f32 v24, v16, v17\n\t"
    "v_cvt_pk_bf16_f32 v25, v18, v19\n\t"
    "v_mfma_f32_16x16x32_bf16 v[16:19], v[0:3], v[188:191], 0\n\t"
    "v_mfma_f32_16x16x32_bf16 v[16:19], v[4:7], v[192:195], v[16:19]\n\t"
    "v_mfma_f32_16x16x32_bf16 v[16:19], v[8:11], v[244:247], v[16:19]\n\t"
    "v_mfma_f32_16x16x32_bf16 v[16:19], v[12:15], v[248:251], v[16:19]\n\t"
    "v_cvt_pk_bf16_f32 v26, v20, v21\n\t"
    "v_cvt_pk_bf16_f32 v27, v22, v23\n\t"
    "s_waitcnt vmcnt(6)\n\t"
    "v_mfma_f32_16x16x32_bf16 v[20:23], v[0:3], v[196:199], 0\n\t"
    "v_mfma_f32_16x16x32_bf16 v[20:23], v[4:7], v[200:203], v[20:23]\n\t"
    "v_mfma_f32_16x16x32_bf16 v[20:23], v[8:11], v[252:255], v[20:23]\n\t"
    "v_mfma_f32_16x16x32_bf16 v[20:23], v[12:15], v[128:131], v[20:23]\n\t"
    "v_cvt_pk_bf16_f32 v28, v80, v81\n\t"
    "v_cvt_pk_bf16_f32 v29, v82, v83\n\t"
    "s_waitcnt lgkmcnt(0)\n\t"
    "s_waitcnt vmcnt(4)\n\t"
    "v_mfma_f32_16x16x32_bf16 v[80:83], v[0:3], v[204:207], 0\n\t"
    "v_mfma_f32_16x16x32_bf16 v[80:83], v[4:7], v[208:211], v[80:83]\n\t"
    "v_mfma_f32_16x16x32_bf16 v[80:83], v[8:11], v[132:135], v[80:83]\n\t"
    "v_mfma_f32_16x16x32_bf16 v[80:83], v[12:15], v[136:139], v[80:83]\n\t"
    "v_cvt_pk_bf16_f32 v30, v16, v17\n\t"
    "v_cvt_pk_bf16_f32 v31, v18, v19\n\t"
    "s_waitcnt vmcnt(2)\n\t"
    "v_mfma_f32_16x16x32_bf16 v[16:19], v[0:3], v[156:159], 0\n\t"
    "buffer_load_dwordx4 v[100:103], v127, s[28:31], s84 offen offset:0 sc1\n\t"
    "s_add_u32 s84, s84, s97\n\t"
    "v_mfma_f32_16x16x32_bf16 v[16:19], v[4:7], v[160:163], v[16:19]\n\t"
    "buffer_load_dwordx4 v[104:107], v127, s[28:31], s85 offen offset:0 sc1\n\t"
    "s_add_u32 s85, s85, s97\n\t"
    "v_mfma_f32_16x16x32_bf16 v[16:19], v[8:11], v[140:143], v[16:19]\n\t"
    "v_mfma_f32_16x16x32_bf16 v[16:19], v[12:15], v[144:147], v[16:19]\n\t"
    "v_cvt_pk_bf16_f32 v32, v20, v21\n\t"
    "v_cvt_pk_bf16_f32 v33, v22, v23\n\t"
    "s_waitcnt vmcnt(2)\n\t"
    "v_mfma_f32_16x16x32_bf16 v[20:23], v[0:3], v[212:215], 0\n\t"
    "buffer_load_dwordx4 v[108:111], v127, s[28:31], s86 offen offset:0 sc1\n\t"
    "s_add_u32 s86, s86, s97\n\t"
    "v_mfma_f32_16x16x32_bf16 v[20:23], v[4:7], v[216:219], v[20:23]\n\t"
    "buffer_load_dwordx4 v[112:115], v127, s[28:31], s87 offen offset:0 sc1\n\t"
    "s_add_u32 s87, s87, s97\n\t"
    "v_mfma_f32_16x16x32_bf16 v[20:23], v[8:11], v[148:151], v[20:23]\n\t"
    "v_mfma_f32_16x16x32_bf16 v[20:23], v[12:15], v[152:155], v[20:23]\n\t"
    "v_cvt_pk_bf16_f32 v34, v80, v81\n\t"
    "v_cvt_pk_bf16_f32 v35, v82, v83\n\t"
    "v_cvt_pk_bf16_f32 v36, v16, v17\n\t"
    "v_cvt_pk_bf16_f32 v37, v18, v19\n\t"
    "s_nop 3\n\t"
    "v_cvt_pk_bf16_f32 v38, v20, v21\n\t"
    "v_cvt_pk_bf16_f32 v39, v22, v23\n\t"
    "v_lshrrev_b32 v41, 2, v127\n\t"
    "s_and_b32 s65, %2, 8\n\t"
    "s_lshl_b32 s65, s65, 10\n\t"
    "s_add_u32 s63, s63, s65\n\t"
    "buffer_store_dword v40, v41, s[28:31], s69 offen sc1\n\t"
    "v_mov_b32 v82, 3108\n\t"
    "s_cmp_eq_u32 s34, 0\n\t"
    "s_cselect_b32 s47, 0x1ff, 0\n\t"
    "L_my_%=:\n\t"
    "ds_read_b32 v81, v82\n\t"
    "s_waitcnt lgkmcnt(0)\n\t"
    "v_cmp_gt_u32 vcc, 8, v81\n\t"
    "s_cbranch_vccz L_mz_%=\n\t"
    "s_sleep 2\n\t"
    "s_branch L_my_%=\n\t"
    "L_mz_%=:\n\t"
    "ds_write_b64 v125, v[24:25] offset:0\n\t"
    "v_mov_b64 v[0:1], 0\n\t"
    "ds_write_b64 v125, v[26:27] offset:128\n\t"
    "v_mov_b64 v[2:3], 0\n\t"
    "ds_write_b64 v125, v[28:29] offset:256\n\t"
    "v_mov_b64 v[4:5], 0\n\t"
    "ds_write_b64 v125, v[30:31] offset:384\n\t"
    "v_mov_b64 v[6:7], 0\n\t"
    "ds_write_b64 v125, v[32:33] offset:512\n\t"
    "ds_write_b64 v125, v[34:35] offset:640\n\t"
    "ds_write_b64 v125, v[36:37] offset:768\n\t"
    "ds_write_b64 v125, v[38:39] offset:872\n\t"
    "v_add_u32 v125, s93, v125\n\t"
    "s_sub_u32 s93, 0, s93\n\t"
    "s_waitcnt lgkmcnt(0)\n\t"
    "s_barrier\n\t"
    "s_memrealtime s[44:45]\n\t"
    "s_waitcnt lgkmcnt(0)\n\t"
    "s_waitcnt vmcnt(4)\n\t"
    "v_add_u32_sdwa v126, s92, v100 dst_sel:DWORD dst_unused:UNUSED_PAD src0_sel:DWORD src1_sel:WORD_1\n\t"
    "v_add_u32_sdwa v100, s92, v100 dst_sel:DWORD dst_unused:UNUSED_PAD src0_sel:DWORD src1_sel:WORD_0\n\t"
    "ds_read_b64 v[84:85], v100 offset:0\n\t"
    "ds_read_b64 v[86:87], v126 offset:0\n\t"
    "v_add_u32_sdwa v90, s92, v101 dst_sel:DWORD dst_unused:UNUSED_PAD src0_sel:DWORD src1_sel:WORD_1\n\t"
    "v_add_u32_sdwa v101, s92, v101 dst_sel:DWORD dst_unused:UNUSED_PAD src0_sel:DWORD src1_sel:WORD_0\n\t"
    "ds_read_b64 v[88:89], v101 offset:0\n\t"
    "ds_read_b64 v[90:91], v90 offset:0\n\t"
    "v_add_u32 v63, s96, v127\n\t"
    "s_add_u32 s96, s96, 0x1000\n\t"
    "s_and_b32 s96, s96, 0x3000\n\t"
    "v_add_u32 v63, 128000, v63\n\t"
    "ds_read_b128 v[48:51], v63 offset:0\n\t"
    "ds_read_b128 v[52:55], v63 offset:1024\n\t"
    "ds_read_b128 v[56:59], v63 offset:2048\n\t"
    "ds_read_b128 v[60:63], v63 offset:3072\n\t"
    "v_add_u32_sdwa v126, s92, v102 dst_sel:DWORD dst_unused:UNUSED_PAD src0_sel:DWORD src1_sel:WORD_1\n\t"
    "v_add_u32_sdwa v102, s92, v102 dst_sel:DWORD dst_unused:UNUSED_PAD src0_sel:DWORD src1_sel:WORD_0\n\t"
    "ds_read_b64 v[92:93], v102 offset:0\n\t"
    "ds_read_b64 v[94:95], v126 offset:0\n\t"
    "v_add_u32_sdwa v98, s92, v103 dst_sel:DWORD dst_unused:UNUSED_PAD src0_sel:DWORD src1_sel:WORD_1\n\t"
    "v_add_u32_sdwa v103, s92, v103 dst_sel:DWORD dst_unused:UNUSED_PAD src0_sel:DWORD src1_sel:WORD_0\n\t"
    "ds_read_b64 v[96:97], v103 offset:0\n\t"
    "ds_read_b64 v[98:99], v98 offset:0\n\t"
    "buffer_load_dwordx4 v[100:103], v127, s[28:31], s88 offen offset:0 sc1\n\t"
    "s_add_u32 s88, s88, s97\n\t"
    "s_waitcnt vmcnt(4)\n\t"
    "v_add_u32_sdwa v126, s92, v104 dst_sel:DWORD dst_unused:UNUSED_PAD src0_sel:DWORD src1_sel:WORD_1\n\t"
    "v_add_u32_sdwa v104, s92, v104 dst_sel:DWORD dst_unused:UNUSED_PAD src0_sel:DWORD src1_sel:WORD_0\n\t"
    "v_mov_b64 v[8:9], 0\n\t"
    "v_mov_b64 v[10:11], 0\n\t"
    "v_mov_b64 v[12:13], 0\n\t"
    "v_mov_b64 v[14:15], 0\n\t"
    "v_mov_b64 v[16:17], 0\n\t"
    "v_mov_b64 v[18:19], 0\n\t"
    "s_cmp_lt_u32 s34, 4\n\t"
    "s_cbranch_scc1 L_spp0_%=\n\t"
    "s_setprio 1\n\t"
    "L_spp0_%=:\n\t"
    "s_waitcnt lgkmcnt(8)\n\t"
    "v_smfmac_f32_16x16x64_bf16 v[0:3], v[120:123], v[84:91], v124\n\t"
    "ds_read_b64 v[84:85], v104 offset:0\n\t"
    "s_waitcnt lgkmcnt(5)\n\t"
    "v_mfma_f32_16x16x32_bf16 v[116:119], v[48:51], v[164:167], 0\n\t"
    "ds_read_b64 v[86:87], v126 offset:0\n\t"
    "v_mfma_f32_16x16x32_bf16 v[116:119], v[52:55], v[168:171], v[116:119]\n\t"
    "v_add_u32_sdwa v90, s92, v105 dst_sel:DWORD dst_unused:UNUSED_PAD src0_sel:DWORD src1_sel:WORD_1\n\t"
    "v_add_u32_sdwa v105, s92, v105 dst_sel:DWORD dst_unused:UNUSED_PAD src0_sel:DWORD src1_sel:WORD_0\n\t"
    "ds_read_b64 v[88:89], v105 offset:0\n\t"
    "v_mfma_f32_16x16x32_bf16 v[116:119], v[56:59], v[220:223], v[116:119]\n\t"
    "ds_read_b64 v[90:91], v90 offset:0\n\t"
    "v_mfma_f32_16x16x32_bf16 v[116:119], v[60:63], v[224:227], v[116:119]\n\t"
    "v_add_u32_sdwa v126, s92, v106 dst_sel:DWORD dst_unused:UNUSED_PAD src0_sel:DWORD src1_sel:WORD_1\n\t"
    "v_add_u32_sdwa v106, s92, v106 dst_sel:DWORD dst_unused:UNUSED_PAD src0_sel:DWORD src1_sel:WORD_0\n\t"
    "v_mfma_f32_16x16x32_bf16 v[80:83], v[48:51], v[172:175], 0\n\t"
    "v_mov_b64 v[20:21], 0\n\t"
    "v_mov_b64 v[22:23], 0\n\t"
    "v_mov_b64 v[24:25], 0\n\t"
    "v_mov_b64 v[26:27], 0\n\t"
    "v_mov_b64 v[28:29], 0\n\t"
    "v_mov_b64 v[30:31], 0\n\t"
    "s_waitcnt vmcnt(3) lgkmcnt(4)\n\t"
    "v_smfmac_f32_16x16x64_bf16 v[4:7], v[120:123], v[92:99], v124\n\t"
    "ds_read_b64 v[92:93], v106 offset:0\n\t"
    "v_mfma_f32_16x16x32_bf16 v[80:83], v[52:55], v[176:179], v[80:83]\n\t"
    "ds_read_b64 v[94:95], v126 offset:0\n\t"
    "v_mfma_f32_16x16x32_bf16 v[80:83], v[56:59], v[228:231], v[80:83]\n\t"
    "v_add_u32_sdwa v98, s92, v107 dst_sel:DWORD dst_unused:UNUSED_PAD src0_sel:DWORD src1_sel:WORD_1\n\t"
    "v_add_u32_sdwa v107, s92, v107 dst_sel:DWORD dst_unused:UNUSED_PAD src0_sel:DWORD src1_sel:WORD_0\n\t"
    "ds_read_b64 v[96:97], v107 offset:0\n\t"
    "v_mfma_f32_16x16x32_bf16 v[80:83], v[60:63], v[232:235], v[80:83]\n\t"
    "ds_read_b64 v[98:99], v98 offset:0\n\t"
    "v_cvt_pk_bf16_f32 v116, v116, v117\n\t"
    "v_cvt_pk_bf16_f32 v117, v118, v119\n\t"
    "ds_write_b64 v125, v[116:117] offset:0\n\t"
    "v_mfma_f32_16x16x32_bf16 v[116:119], v[48:51], v[180:183], 0\n\t"
    "buffer_load_dwordx4 v[104:107], v127, s[28:31], s89 offen offset:0 sc1\n\t"
    "s_add_u32 s89, s89, s97\n\t"
    "v_add_u32_sdwa v126, s92, v108 dst_sel:DWORD dst_unused:UNUSED_PAD src0_sel:DWORD src1_sel:WORD_1\n\t"
    "v_add_u32_sdwa v108, s92, v108 dst_sel:DWORD dst_unused:UNUSED_PAD src0_sel:DWORD src1_sel:WORD_0\n\t"
    "v_mfma_f32_16x16x32_bf16 v[116:119], v[52:55], v[184:187], v[116:119]\n\t"
    "v_mov_b64 v[32:33], 0\n\t"
    "v_mov_b64 v[34:35], 0\n\t"
    "v_mov_b64 v[36:37], 0\n\t"
    "v_mov_b64 v[38:39], 0\n\t"
    "v_mov_b64 v[40:41], 0\n\t"
    "v_mov_b64 v[42:43], 0\n\t"
    "v_mov_b64 v[44:45], 0\n\t"
    "v_mov_b64 v[46:47], 0\n\t"
    "s_waitcnt lgkmcnt(5)\n\t"
    "v_smfmac_f32_16x16x64_bf16 v[8:11], v[120:123], v[84:91], v124\n\t"
    "ds_read_b64 v[84:85], v108 offset:0\n\t"
    "v_mfma_f32_16x16x32_bf16 v[116:119], v[56:59], v[236:239], v[116:119]\n\t"
    "ds_read_b64 v[86:87], v126 offset:0\n\t"
    "v_mfma_f32_16x16x32_bf16 v[116:119], v[60:63], v[240:243], v[116:119]\n\t"
    "v_add_u32_sdwa v90, s92, v109 dst_sel:DWORD dst_unused:UNUSED_PAD src0_sel:DWORD src1_sel:WORD_1\n\t"
    "v_add_u32_sdwa v109, s92, v109 dst_sel:DWORD dst_unused:UNUSED_PAD src0_sel:DWORD src1_sel:WORD_0\n\t"
    "ds_read_b64 v[88:89], v109 offset:0\n\t"
    "v_cvt_pk_bf16_f32 v80, v80, v81\n\t"
    "v_cvt_pk_bf16_f32 v81, v82, v83\n\t"
    "ds_write_b64 v125, v[80:81] offset:128\n\t"
    "v_mfma_f32_16x16x32_bf16 v[80:83], v[48:51], v[188:191], 0\n\t"
    "ds_read_b64 v[90:91], v90 offset:0\n\t"
    "v_mfma_f32_16x16x32_bf16 v[80:83], v[52:55], v[192:195], v[80:83]\n\t"
    "v_add_u32_sdwa v126, s92, v110 dst_sel:DWORD dst_unused:UNUSED_PAD src0_sel:DWORD src1_sel:WORD_1\n\t"
    "v_add_u32_sdwa v110, s92, v110 dst_sel:DWORD dst_unused:UNUSED_PAD src0_sel:DWORD src1_sel:WORD_0\n\t"
    "v_mfma_f32_16x16x32_bf16 v[80:83], v[56:59], v[244:247], v[80:83]\n\t"
    "s_waitcnt vmcnt(3) lgkmcnt(6)\n\t"
    "v_smfmac_f32_16x16x64_bf16 v[12:15], v[120:123], v[92:99], v124\n\t"
    "ds_read_b64 v[92:93], v110 offset:0\n\t"
    "v_mfma_f32_16x16x32_bf16 v[80:83], v[60:63], v[248:251], v[80:83]\n\t"
    "ds_read_b64 v[94:95], v126 offset:0\n\t"
    "v_cvt_pk_bf16_f32 v116, v116, v117\n\t"
    "v_cvt_pk_bf16_f32 v117, v118, v119\n\t"
    "ds_write_b64 v125, v[116:117] offset:256\n\t"
    "v_mfma_f32_16x16x32_bf16 v[116:119], v[48:51], v[196:199], 0\n\t"
    "v_add_u32_sdwa v98, s92, v111 dst_sel:DWORD dst_unused:UNUSED_PAD src0_sel:DWORD src1_sel:WORD_1\n\t"
    "v_add_u32_sdwa v111, s92, v111 dst_sel:DWORD dst_unused:UNUSED_PAD src0_sel:DWORD src1_sel:WORD_0\n\t"
    "ds_read_b64 v[96:97], v111 offset:0\n\t"
    "v_mfma_f32_16x16x32_bf16 v[116:119], v[52:55], v[200:203], v[116:119]\n\t"
    "ds_read_b64 v[98:99], v98 offset:0\n\t"
    "v_mfma_f32_16x16x32_bf16 v[116:119], v[56:59], v[252:255], v[116:119]\n\t"
    "buffer_load_dwordx4 v[108:111], v127, s[28:31], s90 offen offset:0 sc1\n\t"
    "s_add_u32 s90, s90, s97\n\t"
    "v_add_u32_sdwa v126, s92, v112 dst_sel:DWORD dst_unused:UNUSED_PAD src0_sel:DWORD src1_sel:WORD_1\n\t"
    "v_add_u32_sdwa v112, s92, v112 dst_sel:DWORD dst_unused:UNUSED_PAD src0_sel:DWORD src1_sel:WORD_0\n\t"
    "v_mfma_f32_16x16x32_bf16 v[116:119], v[60:63], v[128:131], v[116:119]\n\t"
    "v_cvt_pk_bf16_f32 v0, v0, v1\n\t"
    "v_cvt_pk_bf16_f32 v1, v2, v3\n\t"
    "v_cvt_pk_bf16_f32 v80, v80, v81\n\t"
    "v_cvt_pk_bf16_f32 v81, v82, v83\n\t"
    "ds_write_b64 v125, v[80:81] offset:384\n\t"
    "v_add_u32 v79, s96, v127\n\t"
    "s_add_u32 s96, s96, 0x1000\n\t"
    "s_and_b32 s96, s96, 0x3000\n\t"
    "v_add_u32 v79, 128000, v79\n\t"
    "ds_read_b128 v[64:67], v79 offset:0\n\t"
    "ds_read_b128 v[68:71], v79 offset:1024\n\t"
    "ds_read_b128 v[72:75], v79 offset:2048\n\t"
    "ds_read_b128 v[76:79], v79 offset:3072\n\t"
    "s_waitcnt lgkmcnt(10)\n\t"
    "v_smfmac_f32_16x16x64_bf16 v[16:19], v[120:123], v[84:91], v124\n\t"
    "ds_read_b64 v[84:85], v112 offset:0\n\t"
    "v_mfma_f32_16x16x32_bf16 v[80:83], v[48:51], v[204:207], 0\n\t"
    "ds_read_b64 v[86:87], v126 offset:0\n\t"
    "v_mfma_f32_16x16x32_bf16 v[80:83], v[52:55], v[208:211], v[80:83]\n\t"
    "v_add_u32_sdwa v90, s92, v113 dst_sel:DWORD dst_unused:UNUSED_PAD src0_sel:DWORD src1_sel:WORD_1\n\t"
    "v_add_u32_sdwa v113, s92, v113 dst_sel:DWORD dst_unused:UNUSED_PAD src0_sel:DWORD src1_sel:WORD_0\n\t"
    "ds_read_b64 v[88:89], v113 offset:0\n\t"
    "v_mfma_f32_16x16x32_bf16 v[80:83], v[56:59], v[132:135], v[80:83]\n\t"
    "ds_read_b64 v[90:91], v90 offset:0\n\t"
    "v_mfma_f32_16x16x32_bf16 v[80:83], v[60:63], v[136:139], v[80:83]\n\t"
    "v_add_u32_sdwa v126, s92, v114 dst_sel:DWORD dst_unused:UNUSED_PAD src0_sel:DWORD src1_sel:WORD_1\n\t"
    "v_add_u32_sdwa v114, s92, v114 dst_sel:DWORD dst_unused:UNUSED_PAD src0_sel:DWORD src1_sel:WORD_0\n\t"
    "v_cvt_pk_bf16_f32 v116, v116, v117\n\t"
    "v_cvt_pk_bf16_f32 v117, v118, v119\n\t"
    "ds_write_b64 v125, v[116:117] offset:512\n\t"
    "v_mfma_f32_16x16x32_bf16 v[116:119], v[48:51], v[156:159], 0\n\t"
    "v_cvt_pk_bf16_f32 v2, v4, v5\n\t"
    "v_cvt_pk_bf16_f32 v3, v6, v7\n\t"
    "v_mov_b64 v[4:5], 0\n\t"
    "v_mov_b64 v[6:7], 0\n\t"
    "s_cmp_lt_u32 s34, 4\n\t"
    "s_cbranch_scc1 L_spp5_%=\n\t"
    "s_setprio 0\n\t"
    "L_spp5_%=:\n\t"
    "s_waitcnt vmcnt(2) lgkmcnt(10)\n\t"
    "v_smfmac_f32_16x16x64_bf16 v[20:23], v[120:123], v[92:99], v124\n\t"
    "ds_read_b64 v[92:93], v114 offset:0\n\t"
    "v_mfma_f32_16x16x32_bf16 v[116:119], v[52:55], v[160:163], v[116:119]\n\t"
    "ds_read_b64 v[94:95], v126 offset:0\n\t"
    "v_mfma_f32_16x16x32_bf16 v[116:119], v[56:59], v[140:143], v[116:119]\n\t"
    "v_add_u32_sdwa v98, s92, v115 dst_sel:DWORD dst_unused:UNUSED_PAD src0_sel:DWORD src1_sel:WORD_1\n\t"
    "v_add_u32_sdwa v115, s92, v115 dst_sel:DWORD dst_unused:UNUSED_PAD src0_sel:DWORD src1_sel:WORD_0\n\t"
    "ds_read_b64 v[96:97], v115 offset:0\n\t"
    "v_mfma_f32_16x16x32_bf16 v[116:119], v[60:63], v[144:147], v[116:119]\n\t"
    "ds_read_b64 v[98:99], v98 offset:0\n\t"
    "v_cvt_pk_bf16_f32 v80, v80, v81\n\t"
    "v_cvt_pk_bf16_f32 v81, v82, v83\n\t"
    "ds_write_b64 v125, v[80:81] offset:640\n\t"
    "v_mfma_f32_16x16x32_bf16 v[80:83], v[48:51], v[212:215], 0\n\t"
    "buffer_load_dwordx4 v[112:115], v127, s[28:31], s91 offen offset:0 sc1\n\t"
    "s_add_u32 s91, s91, s97\n\t"
    "v_add_u32_sdwa v126, s92, v100 dst_sel:DWORD dst_unused:UNUSED_PAD src0_sel:DWORD src1_sel:WORD_1\n\t"
    "v_add_u32_sdwa v100, s92, v100 dst_sel:DWORD dst_unused:UNUSED_PAD src0_sel:DWORD src1_sel:WORD_0\n\t"
    "v_mfma_f32_16x16x32_bf16 v[80:83], v[52:55], v[216:219], v[80:83]\n\t"
    "v_cvt_pk_bf16_f32 v8, v8, v9\n\t"
    "v_cvt_pk_bf16_f32 v9, v10, v11\n\t"
    "s_waitcnt lgkmcnt(6)\n\t"
    "v_smfmac_f32_16x16x64_bf16 v[24:27], v[120:123], v[84:91], v124\n\t"
    "ds_read_b64 v[84:85], v100 offset:0\n\t"
    "v_mfma_f32_16x16x32_bf16 v[80:83], v[56:59], v[148:151], v[80:83]\n\t"
    "ds_read_b64 v[86:87], v126 offset:0\n\t"
    "v_mfma_f32_16x16x32_bf16 v[80:83], v[60:63], v[152:155], v[80:83]\n\t"
    "v_add_u32_sdwa v90, s92, v101 dst_sel:DWORD dst_unused:UNUSED_PAD src0_sel:DWORD src1_sel:WORD_1\n\t"
    "v_add_u32_sdwa v101, s92, v101 dst_sel:DWORD dst_unused:UNUSED_PAD src0_sel:DWORD src1_sel:WORD_0\n\t"
    "ds_read_b64 v[88:89], v101 offset:0\n\t"
    "v_cvt_pk_bf16_f32 v116, v116, v117\n\t"
    "v_cvt_pk_bf16_f32 v117, v118, v119\n\t"
    "ds_write_b64 v125, v[116:117] offset:768\n\t"
    "v_mfma_f32_16x16x32_bf16 v[116:119], v[64:67], v[164:167], 0\n\t"
    "ds_read_b64 v[90:91], v90 offset:0\n\t"
    "v_mfma_f32_16x16x32_bf16 v[116:119], v[68:71], v[168:171], v[116:119]\n\t"
    "v_add_u32_sdwa v126, s92, v102 dst_sel:DWORD dst_unused:UNUSED_PAD src0_sel:DWORD src1_sel:WORD_1\n\t"
    "v_add_u32_sdwa v102, s92, v102 dst_sel:DWORD dst_unused:UNUSED_PAD src0_sel:DWORD src1_sel:WORD_0\n\t"
    "v_mfma_f32_16x16x32_bf16 v[116:119], v[72:75], v[220:223], v[116:119]\n\t"
    "v_cvt_pk_bf16_f32 v10, v12, v13\n\t"
    "v_cvt_pk_bf16_f32 v11, v14, v15\n\t"
    "v_mov_b64 v[12:13], 0\n\t"
    "v_mov_b64 v[14:15], 0\n\t"
    "s_cmp_lg_u32 s47, 0\n\t"
    "s_cbranch_scc1 L_mp2_%=\n\t"
    "L_mp2b_%=:\n\t"
    "s_waitcnt vmcnt(2) lgkmcnt(6)\n\t"
    "v_smfmac_f32_16x16x64_bf16 v[28:31], v[120:123], v[92:99], v124\n\t"
    "ds_read_b64 v[92:93], v102 offset:0\n\t"
    "v_mfma_f32_16x16x32_bf16 v[116:119], v[76:79], v[224:227], v[116:119]\n\t"
    "ds_read_b64 v[94:95], v126 offset:0\n\t"
    "v_cvt_pk_bf16_f32 v80, v80, v81\n\t"
    "v_cvt_pk_bf16_f32 v81, v82, v83\n\t"
    "ds_write_b64 v125, v[80:81] offset:872\n\t"
    "v_mfma_f32_16x16x32_bf16 v[80:83], v[64:67], v[172:175], 0\n\t"
    "v_add_u32_sdwa v98, s92, v103 dst_sel:DWORD dst_unused:UNUSED_PAD src0_sel:DWORD src1_sel:WORD_1\n\t"
    "v_add_u32_sdwa v103, s92, v103 dst_sel:DWORD dst_unused:UNUSED_PAD src0_sel:DWORD src1_sel:WORD_0\n\t"
    "ds_read_b64 v[96:97], v103 offset:0\n\t"
    "v_mfma_f32_16x16x32_bf16 v[80:83], v[68:71], v[176:179], v[80:83]\n\t"
    "ds_read_b64 v[98:99], v98 offset:0\n\t"
    "v_mfma_f32_16x16x32_bf16 v[80:83], v[72:75], v[228:231], v[80:83]\n\t"
    "v_add_u32_sdwa v126, s92, v104 dst_sel:DWORD dst_unused:UNUSED_PAD src0_sel:DWORD src1_sel:WORD_1\n\t"
    "v_add_u32_sdwa v104, s92, v104 dst_sel:DWORD dst_unused:UNUSED_PAD src0_sel:DWORD src1_sel:WORD_0\n\t"
    "v_mfma_f32_16x16x32_bf16 v[80:83], v[76:79], v[232:235], v[80:83]\n\t"
    "v_cvt_pk_bf16_f32 v16, v16, v17\n\t"
    "v_cvt_pk_bf16_f32 v17, v18, v19\n\t"
    "v_cvt_pk_bf16_f32 v116, v116, v117\n\t"
    "v_cvt_pk_bf16_f32 v117, v118, v119\n\t"
    "ds_write_b64 v125, v[116:117] offset:32000\n\t"
    "s_waitcnt lgkmcnt(6)\n\t"
    "v_smfmac_f32_16x16x64_bf16 v[32:35], v[120:123], v[84:91], v124\n\t"
    "ds_read_b64 v[84:85], v104 offset:0\n\t"
    "v_mfma_f32_16x16x32_bf16 v[116:119], v[64:67], v[180:183], 0\n\t"
    "ds_read_b64 v[86:87], v126 offset:0\n\t"
    "v_mfma_f32_16x16x32_bf16 v[116:119], v[68:71], v[184:187], v[116:119]\n\t"
    "v_add_u32_sdwa v90, s92, v105 dst_sel:DWORD dst_unused:UNUSED_PAD src0_sel:DWORD src1_sel:WORD_1\n\t"
    "v_add_u32_sdwa v105, s92, v105 dst_sel:DWORD dst_unused:UNUSED_PAD src0_sel:DWORD src1_sel:WORD_0\n\t"
    "ds_read_b64 v[88:89], v105 offset:0\n\t"
    "v_mfma_f32_16x16x32_bf16 v[116:119], v[72:75], v[236:239], v[116:119]\n\t"
    "ds_read_b64 v[90:91], v90 offset:0\n\t"
    "v_mfma_f32_16x16x32_bf16 v[116:119], v[76:79], v[240:243], v[116:119]\n\t"
    "v_add_u32_sdwa v126, s92, v106 dst_sel:DWORD dst_unused:UNUSED_PAD src0_sel:DWORD src1_sel:WORD_1\n\t"
    "v_add_u32_sdwa v106, s92, v106 dst_sel:DWORD dst_unused:UNUSED_PAD src0_sel:DWORD src1_sel:WORD_0\n\t"
    "v_cvt_pk_bf16_f32 v80, v80, v81\n\t"
    "v_cvt_pk_bf16_f32 v81, v82, v83\n\t"
    "ds_write_b64 v125, v[80:81] offset:32128\n\t"
    "v_mfma_f32_16x16x32_bf16 v[80:83], v[64:67], v[188:191], 0\n\t"
    "v_cvt_pk_bf16_f32 v18, v20, v21\n\t"
    "v_cvt_pk_bf16_f32 v19, v22, v23\n\t"
    "v_mov_b64 v[20:21], 0\n\t"
    "v_mov_b64 v[22:23], 0\n\t"
    "s_waitcnt vmcnt(0)\n\t"
    "s_cmp_lg_u32 s47, 0\n\t"
    "s_cbranch_scc1 L_mp3_%=\n\t"
    "L_mp3b_%=:\n\t"
    "s_barrier\n\t"
    "s_add_u32 s84, s84, s95\n\t"
    "buffer_load_dwordx4 v[100:103], v127, s[28:31], s84 offen offset:0 sc1\n\t"
    "s_lshl_b32 s50, s34, 10\n\t"
    "s_add_u32 s63, s63, s50\n\t"
    "s_add_u32 s50, s50, 0x2000\n\t"
    "s_xor_b32 s50, s50, 0x2000\n\t"
    "s_add_u32 m0, s50, 128000\n\t"
    "s_add_u32 s63, s63, 0x4000\n\t"
    "buffer_load_dwordx4 v127, s[28:31], s63 offen sc1 lds\n\t"
    "s_cmp_lt_u32 s34, 4\n\t"
    "s_cbranch_scc1 L_spp9_%=\n\t"
    "s_setprio 1\n\t"
    "L_spp9_%=:\n\t"
    "s_waitcnt lgkmcnt(6)\n\t"
    "v_smfmac_f32_16x16x64_bf16 v[36:39], v[120:123], v[92:99], v124\n\t"
    "ds_read_b64 v[92:93], v106 offset:0\n\t"
    "v_mfma_f32_16x16x32_bf16 v[80:83], v[68:71], v[192:195], v[80:83]\n\t"
    "ds_read_b64 v[94:95], v126 offset:0\n\t"
    "v_mfma_f32_16x16x32_bf16 v[80:83], v[72:75], v[244:247], v[80:83]\n\t"
    "v_add_u32_sdwa v98, s92, v107 dst_sel:DWORD dst_unused:UNUSED_PAD src0_sel:DWORD src1_sel:WORD_1\n\t"
    "v_add_u32_sdwa v107, s92, v107 dst_sel:DWORD dst_unused:UNUSED_PAD src0_sel:DWORD src1_sel:WORD_0\n\t"
    "ds_read_b64 v[96:97], v107 offset:0\n\t"
    "v_mfma_f32_16x16x32_bf16 v[80:83], v[76:79], v[248:251], v[80:83]\n\t"
    "ds_read_b64 v[98:99], v98 offset:0\n\t"
    "v_cvt_pk_bf16_f32 v116, v116, v117\n\t"
    "v_cvt_pk_bf16_f32 v117, v118, v119\n\t"
    "ds_write_b64 v125, v[116:117] offset:32256\n\t"
    "v_mfma_f32_16x16x32_bf16 v[116:119], v[64:67], v[196:199], 0\n\t"
    "buffer_load_dwordx4 v[104:107], v127, s[28:31], s84 offen offset:1024 sc1\n\t"
    "v_add_u32_sdwa v126, s92, v108 dst_sel:DWORD dst_unused:UNUSED_PAD src0_sel:DWORD src1_sel:WORD_1\n\t"
    "v_add_u32_sdwa v108, s92, v108 dst_sel:DWORD dst_unused:UNUSED_PAD src0_sel:DWORD src1_sel:WORD_0\n\t"
    "v_mfma_f32_16x16x32_bf16 v[116:119], v[68:71], v[200:203], v[116:119]\n\t"
    "v_cvt_pk_bf16_f32 v24, v24, v25\n\t"
    "v_cvt_pk_bf16_f32 v25, v26, v27\n\t"
    "v_mov_b64 v[48:49], 0\n\t"
    "v_mov_b64 v[50:51], 0\n\t"
    "v_mov_b64 v[52:53], 0\n\t"
    "v_mov_b64 v[54:55], 0\n\t"
    "s_waitcnt lgkmcnt(6)\n\t"
    "v_smfmac_f32_16x16x64_bf16 v[40:43], v[120:123], v[84:91], v124\n\t"
    "ds_read_b64 v[84:85], v108 offset:0\n\t"
    "v_mfma_f32_16x16x32_bf16 v[116:119], v[72:75], v[252:255], v[116:119]\n\t"
    "ds_read_b64 v[86:87], v126 offset:0\n\t"
    "v_mfma_f32_16x16x32_bf16 v[116:119], v[76:79], v[128:131], v[116:119]\n\t"
    "v_add_u32_sdwa v90, s92, v109 dst_sel:DWORD dst_unused:UNUSED_PAD src0_sel:DWORD src1_sel:WORD_1\n\t"
    "v_add_u32_sdwa v109, s92, v109 dst_sel:DWORD dst_unused:UNUSED_PAD src0_sel:DWORD src1_sel:WORD_0\n\t"
    "ds_read_b64 v[88:89], v109 offset:0\n\t"
    "v_cvt_pk_bf16_f32 v80, v80, v81\n\t"
    "v_cvt_pk_bf16_f32 v81, v82, v83\n\t"
    "ds_write_b64 v125, v[80:81] offset:32384\n\t"
    "v_mfma_f32_16x16x32_bf16 v[80:83], v[64:67], v[204:207], 0\n\t"
    "ds_read_b64 v[90:91], v90 offset:0\n\t"
    "v_mfma_f32_16x16x32_bf16 v[80:83], v[68:71], v[208:211], v[80:83]\n\t"
    "v_add_u32_sdwa v126, s92, v110 dst_sel:DWORD dst_unused:UNUSED_PAD src0_sel:DWORD src1_sel:WORD_1\n\t"
    "v_add_u32_sdwa v110, s92, v110 dst_sel:DWORD dst_unused:UNUSED_PAD src0_sel:DWORD src1_sel:WORD_0\n\t"
    "v_mfma_f32_16x16x32_bf16 v[80:83], v[72:75], v[132:135], v[80:83]\n\t"
    "v_cvt_pk_bf16_f32 v26, v28, v29\n\t"
    "v_cvt_pk_bf16_f32 v27, v30, v31\n\t"
    "v_mov_b64 v[28:29], 0\n\t"
    "v_mov_b64 v[30:31], 0\n\t"
    "s_waitcnt lgkmcnt(6)\n\t"
    "v_smfmac_f32_16x16x64_bf16 v[44:47], v[120:123], v[92:99], v124\n\t"
    "ds_read_b64 v[92:93], v110 offset:0\n\t"
    "v_mfma_f32_16x16x32_bf16 v[80:83], v[76:79], v[136:139], v[80:83]\n\t"
    "ds_read_b64 v[94:95], v126 offset:0\n\t"
    "v_cvt_pk_bf16_f32 v116, v116, v117\n\t"
    "v_cvt_pk_bf16_f32 v117, v118, v119\n\t"
    "ds_write_b64 v125, v[116:117] offset:32512\n\t"
    "v_mfma_f32_16x16x32_bf16 v[116:119], v[64:67], v[156:159], 0\n\t"
    "v_add_u32_sdwa v98, s92, v111 dst_sel:DWORD dst_unused:UNUSED_PAD src0_sel:DWORD src1_sel:WORD_1\n\t"
    "v_add_u32_sdwa v111, s92, v111 dst_sel:DWORD dst_unused:UNUSED_PAD src0_sel:DWORD src1_sel:WORD_0\n\t"
    "ds_read_b64 v[96:97], v111 offset:0\n\t"
    "v_mfma_f32_16x16x32_bf16 v[116:119], v[68:71], v[160:163], v[116:119]\n\t"
    "ds_read_b64 v[98:99], v98 offset:0\n\t"
    "v_mfma_f32_16x16x32_bf16 v[116:119], v[72:75], v[140:143], v[116:119]\n\t"
    "s_add_u32 s85, s85, s95\n\t"
    "buffer_load_dwordx4 v[108:111], v127, s[28:31], s85 offen offset:0 sc1\n\t"
    "v_add_u32_sdwa v126, s92, v112 dst_sel:DWORD dst_unused:UNUSED_PAD src0_sel:DWORD src1_sel:WORD_1\n\t"
    "v_add_u32_sdwa v112, s92, v112 dst_sel:DWORD dst_unused:UNUSED_PAD src0_sel:DWORD src1_sel:WORD_0\n\t"
    "v_mfma_f32_16x16x32_bf16 v[116:119], v[76:79], v[144:147], v[116:119]\n\t"
    "v_cvt_pk_bf16_f32 v32, v32, v33\n\t"
    "v_cvt_pk_bf16_f32 v33, v34, v35\n\t"
    "v_cvt_pk_bf16_f32 v80, v80, v81\n\t"
    "v_cvt_pk_bf16_f32 v81, v82, v83\n\t"
    "ds_write_b64 v125, v[80:81] offset:32640\n\t"
    "v_mov_b64 v[56:57], 0\n\t"
    "v_mov_b64 v[58:59], 0\n\t"
    "v_mov_b64 v[60:61], 0\n\t"
    "v_mov_b64 v[62:63], 0\n\t"
    "s_cmp_lt_u32 s34, 4\n\t"
    "s_cbranch_scc1 L_spp12_%=\n\t"
    "s_setprio 0\n\t"
    "L_spp12_%=:\n\t"
    "s_waitcnt lgkmcnt(6)\n\t"
    "v_smfmac_f32_16x16x64_bf16 v[48:51], v[120:123], v[84:91], v124\n\t"
    "ds_read_b64 v[84:85], v112 offset:0\n\t"
    "v_mfma_f32_16x16x32_bf16 v[80:83], v[64:67], v[212:215], 0\n\t"
    "ds_read_b64 v[86:87], v126 offset:0\n\t"
    "v_mfma_f32_16x16x32_bf16 v[80:83], v[68:71], v[216:219], v[80:83]\n\t"
    "v_add_u32_sdwa v90, s92, v113 dst_sel:DWORD dst_unused:UNUSED_PAD src0_sel:DWORD src1_sel:WORD_1\n\t"
    "v_add_u32_sdwa v113, s92, v113 dst_sel:DWORD dst_unused:UNUSED_PAD src0_sel:DWORD src1_sel:WORD_0\n\t"
    "ds_read_b64 v[88:89], v113 offset:0\n\t"
    "v_mfma_f32_16x16x32_bf16 v[80:83], v[72:75], v[148:151], v[80:83]\n\t"
    "ds_read_b64 v[90:91], v90 offset:0\n\t"
    "v_mfma_f32_16x16x32_bf16 v[80:83], v[76:79], v[152:155], v[80:83]\n\t"
    "v_add_u32_sdwa v126, s92, v114 dst_sel:DWORD dst_unused:UNUSED_PAD src0_sel:DWORD src1_sel:WORD_1\n\t"
    "v_add_u32_sdwa v114, s92, v114 dst_sel:DWORD dst_unused:UNUSED_PAD src0_sel:DWORD src1_sel:WORD_0\n\t"
    "v_cvt_pk_bf16_f32 v116, v116, v117\n\t"
    "v_cvt_pk_bf16_f32 v117, v118, v119\n\t"
    "ds_write_b64 v125, v[116:117] offset:32768\n\t"
    "s_nop 2\n\t"
    "v_cvt_pk_bf16_f32 v80, v80, v81\n\t"
    "v_cvt_pk_bf16_f32 v81, v82, v83\n\t"
    "ds_write_b64 v125, v[80:81] offset:32872\n\t"
    "v_cvt_pk_bf16_f32 v34, v36, v37\n\t"
    "v_cvt_pk_bf16_f32 v35, v38, v39\n\t"
    "v_mov_b64 v[36:37], 0\n\t"
    "v_mov_b64 v[38:39], 0\n\t"
    "v_cvt_pk_bf16_f32 v40, v40, v41\n\t"
    "v_cvt_pk_bf16_f32 v41, v42, v43\n\t"
    "v_cvt_pk_bf16_f32 v42, v44, v45\n\t"
    "v_cvt_pk_bf16_f32 v43, v46, v47\n\t"
    "v_mov_b64 v[44:45], 0\n\t"
    "v_mov_b64 v[46:47], 0\n\t"
    "s_waitcnt vmcnt(3) lgkmcnt(7)\n\t"
    "v_smfmac_f32_16x16x64_bf16 v[52:55], v[120:123], v[92:99], v124\n\t"
    "ds_read_b64 v[92:93], v114 offset:0\n\t"
    "ds_read_b64 v[94:95], v126 offset:0\n\t"
    "v_add_u32_sdwa v98, s92, v115 dst_sel:DWORD dst_unused:UNUSED_PAD src0_sel:DWORD src1_sel:WORD_1\n\t"
    "v_add_u32_sdwa v115, s92, v115 dst_sel:DWORD dst_unused:UNUSED_PAD src0_sel:DWORD src1_sel:WORD_0\n\t"
    "ds_read_b64 v[96:97], v115 offset:0\n\t"
    "ds_read_b64 v[98:99], v98 offset:0\n\t"
    "buffer_load_dwordx4 v[112:115], v127, s[28:31], s85 offen offset:1024 sc1\n\t"
    "s_sub_u32 s92, 64000, s92\n\t"
    "v_add_u32_sdwa v126, s92, v100 dst_sel:DWORD dst_unused:UNUSED_PAD src0_sel:DWORD src1_sel:WORD_1\n\t"
    "v_add_u32_sdwa v100, s92, v100 dst_sel:DWORD dst_unused:UNUSED_PAD src0_sel:DWORD src1_sel:WORD_0\n\t"
    "v_cvt_pk_bf16_f32 v48, v48, v49\n\t"
    "v_cvt_pk_bf16_f32 v49, v50, v51\n\t"
    "s_waitcnt lgkmcnt(4)\n\t"
    "s_waitcnt lgkmcnt(0)\n\t"
    "s_waitcnt vmcnt(3)\n\t"
    "s_barrier\n\t"
    "v_add_u32 v125, s93, v125\n\t"
    "s_sub_u32 s93, 0, s93\n\t"
    "s_cmp_lt_u32 s34, 4\n\t"
    "s_cbranch_scc1 L_spp14_%=\n\t"
    "s_setprio 1\n\t"
    "L_spp14_%=:\n\t"
    "v_smfmac_f32_16x16x64_bf16 v[56:59], v[120:123], v[84:91], v124\n\t"
    "ds_read_b64 v[84:85], v100 offset:0\n\t"
    "ds_read_b64 v[86:87], v126 offset:0\n\t"
    "v_add_u32_sdwa v90, s92, v101 dst_sel:DWORD dst_unused:UNUSED_PAD src0_sel:DWORD src1_sel:WORD_1\n\t"
    "v_add_u32_sdwa v101, s92, v101 dst_sel:DWORD dst_unused:UNUSED_PAD src0_sel:DWORD src1_sel:WORD_0\n\t"
    "ds_read_b64 v[88:89], v101 offset:0\n\t"
    "ds_read_b64 v[90:91], v90 offset:0\n\t"
    "v_add_u32_sdwa v126, s92, v102 dst_sel:DWORD dst_unused:UNUSED_PAD src0_sel:DWORD src1_sel:WORD_1\n\t"
    "v_add_u32_sdwa v102, s92, v102 dst_sel:DWORD dst_unused:UNUSED_PAD src0_sel:DWORD src1_sel:WORD_0\n\t"
    "v_cvt_pk_bf16_f32 v50, v52, v53\n\t"
    "v_cvt_pk_bf16_f32 v51, v54, v55\n\t"
    "v_mov_b64 v[52:53], 0\n\t"
    "v_mov_b64 v[54:55], 0\n\t"
    "s_waitcnt vmcnt(2)\n\t"
    "v_smfmac_f32_16x16x64_bf16 v[60:63], v[120:123], v[92:99], v124\n\t"
    "ds_read_b64 v[92:93], v102 offset:0\n\t"
    "ds_read_b64 v[94:95], v126 offset:0\n\t"
    "v_add_u32_sdwa v98, s92, v103 dst_sel:DWORD dst_unused:UNUSED_PAD src0_sel:DWORD src1_sel:WORD_1\n\t"
    "v_add_u32_sdwa v103, s92, v103 dst_sel:DWORD dst_unused:UNUSED_PAD src0_sel:DWORD src1_sel:WORD_0\n\t"
    "ds_read_b64 v[96:97], v103 offset:0\n\t"
    "ds_read_b64 v[98:99], v98 offset:0\n\t"
    "s_add_u32 s86, s86, s95\n\t"
    "buffer_load_dwordx4 v[100:103], v127, s[28:31], s86 offen offset:0 sc1\n\t"
    "v_add_u32_sdwa v126, s92, v104 dst_sel:DWORD dst_unused:UNUSED_PAD src0_sel:DWORD src1_sel:WORD_1\n\t"
    "v_add_u32_sdwa v104, s92, v104 dst_sel:DWORD dst_unused:UNUSED_PAD src0_sel:DWORD src1_sel:WORD_0\n\t"
    "v_cvt_pk_bf16_f32 v56, v56, v57\n\t"
    "v_cvt_pk_bf16_f32 v57, v58, v59\n\t"
    "v_add_u32 v79, s96, v127\n\t"
    "s_add_u32 s96, s96, 0x1000\n\t"
    "s_and_b32 s96, s96, 0x3000\n\t"
    "v_add_u32 v79, 128000, v79\n\t"
    "ds_read_b128 v[64:67], v79 offset:0\n\t"
    "ds_read_b128 v[68:71], v79 offset:1024\n\t"
    "ds_read_b128 v[72:75], v79 offset:2048\n\t"
    "ds_read_b128 v[76:79], v79 offset:3072\n\t"
    "v_mov_b64 v[116:117], 0\n\t"
    "v_mov_b64 v[118:119], 0\n\t"
    "v_cvt_pk_bf16_f32 v58, v60, v61\n\t"
    "v_cvt_pk_bf16_f32 v59, v62, v63\n\t"
    "v_mov_b64 v[60:61], 0\n\t"
    "v_mov_b64 v[62:63], 0\n\t"
    "s_cmp_eq_u32 s34, 0\n\t"
    "s_cselect_b32 s48, 2, -1\n\t"
    "s_mov_b32 s66, 4\n\t"
    "s_branch L_loop_%=\n\t"
    ".p2align 9\n\t"
    "s_nop 0\n\t"
    "s_nop 0\n\t"
    "s_nop 0\n\t"
    "s_nop 0\n\t"
    "s_nop 0\n\t"
    "s_nop 0\n\t"
    "s_nop 0\n\t"
    "s_nop 0\n\t"
    "s_nop 0\n\t"
    "s_nop 0\n\t"
    "s_nop 0\n\t"
    "s_nop 0\n\t"
    "s_nop 0\n\t"
    "s_nop 0\n\t"
    "s_nop 0\n\t"
    "s_nop 0\n\t"
    "s_nop 0\n\t"
    "s_nop 0\n\t"
    "s_nop 0\n\t"
    "s_nop 0\n\t"
    "s_nop 0\n\t"
    "s_nop 0\n\t"
    "s_nop 0\n\t"
    "s_nop 0\n\t"
    "s_nop 0\n\t"
    "s_nop 0\n\t"
    "s_nop 0\n\t"
    "s_nop 0\n\t"
    "s_nop 0\n\t"
    "s_nop 0\n\t"
    "s_nop 0\n\t"
    "s_nop 0\n\t"
    "s_nop 0\n\t"
    "L_loop_%=:\n\t"
    "s_waitcnt lgkmcnt(8)\n\t"
    "v_smfmac_f32_16x16x64_bf16 v[116:119], v[120:123], v[84:91], v124\n\t"
    "ds_read_b64 v[84:85], v104 offset:0\n\t"
    "ds_read_b64 v[86:87], v126 offset:0\n\t"
    "v_add_u32_sdwa v90, s92, v105 dst_sel:DWORD dst_unused:UNUSED_PAD src0_sel:DWORD src1_sel:WORD_1\n\t"
    "v_add_u32_sdwa v105, s92, v105 dst_sel:DWORD dst_unused:UNUSED_PAD src0_sel:DWORD src1_sel:WORD_0\n\t"
    "ds_read_b64 v[88:89], v105 offset:0\n\t"
    "ds_read_b64 v[90:91], v90 offset:0\n\t"
    "v_add_u32_sdwa v126, s92, v106 dst_sel:DWORD dst_unused:UNUSED_PAD src0_sel:DWORD src1_sel:WORD_1\n\t"
    "v_add_u32_sdwa v106, s92, v106 dst_sel:DWORD dst_unused:UNUSED_PAD src0_sel:DWORD src1_sel:WORD_0\n\t"
    "s_waitcnt vmcnt(2) lgkmcnt(8)\n\t"
    "v_smfmac_f32_16x16x64_bf16 v[116:119], v[120:123], v[92:99], v124\n\t"
    "ds_read_b64 v[92:93], v106 offset:0\n\t"
    "ds_read_b64 v[94:95], v126 offset:0\n\t"
    "v_add_u32_sdwa v98, s92, v107 dst_sel:DWORD dst_unused:UNUSED_PAD src0_sel:DWORD src1_sel:WORD_1\n\t"
    "v_add_u32_sdwa v107, s92, v107 dst_sel:DWORD dst_unused:UNUSED_PAD src0_sel:DWORD src1_sel:WORD_0\n\t"
    "ds_read_b64 v[96:97], v107 offset:0\n\t"
    "ds_read_b64 v[98:99], v98 offset:0\n\t"
    "buffer_load_dwordx4 v[104:107], v127, s[28:31], s86 offen offset:1024 sc1\n\t"
    "v_add_u32_sdwa v126, s92, v108 dst_sel:DWORD dst_unused:UNUSED_PAD src0_sel:DWORD src1_sel:WORD_1\n\t"
    "v_cvt_pk_bf16_f32 v4, v116, v117\n\t"
    "v_cvt_pk_bf16_f32 v5, v118, v119\n\t"
    "v_and_b32 v119, s67, v7\n\t"
    "v_lshlrev_b32 v118, 16, v7\n\t"
    "v_and_b32 v117, s67, v6\n\t"
    "v_lshlrev_b32 v116, 16, v6\n\t"
    "v_add_u32_sdwa v108, s92, v108 dst_sel:DWORD dst_unused:UNUSED_PAD src0_sel:DWORD src1_sel:WORD_0\n\t"
    "s_waitcnt lgkmcnt(4)\n\t"
    "v_smfmac_f32_16x16x64_bf16 v[116:119], v[120:123], v[84:91], v124\n\t"
    "ds_read_b64 v[84:85], v108 offset:0\n\t"
    "v_mfma_f32_16x16x32_bf16 v[80:83], v[64:67], v[164:167], 0\n\t"
    "ds_read_b64 v[86:87], v126 offset:0\n\t"
    "v_mfma_f32_16x16x32_bf16 v[80:83], v[68:71], v[168:171], v[80:83]\n\t"
    "v_add_u32_sdwa v90, s92, v109 dst_sel:DWORD dst_unused:UNUSED_PAD src0_sel:DWORD src1_sel:WORD_1\n\t"
    "v_add_u32_sdwa v109, s92, v109 dst_sel:DWORD dst_unused:UNUSED_PAD src0_sel:DWORD src1_sel:WORD_0\n\t"
    "ds_read_b64 v[88:89], v109 offset:0\n\t"
    "v_mfma_f32_16x16x32_bf16 v[80:83], v[72:75], v[220:223], v[80:83]\n\t"
    "ds_read_b64 v[90:91], v90 offset:0\n\t"
    "v_mfma_f32_16x16x32_bf16 v[80:83], v[76:79], v[224:227], v[80:83]\n\t"
    "v_add_u32_sdwa v126, s92, v110 dst_sel:DWORD dst_unused:UNUSED_PAD src0_sel:DWORD src1_sel:WORD_1\n\t"
    "v_add_u32_sdwa v110, s92, v110 dst_sel:DWORD dst_unused:UNUSED_PAD src0_sel:DWORD src1_sel:WORD_0\n\t"
    "s_waitcnt vmcnt(2) lgkmcnt(4)\n\t"
    "v_smfmac_f32_16x16x64_bf16 v[116:119], v[120:123], v[92:99], v124\n\t"
    "ds_read_b64 v[92:93], v110 offset:0\n\t"
    "ds_read_b64 v[94:95], v126 offset:0\n\t"
    "v_add_u32_sdwa v98, s92, v111 dst_sel:DWORD dst_unused:UNUSED_PAD src0_sel:DWORD src1_sel:WORD_1\n\t"
    "v_add_u32_sdwa v111, s92, v111 dst_sel:DWORD dst_unused:UNUSED_PAD src0_sel:DWORD src1_sel:WORD_0\n\t"
    "ds_read_b64 v[96:97], v111 offset:0\n\t"
    "ds_read_b64 v[98:99], v98 offset:0\n\t"
    "s_add_u32 s87, s87, s95\n\t"
    "buffer_load_dwordx4 v[108:111], v127, s[28:31], s87 offen offset:0 sc1\n\t"
    "v_add_u32_sdwa v126, s92, v112 dst_sel:DWORD dst_unused:UNUSED_PAD src0_sel:DWORD src1_sel:WORD_1\n\t"
    "v_cvt_pk_bf16_f32 v6, v116, v117\n\t"
    "v_cvt_pk_bf16_f32 v7, v118, v119\n\t"
    "v_and_b32 v119, s67, v13\n\t"
    "v_lshlrev_b32 v118, 16, v13\n\t"
    "v_and_b32 v117, s67, v12\n\t"
    "v_lshlrev_b32 v116, 16, v12\n\t"
    "v_add_u32_sdwa v112, s92, v112 dst_sel:DWORD dst_unused:UNUSED_PAD src0_sel:DWORD src1_sel:WORD_0\n\t"
    "s_waitcnt lgkmcnt(4)\n\t"
    "v_smfmac_f32_16x16x64_bf16 v[116:119], v[120:123], v[84:91], v124\n\t"
    "ds_read_b64 v[84:85], v112 offset:0\n\t"
    "ds_read_b64 v[86:87], v126 offset:0\n\t"
    "v_cvt_pk_bf16_f32 v80, v80, v81\n\t"
    "v_cvt_pk_bf16_f32 v81, v82, v83\n\t"
    "ds_write_b64 v125, v[80:81] offset:0\n\t"
    "v_mfma_f32_16x16x32_bf16 v[80:83], v[64:67], v[172:175], 0\n\t"
    "v_add_u32_sdwa v90, s92, v113 dst_sel:DWORD dst_unused:UNUSED_PAD src0_sel:DWORD src1_sel:WORD_1\n\t"
    "v_add_u32_sdwa v113, s92, v113 dst_sel:DWORD dst_unused:UNUSED_PAD src0_sel:DWORD src1_sel:WORD_0\n\t"
    "ds_read_b64 v[88:89], v113 offset:0\n\t"
    "v_mfma_f32_16x16x32_bf16 v[80:83], v[68:71], v[176:179], v[80:83]\n\t"
    "ds_read_b64 v[90:91], v90 offset:0\n\t"
    "v_mfma_f32_16x16x32_bf16 v[80:83], v[72:75], v[228:231], v[80:83]\n\t"
    "v_mfma_f32_16x16x32_bf16 v[80:83], v[76:79], v[232:235], v[80:83]\n\t"
    "v_add_u32_sdwa v126, s92, v114 dst_sel:DWORD dst_unused:UNUSED_PAD src0_sel:DWORD src1_sel:WORD_1\n\t"
    "v_add_u32_sdwa v114, s92, v114 dst_sel:DWORD dst_unused:UNUSED_PAD src0_sel:DWORD src1_sel:WORD_0\n\t"
    "s_waitcnt vmcnt(2) lgkmcnt(5)\n\t"
    "v_smfmac_f32_16x16x64_bf16 v[116:119], v[120:123], v[92:99], v124\n\t"
    "ds_read_b64 v[92:93], v114 offset:0\n\t"
    "ds_read_b64 v[94:95], v126 offset:0\n\t"
    "v_add_u32_sdwa v98, s92, v115 dst_sel:DWORD dst_unused:UNUSED_PAD src0_sel:DWORD src1_sel:WORD_1\n\t"
    "v_add_u32_sdwa v115, s92, v115 dst_sel:DWORD dst_unused:UNUSED_PAD src0_sel:DWORD src1_sel:WORD_0\n\t"
    "ds_read_b64 v[96:97], v115 offset:0\n\t"
    "ds_read_b64 v[98:99], v98 offset:0\n\t"
    "buffer_load_dwordx4 v[112:115], v127, s[28:31], s87 offen offset:1024 sc1\n\t"
    "v_add_u32_sdwa v126, s92, v100 dst_sel:DWORD dst_unused:UNUSED_PAD src0_sel:DWORD src1_sel:WORD_1\n\t"
    "v_cvt_pk_bf16_f32 v12, v116, v117\n\t"
    "v_cvt_pk_bf16_f32 v13, v118, v119\n\t"
    "v_and_b32 v119, s67, v15\n\t"
    "v_lshlrev_b32 v118, 16, v15\n\t"
    "v_and_b32 v117, s67, v14\n\t"
    "v_lshlrev_b32 v116, 16, v14\n\t"
    "v_add_u32_sdwa v100, s92, v100 dst_sel:DWORD dst_unused:UNUSED_PAD src0_sel:DWORD src1_sel:WORD_0\n\t"
    "s_waitcnt lgkmcnt(4)\n\t"
    "v_smfmac_f32_16x16x64_bf16 v[116:119], v[120:123], v[84:91], v124\n\t"
    "ds_read_b64 v[84:85], v100 offset:0\n\t"
    "ds_read_b64 v[86:87], v126 offset:0\n\t"
    "v_cvt_pk_bf16_f32 v80, v80, v81\n\t"
    "v_cvt_pk_bf16_f32 v81, v82, v83\n\t"
    "ds_write_b64 v125, v[80:81] offset:128\n\t"
    "v_mfma_f32_16x16x32_bf16 v[80:83], v[64:67], v[180:183], 0\n\t"
    "v_add_u32_sdwa v90, s92, v101 dst_sel:DWORD dst_unused:UNUSED_PAD src0_sel:DWORD src1_sel:WORD_1\n\t"
    "v_add_u32_sdwa v101, s92, v101 dst_sel:DWORD dst_unused:UNUSED_PAD src0_sel:DWORD src1_sel:WORD_0\n\t"
    "ds_read_b64 v[88:89], v101 offset:0\n\t"
    "v_mfma_f32_16x16x32_bf16 v[80:83], v[68:71], v[184:187], v[80:83]\n\t"
    "ds_read_b64 v[90:91], v90 offset:0\n\t"
    "v_mfma_f32_16x16x32_bf16 v[80:83], v[72:75], v[236:239], v[80:83]\n\t"
    "v_mfma_f32_16x16x32_bf16 v[80:83], v[76:79], v[240:243], v[80:83]\n\t"
    "v_add_u32_sdwa v126, s92, v102 dst_sel:DWORD dst_unused:UNUSED_PAD src0_sel:DWORD src1_sel:WORD_1\n\t"
    "v_add_u32_sdwa v102, s92, v102 dst_sel:DWORD dst_unused:UNUSED_PAD src0_sel:DWORD src1_sel:WORD_0\n\t"
    "s_waitcnt vmcnt(2) lgkmcnt(5)\n\t"
    "v_smfmac_f32_16x16x64_bf16 v[116:119], v[120:123], v[92:99], v124\n\t"
    "ds_read_b64 v[92:93], v102 offset:0\n\t"
    "ds_read_b64 v[94:95], v126 offset:0\n\t"
    "v_add_u32_sdwa v98, s92, v103 dst_sel:DWORD dst_unused:UNUSED_PAD src0_sel:DWORD src1_sel:WORD_1\n\t"
    "v_add_u32_sdwa v103, s92, v103 dst_sel:DWORD dst_unused:UNUSED_PAD src0_sel:DWORD src1_sel:WORD_0\n\t"
    "ds_read_b64 v[96:97], v103 offset:0\n\t"
    "ds_read_b64 v[98:99], v98 offset:0\n\t"
    "s_add_u32 s88, s88, s95\n\t"
    "buffer_load_dwordx4 v[100:103], v127, s[28:31], s88 offen offset:0 sc1\n\t"
    "v_add_u32_sdwa v126, s92, v104 dst_sel:DWORD dst_unused:UNUSED_PAD src0_sel:DWORD src1_sel:WORD_1\n\t"
    "v_cvt_pk_bf16_f32 v14, v116, v117\n\t"
    "v_cvt_pk_bf16_f32 v15, v118, v119\n\t"
    "v_and_b32 v119, s67, v21\n\t"
    "v_lshlrev_b32 v118, 16, v21\n\t"
    "v_and_b32 v117, s67, v20\n\t"
    "v_lshlrev_b32 v116, 16, v20\n\t"
    "v_add_u32_sdwa v104, s92, v104 dst_sel:DWORD dst_unused:UNUSED_PAD src0_sel:DWORD src1_sel:WORD_0\n\t"
    "s_waitcnt lgkmcnt(4)\n\t"
    "v_smfmac_f32_16x16x64_bf16 v[116:119], v[120:123], v[84:91], v124\n\t"
    "ds_read_b64 v[84:85], v104 offset:0\n\t"
    "ds_read_b64 v[86:87], v126 offset:0\n\t"
    "v_cvt_pk_bf16_f32 v80, v80, v81\n\t"
    "v_cvt_pk_bf16_f32 v81, v82, v83\n\t"
    "ds_write_b64 v125, v[80:81] offset:256\n\t"
    "v_add_u32_sdwa v90, s92, v105 dst_sel:DWORD dst_unused:UNUSED_PAD src0_sel:DWORD src1_sel:WORD_1\n\t"
    "v_add_u32_sdwa v105, s92, v105 dst_sel:DWORD dst_unused:UNUSED_PAD src0_sel:DWORD src1_sel:WORD_0\n\t"
    "ds_read_b64 v[88:89], v105 offset:0\n\t"
    "ds_read_b64 v[90:91], v90 offset:0\n\t"
    "v_add_u32_sdwa v126, s92, v106 dst_sel:DWORD dst_unused:UNUSED_PAD src0_sel:DWORD src1_sel:WORD_1\n\t"
    "v_add_u32_sdwa v106, s92, v106 dst_sel:DWORD dst_unused:UNUSED_PAD src0_sel:DWORD src1_sel:WORD_0\n\t"
    "s_waitcnt vmcnt(2) lgkmcnt(5)\n\t"
    "v_smfmac_f32_16x16x64_bf16 v[116:119], v[120:123], v[92:99], v124\n\t"
    "ds_read_b64 v[92:93], v106 offset:0\n\t"
    "ds_read_b64 v[94:95], v126 offset:0\n\t"
    "v_add_u32_sdwa v98, s92, v107 dst_sel:DWORD dst_unused:UNUSED_PAD src0_sel:DWORD src1_sel:WORD_1\n\t"
    "v_add_u32_sdwa v107, s92, v107 dst_sel:DWORD dst_unused:UNUSED_PAD src0_sel:DWORD src1_sel:WORD_0\n\t"
    "ds_read_b64 v[96:97], v107 offset:0\n\t"
    "ds_read_b64 v[98:99], v98 offset:0\n\t"
    "buffer_load_dwordx4 v[104:107], v127, s[28:31], s88 offen offset:1024 sc1\n\t"
    "v_add_u32_sdwa v126, s92, v108 dst_sel:DWORD dst_unused:UNUSED_PAD src0_sel:DWORD src1_sel:WORD_1\n\t"
    "v_cvt_pk_bf16_f32 v20, v116, v117\n\t"
    "v_cvt_pk_bf16_f32 v21, v118, v119\n\t"
    "v_and_b32 v119, s67, v23\n\t"
    "v_lshlrev_b32 v118, 16, v23\n\t"
    "v_and_b32 v117, s67, v22\n\t"
    "v_lshlrev_b32 v116, 16, v22\n\t"
    "v_add_u32_sdwa v108, s92, v108 dst_sel:DWORD dst_unused:UNUSED_PAD src0_sel:DWORD src1_sel:WORD_0\n\t"
    "s_waitcnt lgkmcnt(4)\n\t"
    "v_smfmac_f32_16x16x64_bf16 v[116:119], v[120:123], v[84:91], v124\n\t"
    "ds_read_b64 v[84:85], v108 offset:0\n\t"
    "v_mfma_f32_16x16x32_bf16 v[80:83], v[64:67], v[188:191], 0\n\t"
    "ds_read_b64 v[86:87], v126 offset:0\n\t"
    "v_mfma_f32_16x16x32_bf16 v[80:83], v[68:71], v[192:195], v[80:83]\n\t"
    "v_add_u32_sdwa v90, s92, v109 dst_sel:DWORD dst_unused:UNUSED_PAD src0_sel:DWORD src1_sel:WORD_1\n\t"
    "v_add_u32_sdwa v109, s92, v109 dst_sel:DWORD dst_unused:UNUSED_PAD src0_sel:DWORD src1_sel:WORD_0\n\t"
    "ds_read_b64 v[88:89], v109 offset:0\n\t"
    "v_mfma_f32_16x16x32_bf16 v[80:83], v[72:75], v[244:247], v[80:83]\n\t"
    "ds_read_b64 v[90:91], v90 offset:0\n\t"
    "v_mfma_f32_16x16x32_bf16 v[80:83], v[76:79], v[248:251], v[80:83]\n\t"
    "v_add_u32_sdwa v126, s92, v110 dst_sel:DWORD dst_unused:UNUSED_PAD src0_sel:DWORD src1_sel:WORD_1\n\t"
    "v_add_u32_sdwa v110, s92, v110 dst_sel:DWORD dst_unused:UNUSED_PAD src0_sel:DWORD src1_sel:WORD_0\n\t"
    "s_waitcnt vmcnt(2) lgkmcnt(4)\n\t"
    "v_smfmac_f32_16x16x64_bf16 v[116:119], v[120:123], v[92:99], v124\n\t"
    "ds_read_b64 v[92:93], v110 offset:0\n\t"
    "ds_read_b64 v[94:95], v126 offset:0\n\t"
    "v_add_u32_sdwa v98, s92, v111 dst_sel:DWORD dst_unused:UNUSED_PAD src0_sel:DWORD src1_sel:WORD_1\n\t"
    "v_add_u32_sdwa v111, s92, v111 dst_sel:DWORD dst_unused:UNUSED_PAD src0_sel:DWORD src1_sel:WORD_0\n\t"
    "ds_read_b64 v[96:97], v111 offset:0\n\t"
    "ds_read_b64 v[98:99], v98 offset:0\n\t"
    "s_add_u32 s89, s89, s95\n\t"
    "buffer_load_dwordx4 v[108:111], v127, s[28:31], s89 offen offset:0 sc1\n\t"
    "v_add_u32_sdwa v126, s92, v112 dst_sel:DWORD dst_unused:UNUSED_PAD src0_sel:DWORD src1_sel:WORD_1\n\t"
    "v_cvt_pk_bf16_f32 v22, v116, v117\n\t"
    "v_cvt_pk_bf16_f32 v23, v118, v119\n\t"
    "v_and_b32 v119, s67, v29\n\t"
    "v_lshlrev_b32 v118, 16, v29\n\t"
    "v_and_b32 v117, s67, v28\n\t"
    "v_lshlrev_b32 v116, 16, v28\n\t"
    "v_add_u32_sdwa v112, s92, v112 dst_sel:DWORD dst_unused:UNUSED_PAD src0_sel:DWORD src1_sel:WORD_0\n\t"
    "s_waitcnt lgkmcnt(4)\n\t"
    "v_smfmac_f32_16x16x64_bf16 v[116:119], v[120:123], v[84:91], v124\n\t"
    "ds_read_b64 v[84:85], v112 offset:0\n\t"
    "ds_read_b64 v[86:87], v126 offset:0\n\t"
    "v_cvt_pk_bf16_f32 v80, v80, v81\n\t"
    "v_cvt_pk_bf16_f32 v81, v82, v83\n\t"
    "ds_write_b64 v125, v[80:81] offset:384\n\t"
    "v_add_u32_sdwa v90, s92, v113 dst_sel:DWORD dst_unused:UNUSED_PAD src0_sel:DWORD src1_sel:WORD_1\n\t"
    "v_add_u32_sdwa v113, s92, v113 dst_sel:DWORD dst_unused:UNUSED_PAD src0_sel:DWORD src1_sel:WORD_0\n\t"
    "ds_read_b64 v[88:89], v113 offset:0\n\t"
    "ds_read_b64 v[90:91], v90 offset:0\n\t"
    "v_add_u32_sdwa v126, s92, v114 dst_sel:DWORD dst_unused:UNUSED_PAD src0_sel:DWORD src1_sel:WORD_1\n\t"
    "v_add_u32_sdwa v114, s92, v114 dst_sel:DWORD dst_unused:UNUSED_PAD src0_sel:DWORD src1_sel:WORD_0\n\t"
    "s_waitcnt vmcnt(2) lgkmcnt(5)\n\t"
    "v_smfmac_f32_16x16x64_bf16 v[116:119], v[120:123], v[92:99], v124\n\t"
    "ds_read_b64 v[92:93], v114 offset:0\n\t"
    "ds_read_b64 v[94:95], v126 offset:0\n\t"
    "v_add_u32_sdwa v98, s92, v115 dst_sel:DWORD dst_unused:UNUSED_PAD src0_sel:DWORD src1_sel:WORD_1\n\t"
    "v_add_u32_sdwa v115, s92, v115 dst_sel:DWORD dst_unused:UNUSED_PAD src0_sel:DWORD src1_sel:WORD_0\n\t"
    "ds_read_b64 v[96:97], v115 offset:0\n\t"
    "ds_read_b64 v[98:99], v98 offset:0\n\t"
    "buffer_load_dwordx4 v[112:115], v127, s[28:31], s89 offen offset:1024 sc1\n\t"
    "v_add_u32_sdwa v126, s92, v100 dst_sel:DWORD dst_unused:UNUSED_PAD src0_sel:DWORD src1_sel:WORD_1\n\t"
    "v_cvt_pk_bf16_f32 v28, v116, v117\n\t"
    "v_cvt_pk_bf16_f32 v29, v118, v119\n\t"
    "v_and_b32 v119, s67, v31\n\t"
    "v_lshlrev_b32 v118, 16, v31\n\t"
    "v_and_b32 v117, s67, v30\n\t"
    "v_lshlrev_b32 v116, 16, v30\n\t"
    "v_add_u32_sdwa v100, s92, v100 dst_sel:DWORD dst_unused:UNUSED_PAD src0_sel:DWORD src1_sel:WORD_0\n\t"
    "s_waitcnt lgkmcnt(4)\n\t"
    "v_smfmac_f32_16x16x64_bf16 v[116:119], v[120:123], v[84:91], v124\n\t"
    "ds_read_b64 v[84:85], v100 offset:0\n\t"
    "v_mfma_f32_16x16x32_bf16 v[80:83], v[64:67], v[196:199], 0\n\t"
    "ds_read_b64 v[86:87], v126 offset:0\n\t"
    "v_mfma_f32_16x16x32_bf16 v[80:83], v[68:71], v[200:203], v[80:83]\n\t"
    "v_add_u32_sdwa v90, s92, v101 dst_sel:DWORD dst_unused:UNUSED_PAD src0_sel:DWORD src1_sel:WORD_1\n\t"
    "v_add_u32_sdwa v101, s92, v101 dst_sel:DWORD dst_unused:UNUSED_PAD src0_sel:DWORD src1_sel:WORD_0\n\t"
    "ds_read_b64 v[88:89], v101 offset:0\n\t"
    "v_mfma_f32_16x16x32_bf16 v[80:83], v[72:75], v[252:255], v[80:83]\n\t"
    "ds_read_b64 v[90:91], v90 offset:0\n\t"
    "v_mfma_f32_16x16x32_bf16 v[80:83], v[76:79], v[128:131], v[80:83]\n\t"
    "v_add_u32_sdwa v126, s92, v102 dst_sel:DWORD dst_unused:UNUSED_PAD src0_sel:DWORD src1_sel:WORD_1\n\t"
    "v_add_u32_sdwa v102, s92, v102 dst_sel:DWORD dst_unused:UNUSED_PAD src0_sel:DWORD src1_sel:WORD_0\n\t"
    "s_xor_b32 s50, s50, 0x2000\n\t"
    "s_add_u32 m0, s50, 128000\n\t"
    "s_add_u32 s63, s63, 0x4000\n\t"
    "buffer_load_dwordx4 v127, s[28:31], s63 offen sc1 lds\n\t"
    "s_waitcnt vmcnt(3) lgkmcnt(4)\n\t"
    "v_smfmac_f32_16x16x64_bf16 v[116:119], v[120:123], v[92:99], v124\n\t"
    "ds_read_b64 v[92:93], v102 offset:0\n\t"
    "ds_read_b64 v[94:95], v126 offset:0\n\t"
    "v_add_u32_sdwa v98, s92, v103 dst_sel:DWORD dst_unused:UNUSED_PAD src0_sel:DWORD src1_sel:WORD_1\n\t"
    "v_add_u32_sdwa v103, s92, v103 dst_sel:DWORD dst_unused:UNUSED_PAD src0_sel:DWORD src1_sel:WORD_0\n\t"
    "ds_read_b64 v[96:97], v103 offset:0\n\t"
    "ds_read_b64 v[98:99], v98 offset:0\n\t"
    "s_add_u32 s90, s90, s95\n\t"
    "buffer_load_dwordx4 v[100:103], v127, s[28:31], s90 offen offset:0 sc1\n\t"
    "v_add_u32_sdwa v126, s92, v104 dst_sel:DWORD dst_unused:UNUSED_PAD src0_sel:DWORD src1_sel:WORD_1\n\t"
    "v_cvt_pk_bf16_f32 v30, v116, v117\n\t"
    "v_cvt_pk_bf16_f32 v31, v118, v119\n\t"
    "v_and_b32 v119, s67, v37\n\t"
    "v_lshlrev_b32 v118, 16, v37\n\t"
    "v_and_b32 v117, s67, v36\n\t"
    "v_lshlrev_b32 v116, 16, v36\n\t"
    "v_add_u32_sdwa v104, s92, v104 dst_sel:DWORD dst_unused:UNUSED_PAD src0_sel:DWORD src1_sel:WORD_0\n\t"
    "s_waitcnt lgkmcnt(4)\n\t"
    "v_smfmac_f32_16x16x64_bf16 v[116:119], v[120:123], v[84:91], v124\n\t"
    "ds_read_b64 v[84:85], v104 offset:0\n\t"
    "ds_read_b64 v[86:87], v126 offset:0\n\t"
    "v_cvt_pk_bf16_f32 v80, v80, v81\n\t"
    "v_cvt_pk_bf16_f32 v81, v82, v83\n\t"
    "ds_write_b64 v125, v[80:81] offset:512\n\t"
    "v_add_u32_sdwa v90, s92, v105 dst_sel:DWORD dst_unused:UNUSED_PAD src0_sel:DWORD src1_sel:WORD_1\n\t"
    "v_add_u32_sdwa v105, s92, v105 dst_sel:DWORD dst_unused:UNUSED_PAD src0_sel:DWORD src1_sel:WORD_0\n\t"
    "ds_read_b64 v[88:89], v105 offset:0\n\t"
    "ds_read_b64 v[90:91], v90 offset:0\n\t"
    "v_add_u32_sdwa v126, s92, v106 dst_sel:DWORD dst_unused:UNUSED_PAD src0_sel:DWORD src1_sel:WORD_1\n\t"
    "v_add_u32_sdwa v106, s92, v106 dst_sel:DWORD dst_unused:UNUSED_PAD src0_sel:DWORD src1_sel:WORD_0\n\t"
    "s_waitcnt vmcnt(3) lgkmcnt(5)\n\t"
    "v_smfmac_f32_16x16x64_bf16 v[116:119], v[120:123], v[92:99], v124\n\t"
    "ds_read_b64 v[92:93], v106 offset:0\n\t"
    "ds_read_b64 v[94:95], v126 offset:0\n\t"
    "v_add_u32_sdwa v98, s92, v107 dst_sel:DWORD dst_unused:UNUSED_PAD src0_sel:DWORD src1_sel:WORD_1\n\t"
    "v_add_u32_sdwa v107, s92, v107 dst_sel:DWORD dst_unused:UNUSED_PAD src0_sel:DWORD src1_sel:WORD_0\n\t"
    "ds_read_b64 v[96:97], v107 offset:0\n\t"
    "ds_read_b64 v[98:99], v98 offset:0\n\t"
    "buffer_load_dwordx4 v[104:107], v127, s[28:31], s90 offen offset:1024 sc1\n\t"
    "v_add_u32_sdwa v126, s92, v108 dst_sel:DWORD dst_unused:UNUSED_PAD src0_sel:DWORD src1_sel:WORD_1\n\t"
    "v_cvt_pk_bf16_f32 v36, v116, v117\n\t"
    "v_cvt_pk_bf16_f32 v37, v118, v119\n\t"
    "v_and_b32 v119, s67, v39\n\t"
    "v_lshlrev_b32 v118, 16, v39\n\t"
    "v_and_b32 v117, s67, v38\n\t"
    "v_lshlrev_b32 v116, 16, v38\n\t"
    "v_add_u32_sdwa v108, s92, v108 dst_sel:DWORD dst_unused:UNUSED_PAD src0_sel:DWORD src1_sel:WORD_0\n\t"
    "s_waitcnt lgkmcnt(4)\n\t"
    "v_smfmac_f32_16x16x64_bf16 v[116:119], v[120:123], v[84:91], v124\n\t"
    "ds_read_b64 v[84:85], v108 offset:0\n\t"
    "v_mfma_f32_16x16x32_bf16 v[80:83], v[64:67], v[204:207], 0\n\t"
    "ds_read_b64 v[86:87], v126 offset:0\n\t"
    "v_mfma_f32_16x16x32_bf16 v[80:83], v[68:71], v[208:211], v[80:83]\n\t"
    "v_add_u32_sdwa v90, s92, v109 dst_sel:DWORD dst_unused:UNUSED_PAD src0_sel:DWORD src1_sel:WORD_1\n\t"
    "v_add_u32_sdwa v109, s92, v109 dst_sel:DWORD dst_unused:UNUSED_PAD src0_sel:DWORD src1_sel:WORD_0\n\t"
    "ds_read_b64 v[88:89], v109 offset:0\n\t"
    "v_mfma_f32_16x16x32_bf16 v[80:83], v[72:75], v[132:135], v[80:83]\n\t"
    "ds_read_b64 v[90:91], v90 offset:0\n\t"
    "v_mfma_f32_16x16x32_bf16 v[80:83], v[76:79], v[136:139], v[80:83]\n\t"
    "v_add_u32_sdwa v126, s92, v110 dst_sel:DWORD dst_unused:UNUSED_PAD src0_sel:DWORD src1_sel:WORD_1\n\t"
    "v_add_u32_sdwa v110, s92, v110 dst_sel:DWORD dst_unused:UNUSED_PAD src0_sel:DWORD src1_sel:WORD_0\n\t"
    "s_cmp_lt_u32 s34, 4\n\t"
    "s_cbranch_scc1 L_tp19_%=\n\t"
    "s_setprio 0\n\t"
    "L_tp19_%=:\n\t"
    "s_waitcnt vmcnt(3) lgkmcnt(4)\n\t"
    "v_smfmac_f32_16x16x64_bf16 v[116:119], v[120:123], v[92:99], v124\n\t"
    "ds_read_b64 v[92:93], v110 offset:0\n\t"
    "ds_read_b64 v[94:95], v126 offset:0\n\t"
    "v_add_u32_sdwa v98, s92, v111 dst_sel:DWORD dst_unused:UNUSED_PAD src0_sel:DWORD src1_sel:WORD_1\n\t"
    "v_add_u32_sdwa v111, s92, v111 dst_sel:DWORD dst_unused:UNUSED_PAD src0_sel:DWORD src1_sel:WORD_0\n\t"
    "ds_read_b64 v[96:97], v111 offset:0\n\t"
    "ds_read_b64 v[98:99], v98 offset:0\n\t"
    "s_add_u32 s91, s91, s95\n\t"
    "buffer_load_dwordx4 v[108:111], v127, s[28:31], s91 offen offset:0 sc1\n\t"
    "v_add_u32_sdwa v126, s92, v112 dst_sel:DWORD dst_unused:UNUSED_PAD src0_sel:DWORD src1_sel:WORD_1\n\t"
    "v_cvt_pk_bf16_f32 v38, v116, v117\n\t"
    "v_cvt_pk_bf16_f32 v39, v118, v119\n\t"
    "v_and_b32 v119, s67, v45\n\t"
    "v_lshlrev_b32 v118, 16, v45\n\t"
    "v_and_b32 v117, s67, v44\n\t"
    "v_lshlrev_b32 v116, 16, v44\n\t"
    "v_add_u32_sdwa v112, s92, v112 dst_sel:DWORD dst_unused:UNUSED_PAD src0_sel:DWORD src1_sel:WORD_0\n\t"
    "s_waitcnt lgkmcnt(4)\n\t"
    "v_smfmac_f32_16x16x64_bf16 v[116:119], v[120:123], v[84:91], v124\n\t"
    "ds_read_b64 v[84:85], v112 offset:0\n\t"
    "ds_read_b64 v[86:87], v126 offset:0\n\t"
    "v_cvt_pk_bf16_f32 v80, v80, v81\n\t"
    "v_cvt_pk_bf16_f32 v81, v82, v83\n\t"
    "ds_write_b64 v125, v[80:81] offset:640\n\t"
    "v_add_u32_sdwa v90, s92, v113 dst_sel:DWORD dst_unused:UNUSED_PAD src0_sel:DWORD src1_sel:WORD_1\n\t"
    "v_add_u32_sdwa v113, s92, v113 dst_sel:DWORD dst_unused:UNUSED_PAD src0_sel:DWORD src1_sel:WORD_0\n\t"
    "ds_read_b64 v[88:89], v113 offset:0\n\t"
    "ds_read_b64 v[90:91], v90 offset:0\n\t"
    "v_add_u32_sdwa v126, s92, v114 dst_sel:DWORD dst_unused:UNUSED_PAD src0_sel:DWORD src1_sel:WORD_1\n\t"
    "v_add_u32_sdwa v114, s92, v114 dst_sel:DWORD dst_unused:UNUSED_PAD src0_sel:DWORD src1_sel:WORD_0\n\t"
    "s_waitcnt vmcnt(2) lgkmcnt(5)\n\t"
    "v_smfmac_f32_16x16x64_bf16 v[116:119], v[120:123], v[92:99], v124\n\t"
    "ds_read_b64 v[92:93], v114 offset:0\n\t"
    "ds_read_b64 v[94:95], v126 offset:0\n\t"
    "v_add_u32_sdwa v98, s92, v115 dst_sel:DWORD dst_unused:UNUSED_PAD src0_sel:DWORD src1_sel:WORD_1\n\t"
    "v_add_u32_sdwa v115, s92, v115 dst_sel:DWORD dst_unused:UNUSED_PAD src0_sel:DWORD src1_sel:WORD_0\n\t"
    "ds_read_b64 v[96:97], v115 offset:0\n\t"
    "ds_read_b64 v[98:99], v98 offset:0\n\t"
    "buffer_load_dwordx4 v[112:115], v127, s[28:31], s91 offen offset:1024 sc1\n\t"
    "v_add_u32_sdwa v126, s92, v100 dst_sel:DWORD dst_unused:UNUSED_PAD src0_sel:DWORD src1_sel:WORD_1\n\t"
    "v_cvt_pk_bf16_f32 v44, v116, v117\n\t"
    "v_cvt_pk_bf16_f32 v45, v118, v119\n\t"
    "v_and_b32 v119, s67, v47\n\t"
    "v_lshlrev_b32 v118, 16, v47\n\t"
    "v_and_b32 v117, s67, v46\n\t"
    "v_lshlrev_b32 v116, 16, v46\n\t"
    "v_add_u32_sdwa v100, s92, v100 dst_sel:DWORD dst_unused:UNUSED_PAD src0_sel:DWORD src1_sel:WORD_0\n\t"
    "s_waitcnt lgkmcnt(4)\n\t"
    "v_smfmac_f32_16x16x64_bf16 v[116:119], v[120:123], v[84:91], v124\n\t"
    "ds_read_b64 v[84:85], v100 offset:0\n\t"
    "v_mfma_f32_16x16x32_bf16 v[80:83], v[64:67], v[156:159], 0\n\t"
    "ds_read_b64 v[86:87], v126 offset:0\n\t"
    "v_mfma_f32_16x16x32_bf16 v[80:83], v[68:71], v[160:163], v[80:83]\n\t"
    "v_add_u32_sdwa v90, s92, v101 dst_sel:DWORD dst_unused:UNUSED_PAD src0_sel:DWORD src1_sel:WORD_1\n\t"
    "v_add_u32_sdwa v101, s92, v101 dst_sel:DWORD dst_unused:UNUSED_PAD src0_sel:DWORD src1_sel:WORD_0\n\t"
    "ds_read_b64 v[88:89], v101 offset:0\n\t"
    "v_mfma_f32_16x16x32_bf16 v[80:83], v[72:75], v[140:143], v[80:83]\n\t"
    "ds_read_b64 v[90:91], v90 offset:0\n\t"
    "v_mfma_f32_16x16x32_bf16 v[80:83], v[76:79], v[144:147], v[80:83]\n\t"
    "v_add_u32_sdwa v126, s92, v102 dst_sel:DWORD dst_unused:UNUSED_PAD src0_sel:DWORD src1_sel:WORD_1\n\t"
    "v_add_u32_sdwa v102, s92, v102 dst_sel:DWORD dst_unused:UNUSED_PAD src0_sel:DWORD src1_sel:WORD_0\n\t"
    "s_waitcnt vmcnt(2) lgkmcnt(4)\n\t"
    "v_smfmac_f32_16x16x64_bf16 v[116:119], v[120:123], v[92:99], v124\n\t"
    "ds_read_b64 v[92:93], v102 offset:0\n\t"
    "ds_read_b64 v[94:95], v126 offset:0\n\t"
    "v_add_u32_sdwa v98, s92, v103 dst_sel:DWORD dst_unused:UNUSED_PAD src0_sel:DWORD src1_sel:WORD_1\n\t"
    "v_add_u32_sdwa v103, s92, v103 dst_sel:DWORD dst_unused:UNUSED_PAD src0_sel:DWORD src1_sel:WORD_0\n\t"
    "ds_read_b64 v[96:97], v103 offset:0\n\t"
    "ds_read_b64 v[98:99], v98 offset:0\n\t"
    "s_add_u32 s84, s84, s94\n\t"
    "buffer_load_dwordx4 v[100:103], v127, s[28:31], s84 offen offset:0 sc1\n\t"
    "v_add_u32_sdwa v126, s92, v104 dst_sel:DWORD dst_unused:UNUSED_PAD src0_sel:DWORD src1_sel:WORD_1\n\t"
    "v_cvt_pk_bf16_f32 v46, v116, v117\n\t"
    "v_cvt_pk_bf16_f32 v47, v118, v119\n\t"
    "v_and_b32 v119, s67, v53\n\t"
    "v_lshlrev_b32 v118, 16, v53\n\t"
    "v_and_b32 v117, s67, v52\n\t"
    "v_lshlrev_b32 v116, 16, v52\n\t"
    "v_add_u32_sdwa v104, s92, v104 dst_sel:DWORD dst_unused:UNUSED_PAD src0_sel:DWORD src1_sel:WORD_0\n\t"
    "s_waitcnt lgkmcnt(4)\n\t"
    "v_smfmac_f32_16x16x64_bf16 v[116:119], v[120:123], v[84:91], v124\n\t"
    "ds_read_b64 v[84:85], v104 offset:0\n\t"
    "ds_read_b64 v[86:87], v126 offset:0\n\t"
    "v_cvt_pk_bf16_f32 v80, v80, v81\n\t"
    "v_cvt_pk_bf16_f32 v81, v82, v83\n\t"
    "ds_write_b64 v125, v[80:81] offset:768\n\t"
    "v_add_u32_sdwa v90, s92, v105 dst_sel:DWORD dst_unused:UNUSED_PAD src0_sel:DWORD src1_sel:WORD_1\n\t"
    "v_add_u32_sdwa v105, s92, v105 dst_sel:DWORD dst_unused:UNUSED_PAD src0_sel:DWORD src1_sel:WORD_0\n\t"
    "ds_read_b64 v[88:89], v105 offset:0\n\t"
    "ds_read_b64 v[90:91], v90 offset:0\n\t"
    "v_add_u32_sdwa v126, s92, v106 dst_sel:DWORD dst_unused:UNUSED_PAD src0_sel:DWORD src1_sel:WORD_1\n\t"
    "v_add_u32_sdwa v106, s92, v106 dst_sel:DWORD dst_unused:UNUSED_PAD src0_sel:DWORD src1_sel:WORD_0\n\t"
    "s_waitcnt vmcnt(2) lgkmcnt(5)\n\t"
    "v_smfmac_f32_16x16x64_bf16 v[116:119], v[120:123], v[92:99], v124\n\t"
    "ds_read_b64 v[92:93], v106 offset:0\n\t"
    "ds_read_b64 v[94:95], v126 offset:0\n\t"
    "v_add_u32_sdwa v98, s92, v107 dst_sel:DWORD dst_unused:UNUSED_PAD src0_sel:DWORD src1_sel:WORD_1\n\t"
    "v_add_u32_sdwa v107, s92, v107 dst_sel:DWORD dst_unused:UNUSED_PAD src0_sel:DWORD src1_sel:WORD_0\n\t"
    "ds_read_b64 v[96:97], v107 offset:0\n\t"
    "ds_read_b64 v[98:99], v98 offset:0\n\t"
    "buffer_load_dwordx4 v[104:107], v127, s[28:31], s84 offen offset:1024 sc1\n\t"
    "v_add_u32_sdwa v126, s92, v108 dst_sel:DWORD dst_unused:UNUSED_PAD src0_sel:DWORD src1_sel:WORD_1\n\t"
    "v_cvt_pk_bf16_f32 v52, v116, v117\n\t"
    "v_cvt_pk_bf16_f32 v53, v118, v119\n\t"
    "v_and_b32 v119, s67, v55\n\t"
    "v_lshlrev_b32 v118, 16, v55\n\t"
    "v_and_b32 v117, s67, v54\n\t"
    "v_lshlrev_b32 v116, 16, v54\n\t"
    "v_add_u32_sdwa v108, s92, v108 dst_sel:DWORD dst_unused:UNUSED_PAD src0_sel:DWORD src1_sel:WORD_0\n\t"
    "s_waitcnt lgkmcnt(4)\n\t"
    "v_smfmac_f32_16x16x64_bf16 v[116:119], v[120:123], v[84:91], v124\n\t"
    "ds_read_b64 v[84:85], v108 offset:0\n\t"
    "v_mfma_f32_16x16x32_bf16 v[80:83], v[64:67], v[212:215], 0\n\t"
    "ds_read_b64 v[86:87], v126 offset:0\n\t"
    "v_mfma_f32_16x16x32_bf16 v[80:83], v[68:71], v[216:219], v[80:83]\n\t"
    "v_add_u32_sdwa v90, s92, v109 dst_sel:DWORD dst_unused:UNUSED_PAD src0_sel:DWORD src1_sel:WORD_1\n\t"
    "v_add_u32_sdwa v109, s92, v109 dst_sel:DWORD dst_unused:UNUSED_PAD src0_sel:DWORD src1_sel:WORD_0\n\t"
    "ds_read_b64 v[88:89], v109 offset:0\n\t"
    "v_mfma_f32_16x16x32_bf16 v[80:83], v[72:75], v[148:151], v[80:83]\n\t"
    "ds_read_b64 v[90:91], v90 offset:0\n\t"
    "v_mfma_f32_16x16x32_bf16 v[80:83], v[76:79], v[152:155], v[80:83]\n\t"
    "v_add_u32 v79, s96, v127\n\t"
    "s_add_u32 s96, s96, 0x1000\n\t"
    "s_and_b32 s96, s96, 0x3000\n\t"
    "v_add_u32 v79, 128000, v79\n\t"
    "ds_read_b128 v[64:67], v79 offset:0\n\t"
    "ds_read_b128 v[68:71], v79 offset:1024\n\t"
    "ds_read_b128 v[72:75], v79 offset:2048\n\t"
    "ds_read_b128 v[76:79], v79 offset:3072\n\t"
    "v_add_u32_sdwa v126, s92, v110 dst_sel:DWORD dst_unused:UNUSED_PAD src0_sel:DWORD src1_sel:WORD_1\n\t"
    "v_add_u32_sdwa v110, s92, v110 dst_sel:DWORD dst_unused:UNUSED_PAD src0_sel:DWORD src1_sel:WORD_0\n\t"
    "s_waitcnt vmcnt(2) lgkmcnt(8)\n\t"
    "v_smfmac_f32_16x16x64_bf16 v[116:119], v[120:123], v[92:99], v124\n\t"
    "ds_read_b64 v[92:93], v110 offset:0\n\t"
    "ds_read_b64 v[94:95], v126 offset:0\n\t"
    "v_add_u32_sdwa v98, s92, v111 dst_sel:DWORD dst_unused:UNUSED_PAD src0_sel:DWORD src1_sel:WORD_1\n\t"
    "v_add_u32_sdwa v111, s92, v111 dst_sel:DWORD dst_unused:UNUSED_PAD src0_sel:DWORD src1_sel:WORD_0\n\t"
    "ds_read_b64 v[96:97], v111 offset:0\n\t"
    "ds_read_b64 v[98:99], v98 offset:0\n\t"
    "s_add_u32 s85, s85, s94\n\t"
    "buffer_load_dwordx4 v[108:111], v127, s[28:31], s85 offen offset:0 sc1\n\t"
    "v_add_u32_sdwa v126, s92, v112 dst_sel:DWORD dst_unused:UNUSED_PAD src0_sel:DWORD src1_sel:WORD_1\n\t"
    "v_cvt_pk_bf16_f32 v54, v116, v117\n\t"
    "v_cvt_pk_bf16_f32 v55, v118, v119\n\t"
    "v_and_b32 v119, s67, v61\n\t"
    "v_lshlrev_b32 v118, 16, v61\n\t"
    "v_and_b32 v117, s67, v60\n\t"
    "v_lshlrev_b32 v116, 16, v60\n\t"
    "v_add_u32_sdwa v112, s92, v112 dst_sel:DWORD dst_unused:UNUSED_PAD src0_sel:DWORD src1_sel:WORD_0\n\t"
    "s_waitcnt lgkmcnt(8)\n\t"
    "v_smfmac_f32_16x16x64_bf16 v[116:119], v[120:123], v[84:91], v124\n\t"
    "ds_read_b64 v[84:85], v112 offset:0\n\t"
    "ds_read_b64 v[86:87], v126 offset:0\n\t"
    "v_cvt_pk_bf16_f32 v80, v80, v81\n\t"
    "v_cvt_pk_bf16_f32 v81, v82, v83\n\t"
    "ds_write_b64 v125, v[80:81] offset:872\n\t"
    "v_add_u32_sdwa v90, s92, v113 dst_sel:DWORD dst_unused:UNUSED_PAD src0_sel:DWORD src1_sel:WORD_1\n\t"
    "v_add_u32_sdwa v113, s92, v113 dst_sel:DWORD dst_unused:UNUSED_PAD src0_sel:DWORD src1_sel:WORD_0\n\t"
    "ds_read_b64 v[88:89], v113 offset:0\n\t"
    "ds_read_b64 v[90:91], v90 offset:0\n\t"
    "v_add_u32_sdwa v126, s92, v114 dst_sel:DWORD dst_unused:UNUSED_PAD src0_sel:DWORD src1_sel:WORD_1\n\t"
    "v_add_u32_sdwa v114, s92, v114 dst_sel:DWORD dst_unused:UNUSED_PAD src0_sel:DWORD src1_sel:WORD_0\n\t"
    "s_waitcnt vmcnt(2) lgkmcnt(5)\n\t"
    "v_smfmac_f32_16x16x64_bf16 v[116:119], v[120:123], v[92:99], v124\n\t"
    "ds_read_b64 v[92:93], v114 offset:0\n\t"
    "ds_read_b64 v[94:95], v126 offset:0\n\t"
    "v_add_u32_sdwa v98, s92, v115 dst_sel:DWORD dst_unused:UNUSED_PAD src0_sel:DWORD src1_sel:WORD_1\n\t"
    "v_add_u32_sdwa v115, s92, v115 dst_sel:DWORD dst_unused:UNUSED_PAD src0_sel:DWORD src1_sel:WORD_0\n\t"
    "ds_read_b64 v[96:97], v115 offset:0\n\t"
    "ds_read_b64 v[98:99], v98 offset:0\n\t"
    "buffer_load_dwordx4 v[112:115], v127, s[28:31], s85 offen offset:1024 sc1\n\t"
    "v_add_u32_sdwa v126, s92, v100 dst_sel:DWORD dst_unused:UNUSED_PAD src0_sel:DWORD src1_sel:WORD_1\n\t"
    "v_cvt_pk_bf16_f32 v60, v116, v117\n\t"
    "v_cvt_pk_bf16_f32 v61, v118, v119\n\t"
    "v_and_b32 v119, s67, v63\n\t"
    "v_lshlrev_b32 v118, 16, v63\n\t"
    "v_and_b32 v117, s67, v62\n\t"
    "v_lshlrev_b32 v116, 16, v62\n\t"
    "v_add_u32_sdwa v100, s92, v100 dst_sel:DWORD dst_unused:UNUSED_PAD src0_sel:DWORD src1_sel:WORD_0\n\t"
    "s_waitcnt lgkmcnt(4)\n\t"
    "v_smfmac_f32_16x16x64_bf16 v[116:119], v[120:123], v[84:91], v124\n\t"
    "ds_read_b64 v[84:85], v100 offset:0\n\t"
    "ds_read_b64 v[86:87], v126 offset:0\n\t"
    "v_add_u32_sdwa v90, s92, v101 dst_sel:DWORD dst_unused:UNUSED_PAD src0_sel:DWORD src1_sel:WORD_1\n\t"
    "v_add_u32_sdwa v101, s92, v101 dst_sel:DWORD dst_unused:UNUSED_PAD src0_sel:DWORD src1_sel:WORD_0\n\t"
    "ds_read_b64 v[88:89], v101 offset:0\n\t"
    "ds_read_b64 v[90:91], v90 offset:0\n\t"
    "v_add_u32_sdwa v126, s92, v102 dst_sel:DWORD dst_unused:UNUSED_PAD src0_sel:DWORD src1_sel:WORD_1\n\t"
    "v_add_u32_sdwa v102, s92, v102 dst_sel:DWORD dst_unused:UNUSED_PAD src0_sel:DWORD src1_sel:WORD_0\n\t"
    "s_cmp_lt_u32 s34, 4\n\t"
    "s_cbranch_scc1 L_tp31_%=\n\t"
    "s_setprio 1\n\t"
    "L_tp31_%=:\n\t"
    "s_waitcnt vmcnt(2) lgkmcnt(4)\n\t"
    "v_smfmac_f32_16x16x64_bf16 v[116:119], v[120:123], v[92:99], v124\n\t"
    "ds_read_b64 v[92:93], v102 offset:0\n\t"
    "ds_read_b64 v[94:95], v126 offset:0\n\t"
    "v_add_u32_sdwa v98, s92, v103 dst_sel:DWORD dst_unused:UNUSED_PAD src0_sel:DWORD src1_sel:WORD_1\n\t"
    "v_add_u32_sdwa v103, s92, v103 dst_sel:DWORD dst_unused:UNUSED_PAD src0_sel:DWORD src1_sel:WORD_0\n\t"
    "ds_read_b64 v[96:97], v103 offset:0\n\t"
    "ds_read_b64 v[98:99], v98 offset:0\n\t"
    "s_add_u32 s86, s86, s94\n\t"
    "buffer_load_dwordx4 v[100:103], v127, s[28:31], s86 offen offset:0 sc1\n\t"
    "v_add_u32_sdwa v126, s92, v104 dst_sel:DWORD dst_unused:UNUSED_PAD src0_sel:DWORD src1_sel:WORD_1\n\t"
    "v_cvt_pk_bf16_f32 v62, v116, v117\n\t"
    "v_cvt_pk_bf16_f32 v63, v118, v119\n\t"
    "v_and_b32 v119, s67, v1\n\t"
    "v_lshlrev_b32 v118, 16, v1\n\t"
    "v_and_b32 v117, s67, v0\n\t"
    "v_lshlrev_b32 v116, 16, v0\n\t"
    "v_add_u32_sdwa v104, s92, v104 dst_sel:DWORD dst_unused:UNUSED_PAD src0_sel:DWORD src1_sel:WORD_0\n\t"
    "s_waitcnt lgkmcnt(4)\n\t"
    "v_smfmac_f32_16x16x64_bf16 v[116:119], v[120:123], v[84:91], v124\n\t"
    "ds_read_b64 v[84:85], v104 offset:0\n\t"
    "v_mfma_f32_16x16x32_bf16 v[80:83], v[64:67], v[164:167], 0\n\t"
    "ds_read_b64 v[86:87], v126 offset:0\n\t"
    "v_mfma_f32_16x16x32_bf16 v[80:83], v[68:71], v[168:171], v[80:83]\n\t"
    "v_add_u32_sdwa v90, s92, v105 dst_sel:DWORD dst_unused:UNUSED_PAD src0_sel:DWORD src1_sel:WORD_1\n\t"
    "v_add_u32_sdwa v105, s92, v105 dst_sel:DWORD dst_unused:UNUSED_PAD src0_sel:DWORD src1_sel:WORD_0\n\t"
    "ds_read_b64 v[88:89], v105 offset:0\n\t"
    "v_mfma_f32_16x16x32_bf16 v[80:83], v[72:75], v[220:223], v[80:83]\n\t"
    "ds_read_b64 v[90:91], v90 offset:0\n\t"
    "v_mfma_f32_16x16x32_bf16 v[80:83], v[76:79], v[224:227], v[80:83]\n\t"
    "v_add_u32_sdwa v126, s92, v106 dst_sel:DWORD dst_unused:UNUSED_PAD src0_sel:DWORD src1_sel:WORD_1\n\t"
    "v_add_u32_sdwa v106, s92, v106 dst_sel:DWORD dst_unused:UNUSED_PAD src0_sel:DWORD src1_sel:WORD_0\n\t"
    "s_waitcnt vmcnt(2) lgkmcnt(4)\n\t"
    "v_smfmac_f32_16x16x64_bf16 v[116:119], v[120:123], v[92:99], v124\n\t"
    "ds_read_b64 v[92:93], v106 offset:0\n\t"
    "ds_read_b64 v[94:95], v126 offset:0\n\t"
    "v_add_u32_sdwa v98, s92, v107 dst_sel:DWORD dst_unused:UNUSED_PAD src0_sel:DWORD src1_sel:WORD_1\n\t"
    "v_add_u32_sdwa v107, s92, v107 dst_sel:DWORD dst_unused:UNUSED_PAD src0_sel:DWORD src1_sel:WORD_0\n\t"
    "ds_read_b64 v[96:97], v107 offset:0\n\t"
    "ds_read_b64 v[98:99], v98 offset:0\n\t"
    "buffer_load_dwordx4 v[104:107], v127, s[28:31], s86 offen offset:1024 sc1\n\t"
    "v_add_u32_sdwa v126, s92, v108 dst_sel:DWORD dst_unused:UNUSED_PAD src0_sel:DWORD src1_sel:WORD_1\n\t"
    "v_cvt_pk_bf16_f32 v0, v116, v117\n\t"
    "v_cvt_pk_bf16_f32 v1, v118, v119\n\t"
    "v_and_b32 v119, s67, v3\n\t"
    "v_lshlrev_b32 v118, 16, v3\n\t"
    "v_and_b32 v117, s67, v2\n\t"
    "v_lshlrev_b32 v116, 16, v2\n\t"
    "v_add_u32_sdwa v108, s92, v108 dst_sel:DWORD dst_unused:UNUSED_PAD src0_sel:DWORD src1_sel:WORD_0\n\t"
    "s_waitcnt lgkmcnt(4)\n\t"
    "v_smfmac_f32_16x16x64_bf16 v[116:119], v[120:123], v[84:91], v124\n\t"
    "ds_read_b64 v[84:85], v108 offset:0\n\t"
    "ds_read_b64 v[86:87], v126 offset:0\n\t"
    "v_cvt_pk_bf16_f32 v80, v80, v81\n\t"
    "v_cvt_pk_bf16_f32 v81, v82, v83\n\t"
    "ds_write_b64 v125, v[80:81] offset:32000\n\t"
    "v_mfma_f32_16x16x32_bf16 v[80:83], v[64:67], v[172:175], 0\n\t"
    "v_add_u32_sdwa v90, s92, v109 dst_sel:DWORD dst_unused:UNUSED_PAD src0_sel:DWORD src1_sel:WORD_1\n\t"
    "v_add_u32_sdwa v109, s92, v109 dst_sel:DWORD dst_unused:UNUSED_PAD src0_sel:DWORD src1_sel:WORD_0\n\t"
    "ds_read_b64 v[88:89], v109 offset:0\n\t"
    "v_mfma_f32_16x16x32_bf16 v[80:83], v[68:71], v[176:179], v[80:83]\n\t"
    "ds_read_b64 v[90:91], v90 offset:0\n\t"
    "v_mfma_f32_16x16x32_bf16 v[80:83], v[72:75], v[228:231], v[80:83]\n\t"
    "v_mfma_f32_16x16x32_bf16 v[80:83], v[76:79], v[232:235], v[80:83]\n\t"
    "v_add_u32_sdwa v126, s92, v110 dst_sel:DWORD dst_unused:UNUSED_PAD src0_sel:DWORD src1_sel:WORD_1\n\t"
    "v_add_u32_sdwa v110, s92, v110 dst_sel:DWORD dst_unused:UNUSED_PAD src0_sel:DWORD src1_sel:WORD_0\n\t"
    "s_waitcnt vmcnt(2) lgkmcnt(5)\n\t"
    "v_smfmac_f32_16x16x64_bf16 v[116:119], v[120:123], v[92:99], v124\n\t"
    "ds_read_b64 v[92:93], v110 offset:0\n\t"
    "ds_read_b64 v[94:95], v126 offset:0\n\t"
    "v_add_u32_sdwa v98, s92, v111 dst_sel:DWORD dst_unused:UNUSED_PAD src0_sel:DWORD src1_sel:WORD_1\n\t"
    "v_add_u32_sdwa v111, s92, v111 dst_sel:DWORD dst_unused:UNUSED_PAD src0_sel:DWORD src1_sel:WORD_0\n\t"
    "ds_read_b64 v[96:97], v111 offset:0\n\t"
    "ds_read_b64 v[98:99], v98 offset:0\n\t"
    "s_add_u32 s87, s87, s94\n\t"
    "buffer_load_dwordx4 v[108:111], v127, s[28:31], s87 offen offset:0 sc1\n\t"
    "v_add_u32_sdwa v126, s92, v112 dst_sel:DWORD dst_unused:UNUSED_PAD src0_sel:DWORD src1_sel:WORD_1\n\t"
    "v_cvt_pk_bf16_f32 v2, v116, v117\n\t"
    "v_cvt_pk_bf16_f32 v3, v118, v119\n\t"
    "v_and_b32 v119, s67, v9\n\t"
    "v_lshlrev_b32 v118, 16, v9\n\t"
    "v_and_b32 v117, s67, v8\n\t"
    "v_lshlrev_b32 v116, 16, v8\n\t"
    "v_add_u32_sdwa v112, s92, v112 dst_sel:DWORD dst_unused:UNUSED_PAD src0_sel:DWORD src1_sel:WORD_0\n\t"
    "s_waitcnt lgkmcnt(4)\n\t"
    "v_smfmac_f32_16x16x64_bf16 v[116:119], v[120:123], v[84:91], v124\n\t"
    "ds_read_b64 v[84:85], v112 offset:0\n\t"
    "ds_read_b64 v[86:87], v126 offset:0\n\t"
    "v_cvt_pk_bf16_f32 v80, v80, v81\n\t"
    "v_cvt_pk_bf16_f32 v81, v82, v83\n\t"
    "ds_write_b64 v125, v[80:81] offset:32128\n\t"
    "v_add_u32_sdwa v90, s92, v113 dst_sel:DWORD dst_unused:UNUSED_PAD src0_sel:DWORD src1_sel:WORD_1\n\t"
    "v_add_u32_sdwa v113, s92, v113 dst_sel:DWORD dst_unused:UNUSED_PAD src0_sel:DWORD src1_sel:WORD_0\n\t"
    "ds_read_b64 v[88:89], v113 offset:0\n\t"
    "ds_read_b64 v[90:91], v90 offset:0\n\t"
    "v_add_u32_sdwa v126, s92, v114 dst_sel:DWORD dst_unused:UNUSED_PAD src0_sel:DWORD src1_sel:WORD_1\n\t"
    "v_add_u32_sdwa v114, s92, v114 dst_sel:DWORD dst_unused:UNUSED_PAD src0_sel:DWORD src1_sel:WORD_0\n\t"
    "s_waitcnt vmcnt(2) lgkmcnt(5)\n\t"
    "v_smfmac_f32_16x16x64_bf16 v[116:119], v[120:123], v[92:99], v124\n\t"
    "ds_read_b64 v[92:93], v114 offset:0\n\t"
    "ds_read_b64 v[94:95], v126 offset:0\n\t"
    "v_add_u32_sdwa v98, s92, v115 dst_sel:DWORD dst_unused:UNUSED_PAD src0_sel:DWORD src1_sel:WORD_1\n\t"
    "v_add_u32_sdwa v115, s92, v115 dst_sel:DWORD dst_unused:UNUSED_PAD src0_sel:DWORD src1_sel:WORD_0\n\t"
    "ds_read_b64 v[96:97], v115 offset:0\n\t"
    "ds_read_b64 v[98:99], v98 offset:0\n\t"
    "buffer_load_dwordx4 v[112:115], v127, s[28:31], s87 offen offset:1024 sc1\n\t"
    "v_add_u32_sdwa v126, s92, v100 dst_sel:DWORD dst_unused:UNUSED_PAD src0_sel:DWORD src1_sel:WORD_1\n\t"
    "v_cvt_pk_bf16_f32 v8, v116, v117\n\t"
    "v_cvt_pk_bf16_f32 v9, v118, v119\n\t"
    "v_and_b32 v119, s67, v11\n\t"
    "v_lshlrev_b32 v118, 16, v11\n\t"
    "v_and_b32 v117, s67, v10\n\t"
    "v_lshlrev_b32 v116, 16, v10\n\t"
    "v_add_u32_sdwa v100, s92, v100 dst_sel:DWORD dst_unused:UNUSED_PAD src0_sel:DWORD src1_sel:WORD_0\n\t"
    "s_waitcnt lgkmcnt(4)\n\t"
    "v_smfmac_f32_16x16x64_bf16 v[116:119], v[120:123], v[84:91], v124\n\t"
    "ds_read_b64 v[84:85], v100 offset:0\n\t"
    "v_mfma_f32_16x16x32_bf16 v[80:83], v[64:67], v[180:183], 0\n\t"
    "ds_read_b64 v[86:87], v126 offset:0\n\t"
    "v_mfma_f32_16x16x32_bf16 v[80:83], v[68:71], v[184:187], v[80:83]\n\t"
    "v_add_u32_sdwa v90, s92, v101 dst_sel:DWORD dst_unused:UNUSED_PAD src0_sel:DWORD src1_sel:WORD_1\n\t"
    "v_add_u32_sdwa v101, s92, v101 dst_sel:DWORD dst_unused:UNUSED_PAD src0_sel:DWORD src1_sel:WORD_0\n\t"
    "ds_read_b64 v[88:89], v101 offset:0\n\t"
    "v_mfma_f32_16x16x32_bf16 v[80:83], v[72:75], v[236:239], v[80:83]\n\t"
    "ds_read_b64 v[90:91], v90 offset:0\n\t"
    "v_mfma_f32_16x16x32_bf16 v[80:83], v[76:79], v[240:243], v[80:83]\n\t"
    "v_add_u32_sdwa v126, s92, v102 dst_sel:DWORD dst_unused:UNUSED_PAD src0_sel:DWORD src1_sel:WORD_1\n\t"
    "v_add_u32_sdwa v102, s92, v102 dst_sel:DWORD dst_unused:UNUSED_PAD src0_sel:DWORD src1_sel:WORD_0\n\t"
    "s_waitcnt vmcnt(2) lgkmcnt(4)\n\t"
    "v_smfmac_f32_16x16x64_bf16 v[116:119], v[120:123], v[92:99], v124\n\t"
    "ds_read_b64 v[92:93], v102 offset:0\n\t"
    "ds_read_b64 v[94:95], v126 offset:0\n\t"
    "v_add_u32_sdwa v98, s92, v103 dst_sel:DWORD dst_unused:UNUSED_PAD src0_sel:DWORD src1_sel:WORD_1\n\t"
    "v_add_u32_sdwa v103, s92, v103 dst_sel:DWORD dst_unused:UNUSED_PAD src0_sel:DWORD src1_sel:WORD_0\n\t"
    "ds_read_b64 v[96:97], v103 offset:0\n\t"
    "ds_read_b64 v[98:99], v98 offset:0\n\t"
    "s_add_u32 s88, s88, s94\n\t"
    "buffer_load_dwordx4 v[100:103], v127, s[28:31], s88 offen offset:0 sc1\n\t"
    "v_add_u32_sdwa v126, s92, v104 dst_sel:DWORD dst_unused:UNUSED_PAD src0_sel:DWORD src1_sel:WORD_1\n\t"
    "v_cvt_pk_bf16_f32 v10, v116, v117\n\t"
    "v_cvt_pk_bf16_f32 v11, v118, v119\n\t"
    "v_and_b32 v119, s67, v17\n\t"
    "v_lshlrev_b32 v118, 16, v17\n\t"
    "v_and_b32 v117, s67, v16\n\t"
    "v_lshlrev_b32 v116, 16, v16\n\t"
    "v_add_u32_sdwa v104, s92, v104 dst_sel:DWORD dst_unused:UNUSED_PAD src0_sel:DWORD src1_sel:WORD_0\n\t"
    "s_waitcnt lgkmcnt(4)\n\t"
    "v_smfmac_f32_16x16x64_bf16 v[116:119], v[120:123], v[84:91], v124\n\t"
    "ds_read_b64 v[84:85], v104 offset:0\n\t"
    "ds_read_b64 v[86:87], v126 offset:0\n\t"
    "v_cvt_pk_bf16_f32 v80, v80, v81\n\t"
    "v_cvt_pk_bf16_f32 v81, v82, v83\n\t"
    "ds_write_b64 v125, v[80:81] offset:32256\n\t"
    "v_add_u32_sdwa v90, s92, v105 dst_sel:DWORD dst_unused:UNUSED_PAD src0_sel:DWORD src1_sel:WORD_1\n\t"
    "v_add_u32_sdwa v105, s92, v105 dst_sel:DWORD dst_unused:UNUSED_PAD src0_sel:DWORD src1_sel:WORD_0\n\t"
    "ds_read_b64 v[88:89], v105 offset:0\n\t"
    "ds_read_b64 v[90:91], v90 offset:0\n\t"
    "v_add_u32_sdwa v126, s92, v106 dst_sel:DWORD dst_unused:UNUSED_PAD src0_sel:DWORD src1_sel:WORD_1\n\t"
    "v_add_u32_sdwa v106, s92, v106 dst_sel:DWORD dst_unused:UNUSED_PAD src0_sel:DWORD src1_sel:WORD_0\n\t"
    "s_waitcnt vmcnt(2) lgkmcnt(5)\n\t"
    "v_smfmac_f32_16x16x64_bf16 v[116:119], v[120:123], v[92:99], v124\n\t"
    "ds_read_b64 v[92:93], v106 offset:0\n\t"
    "ds_read_b64 v[94:95], v126 offset:0\n\t"
    "v_add_u32_sdwa v98, s92, v107 dst_sel:DWORD dst_unused:UNUSED_PAD src0_sel:DWORD src1_sel:WORD_1\n\t"
    "v_add_u32_sdwa v107, s92, v107 dst_sel:DWORD dst_unused:UNUSED_PAD src0_sel:DWORD src1_sel:WORD_0\n\t"
    "ds_read_b64 v[96:97], v107 offset:0\n\t"
    "ds_read_b64 v[98:99], v98 offset:0\n\t"
    "buffer_load_dwordx4 v[104:107], v127, s[28:31], s88 offen offset:1024 sc1\n\t"
    "v_add_u32_sdwa v126, s92, v108 dst_sel:DWORD dst_unused:UNUSED_PAD src0_sel:DWORD src1_sel:WORD_1\n\t"
    "v_cvt_pk_bf16_f32 v16, v116, v117\n\t"
    "v_cvt_pk_bf16_f32 v17, v118, v119\n\t"
    "v_and_b32 v119, s67, v19\n\t"
    "v_lshlrev_b32 v118, 16, v19\n\t"
    "v_and_b32 v117, s67, v18\n\t"
    "v_lshlrev_b32 v116, 16, v18\n\t"
    "v_add_u32_sdwa v108, s92, v108 dst_sel:DWORD dst_unused:UNUSED_PAD src0_sel:DWORD src1_sel:WORD_0\n\t"
    "s_waitcnt lgkmcnt(4)\n\t"
    "v_smfmac_f32_16x16x64_bf16 v[116:119], v[120:123], v[84:91], v124\n\t"
    "ds_read_b64 v[84:85], v108 offset:0\n\t"
    "v_mfma_f32_16x16x32_bf16 v[80:83], v[64:67], v[188:191], 0\n\t"
    "ds_read_b64 v[86:87], v126 offset:0\n\t"
    "v_mfma_f32_16x16x32_bf16 v[80:83], v[68:71], v[192:195], v[80:83]\n\t"
    "v_add_u32_sdwa v90, s92, v109 dst_sel:DWORD dst_unused:UNUSED_PAD src0_sel:DWORD src1_sel:WORD_1\n\t"
    "v_add_u32_sdwa v109, s92, v109 dst_sel:DWORD dst_unused:UNUSED_PAD src0_sel:DWORD src1_sel:WORD_0\n\t"
    "ds_read_b64 v[88:89], v109 offset:0\n\t"
    "v_mfma_f32_16x16x32_bf16 v[80:83], v[72:75], v[244:247], v[80:83]\n\t"
    "ds_read_b64 v[90:91], v90 offset:0\n\t"
    "v_mfma_f32_16x16x32_bf16 v[80:83], v[76:79], v[248:251], v[80:83]\n\t"
    "v_add_u32_sdwa v126, s92, v110 dst_sel:DWORD dst_unused:UNUSED_PAD src0_sel:DWORD src1_sel:WORD_1\n\t"
    "v_add_u32_sdwa v110, s92, v110 dst_sel:DWORD dst_unused:UNUSED_PAD src0_sel:DWORD src1_sel:WORD_0\n\t"
    "s_waitcnt vmcnt(2) lgkmcnt(4)\n\t"
    "v_smfmac_f32_16x16x64_bf16 v[116:119], v[120:123], v[92:99], v124\n\t"
    "ds_read_b64 v[92:93], v110 offset:0\n\t"
    "ds_read_b64 v[94:95], v126 offset:0\n\t"
    "v_add_u32_sdwa v98, s92, v111 dst_sel:DWORD dst_unused:UNUSED_PAD src0_sel:DWORD src1_sel:WORD_1\n\t"
    "v_add_u32_sdwa v111, s92, v111 dst_sel:DWORD dst_unused:UNUSED_PAD src0_sel:DWORD src1_sel:WORD_0\n\t"
    "ds_read_b64 v[96:97], v111 offset:0\n\t"
    "ds_read_b64 v[98:99], v98 offset:0\n\t"
    "s_add_u32 s89, s89, s94\n\t"
    "buffer_load_dwordx4 v[108:111], v127, s[28:31], s89 offen offset:0 sc1\n\t"
    "v_add_u32_sdwa v126, s92, v112 dst_sel:DWORD dst_unused:UNUSED_PAD src0_sel:DWORD src1_sel:WORD_1\n\t"
    "v_cvt_pk_bf16_f32 v18, v116, v117\n\t"
    "v_cvt_pk_bf16_f32 v19, v118, v119\n\t"
    "v_and_b32 v119, s67, v25\n\t"
    "v_lshlrev_b32 v118, 16, v25\n\t"
    "v_and_b32 v117, s67, v24\n\t"
    "v_lshlrev_b32 v116, 16, v24\n\t"
    "v_add_u32_sdwa v112, s92, v112 dst_sel:DWORD dst_unused:UNUSED_PAD src0_sel:DWORD src1_sel:WORD_0\n\t"
    "s_waitcnt lgkmcnt(4)\n\t"
    "v_smfmac_f32_16x16x64_bf16 v[116:119], v[120:123], v[84:91], v124\n\t"
    "ds_read_b64 v[84:85], v112 offset:0\n\t"
    "ds_read_b64 v[86:87], v126 offset:0\n\t"
    "v_cvt_pk_bf16_f32 v80, v80, v81\n\t"
    "v_cvt_pk_bf16_f32 v81, v82, v83\n\t"
    "ds_write_b64 v125, v[80:81] offset:32384\n\t"
    "v_add_u32_sdwa v90, s92, v113 dst_sel:DWORD dst_unused:UNUSED_PAD src0_sel:DWORD src1_sel:WORD_1\n\t"
    "v_add_u32_sdwa v113, s92, v113 dst_sel:DWORD dst_unused:UNUSED_PAD src0_sel:DWORD src1_sel:WORD_0\n\t"
    "ds_read_b64 v[88:89], v113 offset:0\n\t"
    "ds_read_b64 v[90:91], v90 offset:0\n\t"
    "v_add_u32_sdwa v126, s92, v114 dst_sel:DWORD dst_unused:UNUSED_PAD src0_sel:DWORD src1_sel:WORD_1\n\t"
    "v_add_u32_sdwa v114, s92, v114 dst_sel:DWORD dst_unused:UNUSED_PAD src0_sel:DWORD src1_sel:WORD_0\n\t"
    "s_waitcnt vmcnt(2) lgkmcnt(5)\n\t"
    "v_smfmac_f32_16x16x64_bf16 v[116:119], v[120:123], v[92:99], v124\n\t"
    "ds_read_b64 v[92:93], v114 offset:0\n\t"
    "ds_read_b64 v[94:95], v126 offset:0\n\t"
    "v_add_u32_sdwa v98, s92, v115 dst_sel:DWORD dst_unused:UNUSED_PAD src0_sel:DWORD src1_sel:WORD_1\n\t"
    "v_add_u32_sdwa v115, s92, v115 dst_sel:DWORD dst_unused:UNUSED_PAD src0_sel:DWORD src1_sel:WORD_0\n\t"
    "ds_read_b64 v[96:97], v115 offset:0\n\t"
    "ds_read_b64 v[98:99], v98 offset:0\n\t"
    "buffer_load_dwordx4 v[112:115], v127, s[28:31], s89 offen offset:1024 sc1\n\t"
    "v_add_u32_sdwa v126, s92, v100 dst_sel:DWORD dst_unused:UNUSED_PAD src0_sel:DWORD src1_sel:WORD_1\n\t"
    "v_cvt_pk_bf16_f32 v24, v116, v117\n\t"
    "v_cvt_pk_bf16_f32 v25, v118, v119\n\t"
    "v_and_b32 v119, s67, v27\n\t"
    "v_lshlrev_b32 v118, 16, v27\n\t"
    "v_and_b32 v117, s67, v26\n\t"
    "v_lshlrev_b32 v116, 16, v26\n\t"
    "v_add_u32_sdwa v100, s92, v100 dst_sel:DWORD dst_unused:UNUSED_PAD src0_sel:DWORD src1_sel:WORD_0\n\t"
    "s_waitcnt lgkmcnt(4)\n\t"
    "v_smfmac_f32_16x16x64_bf16 v[116:119], v[120:123], v[84:91], v124\n\t"
    "ds_read_b64 v[84:85], v100 offset:0\n\t"
    "v_mfma_f32_16x16x32_bf16 v[80:83], v[64:67], v[196:199], 0\n\t"
    "ds_read_b64 v[86:87], v126 offset:0\n\t"
    "v_mfma_f32_16x16x32_bf16 v[80:83], v[68:71], v[200:203], v[80:83]\n\t"
    "v_add_u32_sdwa v90, s92, v101 dst_sel:DWORD dst_unused:UNUSED_PAD src0_sel:DWORD src1_sel:WORD_1\n\t"
    "v_add_u32_sdwa v101, s92, v101 dst_sel:DWORD dst_unused:UNUSED_PAD src0_sel:DWORD src1_sel:WORD_0\n\t"
    "ds_read_b64 v[88:89], v101 offset:0\n\t"
    "v_mfma_f32_16x16x32_bf16 v[80:83], v[72:75], v[252:255], v[80:83]\n\t"
    "ds_read_b64 v[90:91], v90 offset:0\n\t"
    "v_mfma_f32_16x16x32_bf16 v[80:83], v[76:79], v[128:131], v[80:83]\n\t"
    "v_add_u32_sdwa v126, s92, v102 dst_sel:DWORD dst_unused:UNUSED_PAD src0_sel:DWORD src1_sel:WORD_1\n\t"
    "v_add_u32_sdwa v102, s92, v102 dst_sel:DWORD dst_unused:UNUSED_PAD src0_sel:DWORD src1_sel:WORD_0\n\t"
    "s_waitcnt vmcnt(2) lgkmcnt(4)\n\t"
    "v_smfmac_f32_16x16x64_bf16 v[116:119], v[120:123], v[92:99], v124\n\t"
    "ds_read_b64 v[92:93], v102 offset:0\n\t"
    "ds_read_b64 v[94:95], v126 offset:0\n\t"
    "v_add_u32_sdwa v98, s92, v103 dst_sel:DWORD dst_unused:UNUSED_PAD src0_sel:DWORD src1_sel:WORD_1\n\t"
    "v_add_u32_sdwa v103, s92, v103 dst_sel:DWORD dst_unused:UNUSED_PAD src0_sel:DWORD src1_sel:WORD_0\n\t"
    "ds_read_b64 v[96:97], v103 offset:0\n\t"
    "ds_read_b64 v[98:99], v98 offset:0\n\t"
    "s_add_u32 s90, s90, s94\n\t"
    "buffer_load_dwordx4 v[100:103], v127, s[28:31], s90 offen offset:0 sc1\n\t"
    "v_add_u32_sdwa v126, s92, v104 dst_sel:DWORD dst_unused:UNUSED_PAD src0_sel:DWORD src1_sel:WORD_1\n\t"
    "v_cvt_pk_bf16_f32 v26, v116, v117\n\t"
    "v_cvt_pk_bf16_f32 v27, v118, v119\n\t"
    "v_and_b32 v119, s67, v33\n\t"
    "v_lshlrev_b32 v118, 16, v33\n\t"
    "v_and_b32 v117, s67, v32\n\t"
    "v_lshlrev_b32 v116, 16, v32\n\t"
    "v_add_u32_sdwa v104, s92, v104 dst_sel:DWORD dst_unused:UNUSED_PAD src0_sel:DWORD src1_sel:WORD_0\n\t"
    "s_waitcnt lgkmcnt(4)\n\t"
    "v_smfmac_f32_16x16x64_bf16 v[116:119], v[120:123], v[84:91], v124\n\t"
    "ds_read_b64 v[84:85], v104 offset:0\n\t"
    "ds_read_b64 v[86:87], v126 offset:0\n\t"
    "v_cvt_pk_bf16_f32 v80, v80, v81\n\t"
    "v_cvt_pk_bf16_f32 v81, v82, v83\n\t"
    "ds_write_b64 v125, v[80:81] offset:32512\n\t"
    "v_add_u32_sdwa v90, s92, v105 dst_sel:DWORD dst_unused:UNUSED_PAD src0_sel:DWORD src1_sel:WORD_1\n\t"
    "v_add_u32_sdwa v105, s92, v105 dst_sel:DWORD dst_unused:UNUSED_PAD src0_sel:DWORD src1_sel:WORD_0\n\t"
    "ds_read_b64 v[88:89], v105 offset:0\n\t"
    "ds_read_b64 v[90:91], v90 offset:0\n\t"
    "v_add_u32_sdwa v126, s92, v106 dst_sel:DWORD dst_unused:UNUSED_PAD src0_sel:DWORD src1_sel:WORD_1\n\t"
    "v_add_u32_sdwa v106, s92, v106 dst_sel:DWORD dst_unused:UNUSED_PAD src0_sel:DWORD src1_sel:WORD_0\n\t"
    "s_waitcnt vmcnt(2) lgkmcnt(5)\n\t"
    "v_smfmac_f32_16x16x64_bf16 v[116:119], v[120:123], v[92:99], v124\n\t"
    "ds_read_b64 v[92:93], v106 offset:0\n\t"
    "ds_read_b64 v[94:95], v126 offset:0\n\t"
    "v_add_u32_sdwa v98, s92, v107 dst_sel:DWORD dst_unused:UNUSED_PAD src0_sel:DWORD src1_sel:WORD_1\n\t"
    "v_add_u32_sdwa v107, s92, v107 dst_sel:DWORD dst_unused:UNUSED_PAD src0_sel:DWORD src1_sel:WORD_0\n\t"
    "ds_read_b64 v[96:97], v107 offset:0\n\t"
    "ds_read_b64 v[98:99], v98 offset:0\n\t"
    "buffer_load_dwordx4 v[104:107], v127, s[28:31], s90 offen offset:1024 sc1\n\t"
    "v_add_u32_sdwa v126, s92, v108 dst_sel:DWORD dst_unused:UNUSED_PAD src0_sel:DWORD src1_sel:WORD_1\n\t"
    "v_cvt_pk_bf16_f32 v32, v116, v117\n\t"
    "v_cvt_pk_bf16_f32 v33, v118, v119\n\t"
    "v_and_b32 v119, s67, v35\n\t"
    "v_lshlrev_b32 v118, 16, v35\n\t"
    "v_and_b32 v117, s67, v34\n\t"
    "v_lshlrev_b32 v116, 16, v34\n\t"
    "v_add_u32_sdwa v108, s92, v108 dst_sel:DWORD dst_unused:UNUSED_PAD src0_sel:DWORD src1_sel:WORD_0\n\t"
    "s_waitcnt lgkmcnt(4)\n\t"
    "v_smfmac_f32_16x16x64_bf16 v[116:119], v[120:123], v[84:91], v124\n\t"
    "ds_read_b64 v[84:85], v108 offset:0\n\t"
    "v_mfma_f32_16x16x32_bf16 v[80:83], v[64:67], v[204:207], 0\n\t"
    "ds_read_b64 v[86:87], v126 offset:0\n\t"
    "v_mfma_f32_16x16x32_bf16 v[80:83], v[68:71], v[208:211], v[80:83]\n\t"
    "v_add_u32_sdwa v90, s92, v109 dst_sel:DWORD dst_unused:UNUSED_PAD src0_sel:DWORD src1_sel:WORD_1\n\t"
    "v_add_u32_sdwa v109, s92, v109 dst_sel:DWORD dst_unused:UNUSED_PAD src0_sel:DWORD src1_sel:WORD_0\n\t"
    "ds_read_b64 v[88:89], v109 offset:0\n\t"
    "v_mfma_f32_16x16x32_bf16 v[80:83], v[72:75], v[132:135], v[80:83]\n\t"
    "ds_read_b64 v[90:91], v90 offset:0\n\t"
    "v_mfma_f32_16x16x32_bf16 v[80:83], v[76:79], v[136:139], v[80:83]\n\t"
    "v_add_u32_sdwa v126, s92, v110 dst_sel:DWORD dst_unused:UNUSED_PAD src0_sel:DWORD src1_sel:WORD_1\n\t"
    "v_add_u32_sdwa v110, s92, v110 dst_sel:DWORD dst_unused:UNUSED_PAD src0_sel:DWORD src1_sel:WORD_0\n\t"
    "s_cmp_lt_u32 s34, 4\n\t"
    "s_cbranch_scc1 L_tp51_%=\n\t"
    "s_setprio 0\n\t"
    "L_tp51_%=:\n\t"
    "s_waitcnt vmcnt(2) lgkmcnt(4)\n\t"
    "v_smfmac_f32_16x16x64_bf16 v[116:119], v[120:123], v[92:99], v124\n\t"
    "ds_read_b64 v[92:93], v110 offset:0\n\t"
    "ds_read_b64 v[94:95], v126 offset:0\n\t"
    "v_add_u32_sdwa v98, s92, v111 dst_sel:DWORD dst_unused:UNUSED_PAD src0_sel:DWORD src1_sel:WORD_1\n\t"
    "v_add_u32_sdwa v111, s92, v111 dst_sel:DWORD dst_unused:UNUSED_PAD src0_sel:DWORD src1_sel:WORD_0\n\t"
    "ds_read_b64 v[96:97], v111 offset:0\n\t"
    "ds_read_b64 v[98:99], v98 offset:0\n\t"
    "s_add_u32 s91, s91, s94\n\t"
    "buffer_load_dwordx4 v[108:111], v127, s[28:31], s91 offen offset:0 sc1\n\t"
    "v_add_u32_sdwa v126, s92, v112 dst_sel:DWORD dst_unused:UNUSED_PAD src0_sel:DWORD src1_sel:WORD_1\n\t"
    "v_cvt_pk_bf16_f32 v34, v116, v117\n\t"
    "v_cvt_pk_bf16_f32 v35, v118, v119\n\t"
    "v_and_b32 v119, s67, v41\n\t"
    "v_lshlrev_b32 v118, 16, v41\n\t"
    "v_and_b32 v117, s67, v40\n\t"
    "v_lshlrev_b32 v116, 16, v40\n\t"
    "v_add_u32_sdwa v112, s92, v112 dst_sel:DWORD dst_unused:UNUSED_PAD src0_sel:DWORD src1_sel:WORD_0\n\t"
    "s_waitcnt lgkmcnt(4)\n\t"
    "v_smfmac_f32_16x16x64_bf16 v[116:119], v[120:123], v[84:91], v124\n\t"
    "ds_read_b64 v[84:85], v112 offset:0\n\t"
    "ds_read_b64 v[86:87], v126 offset:0\n\t"
    "v_cvt_pk_bf16_f32 v80, v80, v81\n\t"
    "v_cvt_pk_bf16_f32 v81, v82, v83\n\t"
    "ds_write_b64 v125, v[80:81] offset:32640\n\t"
    "v_add_u32_sdwa v90, s92, v113 dst_sel:DWORD dst_unused:UNUSED_PAD src0_sel:DWORD src1_sel:WORD_1\n\t"
    "v_add_u32_sdwa v113, s92, v113 dst_sel:DWORD dst_unused:UNUSED_PAD src0_sel:DWORD src1_sel:WORD_0\n\t"
    "ds_read_b64 v[88:89], v113 offset:0\n\t"
    "ds_read_b64 v[90:91], v90 offset:0\n\t"
    "v_add_u32_sdwa v126, s92, v114 dst_sel:DWORD dst_unused:UNUSED_PAD src0_sel:DWORD src1_sel:WORD_1\n\t"
    "v_add_u32_sdwa v114, s92, v114 dst_sel:DWORD dst_unused:UNUSED_PAD src0_sel:DWORD src1_sel:WORD_0\n\t"
    "s_waitcnt vmcnt(2) lgkmcnt(5)\n\t"
    "v_smfmac_f32_16x16x64_bf16 v[116:119], v[120:123], v[92:99], v124\n\t"
    "ds_read_b64 v[92:93], v114 offset:0\n\t"
    "ds_read_b64 v[94:95], v126 offset:0\n\t"
    "v_add_u32_sdwa v98, s92, v115 dst_sel:DWORD dst_unused:UNUSED_PAD src0_sel:DWORD src1_sel:WORD_1\n\t"
    "v_add_u32_sdwa v115, s92, v115 dst_sel:DWORD dst_unused:UNUSED_PAD src0_sel:DWORD src1_sel:WORD_0\n\t"
    "ds_read_b64 v[96:97], v115 offset:0\n\t"
    "ds_read_b64 v[98:99], v98 offset:0\n\t"
    "buffer_load_dwordx4 v[112:115], v127, s[28:31], s91 offen offset:1024 sc1\n\t"
    "v_add_u32_sdwa v126, s92, v100 dst_sel:DWORD dst_unused:UNUSED_PAD src0_sel:DWORD src1_sel:WORD_1\n\t"
    "v_cvt_pk_bf16_f32 v40, v116, v117\n\t"
    "v_cvt_pk_bf16_f32 v41, v118, v119\n\t"
    "v_and_b32 v119, s67, v43\n\t"
    "v_lshlrev_b32 v118, 16, v43\n\t"
    "v_and_b32 v117, s67, v42\n\t"
    "v_lshlrev_b32 v116, 16, v42\n\t"
    "v_add_u32_sdwa v100, s92, v100 dst_sel:DWORD dst_unused:UNUSED_PAD src0_sel:DWORD src1_sel:WORD_0\n\t"
    "s_waitcnt lgkmcnt(4)\n\t"
    "v_smfmac_f32_16x16x64_bf16 v[116:119], v[120:123], v[84:91], v124\n\t"
    "ds_read_b64 v[84:85], v100 offset:0\n\t"
    "v_mfma_f32_16x16x32_bf16 v[80:83], v[64:67], v[156:159], 0\n\t"
    "ds_read_b64 v[86:87], v126 offset:0\n\t"
    "v_mfma_f32_16x16x32_bf16 v[80:83], v[68:71], v[160:163], v[80:83]\n\t"
    "v_add_u32_sdwa v90, s92, v101 dst_sel:DWORD dst_unused:UNUSED_PAD src0_sel:DWORD src1_sel:WORD_1\n\t"
    "v_add_u32_sdwa v101, s92, v101 dst_sel:DWORD dst_unused:UNUSED_PAD src0_sel:DWORD src1_sel:WORD_0\n\t"
    "ds_read_b64 v[88:89], v101 offset:0\n\t"
    "v_mfma_f32_16x16x32_bf16 v[80:83], v[72:75], v[140:143], v[80:83]\n\t"
    "ds_read_b64 v[90:91], v90 offset:0\n\t"
    "v_mfma_f32_16x16x32_bf16 v[80:83], v[76:79], v[144:147], v[80:83]\n\t"
    "v_add_u32_sdwa v126, s92, v102 dst_sel:DWORD dst_unused:UNUSED_PAD src0_sel:DWORD src1_sel:WORD_1\n\t"
    "v_add_u32_sdwa v102, s92, v102 dst_sel:DWORD dst_unused:UNUSED_PAD src0_sel:DWORD src1_sel:WORD_0\n\t"
    "s_waitcnt vmcnt(2) lgkmcnt(4)\n\t"
    "v_smfmac_f32_16x16x64_bf16 v[116:119], v[120:123], v[92:99], v124\n\t"
    "ds_read_b64 v[92:93], v102 offset:0\n\t"
    "ds_read_b64 v[94:95], v126 offset:0\n\t"
    "v_add_u32_sdwa v98, s92, v103 dst_sel:DWORD dst_unused:UNUSED_PAD src0_sel:DWORD src1_sel:WORD_1\n\t"
    "v_add_u32_sdwa v103, s92, v103 dst_sel:DWORD dst_unused:UNUSED_PAD src0_sel:DWORD src1_sel:WORD_0\n\t"
    "ds_read_b64 v[96:97], v103 offset:0\n\t"
    "ds_read_b64 v[98:99], v98 offset:0\n\t"
    "s_add_u32 s84, s84, s95\n\t"
    "buffer_load_dwordx4 v[100:103], v127, s[28:31], s84 offen offset:0 sc1\n\t"
    "v_add_u32_sdwa v126, s92, v104 dst_sel:DWORD dst_unused:UNUSED_PAD src0_sel:DWORD src1_sel:WORD_1\n\t"
    "v_cvt_pk_bf16_f32 v42, v116, v117\n\t"
    "v_cvt_pk_bf16_f32 v43, v118, v119\n\t"
    "v_and_b32 v119, s67, v49\n\t"
    "v_lshlrev_b32 v118, 16, v49\n\t"
    "v_and_b32 v117, s67, v48\n\t"
    "v_lshlrev_b32 v116, 16, v48\n\t"
    "v_add_u32_sdwa v104, s92, v104 dst_sel:DWORD dst_unused:UNUSED_PAD src0_sel:DWORD src1_sel:WORD_0\n\t"
    "s_waitcnt lgkmcnt(4)\n\t"
    "v_smfmac_f32_16x16x64_bf16 v[116:119], v[120:123], v[84:91], v124\n\t"
    "ds_read_b64 v[84:85], v104 offset:0\n\t"
    "ds_read_b64 v[86:87], v126 offset:0\n\t"
    "v_cvt_pk_bf16_f32 v80, v80, v81\n\t"
    "v_cvt_pk_bf16_f32 v81, v82, v83\n\t"
    "ds_write_b64 v125, v[80:81] offset:32768\n\t"
    "v_add_u32_sdwa v90, s92, v105 dst_sel:DWORD dst_unused:UNUSED_PAD src0_sel:DWORD src1_sel:WORD_1\n\t"
    "v_add_u32_sdwa v105, s92, v105 dst_sel:DWORD dst_unused:UNUSED_PAD src0_sel:DWORD src1_sel:WORD_0\n\t"
    "ds_read_b64 v[88:89], v105 offset:0\n\t"
    "ds_read_b64 v[90:91], v90 offset:0\n\t"
    "v_add_u32_sdwa v126, s92, v106 dst_sel:DWORD dst_unused:UNUSED_PAD src0_sel:DWORD src1_sel:WORD_1\n\t"
    "v_add_u32_sdwa v106, s92, v106 dst_sel:DWORD dst_unused:UNUSED_PAD src0_sel:DWORD src1_sel:WORD_0\n\t"
    "s_waitcnt vmcnt(2) lgkmcnt(5)\n\t"
    "v_smfmac_f32_16x16x64_bf16 v[116:119], v[120:123], v[92:99], v124\n\t"
    "ds_read_b64 v[92:93], v106 offset:0\n\t"
    "ds_read_b64 v[94:95], v126 offset:0\n\t"
    "v_add_u32_sdwa v98, s92, v107 dst_sel:DWORD dst_unused:UNUSED_PAD src0_sel:DWORD src1_sel:WORD_1\n\t"
    "v_add_u32_sdwa v107, s92, v107 dst_sel:DWORD dst_unused:UNUSED_PAD src0_sel:DWORD src1_sel:WORD_0\n\t"
    "ds_read_b64 v[96:97], v107 offset:0\n\t"
    "ds_read_b64 v[98:99], v98 offset:0\n\t"
    "buffer_load_dwordx4 v[104:107], v127, s[28:31], s84 offen offset:1024 sc1\n\t"
    "v_add_u32_sdwa v126, s92, v108 dst_sel:DWORD dst_unused:UNUSED_PAD src0_sel:DWORD src1_sel:WORD_1\n\t"
    "v_cvt_pk_bf16_f32 v48, v116, v117\n\t"
    "v_cvt_pk_bf16_f32 v49, v118, v119\n\t"
    "v_and_b32 v119, s67, v51\n\t"
    "v_lshlrev_b32 v118, 16, v51\n\t"
    "v_and_b32 v117, s67, v50\n\t"
    "v_lshlrev_b32 v116, 16, v50\n\t"
    "v_add_u32_sdwa v108, s92, v108 dst_sel:DWORD dst_unused:UNUSED_PAD src0_sel:DWORD src1_sel:WORD_0\n\t"
    "s_waitcnt lgkmcnt(4)\n\t"
    "v_smfmac_f32_16x16x64_bf16 v[116:119], v[120:123], v[84:91], v124\n\t"
    "ds_read_b64 v[84:85], v108 offset:0\n\t"
    "v_mfma_f32_16x16x32_bf16 v[80:83], v[64:67], v[212:215], 0\n\t"
    "ds_read_b64 v[86:87], v126 offset:0\n\t"
    "v_mfma_f32_16x16x32_bf16 v[80:83], v[68:71], v[216:219], v[80:83]\n\t"
    "v_add_u32_sdwa v90, s92, v109 dst_sel:DWORD dst_unused:UNUSED_PAD src0_sel:DWORD src1_sel:WORD_1\n\t"
    "v_add_u32_sdwa v109, s92, v109 dst_sel:DWORD dst_unused:UNUSED_PAD src0_sel:DWORD src1_sel:WORD_0\n\t"
    "ds_read_b64 v[88:89], v109 offset:0\n\t"
    "v_mfma_f32_16x16x32_bf16 v[80:83], v[72:75], v[148:151], v[80:83]\n\t"
    "ds_read_b64 v[90:91], v90 offset:0\n\t"
    "v_mfma_f32_16x16x32_bf16 v[80:83], v[76:79], v[152:155], v[80:83]\n\t"
    "v_add_u32_sdwa v126, s92, v110 dst_sel:DWORD dst_unused:UNUSED_PAD src0_sel:DWORD src1_sel:WORD_1\n\t"
    "v_add_u32_sdwa v110, s92, v110 dst_sel:DWORD dst_unused:UNUSED_PAD src0_sel:DWORD src1_sel:WORD_0\n\t"
    "s_waitcnt vmcnt(2) lgkmcnt(4)\n\t"
    "v_smfmac_f32_16x16x64_bf16 v[116:119], v[120:123], v[92:99], v124\n\t"
    "ds_read_b64 v[92:93], v110 offset:0\n\t"
    "ds_read_b64 v[94:95], v126 offset:0\n\t"
    "v_add_u32_sdwa v98, s92, v111 dst_sel:DWORD dst_unused:UNUSED_PAD src0_sel:DWORD src1_sel:WORD_1\n\t"
    "v_add_u32_sdwa v111, s92, v111 dst_sel:DWORD dst_unused:UNUSED_PAD src0_sel:DWORD src1_sel:WORD_0\n\t"
    "ds_read_b64 v[96:97], v111 offset:0\n\t"
    "ds_read_b64 v[98:99], v98 offset:0\n\t"
    "s_add_u32 s85, s85, s95\n\t"
    "buffer_load_dwordx4 v[108:111], v127, s[28:31], s85 offen offset:0 sc1\n\t"
    "v_add_u32_sdwa v126, s92, v112 dst_sel:DWORD dst_unused:UNUSED_PAD src0_sel:DWORD src1_sel:WORD_1\n\t"
    "v_cvt_pk_bf16_f32 v50, v116, v117\n\t"
    "v_cvt_pk_bf16_f32 v51, v118, v119\n\t"
    "v_and_b32 v119, s67, v57\n\t"
    "v_lshlrev_b32 v118, 16, v57\n\t"
    "v_and_b32 v117, s67, v56\n\t"
    "v_lshlrev_b32 v116, 16, v56\n\t"
    "v_add_u32_sdwa v112, s92, v112 dst_sel:DWORD dst_unused:UNUSED_PAD src0_sel:DWORD src1_sel:WORD_0\n\t"
    "s_waitcnt lgkmcnt(4)\n\t"
    "v_smfmac_f32_16x16x64_bf16 v[116:119], v[120:123], v[84:91], v124\n\t"
    "ds_read_b64 v[84:85], v112 offset:0\n\t"
    "ds_read_b64 v[86:87], v126 offset:0\n\t"
    "v_cvt_pk_bf16_f32 v80, v80, v81\n\t"
    "v_cvt_pk_bf16_f32 v81, v82, v83\n\t"
    "ds_write_b64 v125, v[80:81] offset:32872\n\t"
    "v_add_u32_sdwa v90, s92, v113 dst_sel:DWORD dst_unused:UNUSED_PAD src0_sel:DWORD src1_sel:WORD_1\n\t"
    "v_add_u32_sdwa v113, s92, v113 dst_sel:DWORD dst_unused:UNUSED_PAD src0_sel:DWORD src1_sel:WORD_0\n\t"
    "ds_read_b64 v[88:89], v113 offset:0\n\t"
    "ds_read_b64 v[90:91], v90 offset:0\n\t"
    "v_add_u32_sdwa v126, s92, v114 dst_sel:DWORD dst_unused:UNUSED_PAD src0_sel:DWORD src1_sel:WORD_1\n\t"
    "v_add_u32_sdwa v114, s92, v114 dst_sel:DWORD dst_unused:UNUSED_PAD src0_sel:DWORD src1_sel:WORD_0\n\t"
    "s_waitcnt vmcnt(2) lgkmcnt(5)\n\t"
    "v_smfmac_f32_16x16x64_bf16 v[116:119], v[120:123], v[92:99], v124\n\t"
    "ds_read_b64 v[92:93], v114 offset:0\n\t"
    "ds_read_b64 v[94:95], v126 offset:0\n\t"
    "v_add_u32_sdwa v98, s92, v115 dst_sel:DWORD dst_unused:UNUSED_PAD src0_sel:DWORD src1_sel:WORD_1\n\t"
    "v_add_u32_sdwa v115, s92, v115 dst_sel:DWORD dst_unused:UNUSED_PAD src0_sel:DWORD src1_sel:WORD_0\n\t"
    "ds_read_b64 v[96:97], v115 offset:0\n\t"
    "ds_read_b64 v[98:99], v98 offset:0\n\t"
    "buffer_load_dwordx4 v[112:115], v127, s[28:31], s85 offen offset:1024 sc1\n\t"
    "s_sub_u32 s92, 64000, s92\n\t"
    "v_add_u32_sdwa v126, s92, v100 dst_sel:DWORD dst_unused:UNUSED_PAD src0_sel:DWORD src1_sel:WORD_1\n\t"
    "v_cvt_pk_bf16_f32 v56, v116, v117\n\t"
    "v_cvt_pk_bf16_f32 v57, v118, v119\n\t"
    "v_and_b32 v119, s67, v59\n\t"
    "v_lshlrev_b32 v118, 16, v59\n\t"
    "v_and_b32 v117, s67, v58\n\t"
    "v_lshlrev_b32 v116, 16, v58\n\t"
    "v_add_u32_sdwa v100, s92, v100 dst_sel:DWORD dst_unused:UNUSED_PAD src0_sel:DWORD src1_sel:WORD_0\n\t"
    "s_waitcnt lgkmcnt(0)\n\t"
    "s_barrier\n\t"
    "v_add_u32 v125, s93, v125\n\t"
    "s_sub_u32 s93, 0, s93\n\t"
    "v_smfmac_f32_16x16x64_bf16 v[116:119], v[120:123], v[84:91], v124\n\t"
    "ds_read_b64 v[84:85], v100 offset:0\n\t"
    "ds_read_b64 v[86:87], v126 offset:0\n\t"
    "v_add_u32_sdwa v90, s92, v101 dst_sel:DWORD dst_unused:UNUSED_PAD src0_sel:DWORD src1_sel:WORD_1\n\t"
    "v_add_u32_sdwa v101, s92, v101 dst_sel:DWORD dst_unused:UNUSED_PAD src0_sel:DWORD src1_sel:WORD_0\n\t"
    "ds_read_b64 v[88:89], v101 offset:0\n\t"
    "ds_read_b64 v[90:91], v90 offset:0\n\t"
    "v_add_u32_sdwa v126, s92, v102 dst_sel:DWORD dst_unused:UNUSED_PAD src0_sel:DWORD src1_sel:WORD_1\n\t"
    "v_add_u32_sdwa v102, s92, v102 dst_sel:DWORD dst_unused:UNUSED_PAD src0_sel:DWORD src1_sel:WORD_0\n\t"
    "s_cmp_lt_u32 s34, 4\n\t"
    "s_cbranch_scc1 L_tp63_%=\n\t"
    "s_setprio 1\n\t"
    "L_tp63_%=:\n\t"
    "s_waitcnt vmcnt(2)\n\t"
    "v_smfmac_f32_16x16x64_bf16 v[116:119], v[120:123], v[92:99], v124\n\t"
    "ds_read_b64 v[92:93], v102 offset:0\n\t"
    "ds_read_b64 v[94:95], v126 offset:0\n\t"
    "v_add_u32_sdwa v98, s92, v103 dst_sel:DWORD dst_unused:UNUSED_PAD src0_sel:DWORD src1_sel:WORD_1\n\t"
    "v_add_u32_sdwa v103, s92, v103 dst_sel:DWORD dst_unused:UNUSED_PAD src0_sel:DWORD src1_sel:WORD_0\n\t"
    "ds_read_b64 v[96:97], v103 offset:0\n\t"
    "ds_read_b64 v[98:99], v98 offset:0\n\t"
    "s_add_u32 s86, s86, s95\n\t"
    "buffer_load_dwordx4 v[100:103], v127, s[28:31], s86 offen offset:0 sc1\n\t"
    "v_add_u32_sdwa v126, s92, v104 dst_sel:DWORD dst_unused:UNUSED_PAD src0_sel:DWORD src1_sel:WORD_1\n\t"
    "v_cvt_pk_bf16_f32 v58, v116, v117\n\t"
    "v_cvt_pk_bf16_f32 v59, v118, v119\n\t"
    "v_and_b32 v119, s67, v5\n\t"
    "v_lshlrev_b32 v118, 16, v5\n\t"
    "v_and_b32 v117, s67, v4\n\t"
    "v_lshlrev_b32 v116, 16, v4\n\t"
    "v_add_u32_sdwa v104, s92, v104 dst_sel:DWORD dst_unused:UNUSED_PAD src0_sel:DWORD src1_sel:WORD_0\n\t"
    "v_add_u32 v79, s96, v127\n\t"
    "s_add_u32 s96, s96, 0x1000\n\t"
    "s_and_b32 s96, s96, 0x3000\n\t"
    "v_add_u32 v79, 128000, v79\n\t"
    "ds_read_b128 v[64:67], v79 offset:0\n\t"
    "ds_read_b128 v[68:71], v79 offset:1024\n\t"
    "ds_read_b128 v[72:75], v79 offset:2048\n\t"
    "ds_read_b128 v[76:79], v79 offset:3072\n\t"
    "s_cmp_eq_u32 s66, s48\n\t"
    "s_cbranch_scc1 L_pub_%=\n\t"
    "L_pubb_%=:\n\t"
    "s_sub_u32 s66, s66, 1\n\t"
    "s_cmp_lg_u32 s66, 0\n\t"
    "s_cbranch_scc1 L_loop_%=\n\t"
    "s_mov_b32 s92, 0xffff\n\t"
    "s_waitcnt lgkmcnt(8)\n\t"
    "v_smfmac_f32_16x16x64_bf16 v[116:119], v[120:123], v[84:91], v124\n\t"
    "ds_read_b64 v[84:85], v104 offset:0\n\t"
    "ds_read_b64 v[86:87], v126 offset:0\n\t"
    "v_lshrrev_b32 v90, 16, v105\n\t"
    "v_and_b32 v105, s92, v105\n\t"
    "ds_read_b64 v[88:89], v105 offset:0\n\t"
    "ds_read_b64 v[90:91], v90 offset:0\n\t"
    "v_lshrrev_b32 v126, 16, v106\n\t"
    "v_and_b32 v106, s92, v106\n\t"
    "s_waitcnt vmcnt(2) lgkmcnt(8)\n\t"
    "v_smfmac_f32_16x16x64_bf16 v[116:119], v[120:123], v[92:99], v124\n\t"
    "ds_read_b64 v[92:93], v106 offset:0\n\t"
    "s_waitcnt lgkmcnt(5)\n\t"
    "v_mfma_f32_16x16x32_bf16 v[80:83], v[64:67], v[164:167], 0\n\t"
    "ds_read_b64 v[94:95], v126 offset:0\n\t"
    "v_mfma_f32_16x16x32_bf16 v[80:83], v[68:71], v[168:171], v[80:83]\n\t"
    "v_lshrrev_b32 v98, 16, v107\n\t"
    "v_and_b32 v107, s92, v107\n\t"
    "ds_read_b64 v[96:97], v107 offset:0\n\t"
    "v_mfma_f32_16x16x32_bf16 v[80:83], v[72:75], v[220:223], v[80:83]\n\t"
    "ds_read_b64 v[98:99], v98 offset:0\n\t"
    "v_mfma_f32_16x16x32_bf16 v[80:83], v[76:79], v[224:227], v[80:83]\n\t"
    "v_lshrrev_b32 v126, 16, v108\n\t"
    "v_cvt_pk_bf16_f32 v4, v116, v117\n\t"
    "v_cvt_pk_bf16_f32 v5, v118, v119\n\t"
    "v_and_b32 v119, s67, v7\n\t"
    "v_lshlrev_b32 v118, 16, v7\n\t"
    "v_and_b32 v117, s67, v6\n\t"
    "v_lshlrev_b32 v116, 16, v6\n\t"
    "v_and_b32 v108, s92, v108\n\t"
    "s_waitcnt lgkmcnt(4)\n\t"
    "v_smfmac_f32_16x16x64_bf16 v[116:119], v[120:123], v[84:91], v124\n\t"
    "ds_read_b64 v[84:85], v108 offset:0\n\t"
    "v_mfma_f32_16x16x32_bf16 v[104:107], v[64:67], v[172:175], 0\n\t"
    "ds_read_b64 v[86:87], v126 offset:0\n\t"
    "v_mfma_f32_16x16x32_bf16 v[104:107], v[68:71], v[176:179], v[104:107]\n\t"
    "v_lshrrev_b32 v90, 16, v109\n\t"
    "v_and_b32 v109, s92, v109\n\t"
    "ds_read_b64 v[88:89], v109 offset:0\n\t"
    "v_mfma_f32_16x16x32_bf16 v[104:107], v[72:75], v[228:231], v[104:107]\n\t"
    "ds_read_b64 v[90:91], v90 offset:0\n\t"
    "v_mfma_f32_16x16x32_bf16 v[104:107], v[76:79], v[232:235], v[104:107]\n\t"
    "v_lshrrev_b32 v126, 16, v110\n\t"
    "v_cvt_pk_bf16_f32 v80, v80, v81\n\t"
    "v_cvt_pk_bf16_f32 v81, v82, v83\n\t"
    "ds_write_b64 v125, v[80:81] offset:0\n\t"
    "v_mfma_f32_16x16x32_bf16 v[80:83], v[64:67], v[180:183], 0\n\t"
    "v_and_b32 v110, s92, v110\n\t"
    "v_mfma_f32_16x16x32_bf16 v[80:83], v[68:71], v[184:187], v[80:83]\n\t"
    "v_mfma_f32_16x16x32_bf16 v[80:83], v[72:75], v[236:239], v[80:83]\n\t"
    "s_waitcnt vmcnt(1) lgkmcnt(5)\n\t"
    "v_smfmac_f32_16x16x64_bf16 v[116:119], v[120:123], v[92:99], v124\n\t"
    "ds_read_b64 v[92:93], v110 offset:0\n\t"
    "v_mfma_f32_16x16x32_bf16 v[80:83], v[76:79], v[240:243], v[80:83]\n\t"
    "ds_read_b64 v[94:95], v126 offset:0\n\t"
    "v_cvt_pk_bf16_f32 v104, v104, v105\n\t"
    "v_cvt_pk_bf16_f32 v105, v106, v107\n\t"
    "ds_write_b64 v125, v[104:105] offset:128\n\t"
    "v_mfma_f32_16x16x32_bf16 v[104:107], v[64:67], v[188:191], 0\n\t"
    "v_lshrrev_b32 v98, 16, v111\n\t"
    "v_and_b32 v111, s92, v111\n\t"
    "ds_read_b64 v[96:97], v111 offset:0\n\t"
    "v_mfma_f32_16x16x32_bf16 v[104:107], v[68:71], v[192:195], v[104:107]\n\t"
    "ds_read_b64 v[98:99], v98 offset:0\n\t"
    "v_mfma_f32_16x16x32_bf16 v[104:107], v[72:75], v[244:247], v[104:107]\n\t"
    "buffer_load_dwordx4 v[108:111], v127, s[28:31], s86 offen offset:1024 sc1\n\t"
    "v_lshrrev_b32 v126, 16, v112\n\t"
    "v_cvt_pk_bf16_f32 v6, v116, v117\n\t"
    "v_cvt_pk_bf16_f32 v7, v118, v119\n\t"
    "v_and_b32 v119, s67, v13\n\t"
    "v_lshlrev_b32 v118, 16, v13\n\t"
    "v_and_b32 v117, s67, v12\n\t"
    "v_lshlrev_b32 v116, 16, v12\n\t"
    "v_and_b32 v112, s92, v112\n\t"
    "s_waitcnt lgkmcnt(6)\n\t"
    "v_smfmac_f32_16x16x64_bf16 v[116:119], v[120:123], v[84:91], v124\n\t"
    "ds_read_b64 v[84:85], v112 offset:0\n\t"
    "v_mfma_f32_16x16x32_bf16 v[104:107], v[76:79], v[248:251], v[104:107]\n\t"
    "ds_read_b64 v[86:87], v126 offset:0\n\t"
    "v_cvt_pk_bf16_f32 v80, v80, v81\n\t"
    "v_cvt_pk_bf16_f32 v81, v82, v83\n\t"
    "ds_write_b64 v125, v[80:81] offset:256\n\t"
    "v_mfma_f32_16x16x32_bf16 v[80:83], v[64:67], v[196:199], 0\n\t"
    "v_lshrrev_b32 v90, 16, v113\n\t"
    "v_and_b32 v113, s92, v113\n\t"
    "ds_read_b64 v[88:89], v113 offset:0\n\t"
    "v_mfma_f32_16x16x32_bf16 v[80:83], v[68:71], v[200:203], v[80:83]\n\t"
    "ds_read_b64 v[90:91], v90 offset:0\n\t"
    "v_mfma_f32_16x16x32_bf16 v[80:83], v[72:75], v[252:255], v[80:83]\n\t"
    "v_lshrrev_b32 v126, 16, v114\n\t"
    "v_mfma_f32_16x16x32_bf16 v[80:83], v[76:79], v[128:131], v[80:83]\n\t"
    "v_and_b32 v114, s92, v114\n\t"
    "v_cvt_pk_bf16_f32 v104, v104, v105\n\t"
    "v_cvt_pk_bf16_f32 v105, v106, v107\n\t"
    "ds_write_b64 v125, v[104:105] offset:384\n\t"
    "v_mfma_f32_16x16x32_bf16 v[104:107], v[64:67], v[204:207], 0\n\t"
    "s_waitcnt vmcnt(1) lgkmcnt(6)\n\t"
    "v_smfmac_f32_16x16x64_bf16 v[116:119], v[120:123], v[92:99], v124\n\t"
    "ds_read_b64 v[92:93], v114 offset:0\n\t"
    "v_mfma_f32_16x16x32_bf16 v[104:107], v[68:71], v[208:211], v[104:107]\n\t"
    "ds_read_b64 v[94:95], v126 offset:0\n\t"
    "v_mfma_f32_16x16x32_bf16 v[104:107], v[72:75], v[132:135], v[104:107]\n\t"
    "v_lshrrev_b32 v98, 16, v115\n\t"
    "v_and_b32 v115, s92, v115\n\t"
    "ds_read_b64 v[96:97], v115 offset:0\n\t"
    "v_mfma_f32_16x16x32_bf16 v[104:107], v[76:79], v[136:139], v[104:107]\n\t"
    "ds_read_b64 v[98:99], v98 offset:0\n\t"
    "v_cvt_pk_bf16_f32 v80, v80, v81\n\t"
    "v_cvt_pk_bf16_f32 v81, v82, v83\n\t"
    "ds_write_b64 v125, v[80:81] offset:512\n\t"
    "v_mfma_f32_16x16x32_bf16 v[80:83], v[64:67], v[156:159], 0\n\t"
    "s_add_u32 s87, s87, s95\n\t"
    "buffer_load_dwordx4 v[112:115], v127, s[28:31], s87 offen offset:0 sc1\n\t"
    "v_lshrrev_b32 v126, 16, v100\n\t"
    "v_mfma_f32_16x16x32_bf16 v[80:83], v[68:71], v[160:163], v[80:83]\n\t"
    "v_cvt_pk_bf16_f32 v12, v116, v117\n\t"
    "v_cvt_pk_bf16_f32 v13, v118, v119\n\t"
    "v_and_b32 v119, s67, v15\n\t"
    "v_lshlrev_b32 v118, 16, v15\n\t"
    "v_and_b32 v117, s67, v14\n\t"
    "v_lshlrev_b32 v116, 16, v14\n\t"
    "v_and_b32 v100, s92, v100\n\t"
    "s_waitcnt lgkmcnt(6)\n\t"
    "v_smfmac_f32_16x16x64_bf16 v[116:119], v[120:123], v[84:91], v124\n\t"
    "ds_read_b64 v[84:85], v100 offset:0\n\t"
    "v_mfma_f32_16x16x32_bf16 v[80:83], v[72:75], v[140:143], v[80:83]\n\t"
    "ds_read_b64 v[86:87], v126 offset:0\n\t"
    "v_mfma_f32_16x16x32_bf16 v[80:83], v[76:79], v[144:147], v[80:83]\n\t"
    "v_lshrrev_b32 v90, 16, v101\n\t"
    "v_and_b32 v101, s92, v101\n\t"
    "ds_read_b64 v[88:89], v101 offset:0\n\t"
    "v_cvt_pk_bf16_f32 v104, v104, v105\n\t"
    "v_cvt_pk_bf16_f32 v105, v106, v107\n\t"
    "ds_write_b64 v125, v[104:105] offset:640\n\t"
    "v_mfma_f32_16x16x32_bf16 v[104:107], v[64:67], v[212:215], 0\n\t"
    "ds_read_b64 v[90:91], v90 offset:0\n\t"
    "v_mfma_f32_16x16x32_bf16 v[104:107], v[68:71], v[216:219], v[104:107]\n\t"
    "v_lshrrev_b32 v126, 16, v102\n\t"
    "v_mfma_f32_16x16x32_bf16 v[104:107], v[72:75], v[148:151], v[104:107]\n\t"
    "v_and_b32 v102, s92, v102\n\t"
    "v_mfma_f32_16x16x32_bf16 v[104:107], v[76:79], v[152:155], v[104:107]\n\t"
    "v_add_u32 v79, s96, v127\n\t"
    "s_add_u32 s96, s96, 0x1000\n\t"
    "s_and_b32 s96, s96, 0x3000\n\t"
    "v_add_u32 v79, 128000, v79\n\t"
    "ds_read_b128 v[64:67], v79 offset:0\n\t"
    "ds_read_b128 v[68:71], v79 offset:1024\n\t"
    "ds_read_b128 v[72:75], v79 offset:2048\n\t"
    "ds_read_b128 v[76:79], v79 offset:3072\n\t"
    "v_cvt_pk_bf16_f32 v80, v80, v81\n\t"
    "v_cvt_pk_bf16_f32 v81, v82, v83\n\t"
    "ds_write_b64 v125, v[80:81] offset:768\n\t"
    "s_waitcnt vmcnt(1) lgkmcnt(11)\n\t"
    "v_smfmac_f32_16x16x64_bf16 v[116:119], v[120:123], v[92:99], v124\n\t"
    "ds_read_b64 v[92:93], v102 offset:0\n\t"
    "ds_read_b64 v[94:95], v126 offset:0\n\t"
    "v_lshrrev_b32 v98, 16, v103\n\t"
    "v_and_b32 v103, s92, v103\n\t"
    "ds_read_b64 v[96:97], v103 offset:0\n\t"
    "ds_read_b64 v[98:99], v98 offset:0\n\t"
    "buffer_load_dwordx4 v[100:103], v127, s[28:31], s87 offen offset:1024 sc1\n\t"
    "v_lshrrev_b32 v126, 16, v108\n\t"
    "v_cvt_pk_bf16_f32 v14, v116, v117\n\t"
    "v_cvt_pk_bf16_f32 v15, v118, v119\n\t"
    "v_and_b32 v119, s67, v21\n\t"
    "v_lshlrev_b32 v118, 16, v21\n\t"
    "v_and_b32 v117, s67, v20\n\t"
    "v_lshlrev_b32 v116, 16, v20\n\t"
    "v_and_b32 v108, s92, v108\n\t"
    "s_cmp_lt_u32 s34, 4\n\t"
    "s_cbranch_scc1 L_tp152_%=\n\t"
    "s_setprio 0\n\t"
    "L_tp152_%=:\n\t"
    "s_waitcnt lgkmcnt(9)\n\t"
    "v_smfmac_f32_16x16x64_bf16 v[116:119], v[120:123], v[84:91], v124\n\t"
    "ds_read_b64 v[84:85], v108 offset:0\n\t"
    "s_waitcnt lgkmcnt(6)\n\t"
    "v_mfma_f32_16x16x32_bf16 v[80:83], v[64:67], v[164:167], 0\n\t"
    "ds_read_b64 v[86:87], v126 offset:0\n\t"
    "v_mfma_f32_16x16x32_bf16 v[80:83], v[68:71], v[168:171], v[80:83]\n\t"
    "v_lshrrev_b32 v90, 16, v109\n\t"
    "v_and_b32 v109, s92, v109\n\t"
    "ds_read_b64 v[88:89], v109 offset:0\n\t"
    "v_mfma_f32_16x16x32_bf16 v[80:83], v[72:75], v[220:223], v[80:83]\n\t"
    "ds_read_b64 v[90:91], v90 offset:0\n\t"
    "v_mfma_f32_16x16x32_bf16 v[80:83], v[76:79], v[224:227], v[80:83]\n\t"
    "v_lshrrev_b32 v126, 16, v110\n\t"
    "v_cvt_pk_bf16_f32 v104, v104, v105\n\t"
    "v_cvt_pk_bf16_f32 v105, v106, v107\n\t"
    "ds_write_b64 v125, v[104:105] offset:872\n\t"
    "v_mfma_f32_16x16x32_bf16 v[104:107], v[64:67], v[172:175], 0\n\t"
    "v_and_b32 v110, s92, v110\n\t"
    "v_mfma_f32_16x16x32_bf16 v[104:107], v[68:71], v[176:179], v[104:107]\n\t"
    "s_waitcnt vmcnt(1) lgkmcnt(5)\n\t"
    "v_smfmac_f32_16x16x64_bf16 v[116:119], v[120:123], v[92:99], v124\n\t"
    "ds_read_b64 v[92:93], v110 offset:0\n\t"
    "v_mfma_f32_16x16x32_bf16 v[104:107], v[72:75], v[228:231], v[104:107]\n\t"
    "ds_read_b64 v[94:95], v126 offset:0\n\t"
    "v_mfma_f32_16x16x32_bf16 v[104:107], v[76:79], v[232:235], v[104:107]\n\t"
    "v_lshrrev_b32 v98, 16, v111\n\t"
    "v_and_b32 v111, s92, v111\n\t"
    "ds_read_b64 v[96:97], v111 offset:0\n\t"
    "v_cvt_pk_bf16_f32 v80, v80, v81\n\t"
    "v_cvt_pk_bf16_f32 v81, v82, v83\n\t"
    "ds_write_b64 v125, v[80:81] offset:32000\n\t"
    "v_mfma_f32_16x16x32_bf16 v[80:83], v[64:67], v[180:183], 0\n\t"
    "ds_read_b64 v[98:99], v98 offset:0\n\t"
    "v_mfma_f32_16x16x32_bf16 v[80:83], v[68:71], v[184:187], v[80:83]\n\t"
    "s_add_u32 s84, s84, 0x8000\n\t"
    "buffer_load_dwordx4 v[108:111], v127, s[28:31], s84 offen offset:0 sc1\n\t"
    "v_lshrrev_b32 v126, 16, v112\n\t"
    "v_mfma_f32_16x16x32_bf16 v[80:83], v[72:75], v[236:239], v[80:83]\n\t"
    "v_cvt_pk_bf16_f32 v20, v116, v117\n\t"
    "v_cvt_pk_bf16_f32 v21, v118, v119\n\t"
    "v_and_b32 v119, s67, v23\n\t"
    "v_lshlrev_b32 v118, 16, v23\n\t"
    "v_and_b32 v117, s67, v22\n\t"
    "v_lshlrev_b32 v116, 16, v22\n\t"
    "v_and_b32 v112, s92, v112\n\t"
    "s_waitcnt lgkmcnt(6)\n\t"
    "v_smfmac_f32_16x16x64_bf16 v[116:119], v[120:123], v[84:91], v124\n\t"
    "ds_read_b64 v[84:85], v112 offset:0\n\t"
    "v_mfma_f32_16x16x32_bf16 v[80:83], v[76:79], v[240:243], v[80:83]\n\t"
    "ds_read_b64 v[86:87], v126 offset:0\n\t"
    "v_cvt_pk_bf16_f32 v104, v104, v105\n\t"
    "v_cvt_pk_bf16_f32 v105, v106, v107\n\t"
    "ds_write_b64 v125, v[104:105] offset:32128\n\t"
    "v_mfma_f32_16x16x32_bf16 v[104:107], v[64:67], v[188:191], 0\n\t"
    "v_lshrrev_b32 v90, 16, v113\n\t"
    "v_and_b32 v113, s92, v113\n\t"
    "ds_read_b64 v[88:89], v113 offset:0\n\t"
    "v_mfma_f32_16x16x32_bf16 v[104:107], v[68:71], v[192:195], v[104:107]\n\t"
    "ds_read_b64 v[90:91], v90 offset:0\n\t"
    "v_mfma_f32_16x16x32_bf16 v[104:107], v[72:75], v[244:247], v[104:107]\n\t"
    "v_lshrrev_b32 v126, 16, v114\n\t"
    "v_mfma_f32_16x16x32_bf16 v[104:107], v[76:79], v[248:251], v[104:107]\n\t"
    "v_and_b32 v114, s92, v114\n\t"
    "v_cvt_pk_bf16_f32 v80, v80, v81\n\t"
    "v_cvt_pk_bf16_f32 v81, v82, v83\n\t"
    "ds_write_b64 v125, v[80:81] offset:32256\n\t"
    "v_mfma_f32_16x16x32_bf16 v[80:83], v[64:67], v[196:199], 0\n\t"
    "s_waitcnt vmcnt(1) lgkmcnt(6)\n\t"
    "v_smfmac_f32_16x16x64_bf16 v[116:119], v[120:123], v[92:99], v124\n\t"
    "ds_read_b64 v[92:93], v114 offset:0\n\t"
    "v_mfma_f32_16x16x32_bf16 v[80:83], v[68:71], v[200:203], v[80:83]\n\t"
    "ds_read_b64 v[94:95], v126 offset:0\n\t"
    "v_mfma_f32_16x16x32_bf16 v[80:83], v[72:75], v[252:255], v[80:83]\n\t"
    "v_lshrrev_b32 v98, 16, v115\n\t"
    "v_and_b32 v115, s92, v115\n\t"
    "ds_read_b64 v[96:97], v115 offset:0\n\t"
    "v_mfma_f32_16x16x32_bf16 v[80:83], v[76:79], v[128:131], v[80:83]\n\t"
    "ds_read_b64 v[98:99], v98 offset:0\n\t"
    "v_cvt_pk_bf16_f32 v104, v104, v105\n\t"
    "v_cvt_pk_bf16_f32 v105, v106, v107\n\t"
    "ds_write_b64 v125, v[104:105] offset:32384\n\t"
    "v_mfma_f32_16x16x32_bf16 v[104:107], v[64:67], v[204:207], 0\n\t"
    "buffer_load_dwordx4 v[112:115], v127, s[28:31], s84 offen offset:1024 sc1\n\t"
    "v_lshrrev_b32 v126, 16, v100\n\t"
    "v_cvt_pk_bf16_f32 v22, v116, v117\n\t"
    "v_cvt_pk_bf16_f32 v23, v118, v119\n\t"
    "v_and_b32 v119, s67, v29\n\t"
    "v_lshlrev_b32 v118, 16, v29\n\t"
    "v_and_b32 v117, s67, v28\n\t"
    "v_lshlrev_b32 v116, 16, v28\n\t"
    "v_and_b32 v100, s92, v100\n\t"
    "s_waitcnt lgkmcnt(6)\n\t"
    "v_smfmac_f32_16x16x64_bf16 v[116:119], v[120:123], v[84:91], v124\n\t"
    "ds_read_b64 v[84:85], v100 offset:0\n\t"
    "v_mfma_f32_16x16x32_bf16 v[104:107], v[68:71], v[208:211], v[104:107]\n\t"
    "ds_read_b64 v[86:87], v126 offset:0\n\t"
    "v_mfma_f32_16x16x32_bf16 v[104:107], v[72:75], v[132:135], v[104:107]\n\t"
    "v_lshrrev_b32 v90, 16, v101\n\t"
    "v_and_b32 v101, s92, v101\n\t"
    "ds_read_b64 v[88:89], v101 offset:0\n\t"
    "v_mfma_f32_16x16x32_bf16 v[104:107], v[76:79], v[136:139], v[104:107]\n\t"
    "ds_read_b64 v[90:91], v90 offset:0\n\t"
    "v_cvt_pk_bf16_f32 v80, v80, v81\n\t"
    "v_cvt_pk_bf16_f32 v81, v82, v83\n\t"
    "ds_write_b64 v125, v[80:81] offset:32512\n\t"
    "v_mfma_f32_16x16x32_bf16 v[80:83], v[64:67], v[156:159], 0\n\t"
    "v_lshrrev_b32 v126, 16, v102\n\t"
    "v_mfma_f32_16x16x32_bf16 v[80:83], v[68:71], v[160:163], v[80:83]\n\t"
    "v_and_b32 v102, s92, v102\n\t"
    "v_mfma_f32_16x16x32_bf16 v[80:83], v[72:75], v[140:143], v[80:83]\n\t"
    "v_mfma_f32_16x16x32_bf16 v[80:83], v[76:79], v[144:147], v[80:83]\n\t"
    "v_cvt_pk_bf16_f32 v104, v104, v105\n\t"
    "v_cvt_pk_bf16_f32 v105, v106, v107\n\t"
    "ds_write_b64 v125, v[104:105] offset:32640\n\t"
    "s_waitcnt vmcnt(1) lgkmcnt(7)\n\t"
    "v_smfmac_f32_16x16x64_bf16 v[116:119], v[120:123], v[92:99], v124\n\t"
    "ds_read_b64 v[92:93], v102 offset:0\n\t"
    "v_mfma_f32_16x16x32_bf16 v[104:107], v[64:67], v[212:215], 0\n\t"
    "ds_read_b64 v[94:95], v126 offset:0\n\t"
    "v_mfma_f32_16x16x32_bf16 v[104:107], v[68:71], v[216:219], v[104:107]\n\t"
    "v_lshrrev_b32 v98, 16, v103\n\t"
    "v_and_b32 v103, s92, v103\n\t"
    "ds_read_b64 v[96:97], v103 offset:0\n\t"
    "v_mfma_f32_16x16x32_bf16 v[104:107], v[72:75], v[148:151], v[104:107]\n\t"
    "ds_read_b64 v[98:99], v98 offset:0\n\t"
    "v_mfma_f32_16x16x32_bf16 v[104:107], v[76:79], v[152:155], v[104:107]\n\t"
    "s_add_u32 s85, s85, 0x8000\n\t"
    "buffer_load_dwordx4 v[100:103], v127, s[28:31], s85 offen offset:0 sc1\n\t"
    "v_lshrrev_b32 v126, 16, v108\n\t"
    "v_cvt_pk_bf16_f32 v80, v80, v81\n\t"
    "v_cvt_pk_bf16_f32 v81, v82, v83\n\t"
    "ds_write_b64 v125, v[80:81] offset:32768\n\t"
    "s_nop 1\n\t"
    "v_cvt_pk_bf16_f32 v104, v104, v105\n\t"
    "v_cvt_pk_bf16_f32 v105, v106, v107\n\t"
    "ds_write_b64 v125, v[104:105] offset:32872\n\t"
    "v_cvt_pk_bf16_f32 v28, v116, v117\n\t"
    "v_cvt_pk_bf16_f32 v29, v118, v119\n\t"
    "v_and_b32 v119, s67, v31\n\t"
    "v_lshlrev_b32 v118, 16, v31\n\t"
    "v_and_b32 v117, s67, v30\n\t"
    "v_lshlrev_b32 v116, 16, v30\n\t"
    "v_and_b32 v108, s92, v108\n\t"
    "s_waitcnt lgkmcnt(0)\n\t"
    "s_barrier\n\t"
    "v_smfmac_f32_16x16x64_bf16 v[116:119], v[120:123], v[84:91], v124\n\t"
    "ds_read_b64 v[84:85], v108 offset:64000\n\t"
    "ds_read_b64 v[86:87], v126 offset:64000\n\t"
    "v_lshrrev_b32 v90, 16, v109\n\t"
    "v_and_b32 v109, s92, v109\n\t"
    "ds_read_b64 v[88:89], v109 offset:64000\n\t"
    "ds_read_b64 v[90:91], v90 offset:64000\n\t"
    "buffer_load_dwordx4 v[104:107], v127, s[28:31], s85 offen offset:1024 sc1\n\t"
    "v_lshrrev_b32 v126, 16, v110\n\t"
    "v_and_b32 v110, s92, v110\n\t"
    "s_cmp_lt_u32 s34, 4\n\t"
    "s_cbranch_scc1 L_tp159_%=\n\t"
    "s_setprio 1\n\t"
    "L_tp159_%=:\n\t"
    "s_waitcnt vmcnt(2)\n\t"
    "v_smfmac_f32_16x16x64_bf16 v[116:119], v[120:123], v[92:99], v124\n\t"
    "ds_read_b64 v[92:93], v110 offset:64000\n\t"
    "ds_read_b64 v[94:95], v126 offset:64000\n\t"
    "v_lshrrev_b32 v98, 16, v111\n\t"
    "v_and_b32 v111, s92, v111\n\t"
    "ds_read_b64 v[96:97], v111 offset:64000\n\t"
    "ds_read_b64 v[98:99], v98 offset:64000\n\t"
    "s_add_u32 s86, s86, 0x8000\n\t"
    "buffer_load_dwordx4 v[108:111], v127, s[28:31], s86 offen offset:0 sc1\n\t"
    "v_lshrrev_b32 v252, 16, v112\n\t"
    "v_and_b32 v112, s92, v112\n\t"
    "v_lshrrev_b32 v253, 16, v113\n\t"
    "v_cvt_pk_bf16_f32 v30, v116, v117\n\t"
    "v_cvt_pk_bf16_f32 v31, v118, v119\n\t"
    "v_and_b32 v119, s67, v5\n\t"
    "v_lshlrev_b32 v118, 16, v5\n\t"
    "v_and_b32 v117, s67, v4\n\t"
    "v_lshlrev_b32 v116, 16, v4\n\t"
    "v_and_b32 v113, s92, v113\n\t"
    "v_mov_b32 v82, 0xbdd2d3e8\n\t"
    "v_mov_b32 v83, 0xbdd2d3e8\n\t"
    "v_mov_b32 v81, 0xc0135761\n\t"
    "v_lshrrev_b32 v125, 2, v127\n\t"
    "v_mov_b32 v80, 0xc0135761\n\t"
    "s_waitcnt lgkmcnt(4)\n\t"
    "v_smfmac_f32_16x16x64_bf16 v[116:119], v[120:123], v[84:91], v124\n\t"
    "ds_read_b64 v[84:85], v112 offset:64000\n\t"
    "ds_read_b64 v[86:87], v252 offset:64000\n\t"
    "ds_read_b64 v[88:89], v113 offset:64000\n\t"
    "ds_read_b64 v[90:91], v253 offset:64000\n\t"
    "v_lshrrev_b32 v254, 16, v114\n\t"
    "v_and_b32 v114, s92, v114\n\t"
    "v_lshrrev_b32 v255, 16, v115\n\t"
    "v_and_b32 v115, s92, v115\n\t"
    "s_lshl_b32 s65, s34, 10\n\t"
    "s_add_u32 s65, s65, 31457280\n\t"
    "s_xor_b32 s61, %2, 8\n\t"
    "s_mul_i32 s61, s61, 69632\n\t"
    "s_add_u32 s61, s61, s65\n\t"
    "s_mul_i32 s94, %2, 69632\n\t"
    "s_add_u32 s94, s94, s65\n\t"
    "s_lshl_b32 s65, s34, 9\n\t"
    "s_sub_u32 s65, 0x10000, s65\n\t"
    "s_add_u32 s93, s94, s65\n\t"
    "s_add_u32 s96, s61, s65\n\t"
    "v_mov_b32 v165, 1\n\t"
    "s_or_b32 s97, %2, s34\n\t"
    "s_cbranch_scc0 L_part0_%=\n\t"
    "L_partb0_%=:\n\t"
    "s_waitcnt vmcnt(2) lgkmcnt(4)\n\t"
    "v_smfmac_f32_16x16x64_bf16 v[116:119], v[120:123], v[92:99], v124\n\t"
    "ds_read_b64 v[92:93], v114 offset:64000\n\t"
    "ds_read_b64 v[94:95], v254 offset:64000\n\t"
    "ds_read_b64 v[96:97], v115 offset:64000\n\t"
    "ds_read_b64 v[98:99], v255 offset:64000\n\t"
    "buffer_load_dwordx4 v[172:175], v127, s[28:31], s86 offen offset:1024 sc1\n\t"
    "v_lshrrev_b32 v252, 16, v100\n\t"
    "v_and_b32 v100, s92, v100\n\t"
    "v_lshrrev_b32 v253, 16, v101\n\t"
    "v_cvt_pk_bf16_f32 v4, v116, v117\n\t"
    "v_cvt_pk_bf16_f32 v5, v118, v119\n\t"
    "v_and_b32 v119, s67, v7\n\t"
    "v_lshlrev_b32 v118, 16, v7\n\t"
    "v_and_b32 v117, s67, v6\n\t"
    "v_lshlrev_b32 v116, 16, v6\n\t"
    "v_and_b32 v101, s92, v101\n\t"
    "v_mov_b32 v156, v120\n\t"
    "v_mov_b32 v160, 0\n\t"
    "v_mov_b32 v161, v121\n\t"
    "v_mov_b32 v157, 0\n\t"
    "v_mov_b32 v158, v122\n\t"
    "v_mov_b32 v162, 0\n\t"
    "v_mov_b32 v163, v123\n\t"
    "v_mov_b32 v159, 0\n\t"
    "s_waitcnt lgkmcnt(4)\n\t"
    "v_smfmac_f32_16x16x64_bf16 v[116:119], v[120:123], v[84:91], v124\n\t"
    "ds_read_b64 v[84:85], v100 offset:64000\n\t"
    "ds_read_b64 v[86:87], v252 offset:64000\n\t"
    "ds_read_b64 v[88:89], v101 offset:64000\n\t"
    "ds_read_b64 v[90:91], v253 offset:64000\n\t"
    "s_add_u32 s87, s87, 0x8000\n\t"
    "buffer_load_dwordx4 v[176:179], v127, s[28:31], s87 offen offset:0 sc1\n\t"
    "v_lshrrev_b32 v254, 16, v102\n\t"
    "v_and_b32 v102, s92, v102\n\t"
    "v_lshrrev_b32 v255, 16, v103\n\t"
    "v_and_b32 v103, s92, v103\n\t"
    "s_waitcnt vmcnt(3) lgkmcnt(4)\n\t"
    "v_smfmac_f32_16x16x64_bf16 v[116:119], v[120:123], v[92:99], v124\n\t"
    "ds_read_b64 v[92:93], v102 offset:64000\n\t"
    "ds_read_b64 v[94:95], v254 offset:64000\n\t"
    "ds_read_b64 v[96:97], v103 offset:64000\n\t"
    "ds_read_b64 v[98:99], v255 offset:64000\n\t"
    "buffer_load_dwordx4 v[180:183], v127, s[28:31], s87 offen offset:1024 sc1\n\t"
    "v_lshrrev_b32 v252, 16, v104\n\t"
    "v_and_b32 v104, s92, v104\n\t"
    "v_lshrrev_b32 v253, 16, v105\n\t"
    "v_cvt_pk_bf16_f32 v6, v116, v117\n\t"
    "v_cvt_pk_bf16_f32 v7, v118, v119\n\t"
    "v_and_b32 v119, s67, v13\n\t"
    "v_lshlrev_b32 v118, 16, v13\n\t"
    "v_and_b32 v117, s67, v12\n\t"
    "v_lshlrev_b32 v116, 16, v12\n\t"
    "v_and_b32 v105, s92, v105\n\t"
    "buffer_store_dwordx4 v[4:7], v127, s[28:31], s61 offen sc1\n\t"
    "s_waitcnt lgkmcnt(4)\n\t"
    "v_smfmac_f32_16x16x64_bf16 v[116:119], v[120:123], v[84:91], v124\n\t"
    "ds_read_b64 v[84:85], v104 offset:64000\n\t"
    "ds_read_b64 v[86:87], v252 offset:64000\n\t"
    "ds_read_b64 v[88:89], v105 offset:64000\n\t"
    "ds_read_b64 v[90:91], v253 offset:64000\n\t"
    "s_add_u32 s88, s88, s95\n\t"
    "buffer_load_dwordx4 v[184:187], v127, s[28:31], s88 offen offset:0 sc1\n\t"
    "v_lshrrev_b32 v254, 16, v106\n\t"
    "v_and_b32 v106, s92, v106\n\t"
    "v_lshrrev_b32 v255, 16, v107\n\t"
    "v_and_b32 v107, s92, v107\n\t"
    "s_waitcnt vmcnt(5) lgkmcnt(4)\n\t"
    "v_smfmac_f32_16x16x64_bf16 v[116:119], v[120:123], v[92:99], v124\n\t"
    "ds_read_b64 v[92:93], v106 offset:64000\n\t"
    "ds_read_b64 v[94:95], v254 offset:64000\n\t"
    "ds_read_b64 v[96:97], v107 offset:64000\n\t"
    "ds_read_b64 v[98:99], v255 offset:64000\n\t"
    "s_add_u32 s88, s88, 0x8000\n\t"
    "buffer_load_dwordx4 v[188:191], v127, s[28:31], s88 offen offset:0 sc1\n\t"
    "v_lshrrev_b32 v252, 16, v108\n\t"
    "v_and_b32 v108, s92, v108\n\t"
    "v_lshrrev_b32 v253, 16, v109\n\t"
    "v_cvt_pk_bf16_f32 v12, v116, v117\n\t"
    "v_cvt_pk_bf16_f32 v13, v118, v119\n\t"
    "v_and_b32 v119, s67, v15\n\t"
    "v_lshlrev_b32 v118, 16, v15\n\t"
    "v_and_b32 v117, s67, v14\n\t"
    "v_lshlrev_b32 v116, 16, v14\n\t"
    "v_and_b32 v109, s92, v109\n\t"
    "s_waitcnt lgkmcnt(4)\n\t"
    "v_smfmac_f32_16x16x64_bf16 v[116:119], v[120:123], v[84:91], v124\n\t"
    "ds_read_b64 v[84:85], v108 offset:64000\n\t"
    "ds_read_b64 v[86:87], v252 offset:64000\n\t"
    "ds_read_b64 v[88:89], v109 offset:64000\n\t"
    "ds_read_b64 v[90:91], v253 offset:64000\n\t"
    "s_sub_u32 s88, s88, 0x8000\n\t"
    "buffer_load_dwordx4 v[236:239], v127, s[28:31], s88 offen offset:1024 sc1\n\t"
    "v_lshrrev_b32 v254, 16, v110\n\t"
    "v_and_b32 v110, s92, v110\n\t"
    "v_lshrrev_b32 v255, 16, v111\n\t"
    "v_and_b32 v111, s92, v111\n\t"
    "s_waitcnt vmcnt(6) lgkmcnt(4)\n\t"
    "v_smfmac_f32_16x16x64_bf16 v[116:119], v[120:123], v[92:99], v124\n\t"
    "ds_read_b64 v[92:93], v110 offset:64000\n\t"
    "ds_read_b64 v[94:95], v254 offset:64000\n\t"
    "ds_read_b64 v[96:97], v111 offset:64000\n\t"
    "ds_read_b64 v[98:99], v255 offset:64000\n\t"
    "s_add_u32 s88, s88, 0x8000\n\t"
    "buffer_load_dwordx4 v[192:195], v127, s[28:31], s88 offen offset:1024 sc1\n\t"
    "v_lshrrev_b32 v252, 16, v172\n\t"
    "v_and_b32 v172, s92, v172\n\t"
    "v_lshrrev_b32 v253, 16, v173\n\t"
    "v_cvt_pk_bf16_f32 v14, v116, v117\n\t"
    "v_cvt_pk_bf16_f32 v15, v118, v119\n\t"
    "v_and_b32 v119, s67, v21\n\t"
    "v_lshlrev_b32 v118, 16, v21\n\t"
    "v_and_b32 v117, s67, v20\n\t"
    "v_lshlrev_b32 v116, 16, v20\n\t"
    "v_and_b32 v173, s92, v173\n\t"
    "s_waitcnt lgkmcnt(4)\n\t"
    "v_smfmac_f32_16x16x64_bf16 v[116:119], v[120:123], v[84:91], v124\n\t"
    "ds_read_b64 v[84:85], v172 offset:64000\n\t"
    "ds_read_b64 v[86:87], v252 offset:64000\n\t"
    "ds_read_b64 v[88:89], v173 offset:64000\n\t"
    "ds_read_b64 v[90:91], v253 offset:64000\n\t"
    "v_lshrrev_b32 v254, 16, v174\n\t"
    "v_and_b32 v174, s92, v174\n\t"
    "v_lshrrev_b32 v255, 16, v175\n\t"
    "v_and_b32 v175, s92, v175\n\t"
    "s_cmp_lg_u32 s97, 0\n\t"
    "s_cbranch_scc0 L_part1_%=\n\t"
    "L_partb1_%=:\n\t"
    "s_waitcnt vmcnt(6) lgkmcnt(4)\n\t"
    "v_smfmac_f32_16x16x64_bf16 v[116:119], v[120:123], v[92:99], v124\n\t"
    "ds_read_b64 v[92:93], v174 offset:64000\n\t"
    "ds_read_b64 v[94:95], v254 offset:64000\n\t"
    "ds_read_b64 v[96:97], v175 offset:64000\n\t"
    "ds_read_b64 v[98:99], v255 offset:64000\n\t"
    "s_add_u32 s89, s89, s95\n\t"
    "buffer_load_dwordx4 v[196:199], v127, s[28:31], s89 offen offset:0 sc1\n\t"
    "v_lshrrev_b32 v252, 16, v176\n\t"
    "v_and_b32 v176, s92, v176\n\t"
    "v_lshrrev_b32 v253, 16, v177\n\t"
    "v_cvt_pk_bf16_f32 v20, v116, v117\n\t"
    "v_cvt_pk_bf16_f32 v21, v118, v119\n\t"
    "v_and_b32 v119, s67, v23\n\t"
    "v_lshlrev_b32 v118, 16, v23\n\t"
    "v_and_b32 v117, s67, v22\n\t"
    "v_lshlrev_b32 v116, 16, v22\n\t"
    "v_and_b32 v177, s92, v177\n\t"
    "s_waitcnt lgkmcnt(4)\n\t"
    "v_smfmac_f32_16x16x64_bf16 v[116:119], v[120:123], v[84:91], v124\n\t"
    "ds_read_b64 v[84:85], v176 offset:64000\n\t"
    "ds_read_b64 v[86:87], v252 offset:64000\n\t"
    "ds_read_b64 v[88:89], v177 offset:64000\n\t"
    "ds_read_b64 v[90:91], v253 offset:64000\n\t"
    "v_lshrrev_b32 v254, 16, v178\n\t"
    "v_and_b32 v178, s92, v178\n\t"
    "v_lshrrev_b32 v255, 16, v179\n\t"
    "v_and_b32 v179, s92, v179\n\t"
    "s_waitcnt vmcnt(6) lgkmcnt(4)\n\t"
    "v_smfmac_f32_16x16x64_bf16 v[116:119], v[120:123], v[92:99], v124\n\t"
    "ds_read_b64 v[92:93], v178 offset:64000\n\t"
    "ds_read_b64 v[94:95], v254 offset:64000\n\t"
    "ds_read_b64 v[96:97], v179 offset:64000\n\t"
    "ds_read_b64 v[98:99], v255 offset:64000\n\t"
    "s_add_u32 s89, s89, 0x8000\n\t"
    "buffer_load_dwordx4 v[200:203], v127, s[28:31], s89 offen offset:0 sc1\n\t"
    "v_lshrrev_b32 v252, 16, v180\n\t"
    "v_and_b32 v180, s92, v180\n\t"
    "v_lshrrev_b32 v253, 16, v181\n\t"
    "v_cvt_pk_bf16_f32 v22, v116, v117\n\t"
    "v_cvt_pk_bf16_f32 v23, v118, v119\n\t"
    "v_and_b32 v119, s67, v29\n\t"
    "v_lshlrev_b32 v118, 16, v29\n\t"
    "v_and_b32 v117, s67, v28\n\t"
    "v_lshlrev_b32 v116, 16, v28\n\t"
    "v_and_b32 v181, s92, v181\n\t"
    "s_add_u32 s61, s61, 0x2000\n\t"
    "buffer_store_dwordx4 v[12:15], v127, s[28:31], s61 offen sc1\n\t"
    "s_waitcnt lgkmcnt(4)\n\t"
    "v_smfmac_f32_16x16x64_bf16 v[116:119], v[120:123], v[84:91], v124\n\t"
    "ds_read_b64 v[84:85], v180 offset:64000\n\t"
    "ds_read_b64 v[86:87], v252 offset:64000\n\t"
    "ds_read_b64 v[88:89], v181 offset:64000\n\t"
    "ds_read_b64 v[90:91], v253 offset:64000\n\t"
    "v_lshrrev_b32 v254, 16, v182\n\t"
    "v_and_b32 v182, s92, v182\n\t"
    "v_lshrrev_b32 v255, 16, v183\n\t"
    "v_and_b32 v183, s92, v183\n\t"
    "s_waitcnt vmcnt(6) lgkmcnt(4)\n\t"
    "v_smfmac_f32_16x16x64_bf16 v[116:119], v[120:123], v[92:99], v124\n\t"
    "ds_read_b64 v[92:93], v182 offset:64000\n\t"
    "ds_read_b64 v[94:95], v254 offset:64000\n\t"
    "ds_read_b64 v[96:97], v183 offset:64000\n\t"
    "ds_read_b64 v[98:99], v255 offset:64000\n\t"
    "s_sub_u32 s89, s89, 0x8000\n\t"
    "buffer_load_dwordx4 v[204:207], v127, s[28:31], s89 offen offset:1024 sc1\n\t"
    "v_lshrrev_b32 v252, 16, v184\n\t"
    "v_and_b32 v184, s92, v184\n\t"
    "v_lshrrev_b32 v253, 16, v185\n\t"
    "v_cvt_pk_bf16_f32 v28, v116, v117\n\t"
    "v_cvt_pk_bf16_f32 v29, v118, v119\n\t"
    "v_and_b32 v119, s67, v31\n\t"
    "v_lshlrev_b32 v118, 16, v31\n\t"
    "v_and_b32 v117, s67, v30\n\t"
    "v_lshlrev_b32 v116, 16, v30\n\t"
    "v_and_b32 v185, s92, v185\n\t"
    "s_waitcnt lgkmcnt(4)\n\t"
    "v_smfmac_f32_16x16x64_bf16 v[116:119], v[120:123], v[84:91], v124\n\t"
    "ds_read_b64 v[84:85], v184 offset:0\n\t"
    "ds_read_b64 v[86:87], v252 offset:0\n\t"
    "ds_read_b64 v[88:89], v185 offset:0\n\t"
    "ds_read_b64 v[90:91], v253 offset:0\n\t"
    "v_lshrrev_b32 v254, 16, v186\n\t"
    "v_and_b32 v186, s92, v186\n\t"
    "v_lshrrev_b32 v255, 16, v187\n\t"
    "v_and_b32 v187, s92, v187\n\t"
    "s_waitcnt vmcnt(6) lgkmcnt(4)\n\t"
    "v_smfmac_f32_16x16x64_bf16 v[116:119], v[120:123], v[92:99], v124\n\t"
    "ds_read_b64 v[92:93], v186 offset:0\n\t"
    "ds_read_b64 v[94:95], v254 offset:0\n\t"
    "ds_read_b64 v[96:97], v187 offset:0\n\t"
    "ds_read_b64 v[98:99], v255 offset:0\n\t"
    "s_add_u32 s89, s89, 0x8000\n\t"
    "buffer_load_dwordx4 v[144:147], v127, s[28:31], s89 offen offset:1024 sc1\n\t"
    "v_lshrrev_b32 v252, 16, v188\n\t"
    "v_and_b32 v188, s92, v188\n\t"
    "v_lshrrev_b32 v253, 16, v189\n\t"
    "v_cvt_pk_bf16_f32 v30, v116, v117\n\t"
    "v_cvt_pk_bf16_f32 v31, v118, v119\n\t"
    "v_and_b32 v119, s67, v37\n\t"
    "v_lshlrev_b32 v118, 16, v37\n\t"
    "v_and_b32 v117, s67, v36\n\t"
    "v_lshlrev_b32 v116, 16, v36\n\t"
    "v_and_b32 v189, s92, v189\n\t"
    "s_waitcnt lgkmcnt(4)\n\t"
    "v_smfmac_f32_16x16x64_bf16 v[116:119], v[120:123], v[84:91], v124\n\t"
    "ds_read_b64 v[84:85], v188 offset:64000\n\t"
    "ds_read_b64 v[86:87], v252 offset:64000\n\t"
    "ds_read_b64 v[88:89], v189 offset:64000\n\t"
    "ds_read_b64 v[90:91], v253 offset:64000\n\t"
    "v_lshrrev_b32 v254, 16, v190\n\t"
    "v_and_b32 v190, s92, v190\n\t"
    "v_lshrrev_b32 v255, 16, v191\n\t"
    "v_and_b32 v191, s92, v191\n\t"
    "s_waitcnt vmcnt(6) lgkmcnt(4)\n\t"
    "v_smfmac_f32_16x16x64_bf16 v[116:119], v[120:123], v[92:99], v124\n\t"
    "ds_read_b64 v[92:93], v190 offset:64000\n\t"
    "ds_read_b64 v[94:95], v254 offset:64000\n\t"
    "ds_read_b64 v[96:97], v191 offset:64000\n\t"
    "ds_read_b64 v[98:99], v255 offset:64000\n\t"
    "s_add_u32 s90, s90, s95\n\t"
    "buffer_load_dwordx4 v[148:151], v127, s[28:31], s90 offen offset:0 sc1\n\t"
    "v_lshrrev_b32 v252, 16, v236\n\t"
    "v_and_b32 v236, s92, v236\n\t"
    "v_lshrrev_b32 v253, 16, v237\n\t"
    "v_and_b32 v237, s92, v237\n\t"
    "s_waitcnt lgkmcnt(4)\n\t"
    "v_smfmac_f32_16x16x64_bf16 v[116:119], v[120:123], v[84:91], v124\n\t"
    "ds_read_b64 v[84:85], v236 offset:0\n\t"
    "ds_read_b64 v[86:87], v252 offset:0\n\t"
    "ds_read_b64 v[88:89], v237 offset:0\n\t"
    "ds_read_b64 v[90:91], v253 offset:0\n\t"
    "v_lshrrev_b32 v254, 16, v238\n\t"
    "v_and_b32 v238, s92, v238\n\t"
    "v_lshrrev_b32 v255, 16, v239\n\t"
    "v_and_b32 v239, s92, v239\n\t"
    "s_waitcnt vmcnt(6) lgkmcnt(4)\n\t"
    "v_smfmac_f32_16x16x64_bf16 v[116:119], v[120:123], v[92:99], v124\n\t"
    "ds_read_b64 v[92:93], v238 offset:0\n\t"
    "ds_read_b64 v[94:95], v254 offset:0\n\t"
    "ds_read_b64 v[96:97], v239 offset:0\n\t"
    "ds_read_b64 v[98:99], v255 offset:0\n\t"
    "s_add_u32 s90, s90, 0x8000\n\t"
    "buffer_load_dwordx4 v[152:155], v127, s[28:31], s90 offen offset:0 sc1\n\t"
    "v_lshrrev_b32 v252, 16, v192\n\t"
    "v_and_b32 v192, s92, v192\n\t"
    "v_lshrrev_b32 v253, 16, v193\n\t"
    "v_cvt_pk_bf16_f32 v36, v116, v117\n\t"
    "v_cvt_pk_bf16_f32 v37, v118, v119\n\t"
    "v_and_b32 v119, s67, v39\n\t"
    "v_lshlrev_b32 v118, 16, v39\n\t"
    "v_and_b32 v117, s67, v38\n\t"
    "v_lshlrev_b32 v116, 16, v38\n\t"
    "v_and_b32 v193, s92, v193\n\t"
    "s_waitcnt vmcnt(4)\n\t"
    "s_mov_b64 exec, 0xffffffff\n\t"
    "buffer_store_dword v165, v125, s[28:31], s96 offen offset:0 sc1\n\t"
    "s_mov_b64 exec, -1\n\t"
    "s_add_u32 s61, s61, 0x2000\n\t"
    "buffer_store_dwordx4 v[20:23], v127, s[28:31], s61 offen sc1\n\t"
    "s_cmp_lt_u32 s34, 4\n\t"
    "s_cbranch_scc1 L_tp180_%=\n\t"
    "s_setprio 0\n\t"
    "L_tp180_%=:\n\t"
    "s_waitcnt lgkmcnt(4)\n\t"
    "v_smfmac_f32_16x16x64_bf16 v[116:119], v[120:123], v[84:91], v124\n\t"
    "ds_read_b64 v[84:85], v192 offset:64000\n\t"
    "ds_read_b64 v[86:87], v252 offset:64000\n\t"
    "ds_read_b64 v[88:89], v193 offset:64000\n\t"
    "ds_read_b64 v[90:91], v253 offset:64000\n\t"
    "v_lshrrev_b32 v254, 16, v194\n\t"
    "v_and_b32 v194, s92, v194\n\t"
    "v_lshrrev_b32 v255, 16, v195\n\t"
    "v_and_b32 v195, s92, v195\n\t"
    "s_waitcnt lgkmcnt(4)\n\t"
    "v_smfmac_f32_16x16x64_bf16 v[116:119], v[120:123], v[92:99], v124\n\t"
    "ds_read_b64 v[92:93], v194 offset:64000\n\t"
    "ds_read_b64 v[94:95], v254 offset:64000\n\t"
    "ds_read_b64 v[96:97], v195 offset:64000\n\t"
    "ds_read_b64 v[98:99], v255 offset:64000\n\t"
    "s_sub_u32 s90, s90, 0x8000\n\t"
    "buffer_load_dwordx4 v[240:243], v127, s[28:31], s90 offen offset:1024 sc1\n\t"
    "v_lshrrev_b32 v252, 16, v196\n\t"
    "v_and_b32 v196, s92, v196\n\t"
    "v_lshrrev_b32 v253, 16, v197\n\t"
    "v_and_b32 v197, s92, v197\n\t"
    "s_waitcnt lgkmcnt(4)\n\t"
    "v_smfmac_f32_16x16x64_bf16 v[116:119], v[120:123], v[84:91], v124\n\t"
    "ds_read_b64 v[84:85], v196 offset:0\n\t"
    "ds_read_b64 v[86:87], v252 offset:0\n\t"
    "ds_read_b64 v[88:89], v197 offset:0\n\t"
    "ds_read_b64 v[90:91], v253 offset:0\n\t"
    "v_lshrrev_b32 v254, 16, v198\n\t"
    "v_and_b32 v198, s92, v198\n\t"
    "v_lshrrev_b32 v255, 16, v199\n\t"
    "v_and_b32 v199, s92, v199\n\t"
    "s_waitcnt lgkmcnt(4)\n\t"
    "v_smfmac_f32_16x16x64_bf16 v[116:119], v[120:123], v[92:99], v124\n\t"
    "ds_read_b64 v[92:93], v198 offset:0\n\t"
    "ds_read_b64 v[94:95], v254 offset:0\n\t"
    "ds_read_b64 v[96:97], v199 offset:0\n\t"
    "ds_read_b64 v[98:99], v255 offset:0\n\t"
    "s_add_u32 s90, s90, 0x8000\n\t"
    "buffer_load_dwordx4 v[208:211], v127, s[28:31], s90 offen offset:1024 sc1\n\t"
    "v_lshrrev_b32 v252, 16, v200\n\t"
    "v_and_b32 v200, s92, v200\n\t"
    "v_lshrrev_b32 v253, 16, v201\n\t"
    "v_cvt_pk_bf16_f32 v38, v116, v117\n\t"
    "v_cvt_pk_bf16_f32 v39, v118, v119\n\t"
    "v_and_b32 v119, s67, v45\n\t"
    "v_lshlrev_b32 v118, 16, v45\n\t"
    "v_and_b32 v117, s67, v44\n\t"
    "v_lshlrev_b32 v116, 16, v44\n\t"
    "v_and_b32 v201, s92, v201\n\t"
    "s_waitcnt lgkmcnt(4)\n\t"
    "v_smfmac_f32_16x16x64_bf16 v[116:119], v[120:123], v[84:91], v124\n\t"
    "ds_read_b64 v[84:85], v200 offset:64000\n\t"
    "ds_read_b64 v[86:87], v252 offset:64000\n\t"
    "ds_read_b64 v[88:89], v201 offset:64000\n\t"
    "ds_read_b64 v[90:91], v253 offset:64000\n\t"
    "v_lshrrev_b32 v254, 16, v202\n\t"
    "v_and_b32 v202, s92, v202\n\t"
    "v_lshrrev_b32 v255, 16, v203\n\t"
    "v_and_b32 v203, s92, v203\n\t"
    "s_waitcnt vmcnt(7) lgkmcnt(4)\n\t"
    "v_smfmac_f32_16x16x64_bf16 v[116:119], v[120:123], v[92:99], v124\n\t"
    "ds_read_b64 v[92:93], v202 offset:64000\n\t"
    "ds_read_b64 v[94:95], v254 offset:64000\n\t"
    "ds_read_b64 v[96:97], v203 offset:64000\n\t"
    "ds_read_b64 v[98:99], v255 offset:64000\n\t"
    "s_add_u32 s91, s91, s95\n\t"
    "buffer_load_dwordx4 v[244:247], v127, s[28:31], s91 offen offset:0 sc1\n\t"
    "v_lshrrev_b32 v252, 16, v204\n\t"
    "v_and_b32 v204, s92, v204\n\t"
    "v_lshrrev_b32 v253, 16, v205\n\t"
    "v_and_b32 v205, s92, v205\n\t"
    "s_waitcnt lgkmcnt(4)\n\t"
    "v_smfmac_f32_16x16x64_bf16 v[116:119], v[120:123], v[84:91], v124\n\t"
    "ds_read_b64 v[84:85], v204 offset:0\n\t"
    "ds_read_b64 v[86:87], v252 offset:0\n\t"
    "ds_read_b64 v[88:89], v205 offset:0\n\t"
    "ds_read_b64 v[90:91], v253 offset:0\n\t"
    "v_lshrrev_b32 v254, 16, v206\n\t"
    "v_and_b32 v206, s92, v206\n\t"
    "v_lshrrev_b32 v255, 16, v207\n\t"
    "v_and_b32 v207, s92, v207\n\t"
    "s_waitcnt vmcnt(7) lgkmcnt(4)\n\t"
    "v_smfmac_f32_16x16x64_bf16 v[116:119], v[120:123], v[92:99], v124\n\t"
    "ds_read_b64 v[92:93], v206 offset:0\n\t"
    "ds_read_b64 v[94:95], v254 offset:0\n\t"
    "ds_read_b64 v[96:97], v207 offset:0\n\t"
    "ds_read_b64 v[98:99], v255 offset:0\n\t"
    "s_add_u32 s91, s91, 0x8000\n\t"
    "buffer_load_dwordx4 v[248:251], v127, s[28:31], s91 offen offset:0 sc1\n\t"
    "v_lshrrev_b32 v252, 16, v144\n\t"
    "v_and_b32 v144, s92, v144\n\t"
    "v_lshrrev_b32 v253, 16, v145\n\t"
    "v_cvt_pk_bf16_f32 v44, v116, v117\n\t"
    "v_cvt_pk_bf16_f32 v45, v118, v119\n\t"
    "v_and_b32 v119, s67, v47\n\t"
    "v_lshlrev_b32 v118, 16, v47\n\t"
    "v_and_b32 v117, s67, v46\n\t"
    "v_lshlrev_b32 v116, 16, v46\n\t"
    "v_and_b32 v145, s92, v145\n\t"
    "s_add_u32 s61, s61, 0x2000\n\t"
    "buffer_store_dwordx4 v[28:31], v127, s[28:31], s61 offen sc1\n\t"
    "s_waitcnt lgkmcnt(4)\n\t"
    "v_smfmac_f32_16x16x64_bf16 v[116:119], v[120:123], v[84:91], v124\n\t"
    "ds_read_b64 v[84:85], v144 offset:64000\n\t"
    "ds_read_b64 v[86:87], v252 offset:64000\n\t"
    "ds_read_b64 v[88:89], v145 offset:64000\n\t"
    "ds_read_b64 v[90:91], v253 offset:64000\n\t"
    "v_lshrrev_b32 v254, 16, v146\n\t"
    "v_and_b32 v146, s92, v146\n\t"
    "v_lshrrev_b32 v255, 16, v147\n\t"
    "v_and_b32 v147, s92, v147\n\t"
    "s_waitcnt vmcnt(8) lgkmcnt(4)\n\t"
    "v_smfmac_f32_16x16x64_bf16 v[116:119], v[120:123], v[92:99], v124\n\t"
    "ds_read_b64 v[92:93], v146 offset:64000\n\t"
    "ds_read_b64 v[94:95], v254 offset:64000\n\t"
    "ds_read_b64 v[96:97], v147 offset:64000\n\t"
    "ds_read_b64 v[98:99], v255 offset:64000\n\t"
    "s_sub_u32 s91, s91, 0x8000\n\t"
    "buffer_load_dwordx4 v[212:215], v127, s[28:31], s91 offen offset:1024 sc1\n\t"
    "v_lshrrev_b32 v252, 16, v148\n\t"
    "v_and_b32 v148, s92, v148\n\t"
    "v_lshrrev_b32 v253, 16, v149\n\t"
    "v_and_b32 v149, s92, v149\n\t"
    "s_waitcnt lgkmcnt(4)\n\t"
    "v_smfmac_f32_16x16x64_bf16 v[116:119], v[120:123], v[84:91], v124\n\t"
    "ds_read_b64 v[84:85], v148 offset:0\n\t"
    "ds_read_b64 v[86:87], v252 offset:0\n\t"
    "ds_read_b64 v[88:89], v149 offset:0\n\t"
    "ds_read_b64 v[90:91], v253 offset:0\n\t"
    "v_lshrrev_b32 v254, 16, v150\n\t"
    "v_and_b32 v150, s92, v150\n\t"
    "v_lshrrev_b32 v255, 16, v151\n\t"
    "v_and_b32 v151, s92, v151\n\t"
    "s_waitcnt vmcnt(8) lgkmcnt(4)\n\t"
    "v_smfmac_f32_16x16x64_bf16 v[116:119], v[120:123], v[92:99], v124\n\t"
    "ds_read_b64 v[92:93], v150 offset:0\n\t"
    "ds_read_b64 v[94:95], v254 offset:0\n\t"
    "ds_read_b64 v[96:97], v151 offset:0\n\t"
    "ds_read_b64 v[98:99], v255 offset:0\n\t"
    "s_add_u32 s91, s91, 0x8000\n\t"
    "buffer_load_dwordx4 v[216:219], v127, s[28:31], s91 offen offset:1024 sc1\n\t"
    "v_lshrrev_b32 v252, 16, v152\n\t"
    "v_and_b32 v152, s92, v152\n\t"
    "v_lshrrev_b32 v253, 16, v153\n\t"
    "v_cvt_pk_bf16_f32 v46, v116, v117\n\t"
    "v_cvt_pk_bf16_f32 v47, v118, v119\n\t"
    "v_and_b32 v119, s67, v53\n\t"
    "v_lshlrev_b32 v118, 16, v53\n\t"
    "v_and_b32 v117, s67, v52\n\t"
    "v_lshlrev_b32 v116, 16, v52\n\t"
    "v_and_b32 v153, s92, v153\n\t"
    "s_waitcnt lgkmcnt(4)\n\t"
    "v_smfmac_f32_16x16x64_bf16 v[116:119], v[120:123], v[84:91], v124\n\t"
    "ds_read_b64 v[84:85], v152 offset:64000\n\t"
    "ds_read_b64 v[86:87], v252 offset:64000\n\t"
    "ds_read_b64 v[88:89], v153 offset:64000\n\t"
    "ds_read_b64 v[90:91], v253 offset:64000\n\t"
    "v_lshrrev_b32 v254, 16, v154\n\t"
    "v_and_b32 v154, s92, v154\n\t"
    "v_lshrrev_b32 v255, 16, v155\n\t"
    "v_and_b32 v155, s92, v155\n\t"
    "s_cmp_lt_u32 s34, 4\n\t"
    "s_cbranch_scc1 L_tp193_%=\n\t"
    "s_setprio 1\n\t"
    "L_tp193_%=:\n\t"
    "s_waitcnt vmcnt(6) lgkmcnt(4)\n\t"
    "v_smfmac_f32_16x16x64_bf16 v[116:119], v[120:123], v[92:99], v124\n\t"
    "ds_read_b64 v[92:93], v154 offset:64000\n\t"
    "ds_read_b64 v[94:95], v254 offset:64000\n\t"
    "ds_read_b64 v[96:97], v155 offset:64000\n\t"
    "ds_read_b64 v[98:99], v255 offset:64000\n\t"
    "s_sub_u32 s84, s84, s95\n\t"
    "buffer_load_dwordx4 v[220:223], v127, s[28:31], s84 offen offset:0 sc1\n\t"
    "v_lshrrev_b32 v252, 16, v240\n\t"
    "v_and_b32 v240, s92, v240\n\t"
    "v_lshrrev_b32 v253, 16, v241\n\t"
    "v_and_b32 v241, s92, v241\n\t"
    "s_waitcnt lgkmcnt(4)\n\t"
    "v_smfmac_f32_16x16x64_bf16 v[116:119], v[120:123], v[84:91], v124\n\t"
    "ds_read_b64 v[84:85], v240 offset:0\n\t"
    "ds_read_b64 v[86:87], v252 offset:0\n\t"
    "ds_read_b64 v[88:89], v241 offset:0\n\t"
    "ds_read_b64 v[90:91], v253 offset:0\n\t"
    "v_lshrrev_b32 v254, 16, v242\n\t"
    "v_and_b32 v242, s92, v242\n\t"
    "v_lshrrev_b32 v255, 16, v243\n\t"
    "v_and_b32 v243, s92, v243\n\t"
    "s_waitcnt vmcnt(6) lgkmcnt(4)\n\t"
    "v_smfmac_f32_16x16x64_bf16 v[116:119], v[120:123], v[92:99], v124\n\t"
    "ds_read_b64 v[92:93], v242 offset:0\n\t"
    "ds_read_b64 v[94:95], v254 offset:0\n\t"
    "ds_read_b64 v[96:97], v243 offset:0\n\t"
    "ds_read_b64 v[98:99], v255 offset:0\n\t"
    "buffer_load_dwordx4 v[224:227], v127, s[28:31], s84 offen offset:1024 sc1\n\t"
    "v_lshrrev_b32 v252, 16, v208\n\t"
    "v_and_b32 v208, s92, v208\n\t"
    "v_lshrrev_b32 v253, 16, v209\n\t"
    "v_cvt_pk_bf16_f32 v52, v116, v117\n\t"
    "v_cvt_pk_bf16_f32 v53, v118, v119\n\t"
    "v_and_b32 v119, s67, v55\n\t"
    "v_lshlrev_b32 v118, 16, v55\n\t"
    "v_and_b32 v117, s67, v54\n\t"
    "v_lshlrev_b32 v116, 16, v54\n\t"
    "v_and_b32 v209, s92, v209\n\t"
    "s_waitcnt vmcnt(4)\n\t"
    "s_mov_b64 exec, 0xffffffff\n\t"
    "buffer_store_dword v165, v125, s[28:31], s96 offen offset:128 sc1\n\t"
    "s_mov_b64 exec, -1\n\t"
    "s_add_u32 s61, s61, 0x2000\n\t"
    "buffer_store_dwordx4 v[36:39], v127, s[28:31], s61 offen sc1\n\t"
    "s_waitcnt lgkmcnt(4)\n\t"
    "v_smfmac_f32_16x16x64_bf16 v[116:119], v[120:123], v[84:91], v124\n\t"
    "ds_read_b64 v[84:85], v208 offset:64000\n\t"
    "ds_read_b64 v[86:87], v252 offset:64000\n\t"
    "ds_read_b64 v[88:89], v209 offset:64000\n\t"
    "ds_read_b64 v[90:91], v253 offset:64000\n\t"
    "v_lshrrev_b32 v254, 16, v210\n\t"
    "v_and_b32 v210, s92, v210\n\t"
    "v_lshrrev_b32 v255, 16, v211\n\t"
    "v_and_b32 v211, s92, v211\n\t"
    "s_waitcnt lgkmcnt(4)\n\t"
    "v_smfmac_f32_16x16x64_bf16 v[116:119], v[120:123], v[92:99], v124\n\t"
    "ds_read_b64 v[92:93], v210 offset:64000\n\t"
    "ds_read_b64 v[94:95], v254 offset:64000\n\t"
    "ds_read_b64 v[96:97], v211 offset:64000\n\t"
    "ds_read_b64 v[98:99], v255 offset:64000\n\t"
    "s_sub_u32 s85, s85, s95\n\t"
    "buffer_load_dwordx4 v[228:231], v127, s[28:31], s85 offen offset:0 sc1\n\t"
    "v_lshrrev_b32 v252, 16, v244\n\t"
    "v_and_b32 v244, s92, v244\n\t"
    "v_lshrrev_b32 v253, 16, v245\n\t"
    "v_and_b32 v245, s92, v245\n\t"
    "s_waitcnt lgkmcnt(4)\n\t"
    "v_smfmac_f32_16x16x64_bf16 v[116:119], v[120:123], v[84:91], v124\n\t"
    "ds_read_b64 v[84:85], v244 offset:0\n\t"
    "ds_read_b64 v[86:87], v252 offset:0\n\t"
    "ds_read_b64 v[88:89], v245 offset:0\n\t"
    "ds_read_b64 v[90:91], v253 offset:0\n\t"
    "v_lshrrev_b32 v254, 16, v246\n\t"
    "v_and_b32 v246, s92, v246\n\t"
    "v_lshrrev_b32 v255, 16, v247\n\t"
    "v_and_b32 v247, s92, v247\n\t"
    "s_waitcnt lgkmcnt(4)\n\t"
    "v_smfmac_f32_16x16x64_bf16 v[116:119], v[120:123], v[92:99], v124\n\t"
    "ds_read_b64 v[92:93], v246 offset:0\n\t"
    "ds_read_b64 v[94:95], v254 offset:0\n\t"
    "ds_read_b64 v[96:97], v247 offset:0\n\t"
    "ds_read_b64 v[98:99], v255 offset:0\n\t"
    "buffer_load_dwordx4 v[232:235], v127, s[28:31], s85 offen offset:1024 sc1\n\t"
    "v_lshrrev_b32 v252, 16, v248\n\t"
    "v_and_b32 v248, s92, v248\n\t"
    "v_lshrrev_b32 v253, 16, v249\n\t"
    "v_cvt_pk_bf16_f32 v54, v116, v117\n\t"
    "v_cvt_pk_bf16_f32 v55, v118, v119\n\t"
    "v_and_b32 v119, s67, v61\n\t"
    "v_lshlrev_b32 v118, 16, v61\n\t"
    "v_and_b32 v117, s67, v60\n\t"
    "v_lshlrev_b32 v116, 16, v60\n\t"
    "v_and_b32 v249, s92, v249\n\t"
    "s_waitcnt lgkmcnt(4)\n\t"
    "v_smfmac_f32_16x16x64_bf16 v[116:119], v[120:123], v[84:91], v124\n\t"
    "ds_read_b64 v[84:85], v248 offset:64000\n\t"
    "ds_read_b64 v[86:87], v252 offset:64000\n\t"
    "ds_read_b64 v[88:89], v249 offset:64000\n\t"
    "ds_read_b64 v[90:91], v253 offset:64000\n\t"
    "v_lshrrev_b32 v254, 16, v250\n\t"
    "v_and_b32 v250, s92, v250\n\t"
    "v_lshrrev_b32 v255, 16, v251\n\t"
    "v_and_b32 v251, s92, v251\n\t"
    "s_waitcnt vmcnt(7) lgkmcnt(4)\n\t"
    "v_smfmac_f32_16x16x64_bf16 v[116:119], v[120:123], v[92:99], v124\n\t"
    "ds_read_b64 v[92:93], v250 offset:64000\n\t"
    "ds_read_b64 v[94:95], v254 offset:64000\n\t"
    "ds_read_b64 v[96:97], v251 offset:64000\n\t"
    "ds_read_b64 v[98:99], v255 offset:64000\n\t"
    "s_sub_u32 s86, s86, s95\n\t"
    "buffer_load_dwordx4 v[168:171], v127, s[28:31], s86 offen offset:0 sc1\n\t"
    "v_lshrrev_b32 v252, 16, v212\n\t"
    "v_and_b32 v212, s92, v212\n\t"
    "v_lshrrev_b32 v253, 16, v213\n\t"
    "v_and_b32 v213, s92, v213\n\t"
    "s_waitcnt lgkmcnt(4)\n\t"
    "v_smfmac_f32_16x16x64_bf16 v[116:119], v[120:123], v[84:91], v124\n\t"
    "ds_read_b64 v[84:85], v212 offset:0\n\t"
    "ds_read_b64 v[86:87], v252 offset:0\n\t"
    "ds_read_b64 v[88:89], v213 offset:0\n\t"
    "ds_read_b64 v[90:91], v253 offset:0\n\t"
    "v_lshrrev_b32 v254, 16, v214\n\t"
    "v_and_b32 v214, s92, v214\n\t"
    "v_lshrrev_b32 v255, 16, v215\n\t"
    "v_and_b32 v215, s92, v215\n\t"
    "s_waitcnt vmcnt(7) lgkmcnt(4)\n\t"
    "v_smfmac_f32_16x16x64_bf16 v[116:119], v[120:123], v[92:99], v124\n\t"
    "ds_read_b64 v[92:93], v214 offset:0\n\t"
    "ds_read_b64 v[94:95], v254 offset:0\n\t"
    "ds_read_b64 v[96:97], v215 offset:0\n\t"
    "ds_read_b64 v[98:99], v255 offset:0\n\t"
    "s_add_u32 s84, s84, 0x8000\n\t"
    "buffer_load_dwordx4 v[112:115], v127, s[28:31], s84 offen offset:0 sc1\n\t"
    "v_lshrrev_b32 v252, 16, v216\n\t"
    "v_and_b32 v216, s92, v216\n\t"
    "v_lshrrev_b32 v253, 16, v217\n\t"
    "v_cvt_pk_bf16_f32 v60, v116, v117\n\t"
    "v_cvt_pk_bf16_f32 v61, v118, v119\n\t"
    "v_and_b32 v119, s67, v63\n\t"
    "v_lshlrev_b32 v118, 16, v63\n\t"
    "v_and_b32 v117, s67, v62\n\t"
    "v_lshlrev_b32 v116, 16, v62\n\t"
    "v_and_b32 v217, s92, v217\n\t"
    "s_add_u32 s61, s61, 0x2000\n\t"
    "buffer_store_dwordx4 v[44:47], v127, s[28:31], s61 offen sc1\n\t"
    "s_waitcnt lgkmcnt(4)\n\t"
    "v_smfmac_f32_16x16x64_bf16 v[116:119], v[120:123], v[84:91], v124\n\t"
    "ds_read_b64 v[84:85], v216 offset:64000\n\t"
    "ds_read_b64 v[86:87], v252 offset:64000\n\t"
    "ds_read_b64 v[88:89], v217 offset:64000\n\t"
    "ds_read_b64 v[90:91], v253 offset:64000\n\t"
    "v_lshrrev_b32 v254, 16, v218\n\t"
    "v_and_b32 v218, s92, v218\n\t"
    "v_lshrrev_b32 v255, 16, v219\n\t"
    "v_and_b32 v219, s92, v219\n\t"
    "s_mov_b64 exec, 1\n\t"
    "buffer_load_dword v164, off, s[28:31], s93 offset:0 sc1\n\t"
    "s_mov_b64 exec, -1\n\t"
    "s_waitcnt vmcnt(9) lgkmcnt(4)\n\t"
    "v_smfmac_f32_16x16x64_bf16 v[116:119], v[120:123], v[92:99], v124\n\t"
    "ds_read_b64 v[92:93], v218 offset:64000\n\t"
    "ds_read_b64 v[94:95], v254 offset:64000\n\t"
    "ds_read_b64 v[96:97], v219 offset:64000\n\t"
    "ds_read_b64 v[98:99], v255 offset:64000\n\t"
    "buffer_load_dwordx4 v[100:103], v127, s[28:31], s86 offen offset:1024 sc1\n\t"
    "v_lshrrev_b32 v252, 16, v220\n\t"
    "v_and_b32 v220, s92, v220\n\t"
    "v_lshrrev_b32 v253, 16, v221\n\t"
    "v_and_b32 v221, s92, v221\n\t"
    "s_waitcnt lgkmcnt(4)\n\t"
    "v_smfmac_f32_16x16x64_bf16 v[116:119], v[120:123], v[84:91], v124\n\t"
    "ds_read_b64 v[84:85], v220 offset:0\n\t"
    "ds_read_b64 v[86:87], v252 offset:0\n\t"
    "ds_read_b64 v[88:89], v221 offset:0\n\t"
    "ds_read_b64 v[90:91], v253 offset:0\n\t"
    "v_lshrrev_b32 v254, 16, v222\n\t"
    "v_and_b32 v222, s92, v222\n\t"
    "v_lshrrev_b32 v255, 16, v223\n\t"
    "v_and_b32 v223, s92, v223\n\t"
    "s_waitcnt vmcnt(9) lgkmcnt(4)\n\t"
    "v_smfmac_f32_16x16x64_bf16 v[116:119], v[120:123], v[92:99], v124\n\t"
    "ds_read_b64 v[92:93], v222 offset:0\n\t"
    "ds_read_b64 v[94:95], v254 offset:0\n\t"
    "ds_read_b64 v[96:97], v223 offset:0\n\t"
    "ds_read_b64 v[98:99], v255 offset:0\n\t"
    "buffer_load_dwordx4 v[104:107], v127, s[28:31], s84 offen offset:1024 sc1\n\t"
    "v_lshrrev_b32 v252, 16, v224\n\t"
    "v_and_b32 v224, s92, v224\n\t"
    "v_lshrrev_b32 v253, 16, v225\n\t"
    "v_cvt_pk_bf16_f32 v62, v116, v117\n\t"
    "v_cvt_pk_bf16_f32 v63, v118, v119\n\t"
    "v_mov_b64 v[172:173], s[70:71]\n\t"
    "v_mov_b64 v[174:175], s[72:73]\n\t"
    "v_and_b32 v225, s92, v225\n\t"
    "s_waitcnt lgkmcnt(4)\n\t"
    "v_smfmac_f32_16x16x64_bf16 v[172:175], v[120:123], v[84:91], v124\n\t"
    "ds_read_b64 v[84:85], v224 offset:0\n\t"
    "ds_read_b64 v[86:87], v252 offset:0\n\t"
    "ds_read_b64 v[88:89], v225 offset:0\n\t"
    "ds_read_b64 v[90:91], v253 offset:0\n\t"
    "v_lshrrev_b32 v254, 16, v226\n\t"
    "v_and_b32 v226, s92, v226\n\t"
    "v_lshrrev_b32 v255, 16, v227\n\t"
    "v_and_b32 v227, s92, v227\n\t"
    "s_waitcnt vmcnt(7) lgkmcnt(4)\n\t"
    "v_smfmac_f32_16x16x64_bf16 v[172:175], v[120:123], v[92:99], v124\n\t"
    "ds_read_b64 v[92:93], v226 offset:0\n\t"
    "ds_read_b64 v[94:95], v254 offset:0\n\t"
    "ds_read_b64 v[96:97], v227 offset:0\n\t"
    "ds_read_b64 v[98:99], v255 offset:0\n\t"
    "s_sub_u32 s87, s87, s95\n\t"
    "buffer_load_dwordx4 v[108:111], v127, s[28:31], s87 offen offset:0 sc1\n\t"
    "v_lshrrev_b32 v252, 16, v228\n\t"
    "v_and_b32 v228, s92, v228\n\t"
    "v_lshrrev_b32 v253, 16, v229\n\t"
    "v_mov_b64 v[176:177], s[70:71]\n\t"
    "v_mov_b64 v[178:179], s[72:73]\n\t"
    "v_and_b32 v229, s92, v229\n\t"
    "s_waitcnt lgkmcnt(4)\n\t"
    "v_smfmac_f32_16x16x64_bf16 v[176:179], v[120:123], v[84:91], v124\n\t"
    "ds_read_b64 v[84:85], v228 offset:0\n\t"
    "ds_read_b64 v[86:87], v252 offset:0\n\t"
    "ds_read_b64 v[88:89], v229 offset:0\n\t"
    "ds_read_b64 v[90:91], v253 offset:0\n\t"
    "v_lshrrev_b32 v254, 16, v230\n\t"
    "v_and_b32 v230, s92, v230\n\t"
    "v_lshrrev_b32 v255, 16, v231\n\t"
    "v_and_b32 v231, s92, v231\n\t"
    "s_waitcnt vmcnt(3)\n\t"
    "v_readfirstlane_b32 s65, v164\n\t"
    "s_nop 0\n\t"
    "s_cmp_eq_u32 s65, 0\n\t"
    "s_cbranch_scc1 L_xw0_%=\n\t"
    "L_xwb0_%=:\n\t"
    "buffer_load_dwordx4 v[4:7], v127, s[28:31], s94 offen sc1\n\t"
    "s_waitcnt lgkmcnt(4)\n\t"
    "v_smfmac_f32_16x16x64_bf16 v[176:179], v[120:123], v[92:99], v124\n\t"
    "ds_read_b64 v[92:93], v230 offset:0\n\t"
    "ds_read_b64 v[94:95], v254 offset:0\n\t"
    "ds_read_b64 v[96:97], v231 offset:0\n\t"
    "ds_read_b64 v[98:99], v255 offset:0\n\t"
    "s_add_u32 s85, s85, 0x8000\n\t"
    "buffer_load_dwordx4 v[200:203], v127, s[28:31], s85 offen offset:0 sc1\n\t"
    "v_lshrrev_b32 v252, 16, v232\n\t"
    "v_and_b32 v232, s92, v232\n\t"
    "v_lshrrev_b32 v253, 16, v233\n\t"
    "v_mov_b64 v[180:181], s[70:71]\n\t"
    "v_mov_b64 v[182:183], s[72:73]\n\t"
    "v_and_b32 v233, s92, v233\n\t"
    "s_mov_b64 exec, 0xffffffff\n\t"
    "buffer_store_dword v165, v125, s[28:31], s96 offen offset:256 sc1\n\t"
    "s_mov_b64 exec, -1\n\t"
    "s_add_u32 s61, s61, 0x2000\n\t"
    "buffer_store_dwordx4 v[52:55], v127, s[28:31], s61 offen sc1\n\t"
    "s_waitcnt lgkmcnt(4)\n\t"
    "v_smfmac_f32_16x16x64_bf16 v[180:183], v[120:123], v[84:91], v124\n\t"
    "ds_read_b64 v[84:85], v232 offset:0\n\t"
    "ds_read_b64 v[86:87], v252 offset:0\n\t"
    "ds_read_b64 v[88:89], v233 offset:0\n\t"
    "ds_read_b64 v[90:91], v253 offset:0\n\t"
    "v_lshrrev_b32 v254, 16, v234\n\t"
    "v_and_b32 v234, s92, v234\n\t"
    "v_lshrrev_b32 v255, 16, v235\n\t"
    "v_and_b32 v235, s92, v235\n\t"
    "s_waitcnt lgkmcnt(4)\n\t"
    "v_smfmac_f32_16x16x64_bf16 v[180:183], v[120:123], v[92:99], v124\n\t"
    "ds_read_b64 v[92:93], v234 offset:0\n\t"
    "ds_read_b64 v[94:95], v254 offset:0\n\t"
    "ds_read_b64 v[96:97], v235 offset:0\n\t"
    "ds_read_b64 v[98:99], v255 offset:0\n\t"
    "buffer_load_dwordx4 v[204:207], v127, s[28:31], s87 offen offset:1024 sc1\n\t"
    "v_lshrrev_b32 v252, 16, v168\n\t"
    "v_and_b32 v168, s92, v168\n\t"
    "v_lshrrev_b32 v253, 16, v169\n\t"
    "v_mov_b64 v[184:185], s[70:71]\n\t"
    "v_mov_b64 v[186:187], s[72:73]\n\t"
    "v_and_b32 v169, s92, v169\n\t"
    "s_cmp_lt_u32 s34, 4\n\t"
    "s_cbranch_scc1 L_tp214_%=\n\t"
    "s_setprio 0\n\t"
    "L_tp214_%=:\n\t"
    "s_waitcnt lgkmcnt(4)\n\t"
    "v_smfmac_f32_16x16x64_bf16 v[184:187], v[120:123], v[84:91], v124\n\t"
    "ds_read_b64 v[84:85], v168 offset:0\n\t"
    "ds_read_b64 v[86:87], v252 offset:0\n\t"
    "ds_read_b64 v[88:89], v169 offset:0\n\t"
    "ds_read_b64 v[90:91], v253 offset:0\n\t"
    "v_lshrrev_b32 v254, 16, v170\n\t"
    "v_and_b32 v170, s92, v170\n\t"
    "v_lshrrev_b32 v255, 16, v171\n\t"
    "v_and_b32 v171, s92, v171\n\t"
    "s_add_u32 s94, s94, 0x2000\n\t"
    "buffer_load_dwordx4 v[12:15], v127, s[28:31], s94 offen sc1\n\t"
    "s_waitcnt lgkmcnt(4)\n\t"
    "v_smfmac_f32_16x16x64_bf16 v[184:187], v[120:123], v[92:99], v124\n\t"
    "ds_read_b64 v[92:93], v170 offset:0\n\t"
    "ds_read_b64 v[94:95], v254 offset:0\n\t"
    "ds_read_b64 v[96:97], v171 offset:0\n\t"
    "ds_read_b64 v[98:99], v255 offset:0\n\t"
    "buffer_load_dwordx4 v[144:147], v127, s[28:31], s85 offen offset:1024 sc1\n\t"
    "v_lshrrev_b32 v252, 16, v112\n\t"
    "v_and_b32 v112, s92, v112\n\t"
    "v_lshrrev_b32 v253, 16, v113\n\t"
    "v_mov_b64 v[188:189], s[70:71]\n\t"
    "v_mov_b64 v[190:191], s[72:73]\n\t"
    "v_and_b32 v113, s92, v113\n\t"
    "s_waitcnt lgkmcnt(4)\n\t"
    "v_smfmac_f32_16x16x64_bf16 v[188:191], v[120:123], v[84:91], v124\n\t"
    "ds_read_b64 v[84:85], v112 offset:64000\n\t"
    "ds_read_b64 v[86:87], v252 offset:64000\n\t"
    "ds_read_b64 v[88:89], v113 offset:64000\n\t"
    "ds_read_b64 v[90:91], v253 offset:64000\n\t"
    "v_lshrrev_b32 v254, 16, v114\n\t"
    "v_and_b32 v114, s92, v114\n\t"
    "v_lshrrev_b32 v255, 16, v115\n\t"
    "v_and_b32 v115, s92, v115\n\t"
    "s_waitcnt vmcnt(9) lgkmcnt(4)\n\t"
    "v_smfmac_f32_16x16x64_bf16 v[188:191], v[120:123], v[92:99], v124\n\t"
    "ds_read_b64 v[92:93], v114 offset:64000\n\t"
    "ds_read_b64 v[94:95], v254 offset:64000\n\t"
    "ds_read_b64 v[96:97], v115 offset:64000\n\t"
    "ds_read_b64 v[98:99], v255 offset:64000\n\t"
    "s_sub_u32 s88, s88, s95\n\t"
    "buffer_load_dwordx4 v[148:151], v127, s[28:31], s88 offen offset:0 sc1\n\t"
    "v_lshrrev_b32 v252, 16, v100\n\t"
    "v_and_b32 v100, s92, v100\n\t"
    "v_lshrrev_b32 v253, 16, v101\n\t"
    "v_and_b32 v101, s92, v101\n\t"
    "s_waitcnt lgkmcnt(4)\n\t"
    "v_smfmac_f32_16x16x64_bf16 v[172:175], v[120:123], v[84:91], v124\n\t"
    "ds_read_b64 v[84:85], v100 offset:0\n\t"
    "ds_read_b64 v[86:87], v252 offset:0\n\t"
    "ds_read_b64 v[88:89], v101 offset:0\n\t"
    "ds_read_b64 v[90:91], v253 offset:0\n\t"
    "v_lshrrev_b32 v254, 16, v102\n\t"
    "v_and_b32 v102, s92, v102\n\t"
    "v_lshrrev_b32 v255, 16, v103\n\t"
    "v_and_b32 v103, s92, v103\n\t"
    "s_waitcnt vmcnt(7)\n\t"
    "v_smfmac_f32_16x16x64_bf16 v[172:175], v[156:159], v[0:7], v124\n\t"
    "s_waitcnt lgkmcnt(4)\n\t"
    "v_smfmac_f32_16x16x64_bf16 v[172:175], v[120:123], v[92:99], v124\n\t"
    "ds_read_b64 v[92:93], v102 offset:0\n\t"
    "ds_read_b64 v[94:95], v254 offset:0\n\t"
    "ds_read_b64 v[96:97], v103 offset:0\n\t"
    "ds_read_b64 v[98:99], v255 offset:0\n\t"
    "s_add_u32 s86, s86, 0x8000\n\t"
    "buffer_load_dwordx4 v[152:155], v127, s[28:31], s86 offen offset:0 sc1\n\t"
    "v_lshrrev_b32 v252, 16, v104\n\t"
    "v_and_b32 v104, s92, v104\n\t"
    "v_lshrrev_b32 v253, 16, v105\n\t"
    "v_mov_b64 v[192:193], s[70:71]\n\t"
    "v_mov_b64 v[194:195], s[72:73]\n\t"
    "v_and_b32 v105, s92, v105\n\t"
    "s_add_u32 s61, s61, 0x2000\n\t"
    "buffer_store_dwordx4 v[60:63], v127, s[28:31], s61 offen sc1\n\t"
    "v_smfmac_f32_16x16x64_bf16 v[176:179], v[160:163], v[0:7], v124\n\t"
    "v_pk_mul_f32 v[64:65], v[172:173], v[172:173]\n\t"
    "v_pk_mul_f32 v[66:67], v[174:175], v[174:175]\n\t"
    "v_pk_fma_f32 v[64:65], v[64:65], v[82:83], v[80:81]\n\t"
    "v_pk_fma_f32 v[66:67], v[66:67], v[82:83], v[80:81]\n\t"
    "v_pk_mul_f32 v[64:65], v[64:65], v[172:173]\n\t"
    "s_waitcnt lgkmcnt(4)\n\t"
    "v_smfmac_f32_16x16x64_bf16 v[192:195], v[120:123], v[84:91], v124\n\t"
    "ds_read_b64 v[84:85], v104 offset:64000\n\t"
    "ds_read_b64 v[86:87], v252 offset:64000\n\t"
    "ds_read_b64 v[88:89], v105 offset:64000\n\t"
    "ds_read_b64 v[90:91], v253 offset:64000\n\t"
    "v_lshrrev_b32 v254, 16, v106\n\t"
    "v_and_b32 v106, s92, v106\n\t"
    "v_lshrrev_b32 v255, 16, v107\n\t"
    "v_and_b32 v107, s92, v107\n\t"
    "s_mov_b64 exec, 1\n\t"
    "buffer_load_dword v164, off, s[28:31], s93 offset:128 sc1\n\t"
    "s_mov_b64 exec, -1\n\t"
    "v_pk_mul_f32 v[66:67], v[66:67], v[174:175]\n\t"
    "v_exp_f32 v68, v64\n\t"
    "v_exp_f32 v69, v65\n\t"
    "v_exp_f32 v70, v66\n\t"
    "v_exp_f32 v71, v67\n\t"
    "s_waitcnt lgkmcnt(4)\n\t"
    "v_smfmac_f32_16x16x64_bf16 v[192:195], v[120:123], v[92:99], v124\n\t"
    "ds_read_b64 v[92:93], v106 offset:64000\n\t"
    "ds_read_b64 v[94:95], v254 offset:64000\n\t"
    "ds_read_b64 v[96:97], v107 offset:64000\n\t"
    "ds_read_b64 v[98:99], v255 offset:64000\n\t"
    "buffer_load_dwordx4 v[244:247], v127, s[28:31], s88 offen offset:1024 sc1\n\t"
    "v_lshrrev_b32 v252, 16, v108\n\t"
    "v_and_b32 v108, s92, v108\n\t"
    "v_lshrrev_b32 v253, 16, v109\n\t"
    "v_and_b32 v109, s92, v109\n\t"
    "v_pk_fma_f32 v[68:69], v[68:69], s[40:41], s[40:41]\n\t"
    "v_pk_fma_f32 v[70:71], v[70:71], s[42:43], s[42:43]\n\t"
    "v_rcp_f32 v68, v68\n\t"
    "v_rcp_f32 v69, v69\n\t"
    "v_rcp_f32 v70, v70\n\t"
    "s_waitcnt lgkmcnt(4)\n\t"
    "v_smfmac_f32_16x16x64_bf16 v[176:179], v[120:123], v[84:91], v124\n\t"
    "ds_read_b64 v[84:85], v108 offset:0\n\t"
    "ds_read_b64 v[86:87], v252 offset:0\n\t"
    "ds_read_b64 v[88:89], v109 offset:0\n\t"
    "ds_read_b64 v[90:91], v253 offset:0\n\t"
    "v_lshrrev_b32 v254, 16, v110\n\t"
    "v_and_b32 v110, s92, v110\n\t"
    "v_lshrrev_b32 v255, 16, v111\n\t"
    "v_and_b32 v111, s92, v111\n\t"
    "s_waitcnt vmcnt(6)\n\t"
    "v_smfmac_f32_16x16x64_bf16 v[180:183], v[156:159], v[8:15], v124\n\t"
    "v_rcp_f32 v71, v71\n\t"
    "v_pk_mul_f32 v[68:69], v[68:69], v[172:173]\n\t"
    "v_pk_fma_f32 v[68:69], v[70:71], v[174:175], v[68:69]\n\t"
    "v_add_f32 v236, v68, v69\n\t"
    "s_waitcnt lgkmcnt(4)\n\t"
    "v_smfmac_f32_16x16x64_bf16 v[176:179], v[120:123], v[92:99], v124\n\t"
    "ds_read_b64 v[92:93], v110 offset:0\n\t"
    "ds_read_b64 v[94:95], v254 offset:0\n\t"
    "ds_read_b64 v[96:97], v111 offset:0\n\t"
    "ds_read_b64 v[98:99], v255 offset:0\n\t"
    "buffer_load_dwordx4 v[248:251], v127, s[28:31], s86 offen offset:1024 sc1\n\t"
    "v_lshrrev_b32 v252, 16, v200\n\t"
    "v_and_b32 v200, s92, v200\n\t"
    "v_lshrrev_b32 v253, 16, v201\n\t"
    "v_mov_b64 v[196:197], s[70:71]\n\t"
    "v_mov_b64 v[198:199], s[72:73]\n\t"
    "v_and_b32 v201, s92, v201\n\t"
    "v_smfmac_f32_16x16x64_bf16 v[184:187], v[160:163], v[8:15], v124\n\t"
    "v_pk_mul_f32 v[72:73], v[176:177], v[176:177]\n\t"
    "v_pk_mul_f32 v[74:75], v[178:179], v[178:179]\n\t"
    "v_pk_fma_f32 v[72:73], v[72:73], v[82:83], v[80:81]\n\t"
    "v_pk_fma_f32 v[74:75], v[74:75], v[82:83], v[80:81]\n\t"
    "v_pk_mul_f32 v[72:73], v[72:73], v[176:177]\n\t"
    "s_waitcnt lgkmcnt(4)\n\t"
    "v_smfmac_f32_16x16x64_bf16 v[196:199], v[120:123], v[84:91], v124\n\t"
    "ds_read_b64 v[84:85], v200 offset:64000\n\t"
    "ds_read_b64 v[86:87], v252 offset:64000\n\t"
    "ds_read_b64 v[88:89], v201 offset:64000\n\t"
    "ds_read_b64 v[90:91], v253 offset:64000\n\t"
    "v_lshrrev_b32 v254, 16, v202\n\t"
    "v_and_b32 v202, s92, v202\n\t"
    "v_lshrrev_b32 v255, 16, v203\n\t"
    "v_and_b32 v203, s92, v203\n\t"
    "v_pk_mul_f32 v[74:75], v[74:75], v[178:179]\n\t"
    "v_exp_f32 v76, v72\n\t"
    "v_exp_f32 v77, v73\n\t"
    "v_exp_f32 v78, v74\n\t"
    "v_exp_f32 v79, v75\n\t"
    "s_waitcnt lgkmcnt(4)\n\t"
    "v_smfmac_f32_16x16x64_bf16 v[196:199], v[120:123], v[92:99], v124\n\t"
    "ds_read_b64 v[92:93], v202 offset:64000\n\t"
    "ds_read_b64 v[94:95], v254 offset:64000\n\t"
    "ds_read_b64 v[96:97], v203 offset:64000\n\t"
    "ds_read_b64 v[98:99], v255 offset:64000\n\t"
    "s_sub_u32 s89, s89, s95\n\t"
    "buffer_load_dwordx4 v[212:215], v127, s[28:31], s89 offen offset:0 sc1\n\t"
    "v_lshrrev_b32 v252, 16, v204\n\t"
    "v_and_b32 v204, s92, v204\n\t"
    "v_lshrrev_b32 v253, 16, v205\n\t"
    "v_and_b32 v205, s92, v205\n\t"
    "v_pk_fma_f32 v[76:77], v[76:77], s[40:41], s[40:41]\n\t"
    "v_pk_fma_f32 v[78:79], v[78:79], s[42:43], s[42:43]\n\t"
    "v_rcp_f32 v76, v76\n\t"
    "v_rcp_f32 v77, v77\n\t"
    "v_rcp_f32 v78, v78\n\t"
    "s_waitcnt lgkmcnt(4)\n\t"
    "v_smfmac_f32_16x16x64_bf16 v[180:183], v[120:123], v[84:91], v124\n\t"
    "ds_read_b64 v[84:85], v204 offset:0\n\t"
    "ds_read_b64 v[86:87], v252 offset:0\n\t"
    "ds_read_b64 v[88:89], v205 offset:0\n\t"
    "ds_read_b64 v[90:91], v253 offset:0\n\t"
    "v_lshrrev_b32 v254, 16, v206\n\t"
    "v_and_b32 v206, s92, v206\n\t"
    "v_lshrrev_b32 v255, 16, v207\n\t"
    "v_and_b32 v207, s92, v207\n\t"
    "s_waitcnt vmcnt(3)\n\t"
    "v_readfirstlane_b32 s65, v164\n\t"
    "s_nop 0\n\t"
    "s_cmp_eq_u32 s65, 0\n\t"
    "s_cbranch_scc1 L_xw1_%=\n\t"
    "L_xwb1_%=:\n\t"
    "s_add_u32 s94, s94, 0x2000\n\t"
    "buffer_load_dwordx4 v[20:23], v127, s[28:31], s94 offen sc1\n\t"
    "v_rcp_f32 v79, v79\n\t"
    "v_pk_mul_f32 v[76:77], v[76:77], v[176:177]\n\t"
    "v_pk_fma_f32 v[76:77], v[78:79], v[178:179], v[76:77]\n\t"
    "v_add_f32 v237, v76, v77\n\t"
    "s_cmp_lt_u32 s34, 4\n\t"
    "s_cbranch_scc1 L_tp227_%=\n\t"
    "s_setprio 1\n\t"
    "L_tp227_%=:\n\t"
    "s_waitcnt lgkmcnt(4)\n\t"
    "v_smfmac_f32_16x16x64_bf16 v[180:183], v[120:123], v[92:99], v124\n\t"
    "ds_read_b64 v[92:93], v206 offset:0\n\t"
    "ds_read_b64 v[94:95], v254 offset:0\n\t"
    "ds_read_b64 v[96:97], v207 offset:0\n\t"
    "ds_read_b64 v[98:99], v255 offset:0\n\t"
    "s_add_u32 s87, s87, 0x8000\n\t"
    "buffer_load_dwordx4 v[216:219], v127, s[28:31], s87 offen offset:0 sc1\n\t"
    "v_lshrrev_b32 v252, 16, v144\n\t"
    "v_and_b32 v144, s92, v144\n\t"
    "v_lshrrev_b32 v253, 16, v145\n\t"
    "v_mov_b64 v[200:201], s[70:71]\n\t"
    "v_mov_b64 v[202:203], s[72:73]\n\t"
    "v_and_b32 v145, s92, v145\n\t"
    "s_mov_b64 exec, 0xffffffff\n\t"
    "buffer_store_dword v165, v125, s[28:31], s96 offen offset:384 sc1\n\t"
    "s_mov_b64 exec, -1\n\t"
    "v_pk_mul_f32 v[64:65], v[180:181], v[180:181]\n\t"
    "v_pk_mul_f32 v[66:67], v[182:183], v[182:183]\n\t"
    "v_pk_fma_f32 v[64:65], v[64:65], v[82:83], v[80:81]\n\t"
    "v_pk_fma_f32 v[66:67], v[66:67], v[82:83], v[80:81]\n\t"
    "v_pk_mul_f32 v[64:65], v[64:65], v[180:181]\n\t"
    "s_waitcnt lgkmcnt(4)\n\t"
    "v_smfmac_f32_16x16x64_bf16 v[200:203], v[120:123], v[84:91], v124\n\t"
    "ds_read_b64 v[84:85], v144 offset:64000\n\t"
    "ds_read_b64 v[86:87], v252 offset:64000\n\t"
    "ds_read_b64 v[88:89], v145 offset:64000\n\t"
    "ds_read_b64 v[90:91], v253 offset:64000\n\t"
    "v_lshrrev_b32 v254, 16, v146\n\t"
    "v_and_b32 v146, s92, v146\n\t"
    "v_lshrrev_b32 v255, 16, v147\n\t"
    "v_and_b32 v147, s92, v147\n\t"
    "v_pk_mul_f32 v[66:67], v[66:67], v[182:183]\n\t"
    "v_exp_f32 v68, v64\n\t"
    "v_exp_f32 v69, v65\n\t"
    "v_exp_f32 v70, v66\n\t"
    "v_exp_f32 v71, v67\n\t"
    "s_waitcnt lgkmcnt(4)\n\t"
    "v_smfmac_f32_16x16x64_bf16 v[200:203], v[120:123], v[92:99], v124\n\t"
    "ds_read_b64 v[92:93], v146 offset:64000\n\t"
    "ds_read_b64 v[94:95], v254 offset:64000\n\t"
    "ds_read_b64 v[96:97], v147 offset:64000\n\t"
    "ds_read_b64 v[98:99], v255 offset:64000\n\t"
    "buffer_load_dwordx4 v[220:223], v127, s[28:31], s89 offen offset:1024 sc1\n\t"
    "v_lshrrev_b32 v252, 16, v148\n\t"
    "v_and_b32 v148, s92, v148\n\t"
    "v_lshrrev_b32 v253, 16, v149\n\t"
    "v_and_b32 v149, s92, v149\n\t"
    "v_pk_fma_f32 v[68:69], v[68:69], s[40:41], s[40:41]\n\t"
    "v_pk_fma_f32 v[70:71], v[70:71], s[42:43], s[42:43]\n\t"
    "v_rcp_f32 v68, v68\n\t"
    "v_rcp_f32 v69, v69\n\t"
    "v_rcp_f32 v70, v70\n\t"
    "s_waitcnt lgkmcnt(4)\n\t"
    "v_smfmac_f32_16x16x64_bf16 v[184:187], v[120:123], v[84:91], v124\n\t"
    "ds_read_b64 v[84:85], v148 offset:0\n\t"
    "ds_read_b64 v[86:87], v252 offset:0\n\t"
    "ds_read_b64 v[88:89], v149 offset:0\n\t"
    "ds_read_b64 v[90:91], v253 offset:0\n\t"
    "v_lshrrev_b32 v254, 16, v150\n\t"
    "v_and_b32 v150, s92, v150\n\t"
    "v_lshrrev_b32 v255, 16, v151\n\t"
    "v_and_b32 v151, s92, v151\n\t"
    "s_add_u32 s94, s94, 0x2000\n\t"
    "buffer_load_dwordx4 v[28:31], v127, s[28:31], s94 offen sc1\n\t"
    "v_rcp_f32 v71, v71\n\t"
    "v_pk_mul_f32 v[68:69], v[68:69], v[180:181]\n\t"
    "v_pk_fma_f32 v[68:69], v[70:71], v[182:183], v[68:69]\n\t"
    "v_add_f32 v238, v68, v69\n\t"
    "s_waitcnt lgkmcnt(4)\n\t"
    "v_smfmac_f32_16x16x64_bf16 v[184:187], v[120:123], v[92:99], v124\n\t"
    "ds_read_b64 v[92:93], v150 offset:0\n\t"
    "ds_read_b64 v[94:95], v254 offset:0\n\t"
    "ds_read_b64 v[96:97], v151 offset:0\n\t"
    "ds_read_b64 v[98:99], v255 offset:0\n\t"
    "buffer_load_dwordx4 v[116:119], v127, s[28:31], s87 offen offset:1024 sc1\n\t"
    "v_lshrrev_b32 v252, 16, v152\n\t"
    "v_and_b32 v152, s92, v152\n\t"
    "v_lshrrev_b32 v253, 16, v153\n\t"
    "v_mov_b64 v[204:205], s[70:71]\n\t"
    "v_mov_b64 v[206:207], s[72:73]\n\t"
    "v_and_b32 v153, s92, v153\n\t"
    "v_pk_mul_f32 v[72:73], v[184:185], v[184:185]\n\t"
    "v_pk_mul_f32 v[74:75], v[186:187], v[186:187]\n\t"
    "v_pk_fma_f32 v[72:73], v[72:73], v[82:83], v[80:81]\n\t"
    "v_pk_fma_f32 v[74:75], v[74:75], v[82:83], v[80:81]\n\t"
    "v_pk_mul_f32 v[72:73], v[72:73], v[184:185]\n\t"
    "s_waitcnt lgkmcnt(4)\n\t"
    "v_smfmac_f32_16x16x64_bf16 v[204:207], v[120:123], v[84:91], v124\n\t"
    "ds_read_b64 v[84:85], v152 offset:64000\n\t"
    "ds_read_b64 v[86:87], v252 offset:64000\n\t"
    "ds_read_b64 v[88:89], v153 offset:64000\n\t"
    "ds_read_b64 v[90:91], v253 offset:64000\n\t"
    "v_lshrrev_b32 v254, 16, v154\n\t"
    "v_and_b32 v154, s92, v154\n\t"
    "v_lshrrev_b32 v255, 16, v155\n\t"
    "v_and_b32 v155, s92, v155\n\t"
    "v_pk_mul_f32 v[74:75], v[74:75], v[186:187]\n\t"
    "v_exp_f32 v76, v72\n\t"
    "v_exp_f32 v77, v73\n\t"
    "v_exp_f32 v78, v74\n\t"
    "v_exp_f32 v79, v75\n\t"
    "s_waitcnt vmcnt(8) lgkmcnt(4)\n\t"
    "v_smfmac_f32_16x16x64_bf16 v[204:207], v[120:123], v[92:99], v124\n\t"
    "ds_read_b64 v[92:93], v154 offset:64000\n\t"
    "ds_read_b64 v[94:95], v254 offset:64000\n\t"
    "ds_read_b64 v[96:97], v155 offset:64000\n\t"
    "ds_read_b64 v[98:99], v255 offset:64000\n\t"
    "s_sub_u32 s90, s90, s95\n\t"
    "buffer_load_dwordx4 v[224:227], v127, s[28:31], s90 offen offset:0 sc1\n\t"
    "v_lshrrev_b32 v252, 16, v244\n\t"
    "v_and_b32 v244, s92, v244\n\t"
    "v_lshrrev_b32 v253, 16, v245\n\t"
    "v_and_b32 v245, s92, v245\n\t"
    "v_pk_fma_f32 v[76:77], v[76:77], s[40:41], s[40:41]\n\t"
    "v_pk_fma_f32 v[78:79], v[78:79], s[42:43], s[42:43]\n\t"
    "v_rcp_f32 v76, v76\n\t"
    "v_rcp_f32 v77, v77\n\t"
    "v_rcp_f32 v78, v78\n\t"
    "s_waitcnt lgkmcnt(4)\n\t"
    "v_smfmac_f32_16x16x64_bf16 v[188:191], v[120:123], v[84:91], v124\n\t"
    "ds_read_b64 v[84:85], v244 offset:0\n\t"
    "ds_read_b64 v[86:87], v252 offset:0\n\t"
    "ds_read_b64 v[88:89], v245 offset:0\n\t"
    "ds_read_b64 v[90:91], v253 offset:0\n\t"
    "v_lshrrev_b32 v254, 16, v246\n\t"
    "v_and_b32 v246, s92, v246\n\t"
    "v_lshrrev_b32 v255, 16, v247\n\t"
    "v_and_b32 v247, s92, v247\n\t"
    "s_waitcnt vmcnt(6)\n\t"
    "v_smfmac_f32_16x16x64_bf16 v[188:191], v[156:159], v[16:23], v124\n\t"
    "v_rcp_f32 v79, v79\n\t"
    "v_pk_mul_f32 v[76:77], v[76:77], v[184:185]\n\t"
    "v_pk_fma_f32 v[76:77], v[78:79], v[186:187], v[76:77]\n\t"
    "v_add_f32 v239, v76, v77\n\t"
    "v_lshrrev_b32 v147, 4, v127\n\t"
    "v_add_u32 v148, 140288, v127\n\t"
    "s_lshl_b32 s65, %2, 12\n\t"
    "s_add_u32 s65, s65, 16777216\n\t"
    "buffer_load_dwordx4 v[0:3], v127, s[28:31], s65 offen offset:0 sc1\n\t"
    "s_waitcnt lgkmcnt(4)\n\t"
    "v_smfmac_f32_16x16x64_bf16 v[188:191], v[120:123], v[92:99], v124\n\t"
    "ds_read_b64 v[92:93], v246 offset:0\n\t"
    "ds_read_b64 v[94:95], v254 offset:0\n\t"
    "ds_read_b64 v[96:97], v247 offset:0\n\t"
    "ds_read_b64 v[98:99], v255 offset:0\n\t"
    "s_add_u32 s88, s88, 0x8000\n\t"
    "buffer_load_dwordx4 v[228:231], v127, s[28:31], s88 offen offset:0 sc1\n\t"
    "v_lshrrev_b32 v252, 16, v248\n\t"
    "v_and_b32 v248, s92, v248\n\t"
    "v_lshrrev_b32 v253, 16, v249\n\t"
    "v_mov_b64 v[208:209], s[70:71]\n\t"
    "v_mov_b64 v[210:211], s[72:73]\n\t"
    "v_and_b32 v249, s92, v249\n\t"
    "v_smfmac_f32_16x16x64_bf16 v[192:195], v[160:163], v[16:23], v124\n\t"
    "v_pk_mul_f32 v[64:65], v[188:189], v[188:189]\n\t"
    "v_pk_mul_f32 v[66:67], v[190:191], v[190:191]\n\t"
    "v_pk_fma_f32 v[64:65], v[64:65], v[82:83], v[80:81]\n\t"
    "v_pk_fma_f32 v[66:67], v[66:67], v[82:83], v[80:81]\n\t"
    "v_pk_mul_f32 v[64:65], v[64:65], v[188:189]\n\t"
    "s_lshl_b32 s65, %2, 12\n\t"
    "s_add_u32 s65, s65, 16777216\n\t"
    "buffer_load_dwordx4 v[4:7], v127, s[28:31], s65 offen offset:1024 sc1\n\t"
    "s_waitcnt lgkmcnt(4)\n\t"
    "v_smfmac_f32_16x16x64_bf16 v[208:211], v[120:123], v[84:91], v124\n\t"
    "ds_read_b64 v[84:85], v248 offset:64000\n\t"
    "ds_read_b64 v[86:87], v252 offset:64000\n\t"
    "ds_read_b64 v[88:89], v249 offset:64000\n\t"
    "ds_read_b64 v[90:91], v253 offset:64000\n\t"
    "v_lshrrev_b32 v254, 16, v250\n\t"
    "v_and_b32 v250, s92, v250\n\t"
    "v_lshrrev_b32 v255, 16, v251\n\t"
    "v_and_b32 v251, s92, v251\n\t"
    "s_mov_b64 exec, 1\n\t"
    "buffer_load_dword v164, off, s[28:31], s93 offset:256 sc1\n\t"
    "s_mov_b64 exec, -1\n\t"
    "v_pk_mul_f32 v[66:67], v[66:67], v[190:191]\n\t"
    "v_exp_f32 v68, v64\n\t"
    "v_exp_f32 v69, v65\n\t"
    "v_exp_f32 v70, v66\n\t"
    "v_exp_f32 v71, v67\n\t"
    "s_lshl_b32 s65, %2, 12\n\t"
    "s_add_u32 s65, s65, 16777216\n\t"
    "buffer_load_dwordx4 v[8:11], v127, s[28:31], s65 offen offset:2048 sc1\n\t"
    "s_waitcnt lgkmcnt(4)\n\t"
    "v_smfmac_f32_16x16x64_bf16 v[208:211], v[120:123], v[92:99], v124\n\t"
    "ds_read_b64 v[92:93], v250 offset:64000\n\t"
    "ds_read_b64 v[94:95], v254 offset:64000\n\t"
    "ds_read_b64 v[96:97], v251 offset:64000\n\t"
    "ds_read_b64 v[98:99], v255 offset:64000\n\t"
    "buffer_load_dwordx4 v[232:235], v127, s[28:31], s90 offen offset:1024 sc1\n\t"
    "v_lshrrev_b32 v252, 16, v212\n\t"
    "v_and_b32 v212, s92, v212\n\t"
    "v_lshrrev_b32 v253, 16, v213\n\t"
    "v_and_b32 v213, s92, v213\n\t"
    "v_pk_fma_f32 v[68:69], v[68:69], s[40:41], s[40:41]\n\t"
    "v_pk_fma_f32 v[70:71], v[70:71], s[42:43], s[42:43]\n\t"
    "v_rcp_f32 v68, v68\n\t"
    "v_rcp_f32 v69, v69\n\t"
    "v_rcp_f32 v70, v70\n\t"
    "s_lshl_b32 s65, %2, 12\n\t"
    "s_add_u32 s65, s65, 16777216\n\t"
    "buffer_load_dwordx4 v[12:15], v127, s[28:31], s65 offen offset:3072 sc1\n\t"
    "s_waitcnt lgkmcnt(4)\n\t"
    "v_smfmac_f32_16x16x64_bf16 v[192:195], v[120:123], v[84:91], v124\n\t"
    "ds_read_b64 v[84:85], v212 offset:0\n\t"
    "ds_read_b64 v[86:87], v252 offset:0\n\t"
    "ds_read_b64 v[88:89], v213 offset:0\n\t"
    "ds_read_b64 v[90:91], v253 offset:0\n\t"
    "v_lshrrev_b32 v254, 16, v214\n\t"
    "v_and_b32 v214, s92, v214\n\t"
    "v_lshrrev_b32 v255, 16, v215\n\t"
    "v_and_b32 v215, s92, v215\n\t"
    "s_waitcnt vmcnt(9)\n\t"
    "v_smfmac_f32_16x16x64_bf16 v[196:199], v[156:159], v[24:31], v124\n\t"
    "v_rcp_f32 v71, v71\n\t"
    "v_pk_mul_f32 v[68:69], v[68:69], v[188:189]\n\t"
    "v_pk_fma_f32 v[68:69], v[70:71], v[190:191], v[68:69]\n\t"
    "v_add_f32 v240, v68, v69\n\t"
    "ds_read_b128 v[16:19], v148 offset:4096\n\t"
    "s_waitcnt lgkmcnt(5)\n\t"
    "v_smfmac_f32_16x16x64_bf16 v[192:195], v[120:123], v[92:99], v124\n\t"
    "ds_read_b64 v[92:93], v214 offset:0\n\t"
    "ds_read_b64 v[94:95], v254 offset:0\n\t"
    "ds_read_b64 v[96:97], v215 offset:0\n\t"
    "ds_read_b64 v[98:99], v255 offset:0\n\t"
    "buffer_load_dwordx4 v[168:171], v127, s[28:31], s88 offen offset:1024 sc1\n\t"
    "v_lshrrev_b32 v252, 16, v216\n\t"
    "v_and_b32 v216, s92, v216\n\t"
    "v_lshrrev_b32 v253, 16, v217\n\t"
    "v_mov_b64 v[172:173], s[70:71]\n\t"
    "v_mov_b64 v[174:175], s[72:73]\n\t"
    "v_and_b32 v217, s92, v217\n\t"
    "v_smfmac_f32_16x16x64_bf16 v[200:203], v[160:163], v[24:31], v124\n\t"
    "v_pk_mul_f32 v[72:73], v[192:193], v[192:193]\n\t"
    "v_pk_mul_f32 v[74:75], v[194:195], v[194:195]\n\t"
    "v_pk_fma_f32 v[72:73], v[72:73], v[82:83], v[80:81]\n\t"
    "v_pk_fma_f32 v[74:75], v[74:75], v[82:83], v[80:81]\n\t"
    "v_pk_mul_f32 v[72:73], v[72:73], v[192:193]\n\t"
    "ds_read_b128 v[20:23], v148 offset:5120\n\t"
    "s_waitcnt lgkmcnt(6)\n\t"
    "v_smfmac_f32_16x16x64_bf16 v[172:175], v[120:123], v[84:91], v124\n\t"
    "ds_read_b64 v[84:85], v216 offset:64000\n\t"
    "ds_read_b64 v[86:87], v252 offset:64000\n\t"
    "ds_read_b64 v[88:89], v217 offset:64000\n\t"
    "ds_read_b64 v[90:91], v253 offset:64000\n\t"
    "v_lshrrev_b32 v254, 16, v218\n\t"
    "v_and_b32 v218, s92, v218\n\t"
    "v_lshrrev_b32 v255, 16, v219\n\t"
    "v_and_b32 v219, s92, v219\n\t"
    "v_pk_mul_f32 v[74:75], v[74:75], v[194:195]\n\t"
    "v_exp_f32 v76, v72\n\t"
    "v_exp_f32 v77, v73\n\t"
    "v_exp_f32 v78, v74\n\t"
    "v_exp_f32 v79, v75\n\t"
    "ds_read_b128 v[24:27], v148 offset:6144\n\t"
    "s_waitcnt lgkmcnt(6)\n\t"
    "v_smfmac_f32_16x16x64_bf16 v[172:175], v[120:123], v[92:99], v124\n\t"
    "ds_read_b64 v[92:93], v218 offset:64000\n\t"
    "ds_read_b64 v[94:95], v254 offset:64000\n\t"
    "ds_read_b64 v[96:97], v219 offset:64000\n\t"
    "ds_read_b64 v[98:99], v255 offset:64000\n\t"
    "s_sub_u32 s91, s91, s95\n\t"
    "buffer_load_dwordx4 v[112:115], v127, s[28:31], s91 offen offset:0 sc1\n\t"
    "v_lshrrev_b32 v252, 16, v220\n\t"
    "v_and_b32 v220, s92, v220\n\t"
    "v_lshrrev_b32 v253, 16, v221\n\t"
    "v_and_b32 v221, s92, v221\n\t"
    "v_pk_fma_f32 v[76:77], v[76:77], s[40:41], s[40:41]\n\t"
    "v_pk_fma_f32 v[78:79], v[78:79], s[42:43], s[42:43]\n\t"
    "v_rcp_f32 v76, v76\n\t"
    "v_rcp_f32 v77, v77\n\t"
    "v_rcp_f32 v78, v78\n\t"
    "ds_read_b128 v[28:31], v148 offset:7168\n\t"
    "s_waitcnt lgkmcnt(6)\n\t"
    "v_smfmac_f32_16x16x64_bf16 v[196:199], v[120:123], v[84:91], v124\n\t"
    "ds_read_b64 v[84:85], v220 offset:0\n\t"
    "ds_read_b64 v[86:87], v252 offset:0\n\t"
    "ds_read_b64 v[88:89], v221 offset:0\n\t"
    "ds_read_b64 v[90:91], v253 offset:0\n\t"
    "v_lshrrev_b32 v254, 16, v222\n\t"
    "v_and_b32 v222, s92, v222\n\t"
    "v_lshrrev_b32 v255, 16, v223\n\t"
    "v_and_b32 v223, s92, v223\n\t"
    "s_waitcnt vmcnt(5)\n\t"
    "v_readfirstlane_b32 s65, v164\n\t"
    "s_nop 0\n\t"
    "s_cmp_eq_u32 s65, 0\n\t"
    "s_cbranch_scc1 L_xw2_%=\n\t"
    "L_xwb2_%=:\n\t"
    "s_add_u32 s94, s94, 0x2000\n\t"
    "buffer_load_dwordx4 v[36:39], v127, s[28:31], s94 offen sc1\n\t"
    "v_rcp_f32 v79, v79\n\t"
    "v_pk_mul_f32 v[76:77], v[76:77], v[192:193]\n\t"
    "v_pk_fma_f32 v[76:77], v[78:79], v[194:195], v[76:77]\n\t"
    "v_add_f32 v241, v76, v77\n\t"
    "s_waitcnt lgkmcnt(5)\n\t"
    "v_smfmac_f32_16x16x64_bf16 v[196:199], v[120:123], v[92:99], v124\n\t"
    "ds_read_b64 v[92:93], v222 offset:0\n\t"
    "ds_read_b64 v[94:95], v254 offset:0\n\t"
    "ds_read_b64 v[96:97], v223 offset:0\n\t"
    "ds_read_b64 v[98:99], v255 offset:0\n\t"
    "s_add_u32 s89, s89, 0x8000\n\t"
    "buffer_load_dwordx4 v[100:103], v127, s[28:31], s89 offen offset:0 sc1\n\t"
    "v_lshrrev_b32 v252, 16, v116\n\t"
    "v_and_b32 v116, s92, v116\n\t"
    "v_lshrrev_b32 v253, 16, v117\n\t"
    "v_mov_b64 v[176:177], s[70:71]\n\t"
    "v_mov_b64 v[178:179], s[72:73]\n\t"
    "v_and_b32 v117, s92, v117\n\t"
    "v_pk_mul_f32 v[64:65], v[196:197], v[196:197]\n\t"
    "v_pk_mul_f32 v[66:67], v[198:199], v[198:199]\n\t"
    "v_pk_fma_f32 v[64:65], v[64:65], v[82:83], v[80:81]\n\t"
    "v_pk_fma_f32 v[66:67], v[66:67], v[82:83], v[80:81]\n\t"
    "v_pk_mul_f32 v[64:65], v[64:65], v[196:197]\n\t"
    "s_waitcnt lgkmcnt(4)\n\t"
    "v_smfmac_f32_16x16x64_bf16 v[176:179], v[120:123], v[84:91], v124\n\t"
    "ds_read_b64 v[84:85], v116 offset:64000\n\t"
    "ds_read_b64 v[86:87], v252 offset:64000\n\t"
    "ds_read_b64 v[88:89], v117 offset:64000\n\t"
    "ds_read_b64 v[90:91], v253 offset:64000\n\t"
    "v_lshrrev_b32 v254, 16, v118\n\t"
    "v_and_b32 v118, s92, v118\n\t"
    "v_lshrrev_b32 v255, 16, v119\n\t"
    "v_and_b32 v119, s92, v119\n\t"
    "v_pk_mul_f32 v[66:67], v[66:67], v[198:199]\n\t"
    "v_exp_f32 v68, v64\n\t"
    "v_exp_f32 v69, v65\n\t"
    "v_exp_f32 v70, v66\n\t"
    "v_exp_f32 v71, v67\n\t"
    "s_waitcnt lgkmcnt(4)\n\t"
    "v_smfmac_f32_16x16x64_bf16 v[176:179], v[120:123], v[92:99], v124\n\t"
    "ds_read_b64 v[92:93], v118 offset:64000\n\t"
    "ds_read_b64 v[94:95], v254 offset:64000\n\t"
    "ds_read_b64 v[96:97], v119 offset:64000\n\t"
    "ds_read_b64 v[98:99], v255 offset:64000\n\t"
    "buffer_load_dwordx4 v[104:107], v127, s[28:31], s91 offen offset:1024 sc1\n\t"
    "v_lshrrev_b32 v252, 16, v224\n\t"
    "v_and_b32 v224, s92, v224\n\t"
    "v_lshrrev_b32 v253, 16, v225\n\t"
    "v_and_b32 v225, s92, v225\n\t"
    "v_pk_fma_f32 v[68:69], v[68:69], s[40:41], s[40:41]\n\t"
    "v_pk_fma_f32 v[70:71], v[70:71], s[42:43], s[42:43]\n\t"
    "v_rcp_f32 v68, v68\n\t"
    "v_rcp_f32 v69, v69\n\t"
    "v_rcp_f32 v70, v70\n\t"
    "s_waitcnt lgkmcnt(4)\n\t"
    "v_smfmac_f32_16x16x64_bf16 v[200:203], v[120:123], v[84:91], v124\n\t"
    "ds_read_b64 v[84:85], v224 offset:0\n\t"
    "ds_read_b64 v[86:87], v252 offset:0\n\t"
    "ds_read_b64 v[88:89], v225 offset:0\n\t"
    "ds_read_b64 v[90:91], v253 offset:0\n\t"
    "v_lshrrev_b32 v254, 16, v226\n\t"
    "v_and_b32 v226, s92, v226\n\t"
    "v_lshrrev_b32 v255, 16, v227\n\t"
    "v_and_b32 v227, s92, v227\n\t"
    "s_add_u32 s94, s94, 0x2000\n\t"
    "buffer_load_dwordx4 v[44:47], v127, s[28:31], s94 offen sc1\n\t"
    "v_rcp_f32 v71, v71\n\t"
    "v_pk_mul_f32 v[68:69], v[68:69], v[196:197]\n\t"
    "v_pk_fma_f32 v[68:69], v[70:71], v[198:199], v[68:69]\n\t"
    "v_add_f32 v242, v68, v69\n\t"
    "s_lshl_b32 s46, %2, 2\n\t"
    "s_add_u32 s46, s46, 49294336\n\t"
    "s_mov_b64 exec, 1\n\t"
    "buffer_load_dword v144, off, s[28:31], s46 sc1\n\t"
    "s_mov_b64 exec, -1\n\t"
    "s_waitcnt lgkmcnt(4)\n\t"
    "v_smfmac_f32_16x16x64_bf16 v[200:203], v[120:123], v[92:99], v124\n\t"
    "ds_read_b64 v[92:93], v226 offset:0\n\t"
    "ds_read_b64 v[94:95], v254 offset:0\n\t"
    "ds_read_b64 v[96:97], v227 offset:0\n\t"
    "ds_read_b64 v[98:99], v255 offset:0\n\t"
    "buffer_load_dwordx4 v[108:111], v127, s[28:31], s89 offen offset:1024 sc1\n\t"
    "v_lshrrev_b32 v252, 16, v228\n\t"
    "v_and_b32 v228, s92, v228\n\t"
    "v_lshrrev_b32 v253, 16, v229\n\t"
    "v_mov_b64 v[180:181], s[70:71]\n\t"
    "v_mov_b64 v[182:183], s[72:73]\n\t"
    "v_and_b32 v229, s92, v229\n\t"
    "v_pk_mul_f32 v[72:73], v[200:201], v[200:201]\n\t"
    "v_pk_mul_f32 v[74:75], v[202:203], v[202:203]\n\t"
    "v_pk_fma_f32 v[72:73], v[72:73], v[82:83], v[80:81]\n\t"
    "v_pk_fma_f32 v[74:75], v[74:75], v[82:83], v[80:81]\n\t"
    "v_pk_mul_f32 v[72:73], v[72:73], v[200:201]\n\t"
    "s_cmp_lt_u32 s34, 4\n\t"
    "s_cbranch_scc1 L_tp248_%=\n\t"
    "s_setprio 0\n\t"
    "L_tp248_%=:\n\t"
    "s_waitcnt lgkmcnt(4)\n\t"
    "v_smfmac_f32_16x16x64_bf16 v[180:183], v[120:123], v[84:91], v124\n\t"
    "ds_read_b64 v[84:85], v228 offset:64000\n\t"
    "ds_read_b64 v[86:87], v252 offset:64000\n\t"
    "ds_read_b64 v[88:89], v229 offset:64000\n\t"
    "ds_read_b64 v[90:91], v253 offset:64000\n\t"
    "v_lshrrev_b32 v254, 16, v230\n\t"
    "v_and_b32 v230, s92, v230\n\t"
    "v_lshrrev_b32 v255, 16, v231\n\t"
    "v_and_b32 v231, s92, v231\n\t"
    "v_pk_mul_f32 v[74:75], v[74:75], v[202:203]\n\t"
    "v_exp_f32 v76, v72\n\t"
    "v_exp_f32 v77, v73\n\t"
    "v_exp_f32 v78, v74\n\t"
    "v_exp_f32 v79, v75\n\t"
    "s_waitcnt vmcnt(9) lgkmcnt(4)\n\t"
    "v_smfmac_f32_16x16x64_bf16 v[180:183], v[120:123], v[92:99], v124\n\t"
    "ds_read_b64 v[92:93], v230 offset:64000\n\t"
    "ds_read_b64 v[94:95], v254 offset:64000\n\t"
    "ds_read_b64 v[96:97], v231 offset:64000\n\t"
    "ds_read_b64 v[98:99], v255 offset:64000\n\t"
    "s_add_u32 s90, s90, 0x8000\n\t"
    "buffer_load_dwordx4 v[248:251], v127, s[28:31], s90 offen offset:0 sc1\n\t"
    "v_lshrrev_b32 v252, 16, v232\n\t"
    "v_and_b32 v232, s92, v232\n\t"
    "v_lshrrev_b32 v253, 16, v233\n\t"
    "v_and_b32 v233, s92, v233\n\t"
    "v_pk_fma_f32 v[76:77], v[76:77], s[40:41], s[40:41]\n\t"
    "v_pk_fma_f32 v[78:79], v[78:79], s[42:43], s[42:43]\n\t"
    "v_rcp_f32 v76, v76\n\t"
    "v_rcp_f32 v77, v77\n\t"
    "v_rcp_f32 v78, v78\n\t"
    "s_waitcnt lgkmcnt(4)\n\t"
    "v_smfmac_f32_16x16x64_bf16 v[204:207], v[120:123], v[84:91], v124\n\t"
    "ds_read_b64 v[84:85], v232 offset:0\n\t"
    "ds_read_b64 v[86:87], v252 offset:0\n\t"
    "ds_read_b64 v[88:89], v233 offset:0\n\t"
    "ds_read_b64 v[90:91], v253 offset:0\n\t"
    "v_lshrrev_b32 v254, 16, v234\n\t"
    "v_and_b32 v234, s92, v234\n\t"
    "v_lshrrev_b32 v255, 16, v235\n\t"
    "v_and_b32 v235, s92, v235\n\t"
    "s_mov_b64 exec, 1\n\t"
    "buffer_load_dword v164, off, s[28:31], s93 offset:384 sc1\n\t"
    "s_mov_b64 exec, -1\n\t"
    "s_waitcnt vmcnt(7)\n\t"
    "v_smfmac_f32_16x16x64_bf16 v[204:207], v[156:159], v[32:39], v124\n\t"
    "v_rcp_f32 v79, v79\n\t"
    "v_pk_mul_f32 v[76:77], v[76:77], v[200:201]\n\t"
    "v_pk_fma_f32 v[76:77], v[78:79], v[202:203], v[76:77]\n\t"
    "v_add_f32 v243, v76, v77\n\t"
    "s_waitcnt lgkmcnt(4)\n\t"
    "v_smfmac_f32_16x16x64_bf16 v[204:207], v[120:123], v[92:99], v124\n\t"
    "ds_read_b64 v[92:93], v234 offset:0\n\t"
    "ds_read_b64 v[94:95], v254 offset:0\n\t"
    "ds_read_b64 v[96:97], v235 offset:0\n\t"
    "ds_read_b64 v[98:99], v255 offset:0\n\t"
    "buffer_load_dwordx4 v[212:215], v127, s[28:31], s90 offen offset:1024 sc1\n\t"
    "v_lshrrev_b32 v252, 16, v168\n\t"
    "v_and_b32 v168, s92, v168\n\t"
    "v_lshrrev_b32 v253, 16, v169\n\t"
    "v_mov_b64 v[184:185], s[70:71]\n\t"
    "v_mov_b64 v[186:187], s[72:73]\n\t"
    "v_and_b32 v169, s92, v169\n\t"
    "v_smfmac_f32_16x16x64_bf16 v[208:211], v[160:163], v[32:39], v124\n\t"
    "v_pk_mul_f32 v[64:65], v[204:205], v[204:205]\n\t"
    "v_pk_mul_f32 v[66:67], v[206:207], v[206:207]\n\t"
    "v_pk_fma_f32 v[64:65], v[64:65], v[82:83], v[80:81]\n\t"
    "v_pk_fma_f32 v[66:67], v[66:67], v[82:83], v[80:81]\n\t"
    "v_pk_mul_f32 v[64:65], v[64:65], v[204:205]\n\t"
    "ds_read_b128 v[32:35], v148 offset:8192\n\t"
    "s_waitcnt lgkmcnt(5)\n\t"
    "v_smfmac_f32_16x16x64_bf16 v[184:187], v[120:123], v[84:91], v124\n\t"
    "ds_read_b64 v[84:85], v168 offset:64000\n\t"
    "ds_read_b64 v[86:87], v252 offset:64000\n\t"
    "ds_read_b64 v[88:89], v169 offset:64000\n\t"
    "ds_read_b64 v[90:91], v253 offset:64000\n\t"
    "v_lshrrev_b32 v254, 16, v170\n\t"
    "v_and_b32 v170, s92, v170\n\t"
    "v_lshrrev_b32 v255, 16, v171\n\t"
    "v_and_b32 v171, s92, v171\n\t"
    "v_pk_mul_f32 v[66:67], v[66:67], v[206:207]\n\t"
    "v_exp_f32 v68, v64\n\t"
    "v_exp_f32 v69, v65\n\t"
    "v_exp_f32 v70, v66\n\t"
    "v_exp_f32 v71, v67\n\t"
    "ds_read_b128 v[36:39], v148 offset:9216\n\t"
    "s_waitcnt lgkmcnt(6)\n\t"
    "v_smfmac_f32_16x16x64_bf16 v[184:187], v[120:123], v[92:99], v124\n\t"
    "ds_read_b64 v[92:93], v170 offset:64000\n\t"
    "ds_read_b64 v[94:95], v254 offset:64000\n\t"
    "ds_read_b64 v[96:97], v171 offset:64000\n\t"
    "ds_read_b64 v[98:99], v255 offset:64000\n\t"
    "s_add_u32 s91, s91, 0x8000\n\t"
    "buffer_load_dwordx4 v[216:219], v127, s[28:31], s91 offen offset:0 sc1\n\t"
    "v_lshrrev_b32 v252, 16, v112\n\t"
    "v_and_b32 v112, s92, v112\n\t"
    "v_lshrrev_b32 v253, 16, v113\n\t"
    "v_and_b32 v113, s92, v113\n\t"
    "v_pk_fma_f32 v[68:69], v[68:69], s[40:41], s[40:41]\n\t"
    "v_pk_fma_f32 v[70:71], v[70:71], s[42:43], s[42:43]\n\t"
    "v_rcp_f32 v68, v68\n\t"
    "v_rcp_f32 v69, v69\n\t"
    "v_rcp_f32 v70, v70\n\t"
    "s_waitcnt lgkmcnt(5)\n\t"
    "v_smfmac_f32_16x16x64_bf16 v[208:211], v[120:123], v[84:91], v124\n\t"
    "ds_read_b64 v[84:85], v112 offset:0\n\t"
    "ds_read_b64 v[86:87], v252 offset:0\n\t"
    "ds_read_b64 v[88:89], v113 offset:0\n\t"
    "ds_read_b64 v[90:91], v253 offset:0\n\t"
    "v_lshrrev_b32 v254, 16, v114\n\t"
    "v_and_b32 v114, s92, v114\n\t"
    "v_lshrrev_b32 v255, 16, v115\n\t"
    "v_and_b32 v115, s92, v115\n\t"
    "s_waitcnt vmcnt(6)\n\t"
    "v_smfmac_f32_16x16x64_bf16 v[172:175], v[156:159], v[40:47], v124\n\t"
    "v_rcp_f32 v71, v71\n\t"
    "v_pk_mul_f32 v[68:69], v[68:69], v[204:205]\n\t"
    "v_pk_fma_f32 v[68:69], v[70:71], v[206:207], v[68:69]\n\t"
    "v_add_f32 v244, v68, v69\n\t"
    "s_waitcnt vmcnt(5)\n\t"
    "v_readfirstlane_b32 s38, v144\n\t"
    "s_nop 0\n\t"
    "s_cmp_eq_u32 s38, 0\n\t"
    "s_cbranch_scc1 L_rw_%=\n\t"
    "L_rok_%=:\n\t"
    "s_and_b32 s46, s34, 3\n\t"
    "s_mul_i32 s49, s46, 1040\n\t"
    "s_add_u32 s49, s49, 2064\n\t"
    "s_and_b32 s37, s38, 0xffff\n\t"
    "s_lshr_b32 s44, s38, 16\n\t"
    "s_sub_u32 s44, s44, s37\n\t"
    "s_lshl_b32 s37, s37, 2\n\t"
    "s_lshl_b32 s38, s44, 2\n\t"
    "s_mul_i32 s51, s46, s44\n\t"
    "s_add_u32 s48, s51, s44\n\t"
    "s_lshl_b32 s48, s48, 16\n\t"
    "s_add_u32 s48, s48, s51\n\t"
    "s_lshr_b32 s47, s34, 2\n\t"
    "s_add_u32 s48, s48, s47\n\t"
    "s_add_u32 s48, s48, 0x200\n\t"
    "s_mul_i32 s46, s37, 400\n\t"
    "s_add_u32 s96, s10, s46\n\t"
    "s_addc_u32 s97, s11, 0\n\t"
    "s_sub_u32 s98, 6553600, s46\n\t"
    "s_mov_b32 s99, 0x20000\n\t"
    "v_bfe_u32 v154, v147, 2, 2\n\t"
    "v_and_b32 v155, 3, v147\n\t"
    "v_mul_u32_u24 v154, 25, v154\n\t"
    "v_mad_u32_u24 v154, v155, 6, v154\n\t"
    "v_lshlrev_b32 v153, 2, v154\n\t"
    "s_and_b32 s53, s48, 0xff\n\t"
    "s_bfe_u32 s55, s48, 0x80008\n\t"
    "s_add_u32 s54, s53, s55\n\t"
    "s_add_u32 s55, s54, s55\n\t"
    "s_lshr_b32 s64, s48, 16\n\t"
    "s_mul_i32 s66, s53, 400\n\t"
    "buffer_load_dwordx3 v[128:130], v153, s[96:99], s66 offen\n\t"
    "buffer_load_dwordx4 v[132:135], v153, s[96:99], s66 offen offset:12\n\t"
    "s_mul_i32 s66, s54, 400\n\t"
    "buffer_load_dwordx3 v[136:138], v153, s[96:99], s66 offen\n\t"
    "buffer_load_dwordx4 v[140:143], v153, s[96:99], s66 offen offset:12\n\t"
    "v_cmp_eq_u32 s[62:63], 0, v155\n\t"
    "v_and_b32 v151, 48, v147\n\t"
    "v_or_b32 v155, 0x100000, v151\n\t"
    "v_cndmask_b32 v152, v155, v151, s[62:63]\n\t"
    "s_waitcnt lgkmcnt(4)\n\t"
    "v_smfmac_f32_16x16x64_bf16 v[208:211], v[120:123], v[92:99], v124\n\t"
    "ds_read_b64 v[92:93], v114 offset:0\n\t"
    "ds_read_b64 v[94:95], v254 offset:0\n\t"
    "ds_read_b64 v[96:97], v115 offset:0\n\t"
    "ds_read_b64 v[98:99], v255 offset:0\n\t"
    "buffer_load_dwordx4 v[220:223], v127, s[28:31], s91 offen offset:1024 sc1\n\t"
    "v_lshrrev_b32 v252, 16, v100\n\t"
    "v_and_b32 v100, s92, v100\n\t"
    "v_lshrrev_b32 v253, 16, v101\n\t"
    "v_mov_b64 v[188:189], s[70:71]\n\t"
    "v_mov_b64 v[190:191], s[72:73]\n\t"
    "v_and_b32 v101, s92, v101\n\t"
    "v_smfmac_f32_16x16x64_bf16 v[176:179], v[160:163], v[40:47], v124\n\t"
    "v_pk_mul_f32 v[72:73], v[208:209], v[208:209]\n\t"
    "v_pk_mul_f32 v[74:75], v[210:211], v[210:211]\n\t"
    "v_pk_fma_f32 v[72:73], v[72:73], v[82:83], v[80:81]\n\t"
    "v_pk_fma_f32 v[74:75], v[74:75], v[82:83], v[80:81]\n\t"
    "v_pk_mul_f32 v[72:73], v[72:73], v[208:209]\n\t"
    "ds_read_b128 v[40:43], v148 offset:10240\n\t"
    "s_waitcnt lgkmcnt(5)\n\t"
    "v_smfmac_f32_16x16x64_bf16 v[188:191], v[120:123], v[84:91], v124\n\t"
    "ds_read_b64 v[84:85], v100 offset:64000\n\t"
    "ds_read_b64 v[86:87], v252 offset:64000\n\t"
    "ds_read_b64 v[88:89], v101 offset:64000\n\t"
    "ds_read_b64 v[90:91], v253 offset:64000\n\t"
    "v_lshrrev_b32 v254, 16, v102\n\t"
    "v_and_b32 v102, s92, v102\n\t"
    "v_lshrrev_b32 v255, 16, v103\n\t"
    "v_and_b32 v103, s92, v103\n\t"
    "s_waitcnt vmcnt(7)\n\t"
    "v_readfirstlane_b32 s65, v164\n\t"
    "s_nop 0\n\t"
    "s_cmp_eq_u32 s65, 0\n\t"
    "s_cbranch_scc1 L_xw3_%=\n\t"
    "L_xwb3_%=:\n\t"
    "s_add_u32 s94, s94, 0x2000\n\t"
    "buffer_load_dwordx4 v[52:55], v127, s[28:31], s94 offen sc1\n\t"
    "v_pk_mul_f32 v[74:75], v[74:75], v[210:211]\n\t"
    "v_exp_f32 v76, v72\n\t"
    "v_exp_f32 v77, v73\n\t"
    "v_exp_f32 v78, v74\n\t"
    "v_exp_f32 v79, v75\n\t"
    "ds_read_b128 v[44:47], v148 offset:11264\n\t"
    "s_waitcnt lgkmcnt(6)\n\t"
    "v_smfmac_f32_16x16x64_bf16 v[188:191], v[120:123], v[92:99], v124\n\t"
    "ds_read_b64 v[92:93], v102 offset:64000\n\t"
    "ds_read_b64 v[94:95], v254 offset:64000\n\t"
    "ds_read_b64 v[96:97], v103 offset:64000\n\t"
    "ds_read_b64 v[98:99], v255 offset:64000\n\t"
    "v_lshrrev_b32 v252, 16, v104\n\t"
    "v_and_b32 v104, s92, v104\n\t"
    "v_lshrrev_b32 v253, 16, v105\n\t"
    "v_and_b32 v105, s92, v105\n\t"
    "v_pk_fma_f32 v[76:77], v[76:77], s[40:41], s[40:41]\n\t"
    "v_pk_fma_f32 v[78:79], v[78:79], s[42:43], s[42:43]\n\t"
    "v_rcp_f32 v76, v76\n\t"
    "v_rcp_f32 v77, v77\n\t"
    "v_rcp_f32 v78, v78\n\t"
    "s_waitcnt lgkmcnt(5)\n\t"
    "v_smfmac_f32_16x16x64_bf16 v[172:175], v[120:123], v[84:91], v124\n\t"
    "ds_read_b64 v[84:85], v104 offset:0\n\t"
    "ds_read_b64 v[86:87], v252 offset:0\n\t"
    "ds_read_b64 v[88:89], v105 offset:0\n\t"
    "ds_read_b64 v[90:91], v253 offset:0\n\t"
    "v_lshrrev_b32 v254, 16, v106\n\t"
    "v_and_b32 v106, s92, v106\n\t"
    "v_lshrrev_b32 v255, 16, v107\n\t"
    "v_and_b32 v107, s92, v107\n\t"
    "v_rcp_f32 v79, v79\n\t"
    "v_pk_mul_f32 v[76:77], v[76:77], v[208:209]\n\t"
    "v_pk_fma_f32 v[76:77], v[78:79], v[210:211], v[76:77]\n\t"
    "v_add_f32 v245, v76, v77\n\t"
    "s_waitcnt lgkmcnt(4)\n\t"
    "v_smfmac_f32_16x16x64_bf16 v[172:175], v[120:123], v[92:99], v124\n\t"
    "ds_read_b64 v[92:93], v106 offset:0\n\t"
    "ds_read_b64 v[94:95], v254 offset:0\n\t"
    "ds_read_b64 v[96:97], v107 offset:0\n\t"
    "ds_read_b64 v[98:99], v255 offset:0\n\t"
    "v_lshrrev_b32 v252, 16, v108\n\t"
    "v_and_b32 v108, s92, v108\n\t"
    "v_lshrrev_b32 v253, 16, v109\n\t"
    "v_mov_b64 v[192:193], s[70:71]\n\t"
    "v_mov_b64 v[194:195], s[72:73]\n\t"
    "v_and_b32 v109, s92, v109\n\t"
    "v_pk_mul_f32 v[64:65], v[172:173], v[172:173]\n\t"
    "v_pk_mul_f32 v[66:67], v[174:175], v[174:175]\n\t"
    "v_pk_fma_f32 v[64:65], v[64:65], v[82:83], v[80:81]\n\t"
    "v_pk_fma_f32 v[66:67], v[66:67], v[82:83], v[80:81]\n\t"
    "v_pk_mul_f32 v[64:65], v[64:65], v[172:173]\n\t"
    "s_waitcnt lgkmcnt(4)\n\t"
    "v_smfmac_f32_16x16x64_bf16 v[192:195], v[120:123], v[84:91], v124\n\t"
    "ds_read_b64 v[84:85], v108 offset:64000\n\t"
    "ds_read_b64 v[86:87], v252 offset:64000\n\t"
    "ds_read_b64 v[88:89], v109 offset:64000\n\t"
    "ds_read_b64 v[90:91], v253 offset:64000\n\t"
    "v_lshrrev_b32 v254, 16, v110\n\t"
    "v_and_b32 v110, s92, v110\n\t"
    "v_lshrrev_b32 v255, 16, v111\n\t"
    "v_and_b32 v111, s92, v111\n\t"
    "s_add_u32 s94, s94, 0x2000\n\t"
    "buffer_load_dwordx4 v[60:63], v127, s[28:31], s94 offen sc1\n\t"
    "v_pk_mul_f32 v[66:67], v[66:67], v[174:175]\n\t"
    "v_exp_f32 v68, v64\n\t"
    "v_exp_f32 v69, v65\n\t"
    "v_exp_f32 v70, v66\n\t"
    "v_exp_f32 v71, v67\n\t"
    "v_lshrrev_b32 v166, 7, v127\n\t"
    "v_and_b32 v167, 0x70, v127\n\t"
    "v_lshl_add_u32 v126, v166, 8, v167\n\t"
    "v_add_u32 v126, 128, v126\n\t"
    "s_bfe_u32 s46, %2, 0x30003\n\t"
    "s_min_u32 s46, s46, 6\n\t"
    "s_lshl_b32 s69, s46, 3\n\t"
    "s_add_u32 s69, s69, s34\n\t"
    "s_cmp_lt_u32 s34, 4\n\t"
    "s_cbranch_scc1 L_tp261_%=\n\t"
    "s_setprio 1\n\t"
    "L_tp261_%=:\n\t"
    "s_waitcnt lgkmcnt(4)\n\t"
    "v_smfmac_f32_16x16x64_bf16 v[192:195], v[120:123], v[92:99], v124\n\t"
    "ds_read_b64 v[92:93], v110 offset:64000\n\t"
    "ds_read_b64 v[94:95], v254 offset:64000\n\t"
    "ds_read_b64 v[96:97], v111 offset:64000\n\t"
    "ds_read_b64 v[98:99], v255 offset:64000\n\t"
    "v_lshrrev_b32 v252, 16, v248\n\t"
    "v_and_b32 v248, s92, v248\n\t"
    "v_lshrrev_b32 v253, 16, v249\n\t"
    "v_and_b32 v249, s92, v249\n\t"
    "v_pk_fma_f32 v[68:69], v[68:69], s[40:41], s[40:41]\n\t"
    "v_pk_fma_f32 v[70:71], v[70:71], s[42:43], s[42:43]\n\t"
    "v_rcp_f32 v68, v68\n\t"
    "v_rcp_f32 v69, v69\n\t"
    "v_rcp_f32 v70, v70\n\t"
    "s_barrier\n\t"
    "s_waitcnt lgkmcnt(4)\n\t"
    "v_smfmac_f32_16x16x64_bf16 v[176:179], v[120:123], v[84:91], v124\n\t"
    "ds_read_b64 v[84:85], v248 offset:64000\n\t"
    "ds_read_b64 v[86:87], v252 offset:64000\n\t"
    "ds_read_b64 v[88:89], v249 offset:64000\n\t"
    "ds_read_b64 v[90:91], v253 offset:64000\n\t"
    "v_lshrrev_b32 v254, 16, v250\n\t"
    "v_and_b32 v250, s92, v250\n\t"
    "v_lshrrev_b32 v255, 16, v251\n\t"
    "v_and_b32 v251, s92, v251\n\t"
    "v_rcp_f32 v71, v71\n\t"
    "v_pk_mul_f32 v[68:69], v[68:69], v[172:173]\n\t"
    "v_pk_fma_f32 v[68:69], v[70:71], v[174:175], v[68:69]\n\t"
    "v_add_f32 v246, v68, v69\n\t"
    "s_mul_i32 s44, s69, 1040\n\t"
    "s_add_u32 m0, s44, 0\n\t"
    "s_lshl_b32 s44, s69, 11\n\t"
    "buffer_load_dwordx4 v126, s[4:7], s44 offen lds\n\t"
    "s_add_u32 s69, s69, 8\n\t"
    "s_sub_u32 s44, s69, 61\n\t"
    "s_cmp_ge_u32 s69, 61\n\t"
    "s_cselect_b32 s69, s44, s69\n\t"
    "s_waitcnt vmcnt(9) lgkmcnt(4)\n\t"
    "v_smfmac_f32_16x16x64_bf16 v[176:179], v[120:123], v[92:99], v124\n\t"
    "ds_read_b64 v[92:93], v250 offset:64000\n\t"
    "ds_read_b64 v[94:95], v254 offset:64000\n\t"
    "ds_read_b64 v[96:97], v251 offset:64000\n\t"
    "ds_read_b64 v[98:99], v255 offset:64000\n\t"
    "v_lshrrev_b32 v252, 16, v212\n\t"
    "v_and_b32 v212, s92, v212\n\t"
    "v_lshrrev_b32 v253, 16, v213\n\t"
    "v_and_b32 v213, s92, v213\n\t"
    "s_mul_i32 s44, s69, 1040\n\t"
    "s_add_u32 m0, s44, 0\n\t"
    "s_lshl_b32 s44, s69, 11\n\t"
    "buffer_load_dwordx4 v126, s[4:7], s44 offen lds\n\t"
    "s_add_u32 s69, s69, 8\n\t"
    "s_sub_u32 s44, s69, 61\n\t"
    "s_cmp_ge_u32 s69, 61\n\t"
    "s_cselect_b32 s69, s44, s69\n\t"
    "v_lshlrev_b32 v146, 2, v147\n\t"
    "s_lshl_b32 s50, s37, 8\n\t"
    "s_add_u32 s50, s50, 27262976\n\t"
    "s_lshl_b32 s46, s53, 8\n\t"
    "s_add_u32 s46, s46, s50\n\t"
    "buffer_load_dword v144, v146, s[28:31], s46 offen sc1\n\t"
    "buffer_load_dword v145, v146, s[28:31], s46 offen offset:512 sc1\n\t"
    "v_mov_b32 v149, s49\n\t"
    "s_add_u32 s46, s51, 6\n\t"
    "v_mov_b32 v155, s46\n\t"
    "ds_write_b32 v149, v155\n\t"
    "s_mov_b32 s61, 0\n\t"
    "v_mov_b32 v149, 1024\n\t"
    "v_mov_b32 v150, 0\n\t"
    "ds_write_b32 v149, v150\n\t"
    "s_mov_b32 s45, 1\n\t"
    "s_waitcnt lgkmcnt(6)\n\t"
    "v_smfmac_f32_16x16x64_bf16 v[180:183], v[120:123], v[84:91], v124\n\t"
    "ds_read_b64 v[84:85], v212 offset:64000\n\t"
    "ds_read_b64 v[86:87], v252 offset:64000\n\t"
    "ds_read_b64 v[88:89], v213 offset:64000\n\t"
    "ds_read_b64 v[90:91], v253 offset:64000\n\t"
    "v_lshrrev_b32 v254, 16, v214\n\t"
    "v_and_b32 v214, s92, v214\n\t"
    "v_lshrrev_b32 v255, 16, v215\n\t"
    "v_and_b32 v215, s92, v215\n\t"
    "s_waitcnt vmcnt(5)\n\t"
    "v_smfmac_f32_16x16x64_bf16 v[180:183], v[156:159], v[48:55], v124\n\t"
    "v_pk_mul_f32 v[72:73], v[176:177], v[176:177]\n\t"
    "v_pk_mul_f32 v[74:75], v[178:179], v[178:179]\n\t"
    "v_pk_fma_f32 v[72:73], v[72:73], v[82:83], v[80:81]\n\t"
    "v_pk_fma_f32 v[74:75], v[74:75], v[82:83], v[80:81]\n\t"
    "v_pk_mul_f32 v[72:73], v[72:73], v[176:177]\n\t"
    "v_pk_mul_f32 v[74:75], v[74:75], v[178:179]\n\t"
    "v_exp_f32 v76, v72\n\t"
    "s_mul_i32 s44, s69, 1040\n\t"
    "s_add_u32 m0, s44, 0\n\t"
    "s_lshl_b32 s44, s69, 11\n\t"
    "buffer_load_dwordx4 v126, s[4:7], s44 offen lds\n\t"
    "s_add_u32 s69, s69, 8\n\t"
    "s_sub_u32 s44, s69, 61\n\t"
    "s_cmp_ge_u32 s69, 61\n\t"
    "s_cselect_b32 s69, s44, s69\n\t"
    "s_waitcnt lgkmcnt(6)\n\t"
    "v_smfmac_f32_16x16x64_bf16 v[180:183], v[120:123], v[92:99], v124\n\t"
    "ds_read_b64 v[92:93], v214 offset:64000\n\t"
    "ds_read_b64 v[94:95], v254 offset:64000\n\t"
    "ds_read_b64 v[96:97], v215 offset:64000\n\t"
    "ds_read_b64 v[98:99], v255 offset:64000\n\t"
    "v_lshrrev_b32 v252, 16, v216\n\t"
    "v_and_b32 v216, s92, v216\n\t"
    "v_lshrrev_b32 v253, 16, v217\n\t"
    "v_and_b32 v217, s92, v217\n\t"
    "v_smfmac_f32_16x16x64_bf16 v[184:187], v[160:163], v[48:55], v124\n\t"
    "v_exp_f32 v77, v73\n\t"
    "v_exp_f32 v78, v74\n\t"
    "v_exp_f32 v79, v75\n\t"
    "v_pk_fma_f32 v[76:77], v[76:77], s[40:41], s[40:41]\n\t"
    "v_pk_fma_f32 v[78:79], v[78:79], s[42:43], s[42:43]\n\t"
    "v_rcp_f32 v76, v76\n\t"
    "v_rcp_f32 v77, v77\n\t"
    "s_mul_i32 s44, s69, 1040\n\t"
    "s_add_u32 m0, s44, 0\n\t"
    "s_lshl_b32 s44, s69, 11\n\t"
    "buffer_load_dwordx4 v126, s[4:7], s44 offen lds\n\t"
    "s_add_u32 s69, s69, 8\n\t"
    "s_sub_u32 s44, s69, 61\n\t"
    "s_cmp_ge_u32 s69, 61\n\t"
    "s_cselect_b32 s69, s44, s69\n\t"
    "ds_read_b128 v[48:51], v148 offset:12288\n\t"
    "s_waitcnt lgkmcnt(5)\n\t"
    "v_smfmac_f32_16x16x64_bf16 v[184:187], v[120:123], v[84:91], v124\n\t"
    "ds_read_b64 v[84:85], v216 offset:64000\n\t"
    "ds_read_b64 v[86:87], v252 offset:64000\n\t"
    "ds_read_b64 v[88:89], v217 offset:64000\n\t"
    "ds_read_b64 v[90:91], v253 offset:64000\n\t"
    "v_lshrrev_b32 v254, 16, v218\n\t"
    "v_and_b32 v218, s92, v218\n\t"
    "v_lshrrev_b32 v255, 16, v219\n\t"
    "v_and_b32 v219, s92, v219\n\t"
    "v_rcp_f32 v78, v78\n\t"
    "v_rcp_f32 v79, v79\n\t"
    "v_pk_mul_f32 v[76:77], v[76:77], v[176:177]\n\t"
    "v_pk_fma_f32 v[76:77], v[78:79], v[178:179], v[76:77]\n\t"
    "v_add_f32 v247, v76, v77\n\t"
    "v_pk_mul_f32 v[64:65], v[180:181], v[180:181]\n\t"
    "v_pk_mul_f32 v[66:67], v[182:183], v[182:183]\n\t"
    "s_mul_i32 s44, s69, 1040\n\t"
    "s_add_u32 m0, s44, 0\n\t"
    "s_lshl_b32 s44, s69, 11\n\t"
    "buffer_load_dwordx4 v126, s[4:7], s44 offen lds\n\t"
    "s_add_u32 s69, s69, 8\n\t"
    "s_sub_u32 s44, s69, 61\n\t"
    "s_cmp_ge_u32 s69, 61\n\t"
    "s_cselect_b32 s69, s44, s69\n\t"
    "ds_read_b128 v[52:55], v148 offset:13312\n\t"
    "s_waitcnt lgkmcnt(6)\n\t"
    "v_smfmac_f32_16x16x64_bf16 v[184:187], v[120:123], v[92:99], v124\n\t"
    "ds_read_b64 v[92:93], v218 offset:64000\n\t"
    "ds_read_b64 v[94:95], v254 offset:64000\n\t"
    "ds_read_b64 v[96:97], v219 offset:64000\n\t"
    "ds_read_b64 v[98:99], v255 offset:64000\n\t"
    "v_lshrrev_b32 v252, 16, v220\n\t"
    "v_and_b32 v220, s92, v220\n\t"
    "v_lshrrev_b32 v253, 16, v221\n\t"
    "v_and_b32 v221, s92, v221\n\t"
    "v_pk_fma_f32 v[64:65], v[64:65], v[82:83], v[80:81]\n\t"
    "v_pk_fma_f32 v[66:67], v[66:67], v[82:83], v[80:81]\n\t"
    "v_pk_mul_f32 v[64:65], v[64:65], v[180:181]\n\t"
    "v_pk_mul_f32 v[66:67], v[66:67], v[182:183]\n\t"
    "v_exp_f32 v68, v64\n\t"
    "v_exp_f32 v69, v65\n\t"
    "v_exp_f32 v70, v66\n\t"
    "s_mul_i32 s44, s69, 1040\n\t"
    "s_add_u32 m0, s44, 0\n\t"
    "s_lshl_b32 s44, s69, 11\n\t"
    "buffer_load_dwordx4 v126, s[4:7], s44 offen lds\n\t"
    "s_add_u32 s69, s69, 8\n\t"
    "s_sub_u32 s44, s69, 61\n\t"
    "s_cmp_ge_u32 s69, 61\n\t"
    "s_cselect_b32 s69, s44, s69\n\t"
    "s_setprio 0\n\t"
    "s_waitcnt lgkmcnt(5)\n\t"
    "v_smfmac_f32_16x16x64_bf16 v[188:191], v[120:123], v[84:91], v124\n\t"
    "ds_read_b64 v[84:85], v220 offset:64000\n\t"
    "ds_read_b64 v[86:87], v252 offset:64000\n\t"
    "ds_read_b64 v[88:89], v221 offset:64000\n\t"
    "ds_read_b64 v[90:91], v253 offset:64000\n\t"
    "v_lshrrev_b32 v254, 16, v222\n\t"
    "v_and_b32 v222, s92, v222\n\t"
    "v_lshrrev_b32 v255, 16, v223\n\t"
    "v_and_b32 v223, s92, v223\n\t"
    "s_waitcnt vmcnt(8)\n\t"
    "v_smfmac_f32_16x16x64_bf16 v[188:191], v[156:159], v[56:63], v124\n\t"
    "v_exp_f32 v71, v67\n\t"
    "v_pk_fma_f32 v[68:69], v[68:69], s[40:41], s[40:41]\n\t"
    "v_pk_fma_f32 v[70:71], v[70:71], s[42:43], s[42:43]\n\t"
    "v_rcp_f32 v68, v68\n\t"
    "v_rcp_f32 v69, v69\n\t"
    "v_rcp_f32 v70, v70\n\t"
    "v_rcp_f32 v71, v71\n\t"
    "s_mul_i32 s44, s69, 1040\n\t"
    "s_add_u32 m0, s44, 0\n\t"
    "s_lshl_b32 s44, s69, 11\n\t"
    "buffer_load_dwordx4 v126, s[4:7], s44 offen lds\n\t"
    "s_add_u32 s69, s69, 8\n\t"
    "s_sub_u32 s44, s69, 61\n\t"
    "s_cmp_ge_u32 s69, 61\n\t"
    "s_cselect_b32 s69, s44, s69\n\t"
    "s_waitcnt lgkmcnt(4)\n\t"
    "v_smfmac_f32_16x16x64_bf16 v[188:191], v[120:123], v[92:99], v124\n\t"
    "ds_read_b64 v[92:93], v222 offset:64000\n\t"
    "ds_read_b64 v[94:95], v254 offset:64000\n\t"
    "ds_read_b64 v[96:97], v223 offset:64000\n\t"
    "ds_read_b64 v[98:99], v255 offset:64000\n\t"
    "v_smfmac_f32_16x16x64_bf16 v[192:195], v[160:163], v[56:63], v124\n\t"
    "v_pk_mul_f32 v[68:69], v[68:69], v[180:181]\n\t"
    "v_pk_fma_f32 v[68:69], v[70:71], v[182:183], v[68:69]\n\t"
    "v_add_f32 v248, v68, v69\n\t"
    "v_pk_mul_f32 v[72:73], v[184:185], v[184:185]\n\t"
    "v_pk_mul_f32 v[74:75], v[186:187], v[186:187]\n\t"
    "v_pk_fma_f32 v[72:73], v[72:73], v[82:83], v[80:81]\n\t"
    "v_pk_fma_f32 v[74:75], v[74:75], v[82:83], v[80:81]\n\t"
    "s_mul_i32 s44, s69, 1040\n\t"
    "s_add_u32 m0, s44, 0\n\t"
    "s_lshl_b32 s44, s69, 11\n\t"
    "buffer_load_dwordx4 v126, s[4:7], s44 offen lds\n\t"
    "s_add_u32 s69, s69, 8\n\t"
    "s_sub_u32 s44, s69, 61\n\t"
    "s_cmp_ge_u32 s69, 61\n\t"
    "s_cselect_b32 s69, s44, s69\n\t"
    "ds_read_b128 v[56:59], v148 offset:14336\n\t"
    "s_waitcnt lgkmcnt(5)\n\t"
    "v_smfmac_f32_16x16x64_bf16 v[192:195], v[120:123], v[84:91], v124\n\t"
    "v_pk_mul_f32 v[72:73], v[72:73], v[184:185]\n\t"
    "v_pk_mul_f32 v[74:75], v[74:75], v[186:187]\n\t"
    "v_exp_f32 v76, v72\n\t"
    "v_exp_f32 v77, v73\n\t"
    "v_exp_f32 v78, v74\n\t"
    "v_exp_f32 v79, v75\n\t"
    "v_pk_fma_f32 v[76:77], v[76:77], s[40:41], s[40:41]\n\t"
    "ds_read_b128 v[60:63], v148 offset:15360\n\t"
    "s_waitcnt lgkmcnt(2)\n\t"
    "v_smfmac_f32_16x16x64_bf16 v[192:195], v[120:123], v[92:99], v124\n\t"
    "v_pk_fma_f32 v[78:79], v[78:79], s[42:43], s[42:43]\n\t"
    "v_rcp_f32 v76, v76\n\t"
    "v_rcp_f32 v77, v77\n\t"
    "v_rcp_f32 v78, v78\n\t"
    "v_rcp_f32 v79, v79\n\t"
    "v_pk_mul_f32 v[76:77], v[76:77], v[184:185]\n\t"
    "v_pk_fma_f32 v[76:77], v[78:79], v[186:187], v[76:77]\n\t"
    "s_barrier\n\t"
    "v_add_u32 v126, 124928, v126\n\t"
    "s_bfe_u32 s46, %2, 0x30003\n\t"
    "s_min_u32 s46, s46, 6\n\t"
    "s_lshl_b32 s69, s46, 3\n\t"
    "s_add_u32 s69, s69, s34\n\t"
    "s_mul_i32 s44, s69, 1040\n\t"
    "s_add_u32 m0, s44, 63440\n\t"
    "s_lshl_b32 s44, s69, 11\n\t"
    "buffer_load_dwordx4 v126, s[4:7], s44 offen lds\n\t"
    "s_add_u32 s69, s69, 8\n\t"
    "s_sub_u32 s44, s69, 64\n\t"
    "s_cmp_ge_u32 s69, 64\n\t"
    "s_cselect_b32 s69, s44, s69\n\t"
    "s_mul_i32 s44, s69, 1040\n\t"
    "s_add_u32 m0, s44, 63440\n\t"
    "s_lshl_b32 s44, s69, 11\n\t"
    "buffer_load_dwordx4 v126, s[4:7], s44 offen lds\n\t"
    "s_add_u32 s69, s69, 8\n\t"
    "s_sub_u32 s44, s69, 64\n\t"
    "s_cmp_ge_u32 s69, 64\n\t"
    "s_cselect_b32 s69, s44, s69\n\t"
    "s_mul_i32 s44, s69, 1040\n\t"
    "s_add_u32 m0, s44, 63440\n\t"
    "s_lshl_b32 s44, s69, 11\n\t"
    "buffer_load_dwordx4 v126, s[4:7], s44 offen lds\n\t"
    "s_add_u32 s69, s69, 8\n\t"
    "s_sub_u32 s44, s69, 64\n\t"
    "s_cmp_ge_u32 s69, 64\n\t"
    "s_cselect_b32 s69, s44, s69\n\t"
    "s_mul_i32 s44, s69, 1040\n\t"
    "s_add_u32 m0, s44, 63440\n\t"
    "s_lshl_b32 s44, s69, 11\n\t"
    "buffer_load_dwordx4 v126, s[4:7], s44 offen lds\n\t"
    "s_add_u32 s69, s69, 8\n\t"
    "s_sub_u32 s44, s69, 64\n\t"
    "s_cmp_ge_u32 s69, 64\n\t"
    "s_cselect_b32 s69, s44, s69\n\t"
    "s_mul_i32 s44, s69, 1040\n\t"
    "s_add_u32 m0, s44, 63440\n\t"
    "s_lshl_b32 s44, s69, 11\n\t"
    "buffer_load_dwordx4 v126, s[4:7], s44 offen lds\n\t"
    "s_add_u32 s69, s69, 8\n\t"
    "s_sub_u32 s44, s69, 64\n\t"
    "s_cmp_ge_u32 s69, 64\n\t"
    "s_cselect_b32 s69, s44, s69\n\t"
    "s_mul_i32 s44, s69, 1040\n\t"
    "s_add_u32 m0, s44, 63440\n\t"
    "s_lshl_b32 s44, s69, 11\n\t"
    "buffer_load_dwordx4 v126, s[4:7], s44 offen lds\n\t"
    "s_add_u32 s69, s69, 8\n\t"
    "s_sub_u32 s44, s69, 64\n\t"
    "s_cmp_ge_u32 s69, 64\n\t"
    "s_cselect_b32 s69, s44, s69\n\t"
    "s_mul_i32 s44, s69, 1040\n\t"
    "s_add_u32 m0, s44, 63440\n\t"
    "s_lshl_b32 s44, s69, 11\n\t"
    "buffer_load_dwordx4 v126, s[4:7], s44 offen lds\n\t"
    "s_add_u32 s69, s69, 8\n\t"
    "s_sub_u32 s44, s69, 64\n\t"
    "s_cmp_ge_u32 s69, 64\n\t"
    "s_cselect_b32 s69, s44, s69\n\t"
    "s_mul_i32 s44, s69, 1040\n\t"
    "s_add_u32 m0, s44, 63440\n\t"
    "s_lshl_b32 s44, s69, 11\n\t"
    "buffer_load_dwordx4 v126, s[4:7], s44 offen lds\n\t"
    "s_add_u32 s69, s69, 8\n\t"
    "s_sub_u32 s44, s69, 64\n\t"
    "s_cmp_ge_u32 s69, 64\n\t"
    "s_cselect_b32 s69, s44, s69\n\t"
    "ds_read_b128 v[84:87], v148 offset:21504\n\t"
    "ds_read_b128 v[88:91], v148 offset:22528\n\t"
    "v_add_f32 v249, v76, v77\n\t"
    "v_pk_mul_f32 v[64:65], v[188:189], v[188:189]\n\t"
    "v_pk_mul_f32 v[66:67], v[190:191], v[190:191]\n\t"
    "v_pk_fma_f32 v[64:65], v[64:65], v[82:83], v[80:81]\n\t"
    "v_pk_fma_f32 v[66:67], v[66:67], v[82:83], v[80:81]\n\t"
    "v_pk_mul_f32 v[64:65], v[64:65], v[188:189]\n\t"
    "v_pk_mul_f32 v[66:67], v[66:67], v[190:191]\n\t"
    "v_exp_f32 v68, v64\n\t"
    "v_exp_f32 v69, v65\n\t"
    "v_exp_f32 v70, v66\n\t"
    "v_exp_f32 v71, v67\n\t"
    "v_pk_fma_f32 v[68:69], v[68:69], s[40:41], s[40:41]\n\t"
    "v_pk_fma_f32 v[70:71], v[70:71], s[42:43], s[42:43]\n\t"
    "v_rcp_f32 v68, v68\n\t"
    "v_rcp_f32 v69, v69\n\t"
    "v_rcp_f32 v70, v70\n\t"
    "v_rcp_f32 v71, v71\n\t"
    "v_pk_mul_f32 v[68:69], v[68:69], v[188:189]\n\t"
    "v_pk_fma_f32 v[68:69], v[70:71], v[190:191], v[68:69]\n\t"
    "v_add_f32 v250, v68, v69\n\t"
    "v_pk_mul_f32 v[72:73], v[192:193], v[192:193]\n\t"
    "v_pk_mul_f32 v[74:75], v[194:195], v[194:195]\n\t"
    "v_pk_fma_f32 v[72:73], v[72:73], v[82:83], v[80:81]\n\t"
    "v_pk_fma_f32 v[74:75], v[74:75], v[82:83], v[80:81]\n\t"
    "v_pk_mul_f32 v[72:73], v[72:73], v[192:193]\n\t"
    "v_pk_mul_f32 v[74:75], v[74:75], v[194:195]\n\t"
    "v_exp_f32 v76, v72\n\t"
    "v_exp_f32 v77, v73\n\t"
    "v_exp_f32 v78, v74\n\t"
    "v_exp_f32 v79, v75\n\t"
    "v_pk_fma_f32 v[76:77], v[76:77], s[40:41], s[40:41]\n\t"
    "v_pk_fma_f32 v[78:79], v[78:79], s[42:43], s[42:43]\n\t"
    "v_rcp_f32 v76, v76\n\t"
    "v_rcp_f32 v77, v77\n\t"
    "v_rcp_f32 v78, v78\n\t"
    "v_rcp_f32 v79, v79\n\t"
    "v_pk_mul_f32 v[76:77], v[76:77], v[192:193]\n\t"
    "v_pk_fma_f32 v[76:77], v[78:79], v[194:195], v[76:77]\n\t"
    "v_add_f32 v251, v76, v77\n\t"
    "ds_read_b128 v[64:67], v148 offset:16384\n\t"
    "ds_read_b128 v[68:71], v148 offset:17408\n\t"
    "ds_read_b128 v[72:75], v148 offset:18432\n\t"
    "ds_read_b128 v[76:79], v148 offset:19456\n\t"
    "ds_read_b128 v[80:83], v148 offset:20480\n\t"
    "s_waitcnt vmcnt(0) lgkmcnt(0)\n\t"
    "s_mov_b32 s68, 130\n\t"
    "v_mad_u32_u24 v128, v128, s68, v152\n\t"
    "v_and_b32 v128, -16, v128\n\t"
    "v_mad_u32_u24 v129, v129, s68, v151\n\t"
    "v_and_b32 v129, -16, v129\n\t"
    "v_mad_u32_u24 v130, v130, s68, v151\n\t"
    "v_and_b32 v130, -16, v130\n\t"
    "v_mad_u32_u24 v132, v132, s68, v151\n\t"
    "v_and_b32 v132, -16, v132\n\t"
    "v_mad_u32_u24 v133, v133, s68, v151\n\t"
    "v_and_b32 v133, -16, v133\n\t"
    "v_mad_u32_u24 v134, v134, s68, v151\n\t"
    "v_and_b32 v134, -16, v134\n\t"
    "v_mad_u32_u24 v135, v135, s68, v151\n\t"
    "v_and_b32 v135, -16, v135\n\t"
    "v_lshrrev_b32 v122, 4, v147\n\t"
    "v_and_b32 v155, 15, v147\n\t"
    "v_lshlrev_b32 v155, 4, v155\n\t"
    "v_mul_u32_u24 v122, 65536, v122\n\t"
    "v_add_u32 v122, v122, v155\n\t"
    "v_mov_b32 v150, 1\n\t"
    "v_mov_b32 v123, 0\n\t"
    "v_lshlrev_b32 v149, 20, v147\n\t"
    "v_add_u32 v149, s49, v149\n\t"
    "s_mov_b32 s52, s53\n\t"
    "s_cmp_ge_u32 s34, 4\n\t"
    "s_cbranch_scc1 L_st4_%=\n\t"
    "s_bfe_u32 s65, s84, 0xc0002\n\t"
    "s_add_u32 s65, s65, s36\n\t"
    "buffer_store_dword v236, v125, s[28:31], s65 offen offset:0 sc1\n\t"
    "buffer_store_dword v237, v125, s[28:31], s65 offen offset:256 sc1\n\t"
    "s_bfe_u32 s65, s85, 0xc0002\n\t"
    "s_add_u32 s65, s65, s36\n\t"
    "buffer_store_dword v238, v125, s[28:31], s65 offen offset:0 sc1\n\t"
    "buffer_store_dword v239, v125, s[28:31], s65 offen offset:256 sc1\n\t"
    "s_bfe_u32 s65, s86, 0xc0002\n\t"
    "s_add_u32 s65, s65, s36\n\t"
    "buffer_store_dword v240, v125, s[28:31], s65 offen offset:0 sc1\n\t"
    "buffer_store_dword v241, v125, s[28:31], s65 offen offset:256 sc1\n\t"
    "s_bfe_u32 s65, s87, 0xc0002\n\t"
    "s_add_u32 s65, s65, s36\n\t"
    "buffer_store_dword v242, v125, s[28:31], s65 offen offset:0 sc1\n\t"
    "buffer_store_dword v243, v125, s[28:31], s65 offen offset:256 sc1\n\t"
    "s_bfe_u32 s65, s88, 0xc0002\n\t"
    "s_add_u32 s65, s65, s36\n\t"
    "buffer_store_dword v244, v125, s[28:31], s65 offen offset:0 sc1\n\t"
    "buffer_store_dword v245, v125, s[28:31], s65 offen offset:256 sc1\n\t"
    "s_bfe_u32 s65, s89, 0xc0002\n\t"
    "s_add_u32 s65, s65, s36\n\t"
    "buffer_store_dword v246, v125, s[28:31], s65 offen offset:0 sc1\n\t"
    "buffer_store_dword v247, v125, s[28:31], s65 offen offset:256 sc1\n\t"
    "s_bfe_u32 s65, s90, 0xc0002\n\t"
    "s_add_u32 s65, s65, s36\n\t"
    "buffer_store_dword v248, v125, s[28:31], s65 offen offset:0 sc1\n\t"
    "buffer_store_dword v249, v125, s[28:31], s65 offen offset:256 sc1\n\t"
    "s_bfe_u32 s65, s91, 0xc0002\n\t"
    "s_add_u32 s65, s65, s36\n\t"
    "buffer_store_dword v250, v125, s[28:31], s65 offen offset:0 sc1\n\t"
    "buffer_store_dword v251, v125, s[28:31], s65 offen offset:256 sc1\n\t"
    "s_sub_u32 s61, s64, 3\n\t"
    "L_st4_%=:\n\t"
    "s_barrier\n\t"
    "s_mov_b32 s68, 130\n\t"
    "ds_read_b128 v[156:159], v128 offset:0\n\t"
    "ds_read_b128 v[160:163], v128 offset:64\n\t"
    "ds_read_b128 v[164:167], v129 offset:0\n\t"
    "ds_read_b128 v[168:171], v129 offset:64\n\t"
    "ds_read_b128 v[172:175], v130 offset:0\n\t"
    "ds_read_b128 v[176:179], v130 offset:64\n\t"
    "ds_read_b128 v[180:183], v132 offset:0\n\t"
    "ds_read_b128 v[184:187], v132 offset:64\n\t"
    "ds_read_b128 v[188:191], v133 offset:0\n\t"
    "ds_read_b128 v[192:195], v133 offset:64\n\t"
    "ds_read_b128 v[196:199], v134 offset:0\n\t"
    "ds_read_b128 v[200:203], v134 offset:64\n\t"
    "ds_read_b128 v[204:207], v135 offset:0\n\t"
    "ds_read_b128 v[208:211], v135 offset:64\n\t"
    "s_setprio 1\n\t"
    "L_pnn_%=:\n\t"
    "s_mul_i32 s69, s52, 272\n\t"
    "s_add_u32 s69, s69, 130048\n\t"
    "s_mul_i32 s66, s55, 400\n\t"
    "s_waitcnt lgkmcnt(8)\n\t"
    "v_mfma_f32_16x16x32_bf16 v[92:95], v[156:159], v[156:159], 0\n\t"
    "v_add_u32 v147, s69, v146\n\t"
    "v_fmac_f32 v121, v108, v72\n\t"
    "ds_add_rtn_u32 v148, v149, v150\n\t"
    "v_mfma_f32_16x16x32_bf16 v[92:95], v[160:163], v[160:163], v[92:95]\n\t"
    "v_fmac_f32 v120, v109, v73\n\t"
    "v_fmac_f32 v121, v110, v74\n\t"
    "buffer_load_dwordx3 v[128:130], v153, s[96:99], s66 offen\n\t"
    "v_mfma_f32_16x16x32_bf16 v[96:99], v[164:167], v[164:167], 0\n\t"
    "v_fmac_f32 v120, v111, v75\n\t"
    "v_fmac_f32 v121, v112, v76\n\t"
    "v_mfma_f32_16x16x32_bf16 v[96:99], v[168:171], v[168:171], v[96:99]\n\t"
    "v_fmac_f32 v120, v113, v77\n\t"
    "v_fmac_f32 v121, v114, v78\n\t"
    "buffer_load_dwordx4 v[132:135], v153, s[96:99], s66 offen offset:12\n\t"
    "v_mfma_f32_16x16x32_bf16 v[100:103], v[164:167], v[172:175], 0\n\t"
    "v_fmac_f32 v120, v115, v79\n\t"
    "v_fmac_f32 v121, v116, v80\n\t"
    "v_mfma_f32_16x16x32_bf16 v[100:103], v[168:171], v[176:179], v[100:103]\n\t"
    "v_fmac_f32 v120, v117, v81\n\t"
    "v_fmac_f32 v121, v118, v82\n\t"
    "s_waitcnt vmcnt(3)\n\t"
    "s_cmp_ge_u32 s55, s61\n\t"
    "s_cbranch_scc1 L_req0_%=\n\t"
    "L_back0_%=:\n\t"
    "v_mfma_f32_16x16x32_bf16 v[104:107], v[172:175], v[172:175], 0\n\t"
    "v_mad_u32_u24 v136, v136, s68, v152\n\t"
    "v_and_b32 v136, -16, v136\n\t"
    "v_mfma_f32_16x16x32_bf16 v[104:107], v[176:179], v[176:179], v[104:107]\n\t"
    "v_fmac_f32 v120, v119, v83\n\t"
    "v_add_f32 v154, v120, v121\n\t"
    "ds_write_b32 v147, v154\n\t"
    "v_mfma_f32_16x16x32_bf16 v[108:111], v[156:159], v[164:167], 0\n\t"
    "v_fma_f32 v120, v92, v84, v144\n\t"
    "v_mul_f32 v121, v96, v0\n\t"
    "v_mfma_f32_16x16x32_bf16 v[108:111], v[160:163], v[168:171], v[108:111]\n\t"
    "v_fmac_f32 v120, v97, v1\n\t"
    "v_fmac_f32 v121, v98, v2\n\t"
    "s_waitcnt lgkmcnt(8)\n\t"
    "v_mfma_f32_16x16x32_bf16 v[112:115], v[164:167], v[180:183], 0\n\t"
    "v_fmac_f32 v120, v99, v3\n\t"
    "v_fmac_f32 v121, v100, v4\n\t"
    "v_mfma_f32_16x16x32_bf16 v[112:115], v[168:171], v[184:187], v[112:115]\n\t"
    "v_fmac_f32 v120, v101, v5\n\t"
    "v_fmac_f32 v121, v102, v6\n\t"
    "v_mfma_f32_16x16x32_bf16 v[116:119], v[172:175], v[180:183], 0\n\t"
    "v_fmac_f32 v120, v103, v7\n\t"
    "v_fmac_f32 v121, v104, v8\n\t"
    "v_mfma_f32_16x16x32_bf16 v[116:119], v[176:179], v[184:187], v[116:119]\n\t"
    "v_fmac_f32 v120, v105, v9\n\t"
    "v_fmac_f32 v121, v106, v10\n\t"
    "ds_read_b128 v[212:215], v136 offset:0\n\t"
    "ds_read_b128 v[216:219], v136 offset:64\n\t"
    "v_mfma_f32_16x16x32_bf16 v[92:95], v[180:183], v[180:183], 0\n\t"
    "v_fmac_f32 v120, v107, v11\n\t"
    "v_mad_u32_u24 v137, v137, s68, v151\n\t"
    "v_mfma_f32_16x16x32_bf16 v[92:95], v[184:187], v[184:187], v[92:95]\n\t"
    "v_and_b32 v137, -16, v137\n\t"
    "v_fmac_f32 v121, v108, v85\n\t"
    "v_mfma_f32_16x16x32_bf16 v[96:99], v[156:159], v[172:175], 0\n\t"
    "v_fmac_f32 v120, v112, v12\n\t"
    "v_fmac_f32 v121, v113, v13\n\t"
    "v_mfma_f32_16x16x32_bf16 v[96:99], v[160:163], v[176:179], v[96:99]\n\t"
    "v_fmac_f32 v120, v114, v14\n\t"
    "v_fmac_f32 v121, v115, v15\n\t"
    "s_lshl_b32 s51, s55, 8\n\t"
    "s_add_u32 s51, s51, s50\n\t"
    "s_waitcnt lgkmcnt(8)\n\t"
    "v_mfma_f32_16x16x32_bf16 v[100:103], v[164:167], v[188:191], 0\n\t"
    "v_mad_u32_u24 v138, v138, s68, v151\n\t"
    "v_and_b32 v138, -16, v138\n\t"
    "v_mfma_f32_16x16x32_bf16 v[100:103], v[168:171], v[192:195], v[100:103]\n\t"
    "v_fmac_f32 v120, v116, v16\n\t"
    "v_fmac_f32 v121, v117, v17\n\t"
    "buffer_load_dword v144, v146, s[28:31], s51 offen sc1\n\t"
    "v_mfma_f32_16x16x32_bf16 v[104:107], v[172:175], v[188:191], 0\n\t"
    "v_fmac_f32 v120, v118, v18\n\t"
    "v_fmac_f32 v121, v119, v19\n\t"
    "v_mfma_f32_16x16x32_bf16 v[104:107], v[176:179], v[192:195], v[104:107]\n\t"
    "v_fmac_f32 v120, v92, v20\n\t"
    "v_fmac_f32 v121, v93, v21\n\t"
    "s_waitcnt lgkmcnt(6)\n\t"
    "v_mfma_f32_16x16x32_bf16 v[108:111], v[164:167], v[196:199], 0\n\t"
    "v_fmac_f32 v120, v94, v22\n\t"
    "v_fmac_f32 v121, v95, v23\n\t"
    "v_mfma_f32_16x16x32_bf16 v[108:111], v[168:171], v[200:203], v[108:111]\n\t"
    "v_fmac_f32 v120, v96, v86\n\t"
    "v_mad_u32_u24 v140, v140, s68, v151\n\t"
    "v_mfma_f32_16x16x32_bf16 v[112:115], v[156:159], v[180:183], 0\n\t"
    "v_fmac_f32 v121, v100, v24\n\t"
    "v_fmac_f32 v120, v101, v25\n\t"
    "v_mfma_f32_16x16x32_bf16 v[112:115], v[160:163], v[184:187], v[112:115]\n\t"
    "v_fmac_f32 v121, v102, v26\n\t"
    "v_fmac_f32 v120, v103, v27\n\t"
    "s_waitcnt lgkmcnt(3)\n\t"
    "v_mfma_f32_16x16x32_bf16 v[116:119], v[164:167], v[204:207], 0\n\t"
    "v_readfirstlane_b32 s60, v148\n\t"
    "v_and_b32 v140, -16, v140\n\t"
    "v_mfma_f32_16x16x32_bf16 v[116:119], v[168:171], v[208:211], v[116:119]\n\t"
    "v_fmac_f32 v121, v104, v28\n\t"
    "v_fmac_f32 v120, v105, v29\n\t"
    "ds_read_b128 v[164:167], v137 offset:0\n\t"
    "ds_read_b128 v[168:171], v137 offset:64\n\t"
    "v_mfma_f32_16x16x32_bf16 v[92:95], v[172:175], v[196:199], 0\n\t"
    "v_fmac_f32 v121, v106, v30\n\t"
    "v_fmac_f32 v120, v107, v31\n\t"
    "v_mfma_f32_16x16x32_bf16 v[92:95], v[176:179], v[200:203], v[92:95]\n\t"
    "v_fmac_f32 v121, v108, v32\n\t"
    "v_fmac_f32 v120, v109, v33\n\t"
    "v_mfma_f32_16x16x32_bf16 v[96:99], v[172:175], v[204:207], 0\n\t"
    "v_fmac_f32 v121, v110, v34\n\t"
    "v_fmac_f32 v120, v111, v35\n\t"
    "v_mfma_f32_16x16x32_bf16 v[96:99], v[176:179], v[208:211], v[96:99]\n\t"
    "v_fmac_f32 v121, v112, v87\n\t"
    "v_mad_u32_u24 v141, v141, s68, v151\n\t"
    "ds_read_b128 v[172:175], v138 offset:0\n\t"
    "ds_read_b128 v[176:179], v138 offset:64\n\t"
    "v_mfma_f32_16x16x32_bf16 v[100:103], v[156:159], v[188:191], 0\n\t"
    "v_fmac_f32 v120, v116, v36\n\t"
    "v_fmac_f32 v121, v117, v37\n\t"
    "v_mfma_f32_16x16x32_bf16 v[100:103], v[160:163], v[192:195], v[100:103]\n\t"
    "v_fmac_f32 v120, v118, v38\n\t"
    "v_fmac_f32 v121, v119, v39\n\t"
    "v_mfma_f32_16x16x32_bf16 v[104:107], v[180:183], v[188:191], 0\n\t"
    "v_and_b32 v141, -16, v141\n\t"
    "v_fmac_f32 v120, v92, v40\n\t"
    "v_mfma_f32_16x16x32_bf16 v[104:107], v[184:187], v[192:195], v[104:107]\n\t"
    "v_fmac_f32 v121, v93, v41\n\t"
    "v_fmac_f32 v120, v94, v42\n\t"
    "v_mfma_f32_16x16x32_bf16 v[108:111], v[180:183], v[196:199], 0\n\t"
    "v_fmac_f32 v121, v95, v43\n\t"
    "v_fmac_f32 v120, v96, v44\n\t"
    "v_mfma_f32_16x16x32_bf16 v[108:111], v[184:187], v[200:203], v[108:111]\n\t"
    "v_fmac_f32 v121, v97, v45\n\t"
    "v_fmac_f32 v120, v98, v46\n\t"
    "v_mfma_f32_16x16x32_bf16 v[112:115], v[180:183], v[204:207], 0\n\t"
    "v_fmac_f32 v121, v99, v47\n\t"
    "v_fmac_f32 v120, v100, v88\n\t"
    "v_mfma_f32_16x16x32_bf16 v[112:115], v[184:187], v[208:211], v[112:115]\n\t"
    "v_mad_u32_u24 v142, v142, s68, v151\n\t"
    "v_and_b32 v142, -16, v142\n\t"
    "ds_read_b128 v[180:183], v140 offset:0\n\t"
    "ds_read_b128 v[184:187], v140 offset:64\n\t"
    "s_cmp_eq_u32 s45, 1\n\t"
    "s_cbranch_scc1 L_sig_%=\n\t"
    "L_sigb_%=:\n\t"
    "v_mfma_f32_16x16x32_bf16 v[116:119], v[156:159], v[196:199], 0\n\t"
    "v_fmac_f32 v121, v104, v48\n\t"
    "v_fmac_f32 v120, v105, v49\n\t"
    "v_mfma_f32_16x16x32_bf16 v[116:119], v[160:163], v[200:203], v[116:119]\n\t"
    "v_fmac_f32 v121, v106, v50\n\t"
    "v_fmac_f32 v120, v107, v51\n\t"
    "v_mfma_f32_16x16x32_bf16 v[92:95], v[188:191], v[188:191], 0\n\t"
    "v_mad_u32_u24 v143, v143, s68, v151\n\t"
    "v_and_b32 v143, -16, v143\n\t"
    "v_mfma_f32_16x16x32_bf16 v[92:95], v[192:195], v[192:195], v[92:95]\n\t"
    "v_fmac_f32 v121, v108, v52\n\t"
    "v_fmac_f32 v120, v109, v53\n\t"
    "v_mfma_f32_16x16x32_bf16 v[96:99], v[188:191], v[196:199], 0\n\t"
    "v_fmac_f32 v121, v110, v54\n\t"
    "v_fmac_f32 v120, v111, v55\n\t"
    "v_mfma_f32_16x16x32_bf16 v[96:99], v[192:195], v[200:203], v[96:99]\n\t"
    "v_fmac_f32 v121, v112, v56\n\t"
    "v_fmac_f32 v120, v113, v57\n\t"
    "v_mfma_f32_16x16x32_bf16 v[100:103], v[188:191], v[204:207], 0\n\t"
    "v_fmac_f32 v121, v114, v58\n\t"
    "v_fmac_f32 v120, v115, v59\n\t"
    "v_mfma_f32_16x16x32_bf16 v[100:103], v[192:195], v[208:211], v[100:103]\n\t"
    "v_fmac_f32 v121, v116, v89\n\t"
    "ds_read_b128 v[188:191], v141 offset:0\n\t"
    "ds_read_b128 v[192:195], v141 offset:64\n\t"
    "v_mfma_f32_16x16x32_bf16 v[104:107], v[156:159], v[204:207], 0\n\t"
    "v_fmac_f32 v120, v92, v60\n\t"
    "v_fmac_f32 v121, v93, v61\n\t"
    "v_mfma_f32_16x16x32_bf16 v[104:107], v[160:163], v[208:211], v[104:107]\n\t"
    "v_fmac_f32 v120, v94, v62\n\t"
    "v_fmac_f32 v121, v95, v63\n\t"
    "v_mfma_f32_16x16x32_bf16 v[108:111], v[196:199], v[196:199], 0\n\t"
    "v_fmac_f32 v120, v96, v64\n\t"
    "v_fmac_f32 v121, v97, v65\n\t"
    "v_mfma_f32_16x16x32_bf16 v[108:111], v[200:203], v[200:203], v[108:111]\n\t"
    "v_fmac_f32 v120, v98, v66\n\t"
    "v_fmac_f32 v121, v99, v67\n\t"
    "v_mfma_f32_16x16x32_bf16 v[112:115], v[196:199], v[204:207], 0\n\t"
    "v_fmac_f32 v120, v100, v68\n\t"
    "v_fmac_f32 v121, v101, v69\n\t"
    "v_mfma_f32_16x16x32_bf16 v[112:115], v[200:203], v[208:211], v[112:115]\n\t"
    "v_fmac_f32 v120, v102, v70\n\t"
    "v_fmac_f32 v121, v103, v71\n\t"
    "ds_read_b128 v[196:199], v142 offset:0\n\t"
    "ds_read_b128 v[200:203], v142 offset:64\n\t"
    "v_mfma_f32_16x16x32_bf16 v[116:119], v[204:207], v[204:207], 0\n\t"
    "v_fmac_f32 v120, v104, v90\n\t"
    "v_mfma_f32_16x16x32_bf16 v[116:119], v[208:211], v[208:211], v[116:119]\n\t"
    "ds_read_b128 v[204:207], v143 offset:0\n\t"
    "ds_read_b128 v[208:211], v143 offset:64\n\t"
    "s_mov_b32 s52, s53\n\t"
    "s_mov_b32 s53, s54\n\t"
    "s_mov_b32 s54, s55\n\t"
    "s_mov_b32 s55, s60\n\t"
    "s_cmp_lt_u32 s54, s64\n\t"
    "s_cbranch_scc1 L_pr0_%=\n\t"
    "s_setprio 0\n\t"
    "L_pr0_%=:\n\t"
    "s_cmp_lt_u32 s53, s64\n\t"
    "s_cbranch_scc0 L_done_%=\n\t"
    "s_mul_i32 s69, s52, 272\n\t"
    "s_add_u32 s69, s69, 130048\n\t"
    "s_mul_i32 s66, s55, 400\n\t"
    "s_waitcnt lgkmcnt(8)\n\t"
    "v_mfma_f32_16x16x32_bf16 v[92:95], v[212:215], v[212:215], 0\n\t"
    "v_add_u32 v147, s69, v146\n\t"
    "v_fmac_f32 v121, v108, v72\n\t"
    "ds_add_rtn_u32 v148, v149, v150\n\t"
    "v_mfma_f32_16x16x32_bf16 v[92:95], v[216:219], v[216:219], v[92:95]\n\t"
    "v_fmac_f32 v120, v109, v73\n\t"
    "v_fmac_f32 v121, v110, v74\n\t"
    "buffer_load_dwordx3 v[136:138], v153, s[96:99], s66 offen\n\t"
    "v_mfma_f32_16x16x32_bf16 v[96:99], v[164:167], v[164:167], 0\n\t"
    "v_fmac_f32 v120, v111, v75\n\t"
    "v_fmac_f32 v121, v112, v76\n\t"
    "v_mfma_f32_16x16x32_bf16 v[96:99], v[168:171], v[168:171], v[96:99]\n\t"
    "v_fmac_f32 v120, v113, v77\n\t"
    "v_fmac_f32 v121, v114, v78\n\t"
    "buffer_load_dwordx4 v[140:143], v153, s[96:99], s66 offen offset:12\n\t"
    "v_mfma_f32_16x16x32_bf16 v[100:103], v[164:167], v[172:175], 0\n\t"
    "v_fmac_f32 v120, v115, v79\n\t"
    "v_fmac_f32 v121, v116, v80\n\t"
    "v_mfma_f32_16x16x32_bf16 v[100:103], v[168:171], v[176:179], v[100:103]\n\t"
    "v_fmac_f32 v120, v117, v81\n\t"
    "v_fmac_f32 v121, v118, v82\n\t"
    "s_waitcnt vmcnt(3)\n\t"
    "s_cmp_ge_u32 s55, s61\n\t"
    "s_cbranch_scc1 L_req1_%=\n\t"
    "L_back1_%=:\n\t"
    "v_mfma_f32_16x16x32_bf16 v[104:107], v[172:175], v[172:175], 0\n\t"
    "v_mad_u32_u24 v128, v128, s68, v152\n\t"
    "v_and_b32 v128, -16, v128\n\t"
    "v_mfma_f32_16x16x32_bf16 v[104:107], v[176:179], v[176:179], v[104:107]\n\t"
    "v_fmac_f32 v120, v119, v83\n\t"
    "v_add_f32 v154, v120, v121\n\t"
    "ds_write_b32 v147, v154\n\t"
    "v_mfma_f32_16x16x32_bf16 v[108:111], v[212:215], v[164:167], 0\n\t"
    "v_fma_f32 v120, v92, v84, v145\n\t"
    "v_mul_f32 v121, v96, v0\n\t"
    "v_mfma_f32_16x16x32_bf16 v[108:111], v[216:219], v[168:171], v[108:111]\n\t"
    "v_fmac_f32 v120, v97, v1\n\t"
    "v_fmac_f32 v121, v98, v2\n\t"
    "s_waitcnt lgkmcnt(8)\n\t"
    "v_mfma_f32_16x16x32_bf16 v[112:115], v[164:167], v[180:183], 0\n\t"
    "v_fmac_f32 v120, v99, v3\n\t"
    "v_fmac_f32 v121, v100, v4\n\t"
    "v_mfma_f32_16x16x32_bf16 v[112:115], v[168:171], v[184:187], v[112:115]\n\t"
    "v_fmac_f32 v120, v101, v5\n\t"
    "v_fmac_f32 v121, v102, v6\n\t"
    "v_mfma_f32_16x16x32_bf16 v[116:119], v[172:175], v[180:183], 0\n\t"
    "v_fmac_f32 v120, v103, v7\n\t"
    "v_fmac_f32 v121, v104, v8\n\t"
    "v_mfma_f32_16x16x32_bf16 v[116:119], v[176:179], v[184:187], v[116:119]\n\t"
    "v_fmac_f32 v120, v105, v9\n\t"
    "v_fmac_f32 v121, v106, v10\n\t"
    "ds_read_b128 v[156:159], v128 offset:0\n\t"
    "ds_read_b128 v[160:163], v128 offset:64\n\t"
    "v_mfma_f32_16x16x32_bf16 v[92:95], v[180:183], v[180:183], 0\n\t"
    "v_fmac_f32 v120, v107, v11\n\t"
    "v_mad_u32_u24 v129, v129, s68, v151\n\t"
    "v_mfma_f32_16x16x32_bf16 v[92:95], v[184:187], v[184:187], v[92:95]\n\t"
    "v_and_b32 v129, -16, v129\n\t"
    "v_fmac_f32 v121, v108, v85\n\t"
    "v_mfma_f32_16x16x32_bf16 v[96:99], v[212:215], v[172:175], 0\n\t"
    "v_fmac_f32 v120, v112, v12\n\t"
    "v_fmac_f32 v121, v113, v13\n\t"
    "v_mfma_f32_16x16x32_bf16 v[96:99], v[216:219], v[176:179], v[96:99]\n\t"
    "v_fmac_f32 v120, v114, v14\n\t"
    "v_fmac_f32 v121, v115, v15\n\t"
    "s_lshl_b32 s51, s55, 8\n\t"
    "s_add_u32 s51, s51, s50\n\t"
    "s_waitcnt lgkmcnt(8)\n\t"
    "v_mfma_f32_16x16x32_bf16 v[100:103], v[164:167], v[188:191], 0\n\t"
    "v_mad_u32_u24 v130, v130, s68, v151\n\t"
    "v_and_b32 v130, -16, v130\n\t"
    "v_mfma_f32_16x16x32_bf16 v[100:103], v[168:171], v[192:195], v[100:103]\n\t"
    "v_fmac_f32 v120, v116, v16\n\t"
    "v_fmac_f32 v121, v117, v17\n\t"
    "buffer_load_dword v145, v146, s[28:31], s51 offen sc1\n\t"
    "v_mfma_f32_16x16x32_bf16 v[104:107], v[172:175], v[188:191], 0\n\t"
    "v_fmac_f32 v120, v118, v18\n\t"
    "v_fmac_f32 v121, v119, v19\n\t"
    "v_mfma_f32_16x16x32_bf16 v[104:107], v[176:179], v[192:195], v[104:107]\n\t"
    "v_fmac_f32 v120, v92, v20\n\t"
    "v_fmac_f32 v121, v93, v21\n\t"
    "s_waitcnt lgkmcnt(6)\n\t"
    "v_mfma_f32_16x16x32_bf16 v[108:111], v[164:167], v[196:199], 0\n\t"
    "v_fmac_f32 v120, v94, v22\n\t"
    "v_fmac_f32 v121, v95, v23\n\t"
    "v_mfma_f32_16x16x32_bf16 v[108:111], v[168:171], v[200:203], v[108:111]\n\t"
    "v_fmac_f32 v120, v96, v86\n\t"
    "v_mad_u32_u24 v132, v132, s68, v151\n\t"
    "v_mfma_f32_16x16x32_bf16 v[112:115], v[212:215], v[180:183], 0\n\t"
    "v_fmac_f32 v121, v100, v24\n\t"
    "v_fmac_f32 v120, v101, v25\n\t"
    "v_mfma_f32_16x16x32_bf16 v[112:115], v[216:219], v[184:187], v[112:115]\n\t"
    "v_fmac_f32 v121, v102, v26\n\t"
    "v_fmac_f32 v120, v103, v27\n\t"
    "s_waitcnt lgkmcnt(3)\n\t"
    "v_mfma_f32_16x16x32_bf16 v[116:119], v[164:167], v[204:207], 0\n\t"
    "v_readfirstlane_b32 s60, v148\n\t"
    "v_and_b32 v132, -16, v132\n\t"
    "v_mfma_f32_16x16x32_bf16 v[116:119], v[168:171], v[208:211], v[116:119]\n\t"
    "v_fmac_f32 v121, v104, v28\n\t"
    "v_fmac_f32 v120, v105, v29\n\t"
    "ds_read_b128 v[164:167], v129 offset:0\n\t"
    "ds_read_b128 v[168:171], v129 offset:64\n\t"
    "v_mfma_f32_16x16x32_bf16 v[92:95], v[172:175], v[196:199], 0\n\t"
    "v_fmac_f32 v121, v106, v30\n\t"
    "v_fmac_f32 v120, v107, v31\n\t"
    "v_mfma_f32_16x16x32_bf16 v[92:95], v[176:179], v[200:203], v[92:95]\n\t"
    "v_fmac_f32 v121, v108, v32\n\t"
    "v_fmac_f32 v120, v109, v33\n\t"
    "v_mfma_f32_16x16x32_bf16 v[96:99], v[172:175], v[204:207], 0\n\t"
    "v_fmac_f32 v121, v110, v34\n\t"
    "v_fmac_f32 v120, v111, v35\n\t"
    "v_mfma_f32_16x16x32_bf16 v[96:99], v[176:179], v[208:211], v[96:99]\n\t"
    "v_fmac_f32 v121, v112, v87\n\t"
    "v_mad_u32_u24 v133, v133, s68, v151\n\t"
    "ds_read_b128 v[172:175], v130 offset:0\n\t"
    "ds_read_b128 v[176:179], v130 offset:64\n\t"
    "v_mfma_f32_16x16x32_bf16 v[100:103], v[212:215], v[188:191], 0\n\t"
    "v_fmac_f32 v120, v116, v36\n\t"
    "v_fmac_f32 v121, v117, v37\n\t"
    "v_mfma_f32_16x16x32_bf16 v[100:103], v[216:219], v[192:195], v[100:103]\n\t"
    "v_fmac_f32 v120, v118, v38\n\t"
    "v_fmac_f32 v121, v119, v39\n\t"
    "v_mfma_f32_16x16x32_bf16 v[104:107], v[180:183], v[188:191], 0\n\t"
    "v_and_b32 v133, -16, v133\n\t"
    "v_fmac_f32 v120, v92, v40\n\t"
    "v_mfma_f32_16x16x32_bf16 v[104:107], v[184:187], v[192:195], v[104:107]\n\t"
    "v_fmac_f32 v121, v93, v41\n\t"
    "v_fmac_f32 v120, v94, v42\n\t"
    "v_mfma_f32_16x16x32_bf16 v[108:111], v[180:183], v[196:199], 0\n\t"
    "v_fmac_f32 v121, v95, v43\n\t"
    "v_fmac_f32 v120, v96, v44\n\t"
    "v_mfma_f32_16x16x32_bf16 v[108:111], v[184:187], v[200:203], v[108:111]\n\t"
    "v_fmac_f32 v121, v97, v45\n\t"
    "v_fmac_f32 v120, v98, v46\n\t"
    "v_mfma_f32_16x16x32_bf16 v[112:115], v[180:183], v[204:207], 0\n\t"
    "v_fmac_f32 v121, v99, v47\n\t"
    "v_fmac_f32 v120, v100, v88\n\t"
    "v_mfma_f32_16x16x32_bf16 v[112:115], v[184:187], v[208:211], v[112:115]\n\t"
    "v_mad_u32_u24 v134, v134, s68, v151\n\t"
    "v_and_b32 v134, -16, v134\n\t"
    "ds_read_b128 v[180:183], v132 offset:0\n\t"
    "ds_read_b128 v[184:187], v132 offset:64\n\t"
    "v_mfma_f32_16x16x32_bf16 v[116:119], v[212:215], v[196:199], 0\n\t"
    "v_fmac_f32 v121, v104, v48\n\t"
    "v_fmac_f32 v120, v105, v49\n\t"
    "v_mfma_f32_16x16x32_bf16 v[116:119], v[216:219], v[200:203], v[116:119]\n\t"
    "v_fmac_f32 v121, v106, v50\n\t"
    "v_fmac_f32 v120, v107, v51\n\t"
    "v_mfma_f32_16x16x32_bf16 v[92:95], v[188:191], v[188:191], 0\n\t"
    "v_mad_u32_u24 v135, v135, s68, v151\n\t"
    "v_and_b32 v135, -16, v135\n\t"
    "v_mfma_f32_16x16x32_bf16 v[92:95], v[192:195], v[192:195], v[92:95]\n\t"
    "v_fmac_f32 v121, v108, v52\n\t"
    "v_fmac_f32 v120, v109, v53\n\t"
    "v_mfma_f32_16x16x32_bf16 v[96:99], v[188:191], v[196:199], 0\n\t"
    "v_fmac_f32 v121, v110, v54\n\t"
    "v_fmac_f32 v120, v111, v55\n\t"
    "v_mfma_f32_16x16x32_bf16 v[96:99], v[192:195], v[200:203], v[96:99]\n\t"
    "v_fmac_f32 v121, v112, v56\n\t"
    "v_fmac_f32 v120, v113, v57\n\t"
    "v_mfma_f32_16x16x32_bf16 v[100:103], v[188:191], v[204:207], 0\n\t"
    "v_fmac_f32 v121, v114, v58\n\t"
    "v_fmac_f32 v120, v115, v59\n\t"
    "v_mfma_f32_16x16x32_bf16 v[100:103], v[192:195], v[208:211], v[100:103]\n\t"
    "v_fmac_f32 v121, v116, v89\n\t"
    "ds_read_b128 v[188:191], v133 offset:0\n\t"
    "ds_read_b128 v[192:195], v133 offset:64\n\t"
    "v_mfma_f32_16x16x32_bf16 v[104:107], v[212:215], v[204:207], 0\n\t"
    "v_fmac_f32 v120, v92, v60\n\t"
    "v_fmac_f32 v121, v93, v61\n\t"
    "v_mfma_f32_16x16x32_bf16 v[104:107], v[216:219], v[208:211], v[104:107]\n\t"
    "v_fmac_f32 v120, v94, v62\n\t"
    "v_fmac_f32 v121, v95, v63\n\t"
    "v_mfma_f32_16x16x32_bf16 v[108:111], v[196:199], v[196:199], 0\n\t"
    "v_fmac_f32 v120, v96, v64\n\t"
    "v_fmac_f32 v121, v97, v65\n\t"
    "v_mfma_f32_16x16x32_bf16 v[108:111], v[200:203], v[200:203], v[108:111]\n\t"
    "v_fmac_f32 v120, v98, v66\n\t"
    "v_fmac_f32 v121, v99, v67\n\t"
    "v_mfma_f32_16x16x32_bf16 v[112:115], v[196:199], v[204:207], 0\n\t"
    "v_fmac_f32 v120, v100, v68\n\t"
    "v_fmac_f32 v121, v101, v69\n\t"
    "v_mfma_f32_16x16x32_bf16 v[112:115], v[200:203], v[208:211], v[112:115]\n\t"
    "v_fmac_f32 v120, v102, v70\n\t"
    "v_fmac_f32 v121, v103, v71\n\t"
    "ds_read_b128 v[196:199], v134 offset:0\n\t"
    "ds_read_b128 v[200:203], v134 offset:64\n\t"
    "v_mfma_f32_16x16x32_bf16 v[116:119], v[204:207], v[204:207], 0\n\t"
    "v_fmac_f32 v120, v104, v90\n\t"
    "v_mfma_f32_16x16x32_bf16 v[116:119], v[208:211], v[208:211], v[116:119]\n\t"
    "ds_read_b128 v[204:207], v135 offset:0\n\t"
    "ds_read_b128 v[208:211], v135 offset:64\n\t"
    "s_mov_b32 s52, s53\n\t"
    "s_mov_b32 s53, s54\n\t"
    "s_mov_b32 s54, s55\n\t"
    "s_mov_b32 s55, s60\n\t"
    "s_cmp_lt_u32 s54, s64\n\t"
    "s_cbranch_scc1 L_pr1_%=\n\t"
    "s_setprio 0\n\t"
    "L_pr1_%=:\n\t"
    "s_cmp_lt_u32 s53, s64\n\t"
    "s_cbranch_scc1 L_pnn_%=\n\t"
    "L_done_%=:\n\t"
    "s_mul_i32 s69, s52, 272\n\t"
    "s_add_u32 s69, s69, 130048\n\t"
    "v_add_u32 v147, s69, v146\n\t"
    "v_fmac_f32 v121, v108, v72\n\t"
    "v_fmac_f32 v120, v109, v73\n\t"
    "v_fmac_f32 v121, v110, v74\n\t"
    "v_fmac_f32 v120, v111, v75\n\t"
    "v_fmac_f32 v121, v112, v76\n\t"
    "v_fmac_f32 v120, v113, v77\n\t"
    "v_fmac_f32 v121, v114, v78\n\t"
    "v_fmac_f32 v120, v115, v79\n\t"
    "v_fmac_f32 v121, v116, v80\n\t"
    "v_fmac_f32 v120, v117, v81\n\t"
    "v_fmac_f32 v121, v118, v82\n\t"
    "v_fmac_f32 v120, v119, v83\n\t"
    "v_add_f32 v154, v120, v121\n\t"
    "ds_write_b32 v147, v154\n\t"
    "v_lshrrev_b32 v147, 2, v146\n\t"
    "s_mov_b64 s[92:93], s[22:23]\n\t"
    "s_and_b32 s93, s93, 0xffff\n\t"
    "s_mov_b32 s94, 32768\n\t"
    "s_mov_b32 s95, 0x20000\n\t"
    "s_lshl_b32 s44, s37, 1\n\t"
    "v_and_b32 v155, 15, v147\n\t"
    "v_lshl_add_u32 v155, s34, 4, v155\n\t"
    "v_lshrrev_b32 v154, 4, v147\n\t"
    "v_mul_u32_u24 v122, 272, v155\n\t"
    "v_lshl_add_u32 v122, v154, 6, v122\n\t"
    "v_add_u32 v122, 130048, v122\n\t"
    "v_mul_u32_u24 v154, 2688, v154\n\t"
    "v_lshl_add_u32 v154, v155, 2, v154\n\t"
    "v_add_u32 v154, 152896, v154\n\t"
    "v_cmp_le_u32 vcc, 48, v147\n\t"
    "v_cmp_gt_u32 s[46:47], s38, v155\n\t"
    "v_lshlrev_b32 v155, 1, v155\n\t"
    "s_and_b64 vcc, vcc, s[46:47]\n\t"
    "s_waitcnt lgkmcnt(0)\n\t"
    "s_barrier\n\t"
    "s_cbranch_vccz L_fin_%=\n\t"
    "ds_read_b128 v[92:95], v122 offset:0\n\t"
    "ds_read_b128 v[96:99], v122 offset:16\n\t"
    "ds_read_b128 v[100:103], v122 offset:32\n\t"
    "ds_read_b128 v[104:107], v122 offset:48\n\t"
    "ds_read2_b32 v[108:109], v154 offset0:0 offset1:84\n\t"
    "ds_read2_b32 v[110:111], v154 offset0:168 offset1:252\n\t"
    "v_add_u32 v154, 1344, v154\n\t"
    "ds_read2_b32 v[112:113], v154 offset0:0 offset1:84\n\t"
    "ds_read2_b32 v[114:115], v154 offset0:168 offset1:252\n\t"
    "s_waitcnt lgkmcnt(6)\n\t"
    "v_add_f32 v92, v92, v96\n\t"
    "v_add_f32 v93, v93, v97\n\t"
    "v_add_f32 v94, v94, v98\n\t"
    "v_add_f32 v95, v95, v99\n\t"
    "s_waitcnt lgkmcnt(5)\n\t"
    "v_add_f32 v92, v92, v100\n\t"
    "v_add_f32 v93, v93, v101\n\t"
    "v_add_f32 v94, v94, v102\n\t"
    "v_add_f32 v95, v95, v103\n\t"
    "s_waitcnt lgkmcnt(4)\n\t"
    "v_add_f32 v92, v92, v104\n\t"
    "v_add_f32 v93, v93, v105\n\t"
    "v_add_f32 v94, v94, v106\n\t"
    "v_add_f32 v95, v95, v107\n\t"
    "s_waitcnt lgkmcnt(2)\n\t"
    "v_add_f32 v92, v92, v108\n\t"
    "v_add_f32 v93, v93, v109\n\t"
    "v_add_f32 v94, v94, v110\n\t"
    "v_add_f32 v95, v95, v111\n\t"
    "s_waitcnt lgkmcnt(0)\n\t"
    "v_add_f32 v92, v92, v112\n\t"
    "v_add_f32 v93, v93, v113\n\t"
    "v_add_f32 v94, v94, v114\n\t"
    "v_add_f32 v95, v95, v115\n\t"
    "v_add_f32 v92, v92, v93\n\t"
    "v_add_f32 v94, v94, v95\n\t"
    "v_add_f32 v92, v92, v94\n\t"
    "v_mov_b32 v93, v92\n\t"
    "s_nop 1\n\t"
    "v_permlane16_swap_b32 v93, v92\n\t"
    "s_nop 1\n\t"
    "v_add_f32 v92, v92, v93\n\t"
    "v_mov_b32 v93, v92\n\t"
    "s_nop 1\n\t"
    "v_permlane32_swap_b32 v93, v92\n\t"
    "s_nop 1\n\t"
    "v_add_f32 v92, v92, v93\n\t"
    "v_add_f32 v92, s39, v92\n\t"
    "v_cvt_pk_bf16_f32 v92, v92, v92\n\t"
    "s_mov_b64 exec, vcc\n\t"
    "buffer_store_short v92, v155, s[92:95], s44 offen\n\t"
    "s_mov_b64 exec, -1\n\t"
    "s_branch L_fin_%=\n\t"
    "L_req0_%=:\n\t"
    "s_cmp_lg_u32 s61, 0\n\t"
    "s_cbranch_scc1 L_req0b_%=\n\t"
    "s_bfe_u32 s65, s84, 0xc0002\n\t"
    "s_add_u32 s65, s65, s36\n\t"
    "buffer_store_dword v236, v125, s[28:31], s65 offen offset:0 sc1\n\t"
    "buffer_store_dword v237, v125, s[28:31], s65 offen offset:256 sc1\n\t"
    "s_bfe_u32 s65, s85, 0xc0002\n\t"
    "s_add_u32 s65, s65, s36\n\t"
    "buffer_store_dword v238, v125, s[28:31], s65 offen offset:0 sc1\n\t"
    "buffer_store_dword v239, v125, s[28:31], s65 offen offset:256 sc1\n\t"
    "s_bfe_u32 s65, s86, 0xc0002\n\t"
    "s_add_u32 s65, s65, s36\n\t"
    "buffer_store_dword v240, v125, s[28:31], s65 offen offset:0 sc1\n\t"
    "buffer_store_dword v241, v125, s[28:31], s65 offen offset:256 sc1\n\t"
    "s_bfe_u32 s65, s87, 0xc0002\n\t"
    "s_add_u32 s65, s65, s36\n\t"
    "buffer_store_dword v242, v125, s[28:31], s65 offen offset:0 sc1\n\t"
    "buffer_store_dword v243, v125, s[28:31], s65 offen offset:256 sc1\n\t"
    "s_bfe_u32 s65, s88, 0xc0002\n\t"
    "s_add_u32 s65, s65, s36\n\t"
    "buffer_store_dword v244, v125, s[28:31], s65 offen offset:0 sc1\n\t"
    "buffer_store_dword v245, v125, s[28:31], s65 offen offset:256 sc1\n\t"
    "s_bfe_u32 s65, s89, 0xc0002\n\t"
    "s_add_u32 s65, s65, s36\n\t"
    "buffer_store_dword v246, v125, s[28:31], s65 offen offset:0 sc1\n\t"
    "buffer_store_dword v247, v125, s[28:31], s65 offen offset:256 sc1\n\t"
    "s_bfe_u32 s65, s90, 0xc0002\n\t"
    "s_add_u32 s65, s65, s36\n\t"
    "buffer_store_dword v248, v125, s[28:31], s65 offen offset:0 sc1\n\t"
    "buffer_store_dword v249, v125, s[28:31], s65 offen offset:256 sc1\n\t"
    "s_bfe_u32 s65, s91, 0xc0002\n\t"
    "s_add_u32 s65, s65, s36\n\t"
    "buffer_store_dword v250, v125, s[28:31], s65 offen offset:0 sc1\n\t"
    "buffer_store_dword v251, v125, s[28:31], s65 offen offset:256 sc1\n\t"
    "s_sub_u32 s61, s64, 3\n\t"
    "s_branch L_back0_%=\n\t"
    "L_req0b_%=:\n\t"
    "s_cmp_lg_u32 s61, 1\n\t"
    "s_cbranch_scc1 L_req0c_%=\n\t"
    "v_lshrrev_b32 v154, 16, v122\n\t"
    "v_lshl_add_u32 v154, s34, 2, v154\n\t"
    "v_mul_u32_u24 v154, 336, v154\n\t"
    "v_and_b32 v122, 0xf0, v122\n\t"
    "v_cmp_gt_u32 vcc, 80, v122\n\t"
    "v_add3_u32 v122, v122, v154, s45\n\t"
    "s_mov_b32 s61, -1\n\t"
    "v_add_f32 v124, v220, v224\n\t"
    "v_add_f32 v154, v228, v232\n\t"
    "v_add_f32 v124, v124, v154\n\t"
    "v_add_f32 v125, v221, v225\n\t"
    "v_add_f32 v154, v229, v233\n\t"
    "v_add_f32 v125, v125, v154\n\t"
    "v_add_f32 v126, v222, v226\n\t"
    "v_add_f32 v154, v230, v234\n\t"
    "v_add_f32 v126, v126, v154\n\t"
    "v_add_f32 v127, v223, v227\n\t"
    "v_add_f32 v154, v231, v235\n\t"
    "v_add_f32 v127, v127, v154\n\t"
    "ds_write_b128 v122, v[124:127] offset:0\n\t"
    "s_cmp_le_u32 s38, 64\n\t"
    "s_cbranch_scc1 L_back0_%=\n\t"
    "v_add_f32 v124, v236, v240\n\t"
    "v_add_f32 v154, v244, v248\n\t"
    "v_add_f32 v124, v124, v154\n\t"
    "v_add_f32 v125, v237, v241\n\t"
    "v_add_f32 v154, v245, v249\n\t"
    "v_add_f32 v125, v125, v154\n\t"
    "v_add_f32 v126, v238, v242\n\t"
    "v_add_f32 v154, v246, v250\n\t"
    "v_add_f32 v126, v126, v154\n\t"
    "v_add_f32 v127, v239, v243\n\t"
    "v_add_f32 v154, v247, v251\n\t"
    "v_add_f32 v127, v127, v154\n\t"
    "s_and_saveexec_b64 s[46:47], vcc\n\t"
    "ds_write_b128 v122, v[124:127] offset:256\n\t"
    "s_mov_b64 exec, s[46:47]\n\t"
    "s_branch L_back0_%=\n\t"
    "L_req0c_%=:\n\t"
    "s_lshl_b32 s46, %2, 7\n\t"
    "s_add_u32 s46, s46, 49295360\n\t"
    "s_mov_b32 s47, 1048576\n\t"
    "s_mov_b64 exec, 1\n\t"
    "s_cmp_ge_u32 s55, s64\n\t"
    "s_cbranch_scc1 L_pq0_%=\n\t"
    "buffer_load_dword v123, off, s[28:31], s46 sc1\n\t"
    "s_mov_b64 exec, -1\n\t"
    "s_branch L_back0_%=\n\t"
    "L_pq0_%=:\n\t"
    "v_cmp_eq_u32 vcc, 0, v123\n\t"
    "s_cbranch_vccz L_in0_%=\n\t"
    "L_poll0_%=:\n\t"
    "buffer_load_dword v155, off, s[28:31], s46 sc1\n\t"
    "s_sub_u32 s47, s47, 1\n\t"
    "s_cmp_eq_u32 s47, 0\n\t"
    "s_cbranch_scc1 L_in0_%=\n\t"
    "s_waitcnt vmcnt(0)\n\t"
    "v_cmp_eq_u32 vcc, 0, v155\n\t"
    "s_cbranch_vccz L_in0_%=\n\t"
    "s_sleep 8\n\t"
    "s_branch L_poll0_%=\n\t"
    "L_in0_%=:\n\t"
    "s_mov_b64 exec, -1\n\t"
    "s_lshl_b32 s46, s37, 2\n\t"
    "s_mul_i32 s47, s34, 1048576\n\t"
    "s_add_u32 s46, s46, s47\n\t"
    "s_add_u32 s46, s46, 17825792\n\t"
    "v_and_b32 v155, 0x80, v122\n\t"
    "v_lshl_add_u32 v155, v155, 23, v122\n\t"
    "buffer_load_dwordx4 v[220:223], v122, s[28:31], s46 offen sc1\n\t"
    "s_add_u32 s46, s46, 262144\n\t"
    "buffer_load_dwordx4 v[224:227], v122, s[28:31], s46 offen sc1\n\t"
    "s_add_u32 s46, s46, 262144\n\t"
    "buffer_load_dwordx4 v[228:231], v122, s[28:31], s46 offen sc1\n\t"
    "s_add_u32 s46, s46, 262144\n\t"
    "buffer_load_dwordx4 v[232:235], v122, s[28:31], s46 offen sc1\n\t"
    "s_add_u32 s46, s46, 262144\n\t"
    "s_mov_b32 s61, 1\n\t"
    "s_cmp_le_u32 s38, 64\n\t"
    "s_cbranch_scc1 L_back0_%=\n\t"
    "s_sub_u32 s46, s46, 262144\n\t"
    "buffer_load_dwordx4 v[248:251], v155, s[28:31], s46 offen offset:256 sc1\n\t"
    "s_sub_u32 s46, s46, 262144\n\t"
    "buffer_load_dwordx4 v[244:247], v155, s[28:31], s46 offen offset:256 sc1\n\t"
    "s_sub_u32 s46, s46, 262144\n\t"
    "buffer_load_dwordx4 v[240:243], v155, s[28:31], s46 offen offset:256 sc1\n\t"
    "s_sub_u32 s46, s46, 262144\n\t"
    "buffer_load_dwordx4 v[236:239], v155, s[28:31], s46 offen offset:256 sc1\n\t"
    "s_branch L_back0_%=\n\t"
    "L_req1_%=:\n\t"
    "s_cmp_lg_u32 s61, 1\n\t"
    "s_cbranch_scc1 L_req1c_%=\n\t"
    "v_lshrrev_b32 v154, 16, v122\n\t"
    "v_lshl_add_u32 v154, s34, 2, v154\n\t"
    "v_mul_u32_u24 v154, 336, v154\n\t"
    "v_and_b32 v122, 0xf0, v122\n\t"
    "v_cmp_gt_u32 vcc, 80, v122\n\t"
    "v_add3_u32 v122, v122, v154, s45\n\t"
    "s_mov_b32 s61, -1\n\t"
    "v_add_f32 v124, v220, v224\n\t"
    "v_add_f32 v154, v228, v232\n\t"
    "v_add_f32 v124, v124, v154\n\t"
    "v_add_f32 v125, v221, v225\n\t"
    "v_add_f32 v154, v229, v233\n\t"
    "v_add_f32 v125, v125, v154\n\t"
    "v_add_f32 v126, v222, v226\n\t"
    "v_add_f32 v154, v230, v234\n\t"
    "v_add_f32 v126, v126, v154\n\t"
    "v_add_f32 v127, v223, v227\n\t"
    "v_add_f32 v154, v231, v235\n\t"
    "v_add_f32 v127, v127, v154\n\t"
    "ds_write_b128 v122, v[124:127] offset:0\n\t"
    "s_cmp_le_u32 s38, 64\n\t"
    "s_cbranch_scc1 L_back1_%=\n\t"
    "v_add_f32 v124, v236, v240\n\t"
    "v_add_f32 v154, v244, v248\n\t"
    "v_add_f32 v124, v124, v154\n\t"
    "v_add_f32 v125, v237, v241\n\t"
    "v_add_f32 v154, v245, v249\n\t"
    "v_add_f32 v125, v125, v154\n\t"
    "v_add_f32 v126, v238, v242\n\t"
    "v_add_f32 v154, v246, v250\n\t"
    "v_add_f32 v126, v126, v154\n\t"
    "v_add_f32 v127, v239, v243\n\t"
    "v_add_f32 v154, v247, v251\n\t"
    "v_add_f32 v127, v127, v154\n\t"
    "s_and_saveexec_b64 s[46:47], vcc\n\t"
    "ds_write_b128 v122, v[124:127] offset:256\n\t"
    "s_mov_b64 exec, s[46:47]\n\t"
    "s_branch L_back1_%=\n\t"
    "L_req1c_%=:\n\t"
    "s_lshl_b32 s46, %2, 7\n\t"
    "s_add_u32 s46, s46, 49295360\n\t"
    "s_mov_b32 s47, 1048576\n\t"
    "s_mov_b64 exec, 1\n\t"
    "s_cmp_ge_u32 s55, s64\n\t"
    "s_cbranch_scc1 L_pq1_%=\n\t"
    "buffer_load_dword v123, off, s[28:31], s46 sc1\n\t"
    "s_mov_b64 exec, -1\n\t"
    "s_branch L_back1_%=\n\t"
    "L_pq1_%=:\n\t"
    "v_cmp_eq_u32 vcc, 0, v123\n\t"
    "s_cbranch_vccz L_in1_%=\n\t"
    "L_poll1_%=:\n\t"
    "buffer_load_dword v155, off, s[28:31], s46 sc1\n\t"
    "s_sub_u32 s47, s47, 1\n\t"
    "s_cmp_eq_u32 s47, 0\n\t"
    "s_cbranch_scc1 L_in1_%=\n\t"
    "s_waitcnt vmcnt(0)\n\t"
    "v_cmp_eq_u32 vcc, 0, v155\n\t"
    "s_cbranch_vccz L_in1_%=\n\t"
    "s_sleep 8\n\t"
    "s_branch L_poll1_%=\n\t"
    "L_in1_%=:\n\t"
    "s_mov_b64 exec, -1\n\t"
    "s_lshl_b32 s46, s37, 2\n\t"
    "s_mul_i32 s47, s34, 1048576\n\t"
    "s_add_u32 s46, s46, s47\n\t"
    "s_add_u32 s46, s46, 17825792\n\t"
    "v_and_b32 v155, 0x80, v122\n\t"
    "v_lshl_add_u32 v155, v155, 23, v122\n\t"
    "buffer_load_dwordx4 v[220:223], v122, s[28:31], s46 offen sc1\n\t"
    "s_add_u32 s46, s46, 262144\n\t"
    "buffer_load_dwordx4 v[224:227], v122, s[28:31], s46 offen sc1\n\t"
    "s_add_u32 s46, s46, 262144\n\t"
    "buffer_load_dwordx4 v[228:231], v122, s[28:31], s46 offen sc1\n\t"
    "s_add_u32 s46, s46, 262144\n\t"
    "buffer_load_dwordx4 v[232:235], v122, s[28:31], s46 offen sc1\n\t"
    "s_add_u32 s46, s46, 262144\n\t"
    "s_mov_b32 s61, 1\n\t"
    "s_cmp_le_u32 s38, 64\n\t"
    "s_cbranch_scc1 L_back1_%=\n\t"
    "s_sub_u32 s46, s46, 262144\n\t"
    "buffer_load_dwordx4 v[248:251], v155, s[28:31], s46 offen offset:256 sc1\n\t"
    "s_sub_u32 s46, s46, 262144\n\t"
    "buffer_load_dwordx4 v[244:247], v155, s[28:31], s46 offen offset:256 sc1\n\t"
    "s_sub_u32 s46, s46, 262144\n\t"
    "buffer_load_dwordx4 v[240:243], v155, s[28:31], s46 offen offset:256 sc1\n\t"
    "s_sub_u32 s46, s46, 262144\n\t"
    "buffer_load_dwordx4 v[236:239], v155, s[28:31], s46 offen offset:256 sc1\n\t"
    "s_branch L_back1_%=\n\t"
    "L_sig_%=:\n\t"
    "v_subrev_u32 v155, s49, v149\n\t"
    "v_add_u32 v155, 1024, v155\n\t"
    "s_waitcnt vmcnt(0)\n\t"
    "ds_add_rtn_u32 v148, v155, v150\n\t"
    "s_mov_b32 s45, 152896\n\t"
    "s_mov_b32 s46, 49283072\n\t"
    "v_mov_b32 v155, 1\n\t"
    "s_waitcnt lgkmcnt(0)\n\t"
    "v_readfirstlane_b32 s47, v148\n\t"
    "s_nop 0\n\t"
    "s_cmp_lg_u32 s47, 7\n\t"
    "s_cbranch_scc1 L_sigb_%=\n\t"
    "s_mov_b64 exec, 1\n\t"
    "buffer_atomic_add v155, off, s[28:31], s46 sc0 sc1\n\t"
    "s_mov_b64 exec, -1\n\t"
    "s_waitcnt vmcnt(0)\n\t"
    "v_readfirstlane_b32 s47, v155\n\t"
    "v_lshlrev_b32 v155, 5, v146\n\t"
    "s_cmp_lg_u32 s47, 255\n\t"
    "s_cbranch_scc1 L_sigb_%=\n\t"
    "s_mov_b32 s46, 49295360\n\t"
    "buffer_store_dword v150, v155, s[28:31], s46 offen sc1\n\t"
    "s_add_u32 s46, s46, 0x2000\n\t"
    "buffer_store_dword v150, v155, s[28:31], s46 offen sc1\n\t"
    "s_add_u32 s46, s46, 0x2000\n\t"
    "buffer_store_dword v150, v155, s[28:31], s46 offen sc1\n\t"
    "s_add_u32 s46, s46, 0x2000\n\t"
    "buffer_store_dword v150, v155, s[28:31], s46 offen sc1\n\t"
    "s_add_u32 s46, s46, 0x2000\n\t"
    "s_branch L_sigb_%=\n\t"
    "L_fin_%=:\n\t"
    "s_branch L_pend_%=\n\t"
    "L_pub_%=:\n\t"
    "s_memrealtime s[46:47]\n\t"
    "s_lshl_b32 s49, %2, 2\n\t"
    "s_add_u32 s49, s49, 49293312\n\t"
    "s_waitcnt lgkmcnt(0)\n\t"
    "s_sub_u32 s47, s46, s44\n\t"
    "s_mul_i32 s47, s47, 320\n\t"
    "s_lshr_b32 s47, s47, 8\n\t"
    "s_add_u32 s46, s46, s47\n\t"
    "s_or_b32 s46, s46, 1\n\t"
    "v_mov_b32 v83, s46\n\t"
    "s_mov_b64 exec, 1\n\t"
    "buffer_store_dword v83, off, s[28:31], s49 sc1\n\t"
    "s_mov_b64 exec, -1\n\t"
    "s_branch L_pubb_%=\n\t"
    "L_part0_%=:\n\t"
    "s_mov_b32 s47, 0\n\t"
    "s_call_b64 s[98:99], L_bld_%=\n\t"
    "s_branch L_partb0_%=\n\t"
    "L_part1_%=:\n\t"
    "s_mov_b32 s47, 1048576\n\t"
    "s_waitcnt vmcnt(8)\n\t"
    "s_call_b64 s[98:99], L_bchk_%=\n\t"
    "s_branch L_partb1_%=\n\t"
    "L_bchk_%=:\n\t"
    "v_min_u32 v132, v128, v129\n\t"
    "v_min3_u32 v132, v132, v130, v131\n\t"
    "v_cmp_eq_u32 vcc, 0, v132\n\t"
    "s_cbranch_vccz L_bok_%=\n\t"
    "L_bld_%=:\n\t"
    "s_mov_b32 s46, 49293312\n\t"
    "buffer_load_dwordx4 v[128:131], v127, s[28:31], s46 offen sc1\n\t"
    "s_cmp_eq_u32 s47, 0\n\t"
    "s_cbranch_scc1 L_bret_%=\n\t"
    "s_sub_u32 s47, s47, 1\n\t"
    "s_sleep 16\n\t"
    "s_waitcnt vmcnt(0)\n\t"
    "s_branch L_bchk_%=\n\t"
    "L_bok_%=:\n\t"
    "v_readfirstlane_b32 s46, v128\n\t"
    "v_mov_b32 v138, -4096\n\t"
    "v_mov_b32 v139, 4096\n\t"
    "v_subrev_u32 v128, s46, v128\n\t"
    "v_subrev_u32 v129, s46, v129\n\t"
    "v_subrev_u32 v130, s46, v130\n\t"
    "v_subrev_u32 v131, s46, v131\n\t"
    "v_writelane_b32 v128, 50, 0\n\t"
    "v_add3_u32 v132, v128, v129, v130\n\t"
    "v_add_u32 v132, v132, v131\n\t"
    "s_nop 1\n\t"
    "v_add_u32_dpp v132, v132, v132 row_shr:1 row_mask:0xf bank_mask:0xf\n\t"
    "s_nop 1\n\t"
    "v_add_u32_dpp v132, v132, v132 row_shr:2 row_mask:0xf bank_mask:0xf\n\t"
    "s_nop 1\n\t"
    "v_add_u32_dpp v132, v132, v132 row_shr:4 row_mask:0xf bank_mask:0xf\n\t"
    "s_nop 1\n\t"
    "v_add_u32_dpp v132, v132, v132 row_shr:8 row_mask:0xf bank_mask:0xf\n\t"
    "s_nop 1\n\t"
    "v_add_u32_dpp v132, v132, v132 row_bcast:15 row_mask:0xa bank_mask:0xf\n\t"
    "s_nop 1\n\t"
    "v_add_u32_dpp v132, v132, v132 row_bcast:31 row_mask:0xc bank_mask:0xf\n\t"
    "s_nop 0\n\t"
    "v_readlane_b32 s46, v132, 63\n\t"
    "s_movk_i32 s47, 20\n\t"
    "v_mov_b32 v133, -8192\n\t"
    "v_mov_b32 v134, 5120\n\t"
    "s_ashr_i32 s46, s46, 8\n\t"
    "v_sub_u32 v128, s46, v128\n\t"
    "v_med3_i32 v128, v128, v138, v139\n\t"
    "v_mul_i32_i24 v128, s47, v128\n\t"
    "v_med3_i32 v128, v128, v133, v134\n\t"
    "v_and_b32 v140, 0x3ff, v128\n\t"
    "v_ashrrev_i32 v128, 10, v128\n\t"
    "v_sub_u32 v129, s46, v129\n\t"
    "v_med3_i32 v129, v129, v138, v139\n\t"
    "v_mul_i32_i24 v129, s47, v129\n\t"
    "v_med3_i32 v129, v129, v133, v134\n\t"
    "v_and_b32 v141, 0x3ff, v129\n\t"
    "v_ashrrev_i32 v129, 10, v129\n\t"
    "v_sub_u32 v130, s46, v130\n\t"
    "v_med3_i32 v130, v130, v138, v139\n\t"
    "v_mul_i32_i24 v130, s47, v130\n\t"
    "v_med3_i32 v130, v130, v133, v134\n\t"
    "v_and_b32 v142, 0x3ff, v130\n\t"
    "v_ashrrev_i32 v130, 10, v130\n\t"
    "v_sub_u32 v131, s46, v131\n\t"
    "v_med3_i32 v131, v131, v138, v139\n\t"
    "v_mul_i32_i24 v131, s47, v131\n\t"
    "v_med3_i32 v131, v131, v133, v134\n\t"
    "v_and_b32 v143, 0x3ff, v131\n\t"
    "v_ashrrev_i32 v131, 10, v131\n\t"
    "v_add3_u32 v132, v128, v129, v130\n\t"
    "v_add_u32 v132, v132, v131\n\t"
    "s_nop 1\n\t"
    "v_add_u32_dpp v132, v132, v132 row_shr:1 row_mask:0xf bank_mask:0xf\n\t"
    "s_nop 1\n\t"
    "v_add_u32_dpp v132, v132, v132 row_shr:2 row_mask:0xf bank_mask:0xf\n\t"
    "s_nop 1\n\t"
    "v_add_u32_dpp v132, v132, v132 row_shr:4 row_mask:0xf bank_mask:0xf\n\t"
    "s_nop 1\n\t"
    "v_add_u32_dpp v132, v132, v132 row_shr:8 row_mask:0xf bank_mask:0xf\n\t"
    "s_nop 1\n\t"
    "v_add_u32_dpp v132, v132, v132 row_bcast:15 row_mask:0xa bank_mask:0xf\n\t"
    "s_nop 1\n\t"
    "v_add_u32_dpp v132, v132, v132 row_bcast:31 row_mask:0xc bank_mask:0xf\n\t"
    "s_nop 0\n\t"
    "v_readlane_b32 s46, v132, 63\n\t"
    "s_movk_i32 s47, 0x400\n\t"
    "s_sub_u32 s48, s47, 512\n\t"
    "s_mov_b32 s49, s46\n\t"
    "v_cmp_le_u32 vcc, s48, v140\n\t"
    "s_bcnt1_i32_b64 s44, vcc\n\t"
    "s_add_u32 s49, s49, s44\n\t"
    "v_cmp_le_u32 vcc, s48, v141\n\t"
    "s_bcnt1_i32_b64 s44, vcc\n\t"
    "s_add_u32 s49, s49, s44\n\t"
    "v_cmp_le_u32 vcc, s48, v142\n\t"
    "s_bcnt1_i32_b64 s44, vcc\n\t"
    "s_add_u32 s49, s49, s44\n\t"
    "v_cmp_le_u32 vcc, s48, v143\n\t"
    "s_bcnt1_i32_b64 s44, vcc\n\t"
    "s_add_u32 s49, s49, s44\n\t"
    "s_cmp_lt_i32 s49, 0\n\t"
    "s_cselect_b32 s47, s48, s47\n\t"
    "s_sub_u32 s48, s47, 256\n\t"
    "s_mov_b32 s49, s46\n\t"
    "v_cmp_le_u32 vcc, s48, v140\n\t"
    "s_bcnt1_i32_b64 s44, vcc\n\t"
    "s_add_u32 s49, s49, s44\n\t"
    "v_cmp_le_u32 vcc, s48, v141\n\t"
    "s_bcnt1_i32_b64 s44, vcc\n\t"
    "s_add_u32 s49, s49, s44\n\t"
    "v_cmp_le_u32 vcc, s48, v142\n\t"
    "s_bcnt1_i32_b64 s44, vcc\n\t"
    "s_add_u32 s49, s49, s44\n\t"
    "v_cmp_le_u32 vcc, s48, v143\n\t"
    "s_bcnt1_i32_b64 s44, vcc\n\t"
    "s_add_u32 s49, s49, s44\n\t"
    "s_cmp_lt_i32 s49, 0\n\t"
    "s_cselect_b32 s47, s48, s47\n\t"
    "s_sub_u32 s48, s47, 128\n\t"
    "s_mov_b32 s49, s46\n\t"
    "v_cmp_le_u32 vcc, s48, v140\n\t"
    "s_bcnt1_i32_b64 s44, vcc\n\t"
    "s_add_u32 s49, s49, s44\n\t"
    "v_cmp_le_u32 vcc, s48, v141\n\t"
    "s_bcnt1_i32_b64 s44, vcc\n\t"
    "s_add_u32 s49, s49, s44\n\t"
    "v_cmp_le_u32 vcc, s48, v142\n\t"
    "s_bcnt1_i32_b64 s44, vcc\n\t"
    "s_add_u32 s49, s49, s44\n\t"
    "v_cmp_le_u32 vcc, s48, v143\n\t"
    "s_bcnt1_i32_b64 s44, vcc\n\t"
    "s_add_u32 s49, s49, s44\n\t"
    "s_cmp_lt_i32 s49, 0\n\t"
    "s_cselect_b32 s47, s48, s47\n\t"
    "s_sub_u32 s48, s47, 64\n\t"
    "s_mov_b32 s49, s46\n\t"
    "v_cmp_le_u32 vcc, s48, v140\n\t"
    "s_bcnt1_i32_b64 s44, vcc\n\t"
    "s_add_u32 s49, s49, s44\n\t"
    "v_cmp_le_u32 vcc, s48, v141\n\t"
    "s_bcnt1_i32_b64 s44, vcc\n\t"
    "s_add_u32 s49, s49, s44\n\t"
    "v_cmp_le_u32 vcc, s48, v142\n\t"
    "s_bcnt1_i32_b64 s44, vcc\n\t"
    "s_add_u32 s49, s49, s44\n\t"
    "v_cmp_le_u32 vcc, s48, v143\n\t"
    "s_bcnt1_i32_b64 s44, vcc\n\t"
    "s_add_u32 s49, s49, s44\n\t"
    "s_cmp_lt_i32 s49, 0\n\t"
    "s_cselect_b32 s47, s48, s47\n\t"
    "s_sub_u32 s48, s47, 32\n\t"
    "s_mov_b32 s49, s46\n\t"
    "v_cmp_le_u32 vcc, s48, v140\n\t"
    "s_bcnt1_i32_b64 s44, vcc\n\t"
    "s_add_u32 s49, s49, s44\n\t"
    "v_cmp_le_u32 vcc, s48, v141\n\t"
    "s_bcnt1_i32_b64 s44, vcc\n\t"
    "s_add_u32 s49, s49, s44\n\t"
    "v_cmp_le_u32 vcc, s48, v142\n\t"
    "s_bcnt1_i32_b64 s44, vcc\n\t"
    "s_add_u32 s49, s49, s44\n\t"
    "v_cmp_le_u32 vcc, s48, v143\n\t"
    "s_bcnt1_i32_b64 s44, vcc\n\t"
    "s_add_u32 s49, s49, s44\n\t"
    "s_cmp_lt_i32 s49, 0\n\t"
    "s_cselect_b32 s47, s48, s47\n\t"
    "s_sub_u32 s47, s47, 32\n\t"
    "s_max_u32 s47, s47, 1\n\t"
    "v_cmp_le_u32 vcc, s47, v140\n\t"
    "s_nop 1\n\t"
    "v_addc_co_u32 v128, vcc, 0, v128, vcc\n\t"
    "v_cmp_le_u32 vcc, s47, v141\n\t"
    "s_nop 1\n\t"
    "v_addc_co_u32 v129, vcc, 0, v129, vcc\n\t"
    "v_cmp_le_u32 vcc, s47, v142\n\t"
    "s_nop 1\n\t"
    "v_addc_co_u32 v130, vcc, 0, v130, vcc\n\t"
    "v_cmp_le_u32 vcc, s47, v143\n\t"
    "s_nop 1\n\t"
    "v_addc_co_u32 v131, vcc, 0, v131, vcc\n\t"
    "v_add3_u32 v132, v128, v129, v130\n\t"
    "v_add_u32 v132, v132, v131\n\t"
    "s_nop 1\n\t"
    "v_add_u32_dpp v132, v132, v132 row_shr:1 row_mask:0xf bank_mask:0xf\n\t"
    "s_nop 1\n\t"
    "v_add_u32_dpp v132, v132, v132 row_shr:2 row_mask:0xf bank_mask:0xf\n\t"
    "s_nop 1\n\t"
    "v_add_u32_dpp v132, v132, v132 row_shr:4 row_mask:0xf bank_mask:0xf\n\t"
    "s_nop 1\n\t"
    "v_add_u32_dpp v132, v132, v132 row_shr:8 row_mask:0xf bank_mask:0xf\n\t"
    "s_nop 1\n\t"
    "v_add_u32_dpp v132, v132, v132 row_bcast:15 row_mask:0xa bank_mask:0xf\n\t"
    "s_nop 1\n\t"
    "v_add_u32_dpp v132, v132, v132 row_bcast:31 row_mask:0xc bank_mask:0xf\n\t"
    "s_nop 0\n\t"
    "v_readlane_b32 s46, v132, 63\n\t"
    "v_lshrrev_b32 v135, 2, v127\n\t"
    "s_cmp_le_u32 s46, 256\n\t"
    "s_cselect_b32 s47, -1, 0\n\t"
    "v_add_u32 v133, 4, v135\n\t"
    "v_mul_u32_u24 v134, s46, v133\n\t"
    "v_lshrrev_b32 v134, 8, v134\n\t"
    "v_sub_u32 v134, v132, v134\n\t"
    "v_and_b32 v134, s47, v134\n\t"
    "v_lshl_add_u32 v136, v133, 4, v134\n\t"
    "v_sub_u32 v132, v132, v131\n\t"
    "v_add_u32 v133, 3, v135\n\t"
    "v_mul_u32_u24 v134, s46, v133\n\t"
    "v_lshrrev_b32 v134, 8, v134\n\t"
    "v_sub_u32 v134, v132, v134\n\t"
    "v_and_b32 v134, s47, v134\n\t"
    "v_lshl_add_u32 v137, v133, 4, v134\n\t"
    "v_lshl_or_b32 v143, v136, 16, v137\n\t"
    "v_sub_u32 v132, v132, v130\n\t"
    "v_add_u32 v133, 2, v135\n\t"
    "v_mul_u32_u24 v134, s46, v133\n\t"
    "v_lshrrev_b32 v134, 8, v134\n\t"
    "v_sub_u32 v134, v132, v134\n\t"
    "v_and_b32 v134, s47, v134\n\t"
    "v_lshl_add_u32 v136, v133, 4, v134\n\t"
    "v_lshl_or_b32 v142, v137, 16, v136\n\t"
    "v_sub_u32 v132, v132, v129\n\t"
    "v_add_u32 v133, 1, v135\n\t"
    "v_mul_u32_u24 v134, s46, v133\n\t"
    "v_lshrrev_b32 v134, 8, v134\n\t"
    "v_sub_u32 v134, v132, v134\n\t"
    "v_and_b32 v134, s47, v134\n\t"
    "v_lshl_add_u32 v137, v133, 4, v134\n\t"
    "v_lshl_or_b32 v141, v136, 16, v137\n\t"
    "v_sub_u32 v132, v132, v128\n\t"
    "v_add_u32 v133, 0, v135\n\t"
    "v_mul_u32_u24 v134, s46, v133\n\t"
    "v_lshrrev_b32 v134, 8, v134\n\t"
    "v_sub_u32 v134, v132, v134\n\t"
    "v_and_b32 v134, s47, v134\n\t"
    "v_lshl_add_u32 v136, v133, 4, v134\n\t"
    "v_lshl_or_b32 v140, v137, 16, v136\n\t"
    "s_mov_b32 s46, 49294336\n\t"
    "buffer_store_dwordx4 v[140:143], v127, s[28:31], s46 offen sc1\n\t"
    "s_mov_b32 s97, 1\n\t"
    "L_bret_%=:\n\t"
    "s_setpc_b64 s[98:99]\n\t"
    "L_rw_%=:\n\t"
    "s_mov_b32 s47, 1048576\n\t"
    "L_rw2_%=:\n\t"
    "s_sleep 16\n\t"
    "s_lshl_b32 s46, %2, 2\n\t"
    "s_add_u32 s46, s46, 49294336\n\t"
    "s_mov_b64 exec, 1\n\t"
    "buffer_load_dword v144, off, s[28:31], s46 sc1\n\t"
    "s_mov_b64 exec, -1\n\t"
    "s_waitcnt vmcnt(0)\n\t"
    "v_readfirstlane_b32 s38, v144\n\t"
    "s_sub_u32 s47, s47, 1\n\t"
    "s_cmp_eq_u32 s47, 0\n\t"
    "s_cselect_b32 s38, 0x100000, s38\n\t"
    "s_cmp_eq_u32 s38, 0\n\t"
    "s_cbranch_scc1 L_rw2_%=\n\t"
    "s_branch L_rok_%=\n\t"
    "L_xw0_%=:\n\t"
    "s_mov_b32 s68, 1048576\n\t"
    "L_xv0_%=:\n\t"
    "s_sleep 4\n\t"
    "s_mov_b64 exec, 1\n\t"
    "buffer_load_dword v164, off, s[28:31], s93 offset:0 sc1\n\t"
    "s_mov_b64 exec, -1\n\t"
    "s_sub_u32 s68, s68, 1\n\t"
    "s_cmp_eq_u32 s68, 0\n\t"
    "s_cbranch_scc1 L_xwb0_%=\n\t"
    "s_waitcnt vmcnt(0)\n\t"
    "v_readfirstlane_b32 s65, v164\n\t"
    "s_nop 0\n\t"
    "s_cmp_eq_u32 s65, 0\n\t"
    "s_cbranch_scc1 L_xv0_%=\n\t"
    "s_branch L_xwb0_%=\n\t"
    "L_xw1_%=:\n\t"
    "s_mov_b32 s68, 1048576\n\t"
    "L_xv1_%=:\n\t"
    "s_sleep 4\n\t"
    "s_mov_b64 exec, 1\n\t"
    "buffer_load_dword v164, off, s[28:31], s93 offset:128 sc1\n\t"
    "s_mov_b64 exec, -1\n\t"
    "s_sub_u32 s68, s68, 1\n\t"
    "s_cmp_eq_u32 s68, 0\n\t"
    "s_cbranch_scc1 L_xwb1_%=\n\t"
    "s_waitcnt vmcnt(0)\n\t"
    "v_readfirstlane_b32 s65, v164\n\t"
    "s_nop 0\n\t"
    "s_cmp_eq_u32 s65, 0\n\t"
    "s_cbranch_scc1 L_xv1_%=\n\t"
    "s_branch L_xwb1_%=\n\t"
    "L_xw2_%=:\n\t"
    "s_mov_b32 s68, 1048576\n\t"
    "L_xv2_%=:\n\t"
    "s_sleep 4\n\t"
    "s_mov_b64 exec, 1\n\t"
    "buffer_load_dword v164, off, s[28:31], s93 offset:256 sc1\n\t"
    "s_mov_b64 exec, -1\n\t"
    "s_sub_u32 s68, s68, 1\n\t"
    "s_cmp_eq_u32 s68, 0\n\t"
    "s_cbranch_scc1 L_xwb2_%=\n\t"
    "s_waitcnt vmcnt(0)\n\t"
    "v_readfirstlane_b32 s65, v164\n\t"
    "s_nop 0\n\t"
    "s_cmp_eq_u32 s65, 0\n\t"
    "s_cbranch_scc1 L_xv2_%=\n\t"
    "s_branch L_xwb2_%=\n\t"
    "L_xw3_%=:\n\t"
    "s_mov_b32 s68, 1048576\n\t"
    "L_xv3_%=:\n\t"
    "s_sleep 4\n\t"
    "s_mov_b64 exec, 1\n\t"
    "buffer_load_dword v164, off, s[28:31], s93 offset:384 sc1\n\t"
    "s_mov_b64 exec, -1\n\t"
    "s_sub_u32 s68, s68, 1\n\t"
    "s_cmp_eq_u32 s68, 0\n\t"
    "s_cbranch_scc1 L_xwb3_%=\n\t"
    "s_waitcnt vmcnt(0)\n\t"
    "v_readfirstlane_b32 s65, v164\n\t"
    "s_nop 0\n\t"
    "s_cmp_eq_u32 s65, 0\n\t"
    "s_cbranch_scc1 L_xv3_%=\n\t"
    "s_branch L_xwb3_%=\n\t"
    "L_mp2_%=:\n\t"
    "v_lshrrev_b32 v57, 4, v127\n\t"
    "v_add_u32 v57, 32, v57\n\t"
    "v_mov_b32 v58, 64\n\t"
    "v_lshlrev_b32 v57, 7, v57\n\t"
    "s_mov_b32 s65, 49283072\n\t"
    "v_mov_b32 v56, 256\n\t"
    "s_mov_b32 s66, 1048576\n\t"
    "s_mov_b32 s65, 49283072\n\t"
    "s_mov_b64 exec, 15\n\t"
    "buffer_load_dword v56, v57, s[28:31], s65 offen sc1\n\t"
    "s_mov_b64 exec, -1\n\t"
    "s_branch L_mp2b_%=\n\t"
    "L_mp3_%=:\n\t"
    "v_cmp_gt_u32 vcc, v58, v56\n\t"
    "s_cbranch_vccz L_mp3b_%=\n\t"
    "s_sub_u32 s66, s66, 1\n\t"
    "s_cmp_eq_u32 s66, 0\n\t"
    "s_cbranch_scc1 L_mp3b_%=\n\t"
    "s_sleep 8\n\t"
    "s_mov_b32 s65, 49283072\n\t"
    "s_mov_b64 exec, 15\n\t"
    "buffer_load_dword v56, v57, s[28:31], s65 offen sc1\n\t"
    "s_mov_b64 exec, -1\n\t"
    "s_waitcnt vmcnt(0)\n\t"
    "s_branch L_mp3_%=\n\t"
    "L_pend_%=:\n\t"
    ".Ldcp_end:\n\t"
    : "+v"(tid)
    : "s"(__builtin_amdgcn_kernarg_segment_ptr()), "s"(blockIdx.x + (unsigned)(uintptr_t)smem)
    : "memory", "vcc", "scc", "m0", "s4", "s5", "s6", "s7", "s8", "s9", "s10", "s11", "s12", "s13", "s14", "s15", "s16", "s17", "s18", "s19", "s20", "s21", "s22", "s23", "s24", "s25", "s26", "s27", "s28", "s29", "s30", "s31", "s34", "s35", "s36", "s37", "s38", "s39", "s40", "s41", "s42", "s43", "s44", "s45", "s46", "s47", "s48", "s49", "s50", "s51", "s52", "s53", "s54", "s55", "s56", "s57", "s58", "s59", "s60", "s61", "s62", "s63", "s64", "s65", "s66", "s67", "s68", "s69", "s70", "s71", "s72", "s73", "s74", "s75", "s76", "s77", "s78", "s79", "s80", "s81", "s82", "s83", "s84", "s85", "s86", "s87", "s88", "s89", "s90", "s91", "s92", "s93", "s94", "s95", "s96", "s97", "s98", "s99", "v1", "v2", "v3", "v4", "v5", "v6", "v7", "v8", "v9", "v10", "v11", "v12", "v13", "v14", "v15", "v16", "v17", "v18", "v19", "v20", "v21", "v22", "v23", "v24", "v25", "v26", "v27", "v28", "v29", "v30", "v31", "v32", "v33", "v34", "v35", "v36", "v37", "v38", "v39", "v40", "v41", "v42", "v43", "v44", "v45", "v46", "v47", "v48", "v49", "v50", "v51", "v52", "v53", "v54", "v55", "v56", "v57", "v58", "v59", "v60", "v61", "v62", "v63", "v64", "v65", "v66", "v67", "v68", "v69", "v70", "v71", "v72", "v73", "v74", "v75", "v76", "v77", "v78", "v79", "v80", "v81", "v82", "v83", "v84", "v85", "v86", "v87", "v88", "v89", "v90", "v91", "v92", "v93", "v94", "v95", "v96", "v97", "v98", "v99", "v100", "v101", "v102", "v103", "v104", "v105", "v106", "v107", "v108", "v109", "v110", "v111", "v112", "v113", "v114", "v115", "v116", "v117", "v118", "v119", "v120", "v121", "v122", "v123", "v124", "v125", "v126", "v127", "v128", "v129", "v130", "v131", "v132", "v133", "v134", "v135", "v136", "v137", "v138", "v139", "v140", "v141", "v142", "v143", "v144", "v145", "v146", "v147", "v148", "v149", "v150", "v151", "v152", "v153", "v154", "v155", "v156", "v157", "v158", "v159", "v160", "v161", "v162", "v163", "v164", "v165", "v166", "v167", "v168", "v169", "v170", "v171", "v172", "v173", "v174", "v175", "v176", "v177", "v178", "v179", "v180", "v181", "v182", "v183", "v184", "v185", "v186", "v187", "v188", "v189", "v190", "v191", "v192", "v193", "v194", "v195", "v196", "v197", "v198", "v199", "v200", "v201", "v202", "v203", "v204", "v205", "v206", "v207", "v208", "v209", "v210", "v211", "v212", "v213", "v214", "v215", "v216", "v217", "v218", "v219", "v220", "v221", "v222", "v223", "v224", "v225", "v226", "v227", "v228", "v229", "v230", "v231", "v232", "v233", "v234", "v235", "v236", "v237", "v238", "v239", "v240", "v241", "v242", "v243", "v244", "v245", "v246", "v247", "v248", "v249", "v250", "v251", "v252", "v253", "v254", "v255");
}

extern "C" void kernel_launch(void* const* d_in, const int* in_sizes, int n_in, void* d_out, int out_size, void* d_ws,
                              size_t ws_size, hipStream_t stream) {
  dcp_all<<<dim3(256), dim3(512), 0, stream>>>(d_in[6], d_in[0], d_in[4], d_in[5], d_in[1], d_in[2], d_in[3], d_ws, d_out);
}
```
